```python
import torch

def _softmax(x, axis=-1):
    return torch.softmax(x, dim=axis)
def _gelu(x):
    return torch.nn.functional.gelu(x, approximate="tanh")
def _silu(x):
    return torch.nn.functional.silu(x)
def _sigmoid(x):
    return torch.sigmoid(x)
def _layernorm(x, axis=-1, eps=1e-5):
    m = x.mean(dim=axis, keepdim=True)
    v = x.var(dim=axis, keepdim=True, unbiased=False)
    return (x - m) / torch.sqrt(v + eps)
def _scatter_add(tgt, idx, src, axis=0):
    return tgt.clone().index_add_(axis, idx, src)
def _segment_sum(src, seg, n):
    out = torch.zeros((n,) + src.shape[1:], dtype=src.dtype)
    return out.index_add_(0, seg, src)
def _pad(x, pw):
    flat = tuple(v for pair in reversed(pw) for v in pair)
    return torch.nn.functional.pad(x, flat)

kernel_name = 'standalone__cross_network_dcn__recsys'

def setup_inputs():

    _w = (lambda a: (lambda t: t.to(torch.bfloat16) if t.is_floating_point() else t)(torch.as_tensor(a)))
    B, F, D, L = 16384, 100, 16, 6
    Din = min(F * D, 1024)
    return {
        "x0": _w((lambda *s: torch.randn(*s, dtype=torch.bfloat16) * 0.05)(B, Din)),
        "ws": _w((lambda *s: torch.randn(*s, dtype=torch.bfloat16) * 0.8)(L, Din, Din) * (Din ** -0.5)),
        "bs": _w((lambda *s: torch.randn(*s, dtype=torch.bfloat16) * 3.2)(L, Din) * 0.01),
    }

def reference(x0, ws, bs):
    x = x0
    L = ws.shape[0]
    for l in range(L):
        proj = torch.einsum('bd,de->be', x, ws[l]) + bs[l]
        x = x0 * proj + x
    return x
```

```cpp
#include <hip/hip_runtime.h>
#include <stdint.h>
typedef unsigned short u16;
constexpr size_t WT_BYTES = (size_t)6 * 1024 * 1024 * 2 + 98304;
constexpr size_t FLAG_BYTES = 256 * 1024 + 8192;

__global__ __launch_bounds__(256, 1) void dcn_pair(const u16* __restrict__ x0, u16* __restrict__ wt, const u16* __restrict__ bs,
                                                   u16* __restrict__ ex0, unsigned* __restrict__ flags, u16* __restrict__ out,
                                                   const u16* __restrict__ w) {
  __shared__ __attribute__((aligned(16))) unsigned char lds[163840];
  const int wave = __builtin_amdgcn_readfirstlane(threadIdx.x >> 6);
  const unsigned ldsb = (unsigned)(size_t)lds;
  const unsigned pb = blockIdx.x, pk = 3;
  const unsigned vb = ((pb & ~(8u | (1u << pk))) | (((pb >> pk) & 1u) << 3) | (((pb >> 3) & 1u) << pk)) ^ 0u;
  asm volatile(
    "s_load_dwordx2 s[72:73], %4, 0x30\n"
    "s_load_dwordx2 s[24:25], %4, 0\n"
    "s_load_dwordx8 s[4:11], %4, 0\n"
    "s_load_dwordx4 s[12:15], %4, 0x20\n"
    "s_mov_b32 s65, %1\n"
    "v_mbcnt_lo_u32_b32 v0, -1, 0\n"
    "v_mbcnt_hi_u32_b32 v0, -1, v0\n"
    "v_lshlrev_b32 v240, 4, v0\n"
    "s_getpc_b64 s[48:49]\n"
    "s_and_b32 s49, s49, 0xffff\n"
    "s_mov_b32 s50, CODE_END_%= - . - 64\n"
    "s_mov_b32 s51, 0x20000\n"
    "s_lshr_b32 s60, %3, 3\n"
    "s_lshl_b32 s60, s60, 2\n"
    "s_add_u32 s60, s60, s65\n"
    "s_lshl_b32 s60, s60, 10\n"
    "buffer_load_dwordx4 v[248:251], v240, s[48:51], s60 offen\n"
    "s_bfe_u32 s64, %0, 0x10003\n"
    "s_lshr_b32 s62, %0, 4\n"
    "s_and_b32 s61, %0, 7\n"
    "s_lshl_b32 s60, s62, 3\n"
    "s_add_u32 s60, s60, s61\n"
    "s_lshl_b32 s40, s60, 18\n"
    "s_mov_b32 s26, 262144\n"
    "s_mov_b32 s27, 0x20000\n"
    "s_mov_b32 s74, 12582912\n"
    "s_mov_b32 s75, s27\n"
    "s_lshl_b32 s60, s65, 12\n"
    "s_add_u32 s68, %2, s60\n"
    "s_add_u32 s79, s68, s60\n"
    "s_lshl_b32 s61, s64, 10\n"
    "s_lshl_b32 s60, s65, 7\n"
    "s_add_u32 s70, s61, s60\n"
    "s_lshl_b32 s66, s65, 16\n"
    "s_mov_b32 s42, 0x20a86420\n"
    "s_mov_b32 s43, 0x6420a864\n"
    "s_lshl_b32 s63, s62, 2\n"
    "s_lshr_b64 s[42:43], s[42:43], s63\n"
    "s_and_b32 s41, s42, 15\n"
    "s_lshl_b32 s67, s41, 7\n"
    "s_lshl_b32 s57, s41, 17\n"
    "s_add_u32 s57, s57, s70\n"
    "v_lshrrev_b32 v1, 3, v0\n"
    "v_lshlrev_b32 v1, 11, v1\n"
    "v_lshrrev_b32 v2, 4, v0\n"
    "v_and_b32 v3, 7, v0\n"
    "v_xor_b32 v4, v3, v2\n"
    "v_lshl_add_u32 v255, v4, 4, v1\n"
    "v_xor_b32 v243, 64, v255\n"
    "v_and_b32 v4, 1, v2\n"
    "v_lshlrev_b32 v4, 2, v4\n"
    "v_xor_b32 v4, v4, v3\n"
    "v_lshl_add_u32 v254, v4, 4, v1\n"
    "v_and_b32 v8, 3, v0\n"
    "v_lshlrev_b32 v8, 4, v8\n"
    "v_lshrrev_b32 v9, 2, v0\n"
    "v_and_b32 v9, 3, v9\n"
    "v_lshl_add_u32 v10, v2, 3, v9\n"
    "v_lshrrev_b32 v11, 1, v9\n"
    "v_lshlrev_b32 v11, 6, v11\n"
    "v_lshlrev_b32 v12, 7, v10\n"
    "v_add3_u32 v252, v12, v11, v8\n"
    "v_add_u32 v252, s79, v252\n"
    "v_xor_b32 v253, 64, v252\n"
    "v_and_b32 v5, 15, v0\n"
    "v_lshrrev_b32 v6, 1, v5\n"
    "v_xor_b32 v6, v6, v2\n"
    "v_lshlrev_b32 v5, 7, v5\n"
    "v_lshl_add_u32 v5, v6, 4, v5\n"
    "s_add_u32 s60, %2, 65536\n"
    "v_add_u32 v241, s60, v5\n"
    "v_xor_b32 v242, 64, v241\n"
    "v_mov_b32 v244, 0x3f803f80\n"
    "v_mov_b32 v245, 0x3f80\n"
    "v_mov_b32 v246, 0x3eaa4400\n"
    "v_mov_b32 v247, 0x0\n"
    "s_waitcnt lgkmcnt(0)\n"
    "s_and_b32 s73, s73, 0xffff\n"
    "s_add_u32 m0, s79, 0\n"
    "s_add_u32 s54, s57, 0\n"
    "buffer_load_dwordx4 v254, s[72:75], s54 offen  lds\n"
    "s_addk_i32 m0, 1024\n"
    "s_addk_i32 s54, 16384\n"
    "buffer_load_dwordx4 v254, s[72:75], s54 offen  lds\n"
    "s_addk_i32 m0, 1024\n"
    "s_addk_i32 s54, 16384\n"
    "buffer_load_dwordx4 v254, s[72:75], s54 offen  lds\n"
    "s_addk_i32 m0, 1024\n"
    "s_addk_i32 s54, 16384\n"
    "buffer_load_dwordx4 v254, s[72:75], s54 offen  lds\n"
    "s_add_u32 s24, s24, s40\n"
    "s_addc_u32 s25, s25, 0\n"
    "s_and_b32 s25, s25, 0xffff\n"
    "s_xor_b32 s59, s66, s67\n"
    "s_addk_i32 s67, 128\n"
    "s_cmpk_eq_u32 s67, 1536\n"
    "s_cselect_b32 s67, 0, s67\n"
    "s_add_u32 m0, s68, 65536\n"
    "s_nop 0\n"
    "buffer_load_dwordx4 v255, s[24:27], s59 offen nt lds\n"
    "s_add_u32 m0, s68, 66560\n"
    "s_addk_i32 s59, 16384\n"
    "buffer_load_dwordx4 v243, s[24:27], s59 offen nt lds\n"
    "s_add_u32 m0, s68, 67584\n"
    "s_addk_i32 s59, 16384\n"
    "buffer_load_dwordx4 v255, s[24:27], s59 offen nt lds\n"
    "s_add_u32 m0, s68, 68608\n"
    "s_addk_i32 s59, 16384\n"
    "buffer_load_dwordx4 v243, s[24:27], s59 offen nt lds\n"
    "s_lshl_b32 s84, s64, 3\n"
    "s_add_u32 s84, s84, s65\n"
    "s_add_u32 s85, s84, 4\n"
    "s_sub_u32 s60, s84, s41\n"
    "s_add_u32 s61, s60, 12\n"
    "s_cmp_lt_u32 s84, s41\n"
    "s_cselect_b32 s60, s61, s60\n"
    "s_cmp_lt_u32 s84, 12\n"
    "s_cselect_b32 s84, s60, s84\n"
    "s_sub_u32 s60, s85, s41\n"
    "s_add_u32 s61, s60, 12\n"
    "s_cmp_lt_u32 s85, s41\n"
    "s_cselect_b32 s60, s61, s60\n"
    "s_cmp_lt_u32 s85, 12\n"
    "s_cselect_b32 s85, s60, s85\n"
    "s_cmp_ge_u32 s84, 4\n"
    "s_cselect_b32 s60, s64, 0\n"
    "s_xor_b32 s84, s84, s60\n"
    "s_xor_b32 s85, s85, s64\n"
    "s_lshl_b32 s85, 1, s85\n"
    "s_lshl_b32 s60, 1, s84\n"
    "s_or_b32 s85, s85, s60\n"
    "s_cmp_eq_u32 s65, 0\n"
    "s_cselect_b32 s86, -1, 0\n"
    "s_cmp_eq_u32 %0, 0\n"
    "s_cselect_b32 s87, 4096, 0\n"
    "s_mov_b64 s[62:63], s[12:13]\n"
    "s_add_u32 s62, s62, 262144\n"
    "s_addc_u32 s63, s63, 0\n"
    "s_mov_b32 s80, s62\n"
    "s_and_b32 s81, s63, 0xffff\n"
    "s_mov_b32 s82, 0\n"
    "s_mov_b32 s83, 0x20000\n"
    "s_lshl_b32 s76, %0, 3\n"
    "s_lshl_b32 s77, %0, 2\n"
    "s_lshl_b32 s61, %0, 10\n"
    "s_lshl_b32 s60, s65, 8\n"
    "s_add_u32 s61, s61, s60\n"
    "s_mov_b64 s[62:63], s[12:13]\n"
    "s_add_u32 s62, s62, s61\n"
    "s_addc_u32 s63, s63, 0\n"
    "s_mov_b32 s44, s62\n"
    "s_and_b32 s45, s63, 0xffff\n"
    "s_mov_b32 s46, 256\n"
    "s_mov_b32 s47, 0x20000\n"
    "s_sub_u32 s90, s68, s79\n"
    "s_add_u32 s90, s90, 114688\n"
    "s_mov_b32 s60, s40\n"
    "s_mov_b64 s[62:63], s[14:15]\n"
    "s_add_u32 s62, s62, s60\n"
    "s_addc_u32 s63, s63, 0\n"
    "s_mov_b32 s40, s62\n"
    "s_and_b32 s41, s63, 0xffff\n"
    "s_mov_b32 s42, 262144\n"
    "s_mov_b32 s43, 0x20000\n"
    "s_add_u32 s60, s77, s65\n"
    "s_lshr_b32 s20, s60, 9\n"
    "s_bfe_u32 s21, s60, 0x50004\n"
    "s_and_b32 s22, s60, 15\n"
    "s_lshr_b32 s23, s22, 3\n"
    "s_bfe_u32 s32, s22, 0x10002\n"
    "s_and_b32 s33, s22, 3\n"
    "s_and_b32 s34, s21, 1\n"
    "s_lshl_b32 s34, s34, 1\n"
    "s_bfe_u32 s35, s21, 0x10002\n"
    "s_or_b32 s34, s34, s35\n"
    "s_lshl_b32 s35, s34, 4\n"
    "s_lshl_b32 s36, s32, 3\n"
    "s_add_u32 s35, s35, s36\n"
    "s_bfe_u32 s36, s21, 0x10003\n"
    "s_lshl_b32 s36, s36, 6\n"
    "s_add_u32 s36, s36, s35\n"
    "s_add_u32 s36, s36, 96\n"
    "s_add_u32 s37, s35, 32\n"
    "s_mul_i32 s39, s32, 224\n"
    "s_lshl_b32 s34, s34, 3\n"
    "s_add_u32 s39, s39, s34\n"
    "s_bitcmp1_b32 s21, 3\n"
    "s_cselect_b32 s39, s37, s39\n"
    "s_lshr_b32 s37, s21, 4\n"
    "s_cmp_eq_u32 s37, s23\n"
    "s_cselect_b32 s39, s39, s36\n"
    "s_bfe_u32 s36, s21, 0x10001\n"
    "s_add_u32 s39, s39, s36\n"
    "s_lshr_b32 s34, s21, 1\n"
    "s_xor_b32 s34, s34, s23\n"
    "s_lshl_b32 s35, s34, 3\n"
    "s_add_u32 s36, s35, 96\n"
    "s_sub_u32 s37, 344, s35\n"
    "s_cmp_eq_u32 s32, 0\n"
    "s_cselect_b32 s38, s36, s37\n"
    "s_cmp_lt_u32 s34, 12\n"
    "s_cselect_b32 s71, 1, 0\n"
    "s_andn2_b32 s71, s71, s20\n"
    "s_lshl_b32 s71, s71, 30\n"
    "s_and_b32 s36, s21, 1\n"
    "s_add_u32 s38, s38, s36\n"
    "s_cmp_eq_u32 s20, 0\n"
    "s_cselect_b32 s38, s38, s39\n"
    "s_lshl_b32 s35, s23, 2\n"
    "s_add_u32 s35, s35, s33\n"
    "s_mul_i32 s35, s35, 6\n"
    "s_add_u32 s35, s35, s20\n"
    "s_lshl_b32 s35, s35, 18\n"
    "s_lshl_b32 s38, s38, 10\n"
    "s_add_u32 s89, s35, s38\n"
    "s_or_b32 s89, s89, s71\n"
    "s_lshl_b32 s39, s39, 10\n"
    "s_add_u32 s91, s35, s39\n"
    "s_lshl_b32 s35, s20, 21\n"
    "s_lshl_b32 s36, s21, 16\n"
    "s_add_u32 s35, s35, s36\n"
    "s_lshl_b32 s36, s22, 7\n"
    "s_add_u32 s78, s35, s36\n"
    "s_mov_b64 s[62:63], s[6:7]\n"
    "s_add_u32 s62, s62, 0\n"
    "s_addc_u32 s63, s63, 0\n"
    "s_mov_b32 s28, s62\n"
    "s_and_b32 s29, s63, 0xffff\n"
    "s_mov_b32 s30, 12582912\n"
    "s_mov_b32 s31, 0x20000\n"
    "s_mov_b64 s[62:63], s[8:9]\n"
    "s_add_u32 s62, s62, 0\n"
    "s_addc_u32 s63, s63, 0\n"
    "s_mov_b32 s20, s62\n"
    "s_and_b32 s21, s63, 0xffff\n"
    "s_mov_b32 s22, 12288\n"
    "s_mov_b32 s23, 0x20000\n"
    "s_mov_b64 s[62:63], s[6:7]\n"
    "s_add_u32 s62, s62, 12582912\n"
    "s_addc_u32 s63, s63, 0\n"
    "s_mov_b32 s36, s62\n"
    "s_and_b32 s37, s63, 0xffff\n"
    "s_mov_b32 s38, 98304\n"
    "s_mov_b32 s39, 0x20000\n"
    "s_mov_b64 s[62:63], s[10:11]\n"
    "s_add_u32 s62, s62, 0x1fffff\n"
    "s_addc_u32 s63, s63, 0\n"
    "s_mov_b32 s32, s62\n"
    "s_and_b32 s33, s63, 0xffff\n"
    "s_mov_b32 s34, 201326592\n"
    "s_mov_b32 s35, 0x20000\n"
    "s_and_b32 s32, s32, 0xffe00000\n"
    "s_mov_b32 s58, 0xffff0000\n"
    "s_mov_b32 s52, 0\n"
    "s_mov_b32 s53, 196608\n"
    "s_xor_b32 s60, %0, 8\n"
    "s_lshl_b32 s61, s60, 10\n"
    "s_mov_b64 s[62:63], s[12:13]\n"
    "s_add_u32 s62, s62, s61\n"
    "s_addc_u32 s63, s63, 0\n"
    "s_mov_b32 s48, s62\n"
    "s_and_b32 s49, s63, 0xffff\n"
    "s_mov_b32 s50, 64\n"
    "s_mov_b32 s51, 0x20000\n"
    "s_add_u32 m0, s79, 4096\n"
    "s_addk_i32 s54, 16384\n"
    "buffer_load_dwordx4 v254, s[72:75], s54 offen  lds\n"
    "s_addk_i32 m0, 1024\n"
    "s_addk_i32 s54, 16384\n"
    "buffer_load_dwordx4 v254, s[72:75], s54 offen  lds\n"
    "s_addk_i32 m0, 1024\n"
    "s_addk_i32 s54, 16384\n"
    "buffer_load_dwordx4 v254, s[72:75], s54 offen  lds\n"
    "s_addk_i32 m0, 1024\n"
    "s_addk_i32 s54, 16384\n"
    "buffer_load_dwordx4 v254, s[72:75], s54 offen  lds\n"
    "v_mfma_f32_16x16x4_4b_bf16 v[0:15], v[244:245], v[246:247], 0\n"
    "v_mfma_f32_16x16x4_4b_bf16 v[32:47], v[244:245], v[246:247], 0\n"
    "s_add_u32 m0, s79, 32768\n"
    "s_add_u32 s54, s57, 512\n"
    "buffer_load_dwordx4 v254, s[72:75], s54 offen  lds\n"
    "v_mfma_f32_16x16x4_4b_bf16 v[64:79], v[244:245], v[246:247], 0\n"
    "v_mfma_f32_16x16x4_4b_bf16 v[96:111], v[244:245], v[246:247], 0\n"
    "s_addk_i32 m0, 1024\n"
    "s_addk_i32 s54, 16384\n"
    "buffer_load_dwordx4 v254, s[72:75], s54 offen  lds\n"
    "v_mfma_f32_16x16x4_4b_bf16 v[128:143], v[244:245], v[246:247], 0\n"
    "v_mfma_f32_16x16x4_4b_bf16 v[160:175], v[244:245], v[246:247], 0\n"
    "s_addk_i32 m0, 1024\n"
    "s_addk_i32 s54, 16384\n"
    "buffer_load_dwordx4 v254, s[72:75], s54 offen  lds\n"
    "v_mfma_f32_16x16x4_4b_bf16 v[192:207], v[244:245], v[246:247], 0\n"
    "v_mfma_f32_16x16x4_4b_bf16 v[224:239], v[244:245], v[246:247], 0\n"
    "s_addk_i32 m0, 1024\n"
    "s_addk_i32 s54, 16384\n"
    "buffer_load_dwordx4 v254, s[72:75], s54 offen  lds\n"
    "v_mfma_f32_16x16x4_4b_bf16 v[16:31], v[244:245], v[246:247], 0\n"
    "v_mfma_f32_16x16x4_4b_bf16 v[48:63], v[244:245], v[246:247], 0\n"
    "s_addk_i32 m0, 1024\n"
    "s_addk_i32 s54, 16384\n"
    "buffer_load_dwordx4 v254, s[72:75], s54 offen  lds\n"
    "v_mfma_f32_16x16x4_4b_bf16 v[80:95], v[244:245], v[246:247], 0\n"
    "v_mfma_f32_16x16x4_4b_bf16 v[112:127], v[244:245], v[246:247], 0\n"
    "s_addk_i32 m0, 1024\n"
    "s_addk_i32 s54, 16384\n"
    "buffer_load_dwordx4 v254, s[72:75], s54 offen  lds\n"
    "v_mfma_f32_16x16x4_4b_bf16 v[144:159], v[244:245], v[246:247], 0\n"
    "v_mfma_f32_16x16x4_4b_bf16 v[176:191], v[244:245], v[246:247], 0\n"
    "s_addk_i32 m0, 1024\n"
    "s_addk_i32 s54, 16384\n"
    "buffer_load_dwordx4 v254, s[72:75], s54 offen  lds\n"
    "v_mfma_f32_16x16x4_4b_bf16 v[208:223], v[244:245], v[246:247], 0\n"
    "v_mfma_f32_16x16x4_4b_bf16 a[240:255], v[244:245], v[246:247], 0\n"
    "s_addk_i32 m0, 1024\n"
    "s_addk_i32 s54, 16384\n"
    "buffer_load_dwordx4 v254, s[72:75], s54 offen  lds\n"
    "s_add_u32 s57, s57, 131072\n"
    "s_cmp_ge_u32 s57, 1572864\n"
    "s_cselect_b32 s57, s70, s57\n"
    "s_waitcnt vmcnt(16)\n"
    "ds_read_b64_tr_b16 a[224:225], v252 offset:0\n"
    "ds_read_b64_tr_b16 a[226:227], v252 offset:512\n"
    "ds_read_b64_tr_b16 a[228:229], v252 offset:8\n"
    "ds_read_b64_tr_b16 a[230:231], v252 offset:520\n"
    "ds_read_b64_tr_b16 a[232:233], v253 offset:0\n"
    "ds_read_b64_tr_b16 a[234:235], v253 offset:512\n"
    "ds_read_b64_tr_b16 a[236:237], v253 offset:8\n"
    "ds_read_b64_tr_b16 a[238:239], v253 offset:520\n"
    "s_waitcnt vmcnt(12)\n"
    "s_barrier\n"
    "ds_read_b128 a[128:131], v241 offset:0\n"
    "s_waitcnt lgkmcnt(0)\n"
    "v_mfma_f32_16x16x32_bf16 v[0:3], a[224:227], a[128:131], v[0:3]\n"
    "ds_read_b128 a[132:135], v241 offset:2048\n"
    "s_waitcnt lgkmcnt(0)\n"
    "v_mfma_f32_16x16x32_bf16 v[32:35], a[224:227], a[132:135], v[32:35]\n"
    "ds_read_b128 a[136:139], v241 offset:4096\n"
    "s_waitcnt lgkmcnt(0)\n"
    "v_mfma_f32_16x16x32_bf16 v[64:67], a[224:227], a[136:139], v[64:67]\n"
    "ds_read_b128 a[140:143], v241 offset:6144\n"
    "s_waitcnt lgkmcnt(0)\n"
    "ds_read_b128 a[144:147], v241 offset:8192\n"
    "ds_read_b128 a[148:151], v241 offset:10240\n"
    "ds_read_b128 a[152:155], v241 offset:12288\n"
    "ds_read_b128 a[156:159], v241 offset:14336\n"
    "v_mfma_f32_16x16x32_bf16 v[96:99], a[224:227], a[140:143], v[96:99]\n"
    "s_waitcnt lgkmcnt(3)\n"
    "v_mfma_f32_16x16x32_bf16 v[128:131], a[224:227], a[144:147], v[128:131]\n"
    "s_waitcnt lgkmcnt(2)\n"
    "v_mfma_f32_16x16x32_bf16 v[160:163], a[224:227], a[148:151], v[160:163]\n"
    "s_waitcnt lgkmcnt(1)\n"
    "v_mfma_f32_16x16x32_bf16 v[192:195], a[224:227], a[152:155], v[192:195]\n"
    "s_waitcnt lgkmcnt(0)\n"
    "ds_read_b128 a[160:163], v242 offset:0\n"
    "v_mfma_f32_16x16x32_bf16 v[224:227], a[224:227], a[156:159], v[224:227]\n"
    "ds_read_b128 a[164:167], v242 offset:2048\n"
    "v_mfma_f32_16x16x32_bf16 v[228:231], a[228:231], a[156:159], v[228:231]\n"
    "ds_read_b128 a[168:171], v242 offset:4096\n"
    "v_mfma_f32_16x16x32_bf16 v[196:199], a[228:231], a[152:155], v[196:199]\n"
    "ds_read_b128 a[172:175], v242 offset:6144\n"
    "v_mfma_f32_16x16x32_bf16 v[164:167], a[228:231], a[148:151], v[164:167]\n"
    "ds_read_b128 a[176:179], v242 offset:8192\n"
    "v_mfma_f32_16x16x32_bf16 v[132:135], a[228:231], a[144:147], v[132:135]\n"
    "ds_read_b128 a[180:183], v242 offset:10240\n"
    "v_mfma_f32_16x16x32_bf16 v[100:103], a[228:231], a[140:143], v[100:103]\n"
    "ds_read_b128 a[184:187], v242 offset:12288\n"
    "v_mfma_f32_16x16x32_bf16 v[68:71], a[228:231], a[136:139], v[68:71]\n"
    "ds_read_b128 a[188:191], v242 offset:14336\n"
    "s_add_u32 m0, s79, 131072\n"
    "v_mfma_f32_16x16x32_bf16 v[36:39], a[228:231], a[132:135], v[36:39]\n"
    "s_add_u32 s54, s57, 0\n"
    "v_mfma_f32_16x16x32_bf16 v[4:7], a[228:231], a[128:131], v[4:7]\n"
    "buffer_load_dwordx4 v254, s[72:75], s54 offen  lds\n"
    "v_mfma_f32_16x16x32_bf16 v[8:11], a[232:235], a[128:131], v[8:11]\n"
    "s_addk_i32 m0, 1024\n"
    "s_addk_i32 s54, 16384\n"
    "v_mfma_f32_16x16x32_bf16 v[40:43], a[232:235], a[132:135], v[40:43]\n"
    "buffer_load_dwordx4 v254, s[72:75], s54 offen  lds\n"
    "v_mfma_f32_16x16x32_bf16 v[72:75], a[232:235], a[136:139], v[72:75]\n"
    "s_addk_i32 m0, 1024\n"
    "s_addk_i32 s54, 16384\n"
    "v_mfma_f32_16x16x32_bf16 v[104:107], a[232:235], a[140:143], v[104:107]\n"
    "buffer_load_dwordx4 v254, s[72:75], s54 offen  lds\n"
    "v_mfma_f32_16x16x32_bf16 v[136:139], a[232:235], a[144:147], v[136:139]\n"
    "s_addk_i32 m0, 1024\n"
    "s_addk_i32 s54, 16384\n"
    "v_mfma_f32_16x16x32_bf16 v[168:171], a[232:235], a[148:151], v[168:171]\n"
    "buffer_load_dwordx4 v254, s[72:75], s54 offen  lds\n"
    "v_mfma_f32_16x16x32_bf16 v[200:203], a[232:235], a[152:155], v[200:203]\n"
    "s_addk_i32 m0, 1024\n"
    "s_addk_i32 s54, 16384\n"
    "v_mfma_f32_16x16x32_bf16 v[232:235], a[232:235], a[156:159], v[232:235]\n"
    "buffer_load_dwordx4 v254, s[72:75], s54 offen  lds\n"
    "v_mfma_f32_16x16x32_bf16 v[236:239], a[236:239], a[156:159], v[236:239]\n"
    "s_waitcnt vmcnt(13)\n"
    "ds_read_b64_tr_b16 a[192:193], v252 offset:4096\n"
    "ds_read_b64_tr_b16 a[194:195], v252 offset:4608\n"
    "s_addk_i32 m0, 1024\n"
    "s_addk_i32 s54, 16384\n"
    "buffer_load_dwordx4 v254, s[72:75], s54 offen  lds\n"
    "ds_read_b64_tr_b16 a[196:197], v252 offset:4104\n"
    "ds_read_b64_tr_b16 a[198:199], v252 offset:4616\n"
    "s_bitcmp1_b32 s85, 0\n"
    "s_cbranch_scc1 CP0_%=\n"
    "CR0_%=:\n"
    "v_mfma_f32_16x16x32_bf16 v[204:207], a[236:239], a[152:155], v[204:207]\n"
    "s_addk_i32 m0, 1024\n"
    "s_addk_i32 s54, 16384\n"
    "v_mfma_f32_16x16x32_bf16 v[172:175], a[236:239], a[148:151], v[172:175]\n"
    "buffer_load_dwordx4 v254, s[72:75], s54 offen  lds\n"
    "v_mfma_f32_16x16x32_bf16 v[140:143], a[236:239], a[144:147], v[140:143]\n"
    "ds_read_b64_tr_b16 a[200:201], v253 offset:4096\n"
    "ds_read_b64_tr_b16 a[202:203], v253 offset:4608\n"
    "v_mfma_f32_16x16x32_bf16 v[108:111], a[236:239], a[140:143], v[108:111]\n"
    "s_addk_i32 m0, 1024\n"
    "s_addk_i32 s54, 16384\n"
    "v_mfma_f32_16x16x32_bf16 v[76:79], a[236:239], a[136:139], v[76:79]\n"
    "buffer_load_dwordx4 v254, s[72:75], s54 offen  lds\n"
    "v_mfma_f32_16x16x32_bf16 v[44:47], a[236:239], a[132:135], v[44:47]\n"
    "ds_read_b64_tr_b16 a[204:205], v253 offset:4104\n"
    "ds_read_b64_tr_b16 a[206:207], v253 offset:4616\n"
    "v_mfma_f32_16x16x32_bf16 v[12:15], a[236:239], a[128:131], v[12:15]\n"
    "s_waitcnt lgkmcnt(6)\n"
    "s_xor_b32 s59, s66, s67\n"
    "s_addk_i32 s67, 128\n"
    "v_mfma_f32_16x16x32_bf16 v[0:3], a[192:195], a[160:163], v[0:3]\n"
    "s_cmpk_eq_u32 s67, 1536\n"
    "s_cselect_b32 s67, 0, s67\n"
    "s_add_u32 m0, s68, 81920\n"
    "v_mfma_f32_16x16x32_bf16 v[32:35], a[192:195], a[164:167], v[32:35]\n"
    "s_nop 0\n"
    "v_mfma_f32_16x16x32_bf16 v[64:67], a[192:195], a[168:171], v[64:67]\n"
    "buffer_load_dwordx4 v255, s[24:27], s59 offen nt lds\n"
    "v_mfma_f32_16x16x32_bf16 v[96:99], a[192:195], a[172:175], v[96:99]\n"
    "s_add_u32 m0, s68, 82944\n"
    "s_addk_i32 s59, 16384\n"
    "v_mfma_f32_16x16x32_bf16 v[128:131], a[192:195], a[176:179], v[128:131]\n"
    "buffer_load_dwordx4 v243, s[24:27], s59 offen nt lds\n"
    "s_add_u32 s82, s77, 3076\n"
    "s_and_b32 s82, s82, s86\n"
    "v_mfma_f32_16x16x32_bf16 v[160:163], a[192:195], a[180:183], v[160:163]\n"
    "buffer_load_dword v245, v240, s[80:83], s77 offen offset:3072 sc1\n"
    "v_lshrrev_b32 v246, 1, v240\n"
    "s_lshl_b32 s61, s65, 9\n"
    "s_mov_b32 s82, s87\n"
    "v_mfma_f32_16x16x32_bf16 v[192:195], a[192:195], a[184:187], v[192:195]\n"
    "buffer_load_dwordx2 v[246:247], v246, s[80:83], s61 offen sc1\n"
    "v_mov_b32 v248, 0\n"
    "v_mov_b32 v249, 0\n"
    "v_mov_b32 v250, 0\n"
    "v_mov_b32 v251, 0\n"
    "v_mfma_f32_16x16x32_bf16 v[224:227], a[192:195], a[188:191], v[224:227]\n"
    "s_waitcnt lgkmcnt(4)\n"
    "v_mfma_f32_16x16x32_bf16 v[228:231], a[196:199], a[188:191], v[228:231]\n"
    "buffer_store_dwordx4 v[248:251], v240, s[44:47], 0 offen sc1\n"
    "s_lshl_b32 s60, s65, 8\n"
    "s_sub_u32 s44, s44, s60\n"
    "s_subb_u32 s45, s45, 0\n"
    "s_lshl_b32 s60, s65, 4\n"
    "s_add_u32 s44, s44, s60\n"
    "s_addc_u32 s45, s45, 0\n"
    "s_mov_b32 s46, 4\n"
    "s_add_u32 m0, s68, 83968\n"
    "s_addk_i32 s59, 16384\n"
    "v_mfma_f32_16x16x32_bf16 v[196:199], a[196:199], a[184:187], v[196:199]\n"
    "buffer_load_dwordx4 v255, s[24:27], s59 offen nt lds\n"
    "s_add_u32 m0, s68, 84992\n"
    "s_addk_i32 s59, 16384\n"
    "v_mfma_f32_16x16x32_bf16 v[164:167], a[196:199], a[180:183], v[164:167]\n"
    "buffer_load_dwordx4 v243, s[24:27], s59 offen nt lds\n"
    "v_mfma_f32_16x16x32_bf16 v[132:135], a[196:199], a[176:179], v[132:135]\n"
    "v_mfma_f32_16x16x32_bf16 v[100:103], a[196:199], a[172:175], v[100:103]\n"
    "v_mfma_f32_16x16x32_bf16 v[68:71], a[196:199], a[168:171], v[68:71]\n"
    "v_mfma_f32_16x16x32_bf16 v[36:39], a[196:199], a[164:167], v[36:39]\n"
    "v_mfma_f32_16x16x32_bf16 v[4:7], a[196:199], a[160:163], v[4:7]\n"
    "s_waitcnt lgkmcnt(2)\n"
    "s_waitcnt vmcnt(15)\n"
    "ds_read_b64_tr_b16 a[208:209], v252 offset:32768\n"
    "v_mfma_f32_16x16x32_bf16 v[8:11], a[200:203], a[160:163], v[8:11]\n"
    "ds_read_b64_tr_b16 a[210:211], v252 offset:33280\n"
    "s_add_u32 s60, s77, 4096\n"
    "v_mfma_f32_16x16x32_bf16 v[40:43], a[200:203], a[164:167], v[40:43]\n"
    "s_add_u32 s82, s60, 4\n"
    "s_and_b32 s82, s82, s86\n"
    "v_mfma_f32_16x16x32_bf16 v[72:75], a[200:203], a[168:171], v[72:75]\n"
    "buffer_store_dword v248, v240, s[80:83], s60 offen sc1\n"
    "v_mfma_f32_16x16x32_bf16 v[104:107], a[200:203], a[172:175], v[104:107]\n"
    "ds_read_b64_tr_b16 a[212:213], v252 offset:36864\n"
    "ds_read_b64_tr_b16 a[214:215], v252 offset:37376\n"
    "v_mfma_f32_16x16x32_bf16 v[136:139], a[200:203], a[176:179], v[136:139]\n"
    "ds_read_b64_tr_b16 a[216:217], v252 offset:32776\n"
    "ds_read_b64_tr_b16 a[218:219], v252 offset:33288\n"
    "v_mfma_f32_16x16x32_bf16 v[168:171], a[200:203], a[180:183], v[168:171]\n"
    "ds_read_b64_tr_b16 a[220:221], v252 offset:36872\n"
    "ds_read_b64_tr_b16 a[222:223], v252 offset:37384\n"
    "v_mfma_f32_16x16x32_bf16 v[200:203], a[200:203], a[184:187], v[200:203]\n"
    "v_mfma_f32_16x16x32_bf16 v[232:235], a[200:203], a[188:191], v[232:235]\n"
    "s_waitcnt lgkmcnt(8)\n"
    "ds_read_b64_tr_b16 a[224:225], v253 offset:32768\n"
    "ds_read_b64_tr_b16 a[226:227], v253 offset:33280\n"
    "v_mfma_f32_16x16x32_bf16 v[236:239], a[204:207], a[188:191], v[236:239]\n"
    "v_mfma_f32_16x16x32_bf16 v[204:207], a[204:207], a[184:187], v[204:207]\n"
    "ds_read_b64_tr_b16 a[228:229], v253 offset:36864\n"
    "ds_read_b64_tr_b16 a[230:231], v253 offset:37376\n"
    "v_mfma_f32_16x16x32_bf16 v[172:175], a[204:207], a[180:183], v[172:175]\n"
    "v_mfma_f32_16x16x32_bf16 v[140:143], a[204:207], a[176:179], v[140:143]\n"
    "ds_read_b64_tr_b16 a[232:233], v253 offset:32776\n"
    "ds_read_b64_tr_b16 a[234:235], v253 offset:33288\n"
    "v_mfma_f32_16x16x32_bf16 v[108:111], a[204:207], a[172:175], v[108:111]\n"
    "v_mfma_f32_16x16x32_bf16 v[76:79], a[204:207], a[168:171], v[76:79]\n"
    "ds_read_b64_tr_b16 a[236:237], v253 offset:36872\n"
    "ds_read_b64_tr_b16 a[238:239], v253 offset:37384\n"
    "v_mfma_f32_16x16x32_bf16 v[44:47], a[204:207], a[164:167], v[44:47]\n"
    "v_mfma_f32_16x16x32_bf16 v[12:15], a[204:207], a[160:163], v[12:15]\n"
    "s_waitcnt lgkmcnt(12)\n"
    "v_lshrrev_b32 v250, 3, v240\n"
    "v_and_b32 v250, 14, v250\n"
    "v_mfma_f32_16x16x32_bf16 v[16:19], a[208:211], a[128:131], v[16:19]\n"
    "v_lshrrev_b32 v248, 4, v240\n"
    "v_and_b32 v248, 0x30, v248\n"
    "v_mfma_f32_16x16x32_bf16 v[16:19], a[212:215], a[160:163], v[16:19]\n"
    "v_or_b32 v250, v248, v250\n"
    "v_lshrrev_b32 v248, 1, v240\n"
    "v_mfma_f32_16x16x32_bf16 v[48:51], a[212:215], a[164:167], v[48:51]\n"
    "v_and_b32 v248, 64, v248\n"
    "v_or_b32 v250, v248, v250\n"
    "v_mfma_f32_16x16x32_bf16 v[48:51], a[208:211], a[132:135], v[48:51]\n"
    "s_add_u32 s60, s77, s65\n"
    "s_lshr_b32 s61, s60, 4\n"
    "s_min_u32 s61, s61, 5\n"
    "s_lshl_b32 s61, s61, 11\n"
    "s_add_u32 s22, s61, 2048\n"
    "s_bfe_u32 s61, s60, 0x10003\n"
    "s_lshl_b32 s61, s61, 10\n"
    "s_and_b32 s62, s60, 1\n"
    "s_lshl_b32 s62, s62, 9\n"
    "s_add_u32 s61, s61, s62\n"
    "s_bfe_u32 s62, s60, 0x20001\n"
    "s_lshl_b32 s62, s62, 7\n"
    "s_add_u32 s61, s61, s62\n"
    "v_add_u32 v250, s61, v250\n"
    "v_mfma_f32_16x16x32_bf16 v[80:83], a[208:211], a[136:139], v[80:83]\n"
    "buffer_load_ushort v248, v250, s[20:23], 0 offen\n"
    "v_mfma_f32_16x16x32_bf16 v[80:83], a[212:215], a[168:171], v[80:83]\n"
    "buffer_load_ushort v249, v250, s[20:23], 0 offen offset:2048\n"
    "v_add_u32 v250, 0x1000, v250\n"
    "v_mfma_f32_16x16x32_bf16 v[112:115], a[212:215], a[172:175], v[112:115]\n"
    "buffer_load_ushort v251, v250, s[20:23], 0 offen\n"
    "v_mfma_f32_16x16x32_bf16 v[112:115], a[208:211], a[140:143], v[112:115]\n"
    "buffer_load_ushort v244, v250, s[20:23], 0 offen offset:2048\n"
    "v_add_u32 v250, 0x1000, v250\n"
    "v_mfma_f32_16x16x32_bf16 v[144:147], a[208:211], a[144:147], v[144:147]\n"
    "v_mfma_f32_16x16x32_bf16 v[144:147], a[212:215], a[176:179], v[144:147]\n"
    "v_mfma_f32_16x16x32_bf16 v[176:179], a[212:215], a[180:183], v[176:179]\n"
    "v_mfma_f32_16x16x32_bf16 v[176:179], a[208:211], a[148:151], v[176:179]\n"
    "v_mfma_f32_16x16x32_bf16 v[208:211], a[208:211], a[152:155], v[208:211]\n"
    "v_mfma_f32_16x16x32_bf16 v[208:211], a[212:215], a[184:187], v[208:211]\n"
    "v_mfma_f32_16x16x32_bf16 a[240:243], a[212:215], a[188:191], a[240:243]\n"
    "v_mfma_f32_16x16x32_bf16 a[240:243], a[208:211], a[156:159], a[240:243]\n"
    "s_waitcnt lgkmcnt(8)\n"
    "v_mfma_f32_16x16x32_bf16 a[244:247], a[216:219], a[156:159], a[244:247]\n"
    "v_mfma_f32_16x16x32_bf16 a[244:247], a[220:223], a[188:191], a[244:247]\n"
    "v_mfma_f32_16x16x32_bf16 v[212:215], a[220:223], a[184:187], v[212:215]\n"
    "v_mfma_f32_16x16x32_bf16 v[212:215], a[216:219], a[152:155], v[212:215]\n"
    "v_mfma_f32_16x16x32_bf16 v[180:183], a[216:219], a[148:151], v[180:183]\n"
    "v_mfma_f32_16x16x32_bf16 v[180:183], a[220:223], a[180:183], v[180:183]\n"
    "v_mfma_f32_16x16x32_bf16 v[148:151], a[220:223], a[176:179], v[148:151]\n"
    "v_mfma_f32_16x16x32_bf16 v[148:151], a[216:219], a[144:147], v[148:151]\n"
    "s_xor_b32 s59, s66, s67\n"
    "s_addk_i32 s67, 128\n"
    "s_cmpk_eq_u32 s67, 1536\n"
    "v_mfma_f32_16x16x32_bf16 v[116:119], a[216:219], a[140:143], v[116:119]\n"
    "s_cselect_b32 s67, 0, s67\n"
    "s_add_u32 m0, s68, 98304\n"
    "s_nop 0\n"
    "v_mfma_f32_16x16x32_bf16 v[116:119], a[220:223], a[172:175], v[116:119]\n"
    "buffer_load_dwordx4 v255, s[24:27], s59 offen nt lds\n"
    "v_mfma_f32_16x16x32_bf16 v[84:87], a[220:223], a[168:171], v[84:87]\n"
    "v_mfma_f32_16x16x32_bf16 v[84:87], a[216:219], a[136:139], v[84:87]\n"
    "s_add_u32 m0, s68, 99328\n"
    "s_addk_i32 s59, 16384\n"
    "v_mfma_f32_16x16x32_bf16 v[52:55], a[216:219], a[132:135], v[52:55]\n"
    "buffer_load_dwordx4 v243, s[24:27], s59 offen nt lds\n"
    "v_mfma_f32_16x16x32_bf16 v[52:55], a[220:223], a[164:167], v[52:55]\n"
    "v_mfma_f32_16x16x32_bf16 v[20:23], a[220:223], a[160:163], v[20:23]\n"
    "v_mfma_f32_16x16x32_bf16 v[20:23], a[216:219], a[128:131], v[20:23]\n"
    "s_waitcnt lgkmcnt(4)\n"
    "v_add_u32 v252, 0x20000, v252\n"
    "v_add_u32 v253, 0x20000, v253\n"
    "v_mfma_f32_16x16x32_bf16 v[24:27], a[224:227], a[128:131], v[24:27]\n"
    "s_waitcnt vmcnt(14)\n"
    "ds_read_b64_tr_b16 a[192:193], v252 offset:0\n"
    "ds_read_b64_tr_b16 a[194:195], v252 offset:512\n"
    "v_mfma_f32_16x16x32_bf16 v[24:27], a[228:231], a[160:163], v[24:27]\n"
    "s_add_u32 m0, s68, 100352\n"
    "s_addk_i32 s59, 16384\n"
    "v_mfma_f32_16x16x32_bf16 v[56:59], a[228:231], a[164:167], v[56:59]\n"
    "buffer_load_dwordx4 v255, s[24:27], s59 offen nt lds\n"
    "v_mfma_f32_16x16x32_bf16 v[56:59], a[224:227], a[132:135], v[56:59]\n"
    "ds_read_b64_tr_b16 a[196:197], v252 offset:4096\n"
    "ds_read_b64_tr_b16 a[198:199], v252 offset:4608\n"
    "v_mfma_f32_16x16x32_bf16 v[88:91], a[224:227], a[136:139], v[88:91]\n"
    "ds_read_b64_tr_b16 a[200:201], v252 offset:8\n"
    "ds_read_b64_tr_b16 a[202:203], v252 offset:520\n"
    "v_mfma_f32_16x16x32_bf16 v[88:91], a[228:231], a[168:171], v[88:91]\n"
    "s_add_u32 m0, s68, 101376\n"
    "s_addk_i32 s59, 16384\n"
    "v_mfma_f32_16x16x32_bf16 v[120:123], a[228:231], a[172:175], v[120:123]\n"
    "buffer_load_dwordx4 v243, s[24:27], s59 offen nt lds\n"
    "v_mfma_f32_16x16x32_bf16 v[120:123], a[224:227], a[140:143], v[120:123]\n"
    "ds_read_b64_tr_b16 a[204:205], v252 offset:4104\n"
    "ds_read_b64_tr_b16 a[206:207], v252 offset:4616\n"
    "v_mfma_f32_16x16x32_bf16 v[152:155], a[224:227], a[144:147], v[152:155]\n"
    "ds_read_b64_tr_b16 a[208:209], v253 offset:0\n"
    "ds_read_b64_tr_b16 a[210:211], v253 offset:512\n"
    "v_mfma_f32_16x16x32_bf16 v[152:155], a[228:231], a[176:179], v[152:155]\n"
    "s_waitcnt lgkmcnt(2)\n"
    "s_add_u32 m0, s79, 0\n"
    "s_add_u32 s54, s57, 512\n"
    "v_mfma_f32_16x16x32_bf16 v[184:187], a[228:231], a[180:183], v[184:187]\n"
    "buffer_load_dwordx4 v254, s[72:75], s54 offen  lds\n"
    "v_mfma_f32_16x16x32_bf16 v[184:187], a[224:227], a[148:151], v[184:187]\n"
    "ds_read_b64_tr_b16 a[212:213], v253 offset:4096\n"
    "ds_read_b64_tr_b16 a[214:215], v253 offset:4608\n"
    "v_mfma_f32_16x16x32_bf16 v[216:219], a[224:227], a[152:155], v[216:219]\n"
    "ds_read_b64_tr_b16 a[216:217], v253 offset:8\n"
    "ds_read_b64_tr_b16 a[218:219], v253 offset:520\n"
    "v_mfma_f32_16x16x32_bf16 v[216:219], a[228:231], a[184:187], v[216:219]\n"
    "s_addk_i32 m0, 1024\n"
    "s_addk_i32 s54, 16384\n"
    "buffer_load_dwordx4 v254, s[72:75], s54 offen  lds\n"
    "s_waitcnt vmcnt(11)\n"
    "s_barrier\n"
    "v_mfma_f32_16x16x32_bf16 a[248:251], a[228:231], a[188:191], a[248:251]\n"
    "ds_read_b64_tr_b16 a[220:221], v253 offset:4104\n"
    "ds_read_b64_tr_b16 a[222:223], v253 offset:4616\n"
    "v_mfma_f32_16x16x32_bf16 a[248:251], a[224:227], a[156:159], a[248:251]\n"
    "s_addk_i32 m0, 1024\n"
    "s_addk_i32 s54, 16384\n"
    "v_mfma_f32_16x16x32_bf16 a[252:255], a[232:235], a[156:159], a[252:255]\n"
    "buffer_load_dwordx4 v254, s[72:75], s54 offen  lds\n"
    "v_mfma_f32_16x16x32_bf16 a[252:255], a[236:239], a[188:191], a[252:255]\n"
    "v_mfma_f32_16x16x32_bf16 v[220:223], a[236:239], a[184:187], v[220:223]\n"
    "ds_read_b128 a[156:159], v241 offset:30720\n"
    "v_mfma_f32_16x16x32_bf16 v[220:223], a[232:235], a[152:155], v[220:223]\n"
    "s_addk_i32 m0, 1024\n"
    "s_addk_i32 s54, 16384\n"
    "v_mfma_f32_16x16x32_bf16 v[188:191], a[232:235], a[148:151], v[188:191]\n"
    "buffer_load_dwordx4 v254, s[72:75], s54 offen  lds\n"
    "v_mfma_f32_16x16x32_bf16 v[188:191], a[236:239], a[180:183], v[188:191]\n"
    "ds_read_b128 a[188:191], v242 offset:30720\n"
    "ds_read_b128 a[184:187], v242 offset:28672\n"
    "v_mfma_f32_16x16x32_bf16 v[156:159], a[236:239], a[176:179], v[156:159]\n"
    "ds_read_b128 a[152:155], v241 offset:28672\n"
    "ds_read_b128 a[148:151], v241 offset:26624\n"
    "v_mfma_f32_16x16x32_bf16 v[156:159], a[232:235], a[144:147], v[156:159]\n"
    "s_addk_i32 m0, 1024\n"
    "s_addk_i32 s54, 16384\n"
    "v_mfma_f32_16x16x32_bf16 v[124:127], a[232:235], a[140:143], v[124:127]\n"
    "buffer_load_dwordx4 v254, s[72:75], s54 offen  lds\n"
    "v_mfma_f32_16x16x32_bf16 v[124:127], a[236:239], a[172:175], v[124:127]\n"
    "ds_read_b128 a[180:183], v242 offset:26624\n"
    "ds_read_b128 a[176:179], v242 offset:24576\n"
    "v_mfma_f32_16x16x32_bf16 v[92:95], a[236:239], a[168:171], v[92:95]\n"
    "ds_read_b128 a[144:147], v241 offset:24576\n"
    "ds_read_b128 a[140:143], v241 offset:22528\n"
    "v_mfma_f32_16x16x32_bf16 v[92:95], a[232:235], a[136:139], v[92:95]\n"
    "s_addk_i32 m0, 1024\n"
    "s_addk_i32 s54, 16384\n"
    "v_mfma_f32_16x16x32_bf16 v[60:63], a[232:235], a[132:135], v[60:63]\n"
    "buffer_load_dwordx4 v254, s[72:75], s54 offen  lds\n"
    "v_mfma_f32_16x16x32_bf16 v[60:63], a[236:239], a[164:167], v[60:63]\n"
    "ds_read_b128 a[172:175], v242 offset:22528\n"
    "ds_read_b128 a[168:171], v242 offset:20480\n"
    "v_mfma_f32_16x16x32_bf16 v[28:31], a[236:239], a[160:163], v[28:31]\n"
    "ds_read_b128 a[136:139], v241 offset:20480\n"
    "ds_read_b128 a[132:135], v241 offset:18432\n"
    "v_mfma_f32_16x16x32_bf16 v[28:31], a[232:235], a[128:131], v[28:31]\n"
    "s_waitcnt lgkmcnt(11)\n"
    "s_addk_i32 m0, 1024\n"
    "s_addk_i32 s54, 16384\n"
    "v_mfma_f32_16x16x32_bf16 v[224:227], a[192:195], a[156:159], v[224:227]\n"
    "buffer_load_dwordx4 v254, s[72:75], s54 offen  lds\n"
    "v_mfma_f32_16x16x32_bf16 v[224:227], a[196:199], a[188:191], v[224:227]\n"
    "s_waitcnt lgkmcnt(9)\n"
    "ds_read_b128 a[164:167], v242 offset:18432\n"
    "ds_read_b128 a[160:163], v242 offset:16384\n"
    "v_mfma_f32_16x16x32_bf16 v[192:195], a[196:199], a[184:187], v[192:195]\n"
    "ds_read_b128 a[128:131], v241 offset:16384\n"
    "v_mfma_f32_16x16x32_bf16 v[192:195], a[192:195], a[152:155], v[192:195]\n"
    "s_waitcnt lgkmcnt(10)\n"
    "s_addk_i32 m0, 1024\n"
    "s_addk_i32 s54, 16384\n"
    "v_mfma_f32_16x16x32_bf16 v[160:163], a[192:195], a[148:151], v[160:163]\n"
    "buffer_load_dwordx4 v254, s[72:75], s54 offen  lds\n"
    "v_mfma_f32_16x16x32_bf16 v[160:163], a[196:199], a[180:183], v[160:163]\n"
    "s_waitcnt lgkmcnt(8)\n"
    "s_add_u32 s57, s57, 131072\n"
    "s_cmp_ge_u32 s57, 1572864\n"
    "v_mfma_f32_16x16x32_bf16 v[128:131], a[196:199], a[176:179], v[128:131]\n"
    "s_cselect_b32 s57, s70, s57\n"
    "v_mfma_f32_16x16x32_bf16 v[128:131], a[192:195], a[144:147], v[128:131]\n"
    "s_waitcnt lgkmcnt(6)\n"
    "s_add_u32 m0, s79, 32768\n"
    "s_add_u32 s54, s57, 0\n"
    "v_mfma_f32_16x16x32_bf16 v[96:99], a[192:195], a[140:143], v[96:99]\n"
    "buffer_load_dwordx4 v254, s[72:75], s54 offen  lds\n"
    "v_mfma_f32_16x16x32_bf16 v[96:99], a[196:199], a[172:175], v[96:99]\n"
    "s_waitcnt lgkmcnt(4)\n"
    "v_mfma_f32_16x16x32_bf16 v[64:67], a[196:199], a[168:171], v[64:67]\n"
    "v_mfma_f32_16x16x32_bf16 v[64:67], a[192:195], a[136:139], v[64:67]\n"
    "s_waitcnt lgkmcnt(2)\n"
    "s_addk_i32 m0, 1024\n"
    "s_addk_i32 s54, 16384\n"
    "v_mfma_f32_16x16x32_bf16 v[32:35], a[192:195], a[132:135], v[32:35]\n"
    "buffer_load_dwordx4 v254, s[72:75], s54 offen  lds\n"
    "v_mfma_f32_16x16x32_bf16 v[32:35], a[196:199], a[164:167], v[32:35]\n"
    "s_waitcnt lgkmcnt(0)\n"
    "v_mfma_f32_16x16x32_bf16 v[0:3], a[196:199], a[160:163], v[0:3]\n"
    "v_mfma_f32_16x16x32_bf16 v[0:3], a[192:195], a[128:131], v[0:3]\n"
    "s_addk_i32 m0, 1024\n"
    "s_addk_i32 s54, 16384\n"
    "v_mfma_f32_16x16x32_bf16 v[4:7], a[200:203], a[128:131], v[4:7]\n"
    "buffer_load_dwordx4 v254, s[72:75], s54 offen  lds\n"
    "v_mfma_f32_16x16x32_bf16 v[4:7], a[204:207], a[160:163], v[4:7]\n"
    "v_mfma_f32_16x16x32_bf16 v[36:39], a[204:207], a[164:167], v[36:39]\n"
    "v_mfma_f32_16x16x32_bf16 v[36:39], a[200:203], a[132:135], v[36:39]\n"
    "s_addk_i32 m0, 1024\n"
    "s_addk_i32 s54, 16384\n"
    "v_mfma_f32_16x16x32_bf16 v[68:71], a[200:203], a[136:139], v[68:71]\n"
    "buffer_load_dwordx4 v254, s[72:75], s54 offen  lds\n"
    "v_mfma_f32_16x16x32_bf16 v[68:71], a[204:207], a[168:171], v[68:71]\n"
    "v_mfma_f32_16x16x32_bf16 v[100:103], a[204:207], a[172:175], v[100:103]\n"
    "v_mfma_f32_16x16x32_bf16 v[100:103], a[200:203], a[140:143], v[100:103]\n"
    "s_addk_i32 m0, 1024\n"
    "s_addk_i32 s54, 16384\n"
    "v_mfma_f32_16x16x32_bf16 v[132:135], a[200:203], a[144:147], v[132:135]\n"
    "buffer_load_dwordx4 v254, s[72:75], s54 offen  lds\n"
    "s_bitcmp1_b32 s85, 1\n"
    "s_cbranch_scc1 CP1_%=\n"
    "CR1_%=:\n"
    "v_mfma_f32_16x16x32_bf16 v[132:135], a[204:207], a[176:179], v[132:135]\n"
    "v_mfma_f32_16x16x32_bf16 v[164:167], a[204:207], a[180:183], v[164:167]\n"
    "v_mfma_f32_16x16x32_bf16 v[164:167], a[200:203], a[148:151], v[164:167]\n"
    "s_addk_i32 m0, 1024\n"
    "s_addk_i32 s54, 16384\n"
    "v_mfma_f32_16x16x32_bf16 v[196:199], a[200:203], a[152:155], v[196:199]\n"
    "buffer_load_dwordx4 v254, s[72:75], s54 offen  lds\n"
    "v_mfma_f32_16x16x32_bf16 v[196:199], a[204:207], a[184:187], v[196:199]\n"
    "v_mfma_f32_16x16x32_bf16 v[228:231], a[204:207], a[188:191], v[228:231]\n"
    "v_mfma_f32_16x16x32_bf16 v[228:231], a[200:203], a[156:159], v[228:231]\n"
    "v_add_u32 v252, 0xfffe0000, v252\n"
    "v_add_u32 v253, 0xfffe0000, v253\n"
    "v_mfma_f32_16x16x32_bf16 v[232:235], a[208:211], a[156:159], v[232:235]\n"
    "s_waitcnt vmcnt(6)\n"
    "ds_read_b64_tr_b16 a[224:225], v252 offset:0\n"
    "ds_read_b64_tr_b16 a[226:227], v252 offset:512\n"
    "v_mfma_f32_16x16x32_bf16 v[232:235], a[212:215], a[188:191], v[232:235]\n"
    "s_addk_i32 m0, 1024\n"
    "s_addk_i32 s54, 16384\n"
    "v_mfma_f32_16x16x32_bf16 v[200:203], a[212:215], a[184:187], v[200:203]\n"
    "buffer_load_dwordx4 v254, s[72:75], s54 offen  lds\n"
    "v_lshlrev_b32 v248, 16, v248\n"
    "v_lshlrev_b32 v249, 16, v249\n"
    "v_add_f32 v248, v249, v248\n"
    "v_lshlrev_b32 v251, 16, v251\n"
    "v_add_f32 v248, v251, v248\n"
    "v_lshlrev_b32 v244, 16, v244\n"
    "v_add_f32 v248, v244, v248\n"
    "v_mfma_f32_16x16x32_bf16 v[200:203], a[208:211], a[152:155], v[200:203]\n"
    "buffer_load_ushort v249, v250, s[20:23], 0 offen\n"
    "v_mfma_f32_16x16x32_bf16 v[168:171], a[208:211], a[148:151], v[168:171]\n"
    "buffer_load_ushort v251, v250, s[20:23], 0 offen offset:2048\n"
    "v_add_u32 v250, 0x1000, v250\n"
    "ds_read_b64_tr_b16 a[228:229], v252 offset:4096\n"
    "ds_read_b64_tr_b16 a[230:231], v252 offset:4608\n"
    "v_mfma_f32_16x16x32_bf16 v[168:171], a[212:215], a[180:183], v[168:171]\n"
    "ds_read_b64_tr_b16 a[232:233], v252 offset:8\n"
    "ds_read_b64_tr_b16 a[234:235], v252 offset:520\n"
    "v_mfma_f32_16x16x32_bf16 v[136:139], a[212:215], a[176:179], v[136:139]\n"
    "s_addk_i32 m0, 1024\n"
    "s_addk_i32 s54, 16384\n"
    "v_mfma_f32_16x16x32_bf16 v[136:139], a[208:211], a[144:147], v[136:139]\n"
    "buffer_load_dwordx4 v254, s[72:75], s54 offen  lds\n"
    "v_mfma_f32_16x16x32_bf16 v[104:107], a[208:211], a[140:143], v[104:107]\n"
    "ds_read_b64_tr_b16 a[236:237], v252 offset:4104\n"
    "ds_read_b64_tr_b16 a[238:239], v252 offset:4616\n"
    "v_mfma_f32_16x16x32_bf16 v[104:107], a[212:215], a[172:175], v[104:107]\n"
    "ds_read_b64_tr_b16 a[192:193], v253 offset:0\n"
    "ds_read_b64_tr_b16 a[194:195], v253 offset:512\n"
    "v_mfma_f32_16x16x32_bf16 v[72:75], a[212:215], a[168:171], v[72:75]\n"
    "s_add_u32 m0, s68, 114688\n"
    "s_add_u32 s60, s78, 0\n"
    "s_or_b32 s60, s60, s71\n"
    "v_mfma_f32_16x16x32_bf16 v[72:75], a[208:211], a[136:139], v[72:75]\n"
    "buffer_load_dwordx4 v254, s[72:75], s60 offen nt lds\n"
    "v_mfma_f32_16x16x32_bf16 v[40:43], a[208:211], a[132:135], v[40:43]\n"
    "ds_read_b64_tr_b16 a[196:197], v253 offset:4096\n"
    "ds_read_b64_tr_b16 a[198:199], v253 offset:4608\n"
    "v_mfma_f32_16x16x32_bf16 v[40:43], a[212:215], a[164:167], v[40:43]\n"
    "ds_read_b64_tr_b16 a[200:201], v253 offset:8\n"
    "ds_read_b64_tr_b16 a[202:203], v253 offset:520\n"
    "v_mfma_f32_16x16x32_bf16 v[8:11], a[212:215], a[160:163], v[8:11]\n"
    "s_add_u32 m0, s68, 115712\n"
    "s_add_u32 s60, s78, 16384\n"
    "s_or_b32 s60, s60, s71\n"
    "v_mfma_f32_16x16x32_bf16 v[8:11], a[208:211], a[128:131], v[8:11]\n"
    "buffer_load_dwordx4 v254, s[72:75], s60 offen nt lds\n"
    "v_mfma_f32_16x16x32_bf16 v[12:15], a[216:219], a[128:131], v[12:15]\n"
    "ds_read_b64_tr_b16 a[204:205], v253 offset:4104\n"
    "ds_read_b64_tr_b16 a[206:207], v253 offset:4616\n"
    "v_mfma_f32_16x16x32_bf16 v[12:15], a[220:223], a[160:163], v[12:15]\n"
    "s_add_u32 m0, s68, 116736\n"
    "s_add_u32 s60, s78, 32768\n"
    "s_or_b32 s60, s60, s71\n"
    "v_mfma_f32_16x16x32_bf16 v[44:47], a[220:223], a[164:167], v[44:47]\n"
    "buffer_load_dwordx4 v254, s[72:75], s60 offen nt lds\n"
    "v_mfma_f32_16x16x32_bf16 v[44:47], a[216:219], a[132:135], v[44:47]\n"
    "s_add_u32 m0, s68, 117760\n"
    "s_add_u32 s60, s78, 49152\n"
    "s_or_b32 s60, s60, s71\n"
    "v_mfma_f32_16x16x32_bf16 v[76:79], a[216:219], a[136:139], v[76:79]\n"
    "buffer_load_dwordx4 v254, s[72:75], s60 offen nt lds\n"
    "v_mfma_f32_16x16x32_bf16 v[76:79], a[220:223], a[168:171], v[76:79]\n"
    "v_mfma_f32_16x16x32_bf16 v[108:111], a[220:223], a[172:175], v[108:111]\n"
    "v_mfma_f32_16x16x32_bf16 v[108:111], a[216:219], a[140:143], v[108:111]\n"
    "s_xor_b32 s59, s66, s67\n"
    "s_addk_i32 s67, 128\n"
    "s_cmpk_eq_u32 s67, 1536\n"
    "v_mfma_f32_16x16x32_bf16 v[140:143], a[216:219], a[144:147], v[140:143]\n"
    "s_cselect_b32 s67, 0, s67\n"
    "s_add_u32 m0, s68, 65536\n"
    "s_nop 0\n"
    "v_mfma_f32_16x16x32_bf16 v[140:143], a[220:223], a[176:179], v[140:143]\n"
    "buffer_load_dwordx4 v255, s[24:27], s59 offen nt lds\n"
    "v_mfma_f32_16x16x32_bf16 v[172:175], a[220:223], a[180:183], v[172:175]\n"
    "v_mfma_f32_16x16x32_bf16 v[172:175], a[216:219], a[148:151], v[172:175]\n"
    "s_add_u32 m0, s68, 66560\n"
    "s_addk_i32 s59, 16384\n"
    "v_mfma_f32_16x16x32_bf16 v[204:207], a[216:219], a[152:155], v[204:207]\n"
    "buffer_load_dwordx4 v243, s[24:27], s59 offen nt lds\n"
    "v_mfma_f32_16x16x32_bf16 v[204:207], a[220:223], a[184:187], v[204:207]\n"
    "v_mfma_f32_16x16x32_bf16 v[236:239], a[220:223], a[188:191], v[236:239]\n"
    "v_mfma_f32_16x16x32_bf16 v[236:239], a[216:219], a[156:159], v[236:239]\n"
    "s_waitcnt lgkmcnt(12)\n"
    "s_add_u32 m0, s68, 67584\n"
    "s_addk_i32 s59, 16384\n"
    "v_mfma_f32_16x16x32_bf16 a[240:243], a[224:227], a[156:159], a[240:243]\n"
    "buffer_load_dwordx4 v255, s[24:27], s59 offen nt lds\n"
    "v_mfma_f32_16x16x32_bf16 a[240:243], a[228:231], a[188:191], a[240:243]\n"
    "v_add_u32 v245, 1, v245\n"
    "v_add_u32 v246, 1, v246\n"
    "v_mfma_f32_16x16x32_bf16 v[208:211], a[228:231], a[184:187], v[208:211]\n"
    "v_readfirstlane_b32 s88, v245\n"
    "v_lshrrev_b32 v247, 2, v240\n"
    "v_mfma_f32_16x16x32_bf16 v[208:211], a[224:227], a[152:155], v[208:211]\n"
    "s_lshl_b32 s61, s65, 8\n"
    "s_mov_b32 s82, s87\n"
    "v_mfma_f32_16x16x32_bf16 v[176:179], a[224:227], a[148:151], v[176:179]\n"
    "buffer_store_dword v246, v247, s[80:83], s61 offen offset:2048 sc1\n"
    "v_mfma_f32_16x16x32_bf16 v[176:179], a[228:231], a[180:183], v[176:179]\n"
    "s_add_u32 m0, s68, 68608\n"
    "s_addk_i32 s59, 16384\n"
    "v_mfma_f32_16x16x32_bf16 v[144:147], a[228:231], a[176:179], v[144:147]\n"
    "buffer_load_dwordx4 v243, s[24:27], s59 offen nt lds\n"
    "v_mfma_f32_16x16x32_bf16 v[144:147], a[224:227], a[144:147], v[144:147]\n"
    "s_add_u32 m0, s79, 131072\n"
    "s_add_u32 s54, s57, 512\n"
    "v_mfma_f32_16x16x32_bf16 v[112:115], a[224:227], a[140:143], v[112:115]\n"
    "buffer_load_dwordx4 v254, s[72:75], s54 offen  lds\n"
    "v_mfma_f32_16x16x32_bf16 v[112:115], a[228:231], a[172:175], v[112:115]\n"
    "v_mfma_f32_16x16x32_bf16 v[80:83], a[228:231], a[168:171], v[80:83]\n"
    "v_mfma_f32_16x16x32_bf16 v[80:83], a[224:227], a[136:139], v[80:83]\n"
    "s_addk_i32 m0, 1024\n"
    "s_addk_i32 s54, 16384\n"
    "v_mfma_f32_16x16x32_bf16 v[48:51], a[224:227], a[132:135], v[48:51]\n"
    "buffer_load_dwordx4 v254, s[72:75], s54 offen  lds\n"
    "v_mfma_f32_16x16x32_bf16 v[48:51], a[228:231], a[164:167], v[48:51]\n"
    "v_mfma_f32_16x16x32_bf16 v[16:19], a[228:231], a[160:163], v[16:19]\n"
    "v_mfma_f32_16x16x32_bf16 v[16:19], a[224:227], a[128:131], v[16:19]\n"
    "s_waitcnt lgkmcnt(8)\n"
    "s_addk_i32 m0, 1024\n"
    "s_addk_i32 s54, 16384\n"
    "v_mfma_f32_16x16x32_bf16 v[20:23], a[232:235], a[128:131], v[20:23]\n"
    "buffer_load_dwordx4 v254, s[72:75], s54 offen  lds\n"
    "v_mfma_f32_16x16x32_bf16 v[20:23], a[236:239], a[160:163], v[20:23]\n"
    "v_mfma_f32_16x16x32_bf16 v[52:55], a[236:239], a[164:167], v[52:55]\n"
    "v_mfma_f32_16x16x32_bf16 v[52:55], a[232:235], a[132:135], v[52:55]\n"
    "s_addk_i32 m0, 1024\n"
    "s_addk_i32 s54, 16384\n"
    "v_mfma_f32_16x16x32_bf16 v[84:87], a[232:235], a[136:139], v[84:87]\n"
    "buffer_load_dwordx4 v254, s[72:75], s54 offen  lds\n"
    "v_mfma_f32_16x16x32_bf16 v[84:87], a[236:239], a[168:171], v[84:87]\n"
    "v_mfma_f32_16x16x32_bf16 v[116:119], a[236:239], a[172:175], v[116:119]\n"
    "v_mfma_f32_16x16x32_bf16 v[116:119], a[232:235], a[140:143], v[116:119]\n"
    "s_addk_i32 m0, 1024\n"
    "s_addk_i32 s54, 16384\n"
    "v_mfma_f32_16x16x32_bf16 v[148:151], a[232:235], a[144:147], v[148:151]\n"
    "buffer_load_dwordx4 v254, s[72:75], s54 offen  lds\n"
    "v_mfma_f32_16x16x32_bf16 v[148:151], a[236:239], a[176:179], v[148:151]\n"
    "v_mfma_f32_16x16x32_bf16 v[180:183], a[236:239], a[180:183], v[180:183]\n"
    "v_mfma_f32_16x16x32_bf16 v[180:183], a[232:235], a[148:151], v[180:183]\n"
    "s_addk_i32 m0, 1024\n"
    "s_addk_i32 s54, 16384\n"
    "v_mfma_f32_16x16x32_bf16 v[212:215], a[232:235], a[152:155], v[212:215]\n"
    "buffer_load_dwordx4 v254, s[72:75], s54 offen  lds\n"
    "v_mfma_f32_16x16x32_bf16 v[212:215], a[236:239], a[184:187], v[212:215]\n"
    "v_mfma_f32_16x16x32_bf16 a[244:247], a[236:239], a[188:191], a[244:247]\n"
    "v_mfma_f32_16x16x32_bf16 a[244:247], a[232:235], a[156:159], a[244:247]\n"
    "s_waitcnt lgkmcnt(4)\n"
    "s_waitcnt vmcnt(15)\n"
    "ds_read_b64_tr_b16 a[208:209], v252 offset:32768\n"
    "v_mfma_f32_16x16x32_bf16 a[248:251], a[192:195], a[156:159], a[248:251]\n"
    "ds_read_b64_tr_b16 a[210:211], v252 offset:33280\n"
    "s_addk_i32 m0, 1024\n"
    "v_mfma_f32_16x16x32_bf16 a[248:251], a[196:199], a[188:191], a[248:251]\n"
    "s_addk_i32 s54, 16384\n"
    "v_mfma_f32_16x16x32_bf16 v[216:219], a[196:199], a[184:187], v[216:219]\n"
    "buffer_load_dwordx4 v254, s[72:75], s54 offen  lds\n"
    "v_mfma_f32_16x16x32_bf16 v[216:219], a[192:195], a[152:155], v[216:219]\n"
    "ds_read_b64_tr_b16 a[212:213], v252 offset:36864\n"
    "ds_read_b64_tr_b16 a[214:215], v252 offset:37376\n"
    "v_mfma_f32_16x16x32_bf16 v[184:187], a[192:195], a[148:151], v[184:187]\n"
    "ds_read_b64_tr_b16 a[216:217], v252 offset:32776\n"
    "ds_read_b64_tr_b16 a[218:219], v252 offset:33288\n"
    "v_mfma_f32_16x16x32_bf16 v[184:187], a[196:199], a[180:183], v[184:187]\n"
    "s_addk_i32 m0, 1024\n"
    "s_addk_i32 s54, 16384\n"
    "v_mfma_f32_16x16x32_bf16 v[152:155], a[196:199], a[176:179], v[152:155]\n"
    "buffer_load_dwordx4 v254, s[72:75], s54 offen  lds\n"
    "v_mfma_f32_16x16x32_bf16 v[152:155], a[192:195], a[144:147], v[152:155]\n"
    "s_add_u32 s57, s57, 131072\n"
    "s_cmp_ge_u32 s57, 1572864\n"
    "s_cselect_b32 s57, s70, s57\n"
    "v_mfma_f32_16x16x32_bf16 v[120:123], a[192:195], a[140:143], v[120:123]\n"
    "ds_read_b64_tr_b16 a[220:221], v252 offset:36872\n"
    "ds_read_b64_tr_b16 a[222:223], v252 offset:37384\n"
    "v_mfma_f32_16x16x32_bf16 v[120:123], a[196:199], a[172:175], v[120:123]\n"
    "ds_read_b64_tr_b16 a[224:225], v253 offset:32768\n"
    "ds_read_b64_tr_b16 a[226:227], v253 offset:33280\n"
    "v_mfma_f32_16x16x32_bf16 v[88:91], a[196:199], a[168:171], v[88:91]\n"
    "s_waitcnt lgkmcnt(10)\n"
    "s_add_u32 m0, s79, 0\n"
    "s_add_u32 s54, s57, 0\n"
    "v_mfma_f32_16x16x32_bf16 v[88:91], a[192:195], a[136:139], v[88:91]\n"
    "buffer_load_dwordx4 v254, s[72:75], s54 offen  lds\n"
    "v_mfma_f32_16x16x32_bf16 v[56:59], a[192:195], a[132:135], v[56:59]\n"
    "ds_read_b64_tr_b16 a[228:229], v253 offset:36864\n"
    "ds_read_b64_tr_b16 a[230:231], v253 offset:37376\n"
    "v_mfma_f32_16x16x32_bf16 v[56:59], a[196:199], a[164:167], v[56:59]\n"
    "ds_read_b64_tr_b16 a[232:233], v253 offset:32776\n"
    "ds_read_b64_tr_b16 a[234:235], v253 offset:33288\n"
    "s_addk_i32 m0, 1024\n"
    "s_addk_i32 s54, 16384\n"
    "buffer_load_dwordx4 v254, s[72:75], s54 offen  lds\n"
    "s_barrier\n"
    "v_mfma_f32_16x16x32_bf16 v[24:27], a[196:199], a[160:163], v[24:27]\n"
    "ds_read_b64_tr_b16 a[236:237], v253 offset:36872\n"
    "ds_read_b64_tr_b16 a[238:239], v253 offset:37384\n"
    "v_mfma_f32_16x16x32_bf16 v[24:27], a[192:195], a[128:131], v[24:27]\n"
    "s_addk_i32 m0, 1024\n"
    "s_addk_i32 s54, 16384\n"
    "v_mfma_f32_16x16x32_bf16 v[28:31], a[200:203], a[128:131], v[28:31]\n"
    "buffer_load_dwordx4 v254, s[72:75], s54 offen  lds\n"
    "v_mfma_f32_16x16x32_bf16 v[28:31], a[204:207], a[160:163], v[28:31]\n"
    "s_add_u32 s60, s77, s65\n"
    "s_bfe_u32 s61, s60, 0x30001\n"
    "s_mul_i32 s61, s61, 6\n"
    "v_mfma_f32_16x16x32_bf16 v[60:63], a[204:207], a[164:167], v[60:63]\n"
    "s_lshr_b32 s62, s60, 4\n"
    "s_add_u32 s61, s61, s62\n"
    "s_lshl_b32 s61, s61, 1\n"
    "v_mfma_f32_16x16x32_bf16 v[60:63], a[200:203], a[132:135], v[60:63]\n"
    "s_and_b32 s62, s60, 1\n"
    "s_add_u32 s61, s61, s62\n"
    "s_lshl_b32 s61, s61, 10\n"
    "s_cmp_lt_u32 s60, 96\n"
    "s_cselect_b32 s61, s61, 98304\n"
    "v_lshlrev_b32 v249, 16, v249\n"
    "v_add_f32 v248, v249, v248\n"
    "v_lshlrev_b32 v251, 16, v251\n"
    "v_add_f32 v248, v251, v248\n"
    "v_add_f32 v248, 0xc3ffaa80, v248\n"
    "v_mfma_f32_16x16x32_bf16 v[92:95], a[200:203], a[136:139], v[92:95]\n"
    "buffer_store_dwordx4 v[248:251], v240, s[36:39], s61 offen sc1\n"
    "s_lshl_b32 s61, s64, 2\n"
    "s_add_u32 s61, s61, s65\n"
    "s_mul_i32 s55, s61, 12288\n"
    "s_mul_i32 s61, s61, 1572864\n"
    "s_mov_b64 s[20:21], s[6:7]\n"
    "s_add_u32 s20, s20, s61\n"
    "s_addc_u32 s21, s21, 0\n"
    "s_and_b32 s21, s21, 0xffff\n"
    "s_mov_b32 s22, 1572864\n"
    "v_mfma_f32_16x16x32_bf16 v[92:95], a[204:207], a[168:171], v[92:95]\n"
    "ds_read_b128 a[128:131], v241 offset:32768\n"
    "s_addk_i32 m0, 1024\n"
    "v_mfma_f32_16x16x32_bf16 v[124:127], a[204:207], a[172:175], v[124:127]\n"
    "s_addk_i32 s54, 16384\n"
    "v_mfma_f32_16x16x32_bf16 v[124:127], a[200:203], a[140:143], v[124:127]\n"
    "buffer_load_dwordx4 v254, s[72:75], s54 offen  lds\n"
    "ds_read_b128 a[160:163], v242 offset:32768\n"
    "ds_read_b128 a[164:167], v242 offset:34816\n"
    "v_mfma_f32_16x16x32_bf16 v[156:159], a[200:203], a[144:147], v[156:159]\n"
    "ds_read_b128 a[132:135], v241 offset:34816\n"
    "ds_read_b128 a[136:139], v241 offset:36864\n"
    "v_mfma_f32_16x16x32_bf16 v[156:159], a[204:207], a[176:179], v[156:159]\n"
    "s_addk_i32 m0, 1024\n"
    "s_addk_i32 s54, 16384\n"
    "v_mfma_f32_16x16x32_bf16 v[188:191], a[204:207], a[180:183], v[188:191]\n"
    "buffer_load_dwordx4 v254, s[72:75], s54 offen  lds\n"
    "ds_read_b128 a[168:171], v242 offset:36864\n"
    "v_mfma_f32_16x16x32_bf16 v[188:191], a[200:203], a[148:151], v[188:191]\n"
    "ds_read_b128 a[172:175], v242 offset:38912\n"
    "ds_read_b128 a[140:143], v241 offset:38912\n"
    "v_mfma_f32_16x16x32_bf16 v[220:223], a[200:203], a[152:155], v[220:223]\n"
    "ds_read_b128 a[144:147], v241 offset:40960\n"
    "s_addk_i32 m0, 1024\n"
    "v_mfma_f32_16x16x32_bf16 v[220:223], a[204:207], a[184:187], v[220:223]\n"
    "s_addk_i32 s54, 16384\n"
    "v_mfma_f32_16x16x32_bf16 a[252:255], a[204:207], a[188:191], a[252:255]\n"
    "buffer_load_dwordx4 v254, s[72:75], s54 offen  lds\n"
    "ds_read_b128 a[176:179], v242 offset:40960\n"
    "v_mfma_f32_16x16x32_bf16 a[252:255], a[200:203], a[156:159], a[252:255]\n"
    "s_waitcnt lgkmcnt(8)\n"
    "ds_read_b128 a[180:183], v242 offset:43008\n"
    "ds_read_b128 a[148:151], v241 offset:43008\n"
    "v_mfma_f32_16x16x32_bf16 v[0:3], a[208:211], a[128:131], v[0:3]\n"
    "ds_read_b128 a[152:155], v241 offset:45056\n"
    "s_addk_i32 m0, 1024\n"
    "v_mfma_f32_16x16x32_bf16 v[0:3], a[212:215], a[160:163], v[0:3]\n"
    "s_waitcnt lgkmcnt(9)\n"
    "s_addk_i32 s54, 16384\n"
    "v_mfma_f32_16x16x32_bf16 v[32:35], a[212:215], a[164:167], v[32:35]\n"
    "buffer_load_dwordx4 v254, s[72:75], s54 offen  lds\n"
    "ds_read_b128 a[184:187], v242 offset:45056\n"
    "ds_read_b128 a[188:191], v242 offset:47104\n"
    "ds_read_b128 a[156:159], v241 offset:47104\n"
    "v_mfma_f32_16x16x32_bf16 v[32:35], a[208:211], a[132:135], v[32:35]\n"
    "s_waitcnt lgkmcnt(10)\n"
    "s_addk_i32 m0, 1024\n"
    "s_addk_i32 s54, 16384\n"
    "v_mfma_f32_16x16x32_bf16 v[64:67], a[208:211], a[136:139], v[64:67]\n"
    "buffer_load_dwordx4 v254, s[72:75], s54 offen  lds\n"
    "v_mfma_f32_16x16x32_bf16 v[64:67], a[212:215], a[168:171], v[64:67]\n"
    "s_waitcnt lgkmcnt(8)\n"
    "v_mfma_f32_16x16x32_bf16 v[96:99], a[212:215], a[172:175], v[96:99]\n"
    "v_mfma_f32_16x16x32_bf16 v[96:99], a[208:211], a[140:143], v[96:99]\n"
    "s_waitcnt lgkmcnt(6)\n"
    "s_add_u32 m0, s79, 32768\n"
    "s_add_u32 s54, s57, 512\n"
    "v_mfma_f32_16x16x32_bf16 v[128:131], a[208:211], a[144:147], v[128:131]\n"
    "buffer_load_dwordx4 v254, s[72:75], s54 offen  lds\n"
    "v_mfma_f32_16x16x32_bf16 v[128:131], a[212:215], a[176:179], v[128:131]\n"
    "s_waitcnt lgkmcnt(4)\n"
    "v_mfma_f32_16x16x32_bf16 v[160:163], a[212:215], a[180:183], v[160:163]\n"
    "v_mfma_f32_16x16x32_bf16 v[160:163], a[208:211], a[148:151], v[160:163]\n"
    "s_waitcnt lgkmcnt(2)\n"
    "s_addk_i32 m0, 1024\n"
    "s_addk_i32 s54, 16384\n"
    "v_mfma_f32_16x16x32_bf16 v[192:195], a[208:211], a[152:155], v[192:195]\n"
    "buffer_load_dwordx4 v254, s[72:75], s54 offen  lds\n"
    "v_mfma_f32_16x16x32_bf16 v[192:195], a[212:215], a[184:187], v[192:195]\n"
    "s_waitcnt lgkmcnt(0)\n"
    "v_mfma_f32_16x16x32_bf16 v[224:227], a[212:215], a[188:191], v[224:227]\n"
    "v_mfma_f32_16x16x32_bf16 v[224:227], a[208:211], a[156:159], v[224:227]\n"
    "s_addk_i32 m0, 1024\n"
    "s_addk_i32 s54, 16384\n"
    "v_mfma_f32_16x16x32_bf16 v[228:231], a[216:219], a[156:159], v[228:231]\n"
    "buffer_load_dwordx4 v254, s[72:75], s54 offen  lds\n"
    "v_mfma_f32_16x16x32_bf16 v[228:231], a[220:223], a[188:191], v[228:231]\n"
    "v_mfma_f32_16x16x32_bf16 v[196:199], a[220:223], a[184:187], v[196:199]\n"
    "v_mfma_f32_16x16x32_bf16 v[196:199], a[216:219], a[152:155], v[196:199]\n"
    "s_addk_i32 m0, 1024\n"
    "s_addk_i32 s54, 16384\n"
    "v_mfma_f32_16x16x32_bf16 v[164:167], a[216:219], a[148:151], v[164:167]\n"
    "buffer_load_dwordx4 v254, s[72:75], s54 offen  lds\n"
    "v_mfma_f32_16x16x32_bf16 v[164:167], a[220:223], a[180:183], v[164:167]\n"
    "s_lshl_b32 s59, s64, 7\n"
    "s_or_b32 s66, s66, s59\n"
    "s_xor_b32 s59, s66, s67\n"
    "v_mfma_f32_16x16x32_bf16 v[132:135], a[220:223], a[176:179], v[132:135]\n"
    "s_addk_i32 s67, 128\n"
    "s_cmpk_eq_u32 s67, 1536\n"
    "s_cselect_b32 s67, 0, s67\n"
    "v_mfma_f32_16x16x32_bf16 v[132:135], a[216:219], a[144:147], v[132:135]\n"
    "s_add_u32 m0, s68, 81920\n"
    "s_nop 0\n"
    "v_mfma_f32_16x16x32_bf16 v[100:103], a[216:219], a[140:143], v[100:103]\n"
    "buffer_load_dwordx4 v255, s[24:27], s59 offen  lds\n"
    "s_add_u32 m0, s68, 82944\n"
    "s_addk_i32 s59, 16384\n"
    "buffer_load_dwordx4 v243, s[24:27], s59 offen  lds\n"
    "s_bitcmp1_b32 s85, 2\n"
    "s_cbranch_scc1 CP2_%=\n"
    "CR2_%=:\n"
    "v_mfma_f32_16x16x32_bf16 v[100:103], a[220:223], a[172:175], v[100:103]\n"
    "v_mfma_f32_16x16x32_bf16 v[68:71], a[220:223], a[168:171], v[68:71]\n"
    "s_add_u32 m0, s68, 83968\n"
    "s_addk_i32 s59, 16384\n"
    "v_mfma_f32_16x16x32_bf16 v[68:71], a[216:219], a[136:139], v[68:71]\n"
    "buffer_load_dwordx4 v255, s[24:27], s59 offen  lds\n"
    "v_mfma_f32_16x16x32_bf16 v[36:39], a[216:219], a[132:135], v[36:39]\n"
    "v_mfma_f32_16x16x32_bf16 v[36:39], a[220:223], a[164:167], v[36:39]\n"
    "s_add_u32 m0, s68, 84992\n"
    "s_addk_i32 s59, 16384\n"
    "v_mfma_f32_16x16x32_bf16 v[4:7], a[220:223], a[160:163], v[4:7]\n"
    "buffer_load_dwordx4 v243, s[24:27], s59 offen  lds\n"
    "v_mfma_f32_16x16x32_bf16 v[4:7], a[216:219], a[128:131], v[4:7]\n"
    "v_add_u32 v252, 0x20000, v252\n"
    "v_add_u32 v253, 0x20000, v253\n"
    "v_mfma_f32_16x16x32_bf16 v[8:11], a[224:227], a[128:131], v[8:11]\n"
    "s_waitcnt vmcnt(17)\n"
    "ds_read_b64_tr_b16 a[192:193], v252 offset:0\n"
    "ds_read_b64_tr_b16 a[194:195], v252 offset:512\n"
    "v_mfma_f32_16x16x32_bf16 v[8:11], a[228:231], a[160:163], v[8:11]\n"
    "s_add_u32 m0, s79, 36864\n"
    "s_addk_i32 s54, 16384\n"
    "v_mfma_f32_16x16x32_bf16 v[40:43], a[228:231], a[164:167], v[40:43]\n"
    "buffer_load_dwordx4 v254, s[72:75], s54 offen  lds\n"
    "v_mfma_f32_16x16x32_bf16 v[40:43], a[224:227], a[132:135], v[40:43]\n"
    "ds_read_b64_tr_b16 a[196:197], v252 offset:4096\n"
    "ds_read_b64_tr_b16 a[198:199], v252 offset:4608\n"
    "v_mfma_f32_16x16x32_bf16 v[72:75], a[224:227], a[136:139], v[72:75]\n"
    "v_add_u32 v246, s90, v252\n"
    "v_add_u32 v246, 0xfffe0000, v246\n"
    "v_mfma_f32_16x16x32_bf16 v[72:75], a[228:231], a[168:171], v[72:75]\n"
    "ds_read_b64_tr_b16 v[248:249], v246 offset:0\n"
    "ds_read_b64_tr_b16 v[250:251], v246 offset:512\n"
    "v_mfma_f32_16x16x32_bf16 v[104:107], a[228:231], a[172:175], v[104:107]\n"
    "ds_read_b64_tr_b16 a[200:201], v252 offset:8\n"
    "ds_read_b64_tr_b16 a[202:203], v252 offset:520\n"
    "v_mfma_f32_16x16x32_bf16 v[104:107], a[224:227], a[140:143], v[104:107]\n"
    "s_addk_i32 m0, 1024\n"
    "s_addk_i32 s54, 16384\n"
    "v_mfma_f32_16x16x32_bf16 v[136:139], a[224:227], a[144:147], v[136:139]\n"
    "buffer_load_dwordx4 v254, s[72:75], s54 offen  lds\n"
    "ds_read_b64_tr_b16 a[204:205], v252 offset:4104\n"
    "ds_read_b64_tr_b16 a[206:207], v252 offset:4616\n"
    "v_mfma_f32_16x16x32_bf16 v[136:139], a[228:231], a[176:179], v[136:139]\n"
    "ds_read_b64_tr_b16 a[208:209], v253 offset:0\n"
    "ds_read_b64_tr_b16 a[210:211], v253 offset:512\n"
    "v_mfma_f32_16x16x32_bf16 v[168:171], a[228:231], a[180:183], v[168:171]\n"
    "ds_read_b64_tr_b16 a[212:213], v253 offset:4096\n"
    "ds_read_b64_tr_b16 a[214:215], v253 offset:4608\n"
    "v_mfma_f32_16x16x32_bf16 v[168:171], a[224:227], a[148:151], v[168:171]\n"
    "ds_read_b64_tr_b16 a[216:217], v253 offset:8\n"
    "ds_read_b64_tr_b16 a[218:219], v253 offset:520\n"
    "v_mfma_f32_16x16x32_bf16 v[200:203], a[224:227], a[152:155], v[200:203]\n"
    "s_add_u32 s62, s89, 0\n"
    "s_waitcnt lgkmcnt(10)\n"
    "v_mfma_f32_16x16x32_bf16 v[200:203], a[228:231], a[184:187], v[200:203]\n"
    "buffer_store_dwordx4 v[248:251], v240, s[28:31], s62 offen offset:0 sc1\n"
    "s_nop 0\n"
    "ds_read_b64_tr_b16 a[220:221], v253 offset:4104\n"
    "ds_read_b64_tr_b16 a[222:223], v253 offset:4616\n"
    "v_mfma_f32_16x16x32_bf16 v[232:235], a[228:231], a[188:191], v[232:235]\n"
    "v_mfma_f32_16x16x32_bf16 v[232:235], a[224:227], a[156:159], v[232:235]\n"
    "v_mfma_f32_16x16x32_bf16 v[236:239], a[232:235], a[156:159], v[236:239]\n"
    "v_mfma_f32_16x16x32_bf16 v[236:239], a[236:239], a[188:191], v[236:239]\n"
    "v_mfma_f32_16x16x32_bf16 v[204:207], a[236:239], a[184:187], v[204:207]\n"
    "v_add_u32 v246, s90, v252\n"
    "v_add_u32 v246, 0xfffe0000, v246\n"
    "v_mfma_f32_16x16x32_bf16 v[204:207], a[232:235], a[152:155], v[204:207]\n"
    "ds_read_b64_tr_b16 v[248:249], v246 offset:8\n"
    "ds_read_b64_tr_b16 v[250:251], v246 offset:520\n"
    "v_mfma_f32_16x16x32_bf16 v[172:175], a[232:235], a[148:151], v[172:175]\n"
    "v_mfma_f32_16x16x32_bf16 v[172:175], a[236:239], a[180:183], v[172:175]\n"
    "v_mfma_f32_16x16x32_bf16 v[140:143], a[236:239], a[176:179], v[140:143]\n"
    "v_mfma_f32_16x16x32_bf16 v[140:143], a[232:235], a[144:147], v[140:143]\n"
    "v_mfma_f32_16x16x32_bf16 v[108:111], a[232:235], a[140:143], v[108:111]\n"
    "v_mfma_f32_16x16x32_bf16 v[108:111], a[236:239], a[172:175], v[108:111]\n"
    "v_mfma_f32_16x16x32_bf16 v[76:79], a[236:239], a[168:171], v[76:79]\n"
    "v_mfma_f32_16x16x32_bf16 v[76:79], a[232:235], a[136:139], v[76:79]\n"
    "v_mfma_f32_16x16x32_bf16 v[44:47], a[232:235], a[132:135], v[44:47]\n"
    "s_add_u32 s62, s89, 0\n"
    "s_waitcnt lgkmcnt(0)\n"
    "v_mfma_f32_16x16x32_bf16 v[44:47], a[236:239], a[164:167], v[44:47]\n"
    "buffer_store_dwordx4 v[248:251], v240, s[28:31], s62 offen offset:2048 sc1\n"
    "s_nop 0\n"
    "v_mfma_f32_16x16x32_bf16 v[12:15], a[236:239], a[160:163], v[12:15]\n"
    "v_mfma_f32_16x16x32_bf16 v[12:15], a[232:235], a[128:131], v[12:15]\n"
    "v_mfma_f32_16x16x32_bf16 v[16:19], a[192:195], a[128:131], v[16:19]\n"
    "v_mfma_f32_16x16x32_bf16 v[16:19], a[196:199], a[160:163], v[16:19]\n"
    "v_mfma_f32_16x16x32_bf16 v[48:51], a[196:199], a[164:167], v[48:51]\n"
    "v_add_u32 v246, s90, v253\n"
    "v_add_u32 v246, 0xfffe0000, v246\n"
    "v_mfma_f32_16x16x32_bf16 v[48:51], a[192:195], a[132:135], v[48:51]\n"
    "ds_read_b64_tr_b16 v[248:249], v246 offset:0\n"
    "ds_read_b64_tr_b16 v[250:251], v246 offset:512\n"
    "v_mfma_f32_16x16x32_bf16 v[80:83], a[192:195], a[136:139], v[80:83]\n"
    "v_mfma_f32_16x16x32_bf16 v[80:83], a[196:199], a[168:171], v[80:83]\n"
    "v_mfma_f32_16x16x32_bf16 v[112:115], a[196:199], a[172:175], v[112:115]\n"
    "v_mfma_f32_16x16x32_bf16 v[112:115], a[192:195], a[140:143], v[112:115]\n"
    "v_mfma_f32_16x16x32_bf16 v[144:147], a[192:195], a[144:147], v[144:147]\n"
    "v_mfma_f32_16x16x32_bf16 v[144:147], a[196:199], a[176:179], v[144:147]\n"
    "v_mfma_f32_16x16x32_bf16 v[176:179], a[196:199], a[180:183], v[176:179]\n"
    "v_mfma_f32_16x16x32_bf16 v[176:179], a[192:195], a[148:151], v[176:179]\n"
    "v_mfma_f32_16x16x32_bf16 v[208:211], a[192:195], a[152:155], v[208:211]\n"
    "s_add_u32 s62, s89, 4096\n"
    "s_waitcnt lgkmcnt(0)\n"
    "v_mfma_f32_16x16x32_bf16 v[208:211], a[196:199], a[184:187], v[208:211]\n"
    "buffer_store_dwordx4 v[248:251], v240, s[28:31], s62 offen offset:0 sc1\n"
    "s_nop 0\n"
    "v_mfma_f32_16x16x32_bf16 a[240:243], a[196:199], a[188:191], a[240:243]\n"
    "v_mfma_f32_16x16x32_bf16 a[240:243], a[192:195], a[156:159], a[240:243]\n"
    "v_mfma_f32_16x16x32_bf16 a[244:247], a[200:203], a[156:159], a[244:247]\n"
    "v_mfma_f32_16x16x32_bf16 a[244:247], a[204:207], a[188:191], a[244:247]\n"
    "v_mfma_f32_16x16x32_bf16 v[212:215], a[204:207], a[184:187], v[212:215]\n"
    "v_add_u32 v246, s90, v253\n"
    "v_add_u32 v246, 0xfffe0000, v246\n"
    "v_mfma_f32_16x16x32_bf16 v[212:215], a[200:203], a[152:155], v[212:215]\n"
    "ds_read_b64_tr_b16 v[248:249], v246 offset:8\n"
    "ds_read_b64_tr_b16 v[250:251], v246 offset:520\n"
    "v_mfma_f32_16x16x32_bf16 v[180:183], a[200:203], a[148:151], v[180:183]\n"
    "v_mfma_f32_16x16x32_bf16 v[180:183], a[204:207], a[180:183], v[180:183]\n"
    "v_mfma_f32_16x16x32_bf16 v[148:151], a[204:207], a[176:179], v[148:151]\n"
    "v_mfma_f32_16x16x32_bf16 v[148:151], a[200:203], a[144:147], v[148:151]\n"
    "v_mfma_f32_16x16x32_bf16 v[116:119], a[200:203], a[140:143], v[116:119]\n"
    "v_mfma_f32_16x16x32_bf16 v[116:119], a[204:207], a[172:175], v[116:119]\n"
    "v_mfma_f32_16x16x32_bf16 v[84:87], a[204:207], a[168:171], v[84:87]\n"
    "v_mfma_f32_16x16x32_bf16 v[84:87], a[200:203], a[136:139], v[84:87]\n"
    "v_mfma_f32_16x16x32_bf16 v[52:55], a[200:203], a[132:135], v[52:55]\n"
    "s_add_u32 s62, s89, 4096\n"
    "s_waitcnt lgkmcnt(0)\n"
    "v_mfma_f32_16x16x32_bf16 v[52:55], a[204:207], a[164:167], v[52:55]\n"
    "buffer_store_dwordx4 v[248:251], v240, s[28:31], s62 offen offset:2048 sc1\n"
    "s_nop 0\n"
    "v_mfma_f32_16x16x32_bf16 v[20:23], a[204:207], a[160:163], v[20:23]\n"
    "v_mfma_f32_16x16x32_bf16 v[20:23], a[200:203], a[128:131], v[20:23]\n"
    "v_add_u32 v252, 0xfffe0000, v252\n"
    "v_add_u32 v253, 0xfffe0000, v253\n"
    "v_mfma_f32_16x16x32_bf16 v[24:27], a[208:211], a[128:131], v[24:27]\n"
    "s_waitcnt vmcnt(14)\n"
    "ds_read_b64_tr_b16 a[224:225], v252 offset:0\n"
    "ds_read_b64_tr_b16 a[226:227], v252 offset:512\n"
    "v_mfma_f32_16x16x32_bf16 v[24:27], a[212:215], a[160:163], v[24:27]\n"
    "ds_read_b64_tr_b16 a[228:229], v252 offset:4096\n"
    "ds_read_b64_tr_b16 a[230:231], v252 offset:4608\n"
    "v_mfma_f32_16x16x32_bf16 v[56:59], a[212:215], a[164:167], v[56:59]\n"
    "v_mfma_f32_16x16x32_bf16 v[56:59], a[208:211], a[132:135], v[56:59]\n"
    "ds_read_b64_tr_b16 a[232:233], v252 offset:8\n"
    "ds_read_b64_tr_b16 a[234:235], v252 offset:520\n"
    "v_mfma_f32_16x16x32_bf16 v[88:91], a[208:211], a[136:139], v[88:91]\n"
    "v_mfma_f32_16x16x32_bf16 v[88:91], a[212:215], a[168:171], v[88:91]\n"
    "ds_read_b64_tr_b16 a[236:237], v252 offset:4104\n"
    "ds_read_b64_tr_b16 a[238:239], v252 offset:4616\n"
    "v_mfma_f32_16x16x32_bf16 v[120:123], a[212:215], a[172:175], v[120:123]\n"
    "v_mfma_f32_16x16x32_bf16 v[120:123], a[208:211], a[140:143], v[120:123]\n"
    "ds_read_b64_tr_b16 a[192:193], v253 offset:0\n"
    "ds_read_b64_tr_b16 a[194:195], v253 offset:512\n"
    "v_mfma_f32_16x16x32_bf16 v[152:155], a[208:211], a[144:147], v[152:155]\n"
    "v_mfma_f32_16x16x32_bf16 v[152:155], a[212:215], a[176:179], v[152:155]\n"
    "ds_read_b64_tr_b16 a[196:197], v253 offset:4096\n"
    "ds_read_b64_tr_b16 a[198:199], v253 offset:4608\n"
    "v_mfma_f32_16x16x32_bf16 v[184:187], a[212:215], a[180:183], v[184:187]\n"
    "v_mfma_f32_16x16x32_bf16 v[184:187], a[208:211], a[148:151], v[184:187]\n"
    "ds_read_b64_tr_b16 a[200:201], v253 offset:8\n"
    "ds_read_b64_tr_b16 a[202:203], v253 offset:520\n"
    "v_mfma_f32_16x16x32_bf16 v[216:219], a[208:211], a[152:155], v[216:219]\n"
    "v_mfma_f32_16x16x32_bf16 v[216:219], a[212:215], a[184:187], v[216:219]\n"
    "s_barrier\n"
    "ds_read_b64_tr_b16 a[204:205], v253 offset:4104\n"
    "ds_read_b64_tr_b16 a[206:207], v253 offset:4616\n"
    "v_mfma_f32_16x16x32_bf16 a[248:251], a[212:215], a[188:191], a[248:251]\n"
    "v_mfma_f32_16x16x32_bf16 a[248:251], a[208:211], a[156:159], a[248:251]\n"
    "v_mfma_f32_16x16x32_bf16 a[252:255], a[216:219], a[156:159], a[252:255]\n"
    "v_mfma_f32_16x16x32_bf16 a[252:255], a[220:223], a[188:191], a[252:255]\n"
    "v_mfma_f32_16x16x32_bf16 v[220:223], a[220:223], a[184:187], v[220:223]\n"
    "ds_read_b128 a[156:159], v241 offset:14336\n"
    "v_mfma_f32_16x16x32_bf16 v[220:223], a[216:219], a[152:155], v[220:223]\n"
    "ds_read_b128 a[188:191], v242 offset:14336\n"
    "v_mfma_f32_16x16x32_bf16 v[188:191], a[216:219], a[148:151], v[188:191]\n"
    "ds_read_b128 a[184:187], v242 offset:12288\n"
    "v_mfma_f32_16x16x32_bf16 v[188:191], a[220:223], a[180:183], v[188:191]\n"
    "ds_read_b128 a[152:155], v241 offset:12288\n"
    "v_mfma_f32_16x16x32_bf16 v[156:159], a[220:223], a[176:179], v[156:159]\n"
    "ds_read_b128 a[148:151], v241 offset:10240\n"
    "v_mfma_f32_16x16x32_bf16 v[156:159], a[216:219], a[144:147], v[156:159]\n"
    "ds_read_b128 a[180:183], v242 offset:10240\n"
    "v_mfma_f32_16x16x32_bf16 v[124:127], a[216:219], a[140:143], v[124:127]\n"
    "ds_read_b128 a[176:179], v242 offset:8192\n"
    "v_mfma_f32_16x16x32_bf16 v[124:127], a[220:223], a[172:175], v[124:127]\n"
    "ds_read_b128 a[144:147], v241 offset:8192\n"
    "v_mfma_f32_16x16x32_bf16 v[92:95], a[220:223], a[168:171], v[92:95]\n"
    "ds_read_b128 a[140:143], v241 offset:6144\n"
    "v_mfma_f32_16x16x32_bf16 v[92:95], a[216:219], a[136:139], v[92:95]\n"
    "s_addk_i32 m0, 1024\n"
    "s_addk_i32 s54, 16384\n"
    "v_mfma_f32_16x16x32_bf16 v[60:63], a[216:219], a[132:135], v[60:63]\n"
    "buffer_load_dwordx4 v254, s[72:75], s54 offen  lds\n"
    "v_mfma_f32_16x16x32_bf16 v[60:63], a[220:223], a[164:167], v[60:63]\n"
    "ds_read_b128 a[172:175], v242 offset:6144\n"
    "ds_read_b128 a[168:171], v242 offset:4096\n"
    "v_mfma_f32_16x16x32_bf16 v[28:31], a[220:223], a[160:163], v[28:31]\n"
    "ds_read_b128 a[136:139], v241 offset:4096\n"
    "ds_read_b128 a[132:135], v241 offset:2048\n"
    "v_mfma_f32_16x16x32_bf16 v[28:31], a[216:219], a[128:131], v[28:31]\n"
    "s_waitcnt lgkmcnt(11)\n"
    "s_addk_i32 m0, 1024\n"
    "s_addk_i32 s54, 16384\n"
    "v_mfma_f32_16x16x32_bf16 v[224:227], a[224:227], a[156:159], v[224:227]\n"
    "buffer_load_dwordx4 v254, s[72:75], s54 offen  lds\n"
    "v_mfma_f32_16x16x32_bf16 v[224:227], a[228:231], a[188:191], v[224:227]\n"
    "s_waitcnt lgkmcnt(9)\n"
    "s_add_u32 s57, s57, 131072\n"
    "s_cmp_ge_u32 s57, 1572864\n"
    "v_mfma_f32_16x16x32_bf16 v[192:195], a[228:231], a[184:187], v[192:195]\n"
    "s_cselect_b32 s57, s70, s57\n"
    "ds_read_b128 a[164:167], v242 offset:2048\n"
    "ds_read_b128 a[160:163], v242 offset:0\n"
    "ds_read_b128 a[128:131], v241 offset:0\n"
    "v_mfma_f32_16x16x32_bf16 v[192:195], a[224:227], a[152:155], v[192:195]\n"
    "s_waitcnt lgkmcnt(10)\n"
    "s_add_u32 m0, s79, 131072\n"
    "s_lshl_b32 s54, s64, 17\n"
    "v_mfma_f32_16x16x32_bf16 v[160:163], a[224:227], a[148:151], v[160:163]\n"
    "s_xor_b32 s54, s54, s57\n"
    "v_mfma_f32_16x16x32_bf16 v[160:163], a[228:231], a[180:183], v[160:163]\n"
    "s_waitcnt lgkmcnt(8)\n"
    "v_mfma_f32_16x16x32_bf16 v[128:131], a[228:231], a[176:179], v[128:131]\n"
    "buffer_load_dwordx4 v254, s[72:75], s54 offen  lds\n"
    "v_mfma_f32_16x16x32_bf16 v[128:131], a[224:227], a[144:147], v[128:131]\n"
    "s_waitcnt lgkmcnt(6)\n"
    "s_addk_i32 m0, 1024\n"
    "s_addk_i32 s54, 16384\n"
    "v_mfma_f32_16x16x32_bf16 v[96:99], a[224:227], a[140:143], v[96:99]\n"
    "buffer_load_dwordx4 v254, s[72:75], s54 offen  lds\n"
    "v_mfma_f32_16x16x32_bf16 v[96:99], a[228:231], a[172:175], v[96:99]\n"
    "s_waitcnt lgkmcnt(4)\n"
    "v_mfma_f32_16x16x32_bf16 v[64:67], a[228:231], a[168:171], v[64:67]\n"
    "v_mfma_f32_16x16x32_bf16 v[64:67], a[224:227], a[136:139], v[64:67]\n"
    "s_waitcnt lgkmcnt(2)\n"
    "s_addk_i32 m0, 1024\n"
    "s_addk_i32 s54, 16384\n"
    "v_mfma_f32_16x16x32_bf16 v[32:35], a[224:227], a[132:135], v[32:35]\n"
    "buffer_load_dwordx4 v254, s[72:75], s54 offen  lds\n"
    "v_mfma_f32_16x16x32_bf16 v[32:35], a[228:231], a[164:167], v[32:35]\n"
    "s_waitcnt lgkmcnt(0)\n"
    "v_mfma_f32_16x16x32_bf16 v[0:3], a[228:231], a[160:163], v[0:3]\n"
    "v_mfma_f32_16x16x32_bf16 v[0:3], a[224:227], a[128:131], v[0:3]\n"
    "s_addk_i32 m0, 1024\n"
    "s_addk_i32 s54, 16384\n"
    "v_mfma_f32_16x16x32_bf16 v[4:7], a[232:235], a[128:131], v[4:7]\n"
    "buffer_load_dwordx4 v254, s[72:75], s54 offen  lds\n"
    "v_mfma_f32_16x16x32_bf16 v[4:7], a[236:239], a[160:163], v[4:7]\n"
    "s_add_u32 s82, s77, 2052\n"
    "s_and_b32 s82, s82, s86\n"
    "v_mfma_f32_16x16x32_bf16 v[36:39], a[236:239], a[164:167], v[36:39]\n"
    "buffer_load_dword v244, v240, s[80:83], s77 offen offset:2048 sc1\n"
    "v_mfma_f32_16x16x32_bf16 v[36:39], a[232:235], a[132:135], v[36:39]\n"
    "s_addk_i32 m0, 1024\n"
    "s_addk_i32 s54, 16384\n"
    "v_mfma_f32_16x16x32_bf16 v[68:71], a[232:235], a[136:139], v[68:71]\n"
    "buffer_load_dwordx4 v254, s[72:75], s54 offen  lds\n"
    "v_mfma_f32_16x16x32_bf16 v[68:71], a[236:239], a[168:171], v[68:71]\n"
    "v_mfma_f32_16x16x32_bf16 v[100:103], a[236:239], a[172:175], v[100:103]\n"
    "v_mfma_f32_16x16x32_bf16 v[100:103], a[232:235], a[140:143], v[100:103]\n"
    "s_addk_i32 m0, 1024\n"
    "s_addk_i32 s54, 16384\n"
    "v_mfma_f32_16x16x32_bf16 v[132:135], a[232:235], a[144:147], v[132:135]\n"
    "buffer_load_dwordx4 v254, s[72:75], s54 offen  lds\n"
    "s_bitcmp1_b32 s85, 3\n"
    "s_cbranch_scc1 CP3_%=\n"
    "CR3_%=:\n"
    "v_mfma_f32_16x16x32_bf16 v[132:135], a[236:239], a[176:179], v[132:135]\n"
    "v_mfma_f32_16x16x32_bf16 v[164:167], a[236:239], a[180:183], v[164:167]\n"
    "v_mfma_f32_16x16x32_bf16 v[164:167], a[232:235], a[148:151], v[164:167]\n"
    "s_addk_i32 m0, 1024\n"
    "s_addk_i32 s54, 16384\n"
    "v_mfma_f32_16x16x32_bf16 v[196:199], a[232:235], a[152:155], v[196:199]\n"
    "buffer_load_dwordx4 v254, s[72:75], s54 offen  lds\n"
    "v_mfma_f32_16x16x32_bf16 v[196:199], a[236:239], a[184:187], v[196:199]\n"
    "v_mfma_f32_16x16x32_bf16 v[228:231], a[236:239], a[188:191], v[228:231]\n"
    "v_mfma_f32_16x16x32_bf16 v[228:231], a[232:235], a[156:159], v[228:231]\n"
    "s_waitcnt vmcnt(8)\n"
    "ds_read_b64_tr_b16 a[208:209], v252 offset:32768\n"
    "ds_read_b64_tr_b16 a[210:211], v252 offset:33280\n"
    "v_mfma_f32_16x16x32_bf16 v[232:235], a[192:195], a[156:159], v[232:235]\n"
    "s_addk_i32 m0, 1024\n"
    "s_addk_i32 s54, 16384\n"
    "v_mfma_f32_16x16x32_bf16 v[232:235], a[196:199], a[188:191], v[232:235]\n"
    "buffer_load_dwordx4 v254, s[72:75], s54 offen  lds\n"
    "v_mfma_f32_16x16x32_bf16 v[200:203], a[196:199], a[184:187], v[200:203]\n"
    "ds_read_b64_tr_b16 a[212:213], v252 offset:36864\n"
    "ds_read_b64_tr_b16 a[214:215], v252 offset:37376\n"
    "v_mfma_f32_16x16x32_bf16 v[200:203], a[192:195], a[152:155], v[200:203]\n"
    "ds_read_b64_tr_b16 a[216:217], v252 offset:32776\n"
    "ds_read_b64_tr_b16 a[218:219], v252 offset:33288\n"
    "v_mfma_f32_16x16x32_bf16 v[168:171], a[192:195], a[148:151], v[168:171]\n"
    "s_xor_b32 s59, s66, s67\n"
    "s_addk_i32 s67, 128\n"
    "s_cmpk_eq_u32 s67, 1536\n"
    "v_mfma_f32_16x16x32_bf16 v[168:171], a[196:199], a[180:183], v[168:171]\n"
    "s_cselect_b32 s67, 0, s67\n"
    "s_add_u32 m0, s68, 98304\n"
    "s_nop 0\n"
    "v_mfma_f32_16x16x32_bf16 v[136:139], a[196:199], a[176:179], v[136:139]\n"
    "buffer_load_dwordx4 v255, s[24:27], s59 offen  lds\n"
    "v_mfma_f32_16x16x32_bf16 v[136:139], a[192:195], a[144:147], v[136:139]\n"
    "ds_read_b64_tr_b16 a[220:221], v252 offset:36872\n"
    "ds_read_b64_tr_b16 a[222:223], v252 offset:37384\n"
    "v_mfma_f32_16x16x32_bf16 v[104:107], a[192:195], a[140:143], v[104:107]\n"
    "ds_read_b64_tr_b16 a[224:225], v253 offset:32768\n"
    "ds_read_b64_tr_b16 a[226:227], v253 offset:33280\n"
    "v_mfma_f32_16x16x32_bf16 v[104:107], a[196:199], a[172:175], v[104:107]\n"
    "s_add_u32 m0, s68, 99328\n"
    "s_addk_i32 s59, 16384\n"
    "v_mfma_f32_16x16x32_bf16 v[72:75], a[196:199], a[168:171], v[72:75]\n"
    "buffer_load_dwordx4 v243, s[24:27], s59 offen  lds\n"
    "v_mfma_f32_16x16x32_bf16 v[72:75], a[192:195], a[136:139], v[72:75]\n"
    "ds_read_b64_tr_b16 a[228:229], v253 offset:36864\n"
    "ds_read_b64_tr_b16 a[230:231], v253 offset:37376\n"
    "v_mfma_f32_16x16x32_bf16 v[40:43], a[192:195], a[132:135], v[40:43]\n"
    "ds_read_b64_tr_b16 a[232:233], v253 offset:32776\n"
    "ds_read_b64_tr_b16 a[234:235], v253 offset:33288\n"
    "v_mfma_f32_16x16x32_bf16 v[40:43], a[196:199], a[164:167], v[40:43]\n"
    "s_add_u32 m0, s68, 100352\n"
    "s_addk_i32 s59, 16384\n"
    "v_mfma_f32_16x16x32_bf16 v[8:11], a[196:199], a[160:163], v[8:11]\n"
    "buffer_load_dwordx4 v255, s[24:27], s59 offen  lds\n"
    "v_mfma_f32_16x16x32_bf16 v[8:11], a[192:195], a[128:131], v[8:11]\n"
    "ds_read_b64_tr_b16 a[236:237], v253 offset:36872\n"
    "ds_read_b64_tr_b16 a[238:239], v253 offset:37384\n"
    "v_mfma_f32_16x16x32_bf16 v[12:15], a[200:203], a[128:131], v[12:15]\n"
    "s_add_u32 m0, s68, 101376\n"
    "s_addk_i32 s59, 16384\n"
    "v_mfma_f32_16x16x32_bf16 v[12:15], a[204:207], a[160:163], v[12:15]\n"
    "buffer_load_dwordx4 v243, s[24:27], s59 offen  lds\n"
    "v_mfma_f32_16x16x32_bf16 v[44:47], a[204:207], a[164:167], v[44:47]\n"
    "v_mfma_f32_16x16x32_bf16 v[44:47], a[200:203], a[132:135], v[44:47]\n"
    "s_add_u32 m0, s79, 0\n"
    "s_lshl_b32 s54, s64, 17\n"
    "s_xor_b32 s54, s54, s57\n"
    "v_mfma_f32_16x16x32_bf16 v[76:79], a[200:203], a[136:139], v[76:79]\n"
    "s_addk_i32 s54, 512\n"
    "v_mfma_f32_16x16x32_bf16 v[76:79], a[204:207], a[168:171], v[76:79]\n"
    "buffer_load_dwordx4 v254, s[72:75], s54 offen  lds\n"
    "v_mfma_f32_16x16x32_bf16 v[108:111], a[204:207], a[172:175], v[108:111]\n"
    "v_mfma_f32_16x16x32_bf16 v[108:111], a[200:203], a[140:143], v[108:111]\n"
    "s_addk_i32 m0, 1024\n"
    "s_addk_i32 s54, 16384\n"
    "v_mfma_f32_16x16x32_bf16 v[140:143], a[200:203], a[144:147], v[140:143]\n"
    "buffer_load_dwordx4 v254, s[72:75], s54 offen  lds\n"
    "v_mfma_f32_16x16x32_bf16 v[140:143], a[204:207], a[176:179], v[140:143]\n"
    "v_mfma_f32_16x16x32_bf16 v[172:175], a[204:207], a[180:183], v[172:175]\n"
    "v_mfma_f32_16x16x32_bf16 v[172:175], a[200:203], a[148:151], v[172:175]\n"
    "s_addk_i32 m0, 1024\n"
    "s_addk_i32 s54, 16384\n"
    "v_mfma_f32_16x16x32_bf16 v[204:207], a[200:203], a[152:155], v[204:207]\n"
    "buffer_load_dwordx4 v254, s[72:75], s54 offen  lds\n"
    "v_mfma_f32_16x16x32_bf16 v[204:207], a[204:207], a[184:187], v[204:207]\n"
    "v_mfma_f32_16x16x32_bf16 v[236:239], a[204:207], a[188:191], v[236:239]\n"
    "v_mfma_f32_16x16x32_bf16 v[236:239], a[200:203], a[156:159], v[236:239]\n"
    "s_waitcnt lgkmcnt(12)\n"
    "s_addk_i32 m0, 1024\n"
    "s_addk_i32 s54, 16384\n"
    "v_mfma_f32_16x16x32_bf16 a[240:243], a[208:211], a[156:159], a[240:243]\n"
    "buffer_load_dwordx4 v254, s[72:75], s54 offen  lds\n"
    "v_mfma_f32_16x16x32_bf16 a[240:243], a[212:215], a[188:191], a[240:243]\n"
    "v_mfma_f32_16x16x32_bf16 v[208:211], a[212:215], a[184:187], v[208:211]\n"
    "v_mfma_f32_16x16x32_bf16 v[208:211], a[208:211], a[152:155], v[208:211]\n"
    "s_addk_i32 m0, 1024\n"
    "s_addk_i32 s54, 16384\n"
    "v_mfma_f32_16x16x32_bf16 v[176:179], a[208:211], a[148:151], v[176:179]\n"
    "buffer_load_dwordx4 v254, s[72:75], s54 offen  lds\n"
    "v_mfma_f32_16x16x32_bf16 v[176:179], a[212:215], a[180:183], v[176:179]\n"
    "v_mfma_f32_16x16x32_bf16 v[144:147], a[212:215], a[176:179], v[144:147]\n"
    "v_mfma_f32_16x16x32_bf16 v[144:147], a[208:211], a[144:147], v[144:147]\n"
    "s_addk_i32 m0, 1024\n"
    "s_addk_i32 s54, 16384\n"
    "v_mfma_f32_16x16x32_bf16 v[112:115], a[208:211], a[140:143], v[112:115]\n"
    "buffer_load_dwordx4 v254, s[72:75], s54 offen  lds\n"
    "v_mfma_f32_16x16x32_bf16 v[112:115], a[212:215], a[172:175], v[112:115]\n"
    "v_mfma_f32_16x16x32_bf16 v[80:83], a[212:215], a[168:171], v[80:83]\n"
    "v_mfma_f32_16x16x32_bf16 v[80:83], a[208:211], a[136:139], v[80:83]\n"
    "s_addk_i32 m0, 1024\n"
    "s_addk_i32 s54, 16384\n"
    "v_mfma_f32_16x16x32_bf16 v[48:51], a[208:211], a[132:135], v[48:51]\n"
    "buffer_load_dwordx4 v254, s[72:75], s54 offen  lds\n"
    "v_mfma_f32_16x16x32_bf16 v[48:51], a[212:215], a[164:167], v[48:51]\n"
    "v_mfma_f32_16x16x32_bf16 v[16:19], a[212:215], a[160:163], v[16:19]\n"
    "v_mfma_f32_16x16x32_bf16 v[16:19], a[208:211], a[128:131], v[16:19]\n"
    "s_waitcnt lgkmcnt(8)\n"
    "s_addk_i32 m0, 1024\n"
    "s_addk_i32 s54, 16384\n"
    "v_mfma_f32_16x16x32_bf16 v[20:23], a[216:219], a[128:131], v[20:23]\n"
    "buffer_load_dwordx4 v254, s[72:75], s54 offen  lds\n"
    "v_mfma_f32_16x16x32_bf16 v[20:23], a[220:223], a[160:163], v[20:23]\n"
    "s_add_u32 s57, s57, 131072\n"
    "s_cmp_ge_u32 s57, 1572864\n"
    "s_cselect_b32 s57, s70, s57\n"
    "v_mfma_f32_16x16x32_bf16 v[52:55], a[220:223], a[164:167], v[52:55]\n"
    "v_mfma_f32_16x16x32_bf16 v[52:55], a[216:219], a[132:135], v[52:55]\n"
    "s_waitcnt lgkmcnt(0)\n"
    "s_add_u32 m0, s79, 32768\n"
    "s_lshl_b32 s54, s64, 17\n"
    "v_mfma_f32_16x16x32_bf16 v[84:87], a[216:219], a[136:139], v[84:87]\n"
    "s_xor_b32 s54, s54, s57\n"
    "v_mfma_f32_16x16x32_bf16 v[84:87], a[220:223], a[168:171], v[84:87]\n"
    "buffer_load_dwordx4 v254, s[72:75], s54 offen  lds\n"
    "v_mfma_f32_16x16x32_bf16 v[116:119], a[220:223], a[172:175], v[116:119]\n"
    "v_mfma_f32_16x16x32_bf16 v[116:119], a[216:219], a[140:143], v[116:119]\n"
    "s_addk_i32 m0, 1024\n"
    "s_addk_i32 s54, 16384\n"
    "v_mfma_f32_16x16x32_bf16 v[148:151], a[216:219], a[144:147], v[148:151]\n"
    "buffer_load_dwordx4 v254, s[72:75], s54 offen  lds\n"
    "v_mfma_f32_16x16x32_bf16 v[148:151], a[220:223], a[176:179], v[148:151]\n"
    "v_mfma_f32_16x16x32_bf16 v[180:183], a[220:223], a[180:183], v[180:183]\n"
    "v_mfma_f32_16x16x32_bf16 v[180:183], a[216:219], a[148:151], v[180:183]\n"
    "s_addk_i32 m0, 1024\n"
    "s_addk_i32 s54, 16384\n"
    "v_mfma_f32_16x16x32_bf16 v[212:215], a[216:219], a[152:155], v[212:215]\n"
    "buffer_load_dwordx4 v254, s[72:75], s54 offen  lds\n"
    "v_mfma_f32_16x16x32_bf16 v[212:215], a[220:223], a[184:187], v[212:215]\n"
    "v_mfma_f32_16x16x32_bf16 a[244:247], a[220:223], a[188:191], a[244:247]\n"
    "v_mfma_f32_16x16x32_bf16 a[244:247], a[216:219], a[156:159], a[244:247]\n"
    "v_add_u32 v252, 0x20000, v252\n"
    "v_add_u32 v253, 0x20000, v253\n"
    "v_mfma_f32_16x16x32_bf16 a[248:251], a[224:227], a[156:159], a[248:251]\n"
    "s_waitcnt vmcnt(15)\n"
    "ds_read_b64_tr_b16 a[192:193], v252 offset:0\n"
    "ds_read_b64_tr_b16 a[194:195], v252 offset:512\n"
    "v_mfma_f32_16x16x32_bf16 a[248:251], a[228:231], a[188:191], a[248:251]\n"
    "s_addk_i32 m0, 1024\n"
    "s_addk_i32 s54, 16384\n"
    "v_mfma_f32_16x16x32_bf16 v[216:219], a[228:231], a[184:187], v[216:219]\n"
    "buffer_load_dwordx4 v254, s[72:75], s54 offen  lds\n"
    "v_mfma_f32_16x16x32_bf16 v[216:219], a[224:227], a[152:155], v[216:219]\n"
    "ds_read_b64_tr_b16 a[196:197], v252 offset:4096\n"
    "ds_read_b64_tr_b16 a[198:199], v252 offset:4608\n"
    "v_mfma_f32_16x16x32_bf16 v[184:187], a[224:227], a[148:151], v[184:187]\n"
    "ds_read_b64_tr_b16 a[200:201], v252 offset:8\n"
    "ds_read_b64_tr_b16 a[202:203], v252 offset:520\n"
    "v_mfma_f32_16x16x32_bf16 v[184:187], a[228:231], a[180:183], v[184:187]\n"
    "s_addk_i32 m0, 1024\n"
    "s_addk_i32 s54, 16384\n"
    "v_mfma_f32_16x16x32_bf16 v[152:155], a[228:231], a[176:179], v[152:155]\n"
    "buffer_load_dwordx4 v254, s[72:75], s54 offen  lds\n"
    "v_mfma_f32_16x16x32_bf16 v[152:155], a[224:227], a[144:147], v[152:155]\n"
    "ds_read_b64_tr_b16 a[204:205], v252 offset:4104\n"
    "ds_read_b64_tr_b16 a[206:207], v252 offset:4616\n"
    "v_mfma_f32_16x16x32_bf16 v[120:123], a[224:227], a[140:143], v[120:123]\n"
    "ds_read_b64_tr_b16 a[208:209], v253 offset:0\n"
    "ds_read_b64_tr_b16 a[210:211], v253 offset:512\n"
    "v_mfma_f32_16x16x32_bf16 v[120:123], a[228:231], a[172:175], v[120:123]\n"
    "s_addk_i32 m0, 1024\n"
    "s_addk_i32 s54, 16384\n"
    "v_mfma_f32_16x16x32_bf16 v[88:91], a[228:231], a[168:171], v[88:91]\n"
    "buffer_load_dwordx4 v254, s[72:75], s54 offen  lds\n"
    "v_mfma_f32_16x16x32_bf16 v[88:91], a[224:227], a[136:139], v[88:91]\n"
    "ds_read_b64_tr_b16 a[212:213], v253 offset:4096\n"
    "ds_read_b64_tr_b16 a[214:215], v253 offset:4608\n"
    "v_mfma_f32_16x16x32_bf16 v[56:59], a[224:227], a[132:135], v[56:59]\n"
    "ds_read_b64_tr_b16 a[216:217], v253 offset:8\n"
    "ds_read_b64_tr_b16 a[218:219], v253 offset:520\n"
    "v_mfma_f32_16x16x32_bf16 v[56:59], a[228:231], a[164:167], v[56:59]\n"
    "s_addk_i32 m0, 1024\n"
    "s_addk_i32 s54, 16384\n"
    "buffer_load_dwordx4 v254, s[72:75], s54 offen  lds\n"
    "s_barrier\n"
    "v_mfma_f32_16x16x32_bf16 v[24:27], a[228:231], a[160:163], v[24:27]\n"
    "ds_read_b64_tr_b16 a[220:221], v253 offset:4104\n"
    "ds_read_b64_tr_b16 a[222:223], v253 offset:4616\n"
    "v_mfma_f32_16x16x32_bf16 v[24:27], a[224:227], a[128:131], v[24:27]\n"
    "s_addk_i32 m0, 1024\n"
    "s_addk_i32 s54, 16384\n"
    "v_mfma_f32_16x16x32_bf16 v[28:31], a[232:235], a[128:131], v[28:31]\n"
    "buffer_load_dwordx4 v254, s[72:75], s54 offen  lds\n"
    "v_mfma_f32_16x16x32_bf16 v[28:31], a[236:239], a[160:163], v[28:31]\n"
    "v_mfma_f32_16x16x32_bf16 v[60:63], a[236:239], a[164:167], v[60:63]\n"
    "ds_read_b128 a[128:131], v241 offset:16384\n"
    "v_mfma_f32_16x16x32_bf16 v[60:63], a[232:235], a[132:135], v[60:63]\n"
    "s_waitcnt lgkmcnt(1)\n"
    "s_add_u32 m0, s79, 131072\n"
    "s_lshl_b32 s54, s64, 17\n"
    "v_mfma_f32_16x16x32_bf16 v[92:95], a[232:235], a[136:139], v[92:95]\n"
    "s_xor_b32 s54, s54, s57\n"
    "s_addk_i32 s54, 512\n"
    "v_mfma_f32_16x16x32_bf16 v[92:95], a[236:239], a[168:171], v[92:95]\n"
    "buffer_load_dwordx4 v254, s[72:75], s54 offen  lds\n"
    "v_mfma_f32_16x16x32_bf16 v[124:127], a[236:239], a[172:175], v[124:127]\n"
    "ds_read_b128 a[160:163], v242 offset:16384\n"
    "ds_read_b128 a[164:167], v242 offset:18432\n"
    "v_mfma_f32_16x16x32_bf16 v[124:127], a[232:235], a[140:143], v[124:127]\n"
    "ds_read_b128 a[132:135], v241 offset:18432\n"
    "ds_read_b128 a[136:139], v241 offset:20480\n"
    "v_mfma_f32_16x16x32_bf16 v[156:159], a[232:235], a[144:147], v[156:159]\n"
    "s_addk_i32 m0, 1024\n"
    "s_addk_i32 s54, 16384\n"
    "v_mfma_f32_16x16x32_bf16 v[156:159], a[236:239], a[176:179], v[156:159]\n"
    "buffer_load_dwordx4 v254, s[72:75], s54 offen  lds\n"
    "v_mfma_f32_16x16x32_bf16 v[188:191], a[236:239], a[180:183], v[188:191]\n"
    "ds_read_b128 a[168:171], v242 offset:20480\n"
    "ds_read_b128 a[172:175], v242 offset:22528\n"
    "v_mfma_f32_16x16x32_bf16 v[188:191], a[232:235], a[148:151], v[188:191]\n"
    "ds_read_b128 a[140:143], v241 offset:22528\n"
    "ds_read_b128 a[144:147], v241 offset:24576\n"
    "v_mfma_f32_16x16x32_bf16 v[220:223], a[232:235], a[152:155], v[220:223]\n"
    "s_addk_i32 m0, 1024\n"
    "s_addk_i32 s54, 16384\n"
    "v_mfma_f32_16x16x32_bf16 v[220:223], a[236:239], a[184:187], v[220:223]\n"
    "buffer_load_dwordx4 v254, s[72:75], s54 offen  lds\n"
    "v_mfma_f32_16x16x32_bf16 a[252:255], a[236:239], a[188:191], a[252:255]\n"
    "ds_read_b128 a[176:179], v242 offset:24576\n"
    "ds_read_b128 a[180:183], v242 offset:26624\n"
    "v_mfma_f32_16x16x32_bf16 a[252:255], a[232:235], a[156:159], a[252:255]\n"
    "s_waitcnt lgkmcnt(9)\n"
    "ds_read_b128 a[148:151], v241 offset:26624\n"
    "ds_read_b128 a[152:155], v241 offset:28672\n"
    "v_mfma_f32_16x16x32_bf16 v[0:3], a[192:195], a[128:131], v[0:3]\n"
    "s_addk_i32 m0, 1024\n"
    "s_addk_i32 s54, 16384\n"
    "v_mfma_f32_16x16x32_bf16 v[0:3], a[196:199], a[160:163], v[0:3]\n"
    "s_waitcnt lgkmcnt(9)\n"
    "v_mfma_f32_16x16x32_bf16 v[32:35], a[196:199], a[164:167], v[32:35]\n"
    "buffer_load_dwordx4 v254, s[72:75], s54 offen  lds\n"
    "ds_read_b128 a[184:187], v242 offset:28672\n"
    "ds_read_b128 a[188:191], v242 offset:30720\n"
    "ds_read_b128 a[156:159], v241 offset:30720\n"
    "v_mfma_f32_16x16x32_bf16 v[32:35], a[192:195], a[132:135], v[32:35]\n"
    "s_waitcnt lgkmcnt(10)\n"
    "s_add_u32 s82, s76, 8\n"
    "s_and_b32 s82, s82, s86\n"
    "v_mfma_f32_16x16x32_bf16 v[64:67], a[192:195], a[136:139], v[64:67]\n"
    "buffer_store_dwordx2 v[244:245], v240, s[80:83], s76 offen sc1\n"
    "v_mfma_f32_16x16x32_bf16 v[64:67], a[196:199], a[168:171], v[64:67]\n"
    "s_waitcnt lgkmcnt(8)\n"
    "s_addk_i32 m0, 1024\n"
    "s_addk_i32 s54, 16384\n"
    "v_mfma_f32_16x16x32_bf16 v[96:99], a[196:199], a[172:175], v[96:99]\n"
    "buffer_load_dwordx4 v254, s[72:75], s54 offen  lds\n"
    "v_mfma_f32_16x16x32_bf16 v[96:99], a[192:195], a[140:143], v[96:99]\n"
    "s_waitcnt lgkmcnt(6)\n"
    "s_addk_i32 m0, 1024\n"
    "s_addk_i32 s54, 16384\n"
    "v_mfma_f32_16x16x32_bf16 v[128:131], a[192:195], a[144:147], v[128:131]\n"
    "buffer_load_dwordx4 v254, s[72:75], s54 offen  lds\n"
    "v_mfma_f32_16x16x32_bf16 v[128:131], a[196:199], a[176:179], v[128:131]\n"
    "s_waitcnt lgkmcnt(4)\n"
    "v_mfma_f32_16x16x32_bf16 v[160:163], a[196:199], a[180:183], v[160:163]\n"
    "v_mfma_f32_16x16x32_bf16 v[160:163], a[192:195], a[148:151], v[160:163]\n"
    "s_waitcnt lgkmcnt(2)\n"
    "s_addk_i32 m0, 1024\n"
    "s_addk_i32 s54, 16384\n"
    "v_mfma_f32_16x16x32_bf16 v[192:195], a[192:195], a[152:155], v[192:195]\n"
    "buffer_load_dwordx4 v254, s[72:75], s54 offen  lds\n"
    "v_mfma_f32_16x16x32_bf16 v[192:195], a[196:199], a[184:187], v[192:195]\n"
    "s_waitcnt lgkmcnt(0)\n"
    "v_mfma_f32_16x16x32_bf16 v[224:227], a[196:199], a[188:191], v[224:227]\n"
    "v_mfma_f32_16x16x32_bf16 v[224:227], a[192:195], a[156:159], v[224:227]\n"
    "s_addk_i32 m0, 1024\n"
    "s_addk_i32 s54, 16384\n"
    "v_mfma_f32_16x16x32_bf16 v[228:231], a[200:203], a[156:159], v[228:231]\n"
    "buffer_load_dwordx4 v254, s[72:75], s54 offen  lds\n"
    "v_mfma_f32_16x16x32_bf16 v[228:231], a[204:207], a[188:191], v[228:231]\n"
    "s_add_u32 s57, s57, 131072\n"
    "s_cmp_ge_u32 s57, 1572864\n"
    "s_cselect_b32 s57, s70, s57\n"
    "v_mfma_f32_16x16x32_bf16 v[196:199], a[204:207], a[184:187], v[196:199]\n"
    "v_mfma_f32_16x16x32_bf16 v[196:199], a[200:203], a[152:155], v[196:199]\n"
    "v_mfma_f32_16x16x32_bf16 v[164:167], a[200:203], a[148:151], v[164:167]\n"
    "v_mfma_f32_16x16x32_bf16 v[164:167], a[204:207], a[180:183], v[164:167]\n"
    "v_mfma_f32_16x16x32_bf16 v[132:135], a[204:207], a[176:179], v[132:135]\n"
    "s_xor_b32 s59, s66, s67\n"
    "s_addk_i32 s67, 128\n"
    "s_cmpk_eq_u32 s67, 1536\n"
    "v_mfma_f32_16x16x32_bf16 v[132:135], a[200:203], a[144:147], v[132:135]\n"
    "s_cselect_b32 s67, 0, s67\n"
    "s_add_u32 m0, s68, 65536\n"
    "s_nop 0\n"
    "v_mfma_f32_16x16x32_bf16 v[100:103], a[200:203], a[140:143], v[100:103]\n"
    "buffer_load_dwordx4 v255, s[24:27], s59 offen  lds\n"
    "s_bitcmp1_b32 s85, 4\n"
    "s_cbranch_scc1 CP4_%=\n"
    "CR4_%=:\n"
    "v_mfma_f32_16x16x32_bf16 v[100:103], a[204:207], a[172:175], v[100:103]\n"
    "s_add_u32 m0, s68, 66560\n"
    "s_addk_i32 s59, 16384\n"
    "v_mfma_f32_16x16x32_bf16 v[68:71], a[204:207], a[168:171], v[68:71]\n"
    "buffer_load_dwordx4 v243, s[24:27], s59 offen  lds\n"
    "v_mfma_f32_16x16x32_bf16 v[68:71], a[200:203], a[136:139], v[68:71]\n"
    "v_mfma_f32_16x16x32_bf16 v[36:39], a[200:203], a[132:135], v[36:39]\n"
    "s_add_u32 m0, s68, 67584\n"
    "s_addk_i32 s59, 16384\n"
    "v_mfma_f32_16x16x32_bf16 v[36:39], a[204:207], a[164:167], v[36:39]\n"
    "buffer_load_dwordx4 v255, s[24:27], s59 offen  lds\n"
    "v_mfma_f32_16x16x32_bf16 v[4:7], a[204:207], a[160:163], v[4:7]\n"
    "v_mfma_f32_16x16x32_bf16 v[4:7], a[200:203], a[128:131], v[4:7]\n"
    "v_add_u32 v252, 0xfffe0000, v252\n"
    "v_add_u32 v253, 0xfffe0000, v253\n"
    "v_mfma_f32_16x16x32_bf16 v[8:11], a[208:211], a[128:131], v[8:11]\n"
    "s_waitcnt vmcnt(20)\n"
    "ds_read_b64_tr_b16 a[224:225], v252 offset:0\n"
    "ds_read_b64_tr_b16 a[226:227], v252 offset:512\n"
    "v_mfma_f32_16x16x32_bf16 v[8:11], a[212:215], a[160:163], v[8:11]\n"
    "s_add_u32 m0, s68, 68608\n"
    "s_addk_i32 s59, 16384\n"
    "v_mfma_f32_16x16x32_bf16 v[40:43], a[212:215], a[164:167], v[40:43]\n"
    "buffer_load_dwordx4 v243, s[24:27], s59 offen  lds\n"
    "v_mfma_f32_16x16x32_bf16 v[40:43], a[208:211], a[132:135], v[40:43]\n"
    "ds_read_b64_tr_b16 a[228:229], v252 offset:4096\n"
    "ds_read_b64_tr_b16 a[230:231], v252 offset:4608\n"
    "v_mfma_f32_16x16x32_bf16 v[72:75], a[208:211], a[136:139], v[72:75]\n"
    "s_add_u32 m0, s68, 114688\n"
    "s_add_u32 s60, s78, 4194304\n"
    "v_mfma_f32_16x16x32_bf16 v[72:75], a[212:215], a[168:171], v[72:75]\n"
    "buffer_load_dwordx4 v254, s[72:75], s60 offen nt lds\n"
    "v_mfma_f32_16x16x32_bf16 v[104:107], a[212:215], a[172:175], v[104:107]\n"
    "ds_read_b64_tr_b16 a[232:233], v252 offset:8\n"
    "ds_read_b64_tr_b16 a[234:235], v252 offset:520\n"
    "v_mfma_f32_16x16x32_bf16 v[104:107], a[208:211], a[140:143], v[104:107]\n"
    "ds_read_b64_tr_b16 a[236:237], v252 offset:4104\n"
    "ds_read_b64_tr_b16 a[238:239], v252 offset:4616\n"
    "v_mfma_f32_16x16x32_bf16 v[136:139], a[208:211], a[144:147], v[136:139]\n"
    "s_add_u32 m0, s68, 115712\n"
    "s_add_u32 s60, s78, 4210688\n"
    "v_mfma_f32_16x16x32_bf16 v[136:139], a[212:215], a[176:179], v[136:139]\n"
    "buffer_load_dwordx4 v254, s[72:75], s60 offen nt lds\n"
    "v_mfma_f32_16x16x32_bf16 v[168:171], a[212:215], a[180:183], v[168:171]\n"
    "ds_read_b64_tr_b16 a[192:193], v253 offset:0\n"
    "ds_read_b64_tr_b16 a[194:195], v253 offset:512\n"
    "v_mfma_f32_16x16x32_bf16 v[168:171], a[208:211], a[148:151], v[168:171]\n"
    "s_add_u32 m0, s68, 116736\n"
    "s_add_u32 s60, s78, 4227072\n"
    "v_mfma_f32_16x16x32_bf16 v[200:203], a[208:211], a[152:155], v[200:203]\n"
    "buffer_load_dwordx4 v254, s[72:75], s60 offen nt lds\n"
    "ds_read_b64_tr_b16 a[196:197], v253 offset:4096\n"
    "ds_read_b64_tr_b16 a[198:199], v253 offset:4608\n"
    "v_mfma_f32_16x16x32_bf16 v[200:203], a[212:215], a[184:187], v[200:203]\n"
    "ds_read_b64_tr_b16 a[200:201], v253 offset:8\n"
    "ds_read_b64_tr_b16 a[202:203], v253 offset:520\n"
    "v_mfma_f32_16x16x32_bf16 v[232:235], a[212:215], a[188:191], v[232:235]\n"
    "s_add_u32 m0, s68, 117760\n"
    "s_add_u32 s60, s78, 4243456\n"
    "v_mfma_f32_16x16x32_bf16 v[232:235], a[208:211], a[156:159], v[232:235]\n"
    "buffer_load_dwordx4 v254, s[72:75], s60 offen nt lds\n"
    "v_mfma_f32_16x16x32_bf16 v[236:239], a[216:219], a[156:159], v[236:239]\n"
    "ds_read_b64_tr_b16 a[204:205], v253 offset:4104\n"
    "ds_read_b64_tr_b16 a[206:207], v253 offset:4616\n"
    "v_mfma_f32_16x16x32_bf16 v[236:239], a[220:223], a[188:191], v[236:239]\n"
    "v_mfma_f32_16x16x32_bf16 v[204:207], a[220:223], a[184:187], v[204:207]\n"
    "v_mfma_f32_16x16x32_bf16 v[204:207], a[216:219], a[152:155], v[204:207]\n"
    "v_mfma_f32_16x16x32_bf16 v[172:175], a[216:219], a[148:151], v[172:175]\n"
    "v_mfma_f32_16x16x32_bf16 v[172:175], a[220:223], a[180:183], v[172:175]\n"
    "v_mfma_f32_16x16x32_bf16 v[140:143], a[220:223], a[176:179], v[140:143]\n"
    "v_mfma_f32_16x16x32_bf16 v[140:143], a[216:219], a[144:147], v[140:143]\n"
    "v_mfma_f32_16x16x32_bf16 v[108:111], a[216:219], a[140:143], v[108:111]\n"
    "v_mfma_f32_16x16x32_bf16 v[108:111], a[220:223], a[172:175], v[108:111]\n"
    "v_mfma_f32_16x16x32_bf16 v[76:79], a[220:223], a[168:171], v[76:79]\n"
    "v_mfma_f32_16x16x32_bf16 v[76:79], a[216:219], a[136:139], v[76:79]\n"
    "v_mfma_f32_16x16x32_bf16 v[44:47], a[216:219], a[132:135], v[44:47]\n"
    "v_mfma_f32_16x16x32_bf16 v[44:47], a[220:223], a[164:167], v[44:47]\n"
    "v_mfma_f32_16x16x32_bf16 v[12:15], a[220:223], a[160:163], v[12:15]\n"
    "v_mfma_f32_16x16x32_bf16 v[12:15], a[216:219], a[128:131], v[12:15]\n"
    "s_waitcnt lgkmcnt(12)\n"
    "s_lshr_b32 s60, %3, 2\n"
    "s_lshl_b32 s60, s60, 21\n"
    "v_mfma_f32_16x16x32_bf16 v[16:19], a[224:227], a[128:131], v[16:19]\n"
    "s_and_b32 s61, %3, 3\n"
    "s_lshl_b32 s61, s61, 15\n"
    "s_add_u32 s60, s60, s61\n"
    "v_mfma_f32_16x16x32_bf16 v[16:19], a[228:231], a[160:163], v[16:19]\n"
    "s_mul_i32 s61, s65, 0x2400\n"
    "s_and_b32 s61, s61, 0x6400\n"
    "s_add_u32 s60, s60, s61\n"
    "v_mfma_f32_16x16x32_bf16 v[48:51], a[228:231], a[164:167], v[48:51]\n"
    "buffer_store_dwordx4 v[240:243], v240, s[32:35], s60 offen sc1\n"
    "s_xor_b32 s60, s60, 8192\n"
    "v_mfma_f32_16x16x32_bf16 v[48:51], a[224:227], a[132:135], v[48:51]\n"
    "buffer_store_dwordx4 v[240:243], v240, s[32:35], s60 offen sc1\n"
    "v_mfma_f32_16x16x32_bf16 v[80:83], a[224:227], a[136:139], v[80:83]\n"
    "v_mfma_f32_16x16x32_bf16 v[80:83], a[228:231], a[168:171], v[80:83]\n"
    "v_mfma_f32_16x16x32_bf16 v[112:115], a[228:231], a[172:175], v[112:115]\n"
    "v_mfma_f32_16x16x32_bf16 v[112:115], a[224:227], a[140:143], v[112:115]\n"
    "v_mfma_f32_16x16x32_bf16 v[144:147], a[224:227], a[144:147], v[144:147]\n"
    "v_mfma_f32_16x16x32_bf16 v[144:147], a[228:231], a[176:179], v[144:147]\n"
    "v_mfma_f32_16x16x32_bf16 v[176:179], a[228:231], a[180:183], v[176:179]\n"
    "v_mfma_f32_16x16x32_bf16 v[176:179], a[224:227], a[148:151], v[176:179]\n"
    "v_mfma_f32_16x16x32_bf16 v[208:211], a[224:227], a[152:155], v[208:211]\n"
    "v_mfma_f32_16x16x32_bf16 v[208:211], a[228:231], a[184:187], v[208:211]\n"
    "v_mfma_f32_16x16x32_bf16 a[240:243], a[228:231], a[188:191], a[240:243]\n"
    "v_mfma_f32_16x16x32_bf16 a[240:243], a[224:227], a[156:159], a[240:243]\n"
    "s_waitcnt lgkmcnt(8)\n"
    "v_mfma_f32_16x16x32_bf16 a[244:247], a[232:235], a[156:159], a[244:247]\n"
    "v_mfma_f32_16x16x32_bf16 a[244:247], a[236:239], a[188:191], a[244:247]\n"
    "v_mfma_f32_16x16x32_bf16 v[212:215], a[236:239], a[184:187], v[212:215]\n"
    "v_mfma_f32_16x16x32_bf16 v[212:215], a[232:235], a[152:155], v[212:215]\n"
    "v_mfma_f32_16x16x32_bf16 v[180:183], a[232:235], a[148:151], v[180:183]\n"
    "v_mfma_f32_16x16x32_bf16 v[180:183], a[236:239], a[180:183], v[180:183]\n"
    "v_mfma_f32_16x16x32_bf16 v[148:151], a[236:239], a[176:179], v[148:151]\n"
    "v_mfma_f32_16x16x32_bf16 v[148:151], a[232:235], a[144:147], v[148:151]\n"
    "v_mfma_f32_16x16x32_bf16 v[116:119], a[232:235], a[140:143], v[116:119]\n"
    "v_mfma_f32_16x16x32_bf16 v[116:119], a[236:239], a[172:175], v[116:119]\n"
    "v_mfma_f32_16x16x32_bf16 v[84:87], a[236:239], a[168:171], v[84:87]\n"
    "v_mfma_f32_16x16x32_bf16 v[84:87], a[232:235], a[136:139], v[84:87]\n"
    "v_mfma_f32_16x16x32_bf16 v[52:55], a[232:235], a[132:135], v[52:55]\n"
    "v_mfma_f32_16x16x32_bf16 v[52:55], a[236:239], a[164:167], v[52:55]\n"
    "v_mfma_f32_16x16x32_bf16 v[20:23], a[236:239], a[160:163], v[20:23]\n"
    "v_mfma_f32_16x16x32_bf16 v[20:23], a[232:235], a[128:131], v[20:23]\n"
    "s_waitcnt lgkmcnt(4)\n"
    "s_waitcnt vmcnt(19)\n"
    "ds_read_b64_tr_b16 a[208:209], v252 offset:32768\n"
    "v_mfma_f32_16x16x32_bf16 v[24:27], a[192:195], a[128:131], v[24:27]\n"
    "ds_read_b64_tr_b16 a[210:211], v252 offset:33280\n"
    "v_mfma_f32_16x16x32_bf16 v[24:27], a[196:199], a[160:163], v[24:27]\n"
    "ds_read_b64_tr_b16 a[212:213], v252 offset:36864\n"
    "ds_read_b64_tr_b16 a[214:215], v252 offset:37376\n"
    "v_mfma_f32_16x16x32_bf16 v[56:59], a[196:199], a[164:167], v[56:59]\n"
    "v_mfma_f32_16x16x32_bf16 v[56:59], a[192:195], a[132:135], v[56:59]\n"
    "ds_read_b64_tr_b16 a[216:217], v252 offset:32776\n"
    "ds_read_b64_tr_b16 a[218:219], v252 offset:33288\n"
    "v_mfma_f32_16x16x32_bf16 v[88:91], a[192:195], a[136:139], v[88:91]\n"
    "v_mfma_f32_16x16x32_bf16 v[88:91], a[196:199], a[168:171], v[88:91]\n"
    "ds_read_b64_tr_b16 a[220:221], v252 offset:36872\n"
    "ds_read_b64_tr_b16 a[222:223], v252 offset:37384\n"
    "v_mfma_f32_16x16x32_bf16 v[120:123], a[196:199], a[172:175], v[120:123]\n"
    "v_mfma_f32_16x16x32_bf16 v[120:123], a[192:195], a[140:143], v[120:123]\n"
    "ds_read_b64_tr_b16 a[224:225], v253 offset:32768\n"
    "ds_read_b64_tr_b16 a[226:227], v253 offset:33280\n"
    "v_mfma_f32_16x16x32_bf16 v[152:155], a[192:195], a[144:147], v[152:155]\n"
    "v_mfma_f32_16x16x32_bf16 v[152:155], a[196:199], a[176:179], v[152:155]\n"
    "ds_read_b64_tr_b16 a[228:229], v253 offset:36864\n"
    "ds_read_b64_tr_b16 a[230:231], v253 offset:37376\n"
    "v_mfma_f32_16x16x32_bf16 v[184:187], a[196:199], a[180:183], v[184:187]\n"
    "v_mfma_f32_16x16x32_bf16 v[184:187], a[192:195], a[148:151], v[184:187]\n"
    "ds_read_b64_tr_b16 a[232:233], v253 offset:32776\n"
    "ds_read_b64_tr_b16 a[234:235], v253 offset:33288\n"
    "v_mfma_f32_16x16x32_bf16 v[216:219], a[192:195], a[152:155], v[216:219]\n"
    "v_mfma_f32_16x16x32_bf16 v[216:219], a[196:199], a[184:187], v[216:219]\n"
    "s_barrier\n"
    "ds_read_b64_tr_b16 a[236:237], v253 offset:36872\n"
    "ds_read_b64_tr_b16 a[238:239], v253 offset:37384\n"
    "v_mfma_f32_16x16x32_bf16 a[248:251], a[196:199], a[188:191], a[248:251]\n"
    "v_mfma_f32_16x16x32_bf16 a[248:251], a[192:195], a[156:159], a[248:251]\n"
    "s_waitcnt lgkmcnt(15)\n"
    "v_mfma_f32_16x16x32_bf16 a[252:255], a[200:203], a[156:159], a[252:255]\n"
    "v_mfma_f32_16x16x32_bf16 a[252:255], a[204:207], a[188:191], a[252:255]\n"
    "v_mfma_f32_16x16x32_bf16 v[220:223], a[204:207], a[184:187], v[220:223]\n"
    "ds_read_b128 a[156:159], v241 offset:47104\n"
    "v_mfma_f32_16x16x32_bf16 v[220:223], a[200:203], a[152:155], v[220:223]\n"
    "ds_read_b128 a[188:191], v242 offset:47104\n"
    "v_mfma_f32_16x16x32_bf16 v[188:191], a[200:203], a[148:151], v[188:191]\n"
    "ds_read_b128 a[184:187], v242 offset:45056\n"
    "v_mfma_f32_16x16x32_bf16 v[188:191], a[204:207], a[180:183], v[188:191]\n"
    "ds_read_b128 a[152:155], v241 offset:45056\n"
    "v_mfma_f32_16x16x32_bf16 v[156:159], a[204:207], a[176:179], v[156:159]\n"
    "ds_read_b128 a[148:151], v241 offset:43008\n"
    "v_mfma_f32_16x16x32_bf16 v[156:159], a[200:203], a[144:147], v[156:159]\n"
    "ds_read_b128 a[180:183], v242 offset:43008\n"
    "v_mfma_f32_16x16x32_bf16 v[124:127], a[200:203], a[140:143], v[124:127]\n"
    "ds_read_b128 a[176:179], v242 offset:40960\n"
    "v_mfma_f32_16x16x32_bf16 v[124:127], a[204:207], a[172:175], v[124:127]\n"
    "ds_read_b128 a[144:147], v241 offset:40960\n"
    "v_mfma_f32_16x16x32_bf16 v[92:95], a[204:207], a[168:171], v[92:95]\n"
    "ds_read_b128 a[140:143], v241 offset:38912\n"
    "v_mfma_f32_16x16x32_bf16 v[92:95], a[200:203], a[136:139], v[92:95]\n"
    "s_add_u32 m0, s79, 0\n"
    "s_lshl_b32 s54, s64, 17\n"
    "s_xor_b32 s54, s54, s57\n"
    "v_mfma_f32_16x16x32_bf16 v[60:63], a[200:203], a[132:135], v[60:63]\n"
    "buffer_load_dwordx4 v254, s[72:75], s54 offen  lds\n"
    "v_mfma_f32_16x16x32_bf16 v[60:63], a[204:207], a[164:167], v[60:63]\n"
    "ds_read_b128 a[172:175], v242 offset:38912\n"
    "ds_read_b128 a[168:171], v242 offset:36864\n"
    "v_mfma_f32_16x16x32_bf16 v[28:31], a[204:207], a[160:163], v[28:31]\n"
    "ds_read_b128 a[136:139], v241 offset:36864\n"
    "ds_read_b128 a[132:135], v241 offset:34816\n"
    "v_mfma_f32_16x16x32_bf16 v[28:31], a[200:203], a[128:131], v[28:31]\n"
    "s_waitcnt lgkmcnt(11)\n"
    "s_addk_i32 m0, 1024\n"
    "s_addk_i32 s54, 16384\n"
    "v_mfma_f32_16x16x32_bf16 v[224:227], a[208:211], a[156:159], v[224:227]\n"
    "buffer_load_dwordx4 v254, s[72:75], s54 offen  lds\n"
    "v_mfma_f32_16x16x32_bf16 v[224:227], a[212:215], a[188:191], v[224:227]\n"
    "s_waitcnt lgkmcnt(9)\n"
    "ds_read_b128 a[164:167], v242 offset:34816\n"
    "ds_read_b128 a[160:163], v242 offset:32768\n"
    "v_mfma_f32_16x16x32_bf16 v[192:195], a[212:215], a[184:187], v[192:195]\n"
    "ds_read_b128 a[128:131], v241 offset:32768\n"
    "v_mfma_f32_16x16x32_bf16 v[192:195], a[208:211], a[152:155], v[192:195]\n"
    "s_waitcnt lgkmcnt(10)\n"
    "s_addk_i32 m0, 1024\n"
    "s_addk_i32 s54, 16384\n"
    "v_mfma_f32_16x16x32_bf16 v[160:163], a[208:211], a[148:151], v[160:163]\n"
    "buffer_load_dwordx4 v254, s[72:75], s54 offen  lds\n"
    "v_mfma_f32_16x16x32_bf16 v[160:163], a[212:215], a[180:183], v[160:163]\n"
    "s_waitcnt lgkmcnt(8)\n"
    "v_mfma_f32_16x16x32_bf16 v[128:131], a[212:215], a[176:179], v[128:131]\n"
    "v_mfma_f32_16x16x32_bf16 v[128:131], a[208:211], a[144:147], v[128:131]\n"
    "s_waitcnt lgkmcnt(6)\n"
    "s_addk_i32 m0, 1024\n"
    "s_addk_i32 s54, 16384\n"
    "v_mfma_f32_16x16x32_bf16 v[96:99], a[208:211], a[140:143], v[96:99]\n"
    "buffer_load_dwordx4 v254, s[72:75], s54 offen  lds\n"
    "v_mfma_f32_16x16x32_bf16 v[96:99], a[212:215], a[172:175], v[96:99]\n"
    "s_waitcnt lgkmcnt(4)\n"
    "v_mfma_f32_16x16x32_bf16 v[64:67], a[212:215], a[168:171], v[64:67]\n"
    "v_mfma_f32_16x16x32_bf16 v[64:67], a[208:211], a[136:139], v[64:67]\n"
    "s_waitcnt lgkmcnt(2)\n"
    "s_addk_i32 m0, 1024\n"
    "s_addk_i32 s54, 16384\n"
    "v_mfma_f32_16x16x32_bf16 v[32:35], a[208:211], a[132:135], v[32:35]\n"
    "buffer_load_dwordx4 v254, s[72:75], s54 offen  lds\n"
    "v_mfma_f32_16x16x32_bf16 v[32:35], a[212:215], a[164:167], v[32:35]\n"
    "s_waitcnt lgkmcnt(0)\n"
    "v_mfma_f32_16x16x32_bf16 v[0:3], a[212:215], a[160:163], v[0:3]\n"
    "v_mfma_f32_16x16x32_bf16 v[0:3], a[208:211], a[128:131], v[0:3]\n"
    "s_addk_i32 m0, 1024\n"
    "s_addk_i32 s54, 16384\n"
    "v_mfma_f32_16x16x32_bf16 v[4:7], a[216:219], a[128:131], v[4:7]\n"
    "buffer_load_dwordx4 v254, s[72:75], s54 offen  lds\n"
    "v_mfma_f32_16x16x32_bf16 v[4:7], a[220:223], a[160:163], v[4:7]\n"
    "v_mfma_f32_16x16x32_bf16 v[36:39], a[220:223], a[164:167], v[36:39]\n"
    "v_mfma_f32_16x16x32_bf16 v[36:39], a[216:219], a[132:135], v[36:39]\n"
    "s_addk_i32 m0, 1024\n"
    "s_addk_i32 s54, 16384\n"
    "v_mfma_f32_16x16x32_bf16 v[68:71], a[216:219], a[136:139], v[68:71]\n"
    "buffer_load_dwordx4 v254, s[72:75], s54 offen  lds\n"
    "v_mfma_f32_16x16x32_bf16 v[68:71], a[220:223], a[168:171], v[68:71]\n"
    "v_mfma_f32_16x16x32_bf16 v[100:103], a[220:223], a[172:175], v[100:103]\n"
    "v_mfma_f32_16x16x32_bf16 v[100:103], a[216:219], a[140:143], v[100:103]\n"
    "s_addk_i32 m0, 1024\n"
    "s_addk_i32 s54, 16384\n"
    "v_mfma_f32_16x16x32_bf16 v[132:135], a[216:219], a[144:147], v[132:135]\n"
    "buffer_load_dwordx4 v254, s[72:75], s54 offen  lds\n"
    "s_bitcmp1_b32 s85, 5\n"
    "s_cbranch_scc1 CP5_%=\n"
    "CR5_%=:\n"
    "v_mfma_f32_16x16x32_bf16 v[132:135], a[220:223], a[176:179], v[132:135]\n"
    "v_mfma_f32_16x16x32_bf16 v[164:167], a[220:223], a[180:183], v[164:167]\n"
    "v_mfma_f32_16x16x32_bf16 v[164:167], a[216:219], a[148:151], v[164:167]\n"
    "s_xor_b32 s59, s66, s67\n"
    "s_addk_i32 s67, 128\n"
    "s_cmpk_eq_u32 s67, 1536\n"
    "v_mfma_f32_16x16x32_bf16 v[196:199], a[216:219], a[152:155], v[196:199]\n"
    "s_cselect_b32 s67, 0, s67\n"
    "s_add_u32 m0, s68, 81920\n"
    "s_nop 0\n"
    "v_mfma_f32_16x16x32_bf16 v[196:199], a[220:223], a[184:187], v[196:199]\n"
    "buffer_load_dwordx4 v255, s[24:27], s59 offen  lds\n"
    "v_mfma_f32_16x16x32_bf16 v[228:231], a[220:223], a[188:191], v[228:231]\n"
    "v_mfma_f32_16x16x32_bf16 v[228:231], a[216:219], a[156:159], v[228:231]\n"
    "v_add_u32 v252, 0x20000, v252\n"
    "v_add_u32 v253, 0x20000, v253\n"
    "v_mfma_f32_16x16x32_bf16 v[232:235], a[224:227], a[156:159], v[232:235]\n"
    "s_waitcnt vmcnt(19)\n"
    "ds_read_b64_tr_b16 a[192:193], v252 offset:0\n"
    "ds_read_b64_tr_b16 a[194:195], v252 offset:512\n"
    "v_mfma_f32_16x16x32_bf16 v[232:235], a[228:231], a[188:191], v[232:235]\n"
    "s_add_u32 m0, s68, 82944\n"
    "s_addk_i32 s59, 16384\n"
    "v_mfma_f32_16x16x32_bf16 v[200:203], a[228:231], a[184:187], v[200:203]\n"
    "buffer_load_dwordx4 v243, s[24:27], s59 offen  lds\n"
    "v_mfma_f32_16x16x32_bf16 v[200:203], a[224:227], a[152:155], v[200:203]\n"
    "ds_read_b64_tr_b16 a[196:197], v252 offset:4096\n"
    "ds_read_b64_tr_b16 a[198:199], v252 offset:4608\n"
    "v_mfma_f32_16x16x32_bf16 v[168:171], a[224:227], a[148:151], v[168:171]\n"
    "ds_read_b64_tr_b16 a[200:201], v252 offset:8\n"
    "ds_read_b64_tr_b16 a[202:203], v252 offset:520\n"
    "v_mfma_f32_16x16x32_bf16 v[168:171], a[228:231], a[180:183], v[168:171]\n"
    "s_add_u32 m0, s68, 83968\n"
    "s_addk_i32 s59, 16384\n"
    "v_mfma_f32_16x16x32_bf16 v[136:139], a[228:231], a[176:179], v[136:139]\n"
    "buffer_load_dwordx4 v255, s[24:27], s59 offen  lds\n"
    "v_mfma_f32_16x16x32_bf16 v[136:139], a[224:227], a[144:147], v[136:139]\n"
    "ds_read_b64_tr_b16 a[204:205], v252 offset:4104\n"
    "ds_read_b64_tr_b16 a[206:207], v252 offset:4616\n"
    "v_mfma_f32_16x16x32_bf16 v[104:107], a[224:227], a[140:143], v[104:107]\n"
    "ds_read_b64_tr_b16 a[208:209], v253 offset:0\n"
    "ds_read_b64_tr_b16 a[210:211], v253 offset:512\n"
    "v_mfma_f32_16x16x32_bf16 v[104:107], a[228:231], a[172:175], v[104:107]\n"
    "s_add_u32 m0, s68, 84992\n"
    "s_addk_i32 s59, 16384\n"
    "v_mfma_f32_16x16x32_bf16 v[72:75], a[228:231], a[168:171], v[72:75]\n"
    "buffer_load_dwordx4 v243, s[24:27], s59 offen  lds\n"
    "v_mfma_f32_16x16x32_bf16 v[72:75], a[224:227], a[136:139], v[72:75]\n"
    "ds_read_b64_tr_b16 a[212:213], v253 offset:4096\n"
    "ds_read_b64_tr_b16 a[214:215], v253 offset:4608\n"
    "v_mfma_f32_16x16x32_bf16 v[40:43], a[224:227], a[132:135], v[40:43]\n"
    "ds_read_b64_tr_b16 a[216:217], v253 offset:8\n"
    "ds_read_b64_tr_b16 a[218:219], v253 offset:520\n"
    "v_mfma_f32_16x16x32_bf16 v[40:43], a[228:231], a[164:167], v[40:43]\n"
    "s_add_u32 m0, s79, 32768\n"
    "s_lshl_b32 s54, s64, 17\n"
    "s_xor_b32 s54, s54, s57\n"
    "v_mfma_f32_16x16x32_bf16 v[8:11], a[228:231], a[160:163], v[8:11]\n"
    "s_addk_i32 s54, 512\n"
    "v_mfma_f32_16x16x32_bf16 v[8:11], a[224:227], a[128:131], v[8:11]\n"
    "buffer_load_dwordx4 v254, s[72:75], s54 offen  lds\n"
    "v_mfma_f32_16x16x32_bf16 v[12:15], a[232:235], a[128:131], v[12:15]\n"
    "ds_read_b64_tr_b16 a[220:221], v253 offset:4104\n"
    "ds_read_b64_tr_b16 a[222:223], v253 offset:4616\n"
    "v_mfma_f32_16x16x32_bf16 v[12:15], a[236:239], a[160:163], v[12:15]\n"
    "s_addk_i32 m0, 1024\n"
    "s_addk_i32 s54, 16384\n"
    "v_mfma_f32_16x16x32_bf16 v[44:47], a[236:239], a[164:167], v[44:47]\n"
    "buffer_load_dwordx4 v254, s[72:75], s54 offen  lds\n"
    "v_mfma_f32_16x16x32_bf16 v[44:47], a[232:235], a[132:135], v[44:47]\n"
    "s_addk_i32 m0, 1024\n"
    "s_addk_i32 s54, 16384\n"
    "v_mfma_f32_16x16x32_bf16 v[76:79], a[232:235], a[136:139], v[76:79]\n"
    "buffer_load_dwordx4 v254, s[72:75], s54 offen  lds\n"
    "v_mfma_f32_16x16x32_bf16 v[76:79], a[236:239], a[168:171], v[76:79]\n"
    "v_mfma_f32_16x16x32_bf16 v[108:111], a[236:239], a[172:175], v[108:111]\n"
    "v_mfma_f32_16x16x32_bf16 v[108:111], a[232:235], a[140:143], v[108:111]\n"
    "s_addk_i32 m0, 1024\n"
    "s_addk_i32 s54, 16384\n"
    "v_mfma_f32_16x16x32_bf16 v[140:143], a[232:235], a[144:147], v[140:143]\n"
    "buffer_load_dwordx4 v254, s[72:75], s54 offen  lds\n"
    "v_mfma_f32_16x16x32_bf16 v[140:143], a[236:239], a[176:179], v[140:143]\n"
    "v_mfma_f32_16x16x32_bf16 v[172:175], a[236:239], a[180:183], v[172:175]\n"
    "v_mfma_f32_16x16x32_bf16 v[172:175], a[232:235], a[148:151], v[172:175]\n"
    "s_addk_i32 m0, 1024\n"
    "s_addk_i32 s54, 16384\n"
    "v_mfma_f32_16x16x32_bf16 v[204:207], a[232:235], a[152:155], v[204:207]\n"
    "buffer_load_dwordx4 v254, s[72:75], s54 offen  lds\n"
    "v_mfma_f32_16x16x32_bf16 v[204:207], a[236:239], a[184:187], v[204:207]\n"
    "v_mfma_f32_16x16x32_bf16 v[236:239], a[236:239], a[188:191], v[236:239]\n"
    "v_mfma_f32_16x16x32_bf16 v[236:239], a[232:235], a[156:159], v[236:239]\n"
    "s_waitcnt lgkmcnt(12)\n"
    "s_addk_i32 m0, 1024\n"
    "s_addk_i32 s54, 16384\n"
    "v_mfma_f32_16x16x32_bf16 a[240:243], a[192:195], a[156:159], a[240:243]\n"
    "buffer_load_dwordx4 v254, s[72:75], s54 offen  lds\n"
    "v_mfma_f32_16x16x32_bf16 a[240:243], a[196:199], a[188:191], a[240:243]\n"
    "v_mfma_f32_16x16x32_bf16 v[208:211], a[196:199], a[184:187], v[208:211]\n"
    "v_mfma_f32_16x16x32_bf16 v[208:211], a[192:195], a[152:155], v[208:211]\n"
    "s_addk_i32 m0, 1024\n"
    "s_addk_i32 s54, 16384\n"
    "v_mfma_f32_16x16x32_bf16 v[176:179], a[192:195], a[148:151], v[176:179]\n"
    "buffer_load_dwordx4 v254, s[72:75], s54 offen  lds\n"
    "v_mfma_f32_16x16x32_bf16 v[176:179], a[196:199], a[180:183], v[176:179]\n"
    "v_mfma_f32_16x16x32_bf16 v[144:147], a[196:199], a[176:179], v[144:147]\n"
    "v_mfma_f32_16x16x32_bf16 v[144:147], a[192:195], a[144:147], v[144:147]\n"
    "s_addk_i32 m0, 1024\n"
    "s_addk_i32 s54, 16384\n"
    "v_mfma_f32_16x16x32_bf16 v[112:115], a[192:195], a[140:143], v[112:115]\n"
    "buffer_load_dwordx4 v254, s[72:75], s54 offen  lds\n"
    "v_mfma_f32_16x16x32_bf16 v[112:115], a[196:199], a[172:175], v[112:115]\n"
    "s_add_u32 s57, s57, 131072\n"
    "s_cmp_ge_u32 s57, 1572864\n"
    "s_cselect_b32 s57, s70, s57\n"
    "v_mfma_f32_16x16x32_bf16 v[80:83], a[196:199], a[168:171], v[80:83]\n"
    "v_mfma_f32_16x16x32_bf16 v[80:83], a[192:195], a[136:139], v[80:83]\n"
    "s_waitcnt lgkmcnt(0)\n"
    "s_add_u32 m0, s79, 131072\n"
    "s_lshl_b32 s54, s64, 17\n"
    "v_mfma_f32_16x16x32_bf16 v[48:51], a[192:195], a[132:135], v[48:51]\n"
    "s_xor_b32 s54, s54, s57\n"
    "v_mfma_f32_16x16x32_bf16 v[48:51], a[196:199], a[164:167], v[48:51]\n"
    "buffer_load_dwordx4 v254, s[72:75], s54 offen  lds\n"
    "v_mfma_f32_16x16x32_bf16 v[16:19], a[196:199], a[160:163], v[16:19]\n"
    "v_mfma_f32_16x16x32_bf16 v[16:19], a[192:195], a[128:131], v[16:19]\n"
    "s_addk_i32 m0, 1024\n"
    "s_addk_i32 s54, 16384\n"
    "v_mfma_f32_16x16x32_bf16 v[20:23], a[200:203], a[128:131], v[20:23]\n"
    "buffer_load_dwordx4 v254, s[72:75], s54 offen  lds\n"
    "v_mfma_f32_16x16x32_bf16 v[20:23], a[204:207], a[160:163], v[20:23]\n"
    "v_mfma_f32_16x16x32_bf16 v[52:55], a[204:207], a[164:167], v[52:55]\n"
    "v_mfma_f32_16x16x32_bf16 v[52:55], a[200:203], a[132:135], v[52:55]\n"
    "s_addk_i32 m0, 1024\n"
    "s_addk_i32 s54, 16384\n"
    "v_mfma_f32_16x16x32_bf16 v[84:87], a[200:203], a[136:139], v[84:87]\n"
    "buffer_load_dwordx4 v254, s[72:75], s54 offen  lds\n"
    "v_mfma_f32_16x16x32_bf16 v[84:87], a[204:207], a[168:171], v[84:87]\n"
    "v_mfma_f32_16x16x32_bf16 v[116:119], a[204:207], a[172:175], v[116:119]\n"
    "v_mfma_f32_16x16x32_bf16 v[116:119], a[200:203], a[140:143], v[116:119]\n"
    "s_addk_i32 m0, 1024\n"
    "s_addk_i32 s54, 16384\n"
    "v_mfma_f32_16x16x32_bf16 v[148:151], a[200:203], a[144:147], v[148:151]\n"
    "buffer_load_dwordx4 v254, s[72:75], s54 offen  lds\n"
    "v_mfma_f32_16x16x32_bf16 v[148:151], a[204:207], a[176:179], v[148:151]\n"
    "v_mfma_f32_16x16x32_bf16 v[180:183], a[204:207], a[180:183], v[180:183]\n"
    "v_mfma_f32_16x16x32_bf16 v[180:183], a[200:203], a[148:151], v[180:183]\n"
    "s_addk_i32 m0, 1024\n"
    "s_addk_i32 s54, 16384\n"
    "v_mfma_f32_16x16x32_bf16 v[212:215], a[200:203], a[152:155], v[212:215]\n"
    "buffer_load_dwordx4 v254, s[72:75], s54 offen  lds\n"
    "v_mfma_f32_16x16x32_bf16 v[212:215], a[204:207], a[184:187], v[212:215]\n"
    "v_mfma_f32_16x16x32_bf16 a[244:247], a[204:207], a[188:191], a[244:247]\n"
    "v_mfma_f32_16x16x32_bf16 a[244:247], a[200:203], a[156:159], a[244:247]\n"
    "v_add_u32 v252, 0xfffe0000, v252\n"
    "v_add_u32 v253, 0xfffe0000, v253\n"
    "v_mfma_f32_16x16x32_bf16 a[248:251], a[208:211], a[156:159], a[248:251]\n"
    "s_waitcnt vmcnt(17)\n"
    "ds_read_b64_tr_b16 a[224:225], v252 offset:0\n"
    "ds_read_b64_tr_b16 a[226:227], v252 offset:512\n"
    "v_mfma_f32_16x16x32_bf16 a[248:251], a[212:215], a[188:191], a[248:251]\n"
    "s_addk_i32 m0, 1024\n"
    "s_addk_i32 s54, 16384\n"
    "v_mfma_f32_16x16x32_bf16 v[216:219], a[212:215], a[184:187], v[216:219]\n"
    "buffer_load_dwordx4 v254, s[72:75], s54 offen  lds\n"
    "v_mfma_f32_16x16x32_bf16 v[216:219], a[208:211], a[152:155], v[216:219]\n"
    "ds_read_b64_tr_b16 a[228:229], v252 offset:4096\n"
    "ds_read_b64_tr_b16 a[230:231], v252 offset:4608\n"
    "v_mfma_f32_16x16x32_bf16 v[184:187], a[208:211], a[148:151], v[184:187]\n"
    "ds_read_b64_tr_b16 a[232:233], v252 offset:8\n"
    "ds_read_b64_tr_b16 a[234:235], v252 offset:520\n"
    "v_mfma_f32_16x16x32_bf16 v[184:187], a[212:215], a[180:183], v[184:187]\n"
    "s_addk_i32 m0, 1024\n"
    "s_addk_i32 s54, 16384\n"
    "v_mfma_f32_16x16x32_bf16 v[152:155], a[212:215], a[176:179], v[152:155]\n"
    "buffer_load_dwordx4 v254, s[72:75], s54 offen  lds\n"
    "v_mfma_f32_16x16x32_bf16 v[152:155], a[208:211], a[144:147], v[152:155]\n"
    "ds_read_b64_tr_b16 a[236:237], v252 offset:4104\n"
    "ds_read_b64_tr_b16 a[238:239], v252 offset:4616\n"
    "v_mfma_f32_16x16x32_bf16 v[120:123], a[208:211], a[140:143], v[120:123]\n"
    "ds_read_b64_tr_b16 a[192:193], v253 offset:0\n"
    "ds_read_b64_tr_b16 a[194:195], v253 offset:512\n"
    "v_mfma_f32_16x16x32_bf16 v[120:123], a[212:215], a[172:175], v[120:123]\n"
    "s_addk_i32 m0, 1024\n"
    "s_addk_i32 s54, 16384\n"
    "v_mfma_f32_16x16x32_bf16 v[88:91], a[212:215], a[168:171], v[88:91]\n"
    "buffer_load_dwordx4 v254, s[72:75], s54 offen  lds\n"
    "v_mfma_f32_16x16x32_bf16 v[88:91], a[208:211], a[136:139], v[88:91]\n"
    "ds_read_b64_tr_b16 a[196:197], v253 offset:4096\n"
    "ds_read_b64_tr_b16 a[198:199], v253 offset:4608\n"
    "v_mfma_f32_16x16x32_bf16 v[56:59], a[208:211], a[132:135], v[56:59]\n"
    "ds_read_b64_tr_b16 a[200:201], v253 offset:8\n"
    "ds_read_b64_tr_b16 a[202:203], v253 offset:520\n"
    "v_mfma_f32_16x16x32_bf16 v[56:59], a[212:215], a[164:167], v[56:59]\n"
    "s_barrier\n"
    "ds_read_b64_tr_b16 a[204:205], v253 offset:4104\n"
    "ds_read_b64_tr_b16 a[206:207], v253 offset:4616\n"
    "v_mfma_f32_16x16x32_bf16 v[24:27], a[212:215], a[160:163], v[24:27]\n"
    "v_mfma_f32_16x16x32_bf16 v[24:27], a[208:211], a[128:131], v[24:27]\n"
    "v_mfma_f32_16x16x32_bf16 v[28:31], a[216:219], a[128:131], v[28:31]\n"
    "v_mfma_f32_16x16x32_bf16 v[28:31], a[220:223], a[160:163], v[28:31]\n"
    "v_mfma_f32_16x16x32_bf16 v[60:63], a[220:223], a[164:167], v[60:63]\n"
    "ds_read_b128 a[128:131], v241 offset:0\n"
    "v_mfma_f32_16x16x32_bf16 v[60:63], a[216:219], a[132:135], v[60:63]\n"
    "s_waitcnt lgkmcnt(1)\n"
    "s_add_u32 m0, s79, 0\n"
    "s_lshl_b32 s54, s64, 17\n"
    "v_mfma_f32_16x16x32_bf16 v[92:95], a[216:219], a[136:139], v[92:95]\n"
    "s_xor_b32 s54, s54, s57\n"
    "s_addk_i32 s54, 512\n"
    "v_mfma_f32_16x16x32_bf16 v[92:95], a[220:223], a[168:171], v[92:95]\n"
    "buffer_load_dwordx4 v254, s[72:75], s54 offen  lds\n"
    "v_mfma_f32_16x16x32_bf16 v[124:127], a[220:223], a[172:175], v[124:127]\n"
    "ds_read_b128 a[160:163], v242 offset:0\n"
    "ds_read_b128 a[164:167], v242 offset:2048\n"
    "v_mfma_f32_16x16x32_bf16 v[124:127], a[216:219], a[140:143], v[124:127]\n"
    "ds_read_b128 a[132:135], v241 offset:2048\n"
    "ds_read_b128 a[136:139], v241 offset:4096\n"
    "v_mfma_f32_16x16x32_bf16 v[156:159], a[216:219], a[144:147], v[156:159]\n"
    "s_addk_i32 m0, 1024\n"
    "s_addk_i32 s54, 16384\n"
    "v_mfma_f32_16x16x32_bf16 v[156:159], a[220:223], a[176:179], v[156:159]\n"
    "buffer_load_dwordx4 v254, s[72:75], s54 offen  lds\n"
    "v_mfma_f32_16x16x32_bf16 v[188:191], a[220:223], a[180:183], v[188:191]\n"
    "ds_read_b128 a[168:171], v242 offset:4096\n"
    "ds_read_b128 a[172:175], v242 offset:6144\n"
    "v_mfma_f32_16x16x32_bf16 v[188:191], a[216:219], a[148:151], v[188:191]\n"
    "ds_read_b128 a[140:143], v241 offset:6144\n"
    "ds_read_b128 a[144:147], v241 offset:8192\n"
    "v_mfma_f32_16x16x32_bf16 v[220:223], a[216:219], a[152:155], v[220:223]\n"
    "s_addk_i32 m0, 1024\n"
    "s_addk_i32 s54, 16384\n"
    "v_mfma_f32_16x16x32_bf16 v[220:223], a[220:223], a[184:187], v[220:223]\n"
    "buffer_load_dwordx4 v254, s[72:75], s54 offen  lds\n"
    "v_mfma_f32_16x16x32_bf16 a[252:255], a[220:223], a[188:191], a[252:255]\n"
    "ds_read_b128 a[176:179], v242 offset:8192\n"
    "ds_read_b128 a[180:183], v242 offset:10240\n"
    "v_mfma_f32_16x16x32_bf16 a[252:255], a[216:219], a[156:159], a[252:255]\n"
    "s_waitcnt lgkmcnt(9)\n"
    "ds_read_b128 a[148:151], v241 offset:10240\n"
    "ds_read_b128 a[152:155], v241 offset:12288\n"
    "v_mfma_f32_16x16x32_bf16 v[0:3], a[224:227], a[128:131], v[0:3]\n"
    "s_addk_i32 m0, 1024\n"
    "s_addk_i32 s54, 16384\n"
    "v_mfma_f32_16x16x32_bf16 v[0:3], a[228:231], a[160:163], v[0:3]\n"
    "s_waitcnt lgkmcnt(9)\n"
    "v_mfma_f32_16x16x32_bf16 v[32:35], a[228:231], a[164:167], v[32:35]\n"
    "buffer_load_dwordx4 v254, s[72:75], s54 offen  lds\n"
    "ds_read_b128 a[184:187], v242 offset:12288\n"
    "ds_read_b128 a[188:191], v242 offset:14336\n"
    "ds_read_b128 a[156:159], v241 offset:14336\n"
    "v_mfma_f32_16x16x32_bf16 v[32:35], a[224:227], a[132:135], v[32:35]\n"
    "s_waitcnt lgkmcnt(10)\n"
    "s_addk_i32 m0, 1024\n"
    "s_addk_i32 s54, 16384\n"
    "v_mfma_f32_16x16x32_bf16 v[64:67], a[224:227], a[136:139], v[64:67]\n"
    "buffer_load_dwordx4 v254, s[72:75], s54 offen  lds\n"
    "v_mfma_f32_16x16x32_bf16 v[64:67], a[228:231], a[168:171], v[64:67]\n"
    "s_waitcnt lgkmcnt(8)\n"
    "v_mfma_f32_16x16x32_bf16 v[96:99], a[228:231], a[172:175], v[96:99]\n"
    "v_mfma_f32_16x16x32_bf16 v[96:99], a[224:227], a[140:143], v[96:99]\n"
    "s_waitcnt lgkmcnt(6)\n"
    "s_addk_i32 m0, 1024\n"
    "s_addk_i32 s54, 16384\n"
    "v_mfma_f32_16x16x32_bf16 v[128:131], a[224:227], a[144:147], v[128:131]\n"
    "buffer_load_dwordx4 v254, s[72:75], s54 offen  lds\n"
    "v_mfma_f32_16x16x32_bf16 v[128:131], a[228:231], a[176:179], v[128:131]\n"
    "s_waitcnt lgkmcnt(4)\n"
    "v_mfma_f32_16x16x32_bf16 v[160:163], a[228:231], a[180:183], v[160:163]\n"
    "v_mfma_f32_16x16x32_bf16 v[160:163], a[224:227], a[148:151], v[160:163]\n"
    "s_waitcnt lgkmcnt(2)\n"
    "s_addk_i32 m0, 1024\n"
    "s_addk_i32 s54, 16384\n"
    "v_mfma_f32_16x16x32_bf16 v[192:195], a[224:227], a[152:155], v[192:195]\n"
    "buffer_load_dwordx4 v254, s[72:75], s54 offen  lds\n"
    "v_mfma_f32_16x16x32_bf16 v[192:195], a[228:231], a[184:187], v[192:195]\n"
    "s_waitcnt lgkmcnt(0)\n"
    "v_mfma_f32_16x16x32_bf16 v[224:227], a[228:231], a[188:191], v[224:227]\n"
    "v_mfma_f32_16x16x32_bf16 v[224:227], a[224:227], a[156:159], v[224:227]\n"
    "s_addk_i32 m0, 1024\n"
    "s_addk_i32 s54, 16384\n"
    "v_mfma_f32_16x16x32_bf16 v[228:231], a[232:235], a[156:159], v[228:231]\n"
    "buffer_load_dwordx4 v254, s[72:75], s54 offen  lds\n"
    "v_mfma_f32_16x16x32_bf16 v[228:231], a[236:239], a[188:191], v[228:231]\n"
    "s_add_u32 s57, s57, 131072\n"
    "s_cmp_ge_u32 s57, 1572864\n"
    "s_cselect_b32 s57, s70, s57\n"
    "v_mfma_f32_16x16x32_bf16 v[196:199], a[236:239], a[184:187], v[196:199]\n"
    "v_mfma_f32_16x16x32_bf16 v[196:199], a[232:235], a[152:155], v[196:199]\n"
    "v_mfma_f32_16x16x32_bf16 v[164:167], a[232:235], a[148:151], v[164:167]\n"
    "v_mfma_f32_16x16x32_bf16 v[164:167], a[236:239], a[180:183], v[164:167]\n"
    "s_xor_b32 s59, s66, s67\n"
    "s_addk_i32 s67, 128\n"
    "s_cmpk_eq_u32 s67, 1536\n"
    "v_mfma_f32_16x16x32_bf16 v[132:135], a[236:239], a[176:179], v[132:135]\n"
    "s_cselect_b32 s67, 0, s67\n"
    "s_add_u32 m0, s68, 98304\n"
    "s_nop 0\n"
    "v_mfma_f32_16x16x32_bf16 v[132:135], a[232:235], a[144:147], v[132:135]\n"
    "buffer_load_dwordx4 v255, s[24:27], s59 offen  lds\n"
    "v_mfma_f32_16x16x32_bf16 v[100:103], a[232:235], a[140:143], v[100:103]\n"
    "s_bitcmp1_b32 s85, 6\n"
    "s_cbranch_scc1 CP6_%=\n"
    "CR6_%=:\n"
    "s_add_u32 m0, s68, 99328\n"
    "v_mfma_f32_16x16x32_bf16 v[100:103], a[236:239], a[172:175], v[100:103]\n"
    "s_addk_i32 s59, 16384\n"
    "v_mfma_f32_16x16x32_bf16 v[68:71], a[236:239], a[168:171], v[68:71]\n"
    "buffer_load_dwordx4 v243, s[24:27], s59 offen  lds\n"
    "v_mfma_f32_16x16x32_bf16 v[68:71], a[232:235], a[136:139], v[68:71]\n"
    "s_add_u32 m0, s68, 100352\n"
    "s_addk_i32 s59, 16384\n"
    "v_mfma_f32_16x16x32_bf16 v[36:39], a[232:235], a[132:135], v[36:39]\n"
    "buffer_load_dwordx4 v255, s[24:27], s59 offen  lds\n"
    "v_mfma_f32_16x16x32_bf16 v[36:39], a[236:239], a[164:167], v[36:39]\n"
    "v_mfma_f32_16x16x32_bf16 v[4:7], a[236:239], a[160:163], v[4:7]\n"
    "s_add_u32 m0, s68, 101376\n"
    "s_addk_i32 s59, 16384\n"
    "v_mfma_f32_16x16x32_bf16 v[4:7], a[232:235], a[128:131], v[4:7]\n"
    "buffer_load_dwordx4 v243, s[24:27], s59 offen  lds\n"
    "v_mfma_f32_16x16x32_bf16 v[8:11], a[192:195], a[128:131], v[8:11]\n"
    "s_waitcnt vmcnt(20)\n"
    "ds_read_b64_tr_b16 a[208:209], v252 offset:32768\n"
    "ds_read_b64_tr_b16 a[210:211], v252 offset:33280\n"
    "v_mfma_f32_16x16x32_bf16 v[8:11], a[196:199], a[160:163], v[8:11]\n"
    "ds_read_b64_tr_b16 a[212:213], v252 offset:36864\n"
    "ds_read_b64_tr_b16 a[214:215], v252 offset:37376\n"
    "v_mfma_f32_16x16x32_bf16 v[40:43], a[196:199], a[164:167], v[40:43]\n"
    "v_add_u32 v246, s90, v252\n"
    "ds_read_b64_tr_b16 v[248:249], v246 offset:0\n"
    "v_mfma_f32_16x16x32_bf16 v[40:43], a[192:195], a[132:135], v[40:43]\n"
    "ds_read_b64_tr_b16 v[250:251], v246 offset:512\n"
    "ds_read_b64_tr_b16 a[216:217], v252 offset:32776\n"
    "v_mfma_f32_16x16x32_bf16 v[72:75], a[192:195], a[136:139], v[72:75]\n"
    "ds_read_b64_tr_b16 a[218:219], v252 offset:33288\n"
    "v_mfma_f32_16x16x32_bf16 v[72:75], a[196:199], a[168:171], v[72:75]\n"
    "ds_read_b64_tr_b16 a[220:221], v252 offset:36872\n"
    "ds_read_b64_tr_b16 a[222:223], v252 offset:37384\n"
    "v_mfma_f32_16x16x32_bf16 v[104:107], a[196:199], a[172:175], v[104:107]\n"
    "v_mfma_f32_16x16x32_bf16 v[104:107], a[192:195], a[140:143], v[104:107]\n"
    "ds_read_b64_tr_b16 a[224:225], v253 offset:32768\n"
    "ds_read_b64_tr_b16 a[226:227], v253 offset:33280\n"
    "v_mfma_f32_16x16x32_bf16 v[136:139], a[192:195], a[144:147], v[136:139]\n"
    "v_mfma_f32_16x16x32_bf16 v[136:139], a[196:199], a[176:179], v[136:139]\n"
    "ds_read_b64_tr_b16 a[228:229], v253 offset:36864\n"
    "ds_read_b64_tr_b16 a[230:231], v253 offset:37376\n"
    "v_mfma_f32_16x16x32_bf16 v[168:171], a[196:199], a[180:183], v[168:171]\n"
    "v_mfma_f32_16x16x32_bf16 v[168:171], a[192:195], a[148:151], v[168:171]\n"
    "ds_read_b64_tr_b16 a[232:233], v253 offset:32776\n"
    "ds_read_b64_tr_b16 a[234:235], v253 offset:33288\n"
    "v_mfma_f32_16x16x32_bf16 v[200:203], a[192:195], a[152:155], v[200:203]\n"
    "s_add_u32 s62, s91, 524288\n"
    "s_waitcnt lgkmcnt(10)\n"
    "v_mfma_f32_16x16x32_bf16 v[200:203], a[196:199], a[184:187], v[200:203]\n"
    "buffer_store_dwordx4 v[248:251], v240, s[28:31], s62 offen offset:0 sc1\n"
    "s_nop 0\n"
    "ds_read_b64_tr_b16 a[236:237], v253 offset:36872\n"
    "ds_read_b64_tr_b16 a[238:239], v253 offset:37384\n"
    "v_mfma_f32_16x16x32_bf16 v[232:235], a[196:199], a[188:191], v[232:235]\n"
    "v_mfma_f32_16x16x32_bf16 v[232:235], a[192:195], a[156:159], v[232:235]\n"
    "v_mfma_f32_16x16x32_bf16 v[236:239], a[200:203], a[156:159], v[236:239]\n"
    "v_mfma_f32_16x16x32_bf16 v[236:239], a[204:207], a[188:191], v[236:239]\n"
    "v_mfma_f32_16x16x32_bf16 v[204:207], a[204:207], a[184:187], v[204:207]\n"
    "v_add_u32 v246, s90, v252\n"
    "ds_read_b64_tr_b16 v[248:249], v246 offset:8\n"
    "v_mfma_f32_16x16x32_bf16 v[204:207], a[200:203], a[152:155], v[204:207]\n"
    "ds_read_b64_tr_b16 v[250:251], v246 offset:520\n"
    "s_waitcnt lgkmcnt(2)\n"
    "v_mfma_f32_16x16x32_bf16 v[172:175], a[200:203], a[148:151], v[172:175]\n"
    "s_add_u32 m0, s79, 32768\n"
    "s_lshl_b32 s54, s64, 17\n"
    "s_xor_b32 s54, s54, s57\n"
    "v_mfma_f32_16x16x32_bf16 v[172:175], a[204:207], a[180:183], v[172:175]\n"
    "buffer_load_dwordx4 v254, s[72:75], s54 offen  lds\n"
    "v_mfma_f32_16x16x32_bf16 v[140:143], a[204:207], a[176:179], v[140:143]\n"
    "v_mfma_f32_16x16x32_bf16 v[140:143], a[200:203], a[144:147], v[140:143]\n"
    "s_addk_i32 m0, 1024\n"
    "s_addk_i32 s54, 16384\n"
    "v_mfma_f32_16x16x32_bf16 v[108:111], a[200:203], a[140:143], v[108:111]\n"
    "buffer_load_dwordx4 v254, s[72:75], s54 offen  lds\n"
    "v_mfma_f32_16x16x32_bf16 v[108:111], a[204:207], a[172:175], v[108:111]\n"
    "v_mfma_f32_16x16x32_bf16 v[76:79], a[204:207], a[168:171], v[76:79]\n"
    "v_mfma_f32_16x16x32_bf16 v[76:79], a[200:203], a[136:139], v[76:79]\n"
    "v_mfma_f32_16x16x32_bf16 v[44:47], a[200:203], a[132:135], v[44:47]\n"
    "s_add_u32 s62, s91, 524288\n"
    "s_waitcnt lgkmcnt(0)\n"
    "v_mfma_f32_16x16x32_bf16 v[44:47], a[204:207], a[164:167], v[44:47]\n"
    "buffer_store_dwordx4 v[248:251], v240, s[28:31], s62 offen offset:2048 sc1\n"
    "s_nop 0\n"
    "v_mfma_f32_16x16x32_bf16 v[12:15], a[204:207], a[160:163], v[12:15]\n"
    "v_mfma_f32_16x16x32_bf16 v[12:15], a[200:203], a[128:131], v[12:15]\n"
    "v_mfma_f32_16x16x32_bf16 v[16:19], a[208:211], a[128:131], v[16:19]\n"
    "v_mfma_f32_16x16x32_bf16 v[16:19], a[212:215], a[160:163], v[16:19]\n"
    "v_mfma_f32_16x16x32_bf16 v[48:51], a[212:215], a[164:167], v[48:51]\n"
    "v_add_u32 v246, s90, v253\n"
    "ds_read_b64_tr_b16 v[248:249], v246 offset:0\n"
    "v_mfma_f32_16x16x32_bf16 v[48:51], a[208:211], a[132:135], v[48:51]\n"
    "ds_read_b64_tr_b16 v[250:251], v246 offset:512\n"
    "v_mfma_f32_16x16x32_bf16 v[80:83], a[208:211], a[136:139], v[80:83]\n"
    "v_mfma_f32_16x16x32_bf16 v[80:83], a[212:215], a[168:171], v[80:83]\n"
    "v_mfma_f32_16x16x32_bf16 v[112:115], a[212:215], a[172:175], v[112:115]\n"
    "v_mfma_f32_16x16x32_bf16 v[112:115], a[208:211], a[140:143], v[112:115]\n"
    "v_mfma_f32_16x16x32_bf16 v[144:147], a[208:211], a[144:147], v[144:147]\n"
    "v_mfma_f32_16x16x32_bf16 v[144:147], a[212:215], a[176:179], v[144:147]\n"
    "v_mfma_f32_16x16x32_bf16 v[176:179], a[212:215], a[180:183], v[176:179]\n"
    "v_mfma_f32_16x16x32_bf16 v[176:179], a[208:211], a[148:151], v[176:179]\n"
    "v_mfma_f32_16x16x32_bf16 v[208:211], a[208:211], a[152:155], v[208:211]\n"
    "s_add_u32 s62, s91, 528384\n"
    "s_waitcnt lgkmcnt(0)\n"
    "v_mfma_f32_16x16x32_bf16 v[208:211], a[212:215], a[184:187], v[208:211]\n"
    "buffer_store_dwordx4 v[248:251], v240, s[28:31], s62 offen offset:0 sc1\n"
    "s_nop 0\n"
    "v_mfma_f32_16x16x32_bf16 a[240:243], a[212:215], a[188:191], a[240:243]\n"
    "v_mfma_f32_16x16x32_bf16 a[240:243], a[208:211], a[156:159], a[240:243]\n"
    "v_mfma_f32_16x16x32_bf16 a[244:247], a[216:219], a[156:159], a[244:247]\n"
    "v_mfma_f32_16x16x32_bf16 a[244:247], a[220:223], a[188:191], a[244:247]\n"
    "v_mfma_f32_16x16x32_bf16 v[212:215], a[220:223], a[184:187], v[212:215]\n"
    "v_add_u32 v246, s90, v253\n"
    "ds_read_b64_tr_b16 v[248:249], v246 offset:8\n"
    "v_mfma_f32_16x16x32_bf16 v[212:215], a[216:219], a[152:155], v[212:215]\n"
    "ds_read_b64_tr_b16 v[250:251], v246 offset:520\n"
    "v_mfma_f32_16x16x32_bf16 v[180:183], a[216:219], a[148:151], v[180:183]\n"
    "v_mfma_f32_16x16x32_bf16 v[180:183], a[220:223], a[180:183], v[180:183]\n"
    "v_mfma_f32_16x16x32_bf16 v[148:151], a[220:223], a[176:179], v[148:151]\n"
    "v_mfma_f32_16x16x32_bf16 v[148:151], a[216:219], a[144:147], v[148:151]\n"
    "v_mfma_f32_16x16x32_bf16 v[116:119], a[216:219], a[140:143], v[116:119]\n"
    "v_mfma_f32_16x16x32_bf16 v[116:119], a[220:223], a[172:175], v[116:119]\n"
    "v_mfma_f32_16x16x32_bf16 v[84:87], a[220:223], a[168:171], v[84:87]\n"
    "v_mfma_f32_16x16x32_bf16 v[84:87], a[216:219], a[136:139], v[84:87]\n"
    "v_mfma_f32_16x16x32_bf16 v[52:55], a[216:219], a[132:135], v[52:55]\n"
    "s_add_u32 s62, s91, 528384\n"
    "s_waitcnt lgkmcnt(0)\n"
    "v_mfma_f32_16x16x32_bf16 v[52:55], a[220:223], a[164:167], v[52:55]\n"
    "buffer_store_dwordx4 v[248:251], v240, s[28:31], s62 offen offset:2048 sc1\n"
    "s_nop 0\n"
    "v_mfma_f32_16x16x32_bf16 v[20:23], a[220:223], a[160:163], v[20:23]\n"
    "v_mfma_f32_16x16x32_bf16 v[20:23], a[216:219], a[128:131], v[20:23]\n"
    "v_add_u32 v252, 0x20000, v252\n"
    "v_add_u32 v253, 0x20000, v253\n"
    "v_mfma_f32_16x16x32_bf16 v[24:27], a[224:227], a[128:131], v[24:27]\n"
    "s_waitcnt vmcnt(18)\n"
    "ds_read_b64_tr_b16 a[192:193], v252 offset:0\n"
    "ds_read_b64_tr_b16 a[194:195], v252 offset:512\n"
    "v_mfma_f32_16x16x32_bf16 v[24:27], a[228:231], a[160:163], v[24:27]\n"
    "ds_read_b64_tr_b16 a[196:197], v252 offset:4096\n"
    "ds_read_b64_tr_b16 a[198:199], v252 offset:4608\n"
    "v_mfma_f32_16x16x32_bf16 v[56:59], a[228:231], a[164:167], v[56:59]\n"
    "v_mfma_f32_16x16x32_bf16 v[56:59], a[224:227], a[132:135], v[56:59]\n"
    "ds_read_b64_tr_b16 a[200:201], v252 offset:8\n"
    "ds_read_b64_tr_b16 a[202:203], v252 offset:520\n"
    "v_mfma_f32_16x16x32_bf16 v[88:91], a[224:227], a[136:139], v[88:91]\n"
    "v_mfma_f32_16x16x32_bf16 v[88:91], a[228:231], a[168:171], v[88:91]\n"
    "ds_read_b64_tr_b16 a[204:205], v252 offset:4104\n"
    "ds_read_b64_tr_b16 a[206:207], v252 offset:4616\n"
    "v_mfma_f32_16x16x32_bf16 v[120:123], a[228:231], a[172:175], v[120:123]\n"
    "v_mfma_f32_16x16x32_bf16 v[120:123], a[224:227], a[140:143], v[120:123]\n"
    "ds_read_b64_tr_b16 a[208:209], v253 offset:0\n"
    "ds_read_b64_tr_b16 a[210:211], v253 offset:512\n"
    "v_mfma_f32_16x16x32_bf16 v[152:155], a[224:227], a[144:147], v[152:155]\n"
    "v_mfma_f32_16x16x32_bf16 v[152:155], a[228:231], a[176:179], v[152:155]\n"
    "ds_read_b64_tr_b16 a[212:213], v253 offset:4096\n"
    "ds_read_b64_tr_b16 a[214:215], v253 offset:4608\n"
    "v_mfma_f32_16x16x32_bf16 v[184:187], a[228:231], a[180:183], v[184:187]\n"
    "v_mfma_f32_16x16x32_bf16 v[184:187], a[224:227], a[148:151], v[184:187]\n"
    "ds_read_b64_tr_b16 a[216:217], v253 offset:8\n"
    "ds_read_b64_tr_b16 a[218:219], v253 offset:520\n"
    "v_mfma_f32_16x16x32_bf16 v[216:219], a[224:227], a[152:155], v[216:219]\n"
    "v_mfma_f32_16x16x32_bf16 v[216:219], a[228:231], a[184:187], v[216:219]\n"
    "s_barrier\n"
    "ds_read_b64_tr_b16 a[220:221], v253 offset:4104\n"
    "ds_read_b64_tr_b16 a[222:223], v253 offset:4616\n"
    "v_mfma_f32_16x16x32_bf16 a[248:251], a[228:231], a[188:191], a[248:251]\n"
    "v_mfma_f32_16x16x32_bf16 a[248:251], a[224:227], a[156:159], a[248:251]\n"
    "v_lshrrev_b32 v246, 1, v240\n"
    "s_lshl_b32 s61, s65, 9\n"
    "v_mfma_f32_16x16x32_bf16 a[252:255], a[232:235], a[156:159], a[252:255]\n"
    "s_mov_b32 s82, s87\n"
    "v_mfma_f32_16x16x32_bf16 a[252:255], a[236:239], a[188:191], a[252:255]\n"
    "buffer_load_dwordx2 v[246:247], v246, s[80:83], s61 offen sc1\n"
    "v_mfma_f32_16x16x32_bf16 v[220:223], a[236:239], a[184:187], v[220:223]\n"
    "v_lshrrev_b32 v248, 2, v240\n"
    "s_lshl_b32 s61, s65, 8\n"
    "v_mfma_f32_16x16x32_bf16 v[220:223], a[232:235], a[152:155], v[220:223]\n"
    "s_mov_b32 s82, s87\n"
    "v_mfma_f32_16x16x32_bf16 v[188:191], a[232:235], a[148:151], v[188:191]\n"
    "buffer_load_dword v248, v248, s[80:83], s61 offen offset:2048 sc1\n"
    "v_mfma_f32_16x16x32_bf16 v[188:191], a[236:239], a[180:183], v[188:191]\n"
    "ds_read_b128 a[156:159], v241 offset:30720\n"
    "ds_read_b128 a[188:191], v242 offset:30720\n"
    "v_mfma_f32_16x16x32_bf16 v[156:159], a[236:239], a[176:179], v[156:159]\n"
    "ds_read_b128 a[184:187], v242 offset:28672\n"
    "ds_read_b128 a[152:155], v241 offset:28672\n"
    "v_mfma_f32_16x16x32_bf16 v[156:159], a[232:235], a[144:147], v[156:159]\n"
    "ds_read_b128 a[148:151], v241 offset:26624\n"
    "ds_read_b128 a[180:183], v242 offset:26624\n"
    "v_mfma_f32_16x16x32_bf16 v[124:127], a[232:235], a[140:143], v[124:127]\n"
    "ds_read_b128 a[176:179], v242 offset:24576\n"
    "v_mfma_f32_16x16x32_bf16 v[124:127], a[236:239], a[172:175], v[124:127]\n"
    "ds_read_b128 a[144:147], v241 offset:24576\n"
    "v_mfma_f32_16x16x32_bf16 v[92:95], a[236:239], a[168:171], v[92:95]\n"
    "ds_read_b128 a[140:143], v241 offset:22528\n"
    "v_mfma_f32_16x16x32_bf16 v[92:95], a[232:235], a[136:139], v[92:95]\n"
    "s_addk_i32 m0, 1024\n"
    "s_addk_i32 s54, 16384\n"
    "v_mfma_f32_16x16x32_bf16 v[60:63], a[232:235], a[132:135], v[60:63]\n"
    "buffer_load_dwordx4 v254, s[72:75], s54 offen  lds\n"
    "v_mfma_f32_16x16x32_bf16 v[60:63], a[236:239], a[164:167], v[60:63]\n"
    "ds_read_b128 a[172:175], v242 offset:22528\n"
    "ds_read_b128 a[168:171], v242 offset:20480\n"
    "v_mfma_f32_16x16x32_bf16 v[28:31], a[236:239], a[160:163], v[28:31]\n"
    "ds_read_b128 a[136:139], v241 offset:20480\n"
    "ds_read_b128 a[132:135], v241 offset:18432\n"
    "v_mfma_f32_16x16x32_bf16 v[28:31], a[232:235], a[128:131], v[28:31]\n"
    "s_waitcnt lgkmcnt(11)\n"
    "s_addk_i32 m0, 1024\n"
    "s_addk_i32 s54, 16384\n"
    "v_mfma_f32_16x16x32_bf16 v[224:227], a[192:195], a[156:159], v[224:227]\n"
    "buffer_load_dwordx4 v254, s[72:75], s54 offen  lds\n"
    "v_mfma_f32_16x16x32_bf16 v[224:227], a[196:199], a[188:191], v[224:227]\n"
    "s_waitcnt lgkmcnt(9)\n"
    "ds_read_b128 a[164:167], v242 offset:18432\n"
    "ds_read_b128 a[160:163], v242 offset:16384\n"
    "v_mfma_f32_16x16x32_bf16 v[192:195], a[196:199], a[184:187], v[192:195]\n"
    "ds_read_b128 a[128:131], v241 offset:16384\n"
    "v_mfma_f32_16x16x32_bf16 v[192:195], a[192:195], a[152:155], v[192:195]\n"
    "s_waitcnt lgkmcnt(10)\n"
    "s_addk_i32 m0, 1024\n"
    "s_addk_i32 s54, 16384\n"
    "v_mfma_f32_16x16x32_bf16 v[160:163], a[192:195], a[148:151], v[160:163]\n"
    "buffer_load_dwordx4 v254, s[72:75], s54 offen  lds\n"
    "v_mfma_f32_16x16x32_bf16 v[160:163], a[196:199], a[180:183], v[160:163]\n"
    "s_waitcnt lgkmcnt(8)\n"
    "v_mfma_f32_16x16x32_bf16 v[128:131], a[196:199], a[176:179], v[128:131]\n"
    "v_mfma_f32_16x16x32_bf16 v[128:131], a[192:195], a[144:147], v[128:131]\n"
    "s_waitcnt lgkmcnt(6)\n"
    "s_addk_i32 m0, 1024\n"
    "s_addk_i32 s54, 16384\n"
    "v_mfma_f32_16x16x32_bf16 v[96:99], a[192:195], a[140:143], v[96:99]\n"
    "buffer_load_dwordx4 v254, s[72:75], s54 offen  lds\n"
    "v_mfma_f32_16x16x32_bf16 v[96:99], a[196:199], a[172:175], v[96:99]\n"
    "s_waitcnt lgkmcnt(4)\n"
    "v_mfma_f32_16x16x32_bf16 v[64:67], a[196:199], a[168:171], v[64:67]\n"
    "v_mfma_f32_16x16x32_bf16 v[64:67], a[192:195], a[136:139], v[64:67]\n"
    "s_waitcnt lgkmcnt(2)\n"
    "s_addk_i32 m0, 1024\n"
    "s_addk_i32 s54, 16384\n"
    "v_mfma_f32_16x16x32_bf16 v[32:35], a[192:195], a[132:135], v[32:35]\n"
    "buffer_load_dwordx4 v254, s[72:75], s54 offen  lds\n"
    "v_mfma_f32_16x16x32_bf16 v[32:35], a[196:199], a[164:167], v[32:35]\n"
    "s_waitcnt lgkmcnt(0)\n"
    "v_mfma_f32_16x16x32_bf16 v[0:3], a[196:199], a[160:163], v[0:3]\n"
    "v_mfma_f32_16x16x32_bf16 v[0:3], a[192:195], a[128:131], v[0:3]\n"
    "s_addk_i32 m0, 1024\n"
    "s_addk_i32 s54, 16384\n"
    "v_mfma_f32_16x16x32_bf16 v[4:7], a[200:203], a[128:131], v[4:7]\n"
    "buffer_load_dwordx4 v254, s[72:75], s54 offen  lds\n"
    "v_mfma_f32_16x16x32_bf16 v[4:7], a[204:207], a[160:163], v[4:7]\n"
    "v_mfma_f32_16x16x32_bf16 v[36:39], a[204:207], a[164:167], v[36:39]\n"
    "v_mfma_f32_16x16x32_bf16 v[36:39], a[200:203], a[132:135], v[36:39]\n"
    "s_xor_b32 s59, s66, s67\n"
    "s_addk_i32 s67, 128\n"
    "s_cmpk_eq_u32 s67, 1536\n"
    "v_mfma_f32_16x16x32_bf16 v[68:71], a[200:203], a[136:139], v[68:71]\n"
    "s_cselect_b32 s67, 0, s67\n"
    "s_add_u32 m0, s68, 65536\n"
    "s_nop 0\n"
    "v_mfma_f32_16x16x32_bf16 v[68:71], a[204:207], a[168:171], v[68:71]\n"
    "buffer_load_dwordx4 v255, s[24:27], s59 offen  lds\n"
    "v_mfma_f32_16x16x32_bf16 v[100:103], a[204:207], a[172:175], v[100:103]\n"
    "v_mfma_f32_16x16x32_bf16 v[100:103], a[200:203], a[140:143], v[100:103]\n"
    "s_add_u32 m0, s68, 66560\n"
    "s_addk_i32 s59, 16384\n"
    "v_mfma_f32_16x16x32_bf16 v[132:135], a[200:203], a[144:147], v[132:135]\n"
    "buffer_load_dwordx4 v243, s[24:27], s59 offen  lds\n"
    "s_bitcmp1_b32 s85, 7\n"
    "s_cbranch_scc1 CP7_%=\n"
    "CR7_%=:\n"
    "v_mfma_f32_16x16x32_bf16 v[132:135], a[204:207], a[176:179], v[132:135]\n"
    "v_mfma_f32_16x16x32_bf16 v[164:167], a[204:207], a[180:183], v[164:167]\n"
    "v_mfma_f32_16x16x32_bf16 v[164:167], a[200:203], a[148:151], v[164:167]\n"
    "s_add_u32 m0, s68, 67584\n"
    "s_addk_i32 s59, 16384\n"
    "v_mfma_f32_16x16x32_bf16 v[196:199], a[200:203], a[152:155], v[196:199]\n"
    "buffer_load_dwordx4 v255, s[24:27], s59 offen  lds\n"
    "v_mfma_f32_16x16x32_bf16 v[196:199], a[204:207], a[184:187], v[196:199]\n"
    "v_mfma_f32_16x16x32_bf16 v[228:231], a[204:207], a[188:191], v[228:231]\n"
    "v_mfma_f32_16x16x32_bf16 v[228:231], a[200:203], a[156:159], v[228:231]\n"
    "v_add_u32 v252, 0xfffe0000, v252\n"
    "v_add_u32 v253, 0xfffe0000, v253\n"
    "v_mfma_f32_16x16x32_bf16 v[232:235], a[208:211], a[156:159], v[232:235]\n"
    "s_waitcnt vmcnt(21)\n"
    "ds_read_b64_tr_b16 a[224:225], v252 offset:0\n"
    "ds_read_b64_tr_b16 a[226:227], v252 offset:512\n"
    "v_mfma_f32_16x16x32_bf16 v[232:235], a[212:215], a[188:191], v[232:235]\n"
    "s_add_u32 m0, s68, 68608\n"
    "s_addk_i32 s59, 16384\n"
    "v_mfma_f32_16x16x32_bf16 v[200:203], a[212:215], a[184:187], v[200:203]\n"
    "buffer_load_dwordx4 v243, s[24:27], s59 offen  lds\n"
    "v_mfma_f32_16x16x32_bf16 v[200:203], a[208:211], a[152:155], v[200:203]\n"
    "ds_read_b64_tr_b16 a[228:229], v252 offset:4096\n"
    "ds_read_b64_tr_b16 a[230:231], v252 offset:4608\n"
    "v_mfma_f32_16x16x32_bf16 v[168:171], a[208:211], a[148:151], v[168:171]\n"
    "ds_read_b64_tr_b16 a[232:233], v252 offset:8\n"
    "ds_read_b64_tr_b16 a[234:235], v252 offset:520\n"
    "v_mfma_f32_16x16x32_bf16 v[168:171], a[212:215], a[180:183], v[168:171]\n"
    "s_add_u32 m0, s79, 131072\n"
    "s_lshl_b32 s54, s64, 17\n"
    "s_xor_b32 s54, s54, s57\n"
    "v_mfma_f32_16x16x32_bf16 v[136:139], a[212:215], a[176:179], v[136:139]\n"
    "s_addk_i32 s54, 512\n"
    "v_mfma_f32_16x16x32_bf16 v[136:139], a[208:211], a[144:147], v[136:139]\n"
    "buffer_load_dwordx4 v254, s[72:75], s54 offen  lds\n"
    "v_mfma_f32_16x16x32_bf16 v[104:107], a[208:211], a[140:143], v[104:107]\n"
    "ds_read_b64_tr_b16 a[236:237], v252 offset:4104\n"
    "ds_read_b64_tr_b16 a[238:239], v252 offset:4616\n"
    "v_mfma_f32_16x16x32_bf16 v[104:107], a[212:215], a[172:175], v[104:107]\n"
    "ds_read_b64_tr_b16 a[192:193], v253 offset:0\n"
    "ds_read_b64_tr_b16 a[194:195], v253 offset:512\n"
    "v_mfma_f32_16x16x32_bf16 v[72:75], a[212:215], a[168:171], v[72:75]\n"
    "s_addk_i32 m0, 1024\n"
    "s_addk_i32 s54, 16384\n"
    "v_mfma_f32_16x16x32_bf16 v[72:75], a[208:211], a[136:139], v[72:75]\n"
    "buffer_load_dwordx4 v254, s[72:75], s54 offen  lds\n"
    "v_mfma_f32_16x16x32_bf16 v[40:43], a[208:211], a[132:135], v[40:43]\n"
    "ds_read_b64_tr_b16 a[196:197], v253 offset:4096\n"
    "ds_read_b64_tr_b16 a[198:199], v253 offset:4608\n"
    "v_mfma_f32_16x16x32_bf16 v[40:43], a[212:215], a[164:167], v[40:43]\n"
    "ds_read_b64_tr_b16 a[200:201], v253 offset:8\n"
    "ds_read_b64_tr_b16 a[202:203], v253 offset:520\n"
    "v_mfma_f32_16x16x32_bf16 v[8:11], a[212:215], a[160:163], v[8:11]\n"
    "s_addk_i32 m0, 1024\n"
    "s_addk_i32 s54, 16384\n"
    "v_mfma_f32_16x16x32_bf16 v[8:11], a[208:211], a[128:131], v[8:11]\n"
    "buffer_load_dwordx4 v254, s[72:75], s54 offen  lds\n"
    "v_mfma_f32_16x16x32_bf16 v[12:15], a[216:219], a[128:131], v[12:15]\n"
    "ds_read_b64_tr_b16 a[204:205], v253 offset:4104\n"
    "ds_read_b64_tr_b16 a[206:207], v253 offset:4616\n"
    "v_mfma_f32_16x16x32_bf16 v[12:15], a[220:223], a[160:163], v[12:15]\n"
    "s_addk_i32 m0, 1024\n"
    "s_addk_i32 s54, 16384\n"
    "v_mfma_f32_16x16x32_bf16 v[44:47], a[220:223], a[164:167], v[44:47]\n"
    "buffer_load_dwordx4 v254, s[72:75], s54 offen  lds\n"
    "v_mfma_f32_16x16x32_bf16 v[44:47], a[216:219], a[132:135], v[44:47]\n"
    "s_addk_i32 m0, 1024\n"
    "s_addk_i32 s54, 16384\n"
    "v_mfma_f32_16x16x32_bf16 v[76:79], a[216:219], a[136:139], v[76:79]\n"
    "buffer_load_dwordx4 v254, s[72:75], s54 offen  lds\n"
    "v_mfma_f32_16x16x32_bf16 v[76:79], a[220:223], a[168:171], v[76:79]\n"
    "v_mfma_f32_16x16x32_bf16 v[108:111], a[220:223], a[172:175], v[108:111]\n"
    "v_mfma_f32_16x16x32_bf16 v[108:111], a[216:219], a[140:143], v[108:111]\n"
    "s_addk_i32 m0, 1024\n"
    "s_addk_i32 s54, 16384\n"
    "v_mfma_f32_16x16x32_bf16 v[140:143], a[216:219], a[144:147], v[140:143]\n"
    "buffer_load_dwordx4 v254, s[72:75], s54 offen  lds\n"
    "v_mfma_f32_16x16x32_bf16 v[140:143], a[220:223], a[176:179], v[140:143]\n"
    "v_mfma_f32_16x16x32_bf16 v[172:175], a[220:223], a[180:183], v[172:175]\n"
    "v_mfma_f32_16x16x32_bf16 v[172:175], a[216:219], a[148:151], v[172:175]\n"
    "s_addk_i32 m0, 1024\n"
    "s_addk_i32 s54, 16384\n"
    "v_mfma_f32_16x16x32_bf16 v[204:207], a[216:219], a[152:155], v[204:207]\n"
    "buffer_load_dwordx4 v254, s[72:75], s54 offen  lds\n"
    "v_mfma_f32_16x16x32_bf16 v[204:207], a[220:223], a[184:187], v[204:207]\n"
    "v_mfma_f32_16x16x32_bf16 v[236:239], a[220:223], a[188:191], v[236:239]\n"
    "v_mfma_f32_16x16x32_bf16 v[236:239], a[216:219], a[156:159], v[236:239]\n"
    "s_waitcnt lgkmcnt(12)\n"
    "s_addk_i32 m0, 1024\n"
    "s_addk_i32 s54, 16384\n"
    "v_mfma_f32_16x16x32_bf16 a[240:243], a[224:227], a[156:159], a[240:243]\n"
    "buffer_load_dwordx4 v254, s[72:75], s54 offen  lds\n"
    "v_mfma_f32_16x16x32_bf16 a[240:243], a[228:231], a[188:191], a[240:243]\n"
    "s_add_u32 s57, s57, 131072\n"
    "s_cmp_ge_u32 s57, 1572864\n"
    "s_cselect_b32 s57, s70, s57\n"
    "v_mfma_f32_16x16x32_bf16 v[208:211], a[228:231], a[184:187], v[208:211]\n"
    "v_mfma_f32_16x16x32_bf16 v[208:211], a[224:227], a[152:155], v[208:211]\n"
    "s_waitcnt lgkmcnt(0)\n"
    "s_add_u32 m0, s79, 0\n"
    "s_lshl_b32 s54, s64, 17\n"
    "v_mfma_f32_16x16x32_bf16 v[176:179], a[224:227], a[148:151], v[176:179]\n"
    "s_xor_b32 s54, s54, s57\n"
    "v_mfma_f32_16x16x32_bf16 v[176:179], a[228:231], a[180:183], v[176:179]\n"
    "buffer_load_dwordx4 v254, s[72:75], s54 offen  lds\n"
    "v_mfma_f32_16x16x32_bf16 v[144:147], a[228:231], a[176:179], v[144:147]\n"
    "v_mfma_f32_16x16x32_bf16 v[144:147], a[224:227], a[144:147], v[144:147]\n"
    "s_addk_i32 m0, 1024\n"
    "s_addk_i32 s54, 16384\n"
    "v_mfma_f32_16x16x32_bf16 v[112:115], a[224:227], a[140:143], v[112:115]\n"
    "buffer_load_dwordx4 v254, s[72:75], s54 offen  lds\n"
    "v_mfma_f32_16x16x32_bf16 v[112:115], a[228:231], a[172:175], v[112:115]\n"
    "v_mfma_f32_16x16x32_bf16 v[80:83], a[228:231], a[168:171], v[80:83]\n"
    "v_mfma_f32_16x16x32_bf16 v[80:83], a[224:227], a[136:139], v[80:83]\n"
    "s_addk_i32 m0, 1024\n"
    "s_addk_i32 s54, 16384\n"
    "v_mfma_f32_16x16x32_bf16 v[48:51], a[224:227], a[132:135], v[48:51]\n"
    "buffer_load_dwordx4 v254, s[72:75], s54 offen  lds\n"
    "v_mfma_f32_16x16x32_bf16 v[48:51], a[228:231], a[164:167], v[48:51]\n"
    "v_mfma_f32_16x16x32_bf16 v[16:19], a[228:231], a[160:163], v[16:19]\n"
    "v_mfma_f32_16x16x32_bf16 v[16:19], a[224:227], a[128:131], v[16:19]\n"
    "s_addk_i32 m0, 1024\n"
    "s_addk_i32 s54, 16384\n"
    "v_mfma_f32_16x16x32_bf16 v[20:23], a[232:235], a[128:131], v[20:23]\n"
    "buffer_load_dwordx4 v254, s[72:75], s54 offen  lds\n"
    "v_mfma_f32_16x16x32_bf16 v[20:23], a[236:239], a[160:163], v[20:23]\n"
    "v_mfma_f32_16x16x32_bf16 v[52:55], a[236:239], a[164:167], v[52:55]\n"
    "v_mfma_f32_16x16x32_bf16 v[52:55], a[232:235], a[132:135], v[52:55]\n"
    "s_addk_i32 m0, 1024\n"
    "s_addk_i32 s54, 16384\n"
    "v_mfma_f32_16x16x32_bf16 v[84:87], a[232:235], a[136:139], v[84:87]\n"
    "buffer_load_dwordx4 v254, s[72:75], s54 offen  lds\n"
    "v_mfma_f32_16x16x32_bf16 v[84:87], a[236:239], a[168:171], v[84:87]\n"
    "v_mfma_f32_16x16x32_bf16 v[116:119], a[236:239], a[172:175], v[116:119]\n"
    "v_mfma_f32_16x16x32_bf16 v[116:119], a[232:235], a[140:143], v[116:119]\n"
    "s_addk_i32 m0, 1024\n"
    "s_addk_i32 s54, 16384\n"
    "v_mfma_f32_16x16x32_bf16 v[148:151], a[232:235], a[144:147], v[148:151]\n"
    "buffer_load_dwordx4 v254, s[72:75], s54 offen  lds\n"
    "v_mfma_f32_16x16x32_bf16 v[148:151], a[236:239], a[176:179], v[148:151]\n"
    "v_mfma_f32_16x16x32_bf16 v[180:183], a[236:239], a[180:183], v[180:183]\n"
    "v_mfma_f32_16x16x32_bf16 v[180:183], a[232:235], a[148:151], v[180:183]\n"
    "s_addk_i32 m0, 1024\n"
    "s_addk_i32 s54, 16384\n"
    "v_mfma_f32_16x16x32_bf16 v[212:215], a[232:235], a[152:155], v[212:215]\n"
    "buffer_load_dwordx4 v254, s[72:75], s54 offen  lds\n"
    "v_mfma_f32_16x16x32_bf16 v[212:215], a[236:239], a[184:187], v[212:215]\n"
    "v_mfma_f32_16x16x32_bf16 a[244:247], a[236:239], a[188:191], a[244:247]\n"
    "v_mfma_f32_16x16x32_bf16 a[244:247], a[232:235], a[156:159], a[244:247]\n"
    "s_waitcnt vmcnt(19)\n"
    "ds_read_b64_tr_b16 a[208:209], v252 offset:32768\n"
    "ds_read_b64_tr_b16 a[210:211], v252 offset:33280\n"
    "v_mfma_f32_16x16x32_bf16 a[248:251], a[192:195], a[156:159], a[248:251]\n"
    "s_addk_i32 m0, 1024\n"
    "s_addk_i32 s54, 16384\n"
    "v_mfma_f32_16x16x32_bf16 a[248:251], a[196:199], a[188:191], a[248:251]\n"
    "buffer_load_dwordx4 v254, s[72:75], s54 offen  lds\n"
    "v_mfma_f32_16x16x32_bf16 v[216:219], a[196:199], a[184:187], v[216:219]\n"
    "ds_read_b64_tr_b16 a[212:213], v252 offset:36864\n"
    "ds_read_b64_tr_b16 a[214:215], v252 offset:37376\n"
    "v_mfma_f32_16x16x32_bf16 v[216:219], a[192:195], a[152:155], v[216:219]\n"
    "ds_read_b64_tr_b16 a[216:217], v252 offset:32776\n"
    "ds_read_b64_tr_b16 a[218:219], v252 offset:33288\n"
    "v_mfma_f32_16x16x32_bf16 v[184:187], a[192:195], a[148:151], v[184:187]\n"
    "v_mfma_f32_16x16x32_bf16 v[184:187], a[196:199], a[180:183], v[184:187]\n"
    "ds_read_b64_tr_b16 a[220:221], v252 offset:36872\n"
    "ds_read_b64_tr_b16 a[222:223], v252 offset:37384\n"
    "v_mfma_f32_16x16x32_bf16 v[152:155], a[196:199], a[176:179], v[152:155]\n"
    "v_mfma_f32_16x16x32_bf16 v[152:155], a[192:195], a[144:147], v[152:155]\n"
    "ds_read_b64_tr_b16 a[224:225], v253 offset:32768\n"
    "ds_read_b64_tr_b16 a[226:227], v253 offset:33280\n"
    "v_mfma_f32_16x16x32_bf16 v[120:123], a[192:195], a[140:143], v[120:123]\n"
    "v_mfma_f32_16x16x32_bf16 v[120:123], a[196:199], a[172:175], v[120:123]\n"
    "ds_read_b64_tr_b16 a[228:229], v253 offset:36864\n"
    "ds_read_b64_tr_b16 a[230:231], v253 offset:37376\n"
    "v_mfma_f32_16x16x32_bf16 v[88:91], a[196:199], a[168:171], v[88:91]\n"
    "v_mfma_f32_16x16x32_bf16 v[88:91], a[192:195], a[136:139], v[88:91]\n"
    "ds_read_b64_tr_b16 a[232:233], v253 offset:32776\n"
    "ds_read_b64_tr_b16 a[234:235], v253 offset:33288\n"
    "v_mfma_f32_16x16x32_bf16 v[56:59], a[192:195], a[132:135], v[56:59]\n"
    "v_mfma_f32_16x16x32_bf16 v[56:59], a[196:199], a[164:167], v[56:59]\n"
    "v_cmp_ne_u32 vcc, v246, v248\n"
    "s_and_b32 s61, s86, 1\n"
    "s_nop 0\n"
    "s_andn2_b32 vcc_lo, vcc_lo, s61\n"
    "s_cmp_eq_u64 vcc, 0\n"
    "s_cbranch_scc1 LA1_%=\n"
    "s_mov_b32 s69, 0x400000\n"
    "LA2_%=:\n"
    "s_sleep 2\n"
    "v_lshrrev_b32 v246, 1, v240\n"
    "s_lshl_b32 s61, s65, 9\n"
    "s_mov_b32 s82, s87\n"
    "buffer_load_dwordx2 v[246:247], v246, s[80:83], s61 offen sc1\n"
    "s_waitcnt vmcnt(0)\n"
    "s_sub_u32 s69, s69, 1\n"
    "s_cbranch_scc1 LA1_%=\n"
    "v_cmp_ne_u32 vcc, v246, v248\n"
    "s_and_b32 s61, s86, 1\n"
    "s_nop 0\n"
    "s_andn2_b32 vcc_lo, vcc_lo, s61\n"
    "s_cmp_eq_u64 vcc, 0\n"
    "s_cbranch_scc0 LA2_%=\n"
    "LA1_%=:\n"
    "s_barrier\n"
    "v_mfma_f32_16x16x32_bf16 v[24:27], a[196:199], a[160:163], v[24:27]\n"
    "ds_read_b64_tr_b16 a[236:237], v253 offset:36872\n"
    "ds_read_b64_tr_b16 a[238:239], v253 offset:37384\n"
    "v_mfma_f32_16x16x32_bf16 v[24:27], a[192:195], a[128:131], v[24:27]\n"
    "v_mfma_f32_16x16x32_bf16 v[28:31], a[200:203], a[128:131], v[28:31]\n"
    "v_mfma_f32_16x16x32_bf16 v[28:31], a[204:207], a[160:163], v[28:31]\n"
    "v_mfma_f32_16x16x32_bf16 v[60:63], a[204:207], a[164:167], v[60:63]\n"
    "ds_read_b128 a[128:131], v241 offset:32768\n"
    "v_mfma_f32_16x16x32_bf16 v[60:63], a[200:203], a[132:135], v[60:63]\n"
    "s_waitcnt lgkmcnt(1)\n"
    "s_add_u32 m0, s79, 32768\n"
    "s_lshl_b32 s54, s64, 17\n"
    "v_mfma_f32_16x16x32_bf16 v[92:95], a[200:203], a[136:139], v[92:95]\n"
    "s_xor_b32 s54, s54, s57\n"
    "s_addk_i32 s54, 512\n"
    "v_mfma_f32_16x16x32_bf16 v[92:95], a[204:207], a[168:171], v[92:95]\n"
    "buffer_load_dwordx4 v254, s[72:75], s54 offen  lds\n"
    "v_mfma_f32_16x16x32_bf16 v[124:127], a[204:207], a[172:175], v[124:127]\n"
    "ds_read_b128 a[160:163], v242 offset:32768\n"
    "ds_read_b128 a[164:167], v242 offset:34816\n"
    "v_mfma_f32_16x16x32_bf16 v[124:127], a[200:203], a[140:143], v[124:127]\n"
    "ds_read_b128 a[132:135], v241 offset:34816\n"
    "ds_read_b128 a[136:139], v241 offset:36864\n"
    "v_mfma_f32_16x16x32_bf16 v[156:159], a[200:203], a[144:147], v[156:159]\n"
    "s_addk_i32 m0, 1024\n"
    "s_addk_i32 s54, 16384\n"
    "v_mfma_f32_16x16x32_bf16 v[156:159], a[204:207], a[176:179], v[156:159]\n"
    "buffer_load_dwordx4 v254, s[72:75], s54 offen  lds\n"
    "v_mfma_f32_16x16x32_bf16 v[188:191], a[204:207], a[180:183], v[188:191]\n"
    "ds_read_b128 a[168:171], v242 offset:36864\n"
    "ds_read_b128 a[172:175], v242 offset:38912\n"
    "v_mfma_f32_16x16x32_bf16 v[188:191], a[200:203], a[148:151], v[188:191]\n"
    "ds_read_b128 a[140:143], v241 offset:38912\n"
    "ds_read_b128 a[144:147], v241 offset:40960\n"
    "v_mfma_f32_16x16x32_bf16 v[220:223], a[200:203], a[152:155], v[220:223]\n"
    "s_addk_i32 m0, 1024\n"
    "s_addk_i32 s54, 16384\n"
    "v_mfma_f32_16x16x32_bf16 v[220:223], a[204:207], a[184:187], v[220:223]\n"
    "buffer_load_dwordx4 v254, s[72:75], s54 offen  lds\n"
    "v_mfma_f32_16x16x32_bf16 a[252:255], a[204:207], a[188:191], a[252:255]\n"
    "ds_read_b128 a[176:179], v242 offset:40960\n"
    "ds_read_b128 a[180:183], v242 offset:43008\n"
    "v_mfma_f32_16x16x32_bf16 a[252:255], a[200:203], a[156:159], a[252:255]\n"
    "s_waitcnt lgkmcnt(9)\n"
    "ds_read_b128 a[148:151], v241 offset:43008\n"
    "ds_read_b128 a[152:155], v241 offset:45056\n"
    "v_mfma_f32_16x16x32_bf16 v[0:3], a[208:211], a[128:131], v[0:3]\n"
    "s_addk_i32 m0, 1024\n"
    "s_addk_i32 s54, 16384\n"
    "v_mfma_f32_16x16x32_bf16 v[0:3], a[212:215], a[160:163], v[0:3]\n"
    "s_waitcnt lgkmcnt(9)\n"
    "v_mfma_f32_16x16x32_bf16 v[32:35], a[212:215], a[164:167], v[32:35]\n"
    "buffer_load_dwordx4 v254, s[72:75], s54 offen  lds\n"
    "ds_read_b128 a[184:187], v242 offset:45056\n"
    "ds_read_b128 a[188:191], v242 offset:47104\n"
    "ds_read_b128 a[156:159], v241 offset:47104\n"
    "v_lshrrev_b32 v246, 2, v240\n"
    "v_mfma_f32_16x16x32_bf16 v[32:35], a[208:211], a[132:135], v[32:35]\n"
    "s_waitcnt lgkmcnt(10)\n"
    "s_lshl_b32 s61, s65, 8\n"
    "s_mov_b32 s82, s87\n"
    "v_mfma_f32_16x16x32_bf16 v[64:67], a[208:211], a[136:139], v[64:67]\n"
    "buffer_store_dword v247, v246, s[80:83], s61 offen offset:3072 sc1\n"
    "v_mfma_f32_16x16x32_bf16 v[64:67], a[212:215], a[168:171], v[64:67]\n"
    "s_waitcnt lgkmcnt(8)\n"
    "s_addk_i32 m0, 1024\n"
    "s_addk_i32 s54, 16384\n"
    "v_mfma_f32_16x16x32_bf16 v[96:99], a[212:215], a[172:175], v[96:99]\n"
    "buffer_load_dwordx4 v254, s[72:75], s54 offen  lds\n"
    "v_mfma_f32_16x16x32_bf16 v[96:99], a[208:211], a[140:143], v[96:99]\n"
    "s_waitcnt lgkmcnt(6)\n"
    "s_addk_i32 m0, 1024\n"
    "s_addk_i32 s54, 16384\n"
    "v_mfma_f32_16x16x32_bf16 v[128:131], a[208:211], a[144:147], v[128:131]\n"
    "buffer_load_dwordx4 v254, s[72:75], s54 offen  lds\n"
    "v_mfma_f32_16x16x32_bf16 v[128:131], a[212:215], a[176:179], v[128:131]\n"
    "s_waitcnt lgkmcnt(4)\n"
    "v_mfma_f32_16x16x32_bf16 v[160:163], a[212:215], a[180:183], v[160:163]\n"
    "v_mfma_f32_16x16x32_bf16 v[160:163], a[208:211], a[148:151], v[160:163]\n"
    "s_waitcnt lgkmcnt(2)\n"
    "s_addk_i32 m0, 1024\n"
    "s_addk_i32 s54, 16384\n"
    "v_mfma_f32_16x16x32_bf16 v[192:195], a[208:211], a[152:155], v[192:195]\n"
    "buffer_load_dwordx4 v254, s[72:75], s54 offen  lds\n"
    "v_mfma_f32_16x16x32_bf16 v[192:195], a[212:215], a[184:187], v[192:195]\n"
    "s_waitcnt lgkmcnt(0)\n"
    "v_mfma_f32_16x16x32_bf16 v[224:227], a[212:215], a[188:191], v[224:227]\n"
    "v_mfma_f32_16x16x32_bf16 v[224:227], a[208:211], a[156:159], v[224:227]\n"
    "s_addk_i32 m0, 1024\n"
    "s_addk_i32 s54, 16384\n"
    "v_mfma_f32_16x16x32_bf16 v[228:231], a[216:219], a[156:159], v[228:231]\n"
    "buffer_load_dwordx4 v254, s[72:75], s54 offen  lds\n"
    "v_mfma_f32_16x16x32_bf16 v[228:231], a[220:223], a[188:191], v[228:231]\n"
    "s_add_u32 s57, s57, 131072\n"
    "s_cmp_ge_u32 s57, 1572864\n"
    "s_cselect_b32 s57, s70, s57\n"
    "v_mfma_f32_16x16x32_bf16 v[196:199], a[220:223], a[184:187], v[196:199]\n"
    "v_mfma_f32_16x16x32_bf16 v[196:199], a[216:219], a[152:155], v[196:199]\n"
    "v_mfma_f32_16x16x32_bf16 v[164:167], a[216:219], a[148:151], v[164:167]\n"
    "s_xor_b32 s59, s66, s67\n"
    "s_addk_i32 s67, 128\n"
    "s_cmpk_eq_u32 s67, 1536\n"
    "v_mfma_f32_16x16x32_bf16 v[164:167], a[220:223], a[180:183], v[164:167]\n"
    "s_cselect_b32 s67, 0, s67\n"
    "s_add_u32 m0, s68, 81920\n"
    "s_nop 0\n"
    "v_mfma_f32_16x16x32_bf16 v[132:135], a[220:223], a[176:179], v[132:135]\n"
    "buffer_load_dwordx4 v255, s[24:27], s59 offen  lds\n"
    "v_mfma_f32_16x16x32_bf16 v[132:135], a[216:219], a[144:147], v[132:135]\n"
    "s_add_u32 m0, s68, 82944\n"
    "s_addk_i32 s59, 16384\n"
    "v_mfma_f32_16x16x32_bf16 v[100:103], a[216:219], a[140:143], v[100:103]\n"
    "buffer_load_dwordx4 v243, s[24:27], s59 offen  lds\n"
    "s_bitcmp1_b32 s85, 8\n"
    "s_cbranch_scc1 CP8_%=\n"
    "CR8_%=:\n"
    "v_mfma_f32_16x16x32_bf16 v[100:103], a[220:223], a[172:175], v[100:103]\n"
    "v_mfma_f32_16x16x32_bf16 v[68:71], a[220:223], a[168:171], v[68:71]\n"
    "s_add_u32 m0, s68, 83968\n"
    "s_addk_i32 s59, 16384\n"
    "v_mfma_f32_16x16x32_bf16 v[68:71], a[216:219], a[136:139], v[68:71]\n"
    "buffer_load_dwordx4 v255, s[24:27], s59 offen  lds\n"
    "v_mfma_f32_16x16x32_bf16 v[36:39], a[216:219], a[132:135], v[36:39]\n"
    "v_mfma_f32_16x16x32_bf16 v[36:39], a[220:223], a[164:167], v[36:39]\n"
    "s_add_u32 m0, s68, 84992\n"
    "s_addk_i32 s59, 16384\n"
    "v_mfma_f32_16x16x32_bf16 v[4:7], a[220:223], a[160:163], v[4:7]\n"
    "buffer_load_dwordx4 v243, s[24:27], s59 offen  lds\n"
    "v_mfma_f32_16x16x32_bf16 v[4:7], a[216:219], a[128:131], v[4:7]\n"
    "v_add_u32 v252, 0x20000, v252\n"
    "v_add_u32 v253, 0x20000, v253\n"
    "v_mfma_f32_16x16x32_bf16 v[8:11], a[224:227], a[128:131], v[8:11]\n"
    "s_waitcnt vmcnt(21)\n"
    "ds_read_b64_tr_b16 a[192:193], v252 offset:0\n"
    "ds_read_b64_tr_b16 a[194:195], v252 offset:512\n"
    "v_mfma_f32_16x16x32_bf16 v[8:11], a[228:231], a[160:163], v[8:11]\n"
    "s_add_u32 m0, s68, 114688\n"
    "s_add_u32 s60, s78, 8388608\n"
    "v_mfma_f32_16x16x32_bf16 v[40:43], a[228:231], a[164:167], v[40:43]\n"
    "buffer_load_dwordx4 v254, s[72:75], s60 offen nt lds\n"
    "v_mfma_f32_16x16x32_bf16 v[40:43], a[224:227], a[132:135], v[40:43]\n"
    "ds_read_b64_tr_b16 a[196:197], v252 offset:4096\n"
    "ds_read_b64_tr_b16 a[198:199], v252 offset:4608\n"
    "v_mfma_f32_16x16x32_bf16 v[72:75], a[224:227], a[136:139], v[72:75]\n"
    "ds_read_b64_tr_b16 a[200:201], v252 offset:8\n"
    "ds_read_b64_tr_b16 a[202:203], v252 offset:520\n"
    "v_mfma_f32_16x16x32_bf16 v[72:75], a[228:231], a[168:171], v[72:75]\n"
    "s_add_u32 m0, s68, 115712\n"
    "s_add_u32 s60, s78, 8404992\n"
    "v_mfma_f32_16x16x32_bf16 v[104:107], a[228:231], a[172:175], v[104:107]\n"
    "buffer_load_dwordx4 v254, s[72:75], s60 offen nt lds\n"
    "v_mfma_f32_16x16x32_bf16 v[104:107], a[224:227], a[140:143], v[104:107]\n"
    "ds_read_b64_tr_b16 a[204:205], v252 offset:4104\n"
    "ds_read_b64_tr_b16 a[206:207], v252 offset:4616\n"
    "v_mfma_f32_16x16x32_bf16 v[136:139], a[224:227], a[144:147], v[136:139]\n"
    "s_add_u32 m0, s68, 116736\n"
    "s_add_u32 s60, s78, 8421376\n"
    "v_mfma_f32_16x16x32_bf16 v[136:139], a[228:231], a[176:179], v[136:139]\n"
    "buffer_load_dwordx4 v254, s[72:75], s60 offen nt lds\n"
    "v_mfma_f32_16x16x32_bf16 v[168:171], a[228:231], a[180:183], v[168:171]\n"
    "ds_read_b64_tr_b16 a[208:209], v253 offset:0\n"
    "ds_read_b64_tr_b16 a[210:211], v253 offset:512\n"
    "v_mfma_f32_16x16x32_bf16 v[168:171], a[224:227], a[148:151], v[168:171]\n"
    "ds_read_b64_tr_b16 a[212:213], v253 offset:4096\n"
    "ds_read_b64_tr_b16 a[214:215], v253 offset:4608\n"
    "v_mfma_f32_16x16x32_bf16 v[200:203], a[224:227], a[152:155], v[200:203]\n"
    "s_add_u32 m0, s68, 117760\n"
    "s_add_u32 s60, s78, 8437760\n"
    "v_mfma_f32_16x16x32_bf16 v[200:203], a[228:231], a[184:187], v[200:203]\n"
    "buffer_load_dwordx4 v254, s[72:75], s60 offen nt lds\n"
    "v_mfma_f32_16x16x32_bf16 v[232:235], a[228:231], a[188:191], v[232:235]\n"
    "ds_read_b64_tr_b16 a[216:217], v253 offset:8\n"
    "ds_read_b64_tr_b16 a[218:219], v253 offset:520\n"
    "v_mfma_f32_16x16x32_bf16 v[232:235], a[224:227], a[156:159], v[232:235]\n"
    "ds_read_b64_tr_b16 a[220:221], v253 offset:4104\n"
    "ds_read_b64_tr_b16 a[222:223], v253 offset:4616\n"
    "v_mfma_f32_16x16x32_bf16 v[236:239], a[232:235], a[156:159], v[236:239]\n"
    "v_mfma_f32_16x16x32_bf16 v[236:239], a[236:239], a[188:191], v[236:239]\n"
    "v_mfma_f32_16x16x32_bf16 v[204:207], a[236:239], a[184:187], v[204:207]\n"
    "v_mfma_f32_16x16x32_bf16 v[204:207], a[232:235], a[152:155], v[204:207]\n"
    "v_mfma_f32_16x16x32_bf16 v[172:175], a[232:235], a[148:151], v[172:175]\n"
    "v_mfma_f32_16x16x32_bf16 v[172:175], a[236:239], a[180:183], v[172:175]\n"
    "v_mfma_f32_16x16x32_bf16 v[140:143], a[236:239], a[176:179], v[140:143]\n"
    "v_mfma_f32_16x16x32_bf16 v[140:143], a[232:235], a[144:147], v[140:143]\n"
    "v_mfma_f32_16x16x32_bf16 v[108:111], a[232:235], a[140:143], v[108:111]\n"
    "v_mfma_f32_16x16x32_bf16 v[108:111], a[236:239], a[172:175], v[108:111]\n"
    "v_mfma_f32_16x16x32_bf16 v[76:79], a[236:239], a[168:171], v[76:79]\n"
    "v_mfma_f32_16x16x32_bf16 v[76:79], a[232:235], a[136:139], v[76:79]\n"
    "v_mfma_f32_16x16x32_bf16 v[44:47], a[232:235], a[132:135], v[44:47]\n"
    "v_mfma_f32_16x16x32_bf16 v[44:47], a[236:239], a[164:167], v[44:47]\n"
    "v_mfma_f32_16x16x32_bf16 v[12:15], a[236:239], a[160:163], v[12:15]\n"
    "v_mfma_f32_16x16x32_bf16 v[12:15], a[232:235], a[128:131], v[12:15]\n"
    "s_waitcnt lgkmcnt(12)\n"
    "v_mfma_f32_16x16x32_bf16 v[16:19], a[192:195], a[128:131], v[16:19]\n"
    "v_mfma_f32_16x16x32_bf16 v[16:19], a[196:199], a[160:163], v[16:19]\n"
    "v_mfma_f32_16x16x32_bf16 v[48:51], a[196:199], a[164:167], v[48:51]\n"
    "v_mfma_f32_16x16x32_bf16 v[48:51], a[192:195], a[132:135], v[48:51]\n"
    "v_mfma_f32_16x16x32_bf16 v[80:83], a[192:195], a[136:139], v[80:83]\n"
    "v_mfma_f32_16x16x32_bf16 v[80:83], a[196:199], a[168:171], v[80:83]\n"
    "v_mfma_f32_16x16x32_bf16 v[112:115], a[196:199], a[172:175], v[112:115]\n"
    "v_mfma_f32_16x16x32_bf16 v[112:115], a[192:195], a[140:143], v[112:115]\n"
    "v_mfma_f32_16x16x32_bf16 v[144:147], a[192:195], a[144:147], v[144:147]\n"
    "v_mfma_f32_16x16x32_bf16 v[144:147], a[196:199], a[176:179], v[144:147]\n"
    "v_mfma_f32_16x16x32_bf16 v[176:179], a[196:199], a[180:183], v[176:179]\n"
    "v_mfma_f32_16x16x32_bf16 v[176:179], a[192:195], a[148:151], v[176:179]\n"
    "v_mfma_f32_16x16x32_bf16 v[208:211], a[192:195], a[152:155], v[208:211]\n"
    "v_mfma_f32_16x16x32_bf16 v[208:211], a[196:199], a[184:187], v[208:211]\n"
    "v_mfma_f32_16x16x32_bf16 a[240:243], a[196:199], a[188:191], a[240:243]\n"
    "v_mfma_f32_16x16x32_bf16 a[240:243], a[192:195], a[156:159], a[240:243]\n"
    "s_waitcnt lgkmcnt(8)\n"
    "v_mfma_f32_16x16x32_bf16 a[244:247], a[200:203], a[156:159], a[244:247]\n"
    "v_mfma_f32_16x16x32_bf16 a[244:247], a[204:207], a[188:191], a[244:247]\n"
    "v_mfma_f32_16x16x32_bf16 v[212:215], a[204:207], a[184:187], v[212:215]\n"
    "v_mfma_f32_16x16x32_bf16 v[212:215], a[200:203], a[152:155], v[212:215]\n"
    "v_mfma_f32_16x16x32_bf16 v[180:183], a[200:203], a[148:151], v[180:183]\n"
    "v_mfma_f32_16x16x32_bf16 v[180:183], a[204:207], a[180:183], v[180:183]\n"
    "v_mfma_f32_16x16x32_bf16 v[148:151], a[204:207], a[176:179], v[148:151]\n"
    "v_mfma_f32_16x16x32_bf16 v[148:151], a[200:203], a[144:147], v[148:151]\n"
    "v_mfma_f32_16x16x32_bf16 v[116:119], a[200:203], a[140:143], v[116:119]\n"
    "v_mfma_f32_16x16x32_bf16 v[116:119], a[204:207], a[172:175], v[116:119]\n"
    "v_mfma_f32_16x16x32_bf16 v[84:87], a[204:207], a[168:171], v[84:87]\n"
    "v_mfma_f32_16x16x32_bf16 v[84:87], a[200:203], a[136:139], v[84:87]\n"
    "v_mfma_f32_16x16x32_bf16 v[52:55], a[200:203], a[132:135], v[52:55]\n"
    "v_mfma_f32_16x16x32_bf16 v[52:55], a[204:207], a[164:167], v[52:55]\n"
    "v_mfma_f32_16x16x32_bf16 v[20:23], a[204:207], a[160:163], v[20:23]\n"
    "v_mfma_f32_16x16x32_bf16 v[20:23], a[200:203], a[128:131], v[20:23]\n"
    "s_waitcnt lgkmcnt(4)\n"
    "v_add_u32 v252, 0xfffe0000, v252\n"
    "v_add_u32 v253, 0xfffe0000, v253\n"
    "v_mfma_f32_16x16x32_bf16 v[24:27], a[208:211], a[128:131], v[24:27]\n"
    "s_waitcnt vmcnt(17)\n"
    "ds_read_b64_tr_b16 a[224:225], v252 offset:0\n"
    "ds_read_b64_tr_b16 a[226:227], v252 offset:512\n"
    "v_mfma_f32_16x16x32_bf16 v[24:27], a[212:215], a[160:163], v[24:27]\n"
    "ds_read_b64_tr_b16 a[228:229], v252 offset:4096\n"
    "ds_read_b64_tr_b16 a[230:231], v252 offset:4608\n"
    "v_mfma_f32_16x16x32_bf16 v[56:59], a[212:215], a[164:167], v[56:59]\n"
    "v_mfma_f32_16x16x32_bf16 v[56:59], a[208:211], a[132:135], v[56:59]\n"
    "ds_read_b64_tr_b16 a[232:233], v252 offset:8\n"
    "ds_read_b64_tr_b16 a[234:235], v252 offset:520\n"
    "v_mfma_f32_16x16x32_bf16 v[88:91], a[208:211], a[136:139], v[88:91]\n"
    "v_mfma_f32_16x16x32_bf16 v[88:91], a[212:215], a[168:171], v[88:91]\n"
    "ds_read_b64_tr_b16 a[236:237], v252 offset:4104\n"
    "ds_read_b64_tr_b16 a[238:239], v252 offset:4616\n"
    "v_mfma_f32_16x16x32_bf16 v[120:123], a[212:215], a[172:175], v[120:123]\n"
    "v_mfma_f32_16x16x32_bf16 v[120:123], a[208:211], a[140:143], v[120:123]\n"
    "ds_read_b64_tr_b16 a[192:193], v253 offset:0\n"
    "ds_read_b64_tr_b16 a[194:195], v253 offset:512\n"
    "v_mfma_f32_16x16x32_bf16 v[152:155], a[208:211], a[144:147], v[152:155]\n"
    "v_mfma_f32_16x16x32_bf16 v[152:155], a[212:215], a[176:179], v[152:155]\n"
    "ds_read_b64_tr_b16 a[196:197], v253 offset:4096\n"
    "ds_read_b64_tr_b16 a[198:199], v253 offset:4608\n"
    "v_mfma_f32_16x16x32_bf16 v[184:187], a[212:215], a[180:183], v[184:187]\n"
    "v_mfma_f32_16x16x32_bf16 v[184:187], a[208:211], a[148:151], v[184:187]\n"
    "ds_read_b64_tr_b16 a[200:201], v253 offset:8\n"
    "ds_read_b64_tr_b16 a[202:203], v253 offset:520\n"
    "v_mfma_f32_16x16x32_bf16 v[216:219], a[208:211], a[152:155], v[216:219]\n"
    "v_mfma_f32_16x16x32_bf16 v[216:219], a[212:215], a[184:187], v[216:219]\n"
    "s_barrier\n"
    "ds_read_b64_tr_b16 a[204:205], v253 offset:4104\n"
    "ds_read_b64_tr_b16 a[206:207], v253 offset:4616\n"
    "v_mfma_f32_16x16x32_bf16 a[248:251], a[212:215], a[188:191], a[248:251]\n"
    "v_mfma_f32_16x16x32_bf16 a[248:251], a[208:211], a[156:159], a[248:251]\n"
    "s_waitcnt lgkmcnt(15)\n"
    "v_mfma_f32_16x16x32_bf16 a[252:255], a[216:219], a[156:159], a[252:255]\n"
    "v_mfma_f32_16x16x32_bf16 a[252:255], a[220:223], a[188:191], a[252:255]\n"
    "v_mfma_f32_16x16x32_bf16 v[220:223], a[220:223], a[184:187], v[220:223]\n"
    "ds_read_b128 a[156:159], v241 offset:14336\n"
    "v_mfma_f32_16x16x32_bf16 v[220:223], a[216:219], a[152:155], v[220:223]\n"
    "ds_read_b128 a[188:191], v242 offset:14336\n"
    "v_mfma_f32_16x16x32_bf16 v[188:191], a[216:219], a[148:151], v[188:191]\n"
    "ds_read_b128 a[184:187], v242 offset:12288\n"
    "v_mfma_f32_16x16x32_bf16 v[188:191], a[220:223], a[180:183], v[188:191]\n"
    "ds_read_b128 a[152:155], v241 offset:12288\n"
    "v_mfma_f32_16x16x32_bf16 v[156:159], a[220:223], a[176:179], v[156:159]\n"
    "ds_read_b128 a[148:151], v241 offset:10240\n"
    "v_mfma_f32_16x16x32_bf16 v[156:159], a[216:219], a[144:147], v[156:159]\n"
    "ds_read_b128 a[180:183], v242 offset:10240\n"
    "v_mfma_f32_16x16x32_bf16 v[124:127], a[216:219], a[140:143], v[124:127]\n"
    "ds_read_b128 a[176:179], v242 offset:8192\n"
    "v_mfma_f32_16x16x32_bf16 v[124:127], a[220:223], a[172:175], v[124:127]\n"
    "ds_read_b128 a[144:147], v241 offset:8192\n"
    "v_mfma_f32_16x16x32_bf16 v[92:95], a[220:223], a[168:171], v[92:95]\n"
    "ds_read_b128 a[140:143], v241 offset:6144\n"
    "v_mfma_f32_16x16x32_bf16 v[92:95], a[216:219], a[136:139], v[92:95]\n"
    "s_add_u32 m0, s79, 131072\n"
    "s_lshl_b32 s54, s64, 17\n"
    "s_xor_b32 s54, s54, s57\n"
    "v_mfma_f32_16x16x32_bf16 v[60:63], a[216:219], a[132:135], v[60:63]\n"
    "buffer_load_dwordx4 v254, s[72:75], s54 offen  lds\n"
    "v_mfma_f32_16x16x32_bf16 v[60:63], a[220:223], a[164:167], v[60:63]\n"
    "ds_read_b128 a[172:175], v242 offset:6144\n"
    "ds_read_b128 a[168:171], v242 offset:4096\n"
    "v_mfma_f32_16x16x32_bf16 v[28:31], a[220:223], a[160:163], v[28:31]\n"
    "ds_read_b128 a[136:139], v241 offset:4096\n"
    "ds_read_b128 a[132:135], v241 offset:2048\n"
    "v_mfma_f32_16x16x32_bf16 v[28:31], a[216:219], a[128:131], v[28:31]\n"
    "s_waitcnt lgkmcnt(11)\n"
    "s_addk_i32 m0, 1024\n"
    "s_addk_i32 s54, 16384\n"
    "v_mfma_f32_16x16x32_bf16 v[224:227], a[224:227], a[156:159], v[224:227]\n"
    "buffer_load_dwordx4 v254, s[72:75], s54 offen  lds\n"
    "v_mfma_f32_16x16x32_bf16 v[224:227], a[228:231], a[188:191], v[224:227]\n"
    "s_waitcnt lgkmcnt(9)\n"
    "ds_read_b128 a[164:167], v242 offset:2048\n"
    "ds_read_b128 a[160:163], v242 offset:0\n"
    "v_mfma_f32_16x16x32_bf16 v[192:195], a[228:231], a[184:187], v[192:195]\n"
    "ds_read_b128 a[128:131], v241 offset:0\n"
    "v_mfma_f32_16x16x32_bf16 v[192:195], a[224:227], a[152:155], v[192:195]\n"
    "s_waitcnt lgkmcnt(10)\n"
    "s_addk_i32 m0, 1024\n"
    "s_addk_i32 s54, 16384\n"
    "v_mfma_f32_16x16x32_bf16 v[160:163], a[224:227], a[148:151], v[160:163]\n"
    "buffer_load_dwordx4 v254, s[72:75], s54 offen  lds\n"
    "v_mfma_f32_16x16x32_bf16 v[160:163], a[228:231], a[180:183], v[160:163]\n"
    "s_waitcnt lgkmcnt(8)\n"
    "v_mfma_f32_16x16x32_bf16 v[128:131], a[228:231], a[176:179], v[128:131]\n"
    "v_mfma_f32_16x16x32_bf16 v[128:131], a[224:227], a[144:147], v[128:131]\n"
    "s_waitcnt lgkmcnt(6)\n"
    "s_addk_i32 m0, 1024\n"
    "s_addk_i32 s54, 16384\n"
    "v_mfma_f32_16x16x32_bf16 v[96:99], a[224:227], a[140:143], v[96:99]\n"
    "buffer_load_dwordx4 v254, s[72:75], s54 offen  lds\n"
    "v_mfma_f32_16x16x32_bf16 v[96:99], a[228:231], a[172:175], v[96:99]\n"
    "s_waitcnt lgkmcnt(4)\n"
    "v_mfma_f32_16x16x32_bf16 v[64:67], a[228:231], a[168:171], v[64:67]\n"
    "v_mfma_f32_16x16x32_bf16 v[64:67], a[224:227], a[136:139], v[64:67]\n"
    "s_waitcnt lgkmcnt(2)\n"
    "s_addk_i32 m0, 1024\n"
    "s_addk_i32 s54, 16384\n"
    "v_mfma_f32_16x16x32_bf16 v[32:35], a[224:227], a[132:135], v[32:35]\n"
    "buffer_load_dwordx4 v254, s[72:75], s54 offen  lds\n"
    "v_mfma_f32_16x16x32_bf16 v[32:35], a[228:231], a[164:167], v[32:35]\n"
    "s_waitcnt lgkmcnt(0)\n"
    "v_mfma_f32_16x16x32_bf16 v[0:3], a[228:231], a[160:163], v[0:3]\n"
    "v_mfma_f32_16x16x32_bf16 v[0:3], a[224:227], a[128:131], v[0:3]\n"
    "s_addk_i32 m0, 1024\n"
    "s_addk_i32 s54, 16384\n"
    "v_mfma_f32_16x16x32_bf16 v[4:7], a[232:235], a[128:131], v[4:7]\n"
    "buffer_load_dwordx4 v254, s[72:75], s54 offen  lds\n"
    "v_mfma_f32_16x16x32_bf16 v[4:7], a[236:239], a[160:163], v[4:7]\n"
    "v_mfma_f32_16x16x32_bf16 v[36:39], a[236:239], a[164:167], v[36:39]\n"
    "v_mfma_f32_16x16x32_bf16 v[36:39], a[232:235], a[132:135], v[36:39]\n"
    "s_addk_i32 m0, 1024\n"
    "s_addk_i32 s54, 16384\n"
    "v_mfma_f32_16x16x32_bf16 v[68:71], a[232:235], a[136:139], v[68:71]\n"
    "buffer_load_dwordx4 v254, s[72:75], s54 offen  lds\n"
    "v_mfma_f32_16x16x32_bf16 v[68:71], a[236:239], a[168:171], v[68:71]\n"
    "v_mfma_f32_16x16x32_bf16 v[100:103], a[236:239], a[172:175], v[100:103]\n"
    "v_mfma_f32_16x16x32_bf16 v[100:103], a[232:235], a[140:143], v[100:103]\n"
    "s_addk_i32 m0, 1024\n"
    "s_addk_i32 s54, 16384\n"
    "v_mfma_f32_16x16x32_bf16 v[132:135], a[232:235], a[144:147], v[132:135]\n"
    "buffer_load_dwordx4 v254, s[72:75], s54 offen  lds\n"
    "s_bitcmp1_b32 s85, 9\n"
    "s_cbranch_scc1 CP9_%=\n"
    "CR9_%=:\n"
    "v_mfma_f32_16x16x32_bf16 v[132:135], a[236:239], a[176:179], v[132:135]\n"
    "v_mfma_f32_16x16x32_bf16 v[164:167], a[236:239], a[180:183], v[164:167]\n"
    "v_mfma_f32_16x16x32_bf16 v[164:167], a[232:235], a[148:151], v[164:167]\n"
    "s_xor_b32 s59, s66, s67\n"
    "s_addk_i32 s67, 128\n"
    "s_cmpk_eq_u32 s67, 1536\n"
    "v_mfma_f32_16x16x32_bf16 v[196:199], a[232:235], a[152:155], v[196:199]\n"
    "s_cselect_b32 s67, 0, s67\n"
    "s_add_u32 m0, s68, 98304\n"
    "s_nop 0\n"
    "v_mfma_f32_16x16x32_bf16 v[196:199], a[236:239], a[184:187], v[196:199]\n"
    "buffer_load_dwordx4 v255, s[24:27], s59 offen  lds\n"
    "v_mfma_f32_16x16x32_bf16 v[228:231], a[236:239], a[188:191], v[228:231]\n"
    "v_mfma_f32_16x16x32_bf16 v[228:231], a[232:235], a[156:159], v[228:231]\n"
    "s_waitcnt vmcnt(17)\n"
    "ds_read_b64_tr_b16 a[208:209], v252 offset:32768\n"
    "ds_read_b64_tr_b16 a[210:211], v252 offset:33280\n"
    "v_mfma_f32_16x16x32_bf16 v[232:235], a[192:195], a[156:159], v[232:235]\n"
    "s_add_u32 m0, s68, 99328\n"
    "s_addk_i32 s59, 16384\n"
    "v_mfma_f32_16x16x32_bf16 v[232:235], a[196:199], a[188:191], v[232:235]\n"
    "buffer_load_dwordx4 v243, s[24:27], s59 offen  lds\n"
    "v_mfma_f32_16x16x32_bf16 v[200:203], a[196:199], a[184:187], v[200:203]\n"
    "ds_read_b64_tr_b16 a[212:213], v252 offset:36864\n"
    "ds_read_b64_tr_b16 a[214:215], v252 offset:37376\n"
    "v_mfma_f32_16x16x32_bf16 v[200:203], a[192:195], a[152:155], v[200:203]\n"
    "ds_read_b64_tr_b16 a[216:217], v252 offset:32776\n"
    "ds_read_b64_tr_b16 a[218:219], v252 offset:33288\n"
    "v_mfma_f32_16x16x32_bf16 v[168:171], a[192:195], a[148:151], v[168:171]\n"
    "s_add_u32 m0, s68, 100352\n"
    "s_addk_i32 s59, 16384\n"
    "v_mfma_f32_16x16x32_bf16 v[168:171], a[196:199], a[180:183], v[168:171]\n"
    "buffer_load_dwordx4 v255, s[24:27], s59 offen  lds\n"
    "v_mfma_f32_16x16x32_bf16 v[136:139], a[196:199], a[176:179], v[136:139]\n"
    "ds_read_b64_tr_b16 a[220:221], v252 offset:36872\n"
    "ds_read_b64_tr_b16 a[222:223], v252 offset:37384\n"
    "v_mfma_f32_16x16x32_bf16 v[136:139], a[192:195], a[144:147], v[136:139]\n"
    "ds_read_b64_tr_b16 a[224:225], v253 offset:32768\n"
    "ds_read_b64_tr_b16 a[226:227], v253 offset:33280\n"
    "v_mfma_f32_16x16x32_bf16 v[104:107], a[192:195], a[140:143], v[104:107]\n"
    "s_add_u32 m0, s68, 101376\n"
    "s_addk_i32 s59, 16384\n"
    "v_mfma_f32_16x16x32_bf16 v[104:107], a[196:199], a[172:175], v[104:107]\n"
    "buffer_load_dwordx4 v243, s[24:27], s59 offen  lds\n"
    "v_mfma_f32_16x16x32_bf16 v[72:75], a[196:199], a[168:171], v[72:75]\n"
    "ds_read_b64_tr_b16 a[228:229], v253 offset:36864\n"
    "ds_read_b64_tr_b16 a[230:231], v253 offset:37376\n"
    "v_mfma_f32_16x16x32_bf16 v[72:75], a[192:195], a[136:139], v[72:75]\n"
    "ds_read_b64_tr_b16 a[232:233], v253 offset:32776\n"
    "ds_read_b64_tr_b16 a[234:235], v253 offset:33288\n"
    "v_mfma_f32_16x16x32_bf16 v[40:43], a[192:195], a[132:135], v[40:43]\n"
    "s_add_u32 m0, s79, 0\n"
    "s_lshl_b32 s54, s64, 17\n"
    "s_xor_b32 s54, s54, s57\n"
    "v_mfma_f32_16x16x32_bf16 v[40:43], a[196:199], a[164:167], v[40:43]\n"
    "s_addk_i32 s54, 512\n"
    "v_mfma_f32_16x16x32_bf16 v[8:11], a[196:199], a[160:163], v[8:11]\n"
    "buffer_load_dwordx4 v254, s[72:75], s54 offen  lds\n"
    "v_mfma_f32_16x16x32_bf16 v[8:11], a[192:195], a[128:131], v[8:11]\n"
    "ds_read_b64_tr_b16 a[236:237], v253 offset:36872\n"
    "ds_read_b64_tr_b16 a[238:239], v253 offset:37384\n"
    "v_mfma_f32_16x16x32_bf16 v[12:15], a[200:203], a[128:131], v[12:15]\n"
    "s_addk_i32 m0, 1024\n"
    "s_addk_i32 s54, 16384\n"
    "v_mfma_f32_16x16x32_bf16 v[12:15], a[204:207], a[160:163], v[12:15]\n"
    "buffer_load_dwordx4 v254, s[72:75], s54 offen  lds\n"
    "v_mfma_f32_16x16x32_bf16 v[44:47], a[204:207], a[164:167], v[44:47]\n"
    "v_mfma_f32_16x16x32_bf16 v[44:47], a[200:203], a[132:135], v[44:47]\n"
    "s_addk_i32 m0, 1024\n"
    "s_addk_i32 s54, 16384\n"
    "v_mfma_f32_16x16x32_bf16 v[76:79], a[200:203], a[136:139], v[76:79]\n"
    "buffer_load_dwordx4 v254, s[72:75], s54 offen  lds\n"
    "v_mfma_f32_16x16x32_bf16 v[76:79], a[204:207], a[168:171], v[76:79]\n"
    "v_mfma_f32_16x16x32_bf16 v[108:111], a[204:207], a[172:175], v[108:111]\n"
    "v_mfma_f32_16x16x32_bf16 v[108:111], a[200:203], a[140:143], v[108:111]\n"
    "s_addk_i32 m0, 1024\n"
    "s_addk_i32 s54, 16384\n"
    "v_mfma_f32_16x16x32_bf16 v[140:143], a[200:203], a[144:147], v[140:143]\n"
    "buffer_load_dwordx4 v254, s[72:75], s54 offen  lds\n"
    "v_mfma_f32_16x16x32_bf16 v[140:143], a[204:207], a[176:179], v[140:143]\n"
    "v_mfma_f32_16x16x32_bf16 v[172:175], a[204:207], a[180:183], v[172:175]\n"
    "v_mfma_f32_16x16x32_bf16 v[172:175], a[200:203], a[148:151], v[172:175]\n"
    "s_addk_i32 m0, 1024\n"
    "s_addk_i32 s54, 16384\n"
    "v_mfma_f32_16x16x32_bf16 v[204:207], a[200:203], a[152:155], v[204:207]\n"
    "buffer_load_dwordx4 v254, s[72:75], s54 offen  lds\n"
    "v_mfma_f32_16x16x32_bf16 v[204:207], a[204:207], a[184:187], v[204:207]\n"
    "v_mfma_f32_16x16x32_bf16 v[236:239], a[204:207], a[188:191], v[236:239]\n"
    "v_mfma_f32_16x16x32_bf16 v[236:239], a[200:203], a[156:159], v[236:239]\n"
    "s_waitcnt lgkmcnt(12)\n"
    "s_addk_i32 m0, 1024\n"
    "s_addk_i32 s54, 16384\n"
    "v_mfma_f32_16x16x32_bf16 a[240:243], a[208:211], a[156:159], a[240:243]\n"
    "buffer_load_dwordx4 v254, s[72:75], s54 offen  lds\n"
    "v_mfma_f32_16x16x32_bf16 a[240:243], a[212:215], a[188:191], a[240:243]\n"
    "v_mfma_f32_16x16x32_bf16 v[208:211], a[212:215], a[184:187], v[208:211]\n"
    "v_add_u32 v246, s90, v252\n"
    "s_waitcnt vmcnt(18)\n"
    "v_mfma_f32_16x16x32_bf16 v[208:211], a[208:211], a[152:155], v[208:211]\n"
    "ds_read_b64_tr_b16 v[248:249], v246 offset:0\n"
    "ds_read_b64_tr_b16 v[250:251], v246 offset:512\n"
    "v_mfma_f32_16x16x32_bf16 v[176:179], a[208:211], a[148:151], v[176:179]\n"
    "s_addk_i32 m0, 1024\n"
    "s_addk_i32 s54, 16384\n"
    "v_mfma_f32_16x16x32_bf16 v[176:179], a[212:215], a[180:183], v[176:179]\n"
    "buffer_load_dwordx4 v254, s[72:75], s54 offen  lds\n"
    "v_mfma_f32_16x16x32_bf16 v[144:147], a[212:215], a[176:179], v[144:147]\n"
    "v_mfma_f32_16x16x32_bf16 v[144:147], a[208:211], a[144:147], v[144:147]\n"
    "s_addk_i32 m0, 1024\n"
    "s_addk_i32 s54, 16384\n"
    "v_mfma_f32_16x16x32_bf16 v[112:115], a[208:211], a[140:143], v[112:115]\n"
    "buffer_load_dwordx4 v254, s[72:75], s54 offen  lds\n"
    "v_mfma_f32_16x16x32_bf16 v[112:115], a[212:215], a[172:175], v[112:115]\n"
    "s_add_u32 s57, s57, 131072\n"
    "s_cmp_ge_u32 s57, 1572864\n"
    "s_cselect_b32 s57, s70, s57\n"
    "v_mfma_f32_16x16x32_bf16 v[80:83], a[212:215], a[168:171], v[80:83]\n"
    "v_mfma_f32_16x16x32_bf16 v[80:83], a[208:211], a[136:139], v[80:83]\n"
    "s_waitcnt lgkmcnt(2)\n"
    "s_add_u32 m0, s79, 32768\n"
    "s_lshl_b32 s54, s64, 17\n"
    "v_mfma_f32_16x16x32_bf16 v[48:51], a[208:211], a[132:135], v[48:51]\n"
    "s_xor_b32 s54, s54, s57\n"
    "v_mfma_f32_16x16x32_bf16 v[48:51], a[212:215], a[164:167], v[48:51]\n"
    "buffer_load_dwordx4 v254, s[72:75], s54 offen  lds\n"
    "v_mfma_f32_16x16x32_bf16 v[16:19], a[212:215], a[160:163], v[16:19]\n"
    "s_add_u32 s62, s91, 1048576\n"
    "s_waitcnt lgkmcnt(0)\n"
    "v_mfma_f32_16x16x32_bf16 v[16:19], a[208:211], a[128:131], v[16:19]\n"
    "buffer_store_dwordx4 v[248:251], v240, s[28:31], s62 offen offset:0 sc1\n"
    "s_nop 0\n"
    "s_addk_i32 m0, 1024\n"
    "s_addk_i32 s54, 16384\n"
    "v_mfma_f32_16x16x32_bf16 v[20:23], a[216:219], a[128:131], v[20:23]\n"
    "buffer_load_dwordx4 v254, s[72:75], s54 offen  lds\n"
    "v_mfma_f32_16x16x32_bf16 v[20:23], a[220:223], a[160:163], v[20:23]\n"
    "v_mfma_f32_16x16x32_bf16 v[52:55], a[220:223], a[164:167], v[52:55]\n"
    "v_add_u32 v246, s90, v252\n"
    "ds_read_b64_tr_b16 v[248:249], v246 offset:8\n"
    "v_mfma_f32_16x16x32_bf16 v[52:55], a[216:219], a[132:135], v[52:55]\n"
    "ds_read_b64_tr_b16 v[250:251], v246 offset:520\n"
    "s_addk_i32 m0, 1024\n"
    "v_mfma_f32_16x16x32_bf16 v[84:87], a[216:219], a[136:139], v[84:87]\n"
    "s_addk_i32 s54, 16384\n"
    "v_mfma_f32_16x16x32_bf16 v[84:87], a[220:223], a[168:171], v[84:87]\n"
    "buffer_load_dwordx4 v254, s[72:75], s54 offen  lds\n"
    "v_mfma_f32_16x16x32_bf16 v[116:119], a[220:223], a[172:175], v[116:119]\n"
    "v_mfma_f32_16x16x32_bf16 v[116:119], a[216:219], a[140:143], v[116:119]\n"
    "s_addk_i32 m0, 1024\n"
    "s_addk_i32 s54, 16384\n"
    "v_mfma_f32_16x16x32_bf16 v[148:151], a[216:219], a[144:147], v[148:151]\n"
    "buffer_load_dwordx4 v254, s[72:75], s54 offen  lds\n"
    "v_mfma_f32_16x16x32_bf16 v[148:151], a[220:223], a[176:179], v[148:151]\n"
    "v_mfma_f32_16x16x32_bf16 v[180:183], a[220:223], a[180:183], v[180:183]\n"
    "v_mfma_f32_16x16x32_bf16 v[180:183], a[216:219], a[148:151], v[180:183]\n"
    "s_addk_i32 m0, 1024\n"
    "s_addk_i32 s54, 16384\n"
    "v_mfma_f32_16x16x32_bf16 v[212:215], a[216:219], a[152:155], v[212:215]\n"
    "buffer_load_dwordx4 v254, s[72:75], s54 offen  lds\n"
    "v_mfma_f32_16x16x32_bf16 v[212:215], a[220:223], a[184:187], v[212:215]\n"
    "s_add_u32 s62, s91, 1048576\n"
    "s_waitcnt lgkmcnt(0)\n"
    "v_mfma_f32_16x16x32_bf16 a[244:247], a[220:223], a[188:191], a[244:247]\n"
    "buffer_store_dwordx4 v[248:251], v240, s[28:31], s62 offen offset:2048 sc1\n"
    "s_nop 0\n"
    "v_mfma_f32_16x16x32_bf16 a[244:247], a[216:219], a[156:159], a[244:247]\n"
    "v_add_u32 v252, 0x20000, v252\n"
    "v_add_u32 v253, 0x20000, v253\n"
    "v_mfma_f32_16x16x32_bf16 a[248:251], a[224:227], a[156:159], a[248:251]\n"
    "s_waitcnt vmcnt(19)\n"
    "ds_read_b64_tr_b16 a[192:193], v252 offset:0\n"
    "ds_read_b64_tr_b16 a[194:195], v252 offset:512\n"
    "v_mfma_f32_16x16x32_bf16 a[248:251], a[228:231], a[188:191], a[248:251]\n"
    "s_addk_i32 m0, 1024\n"
    "s_addk_i32 s54, 16384\n"
    "v_mfma_f32_16x16x32_bf16 v[216:219], a[228:231], a[184:187], v[216:219]\n"
    "buffer_load_dwordx4 v254, s[72:75], s54 offen  lds\n"
    "v_mfma_f32_16x16x32_bf16 v[216:219], a[224:227], a[152:155], v[216:219]\n"
    "ds_read_b64_tr_b16 a[196:197], v252 offset:4096\n"
    "ds_read_b64_tr_b16 a[198:199], v252 offset:4608\n"
    "v_mfma_f32_16x16x32_bf16 v[184:187], a[224:227], a[148:151], v[184:187]\n"
    "v_add_u32 v246, s90, v253\n"
    "v_add_u32 v246, 0xfffe0000, v246\n"
    "v_mfma_f32_16x16x32_bf16 v[184:187], a[228:231], a[180:183], v[184:187]\n"
    "ds_read_b64_tr_b16 v[248:249], v246 offset:0\n"
    "ds_read_b64_tr_b16 v[250:251], v246 offset:512\n"
    "v_mfma_f32_16x16x32_bf16 v[152:155], a[228:231], a[176:179], v[152:155]\n"
    "ds_read_b64_tr_b16 a[200:201], v252 offset:8\n"
    "ds_read_b64_tr_b16 a[202:203], v252 offset:520\n"
    "v_mfma_f32_16x16x32_bf16 v[152:155], a[224:227], a[144:147], v[152:155]\n"
    "s_addk_i32 m0, 1024\n"
    "s_addk_i32 s54, 16384\n"
    "v_mfma_f32_16x16x32_bf16 v[120:123], a[224:227], a[140:143], v[120:123]\n"
    "buffer_load_dwordx4 v254, s[72:75], s54 offen  lds\n"
    "ds_read_b64_tr_b16 a[204:205], v252 offset:4104\n"
    "ds_read_b64_tr_b16 a[206:207], v252 offset:4616\n"
    "v_mfma_f32_16x16x32_bf16 v[120:123], a[228:231], a[172:175], v[120:123]\n"
    "ds_read_b64_tr_b16 a[208:209], v253 offset:0\n"
    "ds_read_b64_tr_b16 a[210:211], v253 offset:512\n"
    "v_mfma_f32_16x16x32_bf16 v[88:91], a[228:231], a[168:171], v[88:91]\n"
    "s_addk_i32 m0, 1024\n"
    "s_addk_i32 s54, 16384\n"
    "v_mfma_f32_16x16x32_bf16 v[88:91], a[224:227], a[136:139], v[88:91]\n"
    "buffer_load_dwordx4 v254, s[72:75], s54 offen  lds\n"
    "v_mfma_f32_16x16x32_bf16 v[56:59], a[224:227], a[132:135], v[56:59]\n"
    "ds_read_b64_tr_b16 a[212:213], v253 offset:4096\n"
    "ds_read_b64_tr_b16 a[214:215], v253 offset:4608\n"
    "v_mfma_f32_16x16x32_bf16 v[56:59], a[228:231], a[164:167], v[56:59]\n"
    "ds_read_b64_tr_b16 a[216:217], v253 offset:8\n"
    "ds_read_b64_tr_b16 a[218:219], v253 offset:520\n"
    "s_add_u32 s62, s91, 1052672\n"
    "s_waitcnt lgkmcnt(10)\n"
    "buffer_store_dwordx4 v[248:251], v240, s[28:31], s62 offen offset:0 sc1\n"
    "s_nop 0\n"
    "s_barrier\n"
    "ds_read_b64_tr_b16 a[220:221], v253 offset:4104\n"
    "v_mfma_f32_16x16x32_bf16 v[24:27], a[228:231], a[160:163], v[24:27]\n"
    "ds_read_b64_tr_b16 a[222:223], v253 offset:4616\n"
    "v_mfma_f32_16x16x32_bf16 v[24:27], a[224:227], a[128:131], v[24:27]\n"
    "s_add_u32 s82, s77, 3076\n"
    "s_and_b32 s82, s82, s86\n"
    "v_mfma_f32_16x16x32_bf16 v[28:31], a[232:235], a[128:131], v[28:31]\n"
    "buffer_load_dword v244, v240, s[80:83], s77 offen offset:3072 sc1\n"
    "v_mfma_f32_16x16x32_bf16 v[28:31], a[236:239], a[160:163], v[28:31]\n"
    "v_mfma_f32_16x16x32_bf16 v[60:63], a[236:239], a[164:167], v[60:63]\n"
    "v_add_u32 v246, s90, v253\n"
    "v_add_u32 v246, 0xfffe0000, v246\n"
    "v_mfma_f32_16x16x32_bf16 v[60:63], a[232:235], a[132:135], v[60:63]\n"
    "ds_read_b64_tr_b16 v[248:249], v246 offset:8\n"
    "ds_read_b64_tr_b16 v[250:251], v246 offset:520\n"
    "v_mfma_f32_16x16x32_bf16 v[92:95], a[232:235], a[136:139], v[92:95]\n"
    "ds_read_b128 a[128:131], v241 offset:16384\n"
    "s_waitcnt lgkmcnt(3)\n"
    "v_mfma_f32_16x16x32_bf16 v[92:95], a[236:239], a[168:171], v[92:95]\n"
    "s_add_u32 m0, s79, 131072\n"
    "s_lshl_b32 s54, s64, 17\n"
    "s_xor_b32 s54, s54, s57\n"
    "v_mfma_f32_16x16x32_bf16 v[124:127], a[236:239], a[172:175], v[124:127]\n"
    "s_addk_i32 s54, 512\n"
    "v_mfma_f32_16x16x32_bf16 v[124:127], a[232:235], a[140:143], v[124:127]\n"
    "buffer_load_dwordx4 v254, s[72:75], s54 offen  lds\n"
    "ds_read_b128 a[160:163], v242 offset:16384\n"
    "ds_read_b128 a[164:167], v242 offset:18432\n"
    "v_mfma_f32_16x16x32_bf16 v[156:159], a[232:235], a[144:147], v[156:159]\n"
    "ds_read_b128 a[132:135], v241 offset:18432\n"
    "ds_read_b128 a[136:139], v241 offset:20480\n"
    "v_mfma_f32_16x16x32_bf16 v[156:159], a[236:239], a[176:179], v[156:159]\n"
    "s_addk_i32 m0, 1024\n"
    "s_addk_i32 s54, 16384\n"
    "v_mfma_f32_16x16x32_bf16 v[188:191], a[236:239], a[180:183], v[188:191]\n"
    "buffer_load_dwordx4 v254, s[72:75], s54 offen  lds\n"
    "ds_read_b128 a[168:171], v242 offset:20480\n"
    "v_mfma_f32_16x16x32_bf16 v[188:191], a[232:235], a[148:151], v[188:191]\n"
    "ds_read_b128 a[172:175], v242 offset:22528\n"
    "ds_read_b128 a[140:143], v241 offset:22528\n"
    "v_mfma_f32_16x16x32_bf16 v[220:223], a[232:235], a[152:155], v[220:223]\n"
    "ds_read_b128 a[144:147], v241 offset:24576\n"
    "s_addk_i32 m0, 1024\n"
    "v_mfma_f32_16x16x32_bf16 v[220:223], a[236:239], a[184:187], v[220:223]\n"
    "s_addk_i32 s54, 16384\n"
    "v_mfma_f32_16x16x32_bf16 a[252:255], a[236:239], a[188:191], a[252:255]\n"
    "buffer_load_dwordx4 v254, s[72:75], s54 offen  lds\n"
    "ds_read_b128 a[176:179], v242 offset:24576\n"
    "v_mfma_f32_16x16x32_bf16 a[252:255], a[232:235], a[156:159], a[252:255]\n"
    "s_waitcnt lgkmcnt(8)\n"
    "s_add_u32 s62, s91, 1052672\n"
    "s_waitcnt lgkmcnt(10)\n"
    "v_mfma_f32_16x16x32_bf16 v[0:3], a[192:195], a[128:131], v[0:3]\n"
    "buffer_store_dwordx4 v[248:251], v240, s[28:31], s62 offen offset:2048 sc1\n"
    "s_nop 0\n"
    "ds_read_b128 a[180:183], v242 offset:26624\n"
    "ds_read_b128 a[148:151], v241 offset:26624\n"
    "v_mfma_f32_16x16x32_bf16 v[0:3], a[196:199], a[160:163], v[0:3]\n"
    "s_waitcnt lgkmcnt(8)\n"
    "ds_read_b128 a[152:155], v241 offset:28672\n"
    "s_addk_i32 m0, 1024\n"
    "v_mfma_f32_16x16x32_bf16 v[32:35], a[196:199], a[164:167], v[32:35]\n"
    "s_addk_i32 s54, 16384\n"
    "v_mfma_f32_16x16x32_bf16 v[32:35], a[192:195], a[132:135], v[32:35]\n"
    "s_waitcnt lgkmcnt(7)\n"
    "v_mfma_f32_16x16x32_bf16 v[64:67], a[192:195], a[136:139], v[64:67]\n"
    "buffer_load_dwordx4 v254, s[72:75], s54 offen  lds\n"
    "ds_read_b128 a[184:187], v242 offset:28672\n"
    "ds_read_b128 a[188:191], v242 offset:30720\n"
    "ds_read_b128 a[156:159], v241 offset:30720\n"
    "v_mfma_f32_16x16x32_bf16 v[64:67], a[196:199], a[168:171], v[64:67]\n"
    "s_waitcnt lgkmcnt(8)\n"
    "s_addk_i32 m0, 1024\n"
    "s_addk_i32 s54, 16384\n"
    "v_mfma_f32_16x16x32_bf16 v[96:99], a[196:199], a[172:175], v[96:99]\n"
    "buffer_load_dwordx4 v254, s[72:75], s54 offen  lds\n"
    "v_mfma_f32_16x16x32_bf16 v[96:99], a[192:195], a[140:143], v[96:99]\n"
    "s_waitcnt lgkmcnt(6)\n"
    "s_addk_i32 m0, 1024\n"
    "s_addk_i32 s54, 16384\n"
    "v_mfma_f32_16x16x32_bf16 v[128:131], a[192:195], a[144:147], v[128:131]\n"
    "buffer_load_dwordx4 v254, s[72:75], s54 offen  lds\n"
    "v_mfma_f32_16x16x32_bf16 v[128:131], a[196:199], a[176:179], v[128:131]\n"
    "s_waitcnt lgkmcnt(4)\n"
    "v_mfma_f32_16x16x32_bf16 v[160:163], a[196:199], a[180:183], v[160:163]\n"
    "v_mfma_f32_16x16x32_bf16 v[160:163], a[192:195], a[148:151], v[160:163]\n"
    "s_waitcnt lgkmcnt(2)\n"
    "s_addk_i32 m0, 1024\n"
    "s_addk_i32 s54, 16384\n"
    "v_mfma_f32_16x16x32_bf16 v[192:195], a[192:195], a[152:155], v[192:195]\n"
    "buffer_load_dwordx4 v254, s[72:75], s54 offen  lds\n"
    "v_mfma_f32_16x16x32_bf16 v[192:195], a[196:199], a[184:187], v[192:195]\n"
    "s_waitcnt lgkmcnt(0)\n"
    "v_mfma_f32_16x16x32_bf16 v[224:227], a[196:199], a[188:191], v[224:227]\n"
    "v_mfma_f32_16x16x32_bf16 v[224:227], a[192:195], a[156:159], v[224:227]\n"
    "s_addk_i32 m0, 1024\n"
    "s_addk_i32 s54, 16384\n"
    "v_mfma_f32_16x16x32_bf16 v[228:231], a[200:203], a[156:159], v[228:231]\n"
    "buffer_load_dwordx4 v254, s[72:75], s54 offen  lds\n"
    "v_mfma_f32_16x16x32_bf16 v[228:231], a[204:207], a[188:191], v[228:231]\n"
    "s_add_u32 s57, s57, 131072\n"
    "s_cmp_ge_u32 s57, 1572864\n"
    "s_cselect_b32 s57, s70, s57\n"
    "v_mfma_f32_16x16x32_bf16 v[196:199], a[204:207], a[184:187], v[196:199]\n"
    "v_mfma_f32_16x16x32_bf16 v[196:199], a[200:203], a[152:155], v[196:199]\n"
    "v_mfma_f32_16x16x32_bf16 v[164:167], a[200:203], a[148:151], v[164:167]\n"
    "v_mfma_f32_16x16x32_bf16 v[164:167], a[204:207], a[180:183], v[164:167]\n"
    "v_mfma_f32_16x16x32_bf16 v[132:135], a[204:207], a[176:179], v[132:135]\n"
    "s_xor_b32 s59, s66, 1536\n"
    "s_add_u32 m0, s68, 65536\n"
    "s_nop 0\n"
    "v_mfma_f32_16x16x32_bf16 v[132:135], a[200:203], a[144:147], v[132:135]\n"
    "buffer_load_dwordx4 v255, s[24:27], s59 offen  lds\n"
    "v_mfma_f32_16x16x32_bf16 v[100:103], a[200:203], a[140:143], v[100:103]\n"
    "s_bitcmp1_b32 s85, 10\n"
    "s_cbranch_scc1 CP10_%=\n"
    "CR10_%=:\n"
    "v_mfma_f32_16x16x32_bf16 v[100:103], a[204:207], a[172:175], v[100:103]\n"
    "s_add_u32 m0, s68, 66560\n"
    "s_addk_i32 s59, 16384\n"
    "v_mfma_f32_16x16x32_bf16 v[68:71], a[204:207], a[168:171], v[68:71]\n"
    "buffer_load_dwordx4 v243, s[24:27], s59 offen  lds\n"
    "v_mfma_f32_16x16x32_bf16 v[68:71], a[200:203], a[136:139], v[68:71]\n"
    "v_mfma_f32_16x16x32_bf16 v[36:39], a[200:203], a[132:135], v[36:39]\n"
    "s_add_u32 m0, s68, 67584\n"
    "s_addk_i32 s59, 16384\n"
    "v_mfma_f32_16x16x32_bf16 v[36:39], a[204:207], a[164:167], v[36:39]\n"
    "buffer_load_dwordx4 v255, s[24:27], s59 offen  lds\n"
    "v_mfma_f32_16x16x32_bf16 v[4:7], a[204:207], a[160:163], v[4:7]\n"
    "v_mfma_f32_16x16x32_bf16 v[4:7], a[200:203], a[128:131], v[4:7]\n"
    "v_add_u32 v252, 0xfffe0000, v252\n"
    "v_add_u32 v253, 0xfffe0000, v253\n"
    "v_mfma_f32_16x16x32_bf16 v[8:11], a[208:211], a[128:131], v[8:11]\n"
    "s_waitcnt vmcnt(24)\n"
    "ds_read_b64_tr_b16 a[224:225], v252 offset:0\n"
    "ds_read_b64_tr_b16 a[226:227], v252 offset:512\n"
    "v_mfma_f32_16x16x32_bf16 v[8:11], a[212:215], a[160:163], v[8:11]\n"
    "s_add_u32 m0, s68, 68608\n"
    "s_addk_i32 s59, 16384\n"
    "v_mfma_f32_16x16x32_bf16 v[40:43], a[212:215], a[164:167], v[40:43]\n"
    "buffer_load_dwordx4 v243, s[24:27], s59 offen  lds\n"
    "v_mfma_f32_16x16x32_bf16 v[40:43], a[208:211], a[132:135], v[40:43]\n"
    "ds_read_b64_tr_b16 a[228:229], v252 offset:4096\n"
    "ds_read_b64_tr_b16 a[230:231], v252 offset:4608\n"
    "v_mfma_f32_16x16x32_bf16 v[72:75], a[208:211], a[136:139], v[72:75]\n"
    "ds_read_b64_tr_b16 a[232:233], v252 offset:8\n"
    "ds_read_b64_tr_b16 a[234:235], v252 offset:520\n"
    "v_mfma_f32_16x16x32_bf16 v[72:75], a[212:215], a[168:171], v[72:75]\n"
    "ds_read_b64_tr_b16 a[236:237], v252 offset:4104\n"
    "ds_read_b64_tr_b16 a[238:239], v252 offset:4616\n"
    "v_mfma_f32_16x16x32_bf16 v[104:107], a[212:215], a[172:175], v[104:107]\n"
    "v_mfma_f32_16x16x32_bf16 v[104:107], a[208:211], a[140:143], v[104:107]\n"
    "ds_read_b64_tr_b16 a[192:193], v253 offset:0\n"
    "ds_read_b64_tr_b16 a[194:195], v253 offset:512\n"
    "v_mfma_f32_16x16x32_bf16 v[136:139], a[208:211], a[144:147], v[136:139]\n"
    "v_mfma_f32_16x16x32_bf16 v[136:139], a[212:215], a[176:179], v[136:139]\n"
    "ds_read_b64_tr_b16 a[196:197], v253 offset:4096\n"
    "ds_read_b64_tr_b16 a[198:199], v253 offset:4608\n"
    "v_mfma_f32_16x16x32_bf16 v[168:171], a[212:215], a[180:183], v[168:171]\n"
    "v_mfma_f32_16x16x32_bf16 v[168:171], a[208:211], a[148:151], v[168:171]\n"
    "ds_read_b64_tr_b16 a[200:201], v253 offset:8\n"
    "ds_read_b64_tr_b16 a[202:203], v253 offset:520\n"
    "v_mfma_f32_16x16x32_bf16 v[200:203], a[208:211], a[152:155], v[200:203]\n"
    "v_mfma_f32_16x16x32_bf16 v[200:203], a[212:215], a[184:187], v[200:203]\n"
    "ds_read_b64_tr_b16 a[204:205], v253 offset:4104\n"
    "ds_read_b64_tr_b16 a[206:207], v253 offset:4616\n"
    "v_mfma_f32_16x16x32_bf16 v[232:235], a[212:215], a[188:191], v[232:235]\n"
    "v_mfma_f32_16x16x32_bf16 v[232:235], a[208:211], a[156:159], v[232:235]\n"
    "v_mfma_f32_16x16x32_bf16 v[236:239], a[216:219], a[156:159], v[236:239]\n"
    "v_mfma_f32_16x16x32_bf16 v[236:239], a[220:223], a[188:191], v[236:239]\n"
    "v_mfma_f32_16x16x32_bf16 v[204:207], a[220:223], a[184:187], v[204:207]\n"
    "v_mfma_f32_16x16x32_bf16 v[204:207], a[216:219], a[152:155], v[204:207]\n"
    "v_mfma_f32_16x16x32_bf16 v[172:175], a[216:219], a[148:151], v[172:175]\n"
    "v_mfma_f32_16x16x32_bf16 v[172:175], a[220:223], a[180:183], v[172:175]\n"
    "v_mfma_f32_16x16x32_bf16 v[140:143], a[220:223], a[176:179], v[140:143]\n"
    "v_mfma_f32_16x16x32_bf16 v[140:143], a[216:219], a[144:147], v[140:143]\n"
    "v_mfma_f32_16x16x32_bf16 v[108:111], a[216:219], a[140:143], v[108:111]\n"
    "v_mfma_f32_16x16x32_bf16 v[108:111], a[220:223], a[172:175], v[108:111]\n"
    "v_mfma_f32_16x16x32_bf16 v[76:79], a[220:223], a[168:171], v[76:79]\n"
    "v_mfma_f32_16x16x32_bf16 v[76:79], a[216:219], a[136:139], v[76:79]\n"
    "v_mfma_f32_16x16x32_bf16 v[44:47], a[216:219], a[132:135], v[44:47]\n"
    "v_mfma_f32_16x16x32_bf16 v[44:47], a[220:223], a[164:167], v[44:47]\n"
    "v_mfma_f32_16x16x32_bf16 v[12:15], a[220:223], a[160:163], v[12:15]\n"
    "v_mfma_f32_16x16x32_bf16 v[12:15], a[216:219], a[128:131], v[12:15]\n"
    "s_waitcnt lgkmcnt(12)\n"
    "v_mfma_f32_16x16x32_bf16 v[16:19], a[224:227], a[128:131], v[16:19]\n"
    "v_mfma_f32_16x16x32_bf16 v[16:19], a[228:231], a[160:163], v[16:19]\n"
    "v_mfma_f32_16x16x32_bf16 v[48:51], a[228:231], a[164:167], v[48:51]\n"
    "v_mfma_f32_16x16x32_bf16 v[48:51], a[224:227], a[132:135], v[48:51]\n"
    "v_mfma_f32_16x16x32_bf16 v[80:83], a[224:227], a[136:139], v[80:83]\n"
    "v_mfma_f32_16x16x32_bf16 v[80:83], a[228:231], a[168:171], v[80:83]\n"
    "v_mfma_f32_16x16x32_bf16 v[112:115], a[228:231], a[172:175], v[112:115]\n"
    "v_mfma_f32_16x16x32_bf16 v[112:115], a[224:227], a[140:143], v[112:115]\n"
    "v_mfma_f32_16x16x32_bf16 v[144:147], a[224:227], a[144:147], v[144:147]\n"
    "v_mfma_f32_16x16x32_bf16 v[144:147], a[228:231], a[176:179], v[144:147]\n"
    "v_mfma_f32_16x16x32_bf16 v[176:179], a[228:231], a[180:183], v[176:179]\n"
    "v_mfma_f32_16x16x32_bf16 v[176:179], a[224:227], a[148:151], v[176:179]\n"
    "v_mfma_f32_16x16x32_bf16 v[208:211], a[224:227], a[152:155], v[208:211]\n"
    "v_mfma_f32_16x16x32_bf16 v[208:211], a[228:231], a[184:187], v[208:211]\n"
    "v_mfma_f32_16x16x32_bf16 a[240:243], a[228:231], a[188:191], a[240:243]\n"
    "v_mfma_f32_16x16x32_bf16 a[240:243], a[224:227], a[156:159], a[240:243]\n"
    "s_waitcnt lgkmcnt(8)\n"
    "v_mfma_f32_16x16x32_bf16 a[244:247], a[232:235], a[156:159], a[244:247]\n"
    "v_mfma_f32_16x16x32_bf16 a[244:247], a[236:239], a[188:191], a[244:247]\n"
    "v_mfma_f32_16x16x32_bf16 v[212:215], a[236:239], a[184:187], v[212:215]\n"
    "v_mfma_f32_16x16x32_bf16 v[212:215], a[232:235], a[152:155], v[212:215]\n"
    "v_mfma_f32_16x16x32_bf16 v[180:183], a[232:235], a[148:151], v[180:183]\n"
    "v_mfma_f32_16x16x32_bf16 v[180:183], a[236:239], a[180:183], v[180:183]\n"
    "v_mfma_f32_16x16x32_bf16 v[148:151], a[236:239], a[176:179], v[148:151]\n"
    "v_mfma_f32_16x16x32_bf16 v[148:151], a[232:235], a[144:147], v[148:151]\n"
    "v_mfma_f32_16x16x32_bf16 v[116:119], a[232:235], a[140:143], v[116:119]\n"
    "v_mfma_f32_16x16x32_bf16 v[116:119], a[236:239], a[172:175], v[116:119]\n"
    "v_mfma_f32_16x16x32_bf16 v[84:87], a[236:239], a[168:171], v[84:87]\n"
    "v_mfma_f32_16x16x32_bf16 v[84:87], a[232:235], a[136:139], v[84:87]\n"
    "v_mfma_f32_16x16x32_bf16 v[52:55], a[232:235], a[132:135], v[52:55]\n"
    "v_mfma_f32_16x16x32_bf16 v[52:55], a[236:239], a[164:167], v[52:55]\n"
    "v_mfma_f32_16x16x32_bf16 v[20:23], a[236:239], a[160:163], v[20:23]\n"
    "v_mfma_f32_16x16x32_bf16 v[20:23], a[232:235], a[128:131], v[20:23]\n"
    "s_waitcnt lgkmcnt(4)\n"
    "s_waitcnt vmcnt(15)\n"
    "ds_read_b64_tr_b16 a[208:209], v252 offset:32768\n"
    "v_mfma_f32_16x16x32_bf16 v[24:27], a[192:195], a[128:131], v[24:27]\n"
    "ds_read_b64_tr_b16 a[210:211], v252 offset:33280\n"
    "v_mfma_f32_16x16x32_bf16 v[24:27], a[196:199], a[160:163], v[24:27]\n"
    "ds_read_b64_tr_b16 a[212:213], v252 offset:36864\n"
    "ds_read_b64_tr_b16 a[214:215], v252 offset:37376\n"
    "v_mfma_f32_16x16x32_bf16 v[56:59], a[196:199], a[164:167], v[56:59]\n"
    "v_mfma_f32_16x16x32_bf16 v[56:59], a[192:195], a[132:135], v[56:59]\n"
    "ds_read_b64_tr_b16 a[216:217], v252 offset:32776\n"
    "ds_read_b64_tr_b16 a[218:219], v252 offset:33288\n"
    "v_mfma_f32_16x16x32_bf16 v[88:91], a[192:195], a[136:139], v[88:91]\n"
    "v_mfma_f32_16x16x32_bf16 v[88:91], a[196:199], a[168:171], v[88:91]\n"
    "ds_read_b64_tr_b16 a[220:221], v252 offset:36872\n"
    "ds_read_b64_tr_b16 a[222:223], v252 offset:37384\n"
    "v_mfma_f32_16x16x32_bf16 v[120:123], a[196:199], a[172:175], v[120:123]\n"
    "v_mfma_f32_16x16x32_bf16 v[120:123], a[192:195], a[140:143], v[120:123]\n"
    "ds_read_b64_tr_b16 a[224:225], v253 offset:32768\n"
    "ds_read_b64_tr_b16 a[226:227], v253 offset:33280\n"
    "v_mfma_f32_16x16x32_bf16 v[152:155], a[192:195], a[144:147], v[152:155]\n"
    "v_mfma_f32_16x16x32_bf16 v[152:155], a[196:199], a[176:179], v[152:155]\n"
    "ds_read_b64_tr_b16 a[228:229], v253 offset:36864\n"
    "ds_read_b64_tr_b16 a[230:231], v253 offset:37376\n"
    "v_mfma_f32_16x16x32_bf16 v[184:187], a[196:199], a[180:183], v[184:187]\n"
    "v_mfma_f32_16x16x32_bf16 v[184:187], a[192:195], a[148:151], v[184:187]\n"
    "ds_read_b64_tr_b16 a[232:233], v253 offset:32776\n"
    "ds_read_b64_tr_b16 a[234:235], v253 offset:33288\n"
    "v_mfma_f32_16x16x32_bf16 v[216:219], a[192:195], a[152:155], v[216:219]\n"
    "v_mfma_f32_16x16x32_bf16 v[216:219], a[196:199], a[184:187], v[216:219]\n"
    "s_waitcnt vmcnt(13)\n"
    "v_readfirstlane_b32 s62, v244\n"
    "s_cmp_eq_u32 s86, 0\n"
    "s_cbranch_scc1 LA3_%=\n"
    "s_cmp_eq_u32 %0, 0\n"
    "s_cbranch_scc1 LA3_%=\n"
    "s_cmp_eq_u32 s62, s88\n"
    "s_cbranch_scc1 LA3_%=\n"
    "s_mov_b32 s69, 0x400000\n"
    "LA4_%=:\n"
    "s_sleep 2\n"
    "s_add_u32 s82, s77, 2052\n"
    "s_and_b32 s82, s82, s86\n"
    "buffer_load_dword v244, v240, s[80:83], s77 offen offset:2048 sc1\n"
    "s_waitcnt vmcnt(0)\n"
    "s_add_u32 s82, s76, 8\n"
    "s_and_b32 s82, s82, s86\n"
    "buffer_store_dwordx2 v[244:245], v240, s[80:83], s76 offen sc1\n"
    "s_waitcnt vmcnt(0)\n"
    "s_add_u32 s82, s77, 3076\n"
    "s_and_b32 s82, s82, s86\n"
    "buffer_load_dword v244, v240, s[80:83], s77 offen offset:3072 sc1\n"
    "s_waitcnt vmcnt(0)\n"
    "v_readfirstlane_b32 s62, v244\n"
    "s_sub_u32 s69, s69, 1\n"
    "s_cbranch_scc1 LA3_%=\n"
    "s_cmp_eq_u32 s62, s88\n"
    "s_cbranch_scc0 LA4_%=\n"
    "LA3_%=:\n"
    "s_waitcnt vmcnt(9)\n"
    "s_barrier\n"
    "v_mfma_f32_16x16x32_bf16 a[248:251], a[196:199], a[188:191], a[248:251]\n"
    "ds_read_b64_tr_b16 a[236:237], v253 offset:36872\n"
    "ds_read_b64_tr_b16 a[238:239], v253 offset:37384\n"
    "v_mfma_f32_16x16x32_bf16 a[248:251], a[192:195], a[156:159], a[248:251]\n"
    "s_waitcnt lgkmcnt(15)\n"
    "v_mfma_f32_16x16x32_bf16 a[252:255], a[200:203], a[156:159], a[252:255]\n"
    "v_mfma_f32_16x16x32_bf16 a[252:255], a[204:207], a[188:191], a[252:255]\n"
    "v_mfma_f32_16x16x32_bf16 v[220:223], a[204:207], a[184:187], v[220:223]\n"
    "ds_read_b128 a[156:159], v241 offset:47104\n"
    "v_mfma_f32_16x16x32_bf16 v[220:223], a[200:203], a[152:155], v[220:223]\n"
    "s_xor_b32 s59, s66, 1664\n"
    "s_add_u32 m0, s68, 81920\n"
    "s_nop 0\n"
    "v_mfma_f32_16x16x32_bf16 v[188:191], a[200:203], a[148:151], v[188:191]\n"
    "buffer_load_dwordx4 v255, s[24:27], s59 offen  lds\n"
    "v_mfma_f32_16x16x32_bf16 v[188:191], a[204:207], a[180:183], v[188:191]\n"
    "ds_read_b128 a[188:191], v242 offset:47104\n"
    "ds_read_b128 a[184:187], v242 offset:45056\n"
    "v_mfma_f32_16x16x32_bf16 v[156:159], a[204:207], a[176:179], v[156:159]\n"
    "ds_read_b128 a[152:155], v241 offset:45056\n"
    "ds_read_b128 a[148:151], v241 offset:43008\n"
    "v_mfma_f32_16x16x32_bf16 v[156:159], a[200:203], a[144:147], v[156:159]\n"
    "s_add_u32 m0, s68, 82944\n"
    "s_addk_i32 s59, 16384\n"
    "v_mfma_f32_16x16x32_bf16 v[124:127], a[200:203], a[140:143], v[124:127]\n"
    "buffer_load_dwordx4 v243, s[24:27], s59 offen  lds\n"
    "v_mfma_f32_16x16x32_bf16 v[124:127], a[204:207], a[172:175], v[124:127]\n"
    "ds_read_b128 a[180:183], v242 offset:43008\n"
    "ds_read_b128 a[176:179], v242 offset:40960\n"
    "v_mfma_f32_16x16x32_bf16 v[92:95], a[204:207], a[168:171], v[92:95]\n"
    "ds_read_b128 a[144:147], v241 offset:40960\n"
    "ds_read_b128 a[140:143], v241 offset:38912\n"
    "v_mfma_f32_16x16x32_bf16 v[92:95], a[200:203], a[136:139], v[92:95]\n"
    "s_add_u32 m0, s68, 83968\n"
    "s_addk_i32 s59, 16384\n"
    "v_mfma_f32_16x16x32_bf16 v[60:63], a[200:203], a[132:135], v[60:63]\n"
    "buffer_load_dwordx4 v255, s[24:27], s59 offen  lds\n"
    "v_mfma_f32_16x16x32_bf16 v[60:63], a[204:207], a[164:167], v[60:63]\n"
    "ds_read_b128 a[172:175], v242 offset:38912\n"
    "ds_read_b128 a[168:171], v242 offset:36864\n"
    "v_mfma_f32_16x16x32_bf16 v[28:31], a[204:207], a[160:163], v[28:31]\n"
    "ds_read_b128 a[136:139], v241 offset:36864\n"
    "ds_read_b128 a[132:135], v241 offset:34816\n"
    "v_mfma_f32_16x16x32_bf16 v[28:31], a[200:203], a[128:131], v[28:31]\n"
    "s_waitcnt lgkmcnt(11)\n"
    "s_add_u32 m0, s68, 84992\n"
    "s_addk_i32 s59, 16384\n"
    "v_mfma_f32_16x16x32_bf16 v[224:227], a[208:211], a[156:159], v[224:227]\n"
    "buffer_load_dwordx4 v243, s[24:27], s59 offen  lds\n"
    "v_mfma_f32_16x16x32_bf16 v[224:227], a[212:215], a[188:191], v[224:227]\n"
    "s_waitcnt lgkmcnt(9)\n"
    "ds_read_b128 a[164:167], v242 offset:34816\n"
    "ds_read_b128 a[160:163], v242 offset:32768\n"
    "v_mfma_f32_16x16x32_bf16 v[192:195], a[212:215], a[184:187], v[192:195]\n"
    "ds_read_b128 a[128:131], v241 offset:32768\n"
    "s_lshr_b32 s81, %3, 2\n"
    "v_mfma_f32_16x16x32_bf16 v[192:195], a[208:211], a[152:155], v[192:195]\n"
    "s_waitcnt lgkmcnt(10)\n"
    "s_lshl_b32 s81, s81, 21\n"
    "s_and_b32 s61, %3, 3\n"
    "s_lshl_b32 s61, s61, 15\n"
    "s_add_u32 s81, s81, s61\n"
    "s_mul_i32 s61, s65, 0x2400\n"
    "s_and_b32 s61, s61, 0x6400\n"
    "s_add_u32 s81, s81, s61\n"
    "s_mov_b32 s82, 0\n"
    "s_mov_b32 s83, 0\n"
    "v_mfma_f32_16x16x32_bf16 v[160:163], a[208:211], a[148:151], v[160:163]\n"
    "s_xor_b32 s59, s66, 1792\n"
    "s_add_u32 m0, s68, 114688\n"
    "s_nop 0\n"
    "v_mfma_f32_16x16x32_bf16 v[160:163], a[212:215], a[180:183], v[160:163]\n"
    "s_waitcnt lgkmcnt(8)\n"
    "v_mfma_f32_16x16x32_bf16 v[128:131], a[212:215], a[176:179], v[128:131]\n"
    "buffer_load_dwordx4 v255, s[24:27], s59 offen  lds\n"
    "v_mfma_f32_16x16x32_bf16 v[128:131], a[208:211], a[144:147], v[128:131]\n"
    "s_waitcnt lgkmcnt(6)\n"
    "s_add_u32 m0, s68, 115712\n"
    "s_addk_i32 s59, 16384\n"
    "v_mfma_f32_16x16x32_bf16 v[96:99], a[208:211], a[140:143], v[96:99]\n"
    "buffer_load_dwordx4 v243, s[24:27], s59 offen  lds\n"
    "v_mfma_f32_16x16x32_bf16 v[96:99], a[212:215], a[172:175], v[96:99]\n"
    "s_waitcnt lgkmcnt(4)\n"
    "v_mfma_f32_16x16x32_bf16 v[64:67], a[212:215], a[168:171], v[64:67]\n"
    "v_mfma_f32_16x16x32_bf16 v[64:67], a[208:211], a[136:139], v[64:67]\n"
    "s_waitcnt lgkmcnt(2)\n"
    "s_add_u32 m0, s68, 116736\n"
    "s_addk_i32 s59, 16384\n"
    "v_mfma_f32_16x16x32_bf16 v[32:35], a[208:211], a[132:135], v[32:35]\n"
    "buffer_load_dwordx4 v255, s[24:27], s59 offen  lds\n"
    "v_mfma_f32_16x16x32_bf16 v[32:35], a[212:215], a[164:167], v[32:35]\n"
    "s_waitcnt lgkmcnt(0)\n"
    "v_mfma_f32_16x16x32_bf16 v[0:3], a[212:215], a[160:163], v[0:3]\n"
    "v_mfma_f32_16x16x32_bf16 v[0:3], a[208:211], a[128:131], v[0:3]\n"
    "s_add_u32 m0, s68, 117760\n"
    "s_addk_i32 s59, 16384\n"
    "v_mfma_f32_16x16x32_bf16 v[4:7], a[216:219], a[128:131], v[4:7]\n"
    "buffer_load_dwordx4 v243, s[24:27], s59 offen  lds\n"
    "v_mfma_f32_16x16x32_bf16 v[4:7], a[220:223], a[160:163], v[4:7]\n"
    "v_mfma_f32_16x16x32_bf16 v[36:39], a[220:223], a[164:167], v[36:39]\n"
    "v_mfma_f32_16x16x32_bf16 v[36:39], a[216:219], a[132:135], v[36:39]\n"
    "v_mfma_f32_16x16x32_bf16 v[68:71], a[216:219], a[136:139], v[68:71]\n"
    "s_load_dword s80, s[32:33], s81\n"
    "s_xor_b32 s81, s81, 8192\n"
    "s_load_dword s80, s[32:33], s81\n"
    "v_mfma_f32_16x16x32_bf16 v[68:71], a[220:223], a[168:171], v[68:71]\n"
    "s_xor_b32 s81, s81, 8192\n"
    "v_mfma_f32_16x16x32_bf16 v[100:103], a[220:223], a[172:175], v[100:103]\n"
    "v_mfma_f32_16x16x32_bf16 v[100:103], a[216:219], a[140:143], v[100:103]\n"
    "v_mfma_f32_16x16x32_bf16 v[132:135], a[216:219], a[144:147], v[132:135]\n"
    "s_bitcmp1_b32 s85, 11\n"
    "s_cbranch_scc1 CP11_%=\n"
    "CR11_%=:\n"
    "v_mfma_f32_16x16x32_bf16 v[132:135], a[220:223], a[176:179], v[132:135]\n"
    "v_mfma_f32_16x16x32_bf16 v[164:167], a[220:223], a[180:183], v[164:167]\n"
    "v_mfma_f32_16x16x32_bf16 v[164:167], a[216:219], a[148:151], v[164:167]\n"
    "v_mfma_f32_16x16x32_bf16 v[196:199], a[216:219], a[152:155], v[196:199]\n"
    "v_mfma_f32_16x16x32_bf16 v[196:199], a[220:223], a[184:187], v[196:199]\n"
    "v_mfma_f32_16x16x32_bf16 v[228:231], a[220:223], a[188:191], v[228:231]\n"
    "v_mfma_f32_16x16x32_bf16 v[228:231], a[216:219], a[156:159], v[228:231]\n"
    "v_add_u32 v252, 0x20000, v252\n"
    "v_add_u32 v253, 0x20000, v253\n"
    "v_mfma_f32_16x16x32_bf16 v[232:235], a[224:227], a[156:159], v[232:235]\n"
    "s_waitcnt vmcnt(12)\n"
    "ds_read_b64_tr_b16 a[192:193], v252 offset:0\n"
    "ds_read_b64_tr_b16 a[194:195], v252 offset:512\n"
    "v_mfma_f32_16x16x32_bf16 v[232:235], a[228:231], a[188:191], v[232:235]\n"
    "ds_read_b64_tr_b16 a[196:197], v252 offset:4096\n"
    "ds_read_b64_tr_b16 a[198:199], v252 offset:4608\n"
    "v_mfma_f32_16x16x32_bf16 v[200:203], a[228:231], a[184:187], v[200:203]\n"
    "v_mfma_f32_16x16x32_bf16 v[200:203], a[224:227], a[152:155], v[200:203]\n"
    "ds_read_b64_tr_b16 a[200:201], v252 offset:8\n"
    "ds_read_b64_tr_b16 a[202:203], v252 offset:520\n"
    "v_mfma_f32_16x16x32_bf16 v[168:171], a[224:227], a[148:151], v[168:171]\n"
    "v_mfma_f32_16x16x32_bf16 v[168:171], a[228:231], a[180:183], v[168:171]\n"
    "ds_read_b64_tr_b16 a[204:205], v252 offset:4104\n"
    "ds_read_b64_tr_b16 a[206:207], v252 offset:4616\n"
    "v_mfma_f32_16x16x32_bf16 v[136:139], a[228:231], a[176:179], v[136:139]\n"
    "v_mfma_f32_16x16x32_bf16 v[136:139], a[224:227], a[144:147], v[136:139]\n"
    "ds_read_b64_tr_b16 a[208:209], v253 offset:0\n"
    "ds_read_b64_tr_b16 a[210:211], v253 offset:512\n"
    "v_mfma_f32_16x16x32_bf16 v[104:107], a[224:227], a[140:143], v[104:107]\n"
    "v_mfma_f32_16x16x32_bf16 v[104:107], a[228:231], a[172:175], v[104:107]\n"
    "ds_read_b64_tr_b16 a[212:213], v253 offset:4096\n"
    "ds_read_b64_tr_b16 a[214:215], v253 offset:4608\n"
    "v_mfma_f32_16x16x32_bf16 v[72:75], a[228:231], a[168:171], v[72:75]\n"
    "v_mfma_f32_16x16x32_bf16 v[72:75], a[224:227], a[136:139], v[72:75]\n"
    "ds_read_b64_tr_b16 a[216:217], v253 offset:8\n"
    "ds_read_b64_tr_b16 a[218:219], v253 offset:520\n"
    "v_mfma_f32_16x16x32_bf16 v[40:43], a[224:227], a[132:135], v[40:43]\n"
    "v_mfma_f32_16x16x32_bf16 v[40:43], a[228:231], a[164:167], v[40:43]\n"
    "ds_read_b64_tr_b16 a[220:221], v253 offset:4104\n"
    "ds_read_b64_tr_b16 a[222:223], v253 offset:4616\n"
    "v_mfma_f32_16x16x32_bf16 v[8:11], a[228:231], a[160:163], v[8:11]\n"
    "v_mfma_f32_16x16x32_bf16 v[8:11], a[224:227], a[128:131], v[8:11]\n"
    "buffer_load_dwordx4 a[224:227], v240, s[20:23], s53 offen offset:0 sc1\n"
    "buffer_load_dwordx4 a[228:231], v240, s[20:23], s53 offen offset:1024 sc1\n"
    "s_lshr_b32 s60, %3, 3\n"
    "s_lshl_b32 s60, s60, 2\n"
    "s_add_u32 s60, s60, s65\n"
    "s_lshl_b32 s60, s60, 10\n"
    "s_min_u32 s60, s60, LAST_%= - LOOP_%= - 1024\n"
    "s_getpc_b64 s[62:63]\n"
    "LA5_%=:\n"
    "v_mfma_f32_16x16x32_bf16 v[12:15], a[232:235], a[128:131], v[12:15]\n"
    "s_add_u32 s62, s62, LOOP_%= - LA5_%=\n"
    "s_addc_u32 s63, s63, 0\n"
    "s_add_u32 s62, s62, s60\n"
    "v_mfma_f32_16x16x32_bf16 v[12:15], a[236:239], a[160:163], v[12:15]\n"
    "s_addc_u32 s63, s63, 0\n"
    "v_mfma_f32_16x16x32_bf16 v[44:47], a[236:239], a[164:167], v[44:47]\n"
    "global_load_dwordx4 v[248:251], v240, s[62:63]\n"
    "v_mfma_f32_16x16x32_bf16 v[44:47], a[232:235], a[132:135], v[44:47]\n"
    "v_mfma_f32_16x16x32_bf16 v[76:79], a[232:235], a[136:139], v[76:79]\n"
    "v_mfma_f32_16x16x32_bf16 v[76:79], a[236:239], a[168:171], v[76:79]\n"
    "v_mfma_f32_16x16x32_bf16 v[108:111], a[236:239], a[172:175], v[108:111]\n"
    "v_mfma_f32_16x16x32_bf16 v[108:111], a[232:235], a[140:143], v[108:111]\n"
    "v_mfma_f32_16x16x32_bf16 v[140:143], a[232:235], a[144:147], v[140:143]\n"
    "v_mfma_f32_16x16x32_bf16 v[140:143], a[236:239], a[176:179], v[140:143]\n"
    "v_mfma_f32_16x16x32_bf16 v[172:175], a[236:239], a[180:183], v[172:175]\n"
    "v_mfma_f32_16x16x32_bf16 v[172:175], a[232:235], a[148:151], v[172:175]\n"
    "v_mfma_f32_16x16x32_bf16 v[204:207], a[232:235], a[152:155], v[204:207]\n"
    "v_mfma_f32_16x16x32_bf16 v[204:207], a[236:239], a[184:187], v[204:207]\n"
    "v_mfma_f32_16x16x32_bf16 v[236:239], a[236:239], a[188:191], v[236:239]\n"
    "v_mfma_f32_16x16x32_bf16 v[236:239], a[232:235], a[156:159], v[236:239]\n"
    "s_waitcnt lgkmcnt(0)\n"
    "s_xor_b32 s81, s81, 8192\n"
    "s_load_dword s80, s[32:33], s81 offset:128\n"
    "s_load_dword s80, s[32:33], s81 offset:256\n"
    "v_mfma_f32_16x16x32_bf16 a[240:243], a[192:195], a[156:159], a[240:243]\n"
    "buffer_load_dwordx4 a[232:235], v240, s[20:23], s53 offen offset:2048 sc1\n"
    "v_mfma_f32_16x16x32_bf16 a[240:243], a[196:199], a[188:191], a[240:243]\n"
    "buffer_load_dwordx4 a[236:239], v240, s[20:23], s53 offen offset:3072 sc1\n"
    "v_mfma_f32_16x16x32_bf16 v[208:211], a[196:199], a[184:187], v[208:211]\n"
    "s_addk_i32 s53, 4096\n"
    "v_mfma_f32_16x16x32_bf16 v[208:211], a[192:195], a[152:155], v[208:211]\n"
    "v_mfma_f32_16x16x32_bf16 v[176:179], a[192:195], a[148:151], v[176:179]\n"
    "v_mfma_f32_16x16x32_bf16 v[176:179], a[196:199], a[180:183], v[176:179]\n"
    "v_mfma_f32_16x16x32_bf16 v[144:147], a[196:199], a[176:179], v[144:147]\n"
    "v_mfma_f32_16x16x32_bf16 v[144:147], a[192:195], a[144:147], v[144:147]\n"
    "v_mfma_f32_16x16x32_bf16 v[112:115], a[192:195], a[140:143], v[112:115]\n"
    "v_mfma_f32_16x16x32_bf16 v[112:115], a[196:199], a[172:175], v[112:115]\n"
    "v_mfma_f32_16x16x32_bf16 v[80:83], a[196:199], a[168:171], v[80:83]\n"
    "v_mfma_f32_16x16x32_bf16 v[80:83], a[192:195], a[136:139], v[80:83]\n"
    "v_mfma_f32_16x16x32_bf16 v[48:51], a[192:195], a[132:135], v[48:51]\n"
    "v_mfma_f32_16x16x32_bf16 v[48:51], a[196:199], a[164:167], v[48:51]\n"
    "v_mfma_f32_16x16x32_bf16 v[16:19], a[196:199], a[160:163], v[16:19]\n"
    "v_mfma_f32_16x16x32_bf16 v[16:19], a[192:195], a[128:131], v[16:19]\n"
    "s_getreg_b32 s73, hwreg(HW_REG_IB_STS, 8, 4)\n"
    "s_add_u32 s82, s82, s73\n"
    "v_mfma_f32_16x16x32_bf16 v[20:23], a[200:203], a[128:131], v[20:23]\n"
    "buffer_load_dwordx4 a[192:195], v240, s[20:23], s53 offen offset:0 sc1\n"
    "v_mfma_f32_16x16x32_bf16 v[20:23], a[204:207], a[160:163], v[20:23]\n"
    "buffer_load_dwordx4 a[196:199], v240, s[20:23], s53 offen offset:1024 sc1\n"
    "v_mfma_f32_16x16x32_bf16 v[52:55], a[204:207], a[164:167], v[52:55]\n"
    "s_getreg_b32 s73, hwreg(HW_REG_IB_STS, 8, 4)\n"
    "s_add_u32 s82, s82, s73\n"
    "v_mfma_f32_16x16x32_bf16 v[52:55], a[200:203], a[132:135], v[52:55]\n"
    "s_getreg_b32 s73, hwreg(HW_REG_IB_STS, 8, 4)\n"
    "s_add_u32 s82, s82, s73\n"
    "v_mfma_f32_16x16x32_bf16 v[84:87], a[200:203], a[136:139], v[84:87]\n"
    "v_mfma_f32_16x16x32_bf16 v[84:87], a[204:207], a[168:171], v[84:87]\n"
    "s_getreg_b32 s73, hwreg(HW_REG_IB_STS, 8, 4)\n"
    "s_add_u32 s82, s82, s73\n"
    "v_mfma_f32_16x16x32_bf16 v[116:119], a[204:207], a[172:175], v[116:119]\n"
    "v_mfma_f32_16x16x32_bf16 v[116:119], a[200:203], a[140:143], v[116:119]\n"
    "s_getreg_b32 s73, hwreg(HW_REG_IB_STS, 8, 4)\n"
    "s_add_u32 s82, s82, s73\n"
    "v_mfma_f32_16x16x32_bf16 v[148:151], a[200:203], a[144:147], v[148:151]\n"
    "v_mfma_f32_16x16x32_bf16 v[148:151], a[204:207], a[176:179], v[148:151]\n"
    "s_getreg_b32 s73, hwreg(HW_REG_IB_STS, 8, 4)\n"
    "s_add_u32 s82, s82, s73\n"
    "v_mfma_f32_16x16x32_bf16 v[180:183], a[204:207], a[180:183], v[180:183]\n"
    "v_mfma_f32_16x16x32_bf16 v[180:183], a[200:203], a[148:151], v[180:183]\n"
    "s_getreg_b32 s73, hwreg(HW_REG_IB_STS, 8, 4)\n"
    "s_add_u32 s82, s82, s73\n"
    "v_mfma_f32_16x16x32_bf16 v[212:215], a[200:203], a[152:155], v[212:215]\n"
    "v_mfma_f32_16x16x32_bf16 v[212:215], a[204:207], a[184:187], v[212:215]\n"
    "s_getreg_b32 s73, hwreg(HW_REG_IB_STS, 8, 4)\n"
    "s_add_u32 s82, s82, s73\n"
    "v_mfma_f32_16x16x32_bf16 a[244:247], a[204:207], a[188:191], a[244:247]\n"
    "v_mfma_f32_16x16x32_bf16 a[244:247], a[200:203], a[156:159], a[244:247]\n"
    "s_getreg_b32 s73, hwreg(HW_REG_IB_STS, 8, 4)\n"
    "s_add_u32 s82, s82, s73\n"
    "v_mfma_f32_16x16x32_bf16 a[248:251], a[208:211], a[156:159], a[248:251]\n"
    "buffer_load_dwordx4 a[200:203], v240, s[20:23], s53 offen offset:2048 sc1\n"
    "v_mfma_f32_16x16x32_bf16 a[248:251], a[212:215], a[188:191], a[248:251]\n"
    "buffer_load_dwordx4 a[204:207], v240, s[20:23], s53 offen offset:3072 sc1\n"
    "v_mfma_f32_16x16x32_bf16 v[216:219], a[212:215], a[184:187], v[216:219]\n"
    "s_addk_i32 s53, 4096\n"
    "s_getreg_b32 s73, hwreg(HW_REG_IB_STS, 8, 4)\n"
    "s_add_u32 s82, s82, s73\n"
    "v_mfma_f32_16x16x32_bf16 v[216:219], a[208:211], a[152:155], v[216:219]\n"
    "s_getreg_b32 s73, hwreg(HW_REG_IB_STS, 8, 4)\n"
    "s_add_u32 s82, s82, s73\n"
    "v_mfma_f32_16x16x32_bf16 v[184:187], a[208:211], a[148:151], v[184:187]\n"
    "v_mfma_f32_16x16x32_bf16 v[184:187], a[212:215], a[180:183], v[184:187]\n"
    "s_getreg_b32 s73, hwreg(HW_REG_IB_STS, 8, 4)\n"
    "s_add_u32 s82, s82, s73\n"
    "v_mfma_f32_16x16x32_bf16 v[152:155], a[212:215], a[176:179], v[152:155]\n"
    "v_mfma_f32_16x16x32_bf16 v[152:155], a[208:211], a[144:147], v[152:155]\n"
    "s_getreg_b32 s73, hwreg(HW_REG_IB_STS, 8, 4)\n"
    "s_add_u32 s82, s82, s73\n"
    "v_mfma_f32_16x16x32_bf16 v[120:123], a[208:211], a[140:143], v[120:123]\n"
    "v_mfma_f32_16x16x32_bf16 v[120:123], a[212:215], a[172:175], v[120:123]\n"
    "s_getreg_b32 s73, hwreg(HW_REG_IB_STS, 8, 4)\n"
    "s_add_u32 s82, s82, s73\n"
    "v_mfma_f32_16x16x32_bf16 v[88:91], a[212:215], a[168:171], v[88:91]\n"
    "v_mfma_f32_16x16x32_bf16 v[88:91], a[208:211], a[136:139], v[88:91]\n"
    "s_getreg_b32 s73, hwreg(HW_REG_IB_STS, 8, 4)\n"
    "s_add_u32 s82, s82, s73\n"
    "v_mfma_f32_16x16x32_bf16 v[56:59], a[208:211], a[132:135], v[56:59]\n"
    "v_mfma_f32_16x16x32_bf16 v[56:59], a[212:215], a[164:167], v[56:59]\n"
    "s_waitcnt vmcnt(13)\n"
    "s_barrier\n"
    "s_getreg_b32 s73, hwreg(HW_REG_IB_STS, 8, 4)\n"
    "v_mfma_f32_16x16x32_bf16 v[24:27], a[212:215], a[160:163], v[24:27]\n"
    "s_add_u32 s82, s82, s73\n"
    "v_mfma_f32_16x16x32_bf16 v[24:27], a[208:211], a[128:131], v[24:27]\n"
    "buffer_load_dwordx4 a[208:211], v240, s[20:23], s53 offen offset:0 sc1\n"
    "v_mfma_f32_16x16x32_bf16 v[28:31], a[216:219], a[128:131], v[28:31]\n"
    "buffer_load_dwordx4 a[212:215], v240, s[20:23], s53 offen offset:1024 sc1\n"
    "v_mfma_f32_16x16x32_bf16 v[28:31], a[220:223], a[160:163], v[28:31]\n"
    "v_mfma_f32_16x16x32_bf16 v[60:63], a[220:223], a[164:167], v[60:63]\n"
    "ds_read_b128 a[128:131], v241 offset:0\n"
    "v_mfma_f32_16x16x32_bf16 v[60:63], a[216:219], a[132:135], v[60:63]\n"
    "s_xor_b32 s59, s66, 1920\n"
    "s_add_u32 m0, s68, 98304\n"
    "s_nop 0\n"
    "v_mfma_f32_16x16x32_bf16 v[92:95], a[216:219], a[136:139], v[92:95]\n"
    "buffer_load_dwordx4 v255, s[24:27], s59 offen  lds\n"
    "v_mfma_f32_16x16x32_bf16 v[92:95], a[220:223], a[168:171], v[92:95]\n"
    "ds_read_b128 a[160:163], v242 offset:0\n"
    "ds_read_b128 a[164:167], v242 offset:2048\n"
    "v_mfma_f32_16x16x32_bf16 v[124:127], a[220:223], a[172:175], v[124:127]\n"
    "ds_read_b128 a[132:135], v241 offset:2048\n"
    "ds_read_b128 a[136:139], v241 offset:4096\n"
    "v_mfma_f32_16x16x32_bf16 v[124:127], a[216:219], a[140:143], v[124:127]\n"
    "s_add_u32 m0, s68, 99328\n"
    "s_addk_i32 s59, 16384\n"
    "v_mfma_f32_16x16x32_bf16 v[156:159], a[216:219], a[144:147], v[156:159]\n"
    "buffer_load_dwordx4 v243, s[24:27], s59 offen  lds\n"
    "v_mfma_f32_16x16x32_bf16 v[156:159], a[220:223], a[176:179], v[156:159]\n"
    "ds_read_b128 a[168:171], v242 offset:4096\n"
    "ds_read_b128 a[172:175], v242 offset:6144\n"
    "v_mfma_f32_16x16x32_bf16 v[188:191], a[220:223], a[180:183], v[188:191]\n"
    "ds_read_b128 a[140:143], v241 offset:6144\n"
    "ds_read_b128 a[144:147], v241 offset:8192\n"
    "v_mfma_f32_16x16x32_bf16 v[188:191], a[216:219], a[148:151], v[188:191]\n"
    "s_add_u32 m0, s68, 100352\n"
    "s_addk_i32 s59, 16384\n"
    "v_mfma_f32_16x16x32_bf16 v[220:223], a[216:219], a[152:155], v[220:223]\n"
    "buffer_load_dwordx4 v255, s[24:27], s59 offen  lds\n"
    "v_mfma_f32_16x16x32_bf16 v[220:223], a[220:223], a[184:187], v[220:223]\n"
    "ds_read_b128 a[176:179], v242 offset:8192\n"
    "ds_read_b128 a[180:183], v242 offset:10240\n"
    "v_mfma_f32_16x16x32_bf16 a[252:255], a[220:223], a[188:191], a[252:255]\n"
    "ds_read_b128 a[148:151], v241 offset:10240\n"
    "ds_read_b128 a[152:155], v241 offset:12288\n"
    "v_mfma_f32_16x16x32_bf16 a[252:255], a[216:219], a[156:159], a[252:255]\n"
    "s_waitcnt vmcnt(12) lgkmcnt(11)\n"
    "s_add_u32 m0, s68, 101376\n"
    "s_addk_i32 s59, 16384\n"
    "v_mfma_f32_16x16x32_bf16 v[0:3], a[224:227], a[128:131], v[0:3]\n"
    "buffer_load_dwordx4 v243, s[24:27], s59 offen  lds\n"
    "v_mfma_f32_16x16x32_bf16 v[0:3], a[228:231], a[160:163], v[0:3]\n"
    "s_waitcnt lgkmcnt(9)\n"
    "ds_read_b128 a[184:187], v242 offset:12288\n"
    "ds_read_b128 a[188:191], v242 offset:14336\n"
    "v_mfma_f32_16x16x32_bf16 v[32:35], a[228:231], a[164:167], v[32:35]\n"
    "ds_read_b128 a[156:159], v241 offset:14336\n"
    "v_mfma_f32_16x16x32_bf16 v[32:35], a[224:227], a[132:135], v[32:35]\n"
    "s_waitcnt lgkmcnt(10)\n"
    "v_mfma_f32_16x16x32_bf16 v[64:67], a[224:227], a[136:139], v[64:67]\n"
    "buffer_load_dwordx4 a[216:219], v240, s[20:23], s53 offen offset:2048 sc1\n"
    "v_mfma_f32_16x16x32_bf16 v[64:67], a[228:231], a[168:171], v[64:67]\n"
    "s_waitcnt lgkmcnt(8)\n"
    "v_mfma_f32_16x16x32_bf16 v[96:99], a[228:231], a[172:175], v[96:99]\n"
    "buffer_load_dwordx4 a[220:223], v240, s[20:23], s53 offen offset:3072 sc1\n"
    "s_addk_i32 s53, 4096\n"
    "v_mfma_f32_16x16x32_bf16 v[96:99], a[224:227], a[140:143], v[96:99]\n"
    "s_waitcnt lgkmcnt(6)\n"
    "v_mfma_f32_16x16x32_bf16 v[128:131], a[224:227], a[144:147], v[128:131]\n"
    "v_mfma_f32_16x16x32_bf16 v[128:131], a[228:231], a[176:179], v[128:131]\n"
    "s_waitcnt lgkmcnt(4)\n"
    "v_mfma_f32_16x16x32_bf16 v[160:163], a[228:231], a[180:183], v[160:163]\n"
    "v_mfma_f32_16x16x32_bf16 v[160:163], a[224:227], a[148:151], v[160:163]\n"
    "s_waitcnt lgkmcnt(2)\n"
    "v_mfma_f32_16x16x32_bf16 v[192:195], a[224:227], a[152:155], v[192:195]\n"
    "v_mfma_f32_16x16x32_bf16 v[192:195], a[228:231], a[184:187], v[192:195]\n"
    "s_bitcmp1_b32 s85, 12\n"
    "s_cbranch_scc1 CP12_%=\n"
    "CR12_%=:\n"
    "s_waitcnt lgkmcnt(0)\n"
    "v_mfma_f32_16x16x32_bf16 v[224:227], a[228:231], a[188:191], v[224:227]\n"
    "v_mfma_f32_16x16x32_bf16 v[224:227], a[224:227], a[156:159], v[224:227]\n"
    "s_waitcnt lgkmcnt(0)\n"
    "s_xor_b32 s81, s81, 8192\n"
    "s_load_dword s80, s[32:33], s81 offset:128\n"
    "s_load_dword s80, s[32:33], s81 offset:256\n"
    "s_waitcnt vmcnt(12)\n"
    "v_mfma_f32_16x16x32_bf16 v[228:231], a[232:235], a[156:159], v[228:231]\n"
    "buffer_load_dwordx4 a[224:227], v240, s[20:23], s53 offen offset:0 sc1\n"
    "v_mfma_f32_16x16x32_bf16 v[228:231], a[236:239], a[188:191], v[228:231]\n"
    "buffer_load_dwordx4 a[228:231], v240, s[20:23], s53 offen offset:1024 sc1\n"
    "v_mfma_f32_16x16x32_bf16 v[196:199], a[236:239], a[184:187], v[196:199]\n"
    "v_mfma_f32_16x16x32_bf16 v[196:199], a[232:235], a[152:155], v[196:199]\n"
    "v_mfma_f32_16x16x32_bf16 v[164:167], a[232:235], a[148:151], v[164:167]\n"
    "v_mfma_f32_16x16x32_bf16 v[164:167], a[236:239], a[180:183], v[164:167]\n"
    "v_mfma_f32_16x16x32_bf16 v[132:135], a[236:239], a[176:179], v[132:135]\n"
    "v_mfma_f32_16x16x32_bf16 v[132:135], a[232:235], a[144:147], v[132:135]\n"
    "v_mfma_f32_16x16x32_bf16 v[100:103], a[232:235], a[140:143], v[100:103]\n"
    "v_mfma_f32_16x16x32_bf16 v[100:103], a[236:239], a[172:175], v[100:103]\n"
    "v_mfma_f32_16x16x32_bf16 v[68:71], a[236:239], a[168:171], v[68:71]\n"
    "v_mfma_f32_16x16x32_bf16 v[68:71], a[232:235], a[136:139], v[68:71]\n"
    "v_mfma_f32_16x16x32_bf16 v[36:39], a[232:235], a[132:135], v[36:39]\n"
    "v_mfma_f32_16x16x32_bf16 v[36:39], a[236:239], a[164:167], v[36:39]\n"
    "v_mfma_f32_16x16x32_bf16 v[4:7], a[236:239], a[160:163], v[4:7]\n"
    "v_mfma_f32_16x16x32_bf16 v[4:7], a[232:235], a[128:131], v[4:7]\n"
    "s_waitcnt vmcnt(12)\n"
    "s_getreg_b32 s73, hwreg(HW_REG_IB_STS, 8, 4)\n"
    "s_add_u32 s83, s83, s73\n"
    "v_mfma_f32_16x16x32_bf16 v[8:11], a[192:195], a[128:131], v[8:11]\n"
    "buffer_load_dwordx4 a[232:235], v240, s[20:23], s53 offen offset:2048 sc1\n"
    "v_mfma_f32_16x16x32_bf16 v[8:11], a[196:199], a[160:163], v[8:11]\n"
    "buffer_load_dwordx4 a[236:239], v240, s[20:23], s53 offen offset:3072 sc1\n"
    "v_mfma_f32_16x16x32_bf16 v[40:43], a[196:199], a[164:167], v[40:43]\n"
    "s_addk_i32 s53, 4096\n"
    "v_mfma_f32_16x16x32_bf16 v[40:43], a[192:195], a[132:135], v[40:43]\n"
    "buffer_load_dword v253, v240, s[36:39], s55 offen offset:0 sc1\n"
    "v_mfma_f32_16x16x32_bf16 v[72:75], a[192:195], a[136:139], v[72:75]\n"
    "buffer_load_dword v252, v240, s[36:39], s55 offen offset:1024 sc1\n"
    "v_mfma_f32_16x16x32_bf16 v[72:75], a[196:199], a[168:171], v[72:75]\n"
    "s_getreg_b32 s73, hwreg(HW_REG_IB_STS, 8, 4)\n"
    "s_add_u32 s83, s83, s73\n"
    "s_getreg_b32 s73, hwreg(HW_REG_IB_STS, 8, 4)\n"
    "v_mfma_f32_16x16x32_bf16 v[104:107], a[196:199], a[172:175], v[104:107]\n"
    "s_add_u32 s83, s83, s73\n"
    "s_getreg_b32 s73, hwreg(HW_REG_IB_STS, 8, 4)\n"
    "s_add_u32 s83, s83, s73\n"
    "v_mfma_f32_16x16x32_bf16 v[104:107], a[192:195], a[140:143], v[104:107]\n"
    "s_getreg_b32 s73, hwreg(HW_REG_IB_STS, 8, 4)\n"
    "s_add_u32 s83, s83, s73\n"
    "v_mfma_f32_16x16x32_bf16 v[136:139], a[192:195], a[144:147], v[136:139]\n"
    "v_mfma_f32_16x16x32_bf16 v[136:139], a[196:199], a[176:179], v[136:139]\n"
    "s_getreg_b32 s73, hwreg(HW_REG_IB_STS, 8, 4)\n"
    "s_add_u32 s83, s83, s73\n"
    "v_mfma_f32_16x16x32_bf16 v[168:171], a[196:199], a[180:183], v[168:171]\n"
    "v_mfma_f32_16x16x32_bf16 v[168:171], a[192:195], a[148:151], v[168:171]\n"
    "s_getreg_b32 s73, hwreg(HW_REG_IB_STS, 8, 4)\n"
    "s_add_u32 s83, s83, s73\n"
    "v_mfma_f32_16x16x32_bf16 v[200:203], a[192:195], a[152:155], v[200:203]\n"
    "v_mfma_f32_16x16x32_bf16 v[200:203], a[196:199], a[184:187], v[200:203]\n"
    "s_getreg_b32 s73, hwreg(HW_REG_IB_STS, 8, 4)\n"
    "s_add_u32 s83, s83, s73\n"
    "v_mfma_f32_16x16x32_bf16 v[232:235], a[196:199], a[188:191], v[232:235]\n"
    "v_mfma_f32_16x16x32_bf16 v[232:235], a[192:195], a[156:159], v[232:235]\n"
    "s_waitcnt vmcnt(14)\n"
    "s_getreg_b32 s73, hwreg(HW_REG_IB_STS, 8, 4)\n"
    "s_add_u32 s83, s83, s73\n"
    "v_mfma_f32_16x16x32_bf16 v[236:239], a[200:203], a[156:159], v[236:239]\n"
    "buffer_load_dwordx4 a[192:195], v240, s[20:23], s53 offen offset:0 sc1\n"
    "v_mfma_f32_16x16x32_bf16 v[236:239], a[204:207], a[188:191], v[236:239]\n"
    "buffer_load_dwordx4 a[196:199], v240, s[20:23], s53 offen offset:1024 sc1\n"
    "v_mfma_f32_16x16x32_bf16 v[204:207], a[204:207], a[184:187], v[204:207]\n"
    "s_getreg_b32 s73, hwreg(HW_REG_IB_STS, 8, 4)\n"
    "s_add_u32 s83, s83, s73\n"
    "ds_read_b128 a[156:159], v241 offset:30720\n"
    "v_mfma_f32_16x16x32_bf16 v[204:207], a[200:203], a[152:155], v[204:207]\n"
    "s_getreg_b32 s73, hwreg(HW_REG_IB_STS, 8, 4)\n"
    "s_add_u32 s83, s83, s73\n"
    "ds_read_b128 a[188:191], v242 offset:30720\n"
    "v_mfma_f32_16x16x32_bf16 v[172:175], a[200:203], a[148:151], v[172:175]\n"
    "ds_read_b128 a[184:187], v242 offset:28672\n"
    "v_mfma_f32_16x16x32_bf16 v[172:175], a[204:207], a[180:183], v[172:175]\n"
    "s_getreg_b32 s73, hwreg(HW_REG_IB_STS, 8, 4)\n"
    "s_add_u32 s83, s83, s73\n"
    "ds_read_b128 a[152:155], v241 offset:28672\n"
    "v_mfma_f32_16x16x32_bf16 v[140:143], a[204:207], a[176:179], v[140:143]\n"
    "ds_read_b128 a[148:151], v241 offset:26624\n"
    "v_mfma_f32_16x16x32_bf16 v[140:143], a[200:203], a[144:147], v[140:143]\n"
    "s_getreg_b32 s73, hwreg(HW_REG_IB_STS, 8, 4)\n"
    "s_add_u32 s83, s83, s73\n"
    "ds_read_b128 a[180:183], v242 offset:26624\n"
    "v_mfma_f32_16x16x32_bf16 v[108:111], a[200:203], a[140:143], v[108:111]\n"
    "ds_read_b128 a[176:179], v242 offset:24576\n"
    "v_mfma_f32_16x16x32_bf16 v[108:111], a[204:207], a[172:175], v[108:111]\n"
    "s_getreg_b32 s73, hwreg(HW_REG_IB_STS, 8, 4)\n"
    "s_add_u32 s83, s83, s73\n"
    "ds_read_b128 a[144:147], v241 offset:24576\n"
    "v_mfma_f32_16x16x32_bf16 v[76:79], a[204:207], a[168:171], v[76:79]\n"
    "ds_read_b128 a[140:143], v241 offset:22528\n"
    "v_mfma_f32_16x16x32_bf16 v[76:79], a[200:203], a[136:139], v[76:79]\n"
    "s_getreg_b32 s73, hwreg(HW_REG_IB_STS, 8, 4)\n"
    "s_add_u32 s83, s83, s73\n"
    "ds_read_b128 a[172:175], v242 offset:22528\n"
    "v_mfma_f32_16x16x32_bf16 v[44:47], a[200:203], a[132:135], v[44:47]\n"
    "ds_read_b128 a[168:171], v242 offset:20480\n"
    "v_mfma_f32_16x16x32_bf16 v[44:47], a[204:207], a[164:167], v[44:47]\n"
    "s_getreg_b32 s73, hwreg(HW_REG_IB_STS, 8, 4)\n"
    "s_add_u32 s83, s83, s73\n"
    "ds_read_b128 a[136:139], v241 offset:20480\n"
    "v_mfma_f32_16x16x32_bf16 v[12:15], a[204:207], a[160:163], v[12:15]\n"
    "ds_read_b128 a[132:135], v241 offset:18432\n"
    "v_mfma_f32_16x16x32_bf16 v[12:15], a[200:203], a[128:131], v[12:15]\n"
    "s_waitcnt vmcnt(14) lgkmcnt(11)\n"
    "ds_read_b128 a[164:167], v242 offset:18432\n"
    "ds_read_b128 a[160:163], v242 offset:16384\n"
    "v_mfma_f32_16x16x32_bf16 v[224:227], a[208:211], a[156:159], v[224:227]\n"
    "ds_read_b128 a[128:131], v241 offset:16384\n"
    "v_mfma_f32_16x16x32_bf16 v[224:227], a[212:215], a[188:191], v[224:227]\n"
    "s_waitcnt lgkmcnt(12)\n"
    "v_mfma_f32_16x16x32_bf16 v[192:195], a[212:215], a[184:187], v[192:195]\n"
    "buffer_load_dwordx4 a[200:203], v240, s[20:23], s53 offen offset:2048 sc1\n"
    "v_mfma_f32_16x16x32_bf16 v[192:195], a[208:211], a[152:155], v[192:195]\n"
    "s_waitcnt lgkmcnt(10)\n"
    "v_mfma_f32_16x16x32_bf16 v[160:163], a[208:211], a[148:151], v[160:163]\n"
    "buffer_load_dwordx4 a[204:207], v240, s[20:23], s53 offen offset:3072 sc1\n"
    "s_addk_i32 s53, 4096\n"
    "v_mfma_f32_16x16x32_bf16 v[160:163], a[212:215], a[180:183], v[160:163]\n"
    "s_waitcnt lgkmcnt(8)\n"
    "s_bitcmp1_b32 s65, 0\n"
    "s_cselect_b32 s60, s82, s83\n"
    "v_mfma_f32_16x16x32_bf16 v[128:131], a[212:215], a[176:179], v[128:131]\n"
    "s_cselect_b32 s61, s83, s82\n"
    "v_mov_b32 v246, s60\n"
    "v_mov_b32 v247, s61\n"
    "v_mfma_f32_16x16x32_bf16 v[128:131], a[208:211], a[144:147], v[128:131]\n"
    "s_waitcnt lgkmcnt(6)\n"
    "s_lshl_b32 s60, s65, 3\n"
    "s_add_u32 s60, s60, %2\n"
    "v_mfma_f32_16x16x32_bf16 v[96:99], a[208:211], a[140:143], v[96:99]\n"
    "s_add_u32 s60, s60, 131072\n"
    "v_mov_b32 v248, s60\n"
    "v_mfma_f32_16x16x32_bf16 v[96:99], a[212:215], a[172:175], v[96:99]\n"
    "s_waitcnt lgkmcnt(4)\n"
    "v_mfma_f32_16x16x32_bf16 v[64:67], a[212:215], a[168:171], v[64:67]\n"
    "ds_write_b64 v248, v[246:247]\n"
    "v_mfma_f32_16x16x32_bf16 v[64:67], a[208:211], a[136:139], v[64:67]\n"
    "s_waitcnt lgkmcnt(3)\n"
    "v_mfma_f32_16x16x32_bf16 v[32:35], a[208:211], a[132:135], v[32:35]\n"
    "v_mfma_f32_16x16x32_bf16 v[32:35], a[212:215], a[164:167], v[32:35]\n"
    "s_bitcmp1_b32 s85, 13\n"
    "s_cbranch_scc1 CP13_%=\n"
    "CR13_%=:\n"
    "s_waitcnt lgkmcnt(1)\n"
    "v_mfma_f32_16x16x32_bf16 v[0:3], a[212:215], a[160:163], v[0:3]\n"
    "v_mfma_f32_16x16x32_bf16 v[0:3], a[208:211], a[128:131], v[0:3]\n"
    "s_waitcnt vmcnt(10)\n"
    "v_mfma_f32_16x16x32_bf16 v[4:7], a[216:219], a[128:131], v[4:7]\n"
    "buffer_load_dwordx4 a[208:211], v240, s[20:23], s53 offen offset:0 sc1\n"
    "v_mfma_f32_16x16x32_bf16 v[4:7], a[220:223], a[160:163], v[4:7]\n"
    "buffer_load_dwordx4 a[212:215], v240, s[20:23], s53 offen offset:1024 sc1\n"
    "v_mfma_f32_16x16x32_bf16 v[36:39], a[220:223], a[164:167], v[36:39]\n"
    "s_lshr_b32 s60, s65, 1\n"
    "s_lshl_b32 s60, s60, 14\n"
    "s_and_b32 s61, s65, 1\n"
    "v_mfma_f32_16x16x32_bf16 v[36:39], a[216:219], a[132:135], v[36:39]\n"
    "s_lshl_b32 s61, s61, 10\n"
    "s_add_u32 s62, s60, s61\n"
    "s_add_u32 s62, s62, %2\n"
    "v_add_u32 v243, s62, v240\n"
    "s_mov_b32 s71, s62\n"
    "s_add_u32 s56, s62, 131072\n"
    "s_lshl_b32 s60, s60, 1\n"
    "s_add_u32 s60, s60, s61\n"
    "s_add_u32 s32, s32, s60\n"
    "s_lshr_b32 s60, %3, 4\n"
    "s_lshl_b32 s60, s60, 22\n"
    "s_and_b32 s61, %3, 7\n"
    "s_lshl_b32 s61, s61, 19\n"
    "s_add_u32 s54, s60, s61\n"
    "s_add_u32 s54, s54, 134217728\n"
    "s_bitcmp1_b32 %3, 3\n"
    "s_cselect_b32 s60, 0x40000, 0\n"
    "s_xor_b32 s54, s54, s60\n"
    "s_xor_b32 s59, s54, 0x40000\n"
    "s_mov_b64 s[24:25], s[12:13]\n"
    "s_add_u32 s24, s24, 266240\n"
    "s_addc_u32 s25, s25, 0\n"
    "s_and_b32 s25, s25, 0xffff\n"
    "s_mov_b32 s26, 1024\n"
    "s_mov_b32 s66, %2\n"
    "s_add_u32 s57, %2, 65536\n"
    "s_add_u32 s67, %2, 131072\n"
    "v_mfma_f32_16x16x32_bf16 v[68:71], a[216:219], a[136:139], v[68:71]\n"
    "v_mfma_f32_16x16x32_bf16 v[68:71], a[220:223], a[168:171], v[68:71]\n"
    "v_mfma_f32_16x16x32_bf16 v[100:103], a[220:223], a[172:175], v[100:103]\n"
    "v_mfma_f32_16x16x32_bf16 v[100:103], a[216:219], a[140:143], v[100:103]\n"
    "v_mfma_f32_16x16x32_bf16 v[132:135], a[216:219], a[144:147], v[132:135]\n"
    "v_mfma_f32_16x16x32_bf16 v[132:135], a[220:223], a[176:179], v[132:135]\n"
    "v_mfma_f32_16x16x32_bf16 v[164:167], a[220:223], a[180:183], v[164:167]\n"
    "v_mfma_f32_16x16x32_bf16 v[164:167], a[216:219], a[148:151], v[164:167]\n"
    "v_mfma_f32_16x16x32_bf16 v[196:199], a[216:219], a[152:155], v[196:199]\n"
    "v_mfma_f32_16x16x32_bf16 v[196:199], a[220:223], a[184:187], v[196:199]\n"
    "v_mfma_f32_16x16x32_bf16 v[228:231], a[220:223], a[188:191], v[228:231]\n"
    "v_mfma_f32_16x16x32_bf16 v[228:231], a[216:219], a[156:159], v[228:231]\n"
    "s_waitcnt vmcnt(10)\n"
    "v_mfma_f32_16x16x32_bf16 v[232:235], a[224:227], a[156:159], v[232:235]\n"
    "buffer_load_dwordx4 a[216:219], v240, s[20:23], s53 offen offset:2048 sc1\n"
    "v_mfma_f32_16x16x32_bf16 v[232:235], a[228:231], a[188:191], v[232:235]\n"
    "buffer_load_dwordx4 a[220:223], v240, s[20:23], s53 offen offset:3072 sc1\n"
    "v_mfma_f32_16x16x32_bf16 v[200:203], a[228:231], a[184:187], v[200:203]\n"
    "s_addk_i32 s53, 4096\n"
    "v_mfma_f32_16x16x32_bf16 v[200:203], a[224:227], a[152:155], v[200:203]\n"
    "v_mfma_f32_16x16x32_bf16 v[168:171], a[224:227], a[148:151], v[168:171]\n"
    "v_mfma_f32_16x16x32_bf16 v[168:171], a[228:231], a[180:183], v[168:171]\n"
    "v_mfma_f32_16x16x32_bf16 v[136:139], a[228:231], a[176:179], v[136:139]\n"
    "v_mfma_f32_16x16x32_bf16 v[136:139], a[224:227], a[144:147], v[136:139]\n"
    "v_mfma_f32_16x16x32_bf16 v[104:107], a[224:227], a[140:143], v[104:107]\n"
    "v_mfma_f32_16x16x32_bf16 v[104:107], a[228:231], a[172:175], v[104:107]\n"
    "v_mfma_f32_16x16x32_bf16 v[72:75], a[228:231], a[168:171], v[72:75]\n"
    "v_mfma_f32_16x16x32_bf16 v[72:75], a[224:227], a[136:139], v[72:75]\n"
    "v_mfma_f32_16x16x32_bf16 v[40:43], a[224:227], a[132:135], v[40:43]\n"
    "v_mfma_f32_16x16x32_bf16 v[40:43], a[228:231], a[164:167], v[40:43]\n"
    "s_waitcnt lgkmcnt(0)\n"
    "s_barrier\n"
    "v_mfma_f32_16x16x32_bf16 v[8:11], a[228:231], a[160:163], v[8:11]\n"
    "v_mfma_f32_16x16x32_bf16 v[8:11], a[224:227], a[128:131], v[8:11]\n"
    "s_waitcnt vmcnt(10)\n"
    "v_mfma_f32_16x16x32_bf16 v[12:15], a[232:235], a[128:131], v[12:15]\n"
    "buffer_load_dwordx4 a[224:227], v240, s[20:23], s53 offen offset:0 sc1\n"
    "v_mfma_f32_16x16x32_bf16 v[12:15], a[236:239], a[160:163], v[12:15]\n"
    "buffer_load_dwordx4 a[228:231], v240, s[20:23], s53 offen offset:1024 sc1\n"
    "v_mfma_f32_16x16x32_bf16 v[44:47], a[236:239], a[164:167], v[44:47]\n"
    "ds_read_b128 a[128:131], v241 offset:49152\n"
    "v_mfma_f32_16x16x32_bf16 v[44:47], a[232:235], a[132:135], v[44:47]\n"
    "ds_read_b128 a[160:163], v242 offset:49152\n"
    "v_mfma_f32_16x16x32_bf16 v[76:79], a[232:235], a[136:139], v[76:79]\n"
    "ds_read_b128 a[164:167], v242 offset:51200\n"
    "v_mfma_f32_16x16x32_bf16 v[76:79], a[236:239], a[168:171], v[76:79]\n"
    "ds_read_b128 a[132:135], v241 offset:51200\n"
    "v_mfma_f32_16x16x32_bf16 v[108:111], a[236:239], a[172:175], v[108:111]\n"
    "ds_read_b128 a[136:139], v241 offset:53248\n"
    "v_mfma_f32_16x16x32_bf16 v[108:111], a[232:235], a[140:143], v[108:111]\n"
    "ds_read_b128 a[168:171], v242 offset:53248\n"
    "v_mfma_f32_16x16x32_bf16 v[140:143], a[232:235], a[144:147], v[140:143]\n"
    "ds_read_b128 a[172:175], v242 offset:55296\n"
    "v_mfma_f32_16x16x32_bf16 v[140:143], a[236:239], a[176:179], v[140:143]\n"
    "ds_read_b128 a[140:143], v241 offset:55296\n"
    "v_mfma_f32_16x16x32_bf16 v[172:175], a[236:239], a[180:183], v[172:175]\n"
    "ds_read_b128 a[144:147], v241 offset:57344\n"
    "v_mfma_f32_16x16x32_bf16 v[172:175], a[232:235], a[148:151], v[172:175]\n"
    "ds_read_b128 a[176:179], v242 offset:57344\n"
    "v_mfma_f32_16x16x32_bf16 v[204:207], a[232:235], a[152:155], v[204:207]\n"
    "ds_read_b128 a[180:183], v242 offset:59392\n"
    "v_mfma_f32_16x16x32_bf16 v[204:207], a[236:239], a[184:187], v[204:207]\n"
    "ds_read_b128 a[148:151], v241 offset:59392\n"
    "v_mfma_f32_16x16x32_bf16 v[236:239], a[236:239], a[188:191], v[236:239]\n"
    "ds_read_b128 a[152:155], v241 offset:61440\n"
    "v_mfma_f32_16x16x32_bf16 v[236:239], a[232:235], a[156:159], v[236:239]\n"
    "s_waitcnt vmcnt(8) lgkmcnt(11)\n"
    "ds_read_b128 a[184:187], v242 offset:61440\n"
    "ds_read_b128 a[188:191], v242 offset:63488\n"
    "v_mfma_f32_16x16x32_bf16 v[0:3], a[192:195], a[128:131], v[0:3]\n"
    "ds_read_b128 a[156:159], v241 offset:63488\n"
    "v_mfma_f32_16x16x32_bf16 v[0:3], a[196:199], a[160:163], v[0:3]\n"
    "s_waitcnt lgkmcnt(12)\n"
    "v_mfma_f32_16x16x32_bf16 v[32:35], a[196:199], a[164:167], v[32:35]\n"
    "buffer_load_dwordx4 a[232:235], v240, s[20:23], s53 offen offset:2048 sc1\n"
    "v_mfma_f32_16x16x32_bf16 v[32:35], a[192:195], a[132:135], v[32:35]\n"
    "s_waitcnt lgkmcnt(10)\n"
    "v_mfma_f32_16x16x32_bf16 v[64:67], a[192:195], a[136:139], v[64:67]\n"
    "buffer_load_dwordx4 a[236:239], v240, s[20:23], s53 offen offset:3072 sc1\n"
    "s_addk_i32 s53, 4096\n"
    "v_mfma_f32_16x16x32_bf16 v[64:67], a[196:199], a[168:171], v[64:67]\n"
    "s_waitcnt lgkmcnt(8)\n"
    "v_mfma_f32_16x16x32_bf16 v[96:99], a[196:199], a[172:175], v[96:99]\n"
    "v_mfma_f32_16x16x32_bf16 v[96:99], a[192:195], a[140:143], v[96:99]\n"
    "s_waitcnt lgkmcnt(6)\n"
    "v_mfma_f32_16x16x32_bf16 v[128:131], a[192:195], a[144:147], v[128:131]\n"
    "v_mfma_f32_16x16x32_bf16 v[128:131], a[196:199], a[176:179], v[128:131]\n"
    "s_waitcnt lgkmcnt(4)\n"
    "v_mfma_f32_16x16x32_bf16 v[160:163], a[196:199], a[180:183], v[160:163]\n"
    "v_mfma_f32_16x16x32_bf16 v[160:163], a[192:195], a[148:151], v[160:163]\n"
    "s_waitcnt lgkmcnt(2)\n"
    "v_mfma_f32_16x16x32_bf16 v[192:195], a[192:195], a[152:155], v[192:195]\n"
    "v_mfma_f32_16x16x32_bf16 v[192:195], a[196:199], a[184:187], v[192:195]\n"
    "s_bitcmp1_b32 s85, 14\n"
    "s_cbranch_scc1 CP14_%=\n"
    "CR14_%=:\n"
    "s_waitcnt lgkmcnt(0)\n"
    "v_mfma_f32_16x16x32_bf16 v[224:227], a[196:199], a[188:191], v[224:227]\n"
    "v_mfma_f32_16x16x32_bf16 v[224:227], a[192:195], a[156:159], v[224:227]\n"
    "s_waitcnt vmcnt(8)\n"
    "v_mfma_f32_16x16x32_bf16 v[228:231], a[200:203], a[156:159], v[228:231]\n"
    "buffer_load_dwordx4 a[192:195], v240, s[20:23], s53 offen offset:0 sc1\n"
    "v_mfma_f32_16x16x32_bf16 v[228:231], a[204:207], a[188:191], v[228:231]\n"
    "buffer_load_dwordx4 a[196:199], v240, s[20:23], s53 offen offset:1024 sc1\n"
    "v_mfma_f32_16x16x32_bf16 v[196:199], a[204:207], a[184:187], v[196:199]\n"
    "s_add_u32 s60, %2, 131072\n"
    "v_mov_b32 v244, s60\n"
    "ds_read_b128 v[248:251], v244\n"
    "ds_read_b128 v[244:247], v244 offset:16\n"
    "v_mfma_f32_16x16x32_bf16 v[196:199], a[200:203], a[152:155], v[196:199]\n"
    "v_mfma_f32_16x16x32_bf16 v[164:167], a[200:203], a[148:151], v[164:167]\n"
    "v_mfma_f32_16x16x32_bf16 v[164:167], a[204:207], a[180:183], v[164:167]\n"
    "v_mfma_f32_16x16x32_bf16 v[132:135], a[204:207], a[176:179], v[132:135]\n"
    "v_mfma_f32_16x16x32_bf16 v[132:135], a[200:203], a[144:147], v[132:135]\n"
    "v_mfma_f32_16x16x32_bf16 v[100:103], a[200:203], a[140:143], v[100:103]\n"
    "v_mfma_f32_16x16x32_bf16 v[100:103], a[204:207], a[172:175], v[100:103]\n"
    "v_mfma_f32_16x16x32_bf16 v[68:71], a[204:207], a[168:171], v[68:71]\n"
    "v_mfma_f32_16x16x32_bf16 v[68:71], a[200:203], a[136:139], v[68:71]\n"
    "v_mfma_f32_16x16x32_bf16 v[36:39], a[200:203], a[132:135], v[36:39]\n"
    "v_mfma_f32_16x16x32_bf16 v[36:39], a[204:207], a[164:167], v[36:39]\n"
    "v_mfma_f32_16x16x32_bf16 v[4:7], a[204:207], a[160:163], v[4:7]\n"
    "v_mfma_f32_16x16x32_bf16 v[4:7], a[200:203], a[128:131], v[4:7]\n"
    "s_waitcnt vmcnt(8)\n"
    "v_mfma_f32_16x16x32_bf16 v[8:11], a[208:211], a[128:131], v[8:11]\n"
    "buffer_load_dwordx4 a[200:203], v240, s[20:23], s53 offen offset:2048 sc1\n"
    "v_mfma_f32_16x16x32_bf16 v[8:11], a[212:215], a[160:163], v[8:11]\n"
    "buffer_load_dwordx4 a[204:207], v240, s[20:23], s53 offen offset:3072 sc1\n"
    "v_mfma_f32_16x16x32_bf16 v[40:43], a[212:215], a[164:167], v[40:43]\n"
    "s_addk_i32 s53, 4096\n"
    "s_waitcnt lgkmcnt(0)\n"
    "v_add_u32 v248, v248, v250\n"
    "v_add_u32 v249, v249, v251\n"
    "v_add_u32 v244, v244, v246\n"
    "v_add_u32 v245, v245, v247\n"
    "v_readfirstlane_b32 s72, v248\n"
    "v_readfirstlane_b32 s73, v249\n"
    "v_readfirstlane_b32 s74, v244\n"
    "v_readfirstlane_b32 s75, v245\n"
    "v_mfma_f32_16x16x32_bf16 v[40:43], a[208:211], a[132:135], v[40:43]\n"
    "s_add_u32 s60, s72, s74\n"
    "s_add_u32 s61, s73, s75\n"
    "s_cmp_lt_u32 s61, s60\n"
    "s_cselect_b32 s64, 1, 0\n"
    "s_cselect_b32 s72, s73, s72\n"
    "s_cselect_b32 s74, s75, s74\n"
    "s_lshl_b32 s62, s64, 13\n"
    "s_xor_b32 s54, s54, s62\n"
    "s_sub_i32 s73, s72, s74\n"
    "s_sub_i32 s75, s74, s72\n"
    "s_bitcmp1_b32 %3, 0\n"
    "s_cselect_b32 s73, s75, s73\n"
    "s_lshl_b32 s75, s73, 1\n"
    "s_addk_i32 s75, 4097\n"
    "s_sub_i32 s72, s60, s61\n"
    "s_min_i32 s72, s72, 255\n"
    "s_max_i32 s72, s72, -255\n"
    "s_addk_i32 s72, 256\n"
    "s_lshl_b32 s72, s72, 20\n"
    "s_or_b32 s75, s75, s72\n"
    "v_mov_b32 v248, s75\n"
    "s_add_u32 s26, s77, 4\n"
    "s_and_b32 s26, s26, s86\n"
    "v_mfma_f32_16x16x32_bf16 v[72:75], a[208:211], a[136:139], v[72:75]\n"
    "buffer_store_dword v248, v240, s[24:27], s77 offen sc1\n"
    "s_movk_i32 s26, 1024\n"
    "v_mfma_f32_16x16x32_bf16 v[72:75], a[212:215], a[168:171], v[72:75]\n"
    "v_mfma_f32_16x16x32_bf16 v[104:107], a[212:215], a[172:175], v[104:107]\n"
    "v_mfma_f32_16x16x32_bf16 v[104:107], a[208:211], a[140:143], v[104:107]\n"
    "v_mfma_f32_16x16x32_bf16 v[136:139], a[208:211], a[144:147], v[136:139]\n"
    "v_mfma_f32_16x16x32_bf16 v[136:139], a[212:215], a[176:179], v[136:139]\n"
    "v_mfma_f32_16x16x32_bf16 v[168:171], a[212:215], a[180:183], v[168:171]\n"
    "v_mfma_f32_16x16x32_bf16 v[168:171], a[208:211], a[148:151], v[168:171]\n"
    "v_mfma_f32_16x16x32_bf16 v[200:203], a[208:211], a[152:155], v[200:203]\n"
    "v_mfma_f32_16x16x32_bf16 v[200:203], a[212:215], a[184:187], v[200:203]\n"
    "s_barrier\n"
    "v_mfma_f32_16x16x32_bf16 v[232:235], a[212:215], a[188:191], v[232:235]\n"
    "v_mfma_f32_16x16x32_bf16 v[232:235], a[208:211], a[156:159], v[232:235]\n"
    "s_waitcnt vmcnt(9)\n"
    "v_mfma_f32_16x16x32_bf16 v[236:239], a[216:219], a[156:159], v[236:239]\n"
    "buffer_load_dwordx4 a[208:211], v240, s[20:23], s53 offen offset:0 sc1\n"
    "v_mfma_f32_16x16x32_bf16 v[236:239], a[220:223], a[188:191], v[236:239]\n"
    "buffer_load_dwordx4 a[212:215], v240, s[20:23], s53 offen offset:1024 sc1\n"
    "v_mfma_f32_16x16x32_bf16 v[204:207], a[220:223], a[184:187], v[204:207]\n"
    "ds_read_b128 a[156:159], v241 offset:47104\n"
    "v_mfma_f32_16x16x32_bf16 v[204:207], a[216:219], a[152:155], v[204:207]\n"
    "ds_read_b128 a[188:191], v242 offset:47104\n"
    "v_mfma_f32_16x16x32_bf16 v[172:175], a[216:219], a[148:151], v[172:175]\n"
    "ds_read_b128 a[184:187], v242 offset:45056\n"
    "v_mfma_f32_16x16x32_bf16 v[172:175], a[220:223], a[180:183], v[172:175]\n"
    "ds_read_b128 a[152:155], v241 offset:45056\n"
    "v_mfma_f32_16x16x32_bf16 v[140:143], a[220:223], a[176:179], v[140:143]\n"
    "ds_read_b128 a[148:151], v241 offset:43008\n"
    "v_mfma_f32_16x16x32_bf16 v[140:143], a[216:219], a[144:147], v[140:143]\n"
    "ds_read_b128 a[180:183], v242 offset:43008\n"
    "v_mfma_f32_16x16x32_bf16 v[108:111], a[216:219], a[140:143], v[108:111]\n"
    "ds_read_b128 a[176:179], v242 offset:40960\n"
    "v_mfma_f32_16x16x32_bf16 v[108:111], a[220:223], a[172:175], v[108:111]\n"
    "ds_read_b128 a[144:147], v241 offset:40960\n"
    "v_mfma_f32_16x16x32_bf16 v[76:79], a[220:223], a[168:171], v[76:79]\n"
    "ds_read_b128 a[140:143], v241 offset:38912\n"
    "v_mfma_f32_16x16x32_bf16 v[76:79], a[216:219], a[136:139], v[76:79]\n"
    "ds_read_b128 a[172:175], v242 offset:38912\n"
    "v_mfma_f32_16x16x32_bf16 v[44:47], a[216:219], a[132:135], v[44:47]\n"
    "ds_read_b128 a[168:171], v242 offset:36864\n"
    "v_mfma_f32_16x16x32_bf16 v[44:47], a[220:223], a[164:167], v[44:47]\n"
    "ds_read_b128 a[136:139], v241 offset:36864\n"
    "v_mfma_f32_16x16x32_bf16 v[12:15], a[220:223], a[160:163], v[12:15]\n"
    "ds_read_b128 a[132:135], v241 offset:34816\n"
    "v_mfma_f32_16x16x32_bf16 v[12:15], a[216:219], a[128:131], v[12:15]\n"
    "s_waitcnt vmcnt(9) lgkmcnt(11)\n"
    "ds_read_b128 a[164:167], v242 offset:34816\n"
    "ds_read_b128 a[160:163], v242 offset:32768\n"
    "v_mfma_f32_16x16x32_bf16 v[224:227], a[224:227], a[156:159], v[224:227]\n"
    "ds_read_b128 a[128:131], v241 offset:32768\n"
    "v_mfma_f32_16x16x32_bf16 v[224:227], a[228:231], a[188:191], v[224:227]\n"
    "s_waitcnt lgkmcnt(12)\n"
    "v_mfma_f32_16x16x32_bf16 v[192:195], a[228:231], a[184:187], v[192:195]\n"
    "buffer_load_dwordx4 a[216:219], v240, s[20:23], s53 offen offset:2048 sc1\n"
    "v_mfma_f32_16x16x32_bf16 v[192:195], a[224:227], a[152:155], v[192:195]\n"
    "s_waitcnt lgkmcnt(10)\n"
    "v_mfma_f32_16x16x32_bf16 v[160:163], a[224:227], a[148:151], v[160:163]\n"
    "buffer_load_dwordx4 a[220:223], v240, s[20:23], s53 offen offset:3072 sc1\n"
    "s_addk_i32 s53, 4096\n"
    "v_mfma_f32_16x16x32_bf16 v[160:163], a[228:231], a[180:183], v[160:163]\n"
    "s_waitcnt lgkmcnt(8)\n"
    "v_mfma_f32_16x16x32_bf16 v[128:131], a[228:231], a[176:179], v[128:131]\n"
    "v_mfma_f32_16x16x32_bf16 v[128:131], a[224:227], a[144:147], v[128:131]\n"
    "s_waitcnt lgkmcnt(6)\n"
    "v_mfma_f32_16x16x32_bf16 v[96:99], a[224:227], a[140:143], v[96:99]\n"
    "v_mfma_f32_16x16x32_bf16 v[96:99], a[228:231], a[172:175], v[96:99]\n"
    "s_waitcnt lgkmcnt(4)\n"
    "v_mfma_f32_16x16x32_bf16 v[64:67], a[228:231], a[168:171], v[64:67]\n"
    "v_mfma_f32_16x16x32_bf16 v[64:67], a[224:227], a[136:139], v[64:67]\n"
    "s_waitcnt lgkmcnt(2)\n"
    "v_mfma_f32_16x16x32_bf16 v[32:35], a[224:227], a[132:135], v[32:35]\n"
    "v_mfma_f32_16x16x32_bf16 v[32:35], a[228:231], a[164:167], v[32:35]\n"
    "s_bitcmp1_b32 s85, 15\n"
    "s_cbranch_scc1 CP15_%=\n"
    "CR15_%=:\n"
    "s_waitcnt lgkmcnt(0)\n"
    "v_mfma_f32_16x16x32_bf16 v[0:3], a[228:231], a[160:163], v[0:3]\n"
    "v_mfma_f32_16x16x32_bf16 v[0:3], a[224:227], a[128:131], v[0:3]\n"
    "s_waitcnt vmcnt(9)\n"
    "v_mfma_f32_16x16x32_bf16 v[4:7], a[232:235], a[128:131], v[4:7]\n"
    "buffer_load_dwordx4 a[224:227], v240, s[20:23], s53 offen offset:0 sc1\n"
    "v_mfma_f32_16x16x32_bf16 v[4:7], a[236:239], a[160:163], v[4:7]\n"
    "buffer_load_dwordx4 a[228:231], v240, s[20:23], s53 offen offset:1024 sc1\n"
    "v_mfma_f32_16x16x32_bf16 v[36:39], a[236:239], a[164:167], v[36:39]\n"
    "v_mfma_f32_16x16x32_bf16 v[36:39], a[232:235], a[132:135], v[36:39]\n"
    "v_mfma_f32_16x16x32_bf16 v[68:71], a[232:235], a[136:139], v[68:71]\n"
    "v_mfma_f32_16x16x32_bf16 v[68:71], a[236:239], a[168:171], v[68:71]\n"
    "v_mfma_f32_16x16x32_bf16 v[100:103], a[236:239], a[172:175], v[100:103]\n"
    "v_mfma_f32_16x16x32_bf16 v[100:103], a[232:235], a[140:143], v[100:103]\n"
    "v_mfma_f32_16x16x32_bf16 v[132:135], a[232:235], a[144:147], v[132:135]\n"
    "v_mfma_f32_16x16x32_bf16 v[132:135], a[236:239], a[176:179], v[132:135]\n"
    "v_mfma_f32_16x16x32_bf16 v[164:167], a[236:239], a[180:183], v[164:167]\n"
    "v_accvgpr_read_b32 v245, a0\n"
    "v_accvgpr_read_b32 v247, a1\n"
    "v_mfma_f32_16x16x32_bf16 v[164:167], a[232:235], a[148:151], v[164:167]\n"
    "v_lshlrev_b32 v244, 16, v245\n"
    "v_lshlrev_b32 v246, 16, v247\n"
    "v_mfma_f32_16x16x32_bf16 v[196:199], a[232:235], a[152:155], v[196:199]\n"
    "v_and_b32 v245, s58, v245\n"
    "v_and_b32 v247, s58, v247\n"
    "v_mfma_f32_16x16x32_bf16 v[196:199], a[236:239], a[184:187], v[196:199]\n"
    "v_add_f32_dpp v254, v253, v0 row_newbcast:0 row_mask:0xf bank_mask:0xf\n"
    "v_add_f32_dpp v255, v253, v2 row_newbcast:2 row_mask:0xf bank_mask:0xf\n"
    "v_mfma_f32_16x16x32_bf16 v[228:231], a[236:239], a[188:191], v[228:231]\n"
    "v_mul_f32 v244, v244, v254\n"
    "v_mul_f32 v246, v246, v255\n"
    "v_mfma_f32_16x16x32_bf16 v[228:231], a[232:235], a[156:159], v[228:231]\n"
    "s_waitcnt vmcnt(9)\n"
    "v_add_f32_dpp v254, v253, v1 row_newbcast:1 row_mask:0xf bank_mask:0xf\n"
    "v_add_f32_dpp v255, v253, v3 row_newbcast:3 row_mask:0xf bank_mask:0xf\n"
    "v_mfma_f32_16x16x32_bf16 v[232:235], a[192:195], a[156:159], v[232:235]\n"
    "buffer_load_dwordx4 a[232:235], v240, s[20:23], s53 offen offset:2048 sc1\n"
    "v_mfma_f32_16x16x32_bf16 v[232:235], a[196:199], a[188:191], v[232:235]\n"
    "buffer_load_dwordx4 a[236:239], v240, s[20:23], s53 offen offset:3072 sc1\n"
    "v_mfma_f32_16x16x32_bf16 v[200:203], a[196:199], a[184:187], v[200:203]\n"
    "s_addk_i32 s53, 4096\n"
    "v_mul_f32 v245, v245, v254\n"
    "v_mul_f32 v247, v247, v255\n"
    "v_mfma_f32_16x16x32_bf16 v[200:203], a[192:195], a[152:155], v[200:203]\n"
    "v_cvt_pk_bf16_f32 v248, v244, v245\n"
    "v_cvt_pk_bf16_f32 v249, v246, v247\n"
    "v_mfma_f32_16x16x32_bf16 v[168:171], a[192:195], a[148:151], v[168:171]\n"
    "v_accvgpr_read_b32 v245, a2\n"
    "v_accvgpr_read_b32 v247, a3\n"
    "v_mfma_f32_16x16x32_bf16 v[168:171], a[196:199], a[180:183], v[168:171]\n"
    "v_lshlrev_b32 v244, 16, v245\n"
    "v_lshlrev_b32 v246, 16, v247\n"
    "v_mfma_f32_16x16x32_bf16 v[136:139], a[196:199], a[176:179], v[136:139]\n"
    "v_and_b32 v245, s58, v245\n"
    "v_and_b32 v247, s58, v247\n"
    "v_mfma_f32_16x16x32_bf16 v[136:139], a[192:195], a[144:147], v[136:139]\n"
    "s_xor_b32 s91, s54, 0x40000\n"
    "v_add_f32_dpp v254, v253, v4 row_newbcast:4 row_mask:0xf bank_mask:0xf\n"
    "v_add_f32_dpp v255, v253, v6 row_newbcast:6 row_mask:0xf bank_mask:0xf\n"
    "v_mfma_f32_16x16x32_bf16 v[104:107], a[192:195], a[140:143], v[104:107]\n"
    "v_mul_f32 v244, v244, v254\n"
    "v_mul_f32 v246, v246, v255\n"
    "v_mfma_f32_16x16x32_bf16 v[104:107], a[196:199], a[172:175], v[104:107]\n"
    "v_add_f32_dpp v254, v253, v5 row_newbcast:5 row_mask:0xf bank_mask:0xf\n"
    "v_add_f32_dpp v255, v253, v7 row_newbcast:7 row_mask:0xf bank_mask:0xf\n"
    "v_mfma_f32_16x16x32_bf16 v[72:75], a[196:199], a[168:171], v[72:75]\n"
    "v_mul_f32 v245, v245, v254\n"
    "v_mul_f32 v247, v247, v255\n"
    "v_mfma_f32_16x16x32_bf16 v[72:75], a[192:195], a[136:139], v[72:75]\n"
    "v_cvt_pk_bf16_f32 v250, v244, v245\n"
    "v_cvt_pk_bf16_f32 v251, v246, v247\n"
    "v_mfma_f32_16x16x32_bf16 v[40:43], a[192:195], a[132:135], v[40:43]\n"
    "v_accvgpr_read_b32 v245, a4\n"
    "v_accvgpr_read_b32 v247, a5\n"
    "v_mfma_f32_16x16x32_bf16 v[40:43], a[196:199], a[164:167], v[40:43]\n"
    "v_lshlrev_b32 v244, 16, v245\n"
    "v_lshlrev_b32 v246, 16, v247\n"
    "v_mfma_f32_16x16x32_bf16 v[8:11], a[196:199], a[160:163], v[8:11]\n"
    "v_and_b32 v245, s58, v245\n"
    "v_and_b32 v247, s58, v247\n"
    "v_mfma_f32_16x16x32_bf16 v[8:11], a[192:195], a[128:131], v[8:11]\n"
    "s_waitcnt vmcnt(9)\n"
    "v_add_f32_dpp v254, v253, v32 row_newbcast:0 row_mask:0xf bank_mask:0xf\n"
    "v_add_f32_dpp v255, v253, v34 row_newbcast:2 row_mask:0xf bank_mask:0xf\n"
    "v_mfma_f32_16x16x32_bf16 v[12:15], a[200:203], a[128:131], v[12:15]\n"
    "buffer_load_dwordx4 a[192:195], v240, s[20:23], s53 offen offset:0 sc1\n"
    "v_mfma_f32_16x16x32_bf16 v[12:15], a[204:207], a[160:163], v[12:15]\n"
    "buffer_load_dwordx4 a[196:199], v240, s[20:23], s53 offen offset:1024 sc1\n"
    "v_mfma_f32_16x16x32_bf16 v[44:47], a[204:207], a[164:167], v[44:47]\n"
    "v_mul_f32 v244, v244, v254\n"
    "v_mul_f32 v246, v246, v255\n"
    "v_mfma_f32_16x16x32_bf16 v[44:47], a[200:203], a[132:135], v[44:47]\n"
    "v_add_f32_dpp v254, v253, v33 row_newbcast:1 row_mask:0xf bank_mask:0xf\n"
    "v_add_f32_dpp v255, v253, v35 row_newbcast:3 row_mask:0xf bank_mask:0xf\n"
    "v_mfma_f32_16x16x32_bf16 v[76:79], a[200:203], a[136:139], v[76:79]\n"
    "v_mul_f32 v245, v245, v254\n"
    "v_mul_f32 v247, v247, v255\n"
    "v_mfma_f32_16x16x32_bf16 v[76:79], a[204:207], a[168:171], v[76:79]\n"
    "buffer_store_dwordx4 v[248:251], v240, s[32:35], s91 offen sc1\n"
    "ds_write_b128 v243, v[248:251] offset:0\n"
    "v_mfma_f32_16x16x32_bf16 v[108:111], a[204:207], a[172:175], v[108:111]\n"
    "v_cvt_pk_bf16_f32 v248, v244, v245\n"
    "v_cvt_pk_bf16_f32 v249, v246, v247\n"
    "v_mfma_f32_16x16x32_bf16 v[108:111], a[200:203], a[140:143], v[108:111]\n"
    "v_accvgpr_read_b32 v245, a6\n"
    "v_accvgpr_read_b32 v247, a7\n"
    "v_mfma_f32_16x16x32_bf16 v[140:143], a[200:203], a[144:147], v[140:143]\n"
    "v_lshlrev_b32 v244, 16, v245\n"
    "v_lshlrev_b32 v246, 16, v247\n"
    "v_mfma_f32_16x16x32_bf16 v[140:143], a[204:207], a[176:179], v[140:143]\n"
    "v_and_b32 v245, s58, v245\n"
    "v_and_b32 v247, s58, v247\n"
    "v_mfma_f32_16x16x32_bf16 v[172:175], a[204:207], a[180:183], v[172:175]\n"
    "s_xor_b32 s91, s91, 0x800\n"
    "v_add_f32_dpp v254, v253, v36 row_newbcast:4 row_mask:0xf bank_mask:0xf\n"
    "v_add_f32_dpp v255, v253, v38 row_newbcast:6 row_mask:0xf bank_mask:0xf\n"
    "v_mfma_f32_16x16x32_bf16 v[172:175], a[200:203], a[148:151], v[172:175]\n"
    "v_mul_f32 v244, v244, v254\n"
    "v_mul_f32 v246, v246, v255\n"
    "v_mfma_f32_16x16x32_bf16 v[204:207], a[200:203], a[152:155], v[204:207]\n"
    "v_add_f32_dpp v254, v253, v37 row_newbcast:5 row_mask:0xf bank_mask:0xf\n"
    "v_add_f32_dpp v255, v253, v39 row_newbcast:7 row_mask:0xf bank_mask:0xf\n"
    "v_mfma_f32_16x16x32_bf16 v[204:207], a[204:207], a[184:187], v[204:207]\n"
    "v_mul_f32 v245, v245, v254\n"
    "v_mul_f32 v247, v247, v255\n"
    "v_mfma_f32_16x16x32_bf16 v[236:239], a[204:207], a[188:191], v[236:239]\n"
    "v_cvt_pk_bf16_f32 v250, v244, v245\n"
    "v_cvt_pk_bf16_f32 v251, v246, v247\n"
    "v_mfma_f32_16x16x32_bf16 v[236:239], a[200:203], a[156:159], v[236:239]\n"
    "s_waitcnt vmcnt(9)\n"
    "v_accvgpr_read_b32 v245, a8\n"
    "v_accvgpr_read_b32 v247, a9\n"
    "v_mfma_f32_16x16x32_bf16 a[240:243], a[208:211], a[156:159], a[240:243]\n"
    "buffer_load_dwordx4 a[200:203], v240, s[20:23], s53 offen offset:2048 sc1\n"
    "v_mfma_f32_16x16x32_bf16 a[240:243], a[212:215], a[188:191], a[240:243]\n"
    "buffer_load_dwordx4 a[204:207], v240, s[20:23], s53 offen offset:3072 sc1\n"
    "v_mfma_f32_16x16x32_bf16 v[208:211], a[212:215], a[184:187], v[208:211]\n"
    "s_addk_i32 s53, 4096\n"
    "v_lshlrev_b32 v244, 16, v245\n"
    "v_lshlrev_b32 v246, 16, v247\n"
    "v_mfma_f32_16x16x32_bf16 v[208:211], a[208:211], a[152:155], v[208:211]\n"
    "v_and_b32 v245, s58, v245\n"
    "v_and_b32 v247, s58, v247\n"
    "v_mfma_f32_16x16x32_bf16 v[176:179], a[208:211], a[148:151], v[176:179]\n"
    "v_add_f32_dpp v254, v253, v64 row_newbcast:0 row_mask:0xf bank_mask:0xf\n"
    "v_add_f32_dpp v255, v253, v66 row_newbcast:2 row_mask:0xf bank_mask:0xf\n"
    "v_mfma_f32_16x16x32_bf16 v[176:179], a[212:215], a[180:183], v[176:179]\n"
    "v_mul_f32 v244, v244, v254\n"
    "v_mul_f32 v246, v246, v255\n"
    "v_mfma_f32_16x16x32_bf16 v[144:147], a[212:215], a[176:179], v[144:147]\n"
    "v_add_f32_dpp v254, v253, v65 row_newbcast:1 row_mask:0xf bank_mask:0xf\n"
    "v_add_f32_dpp v255, v253, v67 row_newbcast:3 row_mask:0xf bank_mask:0xf\n"
    "v_mfma_f32_16x16x32_bf16 v[144:147], a[208:211], a[144:147], v[144:147]\n"
    "v_mul_f32 v245, v245, v254\n"
    "v_mul_f32 v247, v247, v255\n"
    "v_mfma_f32_16x16x32_bf16 v[112:115], a[208:211], a[140:143], v[112:115]\n"
    "v_mfma_f32_16x16x32_bf16 v[112:115], a[212:215], a[172:175], v[112:115]\n"
    "buffer_store_dwordx4 v[248:251], v240, s[32:35], s91 offen sc1\n"
    "ds_write_b128 v243, v[248:251] offset:2048\n"
    "v_mfma_f32_16x16x32_bf16 v[80:83], a[212:215], a[168:171], v[80:83]\n"
    "v_cvt_pk_bf16_f32 v248, v244, v245\n"
    "v_cvt_pk_bf16_f32 v249, v246, v247\n"
    "v_mfma_f32_16x16x32_bf16 v[80:83], a[208:211], a[136:139], v[80:83]\n"
    "v_accvgpr_read_b32 v245, a10\n"
    "v_accvgpr_read_b32 v247, a11\n"
    "v_mfma_f32_16x16x32_bf16 v[48:51], a[208:211], a[132:135], v[48:51]\n"
    "v_lshlrev_b32 v244, 16, v245\n"
    "v_lshlrev_b32 v246, 16, v247\n"
    "v_mfma_f32_16x16x32_bf16 v[48:51], a[212:215], a[164:167], v[48:51]\n"
    "v_and_b32 v245, s58, v245\n"
    "v_and_b32 v247, s58, v247\n"
    "v_mfma_f32_16x16x32_bf16 v[16:19], a[212:215], a[160:163], v[16:19]\n"
    "s_xor_b32 s91, s91, 0x101800\n"
    "v_add_f32_dpp v254, v253, v68 row_newbcast:4 row_mask:0xf bank_mask:0xf\n"
    "v_add_f32_dpp v255, v253, v70 row_newbcast:6 row_mask:0xf bank_mask:0xf\n"
    "v_mfma_f32_16x16x32_bf16 v[16:19], a[208:211], a[128:131], v[16:19]\n"
    "s_waitcnt vmcnt(10)\n"
    "v_mul_f32 v244, v244, v254\n"
    "v_mul_f32 v246, v246, v255\n"
    "v_mfma_f32_16x16x32_bf16 v[20:23], a[216:219], a[128:131], v[20:23]\n"
    "buffer_load_dwordx4 a[208:211], v240, s[20:23], s53 offen offset:0 sc1\n"
    "v_mfma_f32_16x16x32_bf16 v[20:23], a[220:223], a[160:163], v[20:23]\n"
    "buffer_load_dwordx4 a[212:215], v240, s[20:23], s53 offen offset:1024 sc1\n"
    "v_mfma_f32_16x16x32_bf16 v[52:55], a[220:223], a[164:167], v[52:55]\n"
    "v_add_f32_dpp v254, v253, v69 row_newbcast:5 row_mask:0xf bank_mask:0xf\n"
    "v_add_f32_dpp v255, v253, v71 row_newbcast:7 row_mask:0xf bank_mask:0xf\n"
    "v_mfma_f32_16x16x32_bf16 v[52:55], a[216:219], a[132:135], v[52:55]\n"
    "v_mul_f32 v245, v245, v254\n"
    "v_mul_f32 v247, v247, v255\n"
    "v_mfma_f32_16x16x32_bf16 v[84:87], a[216:219], a[136:139], v[84:87]\n"
    "v_cvt_pk_bf16_f32 v250, v244, v245\n"
    "v_cvt_pk_bf16_f32 v251, v246, v247\n"
    "v_mfma_f32_16x16x32_bf16 v[84:87], a[220:223], a[168:171], v[84:87]\n"
    "v_accvgpr_read_b32 v245, a12\n"
    "v_accvgpr_read_b32 v247, a13\n"
    "v_mfma_f32_16x16x32_bf16 v[116:119], a[220:223], a[172:175], v[116:119]\n"
    "v_lshlrev_b32 v244, 16, v245\n"
    "v_lshlrev_b32 v246, 16, v247\n"
    "v_mfma_f32_16x16x32_bf16 v[116:119], a[216:219], a[140:143], v[116:119]\n"
    "v_and_b32 v245, s58, v245\n"
    "v_and_b32 v247, s58, v247\n"
    "v_mfma_f32_16x16x32_bf16 v[148:151], a[216:219], a[144:147], v[148:151]\n"
    "v_add_f32_dpp v254, v253, v96 row_newbcast:0 row_mask:0xf bank_mask:0xf\n"
    "v_add_f32_dpp v255, v253, v98 row_newbcast:2 row_mask:0xf bank_mask:0xf\n"
    "v_mfma_f32_16x16x32_bf16 v[148:151], a[220:223], a[176:179], v[148:151]\n"
    "v_mul_f32 v244, v244, v254\n"
    "v_mul_f32 v246, v246, v255\n"
    "v_mfma_f32_16x16x32_bf16 v[180:183], a[220:223], a[180:183], v[180:183]\n"
    "v_add_f32_dpp v254, v253, v97 row_newbcast:1 row_mask:0xf bank_mask:0xf\n"
    "v_add_f32_dpp v255, v253, v99 row_newbcast:3 row_mask:0xf bank_mask:0xf\n"
    "v_mfma_f32_16x16x32_bf16 v[180:183], a[216:219], a[148:151], v[180:183]\n"
    "v_mul_f32 v245, v245, v254\n"
    "v_mul_f32 v247, v247, v255\n"
    "v_mfma_f32_16x16x32_bf16 v[212:215], a[216:219], a[152:155], v[212:215]\n"
    "v_mfma_f32_16x16x32_bf16 v[212:215], a[220:223], a[184:187], v[212:215]\n"
    "buffer_store_dwordx4 v[248:251], v240, s[32:35], s91 offen sc1\n"
    "ds_write_b128 v243, v[248:251] offset:4096\n"
    "v_mfma_f32_16x16x32_bf16 a[244:247], a[220:223], a[188:191], a[244:247]\n"
    "v_cvt_pk_bf16_f32 v248, v244, v245\n"
    "v_cvt_pk_bf16_f32 v249, v246, v247\n"
    "v_mfma_f32_16x16x32_bf16 a[244:247], a[216:219], a[156:159], a[244:247]\n"
    "s_waitcnt vmcnt(11)\n"
    "v_accvgpr_read_b32 v245, a14\n"
    "v_accvgpr_read_b32 v247, a15\n"
    "v_mfma_f32_16x16x32_bf16 a[248:251], a[224:227], a[156:159], a[248:251]\n"
    "buffer_load_dwordx4 a[216:219], v240, s[20:23], s53 offen offset:2048 sc1\n"
    "v_mfma_f32_16x16x32_bf16 a[248:251], a[228:231], a[188:191], a[248:251]\n"
    "buffer_load_dwordx4 a[220:223], v240, s[20:23], s53 offen offset:3072 sc1\n"
    "v_mfma_f32_16x16x32_bf16 v[216:219], a[228:231], a[184:187], v[216:219]\n"
    "s_addk_i32 s53, 4096\n"
    "v_lshlrev_b32 v244, 16, v245\n"
    "v_lshlrev_b32 v246, 16, v247\n"
    "v_mfma_f32_16x16x32_bf16 v[216:219], a[224:227], a[152:155], v[216:219]\n"
    "v_and_b32 v245, s58, v245\n"
    "v_and_b32 v247, s58, v247\n"
    "v_mfma_f32_16x16x32_bf16 v[184:187], a[224:227], a[148:151], v[184:187]\n"
    "s_xor_b32 s91, s91, 0x800\n"
    "v_add_f32_dpp v254, v253, v100 row_newbcast:4 row_mask:0xf bank_mask:0xf\n"
    "v_add_f32_dpp v255, v253, v102 row_newbcast:6 row_mask:0xf bank_mask:0xf\n"
    "v_mfma_f32_16x16x32_bf16 v[184:187], a[228:231], a[180:183], v[184:187]\n"
    "v_mul_f32 v244, v244, v254\n"
    "v_mul_f32 v246, v246, v255\n"
    "v_mfma_f32_16x16x32_bf16 v[152:155], a[228:231], a[176:179], v[152:155]\n"
    "v_add_f32_dpp v254, v253, v101 row_newbcast:5 row_mask:0xf bank_mask:0xf\n"
    "v_add_f32_dpp v255, v253, v103 row_newbcast:7 row_mask:0xf bank_mask:0xf\n"
    "v_mfma_f32_16x16x32_bf16 v[152:155], a[224:227], a[144:147], v[152:155]\n"
    "v_mul_f32 v245, v245, v254\n"
    "v_mul_f32 v247, v247, v255\n"
    "v_mfma_f32_16x16x32_bf16 v[120:123], a[224:227], a[140:143], v[120:123]\n"
    "v_cvt_pk_bf16_f32 v250, v244, v245\n"
    "v_cvt_pk_bf16_f32 v251, v246, v247\n"
    "v_mfma_f32_16x16x32_bf16 v[120:123], a[228:231], a[172:175], v[120:123]\n"
    "v_accvgpr_read_b32 v245, a16\n"
    "v_accvgpr_read_b32 v247, a17\n"
    "v_mfma_f32_16x16x32_bf16 v[88:91], a[228:231], a[168:171], v[88:91]\n"
    "v_lshlrev_b32 v244, 16, v245\n"
    "v_lshlrev_b32 v246, 16, v247\n"
    "v_mfma_f32_16x16x32_bf16 v[88:91], a[224:227], a[136:139], v[88:91]\n"
    "v_and_b32 v245, s58, v245\n"
    "v_and_b32 v247, s58, v247\n"
    "v_mfma_f32_16x16x32_bf16 v[56:59], a[224:227], a[132:135], v[56:59]\n"
    "v_add_f32_dpp v254, v253, v128 row_newbcast:0 row_mask:0xf bank_mask:0xf\n"
    "v_add_f32_dpp v255, v253, v130 row_newbcast:2 row_mask:0xf bank_mask:0xf\n"
    "v_mfma_f32_16x16x32_bf16 v[56:59], a[228:231], a[164:167], v[56:59]\n"
    "v_mul_f32 v244, v244, v254\n"
    "v_mul_f32 v246, v246, v255\n"
    "v_mfma_f32_16x16x32_bf16 v[24:27], a[228:231], a[160:163], v[24:27]\n"
    "v_add_f32_dpp v254, v253, v129 row_newbcast:1 row_mask:0xf bank_mask:0xf\n"
    "v_add_f32_dpp v255, v253, v131 row_newbcast:3 row_mask:0xf bank_mask:0xf\n"
    "v_mfma_f32_16x16x32_bf16 v[24:27], a[224:227], a[128:131], v[24:27]\n"
    "s_waitcnt vmcnt(11)\n"
    "v_mul_f32 v245, v245, v254\n"
    "v_mul_f32 v247, v247, v255\n"
    "v_mfma_f32_16x16x32_bf16 v[28:31], a[232:235], a[128:131], v[28:31]\n"
    "buffer_load_dwordx4 a[224:227], v240, s[20:23], s53 offen offset:0 sc1\n"
    "v_mfma_f32_16x16x32_bf16 v[28:31], a[236:239], a[160:163], v[28:31]\n"
    "buffer_load_dwordx4 a[228:231], v240, s[20:23], s53 offen offset:1024 sc1\n"
    "v_mfma_f32_16x16x32_bf16 v[60:63], a[236:239], a[164:167], v[60:63]\n"
    "ds_read_b128 a[128:131], v241 offset:49152\n"
    "v_mfma_f32_16x16x32_bf16 v[60:63], a[232:235], a[132:135], v[60:63]\n"
    "buffer_store_dwordx4 v[248:251], v240, s[32:35], s91 offen sc1\n"
    "ds_write_b128 v243, v[248:251] offset:6144\n"
    "v_mfma_f32_16x16x32_bf16 v[92:95], a[232:235], a[136:139], v[92:95]\n"
    "ds_read_b128 a[160:163], v242 offset:49152\n"
    "ds_read_b128 a[164:167], v242 offset:51200\n"
    "v_mfma_f32_16x16x32_bf16 v[92:95], a[236:239], a[168:171], v[92:95]\n"
    "ds_read_b128 a[132:135], v241 offset:51200\n"
    "v_cvt_pk_bf16_f32 v248, v244, v245\n"
    "v_mfma_f32_16x16x32_bf16 v[124:127], a[236:239], a[172:175], v[124:127]\n"
    "ds_read_b128 a[136:139], v241 offset:53248\n"
    "v_cvt_pk_bf16_f32 v249, v246, v247\n"
    "v_mfma_f32_16x16x32_bf16 v[124:127], a[232:235], a[140:143], v[124:127]\n"
    "ds_read_b128 a[168:171], v242 offset:53248\n"
    "v_accvgpr_read_b32 v245, a18\n"
    "v_mfma_f32_16x16x32_bf16 v[156:159], a[232:235], a[144:147], v[156:159]\n"
    "ds_read_b128 a[172:175], v242 offset:55296\n"
    "v_accvgpr_read_b32 v247, a19\n"
    "v_mfma_f32_16x16x32_bf16 v[156:159], a[236:239], a[176:179], v[156:159]\n"
    "ds_read_b128 a[140:143], v241 offset:55296\n"
    "v_lshlrev_b32 v244, 16, v245\n"
    "v_mfma_f32_16x16x32_bf16 v[188:191], a[236:239], a[180:183], v[188:191]\n"
    "ds_read_b128 a[144:147], v241 offset:57344\n"
    "v_lshlrev_b32 v246, 16, v247\n"
    "v_mfma_f32_16x16x32_bf16 v[188:191], a[232:235], a[148:151], v[188:191]\n"
    "ds_read_b128 a[176:179], v242 offset:57344\n"
    "v_and_b32 v245, s58, v245\n"
    "v_mfma_f32_16x16x32_bf16 v[220:223], a[232:235], a[152:155], v[220:223]\n"
    "ds_read_b128 a[180:183], v242 offset:59392\n"
    "v_and_b32 v247, s58, v247\n"
    "v_mfma_f32_16x16x32_bf16 v[220:223], a[236:239], a[184:187], v[220:223]\n"
    "ds_read_b128 a[148:151], v241 offset:59392\n"
    "v_add_f32_dpp v254, v253, v132 row_newbcast:4 row_mask:0xf bank_mask:0xf\n"
    "v_mfma_f32_16x16x32_bf16 a[252:255], a[236:239], a[188:191], a[252:255]\n"
    "ds_read_b128 a[152:155], v241 offset:61440\n"
    "s_xor_b32 s91, s91, 0x185800\n"
    "v_mfma_f32_16x16x32_bf16 a[252:255], a[232:235], a[156:159], a[252:255]\n"
    "s_waitcnt vmcnt(12) lgkmcnt(11)\n"
    "ds_read_b128 a[184:187], v242 offset:61440\n"
    "ds_read_b128 a[188:191], v242 offset:63488\n"
    "v_mfma_f32_16x16x32_bf16 v[16:19], a[192:195], a[128:131], v[16:19]\n"
    "ds_read_b128 a[156:159], v241 offset:63488\n"
    "v_add_f32_dpp v255, v253, v134 row_newbcast:6 row_mask:0xf bank_mask:0xf\n"
    "v_mfma_f32_16x16x32_bf16 v[16:19], a[196:199], a[160:163], v[16:19]\n"
    "s_waitcnt lgkmcnt(12)\n"
    "v_mul_f32 v244, v244, v254\n"
    "v_mul_f32 v246, v246, v255\n"
    "v_mfma_f32_16x16x32_bf16 v[48:51], a[196:199], a[164:167], v[48:51]\n"
    "buffer_load_dwordx4 a[232:235], v240, s[20:23], s53 offen offset:2048 sc1\n"
    "v_mfma_f32_16x16x32_bf16 v[48:51], a[192:195], a[132:135], v[48:51]\n"
    "s_waitcnt lgkmcnt(10)\n"
    "v_add_f32_dpp v254, v253, v133 row_newbcast:5 row_mask:0xf bank_mask:0xf\n"
    "v_add_f32_dpp v255, v253, v135 row_newbcast:7 row_mask:0xf bank_mask:0xf\n"
    "v_mfma_f32_16x16x32_bf16 v[80:83], a[192:195], a[136:139], v[80:83]\n"
    "buffer_load_dwordx4 a[236:239], v240, s[20:23], s53 offen offset:3072 sc1\n"
    "s_addk_i32 s53, 4096\n"
    "v_mfma_f32_16x16x32_bf16 v[80:83], a[196:199], a[168:171], v[80:83]\n"
    "s_waitcnt lgkmcnt(8)\n"
    "v_mul_f32 v245, v245, v254\n"
    "v_mul_f32 v247, v247, v255\n"
    "v_mfma_f32_16x16x32_bf16 v[112:115], a[196:199], a[172:175], v[112:115]\n"
    "v_cvt_pk_bf16_f32 v250, v244, v245\n"
    "v_cvt_pk_bf16_f32 v251, v246, v247\n"
    "v_mfma_f32_16x16x32_bf16 v[112:115], a[192:195], a[140:143], v[112:115]\n"
    "s_waitcnt lgkmcnt(6)\n"
    "v_accvgpr_read_b32 v245, a20\n"
    "v_accvgpr_read_b32 v247, a21\n"
    "v_mfma_f32_16x16x32_bf16 v[144:147], a[192:195], a[144:147], v[144:147]\n"
    "v_lshlrev_b32 v244, 16, v245\n"
    "v_lshlrev_b32 v246, 16, v247\n"
    "v_mfma_f32_16x16x32_bf16 v[144:147], a[196:199], a[176:179], v[144:147]\n"
    "s_waitcnt lgkmcnt(4)\n"
    "v_and_b32 v245, s58, v245\n"
    "v_and_b32 v247, s58, v247\n"
    "v_mfma_f32_16x16x32_bf16 v[176:179], a[196:199], a[180:183], v[176:179]\n"
    "v_add_f32_dpp v254, v253, v160 row_newbcast:0 row_mask:0xf bank_mask:0xf\n"
    "v_add_f32_dpp v255, v253, v162 row_newbcast:2 row_mask:0xf bank_mask:0xf\n"
    "v_mfma_f32_16x16x32_bf16 v[176:179], a[192:195], a[148:151], v[176:179]\n"
    "s_waitcnt lgkmcnt(2)\n"
    "v_mul_f32 v244, v244, v254\n"
    "v_mul_f32 v246, v246, v255\n"
    "v_mfma_f32_16x16x32_bf16 v[208:211], a[192:195], a[152:155], v[208:211]\n"
    "v_add_f32_dpp v254, v253, v161 row_newbcast:1 row_mask:0xf bank_mask:0xf\n"
    "v_add_f32_dpp v255, v253, v163 row_newbcast:3 row_mask:0xf bank_mask:0xf\n"
    "v_mfma_f32_16x16x32_bf16 v[208:211], a[196:199], a[184:187], v[208:211]\n"
    "s_waitcnt lgkmcnt(0)\n"
    "v_mul_f32 v245, v245, v254\n"
    "v_mul_f32 v247, v247, v255\n"
    "v_mfma_f32_16x16x32_bf16 a[240:243], a[196:199], a[188:191], a[240:243]\n"
    "v_mfma_f32_16x16x32_bf16 a[240:243], a[192:195], a[156:159], a[240:243]\n"
    "s_waitcnt vmcnt(11)\n"
    "v_mfma_f32_16x16x32_bf16 a[244:247], a[200:203], a[156:159], a[244:247]\n"
    "buffer_store_dwordx4 v[248:251], v240, s[32:35], s91 offen sc1\n"
    "ds_write_b128 v243, v[248:251] offset:8192\n"
    "v_mfma_f32_16x16x32_bf16 a[244:247], a[204:207], a[188:191], a[244:247]\n"
    "buffer_load_dwordx4 a[192:195], v240, s[20:23], s53 offen offset:0 sc1\n"
    "v_mfma_f32_16x16x32_bf16 v[212:215], a[204:207], a[184:187], v[212:215]\n"
    "buffer_load_dwordx4 a[196:199], v240, s[20:23], s53 offen offset:1024 sc1\n"
    "v_mfma_f32_16x16x32_bf16 v[212:215], a[200:203], a[152:155], v[212:215]\n"
    "v_cvt_pk_bf16_f32 v248, v244, v245\n"
    "v_cvt_pk_bf16_f32 v249, v246, v247\n"
    "v_mfma_f32_16x16x32_bf16 v[180:183], a[200:203], a[148:151], v[180:183]\n"
    "v_accvgpr_read_b32 v245, a22\n"
    "v_accvgpr_read_b32 v247, a23\n"
    "v_mfma_f32_16x16x32_bf16 v[180:183], a[204:207], a[180:183], v[180:183]\n"
    "v_lshlrev_b32 v244, 16, v245\n"
    "v_lshlrev_b32 v246, 16, v247\n"
    "v_mfma_f32_16x16x32_bf16 v[148:151], a[204:207], a[176:179], v[148:151]\n"
    "v_and_b32 v245, s58, v245\n"
    "v_and_b32 v247, s58, v247\n"
    "v_mfma_f32_16x16x32_bf16 v[148:151], a[200:203], a[144:147], v[148:151]\n"
    "s_xor_b32 s91, s91, 0x800\n"
    "v_add_f32_dpp v254, v253, v164 row_newbcast:4 row_mask:0xf bank_mask:0xf\n"
    "v_add_f32_dpp v255, v253, v166 row_newbcast:6 row_mask:0xf bank_mask:0xf\n"
    "v_mfma_f32_16x16x32_bf16 v[116:119], a[200:203], a[140:143], v[116:119]\n"
    "v_mul_f32 v244, v244, v254\n"
    "v_mul_f32 v246, v246, v255\n"
    "v_mfma_f32_16x16x32_bf16 v[116:119], a[204:207], a[172:175], v[116:119]\n"
    "v_add_f32_dpp v254, v253, v165 row_newbcast:5 row_mask:0xf bank_mask:0xf\n"
    "v_add_f32_dpp v255, v253, v167 row_newbcast:7 row_mask:0xf bank_mask:0xf\n"
    "v_mfma_f32_16x16x32_bf16 v[84:87], a[204:207], a[168:171], v[84:87]\n"
    "v_mul_f32 v245, v245, v254\n"
    "v_mul_f32 v247, v247, v255\n"
    "v_mfma_f32_16x16x32_bf16 v[84:87], a[200:203], a[136:139], v[84:87]\n"
    "v_cvt_pk_bf16_f32 v250, v244, v245\n"
    "v_cvt_pk_bf16_f32 v251, v246, v247\n"
    "v_mfma_f32_16x16x32_bf16 v[52:55], a[200:203], a[132:135], v[52:55]\n"
    "v_accvgpr_read_b32 v245, a24\n"
    "v_accvgpr_read_b32 v247, a25\n"
    "v_mfma_f32_16x16x32_bf16 v[52:55], a[204:207], a[164:167], v[52:55]\n"
    "v_lshlrev_b32 v244, 16, v245\n"
    "v_lshlrev_b32 v246, 16, v247\n"
    "v_mfma_f32_16x16x32_bf16 v[20:23], a[204:207], a[160:163], v[20:23]\n"
    "v_and_b32 v245, s58, v245\n"
    "v_and_b32 v247, s58, v247\n"
    "v_mfma_f32_16x16x32_bf16 v[20:23], a[200:203], a[128:131], v[20:23]\n"
    "s_waitcnt vmcnt(11)\n"
    "v_add_f32_dpp v254, v253, v192 row_newbcast:0 row_mask:0xf bank_mask:0xf\n"
    "v_add_f32_dpp v255, v253, v194 row_newbcast:2 row_mask:0xf bank_mask:0xf\n"
    "v_mfma_f32_16x16x32_bf16 v[24:27], a[208:211], a[128:131], v[24:27]\n"
    "buffer_load_dwordx4 a[200:203], v240, s[20:23], s53 offen offset:2048 sc1\n"
    "v_mfma_f32_16x16x32_bf16 v[24:27], a[212:215], a[160:163], v[24:27]\n"
    "buffer_load_dwordx4 a[204:207], v240, s[20:23], s53 offen offset:3072 sc1\n"
    "v_mfma_f32_16x16x32_bf16 v[56:59], a[212:215], a[164:167], v[56:59]\n"
    "s_addk_i32 s53, 4096\n"
    "v_mul_f32 v244, v244, v254\n"
    "v_mul_f32 v246, v246, v255\n"
    "v_mfma_f32_16x16x32_bf16 v[56:59], a[208:211], a[132:135], v[56:59]\n"
    "v_add_f32_dpp v254, v253, v193 row_newbcast:1 row_mask:0xf bank_mask:0xf\n"
    "v_add_f32_dpp v255, v253, v195 row_newbcast:3 row_mask:0xf bank_mask:0xf\n"
    "v_mfma_f32_16x16x32_bf16 v[88:91], a[208:211], a[136:139], v[88:91]\n"
    "buffer_store_dwordx4 v[248:251], v240, s[32:35], s91 offen sc1\n"
    "ds_write_b128 v243, v[248:251] offset:10240\n"
    "v_mfma_f32_16x16x32_bf16 v[88:91], a[212:215], a[168:171], v[88:91]\n"
    "v_mul_f32 v245, v245, v254\n"
    "v_mul_f32 v247, v247, v255\n"
    "v_mfma_f32_16x16x32_bf16 v[120:123], a[212:215], a[172:175], v[120:123]\n"
    "v_cvt_pk_bf16_f32 v248, v244, v245\n"
    "v_cvt_pk_bf16_f32 v249, v246, v247\n"
    "v_mfma_f32_16x16x32_bf16 v[120:123], a[208:211], a[140:143], v[120:123]\n"
    "s_xor_b32 s91, s91, 0x101800\n"
    "v_accvgpr_read_b32 v245, a26\n"
    "v_accvgpr_read_b32 v247, a27\n"
    "v_mfma_f32_16x16x32_bf16 v[152:155], a[208:211], a[144:147], v[152:155]\n"
    "v_lshlrev_b32 v244, 16, v245\n"
    "v_lshlrev_b32 v246, 16, v247\n"
    "v_mfma_f32_16x16x32_bf16 v[152:155], a[212:215], a[176:179], v[152:155]\n"
    "v_and_b32 v245, s58, v245\n"
    "v_and_b32 v247, s58, v247\n"
    "v_mfma_f32_16x16x32_bf16 v[184:187], a[212:215], a[180:183], v[184:187]\n"
    "v_add_f32_dpp v254, v253, v196 row_newbcast:4 row_mask:0xf bank_mask:0xf\n"
    "v_add_f32_dpp v255, v253, v198 row_newbcast:6 row_mask:0xf bank_mask:0xf\n"
    "v_mfma_f32_16x16x32_bf16 v[184:187], a[208:211], a[148:151], v[184:187]\n"
    "v_mul_f32 v244, v244, v254\n"
    "v_mul_f32 v246, v246, v255\n"
    "v_mfma_f32_16x16x32_bf16 v[216:219], a[208:211], a[152:155], v[216:219]\n"
    "v_add_f32_dpp v254, v253, v197 row_newbcast:5 row_mask:0xf bank_mask:0xf\n"
    "v_add_f32_dpp v255, v253, v199 row_newbcast:7 row_mask:0xf bank_mask:0xf\n"
    "v_mfma_f32_16x16x32_bf16 v[216:219], a[212:215], a[184:187], v[216:219]\n"
    "v_mul_f32 v245, v245, v254\n"
    "v_mul_f32 v247, v247, v255\n"
    "v_mfma_f32_16x16x32_bf16 a[248:251], a[212:215], a[188:191], a[248:251]\n"
    "v_cvt_pk_bf16_f32 v250, v244, v245\n"
    "v_cvt_pk_bf16_f32 v251, v246, v247\n"
    "v_mfma_f32_16x16x32_bf16 a[248:251], a[208:211], a[156:159], a[248:251]\n"
    "s_waitcnt vmcnt(11)\n"
    "v_accvgpr_read_b32 v245, a28\n"
    "v_accvgpr_read_b32 v247, a29\n"
    "v_mfma_f32_16x16x32_bf16 a[252:255], a[216:219], a[156:159], a[252:255]\n"
    "buffer_store_dwordx4 v[248:251], v240, s[32:35], s91 offen sc1\n"
    "ds_write_b128 v243, v[248:251] offset:12288\n"
    "v_mfma_f32_16x16x32_bf16 a[252:255], a[220:223], a[188:191], a[252:255]\n"
    "buffer_load_dwordx4 a[208:211], v240, s[20:23], s53 offen offset:0 sc1\n"
    "v_mfma_f32_16x16x32_bf16 v[220:223], a[220:223], a[184:187], v[220:223]\n"
    "buffer_load_dwordx4 a[212:215], v240, s[20:23], s53 offen offset:1024 sc1\n"
    "v_lshlrev_b32 v244, 16, v245\n"
    "v_lshlrev_b32 v246, 16, v247\n"
    "v_mfma_f32_16x16x32_bf16 v[220:223], a[216:219], a[152:155], v[220:223]\n"
    "ds_read_b128 a[156:159], v241 offset:30720\n"
    "ds_read_b128 a[188:191], v242 offset:30720\n"
    "v_and_b32 v245, s58, v245\n"
    "v_and_b32 v247, s58, v247\n"
    "v_add_f32_dpp v254, v253, v224 row_newbcast:0 row_mask:0xf bank_mask:0xf\n"
    "v_add_f32_dpp v255, v253, v226 row_newbcast:2 row_mask:0xf bank_mask:0xf\n"
    "v_mfma_f32_16x16x32_bf16 v[188:191], a[216:219], a[148:151], v[188:191]\n"
    "ds_read_b128 a[184:187], v242 offset:28672\n"
    "v_mul_f32 v244, v244, v254\n"
    "v_mul_f32 v246, v246, v255\n"
    "v_add_f32_dpp v254, v253, v225 row_newbcast:1 row_mask:0xf bank_mask:0xf\n"
    "v_add_f32_dpp v255, v253, v227 row_newbcast:3 row_mask:0xf bank_mask:0xf\n"
    "v_mfma_f32_16x16x32_bf16 v[188:191], a[220:223], a[180:183], v[188:191]\n"
    "ds_read_b128 a[152:155], v241 offset:28672\n"
    "v_mul_f32 v245, v245, v254\n"
    "v_mul_f32 v247, v247, v255\n"
    "v_cvt_pk_bf16_f32 v248, v244, v245\n"
    "v_mfma_f32_16x16x32_bf16 v[156:159], a[220:223], a[176:179], v[156:159]\n"
    "ds_read_b128 a[148:151], v241 offset:26624\n"
    "v_cvt_pk_bf16_f32 v249, v246, v247\n"
    "v_accvgpr_read_b32 v245, a30\n"
    "v_accvgpr_read_b32 v247, a31\n"
    "v_lshlrev_b32 v244, 16, v245\n"
    "v_mfma_f32_16x16x32_bf16 v[156:159], a[216:219], a[144:147], v[156:159]\n"
    "ds_read_b128 a[180:183], v242 offset:26624\n"
    "v_lshlrev_b32 v246, 16, v247\n"
    "v_and_b32 v245, s58, v245\n"
    "v_and_b32 v247, s58, v247\n"
    "v_mfma_f32_16x16x32_bf16 v[124:127], a[216:219], a[140:143], v[124:127]\n"
    "ds_read_b128 a[176:179], v242 offset:24576\n"
    "v_add_f32_dpp v254, v253, v228 row_newbcast:4 row_mask:0xf bank_mask:0xf\n"
    "v_mfma_f32_16x16x32_bf16 v[124:127], a[220:223], a[172:175], v[124:127]\n"
    "ds_read_b128 a[144:147], v241 offset:24576\n"
    "s_xor_b32 s91, s91, 0x800\n"
    "v_mfma_f32_16x16x32_bf16 v[92:95], a[220:223], a[168:171], v[92:95]\n"
    "ds_read_b128 a[140:143], v241 offset:22528\n"
    "v_add_f32_dpp v255, v253, v230 row_newbcast:6 row_mask:0xf bank_mask:0xf\n"
    "v_mfma_f32_16x16x32_bf16 v[92:95], a[216:219], a[136:139], v[92:95]\n"
    "ds_read_b128 a[172:175], v242 offset:22528\n"
    "v_mul_f32 v244, v244, v254\n"
    "v_mfma_f32_16x16x32_bf16 v[60:63], a[216:219], a[132:135], v[60:63]\n"
    "ds_read_b128 a[168:171], v242 offset:20480\n"
    "v_mul_f32 v246, v246, v255\n"
    "v_mfma_f32_16x16x32_bf16 v[60:63], a[220:223], a[164:167], v[60:63]\n"
    "ds_read_b128 a[136:139], v241 offset:20480\n"
    "v_add_f32_dpp v254, v253, v229 row_newbcast:5 row_mask:0xf bank_mask:0xf\n"
    "v_mfma_f32_16x16x32_bf16 v[28:31], a[220:223], a[160:163], v[28:31]\n"
    "ds_read_b128 a[132:135], v241 offset:18432\n"
    "v_add_f32_dpp v255, v253, v231 row_newbcast:7 row_mask:0xf bank_mask:0xf\n"
    "v_mfma_f32_16x16x32_bf16 v[28:31], a[216:219], a[128:131], v[28:31]\n"
    "s_waitcnt vmcnt(12) lgkmcnt(11)\n"
    "ds_read_b128 a[164:167], v242 offset:18432\n"
    "ds_read_b128 a[160:163], v242 offset:16384\n"
    "v_mfma_f32_16x16x32_bf16 a[240:243], a[224:227], a[156:159], a[240:243]\n"
    "ds_read_b128 a[128:131], v241 offset:16384\n"
    "v_mul_f32 v245, v245, v254\n"
    "v_mfma_f32_16x16x32_bf16 a[240:243], a[228:231], a[188:191], a[240:243]\n"
    "s_waitcnt lgkmcnt(12)\n"
    "v_mul_f32 v247, v247, v255\n"
    "v_cvt_pk_bf16_f32 v250, v244, v245\n"
    "v_mfma_f32_16x16x32_bf16 v[208:211], a[228:231], a[184:187], v[208:211]\n"
    "buffer_load_dwordx4 a[216:219], v240, s[20:23], s53 offen offset:2048 sc1\n"
    "v_mfma_f32_16x16x32_bf16 v[208:211], a[224:227], a[152:155], v[208:211]\n"
    "s_waitcnt lgkmcnt(10)\n"
    "v_cvt_pk_bf16_f32 v251, v246, v247\n"
    "v_mfma_f32_16x16x32_bf16 v[176:179], a[224:227], a[148:151], v[176:179]\n"
    "buffer_load_dwordx4 a[220:223], v240, s[20:23], s53 offen offset:3072 sc1\n"
    "s_addk_i32 s53, 4096\n"
    "v_mfma_f32_16x16x32_bf16 v[176:179], a[228:231], a[180:183], v[176:179]\n"
    "s_waitcnt lgkmcnt(8)\n"
    "v_accvgpr_read_b32 v245, a32\n"
    "v_accvgpr_read_b32 v247, a33\n"
    "v_mfma_f32_16x16x32_bf16 v[144:147], a[228:231], a[176:179], v[144:147]\n"
    "buffer_store_dwordx4 v[248:251], v240, s[32:35], s91 offen sc1\n"
    "ds_write_b128 v243, v[248:251] offset:14336\n"
    "v_mfma_f32_16x16x32_bf16 v[144:147], a[224:227], a[144:147], v[144:147]\n"
    "s_waitcnt lgkmcnt(7)\n"
    "v_lshlrev_b32 v244, 16, v245\n"
    "v_lshlrev_b32 v246, 16, v247\n"
    "v_mfma_f32_16x16x32_bf16 v[112:115], a[224:227], a[140:143], v[112:115]\n"
    "v_and_b32 v245, s58, v245\n"
    "v_and_b32 v247, s58, v247\n"
    "v_mfma_f32_16x16x32_bf16 v[112:115], a[228:231], a[172:175], v[112:115]\n"
    "s_waitcnt lgkmcnt(5)\n"
    "v_add_f32_dpp v254, v253, v8 row_newbcast:8 row_mask:0xf bank_mask:0xf\n"
    "v_add_f32_dpp v255, v253, v10 row_newbcast:10 row_mask:0xf bank_mask:0xf\n"
    "v_mfma_f32_16x16x32_bf16 v[80:83], a[228:231], a[168:171], v[80:83]\n"
    "v_mul_f32 v244, v244, v254\n"
    "v_mul_f32 v246, v246, v255\n"
    "v_mfma_f32_16x16x32_bf16 v[80:83], a[224:227], a[136:139], v[80:83]\n"
    "s_waitcnt lgkmcnt(3)\n"
    "v_add_f32_dpp v254, v253, v9 row_newbcast:9 row_mask:0xf bank_mask:0xf\n"
    "v_add_f32_dpp v255, v253, v11 row_newbcast:11 row_mask:0xf bank_mask:0xf\n"
    "v_mfma_f32_16x16x32_bf16 v[48:51], a[224:227], a[132:135], v[48:51]\n"
    "v_mul_f32 v245, v245, v254\n"
    "v_mul_f32 v247, v247, v255\n"
    "v_mfma_f32_16x16x32_bf16 v[48:51], a[228:231], a[164:167], v[48:51]\n"
    "s_waitcnt lgkmcnt(1)\n"
    "s_xor_b32 s91, s91, 0x195800\n"
    "v_cvt_pk_bf16_f32 v248, v244, v245\n"
    "v_mfma_f32_16x16x32_bf16 v[16:19], a[228:231], a[160:163], v[16:19]\n"
    "v_cvt_pk_bf16_f32 v249, v246, v247\n"
    "v_accvgpr_read_b32 v245, a34\n"
    "v_mfma_f32_16x16x32_bf16 v[16:19], a[224:227], a[128:131], v[16:19]\n"
    "s_waitcnt vmcnt(12)\n"
    "v_accvgpr_read_b32 v247, a35\n"
    "v_lshlrev_b32 v244, 16, v245\n"
    "v_mfma_f32_16x16x32_bf16 v[20:23], a[232:235], a[128:131], v[20:23]\n"
    "buffer_load_dwordx4 a[224:227], v240, s[20:23], s53 offen offset:0 sc1\n"
    "v_mfma_f32_16x16x32_bf16 v[20:23], a[236:239], a[160:163], v[20:23]\n"
    "buffer_load_dwordx4 a[228:231], v240, s[20:23], s53 offen offset:1024 sc1\n"
    "v_mfma_f32_16x16x32_bf16 v[52:55], a[236:239], a[164:167], v[52:55]\n"
    "v_lshlrev_b32 v246, 16, v247\n"
    "v_and_b32 v245, s58, v245\n"
    "v_mfma_f32_16x16x32_bf16 v[52:55], a[232:235], a[132:135], v[52:55]\n"
    "v_and_b32 v247, s58, v247\n"
    "v_add_f32_dpp v254, v253, v12 row_newbcast:12 row_mask:0xf bank_mask:0xf\n"
    "v_mfma_f32_16x16x32_bf16 v[84:87], a[232:235], a[136:139], v[84:87]\n"
    "v_add_f32_dpp v255, v253, v14 row_newbcast:14 row_mask:0xf bank_mask:0xf\n"
    "v_mul_f32 v244, v244, v254\n"
    "v_mfma_f32_16x16x32_bf16 v[84:87], a[236:239], a[168:171], v[84:87]\n"
    "v_mul_f32 v246, v246, v255\n"
    "v_add_f32_dpp v254, v253, v13 row_newbcast:13 row_mask:0xf bank_mask:0xf\n"
    "v_mfma_f32_16x16x32_bf16 v[116:119], a[236:239], a[172:175], v[116:119]\n"
    "v_add_f32_dpp v255, v253, v15 row_newbcast:15 row_mask:0xf bank_mask:0xf\n"
    "v_mul_f32 v245, v245, v254\n"
    "v_mfma_f32_16x16x32_bf16 v[116:119], a[232:235], a[140:143], v[116:119]\n"
    "v_mul_f32 v247, v247, v255\n"
    "v_cvt_pk_bf16_f32 v250, v244, v245\n"
    "v_mfma_f32_16x16x32_bf16 v[148:151], a[232:235], a[144:147], v[148:151]\n"
    "v_cvt_pk_bf16_f32 v251, v246, v247\n"
    "v_accvgpr_read_b32 v245, a36\n"
    "v_mfma_f32_16x16x32_bf16 v[148:151], a[236:239], a[176:179], v[148:151]\n"
    "buffer_store_dwordx4 v[248:251], v240, s[32:35], s91 offen sc1\n"
    "ds_write_b128 v243, v[248:251] offset:32768\n"
    "v_accvgpr_read_b32 v247, a37\n"
    "v_lshlrev_b32 v244, 16, v245\n"
    "v_lshlrev_b32 v246, 16, v247\n"
    "v_and_b32 v245, s58, v245\n"
    "v_mfma_f32_16x16x32_bf16 v[180:183], a[236:239], a[180:183], v[180:183]\n"
    "s_xor_b32 s91, s91, 0x800\n"
    "v_and_b32 v247, s58, v247\n"
    "v_add_f32_dpp v254, v253, v40 row_newbcast:8 row_mask:0xf bank_mask:0xf\n"
    "v_add_f32_dpp v255, v253, v42 row_newbcast:10 row_mask:0xf bank_mask:0xf\n"
    "v_mul_f32 v244, v244, v254\n"
    "v_mfma_f32_16x16x32_bf16 v[180:183], a[232:235], a[148:151], v[180:183]\n"
    "v_mul_f32 v246, v246, v255\n"
    "v_add_f32_dpp v254, v253, v41 row_newbcast:9 row_mask:0xf bank_mask:0xf\n"
    "v_add_f32_dpp v255, v253, v43 row_newbcast:11 row_mask:0xf bank_mask:0xf\n"
    "v_mul_f32 v245, v245, v254\n"
    "v_mfma_f32_16x16x32_bf16 v[212:215], a[232:235], a[152:155], v[212:215]\n"
    "v_mul_f32 v247, v247, v255\n"
    "v_cvt_pk_bf16_f32 v248, v244, v245\n"
    "v_mfma_f32_16x16x32_bf16 v[212:215], a[236:239], a[184:187], v[212:215]\n"
    "v_cvt_pk_bf16_f32 v249, v246, v247\n"
    "v_accvgpr_read_b32 v245, a38\n"
    "v_accvgpr_read_b32 v247, a39\n"
    "v_lshlrev_b32 v244, 16, v245\n"
    "v_mfma_f32_16x16x32_bf16 a[244:247], a[236:239], a[188:191], a[244:247]\n"
    "v_lshlrev_b32 v246, 16, v247\n"
    "v_and_b32 v245, s58, v245\n"
    "v_and_b32 v247, s58, v247\n"
    "v_add_f32_dpp v254, v253, v44 row_newbcast:12 row_mask:0xf bank_mask:0xf\n"
    "v_mfma_f32_16x16x32_bf16 a[244:247], a[232:235], a[156:159], a[244:247]\n"
    "s_waitcnt vmcnt(12)\n"
    "v_add_f32_dpp v255, v253, v46 row_newbcast:14 row_mask:0xf bank_mask:0xf\n"
    "v_mul_f32 v244, v244, v254\n"
    "v_mul_f32 v246, v246, v255\n"
    "v_mfma_f32_16x16x32_bf16 a[248:251], a[192:195], a[156:159], a[248:251]\n"
    "v_add_f32_dpp v254, v253, v45 row_newbcast:13 row_mask:0xf bank_mask:0xf\n"
    "v_add_f32_dpp v255, v253, v47 row_newbcast:15 row_mask:0xf bank_mask:0xf\n"
    "v_mul_f32 v245, v245, v254\n"
    "v_mfma_f32_16x16x32_bf16 a[248:251], a[196:199], a[188:191], a[248:251]\n"
    "v_mul_f32 v247, v247, v255\n"
    "v_cvt_pk_bf16_f32 v250, v244, v245\n"
    "v_cvt_pk_bf16_f32 v251, v246, v247\n"
    "v_accvgpr_read_b32 v245, a40\n"
    "v_mfma_f32_16x16x32_bf16 v[216:219], a[196:199], a[184:187], v[216:219]\n"
    "buffer_store_dwordx4 v[248:251], v240, s[32:35], s91 offen sc1\n"
    "ds_write_b128 v243, v[248:251] offset:34816\n"
    "v_accvgpr_read_b32 v247, a41\n"
    "v_lshlrev_b32 v244, 16, v245\n"
    "v_lshlrev_b32 v246, 16, v247\n"
    "v_and_b32 v245, s58, v245\n"
    "v_mfma_f32_16x16x32_bf16 v[216:219], a[192:195], a[152:155], v[216:219]\n"
    "buffer_load_dwordx4 a[232:235], v240, s[20:23], s53 offen offset:2048 sc1\n"
    "v_and_b32 v247, s58, v247\n"
    "v_add_f32_dpp v254, v253, v72 row_newbcast:8 row_mask:0xf bank_mask:0xf\n"
    "v_add_f32_dpp v255, v253, v74 row_newbcast:10 row_mask:0xf bank_mask:0xf\n"
    "v_mul_f32 v244, v244, v254\n"
    "v_mfma_f32_16x16x32_bf16 v[184:187], a[192:195], a[148:151], v[184:187]\n"
    "buffer_load_dwordx4 a[236:239], v240, s[20:23], s53 offen offset:3072 sc1\n"
    "s_addk_i32 s53, 4096\n"
    "s_xor_b32 s91, s91, 0x101800\n"
    "v_mul_f32 v246, v246, v255\n"
    "v_add_f32_dpp v254, v253, v73 row_newbcast:9 row_mask:0xf bank_mask:0xf\n"
    "v_add_f32_dpp v255, v253, v75 row_newbcast:11 row_mask:0xf bank_mask:0xf\n"
    "v_mul_f32 v245, v245, v254\n"
    "v_mfma_f32_16x16x32_bf16 v[184:187], a[196:199], a[180:183], v[184:187]\n"
    "v_mul_f32 v247, v247, v255\n"
    "v_cvt_pk_bf16_f32 v248, v244, v245\n"
    "v_mfma_f32_16x16x32_bf16 v[152:155], a[196:199], a[176:179], v[152:155]\n"
    "v_cvt_pk_bf16_f32 v249, v246, v247\n"
    "v_accvgpr_read_b32 v245, a42\n"
    "v_accvgpr_read_b32 v247, a43\n"
    "v_lshlrev_b32 v244, 16, v245\n"
    "v_mfma_f32_16x16x32_bf16 v[152:155], a[192:195], a[144:147], v[152:155]\n"
    "v_lshlrev_b32 v246, 16, v247\n"
    "v_and_b32 v245, s58, v245\n"
    "v_and_b32 v247, s58, v247\n"
    "v_add_f32_dpp v254, v253, v76 row_newbcast:12 row_mask:0xf bank_mask:0xf\n"
    "v_mfma_f32_16x16x32_bf16 v[120:123], a[192:195], a[140:143], v[120:123]\n"
    "v_add_f32_dpp v255, v253, v78 row_newbcast:14 row_mask:0xf bank_mask:0xf\n"
    "v_mul_f32 v244, v244, v254\n"
    "v_mul_f32 v246, v246, v255\n"
    "v_mfma_f32_16x16x32_bf16 v[120:123], a[196:199], a[172:175], v[120:123]\n"
    "v_add_f32_dpp v254, v253, v77 row_newbcast:13 row_mask:0xf bank_mask:0xf\n"
    "v_add_f32_dpp v255, v253, v79 row_newbcast:15 row_mask:0xf bank_mask:0xf\n"
    "v_mul_f32 v245, v245, v254\n"
    "v_mfma_f32_16x16x32_bf16 v[88:91], a[196:199], a[168:171], v[88:91]\n"
    "v_mul_f32 v247, v247, v255\n"
    "v_cvt_pk_bf16_f32 v250, v244, v245\n"
    "v_cvt_pk_bf16_f32 v251, v246, v247\n"
    "v_accvgpr_read_b32 v245, a44\n"
    "v_mfma_f32_16x16x32_bf16 v[88:91], a[192:195], a[136:139], v[88:91]\n"
    "buffer_store_dwordx4 v[248:251], v240, s[32:35], s91 offen sc1\n"
    "ds_write_b128 v243, v[248:251] offset:36864\n"
    "v_accvgpr_read_b32 v247, a45\n"
    "v_lshlrev_b32 v244, 16, v245\n"
    "v_lshlrev_b32 v246, 16, v247\n"
    "v_and_b32 v245, s58, v245\n"
    "v_mfma_f32_16x16x32_bf16 v[56:59], a[192:195], a[132:135], v[56:59]\n"
    "s_xor_b32 s91, s91, 0x800\n"
    "v_and_b32 v247, s58, v247\n"
    "v_add_f32_dpp v254, v253, v104 row_newbcast:8 row_mask:0xf bank_mask:0xf\n"
    "v_add_f32_dpp v255, v253, v106 row_newbcast:10 row_mask:0xf bank_mask:0xf\n"
    "v_mul_f32 v244, v244, v254\n"
    "v_mfma_f32_16x16x32_bf16 v[56:59], a[196:199], a[164:167], v[56:59]\n"
    "v_mul_f32 v246, v246, v255\n"
    "v_add_f32_dpp v254, v253, v105 row_newbcast:9 row_mask:0xf bank_mask:0xf\n"
    "v_add_f32_dpp v255, v253, v107 row_newbcast:11 row_mask:0xf bank_mask:0xf\n"
    "v_mfma_f32_16x16x32_bf16 v[24:27], a[196:199], a[160:163], v[24:27]\n"
    "v_mul_f32 v245, v245, v254\n"
    "v_mul_f32 v247, v247, v255\n"
    "v_cvt_pk_bf16_f32 v248, v244, v245\n"
    "v_mfma_f32_16x16x32_bf16 v[24:27], a[192:195], a[128:131], v[24:27]\n"
    "s_waitcnt vmcnt(14)\n"
    "v_cvt_pk_bf16_f32 v249, v246, v247\n"
    "v_accvgpr_read_b32 v245, a46\n"
    "v_accvgpr_read_b32 v247, a47\n"
    "v_lshlrev_b32 v244, 16, v245\n"
    "v_mfma_f32_16x16x32_bf16 v[28:31], a[200:203], a[128:131], v[28:31]\n"
    "v_lshlrev_b32 v246, 16, v247\n"
    "v_and_b32 v245, s58, v245\n"
    "v_and_b32 v247, s58, v247\n"
    "v_add_f32_dpp v254, v253, v108 row_newbcast:12 row_mask:0xf bank_mask:0xf\n"
    "v_mfma_f32_16x16x32_bf16 v[28:31], a[204:207], a[160:163], v[28:31]\n"
    "v_add_f32_dpp v255, v253, v110 row_newbcast:14 row_mask:0xf bank_mask:0xf\n"
    "v_mul_f32 v244, v244, v254\n"
    "v_mul_f32 v246, v246, v255\n"
    "v_mfma_f32_16x16x32_bf16 v[60:63], a[204:207], a[164:167], v[60:63]\n"
    "v_add_f32_dpp v254, v253, v109 row_newbcast:13 row_mask:0xf bank_mask:0xf\n"
    "v_add_f32_dpp v255, v253, v111 row_newbcast:15 row_mask:0xf bank_mask:0xf\n"
    "v_mul_f32 v245, v245, v254\n"
    "v_mfma_f32_16x16x32_bf16 v[60:63], a[200:203], a[132:135], v[60:63]\n"
    "v_mul_f32 v247, v247, v255\n"
    "v_cvt_pk_bf16_f32 v250, v244, v245\n"
    "v_cvt_pk_bf16_f32 v251, v246, v247\n"
    "v_accvgpr_read_b32 v245, a48\n"
    "v_mfma_f32_16x16x32_bf16 v[92:95], a[200:203], a[136:139], v[92:95]\n"
    "buffer_store_dwordx4 v[248:251], v240, s[32:35], s91 offen sc1\n"
    "ds_write_b128 v243, v[248:251] offset:38912\n"
    "v_accvgpr_read_b32 v247, a49\n"
    "v_lshlrev_b32 v244, 16, v245\n"
    "v_lshlrev_b32 v246, 16, v247\n"
    "v_and_b32 v245, s58, v245\n"
    "v_mfma_f32_16x16x32_bf16 v[92:95], a[204:207], a[168:171], v[92:95]\n"
    "buffer_load_dwordx4 a[192:195], v240, s[20:23], s53 offen offset:0 sc1\n"
    "v_and_b32 v247, s58, v247\n"
    "v_add_f32_dpp v254, v253, v136 row_newbcast:8 row_mask:0xf bank_mask:0xf\n"
    "v_add_f32_dpp v255, v253, v138 row_newbcast:10 row_mask:0xf bank_mask:0xf\n"
    "v_mul_f32 v244, v244, v254\n"
    "v_mfma_f32_16x16x32_bf16 v[124:127], a[204:207], a[172:175], v[124:127]\n"
    "buffer_load_dwordx4 a[196:199], v240, s[20:23], s53 offen offset:1024 sc1\n"
    "ds_read_b128 a[128:131], v241 offset:0\n"
    "ds_read_b128 a[160:163], v242 offset:0\n"
    "v_mul_f32 v246, v246, v255\n"
    "v_add_f32_dpp v254, v253, v137 row_newbcast:9 row_mask:0xf bank_mask:0xf\n"
    "v_add_f32_dpp v255, v253, v139 row_newbcast:11 row_mask:0xf bank_mask:0xf\n"
    "v_mfma_f32_16x16x32_bf16 v[124:127], a[200:203], a[140:143], v[124:127]\n"
    "ds_read_b128 a[164:167], v242 offset:2048\n"
    "ds_read_b128 a[132:135], v241 offset:2048\n"
    "v_mul_f32 v245, v245, v254\n"
    "v_mul_f32 v247, v247, v255\n"
    "v_cvt_pk_bf16_f32 v248, v244, v245\n"
    "v_mfma_f32_16x16x32_bf16 v[156:159], a[200:203], a[144:147], v[156:159]\n"
    "ds_read_b128 a[136:139], v241 offset:4096\n"
    "ds_read_b128 a[168:171], v242 offset:4096\n"
    "v_mfma_f32_16x16x32_bf16 v[156:159], a[204:207], a[176:179], v[156:159]\n"
    "ds_read_b128 a[172:175], v242 offset:6144\n"
    "ds_read_b128 a[140:143], v241 offset:6144\n"
    "v_mfma_f32_16x16x32_bf16 v[188:191], a[204:207], a[180:183], v[188:191]\n"
    "ds_read_b128 a[144:147], v241 offset:8192\n"
    "v_cvt_pk_bf16_f32 v249, v246, v247\n"
    "v_mfma_f32_16x16x32_bf16 v[188:191], a[200:203], a[148:151], v[188:191]\n"
    "ds_read_b128 a[176:179], v242 offset:8192\n"
    "v_accvgpr_read_b32 v245, a50\n"
    "v_mfma_f32_16x16x32_bf16 v[220:223], a[200:203], a[152:155], v[220:223]\n"
    "ds_read_b128 a[180:183], v242 offset:10240\n"
    "v_accvgpr_read_b32 v247, a51\n"
    "v_mfma_f32_16x16x32_bf16 v[220:223], a[204:207], a[184:187], v[220:223]\n"
    "ds_read_b128 a[148:151], v241 offset:10240\n"
    "v_lshlrev_b32 v244, 16, v245\n"
    "v_mfma_f32_16x16x32_bf16 a[252:255], a[204:207], a[188:191], a[252:255]\n"
    "ds_read_b128 a[152:155], v241 offset:12288\n"
    "v_lshlrev_b32 v246, 16, v247\n"
    "v_mfma_f32_16x16x32_bf16 a[252:255], a[200:203], a[156:159], a[252:255]\n"
    "s_waitcnt vmcnt(13) lgkmcnt(11)\n"
    "ds_read_b128 a[184:187], v242 offset:12288\n"
    "ds_read_b128 a[188:191], v242 offset:14336\n"
    "v_mfma_f32_16x16x32_bf16 v[16:19], a[208:211], a[128:131], v[16:19]\n"
    "ds_read_b128 a[156:159], v241 offset:14336\n"
    "v_and_b32 v245, s58, v245\n"
    "v_mfma_f32_16x16x32_bf16 v[16:19], a[212:215], a[160:163], v[16:19]\n"
    "s_waitcnt lgkmcnt(12)\n"
    "v_and_b32 v247, s58, v247\n"
    "v_add_f32_dpp v254, v253, v140 row_newbcast:12 row_mask:0xf bank_mask:0xf\n"
    "v_mfma_f32_16x16x32_bf16 v[48:51], a[212:215], a[164:167], v[48:51]\n"
    "buffer_load_dwordx4 a[200:203], v240, s[20:23], s53 offen offset:2048 sc1\n"
    "v_mfma_f32_16x16x32_bf16 v[48:51], a[208:211], a[132:135], v[48:51]\n"
    "s_waitcnt lgkmcnt(10)\n"
    "v_add_f32_dpp v255, v253, v142 row_newbcast:14 row_mask:0xf bank_mask:0xf\n"
    "v_mul_f32 v244, v244, v254\n"
    "v_mfma_f32_16x16x32_bf16 v[80:83], a[208:211], a[136:139], v[80:83]\n"
    "buffer_load_dwordx4 a[204:207], v240, s[20:23], s53 offen offset:3072 sc1\n"
    "s_addk_i32 s53, 4096\n"
    "v_mfma_f32_16x16x32_bf16 v[80:83], a[212:215], a[168:171], v[80:83]\n"
    "s_waitcnt lgkmcnt(8)\n"
    "s_xor_b32 s91, s91, 0x185800\n"
    "v_mul_f32 v246, v246, v255\n"
    "v_mfma_f32_16x16x32_bf16 v[112:115], a[212:215], a[172:175], v[112:115]\n"
    "v_add_f32_dpp v254, v253, v141 row_newbcast:13 row_mask:0xf bank_mask:0xf\n"
    "v_add_f32_dpp v255, v253, v143 row_newbcast:15 row_mask:0xf bank_mask:0xf\n"
    "v_mfma_f32_16x16x32_bf16 v[112:115], a[208:211], a[140:143], v[112:115]\n"
    "s_waitcnt lgkmcnt(6)\n"
    "v_mul_f32 v245, v245, v254\n"
    "v_mul_f32 v247, v247, v255\n"
    "v_mfma_f32_16x16x32_bf16 v[144:147], a[208:211], a[144:147], v[144:147]\n"
    "v_cvt_pk_bf16_f32 v250, v244, v245\n"
    "v_cvt_pk_bf16_f32 v251, v246, v247\n"
    "v_mfma_f32_16x16x32_bf16 v[144:147], a[212:215], a[176:179], v[144:147]\n"
    "s_waitcnt lgkmcnt(4)\n"
    "v_accvgpr_read_b32 v245, a52\n"
    "v_accvgpr_read_b32 v247, a53\n"
    "v_mfma_f32_16x16x32_bf16 v[176:179], a[212:215], a[180:183], v[176:179]\n"
    "v_lshlrev_b32 v244, 16, v245\n"
    "v_lshlrev_b32 v246, 16, v247\n"
    "v_mfma_f32_16x16x32_bf16 v[176:179], a[208:211], a[148:151], v[176:179]\n"
    "s_waitcnt lgkmcnt(2)\n"
    "v_and_b32 v245, s58, v245\n"
    "v_and_b32 v247, s58, v247\n"
    "v_mfma_f32_16x16x32_bf16 v[208:211], a[208:211], a[152:155], v[208:211]\n"
    "buffer_store_dwordx4 v[248:251], v240, s[32:35], s91 offen sc1\n"
    "ds_write_b128 v243, v[248:251] offset:40960\n"
    "v_mfma_f32_16x16x32_bf16 v[208:211], a[212:215], a[184:187], v[208:211]\n"
    "s_waitcnt lgkmcnt(1)\n"
    "v_add_f32_dpp v254, v253, v168 row_newbcast:8 row_mask:0xf bank_mask:0xf\n"
    "v_add_f32_dpp v255, v253, v170 row_newbcast:10 row_mask:0xf bank_mask:0xf\n"
    "v_mfma_f32_16x16x32_bf16 a[240:243], a[212:215], a[188:191], a[240:243]\n"
    "v_mul_f32 v244, v244, v254\n"
    "v_mul_f32 v246, v246, v255\n"
    "v_mfma_f32_16x16x32_bf16 a[240:243], a[208:211], a[156:159], a[240:243]\n"
    "s_waitcnt vmcnt(14)\n"
    "v_add_f32_dpp v254, v253, v169 row_newbcast:9 row_mask:0xf bank_mask:0xf\n"
    "v_add_f32_dpp v255, v253, v171 row_newbcast:11 row_mask:0xf bank_mask:0xf\n"
    "v_mfma_f32_16x16x32_bf16 a[244:247], a[216:219], a[156:159], a[244:247]\n"
    "buffer_load_dwordx4 a[208:211], v240, s[20:23], s53 offen offset:0 sc1\n"
    "v_mfma_f32_16x16x32_bf16 a[244:247], a[220:223], a[188:191], a[244:247]\n"
    "buffer_load_dwordx4 a[212:215], v240, s[20:23], s53 offen offset:1024 sc1\n"
    "v_mfma_f32_16x16x32_bf16 v[212:215], a[220:223], a[184:187], v[212:215]\n"
    "s_xor_b32 s91, s91, 0x800\n"
    "v_mul_f32 v245, v245, v254\n"
    "v_mul_f32 v247, v247, v255\n"
    "v_mfma_f32_16x16x32_bf16 v[212:215], a[216:219], a[152:155], v[212:215]\n"
    "v_cvt_pk_bf16_f32 v248, v244, v245\n"
    "v_cvt_pk_bf16_f32 v249, v246, v247\n"
    "v_mfma_f32_16x16x32_bf16 v[180:183], a[216:219], a[148:151], v[180:183]\n"
    "v_accvgpr_read_b32 v245, a54\n"
    "v_accvgpr_read_b32 v247, a55\n"
    "v_mfma_f32_16x16x32_bf16 v[180:183], a[220:223], a[180:183], v[180:183]\n"
    "v_lshlrev_b32 v244, 16, v245\n"
    "v_lshlrev_b32 v246, 16, v247\n"
    "v_mfma_f32_16x16x32_bf16 v[148:151], a[220:223], a[176:179], v[148:151]\n"
    "v_and_b32 v245, s58, v245\n"
    "v_and_b32 v247, s58, v247\n"
    "v_mfma_f32_16x16x32_bf16 v[148:151], a[216:219], a[144:147], v[148:151]\n"
    "v_add_f32_dpp v254, v253, v172 row_newbcast:12 row_mask:0xf bank_mask:0xf\n"
    "v_add_f32_dpp v255, v253, v174 row_newbcast:14 row_mask:0xf bank_mask:0xf\n"
    "v_mfma_f32_16x16x32_bf16 v[116:119], a[216:219], a[140:143], v[116:119]\n"
    "v_mul_f32 v244, v244, v254\n"
    "v_mul_f32 v246, v246, v255\n"
    "v_mfma_f32_16x16x32_bf16 v[116:119], a[220:223], a[172:175], v[116:119]\n"
    "v_add_f32_dpp v254, v253, v173 row_newbcast:13 row_mask:0xf bank_mask:0xf\n"
    "v_add_f32_dpp v255, v253, v175 row_newbcast:15 row_mask:0xf bank_mask:0xf\n"
    "v_mul_f32 v245, v245, v254\n"
    "v_mul_f32 v247, v247, v255\n"
    "v_mfma_f32_16x16x32_bf16 v[84:87], a[220:223], a[168:171], v[84:87]\n"
    "v_cvt_pk_bf16_f32 v250, v244, v245\n"
    "v_cvt_pk_bf16_f32 v251, v246, v247\n"
    "v_accvgpr_read_b32 v245, a56\n"
    "v_accvgpr_read_b32 v247, a57\n"
    "v_mfma_f32_16x16x32_bf16 v[84:87], a[216:219], a[136:139], v[84:87]\n"
    "buffer_store_dwordx4 v[248:251], v240, s[32:35], s91 offen sc1\n"
    "ds_write_b128 v243, v[248:251] offset:43008\n"
    "v_lshlrev_b32 v244, 16, v245\n"
    "v_lshlrev_b32 v246, 16, v247\n"
    "v_and_b32 v245, s58, v245\n"
    "v_mfma_f32_16x16x32_bf16 v[52:55], a[216:219], a[132:135], v[52:55]\n"
    "s_xor_b32 s91, s91, 0x101800\n"
    "v_and_b32 v247, s58, v247\n"
    "v_add_f32_dpp v254, v253, v200 row_newbcast:8 row_mask:0xf bank_mask:0xf\n"
    "v_add_f32_dpp v255, v253, v202 row_newbcast:10 row_mask:0xf bank_mask:0xf\n"
    "v_mul_f32 v244, v244, v254\n"
    "v_mfma_f32_16x16x32_bf16 v[52:55], a[220:223], a[164:167], v[52:55]\n"
    "v_mul_f32 v246, v246, v255\n"
    "v_add_f32_dpp v254, v253, v201 row_newbcast:9 row_mask:0xf bank_mask:0xf\n"
    "v_add_f32_dpp v255, v253, v203 row_newbcast:11 row_mask:0xf bank_mask:0xf\n"
    "v_mul_f32 v245, v245, v254\n"
    "v_mfma_f32_16x16x32_bf16 v[20:23], a[220:223], a[160:163], v[20:23]\n"
    "v_mul_f32 v247, v247, v255\n"
    "v_cvt_pk_bf16_f32 v248, v244, v245\n"
    "v_cvt_pk_bf16_f32 v249, v246, v247\n"
    "v_mfma_f32_16x16x32_bf16 v[20:23], a[216:219], a[128:131], v[20:23]\n"
    "s_waitcnt vmcnt(14)\n"
    "v_accvgpr_read_b32 v245, a58\n"
    "v_accvgpr_read_b32 v247, a59\n"
    "v_lshlrev_b32 v244, 16, v245\n"
    "v_lshlrev_b32 v246, 16, v247\n"
    "v_mfma_f32_16x16x32_bf16 v[24:27], a[224:227], a[128:131], v[24:27]\n"
    "v_and_b32 v245, s58, v245\n"
    "v_and_b32 v247, s58, v247\n"
    "v_add_f32_dpp v254, v253, v204 row_newbcast:12 row_mask:0xf bank_mask:0xf\n"
    "v_mfma_f32_16x16x32_bf16 v[24:27], a[228:231], a[160:163], v[24:27]\n"
    "v_add_f32_dpp v255, v253, v206 row_newbcast:14 row_mask:0xf bank_mask:0xf\n"
    "v_mul_f32 v244, v244, v254\n"
    "v_mul_f32 v246, v246, v255\n"
    "v_mfma_f32_16x16x32_bf16 v[56:59], a[228:231], a[164:167], v[56:59]\n"
    "v_add_f32_dpp v254, v253, v205 row_newbcast:13 row_mask:0xf bank_mask:0xf\n"
    "v_add_f32_dpp v255, v253, v207 row_newbcast:15 row_mask:0xf bank_mask:0xf\n"
    "v_mul_f32 v245, v245, v254\n"
    "v_mul_f32 v247, v247, v255\n"
    "v_mfma_f32_16x16x32_bf16 v[56:59], a[224:227], a[132:135], v[56:59]\n"
    "v_cvt_pk_bf16_f32 v250, v244, v245\n"
    "v_cvt_pk_bf16_f32 v251, v246, v247\n"
    "v_accvgpr_read_b32 v245, a60\n"
    "v_accvgpr_read_b32 v247, a61\n"
    "v_mfma_f32_16x16x32_bf16 v[88:91], a[224:227], a[136:139], v[88:91]\n"
    "buffer_store_dwordx4 v[248:251], v240, s[32:35], s91 offen sc1\n"
    "ds_write_b128 v243, v[248:251] offset:45056\n"
    "v_lshlrev_b32 v244, 16, v245\n"
    "v_lshlrev_b32 v246, 16, v247\n"
    "v_and_b32 v245, s58, v245\n"
    "v_mfma_f32_16x16x32_bf16 v[88:91], a[228:231], a[168:171], v[88:91]\n"
    "buffer_load_dwordx4 a[216:219], v240, s[20:23], s53 offen offset:2048 sc1\n"
    "v_and_b32 v247, s58, v247\n"
    "v_add_f32_dpp v254, v253, v232 row_newbcast:8 row_mask:0xf bank_mask:0xf\n"
    "v_add_f32_dpp v255, v253, v234 row_newbcast:10 row_mask:0xf bank_mask:0xf\n"
    "v_mul_f32 v244, v244, v254\n"
    "v_mfma_f32_16x16x32_bf16 v[120:123], a[228:231], a[172:175], v[120:123]\n"
    "buffer_load_dwordx4 a[220:223], v240, s[20:23], s53 offen offset:3072 sc1\n"
    "s_addk_i32 s53, 4096\n"
    "s_xor_b32 s91, s91, 0x800\n"
    "v_mul_f32 v246, v246, v255\n"
    "v_add_f32_dpp v254, v253, v233 row_newbcast:9 row_mask:0xf bank_mask:0xf\n"
    "v_add_f32_dpp v255, v253, v235 row_newbcast:11 row_mask:0xf bank_mask:0xf\n"
    "v_mul_f32 v245, v245, v254\n"
    "v_mfma_f32_16x16x32_bf16 v[120:123], a[224:227], a[140:143], v[120:123]\n"
    "v_mul_f32 v247, v247, v255\n"
    "v_cvt_pk_bf16_f32 v248, v244, v245\n"
    "v_cvt_pk_bf16_f32 v249, v246, v247\n"
    "v_mfma_f32_16x16x32_bf16 v[152:155], a[224:227], a[144:147], v[152:155]\n"
    "v_accvgpr_read_b32 v245, a62\n"
    "v_accvgpr_read_b32 v247, a63\n"
    "v_lshlrev_b32 v244, 16, v245\n"
    "v_mfma_f32_16x16x32_bf16 v[152:155], a[228:231], a[176:179], v[152:155]\n"
    "v_lshlrev_b32 v246, 16, v247\n"
    "v_and_b32 v245, s58, v245\n"
    "v_and_b32 v247, s58, v247\n"
    "v_add_f32_dpp v254, v253, v236 row_newbcast:12 row_mask:0xf bank_mask:0xf\n"
    "v_mfma_f32_16x16x32_bf16 v[184:187], a[228:231], a[180:183], v[184:187]\n"
    "v_add_f32_dpp v255, v253, v238 row_newbcast:14 row_mask:0xf bank_mask:0xf\n"
    "v_mul_f32 v244, v244, v254\n"
    "v_mul_f32 v246, v246, v255\n"
    "v_mfma_f32_16x16x32_bf16 v[184:187], a[224:227], a[148:151], v[184:187]\n"
    "v_add_f32_dpp v254, v253, v237 row_newbcast:13 row_mask:0xf bank_mask:0xf\n"
    "v_add_f32_dpp v255, v253, v239 row_newbcast:15 row_mask:0xf bank_mask:0xf\n"
    "v_mul_f32 v245, v245, v254\n"
    "v_mul_f32 v247, v247, v255\n"
    "v_mfma_f32_16x16x32_bf16 v[216:219], a[224:227], a[152:155], v[216:219]\n"
    "v_cvt_pk_bf16_f32 v250, v244, v245\n"
    "v_cvt_pk_bf16_f32 v251, v246, v247\n"
    "v_mfma_f32_16x16x32_bf16 v[216:219], a[228:231], a[184:187], v[216:219]\n"
    "buffer_store_dwordx4 v[248:251], v240, s[32:35], s91 offen sc1\n"
    "ds_write_b128 v243, v[248:251] offset:47104\n"
    "s_waitcnt lgkmcnt(0)\n"
    "s_barrier\n"
    "v_mfma_f32_16x16x32_bf16 a[248:251], a[228:231], a[188:191], a[248:251]\n"
    "v_mfma_f32_16x16x32_bf16 a[248:251], a[224:227], a[156:159], a[248:251]\n"
    "s_waitcnt vmcnt(14)\n"
    "v_mfma_f32_16x16x32_bf16 a[252:255], a[232:235], a[156:159], a[252:255]\n"
    "buffer_load_dwordx4 a[224:227], v240, s[20:23], s53 offen offset:0 sc1\n"
    "v_mfma_f32_16x16x32_bf16 a[252:255], a[236:239], a[188:191], a[252:255]\n"
    "buffer_load_dwordx4 a[228:231], v240, s[20:23], s53 offen offset:1024 sc1\n"
    "v_mfma_f32_16x16x32_bf16 v[220:223], a[236:239], a[184:187], v[220:223]\n"
    "v_add_u32 v241, s66, v240\n"
    "ds_read_b128 a[156:159], v241 offset:14336\n"
    "v_mfma_f32_16x16x32_bf16 v[220:223], a[232:235], a[152:155], v[220:223]\n"
    "ds_read_b128 a[188:191], v241 offset:15360\n"
    "v_mfma_f32_16x16x32_bf16 v[188:191], a[232:235], a[148:151], v[188:191]\n"
    "ds_read_b128 a[184:187], v241 offset:13312\n"
    "v_mfma_f32_16x16x32_bf16 v[188:191], a[236:239], a[180:183], v[188:191]\n"
    "ds_read_b128 a[152:155], v241 offset:12288\n"
    "v_mfma_f32_16x16x32_bf16 v[156:159], a[236:239], a[176:179], v[156:159]\n"
    "ds_read_b128 a[148:151], v241 offset:10240\n"
    "v_mfma_f32_16x16x32_bf16 v[156:159], a[232:235], a[144:147], v[156:159]\n"
    "ds_read_b128 a[180:183], v241 offset:11264\n"
    "v_mfma_f32_16x16x32_bf16 v[124:127], a[232:235], a[140:143], v[124:127]\n"
    "ds_read_b128 a[176:179], v241 offset:9216\n"
    "v_mfma_f32_16x16x32_bf16 v[124:127], a[236:239], a[172:175], v[124:127]\n"
    "ds_read_b128 a[144:147], v241 offset:8192\n"
    "v_mfma_f32_16x16x32_bf16 v[92:95], a[236:239], a[168:171], v[92:95]\n"
    "ds_read_b128 a[140:143], v241 offset:6144\n"
    "v_mfma_f32_16x16x32_bf16 v[92:95], a[232:235], a[136:139], v[92:95]\n"
    "ds_read_b128 a[172:175], v241 offset:7168\n"
    "v_mfma_f32_16x16x32_bf16 v[60:63], a[232:235], a[132:135], v[60:63]\n"
    "ds_read_b128 a[168:171], v241 offset:5120\n"
    "v_mfma_f32_16x16x32_bf16 v[60:63], a[236:239], a[164:167], v[60:63]\n"
    "ds_read_b128 a[136:139], v241 offset:4096\n"
    "v_mfma_f32_16x16x32_bf16 v[28:31], a[236:239], a[160:163], v[28:31]\n"
    "ds_read_b128 a[132:135], v241 offset:2048\n"
    "v_mfma_f32_16x16x32_bf16 v[28:31], a[232:235], a[128:131], v[28:31]\n"
    "ds_read_b128 a[164:167], v241 offset:3072\n"
    "ds_read_b128 a[160:163], v241 offset:1024\n"
    "ds_read_b128 a[128:131], v241 offset:0\n"
    "buffer_load_dwordx4 a[232:235], v240, s[20:23], s53 offen offset:2048 sc1\n"
    "buffer_load_dwordx4 a[236:239], v240, s[20:23], s53 offen offset:3072 sc1\n"
    "s_addk_i32 s53, 4096\n"
    "LOOP_%=:\n"
    "s_waitcnt vmcnt(10) lgkmcnt(14)\n"
    "v_mfma_f32_16x16x32_bf16 v[224:227], a[192:195], a[156:159], v[224:227]\n"
    "s_add_u32 s52, s52, 2\n"
    "s_xor_b32 s54, s54, 0x40000\n"
    "s_xor_b32 s59, s59, 0x40000\n"
    "v_mfma_f32_16x16x32_bf16 v[224:227], a[196:199], a[188:191], v[224:227]\n"
    "s_waitcnt lgkmcnt(12)\n"
    "s_addk_i32 s55, 2048\n"
    "s_xor_b32 s57, s57, 65536\n"
    "v_mfma_f32_16x16x32_bf16 v[192:195], a[196:199], a[184:187], v[192:195]\n"
    "s_xor_b32 s66, s66, 65536\n"
    "s_xor_b32 s71, s71, 65536\n"
    "v_xor_b32 v243, 0x10000, v243\n"
    "v_mfma_f32_16x16x32_bf16 v[192:195], a[192:195], a[152:155], v[192:195]\n"
    "s_waitcnt lgkmcnt(10)\n"
    "v_accvgpr_read_b32 v245, a64\n"
    "v_accvgpr_read_b32 v247, a65\n"
    "v_mfma_f32_16x16x32_bf16 v[160:163], a[192:195], a[148:151], v[160:163]\n"
    "v_lshlrev_b32 v244, 16, v245\n"
    "v_lshlrev_b32 v246, 16, v247\n"
    "v_mfma_f32_16x16x32_bf16 v[160:163], a[196:199], a[180:183], v[160:163]\n"
    "s_waitcnt lgkmcnt(8)\n"
    "v_and_b32 v245, s58, v245\n"
    "v_and_b32 v247, s58, v247\n"
    "v_mfma_f32_16x16x32_bf16 v[128:131], a[196:199], a[176:179], v[128:131]\n"
    "v_add_f32_dpp v254, v252, v16 row_newbcast:0 row_mask:0xf bank_mask:0xf\n"
    "v_add_f32_dpp v255, v252, v18 row_newbcast:2 row_mask:0xf bank_mask:0xf\n"
    "v_mfma_f32_16x16x32_bf16 v[128:131], a[192:195], a[144:147], v[128:131]\n"
    "s_waitcnt lgkmcnt(6)\n"
    "v_mul_f32 v244, v244, v254\n"
    "v_mul_f32 v246, v246, v255\n"
    "v_mfma_f32_16x16x32_bf16 v[96:99], a[192:195], a[140:143], v[96:99]\n"
    "v_add_f32_dpp v254, v252, v17 row_newbcast:1 row_mask:0xf bank_mask:0xf\n"
    "v_add_f32_dpp v255, v252, v19 row_newbcast:3 row_mask:0xf bank_mask:0xf\n"
    "v_mfma_f32_16x16x32_bf16 v[96:99], a[196:199], a[172:175], v[96:99]\n"
    "s_waitcnt lgkmcnt(4)\n"
    "v_mul_f32 v245, v245, v254\n"
    "v_mul_f32 v247, v247, v255\n"
    "v_mfma_f32_16x16x32_bf16 v[64:67], a[196:199], a[168:171], v[64:67]\n"
    "v_cvt_pk_bf16_f32 v248, v244, v245\n"
    "v_cvt_pk_bf16_f32 v249, v246, v247\n"
    "v_mfma_f32_16x16x32_bf16 v[64:67], a[192:195], a[136:139], v[64:67]\n"
    "s_waitcnt lgkmcnt(2)\n"
    "v_accvgpr_read_b32 v245, a66\n"
    "v_accvgpr_read_b32 v247, a67\n"
    "v_mfma_f32_16x16x32_bf16 v[32:35], a[192:195], a[132:135], v[32:35]\n"
    "v_lshlrev_b32 v244, 16, v245\n"
    "v_lshlrev_b32 v246, 16, v247\n"
    "v_mfma_f32_16x16x32_bf16 v[32:35], a[196:199], a[164:167], v[32:35]\n"
    "s_waitcnt lgkmcnt(0)\n"
    "v_and_b32 v245, s58, v245\n"
    "v_and_b32 v247, s58, v247\n"
    "v_mfma_f32_16x16x32_bf16 v[0:3], a[196:199], a[160:163], v[0:3]\n"
    "s_xor_b32 s91, s54, 0x20000\n"
    "v_add_f32_dpp v254, v252, v20 row_newbcast:4 row_mask:0xf bank_mask:0xf\n"
    "v_add_f32_dpp v255, v252, v22 row_newbcast:6 row_mask:0xf bank_mask:0xf\n"
    "v_mfma_f32_16x16x32_bf16 v[0:3], a[192:195], a[128:131], v[0:3]\n"
    "s_waitcnt vmcnt(8)\n"
    "v_mul_f32 v244, v244, v254\n"
    "v_mul_f32 v246, v246, v255\n"
    "v_mfma_f32_16x16x32_bf16 v[4:7], a[200:203], a[128:131], v[4:7]\n"
    "buffer_load_dwordx4 a[192:195], v240, s[20:23], s53 offen offset:0 sc1\n"
    "v_mfma_f32_16x16x32_bf16 v[4:7], a[204:207], a[160:163], v[4:7]\n"
    "buffer_load_dwordx4 a[196:199], v240, s[20:23], s53 offen offset:1024 sc1\n"
    "v_mfma_f32_16x16x32_bf16 v[36:39], a[204:207], a[164:167], v[36:39]\n"
    "v_add_f32_dpp v254, v252, v21 row_newbcast:5 row_mask:0xf bank_mask:0xf\n"
    "v_add_f32_dpp v255, v252, v23 row_newbcast:7 row_mask:0xf bank_mask:0xf\n"
    "v_mfma_f32_16x16x32_bf16 v[36:39], a[200:203], a[132:135], v[36:39]\n"
    "v_mul_f32 v245, v245, v254\n"
    "v_mul_f32 v247, v247, v255\n"
    "v_mfma_f32_16x16x32_bf16 v[68:71], a[200:203], a[136:139], v[68:71]\n"
    "v_cvt_pk_bf16_f32 v250, v244, v245\n"
    "v_cvt_pk_bf16_f32 v251, v246, v247\n"
    "v_mfma_f32_16x16x32_bf16 v[68:71], a[204:207], a[168:171], v[68:71]\n"
    "v_accvgpr_read_b32 v245, a68\n"
    "v_accvgpr_read_b32 v247, a69\n"
    "v_mfma_f32_16x16x32_bf16 v[100:103], a[204:207], a[172:175], v[100:103]\n"
    "v_lshlrev_b32 v244, 16, v245\n"
    "v_lshlrev_b32 v246, 16, v247\n"
    "v_mfma_f32_16x16x32_bf16 v[100:103], a[200:203], a[140:143], v[100:103]\n"
    "v_and_b32 v245, s58, v245\n"
    "v_and_b32 v247, s58, v247\n"
    "v_mfma_f32_16x16x32_bf16 v[132:135], a[200:203], a[144:147], v[132:135]\n"
    "v_add_f32_dpp v254, v252, v48 row_newbcast:0 row_mask:0xf bank_mask:0xf\n"
    "v_add_f32_dpp v255, v252, v50 row_newbcast:2 row_mask:0xf bank_mask:0xf\n"
    "v_mfma_f32_16x16x32_bf16 v[132:135], a[204:207], a[176:179], v[132:135]\n"
    "v_mul_f32 v244, v244, v254\n"
    "v_mul_f32 v246, v246, v255\n"
    "v_mfma_f32_16x16x32_bf16 v[164:167], a[204:207], a[180:183], v[164:167]\n"
    "v_add_f32_dpp v254, v252, v49 row_newbcast:1 row_mask:0xf bank_mask:0xf\n"
    "v_add_f32_dpp v255, v252, v51 row_newbcast:3 row_mask:0xf bank_mask:0xf\n"
    "v_mfma_f32_16x16x32_bf16 v[164:167], a[200:203], a[148:151], v[164:167]\n"
    "v_mul_f32 v245, v245, v254\n"
    "v_mul_f32 v247, v247, v255\n"
    "v_mfma_f32_16x16x32_bf16 v[196:199], a[200:203], a[152:155], v[196:199]\n"
    "v_mfma_f32_16x16x32_bf16 v[196:199], a[204:207], a[184:187], v[196:199]\n"
    "buffer_store_dwordx4 v[248:251], v240, s[32:35], s91 offen sc1\n"
    "ds_write_b128 v243, v[248:251] offset:0\n"
    "v_mfma_f32_16x16x32_bf16 v[228:231], a[204:207], a[188:191], v[228:231]\n"
    "v_cvt_pk_bf16_f32 v248, v244, v245\n"
    "v_cvt_pk_bf16_f32 v249, v246, v247\n"
    "v_mfma_f32_16x16x32_bf16 v[228:231], a[200:203], a[156:159], v[228:231]\n"
    "s_waitcnt vmcnt(9)\n"
    "v_accvgpr_read_b32 v245, a70\n"
    "v_accvgpr_read_b32 v247, a71\n"
    "v_mfma_f32_16x16x32_bf16 v[232:235], a[208:211], a[156:159], v[232:235]\n"
    "buffer_load_dwordx4 a[200:203], v240, s[20:23], s53 offen offset:2048 sc1\n"
    "v_mfma_f32_16x16x32_bf16 v[232:235], a[212:215], a[188:191], v[232:235]\n"
    "buffer_load_dwordx4 a[204:207], v240, s[20:23], s53 offen offset:3072 sc1\n"
    "v_mfma_f32_16x16x32_bf16 v[200:203], a[212:215], a[184:187], v[200:203]\n"
    "s_addk_i32 s53, 4096\n"
    "v_lshlrev_b32 v244, 16, v245\n"
    "v_lshlrev_b32 v246, 16, v247\n"
    "v_mfma_f32_16x16x32_bf16 v[200:203], a[208:211], a[152:155], v[200:203]\n"
    "v_and_b32 v245, s58, v245\n"
    "v_and_b32 v247, s58, v247\n"
    "v_mfma_f32_16x16x32_bf16 v[168:171], a[208:211], a[148:151], v[168:171]\n"
    "s_xor_b32 s91, s91, 0x800\n"
    "v_add_f32_dpp v254, v252, v52 row_newbcast:4 row_mask:0xf bank_mask:0xf\n"
    "v_add_f32_dpp v255, v252, v54 row_newbcast:6 row_mask:0xf bank_mask:0xf\n"
    "v_mfma_f32_16x16x32_bf16 v[168:171], a[212:215], a[180:183], v[168:171]\n"
    "v_mul_f32 v244, v244, v254\n"
    "v_mul_f32 v246, v246, v255\n"
    "v_mfma_f32_16x16x32_bf16 v[136:139], a[212:215], a[176:179], v[136:139]\n"
    "v_add_f32_dpp v254, v252, v53 row_newbcast:5 row_mask:0xf bank_mask:0xf\n"
    "v_add_f32_dpp v255, v252, v55 row_newbcast:7 row_mask:0xf bank_mask:0xf\n"
    "v_mfma_f32_16x16x32_bf16 v[136:139], a[208:211], a[144:147], v[136:139]\n"
    "v_mul_f32 v245, v245, v254\n"
    "v_mul_f32 v247, v247, v255\n"
    "v_mfma_f32_16x16x32_bf16 v[104:107], a[208:211], a[140:143], v[104:107]\n"
    "v_cvt_pk_bf16_f32 v250, v244, v245\n"
    "v_cvt_pk_bf16_f32 v251, v246, v247\n"
    "v_mfma_f32_16x16x32_bf16 v[104:107], a[212:215], a[172:175], v[104:107]\n"
    "v_accvgpr_read_b32 v245, a72\n"
    "v_accvgpr_read_b32 v247, a73\n"
    "v_mfma_f32_16x16x32_bf16 v[72:75], a[212:215], a[168:171], v[72:75]\n"
    "v_lshlrev_b32 v244, 16, v245\n"
    "v_lshlrev_b32 v246, 16, v247\n"
    "v_mfma_f32_16x16x32_bf16 v[72:75], a[208:211], a[136:139], v[72:75]\n"
    "v_and_b32 v245, s58, v245\n"
    "v_and_b32 v247, s58, v247\n"
    "v_mfma_f32_16x16x32_bf16 v[40:43], a[208:211], a[132:135], v[40:43]\n"
    "v_add_f32_dpp v254, v252, v80 row_newbcast:0 row_mask:0xf bank_mask:0xf\n"
    "v_add_f32_dpp v255, v252, v82 row_newbcast:2 row_mask:0xf bank_mask:0xf\n"
    "v_mfma_f32_16x16x32_bf16 v[40:43], a[212:215], a[164:167], v[40:43]\n"
    "v_mul_f32 v244, v244, v254\n"
    "v_mul_f32 v246, v246, v255\n"
    "v_mfma_f32_16x16x32_bf16 v[8:11], a[212:215], a[160:163], v[8:11]\n"
    "v_add_f32_dpp v254, v252, v81 row_newbcast:1 row_mask:0xf bank_mask:0xf\n"
    "v_add_f32_dpp v255, v252, v83 row_newbcast:3 row_mask:0xf bank_mask:0xf\n"
    "v_mfma_f32_16x16x32_bf16 v[8:11], a[208:211], a[128:131], v[8:11]\n"
    "s_waitcnt vmcnt(9)\n"
    "v_mul_f32 v245, v245, v254\n"
    "v_mul_f32 v247, v247, v255\n"
    "v_mfma_f32_16x16x32_bf16 v[12:15], a[216:219], a[128:131], v[12:15]\n"
    "buffer_load_dwordx4 a[208:211], v240, s[20:23], s53 offen offset:0 sc1\n"
    "v_mfma_f32_16x16x32_bf16 v[12:15], a[220:223], a[160:163], v[12:15]\n"
    "buffer_load_dwordx4 a[212:215], v240, s[20:23], s53 offen offset:1024 sc1\n"
    "v_mfma_f32_16x16x32_bf16 v[44:47], a[220:223], a[164:167], v[44:47]\n"
    "ds_read_b128 a[128:131], v241 offset:16384\n"
    "v_mfma_f32_16x16x32_bf16 v[44:47], a[216:219], a[132:135], v[44:47]\n"
    "buffer_store_dwordx4 v[248:251], v240, s[32:35], s91 offen sc1\n"
    "ds_write_b128 v243, v[248:251] offset:2048\n"
    "v_mfma_f32_16x16x32_bf16 v[76:79], a[216:219], a[136:139], v[76:79]\n"
    "ds_read_b128 a[160:163], v241 offset:17408\n"
    "ds_read_b128 a[164:167], v241 offset:19456\n"
    "v_mfma_f32_16x16x32_bf16 v[76:79], a[220:223], a[168:171], v[76:79]\n"
    "ds_read_b128 a[132:135], v241 offset:18432\n"
    "v_cvt_pk_bf16_f32 v248, v244, v245\n"
    "v_mfma_f32_16x16x32_bf16 v[108:111], a[220:223], a[172:175], v[108:111]\n"
    "ds_read_b128 a[136:139], v241 offset:20480\n"
    "v_cvt_pk_bf16_f32 v249, v246, v247\n"
    "v_mfma_f32_16x16x32_bf16 v[108:111], a[216:219], a[140:143], v[108:111]\n"
    "ds_read_b128 a[168:171], v241 offset:21504\n"
    "v_accvgpr_read_b32 v245, a74\n"
    "v_mfma_f32_16x16x32_bf16 v[140:143], a[216:219], a[144:147], v[140:143]\n"
    "ds_read_b128 a[172:175], v241 offset:23552\n"
    "v_accvgpr_read_b32 v247, a75\n"
    "v_mfma_f32_16x16x32_bf16 v[140:143], a[220:223], a[176:179], v[140:143]\n"
    "ds_read_b128 a[140:143], v241 offset:22528\n"
    "v_lshlrev_b32 v244, 16, v245\n"
    "v_mfma_f32_16x16x32_bf16 v[172:175], a[220:223], a[180:183], v[172:175]\n"
    "ds_read_b128 a[144:147], v241 offset:24576\n"
    "v_lshlrev_b32 v246, 16, v247\n"
    "v_mfma_f32_16x16x32_bf16 v[172:175], a[216:219], a[148:151], v[172:175]\n"
    "ds_read_b128 a[176:179], v241 offset:25600\n"
    "v_and_b32 v245, s58, v245\n"
    "v_mfma_f32_16x16x32_bf16 v[204:207], a[216:219], a[152:155], v[204:207]\n"
    "ds_read_b128 a[180:183], v241 offset:27648\n"
    "v_and_b32 v247, s58, v247\n"
    "v_mfma_f32_16x16x32_bf16 v[204:207], a[220:223], a[184:187], v[204:207]\n"
    "ds_read_b128 a[148:151], v241 offset:26624\n"
    "v_add_f32_dpp v254, v252, v84 row_newbcast:4 row_mask:0xf bank_mask:0xf\n"
    "v_mfma_f32_16x16x32_bf16 v[236:239], a[220:223], a[188:191], v[236:239]\n"
    "ds_read_b128 a[152:155], v241 offset:28672\n"
    "s_xor_b32 s91, s91, 0x101800\n"
    "v_mfma_f32_16x16x32_bf16 v[236:239], a[216:219], a[156:159], v[236:239]\n"
    "s_waitcnt vmcnt(10) lgkmcnt(11)\n"
    "ds_read_b128 a[184:187], v241 offset:29696\n"
    "v_add_f32_dpp v255, v252, v86 row_newbcast:6 row_mask:0xf bank_mask:0xf\n"
    "v_mfma_f32_16x16x32_bf16 v[0:3], a[224:227], a[128:131], v[0:3]\n"
    "buffer_load_dwordx4 a[216:219], v240, s[20:23], s53 offen offset:2048 sc1\n"
    "v_mfma_f32_16x16x32_bf16 v[0:3], a[228:231], a[160:163], v[0:3]\n"
    "s_waitcnt lgkmcnt(10)\n"
    "v_mul_f32 v244, v244, v254\n"
    "v_mul_f32 v246, v246, v255\n"
    "v_mfma_f32_16x16x32_bf16 v[32:35], a[228:231], a[164:167], v[32:35]\n"
    "buffer_load_dwordx4 a[220:223], v240, s[20:23], s53 offen offset:3072 sc1\n"
    "v_mfma_f32_16x16x32_bf16 v[32:35], a[224:227], a[132:135], v[32:35]\n"
    "s_waitcnt lgkmcnt(8)\n"
    "s_addk_i32 s53, 4096\n"
    "ds_read_b128 a[188:191], v241 offset:31744\n"
    "v_mfma_f32_16x16x32_bf16 v[64:67], a[224:227], a[136:139], v[64:67]\n"
    "ds_read_b128 a[156:159], v241 offset:30720\n"
    "v_add_f32_dpp v254, v252, v85 row_newbcast:5 row_mask:0xf bank_mask:0xf\n"
    "v_mfma_f32_16x16x32_bf16 v[64:67], a[228:231], a[168:171], v[64:67]\n"
    "s_waitcnt lgkmcnt(8)\n"
    "v_add_f32_dpp v255, v252, v87 row_newbcast:7 row_mask:0xf bank_mask:0xf\n"
    "v_mul_f32 v245, v245, v254\n"
    "v_mfma_f32_16x16x32_bf16 v[96:99], a[228:231], a[172:175], v[96:99]\n"
    "v_mul_f32 v247, v247, v255\n"
    "v_cvt_pk_bf16_f32 v250, v244, v245\n"
    "v_mfma_f32_16x16x32_bf16 v[96:99], a[224:227], a[140:143], v[96:99]\n"
    "s_waitcnt lgkmcnt(6)\n"
    "v_cvt_pk_bf16_f32 v251, v246, v247\n"
    "v_accvgpr_read_b32 v245, a76\n"
    "v_mfma_f32_16x16x32_bf16 v[128:131], a[224:227], a[144:147], v[128:131]\n"
    "v_accvgpr_read_b32 v247, a77\n"
    "v_lshlrev_b32 v244, 16, v245\n"
    "v_mfma_f32_16x16x32_bf16 v[128:131], a[228:231], a[176:179], v[128:131]\n"
    "s_waitcnt lgkmcnt(4)\n"
    "v_lshlrev_b32 v246, 16, v247\n"
    "v_and_b32 v245, s58, v245\n"
    "v_mfma_f32_16x16x32_bf16 v[160:163], a[228:231], a[180:183], v[160:163]\n"
    "v_and_b32 v247, s58, v247\n"
    "v_add_f32_dpp v254, v252, v112 row_newbcast:0 row_mask:0xf bank_mask:0xf\n"
    "v_mfma_f32_16x16x32_bf16 v[160:163], a[224:227], a[148:151], v[160:163]\n"
    "s_waitcnt lgkmcnt(2)\n"
    "v_add_f32_dpp v255, v252, v114 row_newbcast:2 row_mask:0xf bank_mask:0xf\n"
    "v_mul_f32 v244, v244, v254\n"
    "v_mfma_f32_16x16x32_bf16 v[192:195], a[224:227], a[152:155], v[192:195]\n"
    "v_mul_f32 v246, v246, v255\n"
    "v_add_f32_dpp v254, v252, v113 row_newbcast:1 row_mask:0xf bank_mask:0xf\n"
    "v_mfma_f32_16x16x32_bf16 v[192:195], a[228:231], a[184:187], v[192:195]\n"
    "s_waitcnt lgkmcnt(0)\n"
    "v_add_f32_dpp v255, v252, v115 row_newbcast:3 row_mask:0xf bank_mask:0xf\n"
    "v_mul_f32 v245, v245, v254\n"
    "v_mfma_f32_16x16x32_bf16 v[224:227], a[228:231], a[188:191], v[224:227]\n"
    "v_mul_f32 v247, v247, v255\n"
    "v_mfma_f32_16x16x32_bf16 v[224:227], a[224:227], a[156:159], v[224:227]\n"
    "s_waitcnt vmcnt(10)\n"
    "v_mfma_f32_16x16x32_bf16 v[228:231], a[232:235], a[156:159], v[228:231]\n"
    "buffer_load_dwordx4 a[224:227], v240, s[20:23], s53 offen offset:0 sc1\n"
    "v_mfma_f32_16x16x32_bf16 v[228:231], a[236:239], a[188:191], v[228:231]\n"
    "buffer_load_dwordx4 a[228:231], v240, s[20:23], s53 offen offset:1024 sc1\n"
    "v_mfma_f32_16x16x32_bf16 v[196:199], a[236:239], a[184:187], v[196:199]\n"
    "v_mfma_f32_16x16x32_bf16 v[196:199], a[232:235], a[152:155], v[196:199]\n"
    "buffer_store_dwordx4 v[248:251], v240, s[32:35], s91 offen sc1\n"
    "ds_write_b128 v243, v[248:251] offset:4096\n"
    "v_mfma_f32_16x16x32_bf16 v[164:167], a[232:235], a[148:151], v[164:167]\n"
    "v_cvt_pk_bf16_f32 v248, v244, v245\n"
    "v_cvt_pk_bf16_f32 v249, v246, v247\n"
    "v_mfma_f32_16x16x32_bf16 v[164:167], a[236:239], a[180:183], v[164:167]\n"
    "v_accvgpr_read_b32 v245, a78\n"
    "v_accvgpr_read_b32 v247, a79\n"
    "v_mfma_f32_16x16x32_bf16 v[132:135], a[236:239], a[176:179], v[132:135]\n"
    "v_lshlrev_b32 v244, 16, v245\n"
    "v_lshlrev_b32 v246, 16, v247\n"
    "v_mfma_f32_16x16x32_bf16 v[132:135], a[232:235], a[144:147], v[132:135]\n"
    "v_and_b32 v245, s58, v245\n"
    "v_and_b32 v247, s58, v247\n"
    "v_mfma_f32_16x16x32_bf16 v[100:103], a[232:235], a[140:143], v[100:103]\n"
    "s_xor_b32 s91, s91, 0x800\n"
    "v_add_f32_dpp v254, v252, v116 row_newbcast:4 row_mask:0xf bank_mask:0xf\n"
    "v_add_f32_dpp v255, v252, v118 row_newbcast:6 row_mask:0xf bank_mask:0xf\n"
    "v_mfma_f32_16x16x32_bf16 v[100:103], a[236:239], a[172:175], v[100:103]\n"
    "v_mul_f32 v244, v244, v254\n"
    "v_mul_f32 v246, v246, v255\n"
    "v_mfma_f32_16x16x32_bf16 v[68:71], a[236:239], a[168:171], v[68:71]\n"
    "v_add_f32_dpp v254, v252, v117 row_newbcast:5 row_mask:0xf bank_mask:0xf\n"
    "v_add_f32_dpp v255, v252, v119 row_newbcast:7 row_mask:0xf bank_mask:0xf\n"
    "v_mfma_f32_16x16x32_bf16 v[68:71], a[232:235], a[136:139], v[68:71]\n"
    "v_mul_f32 v245, v245, v254\n"
    "v_mul_f32 v247, v247, v255\n"
    "v_mfma_f32_16x16x32_bf16 v[36:39], a[232:235], a[132:135], v[36:39]\n"
    "v_cvt_pk_bf16_f32 v250, v244, v245\n"
    "v_cvt_pk_bf16_f32 v251, v246, v247\n"
    "v_mfma_f32_16x16x32_bf16 v[36:39], a[236:239], a[164:167], v[36:39]\n"
    "v_accvgpr_read_b32 v245, a80\n"
    "v_accvgpr_read_b32 v247, a81\n"
    "v_mfma_f32_16x16x32_bf16 v[4:7], a[236:239], a[160:163], v[4:7]\n"
    "v_lshlrev_b32 v244, 16, v245\n"
    "v_lshlrev_b32 v246, 16, v247\n"
    "v_mfma_f32_16x16x32_bf16 v[4:7], a[232:235], a[128:131], v[4:7]\n"
    "s_waitcnt vmcnt(11)\n"
    "v_and_b32 v245, s58, v245\n"
    "v_and_b32 v247, s58, v247\n"
    "v_mfma_f32_16x16x32_bf16 v[8:11], a[192:195], a[128:131], v[8:11]\n"
    "buffer_load_dwordx4 a[232:235], v240, s[20:23], s53 offen offset:2048 sc1\n"
    "v_mfma_f32_16x16x32_bf16 v[8:11], a[196:199], a[160:163], v[8:11]\n"
    "buffer_load_dwordx4 a[236:239], v240, s[20:23], s53 offen offset:3072 sc1\n"
    "v_mfma_f32_16x16x32_bf16 v[40:43], a[196:199], a[164:167], v[40:43]\n"
    "s_addk_i32 s53, 4096\n"
    "v_add_f32_dpp v254, v252, v144 row_newbcast:0 row_mask:0xf bank_mask:0xf\n"
    "v_add_f32_dpp v255, v252, v146 row_newbcast:2 row_mask:0xf bank_mask:0xf\n"
    "v_mfma_f32_16x16x32_bf16 v[40:43], a[192:195], a[132:135], v[40:43]\n"
    "v_mul_f32 v244, v244, v254\n"
    "v_mul_f32 v246, v246, v255\n"
    "v_mfma_f32_16x16x32_bf16 v[72:75], a[192:195], a[136:139], v[72:75]\n"
    "v_add_f32_dpp v254, v252, v145 row_newbcast:1 row_mask:0xf bank_mask:0xf\n"
    "v_add_f32_dpp v255, v252, v147 row_newbcast:3 row_mask:0xf bank_mask:0xf\n"
    "v_mfma_f32_16x16x32_bf16 v[72:75], a[196:199], a[168:171], v[72:75]\n"
    "v_mul_f32 v245, v245, v254\n"
    "v_mul_f32 v247, v247, v255\n"
    "v_mfma_f32_16x16x32_bf16 v[104:107], a[196:199], a[172:175], v[104:107]\n"
    "v_mfma_f32_16x16x32_bf16 v[104:107], a[192:195], a[140:143], v[104:107]\n"
    "buffer_store_dwordx4 v[248:251], v240, s[32:35], s91 offen sc1\n"
    "ds_write_b128 v243, v[248:251] offset:6144\n"
    "v_mfma_f32_16x16x32_bf16 v[136:139], a[192:195], a[144:147], v[136:139]\n"
    "v_cvt_pk_bf16_f32 v248, v244, v245\n"
    "v_cvt_pk_bf16_f32 v249, v246, v247\n"
    "v_mfma_f32_16x16x32_bf16 v[136:139], a[196:199], a[176:179], v[136:139]\n"
    "v_accvgpr_read_b32 v245, a82\n"
    "v_accvgpr_read_b32 v247, a83\n"
    "v_mfma_f32_16x16x32_bf16 v[168:171], a[196:199], a[180:183], v[168:171]\n"
    "v_lshlrev_b32 v244, 16, v245\n"
    "v_lshlrev_b32 v246, 16, v247\n"
    "v_mfma_f32_16x16x32_bf16 v[168:171], a[192:195], a[148:151], v[168:171]\n"
    "v_and_b32 v245, s58, v245\n"
    "v_and_b32 v247, s58, v247\n"
    "v_mfma_f32_16x16x32_bf16 v[200:203], a[192:195], a[152:155], v[200:203]\n"
    "s_xor_b32 s91, s91, 0x185800\n"
    "v_add_f32_dpp v254, v252, v148 row_newbcast:4 row_mask:0xf bank_mask:0xf\n"
    "v_add_f32_dpp v255, v252, v150 row_newbcast:6 row_mask:0xf bank_mask:0xf\n"
    "v_mfma_f32_16x16x32_bf16 v[200:203], a[196:199], a[184:187], v[200:203]\n"
    "v_mul_f32 v244, v244, v254\n"
    "v_mul_f32 v246, v246, v255\n"
    "v_mfma_f32_16x16x32_bf16 v[232:235], a[196:199], a[188:191], v[232:235]\n"
    "v_add_f32_dpp v254, v252, v149 row_newbcast:5 row_mask:0xf bank_mask:0xf\n"
    "v_add_f32_dpp v255, v252, v151 row_newbcast:7 row_mask:0xf bank_mask:0xf\n"
    "v_mfma_f32_16x16x32_bf16 v[232:235], a[192:195], a[156:159], v[232:235]\n"
    "s_waitcnt vmcnt(11)\n"
    "v_mul_f32 v245, v245, v254\n"
    "v_mul_f32 v247, v247, v255\n"
    "v_mfma_f32_16x16x32_bf16 v[236:239], a[200:203], a[156:159], v[236:239]\n"
    "buffer_load_dwordx4 a[192:195], v240, s[20:23], s53 offen offset:0 sc1\n"
    "v_mfma_f32_16x16x32_bf16 v[236:239], a[204:207], a[188:191], v[236:239]\n"
    "buffer_load_dwordx4 a[196:199], v240, s[20:23], s53 offen offset:1024 sc1\n"
    "v_mfma_f32_16x16x32_bf16 v[204:207], a[204:207], a[184:187], v[204:207]\n"
    "ds_read_b128 a[156:159], v241 offset:47104\n"
    "v_cvt_pk_bf16_f32 v250, v244, v245\n"
    "v_mfma_f32_16x16x32_bf16 v[204:207], a[200:203], a[152:155], v[204:207]\n"
    "ds_read_b128 a[188:191], v241 offset:48128\n"
    "v_cvt_pk_bf16_f32 v251, v246, v247\n"
    "v_mfma_f32_16x16x32_bf16 v[172:175], a[200:203], a[148:151], v[172:175]\n"
    "ds_read_b128 a[184:187], v241 offset:46080\n"
    "v_accvgpr_read_b32 v245, a84\n"
    "v_mfma_f32_16x16x32_bf16 v[172:175], a[204:207], a[180:183], v[172:175]\n"
    "ds_read_b128 a[152:155], v241 offset:45056\n"
    "v_accvgpr_read_b32 v247, a85\n"
    "v_mfma_f32_16x16x32_bf16 v[140:143], a[204:207], a[176:179], v[140:143]\n"
    "ds_read_b128 a[148:151], v241 offset:43008\n"
    "v_lshlrev_b32 v244, 16, v245\n"
    "v_mfma_f32_16x16x32_bf16 v[140:143], a[200:203], a[144:147], v[140:143]\n"
    "ds_read_b128 a[180:183], v241 offset:44032\n"
    "v_lshlrev_b32 v246, 16, v247\n"
    "v_mfma_f32_16x16x32_bf16 v[108:111], a[200:203], a[140:143], v[108:111]\n"
    "s_add_u32 s62, s52, s64\n"
    "v_mov_b32 v242, s62\n"
    "s_mov_b32 s46, 4\n"
    "v_mfma_f32_16x16x32_bf16 v[108:111], a[204:207], a[172:175], v[108:111]\n"
    "buffer_store_dword v242, v240, s[44:47], 0 offen offset:0 sc1\n"
    "v_mfma_f32_16x16x32_bf16 v[76:79], a[204:207], a[168:171], v[76:79]\n"
    "ds_read_b128 a[176:179], v241 offset:41984\n"
    "ds_read_b128 a[144:147], v241 offset:40960\n"
    "v_mfma_f32_16x16x32_bf16 v[76:79], a[200:203], a[136:139], v[76:79]\n"
    "ds_read_b128 a[140:143], v241 offset:38912\n"
    "ds_read_b128 a[172:175], v241 offset:39936\n"
    "v_mfma_f32_16x16x32_bf16 v[44:47], a[200:203], a[132:135], v[44:47]\n"
    "ds_read_b128 a[168:171], v241 offset:37888\n"
    "v_and_b32 v245, s58, v245\n"
    "v_mfma_f32_16x16x32_bf16 v[44:47], a[204:207], a[164:167], v[44:47]\n"
    "ds_read_b128 a[136:139], v241 offset:36864\n"
    "v_and_b32 v247, s58, v247\n"
    "v_mfma_f32_16x16x32_bf16 v[12:15], a[204:207], a[160:163], v[12:15]\n"
    "ds_read_b128 a[132:135], v241 offset:34816\n"
    "v_add_f32_dpp v254, v252, v176 row_newbcast:0 row_mask:0xf bank_mask:0xf\n"
    "v_mfma_f32_16x16x32_bf16 v[12:15], a[200:203], a[128:131], v[12:15]\n"
    "s_waitcnt vmcnt(12) lgkmcnt(11)\n"
    "ds_read_b128 a[164:167], v241 offset:35840\n"
    "v_add_f32_dpp v255, v252, v178 row_newbcast:2 row_mask:0xf bank_mask:0xf\n"
    "v_mfma_f32_16x16x32_bf16 v[224:227], a[208:211], a[156:159], v[224:227]\n"
    "buffer_load_dwordx4 a[200:203], v240, s[20:23], s53 offen offset:2048 sc1\n"
    "v_mfma_f32_16x16x32_bf16 v[224:227], a[212:215], a[188:191], v[224:227]\n"
    "s_waitcnt lgkmcnt(10)\n"
    "v_mul_f32 v244, v244, v254\n"
    "v_mul_f32 v246, v246, v255\n"
    "v_mfma_f32_16x16x32_bf16 v[192:195], a[212:215], a[184:187], v[192:195]\n"
    "buffer_load_dwordx4 a[204:207], v240, s[20:23], s53 offen offset:3072 sc1\n"
    "v_mfma_f32_16x16x32_bf16 v[192:195], a[208:211], a[152:155], v[192:195]\n"
    "s_waitcnt lgkmcnt(8)\n"
    "s_addk_i32 s53, 4096\n"
    "ds_read_b128 a[160:163], v241 offset:33792\n"
    "v_mfma_f32_16x16x32_bf16 v[160:163], a[208:211], a[148:151], v[160:163]\n"
    "ds_read_b128 a[128:131], v241 offset:32768\n"
    "v_add_f32_dpp v254, v252, v177 row_newbcast:1 row_mask:0xf bank_mask:0xf\n"
    "v_mfma_f32_16x16x32_bf16 v[160:163], a[212:215], a[180:183], v[160:163]\n"
    "s_waitcnt lgkmcnt(8)\n"
    "v_add_f32_dpp v255, v252, v179 row_newbcast:3 row_mask:0xf bank_mask:0xf\n"
    "v_mul_f32 v245, v245, v254\n"
    "v_mfma_f32_16x16x32_bf16 v[128:131], a[212:215], a[176:179], v[128:131]\n"
    "v_mul_f32 v247, v247, v255\n"
    "v_mfma_f32_16x16x32_bf16 v[128:131], a[208:211], a[144:147], v[128:131]\n"
    "s_waitcnt lgkmcnt(6)\n"
    "v_mfma_f32_16x16x32_bf16 v[96:99], a[208:211], a[140:143], v[96:99]\n"
    "buffer_store_dwordx4 v[248:251], v240, s[32:35], s91 offen sc1\n"
    "ds_write_b128 v243, v[248:251] offset:8192\n"
    "v_mfma_f32_16x16x32_bf16 v[96:99], a[212:215], a[172:175], v[96:99]\n"
    "s_waitcnt lgkmcnt(5)\n"
    "v_cvt_pk_bf16_f32 v248, v244, v245\n"
    "v_cvt_pk_bf16_f32 v249, v246, v247\n"
    "v_mfma_f32_16x16x32_bf16 v[64:67], a[212:215], a[168:171], v[64:67]\n"
    "v_accvgpr_read_b32 v245, a86\n"
    "v_accvgpr_read_b32 v247, a87\n"
    "v_mfma_f32_16x16x32_bf16 v[64:67], a[208:211], a[136:139], v[64:67]\n"
    "s_waitcnt lgkmcnt(3)\n"
    "v_lshlrev_b32 v244, 16, v245\n"
    "v_lshlrev_b32 v246, 16, v247\n"
    "v_mfma_f32_16x16x32_bf16 v[32:35], a[208:211], a[132:135], v[32:35]\n"
    "v_and_b32 v245, s58, v245\n"
    "v_and_b32 v247, s58, v247\n"
    "v_mfma_f32_16x16x32_bf16 v[32:35], a[212:215], a[164:167], v[32:35]\n"
    "s_waitcnt lgkmcnt(1)\n"
    "s_xor_b32 s91, s91, 0x800\n"
    "v_add_f32_dpp v254, v252, v180 row_newbcast:4 row_mask:0xf bank_mask:0xf\n"
    "v_mfma_f32_16x16x32_bf16 v[0:3], a[212:215], a[160:163], v[0:3]\n"
    "v_add_f32_dpp v255, v252, v182 row_newbcast:6 row_mask:0xf bank_mask:0xf\n"
    "v_mul_f32 v244, v244, v254\n"
    "v_mfma_f32_16x16x32_bf16 v[0:3], a[208:211], a[128:131], v[0:3]\n"
    "s_waitcnt vmcnt(12)\n"
    "v_mul_f32 v246, v246, v255\n"
    "v_add_f32_dpp v254, v252, v181 row_newbcast:5 row_mask:0xf bank_mask:0xf\n"
    "v_mfma_f32_16x16x32_bf16 v[4:7], a[216:219], a[128:131], v[4:7]\n"
    "buffer_load_dwordx4 a[208:211], v240, s[20:23], s53 offen offset:0 sc1\n"
    "v_mfma_f32_16x16x32_bf16 v[4:7], a[220:223], a[160:163], v[4:7]\n"
    "buffer_load_dwordx4 a[212:215], v240, s[20:23], s53 offen offset:1024 sc1\n"
    "v_mfma_f32_16x16x32_bf16 v[36:39], a[220:223], a[164:167], v[36:39]\n"
    "v_add_f32_dpp v255, v252, v183 row_newbcast:7 row_mask:0xf bank_mask:0xf\n"
    "v_mul_f32 v245, v245, v254\n"
    "v_mfma_f32_16x16x32_bf16 v[36:39], a[216:219], a[132:135], v[36:39]\n"
    "v_mul_f32 v247, v247, v255\n"
    "v_cvt_pk_bf16_f32 v250, v244, v245\n"
    "v_mfma_f32_16x16x32_bf16 v[68:71], a[216:219], a[136:139], v[68:71]\n"
    "v_cvt_pk_bf16_f32 v251, v246, v247\n"
    "v_accvgpr_read_b32 v245, a88\n"
    "v_mfma_f32_16x16x32_bf16 v[68:71], a[220:223], a[168:171], v[68:71]\n"
    "v_accvgpr_read_b32 v247, a89\n"
    "v_lshlrev_b32 v244, 16, v245\n"
    "v_mfma_f32_16x16x32_bf16 v[100:103], a[220:223], a[172:175], v[100:103]\n"
    "v_lshlrev_b32 v246, 16, v247\n"
    "v_and_b32 v245, s58, v245\n"
    "v_mfma_f32_16x16x32_bf16 v[100:103], a[216:219], a[140:143], v[100:103]\n"
    "v_and_b32 v247, s58, v247\n"
    "v_add_f32_dpp v254, v252, v208 row_newbcast:0 row_mask:0xf bank_mask:0xf\n"
    "v_mfma_f32_16x16x32_bf16 v[132:135], a[216:219], a[144:147], v[132:135]\n"
    "v_add_f32_dpp v255, v252, v210 row_newbcast:2 row_mask:0xf bank_mask:0xf\n"
    "v_mul_f32 v244, v244, v254\n"
    "v_mfma_f32_16x16x32_bf16 v[132:135], a[220:223], a[176:179], v[132:135]\n"
    "v_mul_f32 v246, v246, v255\n"
    "v_add_f32_dpp v254, v252, v209 row_newbcast:1 row_mask:0xf bank_mask:0xf\n"
    "v_mfma_f32_16x16x32_bf16 v[164:167], a[220:223], a[180:183], v[164:167]\n"
    "v_add_f32_dpp v255, v252, v211 row_newbcast:3 row_mask:0xf bank_mask:0xf\n"
    "v_mul_f32 v245, v245, v254\n"
    "v_mfma_f32_16x16x32_bf16 v[164:167], a[216:219], a[148:151], v[164:167]\n"
    "v_mul_f32 v247, v247, v255\n"
    "v_mfma_f32_16x16x32_bf16 v[196:199], a[216:219], a[152:155], v[196:199]\n"
    "v_mfma_f32_16x16x32_bf16 v[196:199], a[220:223], a[184:187], v[196:199]\n"
    "buffer_store_dwordx4 v[248:251], v240, s[32:35], s91 offen sc1\n"
    "ds_write_b128 v243, v[248:251] offset:10240\n"
    "v_mfma_f32_16x16x32_bf16 v[228:231], a[220:223], a[188:191], v[228:231]\n"
    "v_cvt_pk_bf16_f32 v248, v244, v245\n"
    "v_cvt_pk_bf16_f32 v249, v246, v247\n"
    "v_mfma_f32_16x16x32_bf16 v[228:231], a[216:219], a[156:159], v[228:231]\n"
    "s_waitcnt vmcnt(13)\n"
    "v_accvgpr_read_b32 v245, a90\n"
    "v_accvgpr_read_b32 v247, a91\n"
    "v_mfma_f32_16x16x32_bf16 v[232:235], a[224:227], a[156:159], v[232:235]\n"
    "buffer_load_dwordx4 a[216:219], v240, s[20:23], s53 offen offset:2048 sc1\n"
    "v_mfma_f32_16x16x32_bf16 v[232:235], a[228:231], a[188:191], v[232:235]\n"
    "buffer_load_dwordx4 a[220:223], v240, s[20:23], s53 offen offset:3072 sc1\n"
    "v_mfma_f32_16x16x32_bf16 v[200:203], a[228:231], a[184:187], v[200:203]\n"
    "s_addk_i32 s53, 4096\n"
    "v_lshlrev_b32 v244, 16, v245\n"
    "v_lshlrev_b32 v246, 16, v247\n"
    "v_mfma_f32_16x16x32_bf16 v[200:203], a[224:227], a[152:155], v[200:203]\n"
    "v_and_b32 v245, s58, v245\n"
    "v_and_b32 v247, s58, v247\n"
    "v_mfma_f32_16x16x32_bf16 v[168:171], a[224:227], a[148:151], v[168:171]\n"
    "s_xor_b32 s91, s91, 0x101800\n"
    "v_add_f32_dpp v254, v252, v212 row_newbcast:4 row_mask:0xf bank_mask:0xf\n"
    "v_add_f32_dpp v255, v252, v214 row_newbcast:6 row_mask:0xf bank_mask:0xf\n"
    "v_mfma_f32_16x16x32_bf16 v[168:171], a[228:231], a[180:183], v[168:171]\n"
    "v_mul_f32 v244, v244, v254\n"
    "v_mul_f32 v246, v246, v255\n"
    "v_mfma_f32_16x16x32_bf16 v[136:139], a[228:231], a[176:179], v[136:139]\n"
    "v_add_f32_dpp v254, v252, v213 row_newbcast:5 row_mask:0xf bank_mask:0xf\n"
    "v_add_f32_dpp v255, v252, v215 row_newbcast:7 row_mask:0xf bank_mask:0xf\n"
    "v_mfma_f32_16x16x32_bf16 v[136:139], a[224:227], a[144:147], v[136:139]\n"
    "v_mul_f32 v245, v245, v254\n"
    "v_mul_f32 v247, v247, v255\n"
    "v_mfma_f32_16x16x32_bf16 v[104:107], a[224:227], a[140:143], v[104:107]\n"
    "v_cvt_pk_bf16_f32 v250, v244, v245\n"
    "v_cvt_pk_bf16_f32 v251, v246, v247\n"
    "v_mfma_f32_16x16x32_bf16 v[104:107], a[228:231], a[172:175], v[104:107]\n"
    "v_accvgpr_read_b32 v245, a92\n"
    "v_accvgpr_read_b32 v247, a93\n"
    "v_mfma_f32_16x16x32_bf16 v[72:75], a[228:231], a[168:171], v[72:75]\n"
    "v_lshlrev_b32 v244, 16, v245\n"
    "v_lshlrev_b32 v246, 16, v247\n"
    "v_mfma_f32_16x16x32_bf16 v[72:75], a[224:227], a[136:139], v[72:75]\n"
    "v_and_b32 v245, s58, v245\n"
    "v_and_b32 v247, s58, v247\n"
    "v_mfma_f32_16x16x32_bf16 v[40:43], a[224:227], a[132:135], v[40:43]\n"
    "v_accvgpr_read_b32 v254, a240\n"
    "v_accvgpr_read_b32 v255, a242\n"
    "v_mfma_f32_16x16x32_bf16 v[40:43], a[228:231], a[164:167], v[40:43]\n"
    "v_add_f32_dpp v254, v252, v254 row_newbcast:0 row_mask:0xf bank_mask:0xf\n"
    "v_add_f32_dpp v255, v252, v255 row_newbcast:2 row_mask:0xf bank_mask:0xf\n"
    "v_mfma_f32_16x16x32_bf16 v[8:11], a[228:231], a[160:163], v[8:11]\n"
    "v_mul_f32 v244, v244, v254\n"
    "v_mul_f32 v246, v246, v255\n"
    "v_mfma_f32_16x16x32_bf16 v[8:11], a[224:227], a[128:131], v[8:11]\n"
    "s_waitcnt vmcnt(12)\n"
    "v_accvgpr_read_b32 v254, a241\n"
    "v_accvgpr_read_b32 v255, a243\n"
    "v_mfma_f32_16x16x32_bf16 v[12:15], a[232:235], a[128:131], v[12:15]\n"
    "buffer_load_dwordx4 a[224:227], v240, s[20:23], s53 offen offset:0 sc1\n"
    "v_mfma_f32_16x16x32_bf16 v[12:15], a[236:239], a[160:163], v[12:15]\n"
    "buffer_load_dwordx4 a[228:231], v240, s[20:23], s53 offen offset:1024 sc1\n"
    "v_mfma_f32_16x16x32_bf16 v[44:47], a[236:239], a[164:167], v[44:47]\n"
    "ds_read_b128 a[128:131], v241 offset:49152\n"
    "v_add_f32_dpp v254, v252, v254 row_newbcast:1 row_mask:0xf bank_mask:0xf\n"
    "v_mfma_f32_16x16x32_bf16 v[44:47], a[232:235], a[132:135], v[44:47]\n"
    "ds_read_b128 a[160:163], v241 offset:50176\n"
    "v_add_f32_dpp v255, v252, v255 row_newbcast:3 row_mask:0xf bank_mask:0xf\n"
    "v_mfma_f32_16x16x32_bf16 v[76:79], a[232:235], a[136:139], v[76:79]\n"
    "ds_read_b128 a[164:167], v241 offset:52224\n"
    "v_mul_f32 v245, v245, v254\n"
    "v_mfma_f32_16x16x32_bf16 v[76:79], a[236:239], a[168:171], v[76:79]\n"
    "ds_read_b128 a[132:135], v241 offset:51200\n"
    "v_mul_f32 v247, v247, v255\n"
    "v_mfma_f32_16x16x32_bf16 v[108:111], a[236:239], a[172:175], v[108:111]\n"
    "ds_read_b128 a[136:139], v241 offset:53248\n"
    "v_mfma_f32_16x16x32_bf16 v[108:111], a[232:235], a[140:143], v[108:111]\n"
    "buffer_store_dwordx4 v[248:251], v240, s[32:35], s91 offen sc1\n"
    "ds_write_b128 v243, v[248:251] offset:12288\n"
    "v_mfma_f32_16x16x32_bf16 v[140:143], a[232:235], a[144:147], v[140:143]\n"
    "ds_read_b128 a[168:171], v241 offset:54272\n"
    "ds_read_b128 a[172:175], v241 offset:56320\n"
    "v_mfma_f32_16x16x32_bf16 v[140:143], a[236:239], a[176:179], v[140:143]\n"
    "ds_read_b128 a[140:143], v241 offset:55296\n"
    "v_cvt_pk_bf16_f32 v248, v244, v245\n"
    "v_mfma_f32_16x16x32_bf16 v[172:175], a[236:239], a[180:183], v[172:175]\n"
    "ds_read_b128 a[144:147], v241 offset:57344\n"
    "v_cvt_pk_bf16_f32 v249, v246, v247\n"
    "v_mfma_f32_16x16x32_bf16 v[172:175], a[232:235], a[148:151], v[172:175]\n"
    "ds_read_b128 a[176:179], v241 offset:58368\n"
    "v_accvgpr_read_b32 v245, a94\n"
    "v_mfma_f32_16x16x32_bf16 v[204:207], a[232:235], a[152:155], v[204:207]\n"
    "ds_read_b128 a[180:183], v241 offset:60416\n"
    "v_accvgpr_read_b32 v247, a95\n"
    "v_mfma_f32_16x16x32_bf16 v[204:207], a[236:239], a[184:187], v[204:207]\n"
    "ds_read_b128 a[148:151], v241 offset:59392\n"
    "v_lshlrev_b32 v244, 16, v245\n"
    "v_mfma_f32_16x16x32_bf16 v[236:239], a[236:239], a[188:191], v[236:239]\n"
    "ds_read_b128 a[152:155], v241 offset:61440\n"
    "v_lshlrev_b32 v246, 16, v247\n"
    "v_mfma_f32_16x16x32_bf16 v[236:239], a[232:235], a[156:159], v[236:239]\n"
    "s_waitcnt vmcnt(12) lgkmcnt(12)\n"
    "ds_read_b128 a[184:187], v241 offset:62464\n"
    "v_and_b32 v245, s58, v245\n"
    "v_mfma_f32_16x16x32_bf16 v[0:3], a[192:195], a[128:131], v[0:3]\n"
    "buffer_load_dwordx4 a[232:235], v240, s[20:23], s53 offen offset:2048 sc1\n"
    "v_mfma_f32_16x16x32_bf16 v[0:3], a[196:199], a[160:163], v[0:3]\n"
    "s_waitcnt lgkmcnt(11)\n"
    "v_and_b32 v247, s58, v247\n"
    "v_accvgpr_read_b32 v254, a244\n"
    "v_mfma_f32_16x16x32_bf16 v[32:35], a[196:199], a[164:167], v[32:35]\n"
    "buffer_load_dwordx4 a[236:239], v240, s[20:23], s53 offen offset:3072 sc1\n"
    "v_mfma_f32_16x16x32_bf16 v[32:35], a[192:195], a[132:135], v[32:35]\n"
    "s_waitcnt lgkmcnt(8)\n"
    "s_addk_i32 s53, 4096\n"
    "ds_read_b128 a[188:191], v241 offset:64512\n"
    "v_mfma_f32_16x16x32_bf16 v[64:67], a[192:195], a[136:139], v[64:67]\n"
    "ds_read_b128 a[156:159], v241 offset:63488\n"
    "v_accvgpr_read_b32 v255, a246\n"
    "v_mfma_f32_16x16x32_bf16 v[64:67], a[196:199], a[168:171], v[64:67]\n"
    "s_waitcnt lgkmcnt(8)\n"
    "v_add_f32_dpp v254, v252, v254 row_newbcast:4 row_mask:0xf bank_mask:0xf\n"
    "v_add_f32_dpp v255, v252, v255 row_newbcast:6 row_mask:0xf bank_mask:0xf\n"
    "v_mfma_f32_16x16x32_bf16 v[96:99], a[196:199], a[172:175], v[96:99]\n"
    "s_xor_b32 s91, s91, 0x800\n"
    "v_mul_f32 v244, v244, v254\n"
    "v_mul_f32 v246, v246, v255\n"
    "v_mfma_f32_16x16x32_bf16 v[96:99], a[192:195], a[140:143], v[96:99]\n"
    "s_waitcnt lgkmcnt(6)\n"
    "v_accvgpr_read_b32 v254, a245\n"
    "v_accvgpr_read_b32 v255, a247\n"
    "v_mfma_f32_16x16x32_bf16 v[128:131], a[192:195], a[144:147], v[128:131]\n"
    "v_add_f32_dpp v254, v252, v254 row_newbcast:5 row_mask:0xf bank_mask:0xf\n"
    "v_add_f32_dpp v255, v252, v255 row_newbcast:7 row_mask:0xf bank_mask:0xf\n"
    "v_mfma_f32_16x16x32_bf16 v[128:131], a[196:199], a[176:179], v[128:131]\n"
    "s_waitcnt lgkmcnt(4)\n"
    "v_mul_f32 v245, v245, v254\n"
    "v_mul_f32 v247, v247, v255\n"
    "v_mfma_f32_16x16x32_bf16 v[160:163], a[196:199], a[180:183], v[160:163]\n"
    "v_cvt_pk_bf16_f32 v250, v244, v245\n"
    "v_cvt_pk_bf16_f32 v251, v246, v247\n"
    "v_mfma_f32_16x16x32_bf16 v[160:163], a[192:195], a[148:151], v[160:163]\n"
    "s_waitcnt lgkmcnt(2)\n"
    "v_mfma_f32_16x16x32_bf16 v[192:195], a[192:195], a[152:155], v[192:195]\n"
    "buffer_store_dwordx4 v[248:251], v240, s[32:35], s91 offen sc1\n"
    "ds_write_b128 v243, v[248:251] offset:14336\n"
    "v_mfma_f32_16x16x32_bf16 v[192:195], a[196:199], a[184:187], v[192:195]\n"
    "s_waitcnt lgkmcnt(1)\n"
    "v_accvgpr_read_b32 v245, a96\n"
    "v_accvgpr_read_b32 v247, a97\n"
    "v_mfma_f32_16x16x32_bf16 v[224:227], a[196:199], a[188:191], v[224:227]\n"
    "v_lshlrev_b32 v244, 16, v245\n"
    "v_lshlrev_b32 v246, 16, v247\n"
    "v_mfma_f32_16x16x32_bf16 v[224:227], a[192:195], a[156:159], v[224:227]\n"
    "s_waitcnt vmcnt(12)\n"
    "v_and_b32 v245, s58, v245\n"
    "v_and_b32 v247, s58, v247\n"
    "v_mfma_f32_16x16x32_bf16 v[228:231], a[200:203], a[156:159], v[228:231]\n"
    "buffer_load_dwordx4 a[192:195], v240, s[20:23], s53 offen offset:0 sc1\n"
    "v_mfma_f32_16x16x32_bf16 v[228:231], a[204:207], a[188:191], v[228:231]\n"
    "buffer_load_dwordx4 a[196:199], v240, s[20:23], s53 offen offset:1024 sc1\n"
    "v_mfma_f32_16x16x32_bf16 v[196:199], a[204:207], a[184:187], v[196:199]\n"
    "v_add_f32_dpp v254, v252, v24 row_newbcast:8 row_mask:0xf bank_mask:0xf\n"
    "v_add_f32_dpp v255, v252, v26 row_newbcast:10 row_mask:0xf bank_mask:0xf\n"
    "v_mfma_f32_16x16x32_bf16 v[196:199], a[200:203], a[152:155], v[196:199]\n"
    "v_mul_f32 v244, v244, v254\n"
    "v_mul_f32 v246, v246, v255\n"
    "v_mfma_f32_16x16x32_bf16 v[164:167], a[200:203], a[148:151], v[164:167]\n"
    "v_add_f32_dpp v254, v252, v25 row_newbcast:9 row_mask:0xf bank_mask:0xf\n"
    "v_add_f32_dpp v255, v252, v27 row_newbcast:11 row_mask:0xf bank_mask:0xf\n"
    "v_mfma_f32_16x16x32_bf16 v[164:167], a[204:207], a[180:183], v[164:167]\n"
    "v_mul_f32 v245, v245, v254\n"
    "v_mul_f32 v247, v247, v255\n"
    "v_mfma_f32_16x16x32_bf16 v[132:135], a[204:207], a[176:179], v[132:135]\n"
    "v_cvt_pk_bf16_f32 v248, v244, v245\n"
    "v_cvt_pk_bf16_f32 v249, v246, v247\n"
    "v_mfma_f32_16x16x32_bf16 v[132:135], a[200:203], a[144:147], v[132:135]\n"
    "v_accvgpr_read_b32 v245, a98\n"
    "v_accvgpr_read_b32 v247, a99\n"
    "v_mfma_f32_16x16x32_bf16 v[100:103], a[200:203], a[140:143], v[100:103]\n"
    "v_lshlrev_b32 v244, 16, v245\n"
    "v_lshlrev_b32 v246, 16, v247\n"
    "v_mfma_f32_16x16x32_bf16 v[100:103], a[204:207], a[172:175], v[100:103]\n"
    "v_and_b32 v245, s58, v245\n"
    "v_and_b32 v247, s58, v247\n"
    "v_mfma_f32_16x16x32_bf16 v[68:71], a[204:207], a[168:171], v[68:71]\n"
    "s_xor_b32 s91, s91, 0x195800\n"
    "v_add_f32_dpp v254, v252, v28 row_newbcast:12 row_mask:0xf bank_mask:0xf\n"
    "v_add_f32_dpp v255, v252, v30 row_newbcast:14 row_mask:0xf bank_mask:0xf\n"
    "v_mfma_f32_16x16x32_bf16 v[68:71], a[200:203], a[136:139], v[68:71]\n"
    "v_mul_f32 v244, v244, v254\n"
    "v_mul_f32 v246, v246, v255\n"
    "v_mfma_f32_16x16x32_bf16 v[36:39], a[200:203], a[132:135], v[36:39]\n"
    "v_add_f32_dpp v254, v252, v29 row_newbcast:13 row_mask:0xf bank_mask:0xf\n"
    "v_add_f32_dpp v255, v252, v31 row_newbcast:15 row_mask:0xf bank_mask:0xf\n"
    "v_mfma_f32_16x16x32_bf16 v[36:39], a[204:207], a[164:167], v[36:39]\n"
    "v_mul_f32 v245, v245, v254\n"
    "v_mul_f32 v247, v247, v255\n"
    "v_mfma_f32_16x16x32_bf16 v[4:7], a[204:207], a[160:163], v[4:7]\n"
    "v_cvt_pk_bf16_f32 v250, v244, v245\n"
    "v_cvt_pk_bf16_f32 v251, v246, v247\n"
    "v_mfma_f32_16x16x32_bf16 v[4:7], a[200:203], a[128:131], v[4:7]\n"
    "s_waitcnt vmcnt(11)\n"
    "v_accvgpr_read_b32 v245, a100\n"
    "v_accvgpr_read_b32 v247, a101\n"
    "v_mfma_f32_16x16x32_bf16 v[8:11], a[208:211], a[128:131], v[8:11]\n"
    "buffer_load_dwordx4 a[200:203], v240, s[20:23], s53 offen offset:2048 sc1\n"
    "v_mfma_f32_16x16x32_bf16 v[8:11], a[212:215], a[160:163], v[8:11]\n"
    "buffer_load_dwordx4 a[204:207], v240, s[20:23], s53 offen offset:3072 sc1\n"
    "v_mfma_f32_16x16x32_bf16 v[40:43], a[212:215], a[164:167], v[40:43]\n"
    "s_addk_i32 s53, 4096\n"
    "v_lshlrev_b32 v244, 16, v245\n"
    "v_lshlrev_b32 v246, 16, v247\n"
    "v_mfma_f32_16x16x32_bf16 v[40:43], a[208:211], a[132:135], v[40:43]\n"
    "v_and_b32 v245, s58, v245\n"
    "v_and_b32 v247, s58, v247\n"
    "v_mfma_f32_16x16x32_bf16 v[72:75], a[208:211], a[136:139], v[72:75]\n"
    "v_add_f32_dpp v254, v252, v56 row_newbcast:8 row_mask:0xf bank_mask:0xf\n"
    "v_add_f32_dpp v255, v252, v58 row_newbcast:10 row_mask:0xf bank_mask:0xf\n"
    "v_mfma_f32_16x16x32_bf16 v[72:75], a[212:215], a[168:171], v[72:75]\n"
    "v_mul_f32 v244, v244, v254\n"
    "v_mul_f32 v246, v246, v255\n"
    "v_mfma_f32_16x16x32_bf16 v[104:107], a[212:215], a[172:175], v[104:107]\n"
    "v_add_f32_dpp v254, v252, v57 row_newbcast:9 row_mask:0xf bank_mask:0xf\n"
    "v_add_f32_dpp v255, v252, v59 row_newbcast:11 row_mask:0xf bank_mask:0xf\n"
    "v_mfma_f32_16x16x32_bf16 v[104:107], a[208:211], a[140:143], v[104:107]\n"
    "v_mul_f32 v245, v245, v254\n"
    "v_mul_f32 v247, v247, v255\n"
    "v_mfma_f32_16x16x32_bf16 v[136:139], a[208:211], a[144:147], v[136:139]\n"
    "v_mfma_f32_16x16x32_bf16 v[136:139], a[212:215], a[176:179], v[136:139]\n"
    "buffer_store_dwordx4 v[248:251], v240, s[32:35], s91 offen sc1\n"
    "ds_write_b128 v243, v[248:251] offset:32768\n"
    "v_mfma_f32_16x16x32_bf16 v[168:171], a[212:215], a[180:183], v[168:171]\n"
    "v_cvt_pk_bf16_f32 v248, v244, v245\n"
    "v_cvt_pk_bf16_f32 v249, v246, v247\n"
    "v_mfma_f32_16x16x32_bf16 v[168:171], a[208:211], a[148:151], v[168:171]\n"
    "v_accvgpr_read_b32 v245, a102\n"
    "v_accvgpr_read_b32 v247, a103\n"
    "v_mfma_f32_16x16x32_bf16 v[200:203], a[208:211], a[152:155], v[200:203]\n"
    "v_lshlrev_b32 v244, 16, v245\n"
    "v_lshlrev_b32 v246, 16, v247\n"
    "v_mfma_f32_16x16x32_bf16 v[200:203], a[212:215], a[184:187], v[200:203]\n"
    "v_and_b32 v245, s58, v245\n"
    "s_waitcnt lgkmcnt(0)\n"
    "s_barrier\n"
    "v_mfma_f32_16x16x32_bf16 v[232:235], a[212:215], a[188:191], v[232:235]\n"
    "v_and_b32 v247, s58, v247\n"
    "v_add_f32_dpp v254, v252, v60 row_newbcast:12 row_mask:0xf bank_mask:0xf\n"
    "v_mfma_f32_16x16x32_bf16 v[232:235], a[208:211], a[156:159], v[232:235]\n"
    "s_waitcnt vmcnt(11)\n"
    "v_add_f32_dpp v255, v252, v62 row_newbcast:14 row_mask:0xf bank_mask:0xf\n"
    "v_mul_f32 v244, v244, v254\n"
    "v_mfma_f32_16x16x32_bf16 v[236:239], a[216:219], a[156:159], v[236:239]\n"
    "buffer_load_dwordx4 a[208:211], v240, s[20:23], s53 offen offset:0 sc1\n"
    "v_mfma_f32_16x16x32_bf16 v[236:239], a[220:223], a[188:191], v[236:239]\n"
    "buffer_load_dwordx4 a[212:215], v240, s[20:23], s53 offen offset:1024 sc1\n"
    "v_mfma_f32_16x16x32_bf16 v[204:207], a[220:223], a[184:187], v[204:207]\n"
    "s_xor_b32 s91, s91, 0x800\n"
    "v_add_u32 v241, s66, v240\n"
    "ds_read_b128 a[156:159], v241 offset:14336\n"
    "v_mfma_f32_16x16x32_bf16 v[204:207], a[216:219], a[152:155], v[204:207]\n"
    "ds_read_b128 a[188:191], v241 offset:15360\n"
    "v_mul_f32 v246, v246, v255\n"
    "v_mfma_f32_16x16x32_bf16 v[172:175], a[216:219], a[148:151], v[172:175]\n"
    "ds_read_b128 a[184:187], v241 offset:13312\n"
    "v_add_f32_dpp v254, v252, v61 row_newbcast:13 row_mask:0xf bank_mask:0xf\n"
    "v_mfma_f32_16x16x32_bf16 v[172:175], a[220:223], a[180:183], v[172:175]\n"
    "ds_read_b128 a[152:155], v241 offset:12288\n"
    "v_add_f32_dpp v255, v252, v63 row_newbcast:15 row_mask:0xf bank_mask:0xf\n"
    "v_mfma_f32_16x16x32_bf16 v[140:143], a[220:223], a[176:179], v[140:143]\n"
    "ds_read_b128 a[148:151], v241 offset:10240\n"
    "v_mul_f32 v245, v245, v254\n"
    "v_mfma_f32_16x16x32_bf16 v[140:143], a[216:219], a[144:147], v[140:143]\n"
    "ds_read_b128 a[180:183], v241 offset:11264\n"
    "v_mul_f32 v247, v247, v255\n"
    "v_mfma_f32_16x16x32_bf16 v[108:111], a[216:219], a[140:143], v[108:111]\n"
    "ds_read_b128 a[176:179], v241 offset:9216\n"
    "v_cvt_pk_bf16_f32 v250, v244, v245\n"
    "v_mfma_f32_16x16x32_bf16 v[108:111], a[220:223], a[172:175], v[108:111]\n"
    "ds_read_b128 a[144:147], v241 offset:8192\n"
    "v_cvt_pk_bf16_f32 v251, v246, v247\n"
    "v_mfma_f32_16x16x32_bf16 v[76:79], a[220:223], a[168:171], v[76:79]\n"
    "ds_read_b128 a[140:143], v241 offset:6144\n"
    "v_accvgpr_read_b32 v245, a104\n"
    "v_mfma_f32_16x16x32_bf16 v[76:79], a[216:219], a[136:139], v[76:79]\n"
    "ds_read_b128 a[172:175], v241 offset:7168\n"
    "v_accvgpr_read_b32 v247, a105\n"
    "v_mfma_f32_16x16x32_bf16 v[44:47], a[216:219], a[132:135], v[44:47]\n"
    "ds_read_b128 a[168:171], v241 offset:5120\n"
    "v_lshlrev_b32 v244, 16, v245\n"
    "v_mfma_f32_16x16x32_bf16 v[44:47], a[220:223], a[164:167], v[44:47]\n"
    "ds_read_b128 a[136:139], v241 offset:4096\n"
    "v_lshlrev_b32 v246, 16, v247\n"
    "v_mfma_f32_16x16x32_bf16 v[12:15], a[220:223], a[160:163], v[12:15]\n"
    "ds_read_b128 a[132:135], v241 offset:2048\n"
    "v_and_b32 v245, s58, v245\n"
    "v_mfma_f32_16x16x32_bf16 v[12:15], a[216:219], a[128:131], v[12:15]\n"
    "s_waitcnt vmcnt(11) lgkmcnt(11)\n"
    "ds_read_b128 a[164:167], v241 offset:3072\n"
    "v_and_b32 v247, s58, v247\n"
    "v_mfma_f32_16x16x32_bf16 v[224:227], a[224:227], a[156:159], v[224:227]\n"
    "buffer_load_dwordx4 a[216:219], v240, s[20:23], s53 offen offset:2048 sc1\n"
    "v_mfma_f32_16x16x32_bf16 v[224:227], a[228:231], a[188:191], v[224:227]\n"
    "s_waitcnt lgkmcnt(10)\n"
    "v_add_f32_dpp v254, v252, v88 row_newbcast:8 row_mask:0xf bank_mask:0xf\n"
    "v_add_f32_dpp v255, v252, v90 row_newbcast:10 row_mask:0xf bank_mask:0xf\n"
    "v_mfma_f32_16x16x32_bf16 v[192:195], a[228:231], a[184:187], v[192:195]\n"
    "buffer_load_dwordx4 a[220:223], v240, s[20:23], s53 offen offset:3072 sc1\n"
    "v_mfma_f32_16x16x32_bf16 v[192:195], a[224:227], a[152:155], v[192:195]\n"
    "s_waitcnt lgkmcnt(8)\n"
    "s_addk_i32 s53, 4096\n"
    "ds_read_b128 a[160:163], v241 offset:1024\n"
    "v_mfma_f32_16x16x32_bf16 v[160:163], a[224:227], a[148:151], v[160:163]\n"
    "ds_read_b128 a[128:131], v241 offset:0\n"
    "v_mul_f32 v244, v244, v254\n"
    "v_mfma_f32_16x16x32_bf16 v[160:163], a[228:231], a[180:183], v[160:163]\n"
    "s_waitcnt lgkmcnt(8)\n"
    "v_mul_f32 v246, v246, v255\n"
    "v_add_f32_dpp v254, v252, v89 row_newbcast:9 row_mask:0xf bank_mask:0xf\n"
    "v_mfma_f32_16x16x32_bf16 v[128:131], a[228:231], a[176:179], v[128:131]\n"
    "v_add_f32_dpp v255, v252, v91 row_newbcast:11 row_mask:0xf bank_mask:0xf\n"
    "v_mul_f32 v245, v245, v254\n"
    "v_mfma_f32_16x16x32_bf16 v[128:131], a[224:227], a[144:147], v[128:131]\n"
    "s_waitcnt lgkmcnt(6)\n"
    "v_mul_f32 v247, v247, v255\n"
    "v_mfma_f32_16x16x32_bf16 v[96:99], a[224:227], a[140:143], v[96:99]\n"
    "v_mfma_f32_16x16x32_bf16 v[96:99], a[228:231], a[172:175], v[96:99]\n"
    "s_waitcnt lgkmcnt(4)\n"
    "v_mfma_f32_16x16x32_bf16 v[64:67], a[228:231], a[168:171], v[64:67]\n"
    "buffer_store_dwordx4 v[248:251], v240, s[32:35], s91 offen sc1\n"
    "ds_write_b128 v243, v[248:251] offset:34816\n"
    "v_mfma_f32_16x16x32_bf16 v[64:67], a[224:227], a[136:139], v[64:67]\n"
    "s_waitcnt lgkmcnt(3)\n"
    "v_cvt_pk_bf16_f32 v248, v244, v245\n"
    "v_cvt_pk_bf16_f32 v249, v246, v247\n"
    "v_mfma_f32_16x16x32_bf16 v[32:35], a[224:227], a[132:135], v[32:35]\n"
    "v_accvgpr_read_b32 v245, a106\n"
    "v_accvgpr_read_b32 v247, a107\n"
    "v_mfma_f32_16x16x32_bf16 v[32:35], a[228:231], a[164:167], v[32:35]\n"
    "s_waitcnt lgkmcnt(1)\n"
    "v_lshlrev_b32 v244, 16, v245\n"
    "v_lshlrev_b32 v246, 16, v247\n"
    "v_mfma_f32_16x16x32_bf16 v[0:3], a[228:231], a[160:163], v[0:3]\n"
    "v_and_b32 v245, s58, v245\n"
    "v_and_b32 v247, s58, v247\n"
    "v_mfma_f32_16x16x32_bf16 v[0:3], a[224:227], a[128:131], v[0:3]\n"
    "s_waitcnt vmcnt(11)\n"
    "v_add_f32_dpp v254, v252, v92 row_newbcast:12 row_mask:0xf bank_mask:0xf\n"
    "v_add_f32_dpp v255, v252, v94 row_newbcast:14 row_mask:0xf bank_mask:0xf\n"
    "v_mfma_f32_16x16x32_bf16 v[4:7], a[232:235], a[128:131], v[4:7]\n"
    "buffer_load_dwordx4 a[224:227], v240, s[20:23], s53 offen offset:0 sc1\n"
    "v_mfma_f32_16x16x32_bf16 v[4:7], a[236:239], a[160:163], v[4:7]\n"
    "buffer_load_dwordx4 a[228:231], v240, s[20:23], s53 offen offset:1024 sc1\n"
    "v_mfma_f32_16x16x32_bf16 v[36:39], a[236:239], a[164:167], v[36:39]\n"
    "s_xor_b32 s91, s91, 0x101800\n"
    "v_mul_f32 v244, v244, v254\n"
    "v_mul_f32 v246, v246, v255\n"
    "v_mfma_f32_16x16x32_bf16 v[36:39], a[232:235], a[132:135], v[36:39]\n"
    "v_add_f32_dpp v254, v252, v93 row_newbcast:13 row_mask:0xf bank_mask:0xf\n"
    "v_add_f32_dpp v255, v252, v95 row_newbcast:15 row_mask:0xf bank_mask:0xf\n"
    "v_mfma_f32_16x16x32_bf16 v[68:71], a[232:235], a[136:139], v[68:71]\n"
    "v_mul_f32 v245, v245, v254\n"
    "v_mul_f32 v247, v247, v255\n"
    "v_mfma_f32_16x16x32_bf16 v[68:71], a[236:239], a[168:171], v[68:71]\n"
    "v_cvt_pk_bf16_f32 v250, v244, v245\n"
    "v_cvt_pk_bf16_f32 v251, v246, v247\n"
    "v_mfma_f32_16x16x32_bf16 v[100:103], a[236:239], a[172:175], v[100:103]\n"
    "v_accvgpr_read_b32 v245, a108\n"
    "v_accvgpr_read_b32 v247, a109\n"
    "v_mfma_f32_16x16x32_bf16 v[100:103], a[232:235], a[140:143], v[100:103]\n"
    "v_lshlrev_b32 v244, 16, v245\n"
    "v_lshlrev_b32 v246, 16, v247\n"
    "v_mfma_f32_16x16x32_bf16 v[132:135], a[232:235], a[144:147], v[132:135]\n"
    "v_and_b32 v245, s58, v245\n"
    "v_and_b32 v247, s58, v247\n"
    "v_mfma_f32_16x16x32_bf16 v[132:135], a[236:239], a[176:179], v[132:135]\n"
    "v_add_f32_dpp v254, v252, v120 row_newbcast:8 row_mask:0xf bank_mask:0xf\n"
    "v_add_f32_dpp v255, v252, v122 row_newbcast:10 row_mask:0xf bank_mask:0xf\n"
    "v_mfma_f32_16x16x32_bf16 v[164:167], a[236:239], a[180:183], v[164:167]\n"
    "v_mul_f32 v244, v244, v254\n"
    "v_mul_f32 v246, v246, v255\n"
    "v_mfma_f32_16x16x32_bf16 v[164:167], a[232:235], a[148:151], v[164:167]\n"
    "v_add_f32_dpp v254, v252, v121 row_newbcast:9 row_mask:0xf bank_mask:0xf\n"
    "v_add_f32_dpp v255, v252, v123 row_newbcast:11 row_mask:0xf bank_mask:0xf\n"
    "v_mfma_f32_16x16x32_bf16 v[196:199], a[232:235], a[152:155], v[196:199]\n"
    "v_mul_f32 v245, v245, v254\n"
    "v_mul_f32 v247, v247, v255\n"
    "v_mfma_f32_16x16x32_bf16 v[196:199], a[236:239], a[184:187], v[196:199]\n"
    "buffer_store_dwordx4 v[248:251], v240, s[32:35], s91 offen sc1\n"
    "ds_write_b128 v243, v[248:251] offset:36864\n"
    "v_mfma_f32_16x16x32_bf16 v[228:231], a[236:239], a[188:191], v[228:231]\n"
    "v_cvt_pk_bf16_f32 v248, v244, v245\n"
    "v_cvt_pk_bf16_f32 v249, v246, v247\n"
    "v_mfma_f32_16x16x32_bf16 v[228:231], a[232:235], a[156:159], v[228:231]\n"
    "s_waitcnt vmcnt(11)\n"
    "v_accvgpr_read_b32 v245, a110\n"
    "v_accvgpr_read_b32 v247, a111\n"
    "v_mfma_f32_16x16x32_bf16 v[232:235], a[192:195], a[156:159], v[232:235]\n"
    "buffer_load_dwordx4 a[232:235], v240, s[20:23], s53 offen offset:2048 sc1\n"
    "v_mfma_f32_16x16x32_bf16 v[232:235], a[196:199], a[188:191], v[232:235]\n"
    "buffer_load_dwordx4 a[236:239], v240, s[20:23], s53 offen offset:3072 sc1\n"
    "v_mfma_f32_16x16x32_bf16 v[200:203], a[196:199], a[184:187], v[200:203]\n"
    "s_addk_i32 s53, 4096\n"
    "v_lshlrev_b32 v244, 16, v245\n"
    "v_lshlrev_b32 v246, 16, v247\n"
    "v_mfma_f32_16x16x32_bf16 v[200:203], a[192:195], a[152:155], v[200:203]\n"
    "v_and_b32 v245, s58, v245\n"
    "v_and_b32 v247, s58, v247\n"
    "v_mfma_f32_16x16x32_bf16 v[168:171], a[192:195], a[148:151], v[168:171]\n"
    "s_xor_b32 s91, s91, 0x800\n"
    "v_add_f32_dpp v254, v252, v124 row_newbcast:12 row_mask:0xf bank_mask:0xf\n"
    "v_add_f32_dpp v255, v252, v126 row_newbcast:14 row_mask:0xf bank_mask:0xf\n"
    "v_mfma_f32_16x16x32_bf16 v[168:171], a[196:199], a[180:183], v[168:171]\n"
    "v_mul_f32 v244, v244, v254\n"
    "v_mul_f32 v246, v246, v255\n"
    "v_mfma_f32_16x16x32_bf16 v[136:139], a[196:199], a[176:179], v[136:139]\n"
    "v_add_f32_dpp v254, v252, v125 row_newbcast:13 row_mask:0xf bank_mask:0xf\n"
    "v_add_f32_dpp v255, v252, v127 row_newbcast:15 row_mask:0xf bank_mask:0xf\n"
    "v_mfma_f32_16x16x32_bf16 v[136:139], a[192:195], a[144:147], v[136:139]\n"
    "v_mul_f32 v245, v245, v254\n"
    "v_mul_f32 v247, v247, v255\n"
    "v_mfma_f32_16x16x32_bf16 v[104:107], a[192:195], a[140:143], v[104:107]\n"
    "v_cvt_pk_bf16_f32 v250, v244, v245\n"
    "v_cvt_pk_bf16_f32 v251, v246, v247\n"
    "v_mfma_f32_16x16x32_bf16 v[104:107], a[196:199], a[172:175], v[104:107]\n"
    "v_accvgpr_read_b32 v245, a112\n"
    "v_accvgpr_read_b32 v247, a113\n"
    "v_mfma_f32_16x16x32_bf16 v[72:75], a[196:199], a[168:171], v[72:75]\n"
    "v_lshlrev_b32 v244, 16, v245\n"
    "v_lshlrev_b32 v246, 16, v247\n"
    "v_mfma_f32_16x16x32_bf16 v[72:75], a[192:195], a[136:139], v[72:75]\n"
    "v_and_b32 v245, s58, v245\n"
    "v_and_b32 v247, s58, v247\n"
    "v_mfma_f32_16x16x32_bf16 v[40:43], a[192:195], a[132:135], v[40:43]\n"
    "v_add_f32_dpp v254, v252, v152 row_newbcast:8 row_mask:0xf bank_mask:0xf\n"
    "v_add_f32_dpp v255, v252, v154 row_newbcast:10 row_mask:0xf bank_mask:0xf\n"
    "v_mfma_f32_16x16x32_bf16 v[40:43], a[196:199], a[164:167], v[40:43]\n"
    "v_mul_f32 v244, v244, v254\n"
    "v_mul_f32 v246, v246, v255\n"
    "v_mfma_f32_16x16x32_bf16 v[8:11], a[196:199], a[160:163], v[8:11]\n"
    "v_add_f32_dpp v254, v252, v153 row_newbcast:9 row_mask:0xf bank_mask:0xf\n"
    "v_add_f32_dpp v255, v252, v155 row_newbcast:11 row_mask:0xf bank_mask:0xf\n"
    "v_mfma_f32_16x16x32_bf16 v[8:11], a[192:195], a[128:131], v[8:11]\n"
    "s_waitcnt vmcnt(11)\n"
    "v_mul_f32 v245, v245, v254\n"
    "v_mul_f32 v247, v247, v255\n"
    "v_mfma_f32_16x16x32_bf16 v[12:15], a[200:203], a[128:131], v[12:15]\n"
    "buffer_load_dwordx4 a[192:195], v240, s[20:23], s53 offen offset:0 sc1\n"
    "v_mfma_f32_16x16x32_bf16 v[12:15], a[204:207], a[160:163], v[12:15]\n"
    "buffer_load_dwordx4 a[196:199], v240, s[20:23], s53 offen offset:1024 sc1\n"
    "v_mfma_f32_16x16x32_bf16 v[44:47], a[204:207], a[164:167], v[44:47]\n"
    "v_mfma_f32_16x16x32_bf16 v[44:47], a[200:203], a[132:135], v[44:47]\n"
    "buffer_store_dwordx4 v[248:251], v240, s[32:35], s91 offen sc1\n"
    "ds_write_b128 v243, v[248:251] offset:38912\n"
    "v_mfma_f32_16x16x32_bf16 v[76:79], a[200:203], a[136:139], v[76:79]\n"
    "v_cvt_pk_bf16_f32 v248, v244, v245\n"
    "v_cvt_pk_bf16_f32 v249, v246, v247\n"
    "v_mfma_f32_16x16x32_bf16 v[76:79], a[204:207], a[168:171], v[76:79]\n"
    "s_mov_b32 s50, 64\n"
    "v_accvgpr_read_b32 v245, a114\n"
    "v_accvgpr_read_b32 v247, a115\n"
    "v_mfma_f32_16x16x32_bf16 v[108:111], a[204:207], a[172:175], v[108:111]\n"
    "buffer_load_dword v242, v240, s[48:51], 0 offen offset:0 sc1\n"
    "v_mfma_f32_16x16x32_bf16 v[108:111], a[200:203], a[140:143], v[108:111]\n"
    "v_lshlrev_b32 v244, 16, v245\n"
    "v_lshlrev_b32 v246, 16, v247\n"
    "v_mfma_f32_16x16x32_bf16 v[140:143], a[200:203], a[144:147], v[140:143]\n"
    "s_xor_b32 s91, s91, 0x185800\n"
    "v_and_b32 v245, s58, v245\n"
    "v_and_b32 v247, s58, v247\n"
    "v_mfma_f32_16x16x32_bf16 v[140:143], a[204:207], a[176:179], v[140:143]\n"
    "v_add_f32_dpp v254, v252, v156 row_newbcast:12 row_mask:0xf bank_mask:0xf\n"
    "v_add_f32_dpp v255, v252, v158 row_newbcast:14 row_mask:0xf bank_mask:0xf\n"
    "v_mfma_f32_16x16x32_bf16 v[172:175], a[204:207], a[180:183], v[172:175]\n"
    "v_mul_f32 v244, v244, v254\n"
    "v_mul_f32 v246, v246, v255\n"
    "v_mfma_f32_16x16x32_bf16 v[172:175], a[200:203], a[148:151], v[172:175]\n"
    "v_add_f32_dpp v254, v252, v157 row_newbcast:13 row_mask:0xf bank_mask:0xf\n"
    "v_add_f32_dpp v255, v252, v159 row_newbcast:15 row_mask:0xf bank_mask:0xf\n"
    "v_mfma_f32_16x16x32_bf16 v[204:207], a[200:203], a[152:155], v[204:207]\n"
    "v_mul_f32 v245, v245, v254\n"
    "v_mul_f32 v247, v247, v255\n"
    "v_mfma_f32_16x16x32_bf16 v[204:207], a[204:207], a[184:187], v[204:207]\n"
    "v_cvt_pk_bf16_f32 v250, v244, v245\n"
    "v_cvt_pk_bf16_f32 v251, v246, v247\n"
    "v_mfma_f32_16x16x32_bf16 v[236:239], a[204:207], a[188:191], v[236:239]\n"
    "v_accvgpr_read_b32 v245, a116\n"
    "v_accvgpr_read_b32 v247, a117\n"
    "v_mfma_f32_16x16x32_bf16 v[236:239], a[200:203], a[156:159], v[236:239]\n"
    "s_waitcnt vmcnt(12)\n"
    "v_lshlrev_b32 v244, 16, v245\n"
    "v_lshlrev_b32 v246, 16, v247\n"
    "v_mfma_f32_16x16x32_bf16 a[240:243], a[208:211], a[156:159], a[240:243]\n"
    "buffer_load_dwordx4 a[200:203], v240, s[20:23], s53 offen offset:2048 sc1\n"
    "v_mfma_f32_16x16x32_bf16 a[240:243], a[212:215], a[188:191], a[240:243]\n"
    "buffer_load_dwordx4 a[204:207], v240, s[20:23], s53 offen offset:3072 sc1\n"
    "v_mfma_f32_16x16x32_bf16 v[208:211], a[212:215], a[184:187], v[208:211]\n"
    "s_addk_i32 s53, 4096\n"
    "v_and_b32 v245, s58, v245\n"
    "v_and_b32 v247, s58, v247\n"
    "v_mfma_f32_16x16x32_bf16 v[208:211], a[208:211], a[152:155], v[208:211]\n"
    "buffer_store_dwordx4 v[248:251], v240, s[32:35], s91 offen sc1\n"
    "ds_write_b128 v243, v[248:251] offset:40960\n"
    "v_mfma_f32_16x16x32_bf16 v[176:179], a[208:211], a[148:151], v[176:179]\n"
    "v_add_f32_dpp v254, v252, v184 row_newbcast:8 row_mask:0xf bank_mask:0xf\n"
    "v_add_f32_dpp v255, v252, v186 row_newbcast:10 row_mask:0xf bank_mask:0xf\n"
    "v_mfma_f32_16x16x32_bf16 v[176:179], a[212:215], a[180:183], v[176:179]\n"
    "v_mul_f32 v244, v244, v254\n"
    "v_mul_f32 v246, v246, v255\n"
    "v_mfma_f32_16x16x32_bf16 v[144:147], a[212:215], a[176:179], v[144:147]\n"
    "s_xor_b32 s91, s91, 0x800\n"
    "v_add_f32_dpp v254, v252, v185 row_newbcast:9 row_mask:0xf bank_mask:0xf\n"
    "v_add_f32_dpp v255, v252, v187 row_newbcast:11 row_mask:0xf bank_mask:0xf\n"
    "v_mfma_f32_16x16x32_bf16 v[144:147], a[208:211], a[144:147], v[144:147]\n"
    "v_mul_f32 v245, v245, v254\n"
    "v_mul_f32 v247, v247, v255\n"
    "v_mfma_f32_16x16x32_bf16 v[112:115], a[208:211], a[140:143], v[112:115]\n"
    "v_cvt_pk_bf16_f32 v248, v244, v245\n"
    "v_cvt_pk_bf16_f32 v249, v246, v247\n"
    "v_mfma_f32_16x16x32_bf16 v[112:115], a[212:215], a[172:175], v[112:115]\n"
    "v_accvgpr_read_b32 v245, a118\n"
    "v_accvgpr_read_b32 v247, a119\n"
    "v_mfma_f32_16x16x32_bf16 v[80:83], a[212:215], a[168:171], v[80:83]\n"
    "v_lshlrev_b32 v244, 16, v245\n"
    "v_lshlrev_b32 v246, 16, v247\n"
    "v_mfma_f32_16x16x32_bf16 v[80:83], a[208:211], a[136:139], v[80:83]\n"
    "v_and_b32 v245, s58, v245\n"
    "v_and_b32 v247, s58, v247\n"
    "v_mfma_f32_16x16x32_bf16 v[48:51], a[208:211], a[132:135], v[48:51]\n"
    "v_add_f32_dpp v254, v252, v188 row_newbcast:12 row_mask:0xf bank_mask:0xf\n"
    "v_add_f32_dpp v255, v252, v190 row_newbcast:14 row_mask:0xf bank_mask:0xf\n"
    "v_mfma_f32_16x16x32_bf16 v[48:51], a[212:215], a[164:167], v[48:51]\n"
    "v_mul_f32 v244, v244, v254\n"
    "v_mul_f32 v246, v246, v255\n"
    "v_mfma_f32_16x16x32_bf16 v[16:19], a[212:215], a[160:163], v[16:19]\n"
    "v_add_f32_dpp v254, v252, v189 row_newbcast:13 row_mask:0xf bank_mask:0xf\n"
    "v_add_f32_dpp v255, v252, v191 row_newbcast:15 row_mask:0xf bank_mask:0xf\n"
    "v_mfma_f32_16x16x32_bf16 v[16:19], a[208:211], a[128:131], v[16:19]\n"
    "s_waitcnt vmcnt(13)\n"
    "v_mul_f32 v245, v245, v254\n"
    "v_mul_f32 v247, v247, v255\n"
    "v_mfma_f32_16x16x32_bf16 v[20:23], a[216:219], a[128:131], v[20:23]\n"
    "buffer_load_dwordx4 a[208:211], v240, s[20:23], s53 offen offset:0 sc1\n"
    "v_mfma_f32_16x16x32_bf16 v[20:23], a[220:223], a[160:163], v[20:23]\n"
    "buffer_load_dwordx4 a[212:215], v240, s[20:23], s53 offen offset:1024 sc1\n"
    "v_cvt_pk_bf16_f32 v250, v244, v245\n"
    "v_cvt_pk_bf16_f32 v251, v246, v247\n"
    "v_mfma_f32_16x16x32_bf16 v[52:55], a[220:223], a[164:167], v[52:55]\n"
    "buffer_store_dwordx4 v[248:251], v240, s[32:35], s91 offen sc1\n"
    "ds_write_b128 v243, v[248:251] offset:43008\n"
    "v_accvgpr_read_b32 v245, a120\n"
    "v_accvgpr_read_b32 v247, a121\n"
    "v_mfma_f32_16x16x32_bf16 v[52:55], a[216:219], a[132:135], v[52:55]\n"
    "v_lshlrev_b32 v244, 16, v245\n"
    "v_lshlrev_b32 v246, 16, v247\n"
    "v_and_b32 v245, s58, v245\n"
    "v_mfma_f32_16x16x32_bf16 v[84:87], a[216:219], a[136:139], v[84:87]\n"
    "s_xor_b32 s91, s91, 0x101800\n"
    "v_and_b32 v247, s58, v247\n"
    "v_add_f32_dpp v254, v252, v216 row_newbcast:8 row_mask:0xf bank_mask:0xf\n"
    "v_mfma_f32_16x16x32_bf16 v[84:87], a[220:223], a[168:171], v[84:87]\n"
    "v_add_f32_dpp v255, v252, v218 row_newbcast:10 row_mask:0xf bank_mask:0xf\n"
    "v_mul_f32 v244, v244, v254\n"
    "v_mfma_f32_16x16x32_bf16 v[116:119], a[220:223], a[172:175], v[116:119]\n"
    "v_mul_f32 v246, v246, v255\n"
    "v_add_f32_dpp v254, v252, v217 row_newbcast:9 row_mask:0xf bank_mask:0xf\n"
    "v_add_f32_dpp v255, v252, v219 row_newbcast:11 row_mask:0xf bank_mask:0xf\n"
    "v_mfma_f32_16x16x32_bf16 v[116:119], a[216:219], a[140:143], v[116:119]\n"
    "v_mul_f32 v245, v245, v254\n"
    "v_mul_f32 v247, v247, v255\n"
    "v_mfma_f32_16x16x32_bf16 v[148:151], a[216:219], a[144:147], v[148:151]\n"
    "v_cvt_pk_bf16_f32 v248, v244, v245\n"
    "v_cvt_pk_bf16_f32 v249, v246, v247\n"
    "v_mfma_f32_16x16x32_bf16 v[148:151], a[220:223], a[176:179], v[148:151]\n"
    "v_accvgpr_read_b32 v245, a122\n"
    "v_accvgpr_read_b32 v247, a123\n"
    "v_mfma_f32_16x16x32_bf16 v[180:183], a[220:223], a[180:183], v[180:183]\n"
    "v_lshlrev_b32 v244, 16, v245\n"
    "v_lshlrev_b32 v246, 16, v247\n"
    "v_and_b32 v245, s58, v245\n"
    "v_mfma_f32_16x16x32_bf16 v[180:183], a[216:219], a[148:151], v[180:183]\n"
    "v_and_b32 v247, s58, v247\n"
    "v_add_f32_dpp v254, v252, v220 row_newbcast:12 row_mask:0xf bank_mask:0xf\n"
    "v_mfma_f32_16x16x32_bf16 v[212:215], a[216:219], a[152:155], v[212:215]\n"
    "v_add_f32_dpp v255, v252, v222 row_newbcast:14 row_mask:0xf bank_mask:0xf\n"
    "v_mul_f32 v244, v244, v254\n"
    "v_mfma_f32_16x16x32_bf16 v[212:215], a[220:223], a[184:187], v[212:215]\n"
    "v_mul_f32 v246, v246, v255\n"
    "v_add_f32_dpp v254, v252, v221 row_newbcast:13 row_mask:0xf bank_mask:0xf\n"
    "v_mfma_f32_16x16x32_bf16 a[244:247], a[220:223], a[188:191], a[244:247]\n"
    "v_add_f32_dpp v255, v252, v223 row_newbcast:15 row_mask:0xf bank_mask:0xf\n"
    "v_mul_f32 v245, v245, v254\n"
    "v_mfma_f32_16x16x32_bf16 a[244:247], a[216:219], a[156:159], a[244:247]\n"
    "v_mul_f32 v247, v247, v255\n"
    "v_cvt_pk_bf16_f32 v250, v244, v245\n"
    "v_cvt_pk_bf16_f32 v251, v246, v247\n"
    "v_accvgpr_read_b32 v245, a124\n"
    "v_accvgpr_read_b32 v247, a125\n"
    "v_lshlrev_b32 v244, 16, v245\n"
    "v_lshlrev_b32 v246, 16, v247\n"
    "v_and_b32 v245, s58, v245\n"
    "v_and_b32 v247, s58, v247\n"
    "v_accvgpr_read_b32 v254, a248\n"
    "v_accvgpr_read_b32 v255, a250\n"
    "s_nop 0\n"
    "v_add_f32_dpp v254, v252, v254 row_newbcast:8 row_mask:0xf bank_mask:0xf\n"
    "v_add_f32_dpp v255, v252, v255 row_newbcast:10 row_mask:0xf bank_mask:0xf\n"
    "v_mul_f32 v244, v244, v254\n"
    "v_mul_f32 v246, v246, v255\n"
    "v_accvgpr_read_b32 v254, a249\n"
    "v_accvgpr_read_b32 v255, a251\n"
    "s_waitcnt vmcnt(13)\n"
    "v_mfma_f32_16x16x32_bf16 a[248:251], a[224:227], a[156:159], a[248:251]\n"
    "buffer_store_dwordx4 v[248:251], v240, s[32:35], s91 offen sc1\n"
    "ds_write_b128 v243, v[248:251] offset:45056\n"
    "v_mfma_f32_16x16x32_bf16 a[248:251], a[228:231], a[188:191], a[248:251]\n"
    "buffer_load_dwordx4 a[216:219], v240, s[20:23], s53 offen offset:2048 sc1\n"
    "v_mfma_f32_16x16x32_bf16 v[216:219], a[228:231], a[184:187], v[216:219]\n"
    "buffer_load_dwordx4 a[220:223], v240, s[20:23], s53 offen offset:3072 sc1\n"
    "v_mfma_f32_16x16x32_bf16 v[216:219], a[224:227], a[152:155], v[216:219]\n"
    "s_addk_i32 s53, 4096\n"
    "v_add_f32_dpp v254, v252, v254 row_newbcast:9 row_mask:0xf bank_mask:0xf\n"
    "v_add_f32_dpp v255, v252, v255 row_newbcast:11 row_mask:0xf bank_mask:0xf\n"
    "v_mfma_f32_16x16x32_bf16 v[184:187], a[224:227], a[148:151], v[184:187]\n"
    "v_mul_f32 v245, v245, v254\n"
    "v_mul_f32 v247, v247, v255\n"
    "v_mfma_f32_16x16x32_bf16 v[184:187], a[228:231], a[180:183], v[184:187]\n"
    "s_xor_b32 s91, s91, 0x800\n"
    "v_cvt_pk_bf16_f32 v248, v244, v245\n"
    "v_cvt_pk_bf16_f32 v249, v246, v247\n"
    "v_mfma_f32_16x16x32_bf16 v[152:155], a[228:231], a[176:179], v[152:155]\n"
    "v_accvgpr_read_b32 v245, a126\n"
    "v_accvgpr_read_b32 v247, a127\n"
    "v_mfma_f32_16x16x32_bf16 v[152:155], a[224:227], a[144:147], v[152:155]\n"
    "v_lshlrev_b32 v244, 16, v245\n"
    "v_lshlrev_b32 v246, 16, v247\n"
    "v_mfma_f32_16x16x32_bf16 v[120:123], a[224:227], a[140:143], v[120:123]\n"
    "v_and_b32 v245, s58, v245\n"
    "v_and_b32 v247, s58, v247\n"
    "v_mfma_f32_16x16x32_bf16 v[120:123], a[228:231], a[172:175], v[120:123]\n"
    "v_accvgpr_read_b32 v254, a252\n"
    "v_accvgpr_read_b32 v255, a254\n"
    "v_mfma_f32_16x16x32_bf16 v[88:91], a[228:231], a[168:171], v[88:91]\n"
    "v_add_f32_dpp v254, v252, v254 row_newbcast:12 row_mask:0xf bank_mask:0xf\n"
    "v_add_f32_dpp v255, v252, v255 row_newbcast:14 row_mask:0xf bank_mask:0xf\n"
    "v_mfma_f32_16x16x32_bf16 v[88:91], a[224:227], a[136:139], v[88:91]\n"
    "v_mul_f32 v244, v244, v254\n"
    "v_mul_f32 v246, v246, v255\n"
    "v_mfma_f32_16x16x32_bf16 v[56:59], a[224:227], a[132:135], v[56:59]\n"
    "v_accvgpr_read_b32 v254, a253\n"
    "v_accvgpr_read_b32 v255, a255\n"
    "v_mfma_f32_16x16x32_bf16 v[56:59], a[228:231], a[164:167], v[56:59]\n"
    "v_add_f32_dpp v254, v252, v254 row_newbcast:13 row_mask:0xf bank_mask:0xf\n"
    "v_add_f32_dpp v255, v252, v255 row_newbcast:15 row_mask:0xf bank_mask:0xf\n"
    "v_mfma_f32_16x16x32_bf16 v[24:27], a[228:231], a[160:163], v[24:27]\n"
    "v_mul_f32 v245, v245, v254\n"
    "v_mul_f32 v247, v247, v255\n"
    "v_mfma_f32_16x16x32_bf16 v[24:27], a[224:227], a[128:131], v[24:27]\n"
    "s_waitcnt vmcnt(13)\n"
    "v_cvt_pk_bf16_f32 v250, v244, v245\n"
    "v_cvt_pk_bf16_f32 v251, v246, v247\n"
    "v_mfma_f32_16x16x32_bf16 v[28:31], a[232:235], a[128:131], v[28:31]\n"
    "buffer_store_dwordx4 v[248:251], v240, s[32:35], s91 offen sc1\n"
    "ds_write_b128 v243, v[248:251] offset:47104\n"
    "v_mfma_f32_16x16x32_bf16 v[28:31], a[236:239], a[160:163], v[28:31]\n"
    "buffer_load_dwordx4 a[224:227], v240, s[20:23], s53 offen offset:0 sc1\n"
    "v_mfma_f32_16x16x32_bf16 v[60:63], a[236:239], a[164:167], v[60:63]\n"
    "buffer_load_dwordx4 a[228:231], v240, s[20:23], s53 offen offset:1024 sc1\n"
    "v_mfma_f32_16x16x32_bf16 v[60:63], a[232:235], a[132:135], v[60:63]\n"
    "ds_read_b128 a[128:131], v241 offset:16384\n"
    "ds_read_b128 a[160:163], v241 offset:17408\n"
    "v_mfma_f32_16x16x32_bf16 v[92:95], a[232:235], a[136:139], v[92:95]\n"
    "ds_read_b128 a[164:167], v241 offset:19456\n"
    "v_mfma_f32_16x16x32_bf16 v[92:95], a[236:239], a[168:171], v[92:95]\n"
    "ds_read_b128 a[132:135], v241 offset:18432\n"
    "v_mfma_f32_16x16x32_bf16 v[124:127], a[236:239], a[172:175], v[124:127]\n"
    "ds_read_b128 a[136:139], v241 offset:20480\n"
    "v_mfma_f32_16x16x32_bf16 v[124:127], a[232:235], a[140:143], v[124:127]\n"
    "ds_read_b128 a[168:171], v241 offset:21504\n"
    "v_mfma_f32_16x16x32_bf16 v[156:159], a[232:235], a[144:147], v[156:159]\n"
    "ds_read_b128 a[172:175], v241 offset:23552\n"
    "v_mfma_f32_16x16x32_bf16 v[156:159], a[236:239], a[176:179], v[156:159]\n"
    "ds_read_b128 a[140:143], v241 offset:22528\n"
    "v_mfma_f32_16x16x32_bf16 v[188:191], a[236:239], a[180:183], v[188:191]\n"
    "ds_read_b128 a[144:147], v241 offset:24576\n"
    "v_mfma_f32_16x16x32_bf16 v[188:191], a[232:235], a[148:151], v[188:191]\n"
    "ds_read_b128 a[176:179], v241 offset:25600\n"
    "v_mfma_f32_16x16x32_bf16 v[220:223], a[232:235], a[152:155], v[220:223]\n"
    "ds_read_b128 a[180:183], v241 offset:27648\n"
    "v_mfma_f32_16x16x32_bf16 v[220:223], a[236:239], a[184:187], v[220:223]\n"
    "ds_read_b128 a[148:151], v241 offset:26624\n"
    "v_mfma_f32_16x16x32_bf16 a[252:255], a[236:239], a[188:191], a[252:255]\n"
    "ds_read_b128 a[152:155], v241 offset:28672\n"
    "v_mfma_f32_16x16x32_bf16 a[252:255], a[232:235], a[156:159], a[252:255]\n"
    "s_waitcnt vmcnt(14) lgkmcnt(11)\n"
    "ds_read_b128 a[184:187], v241 offset:29696\n"
    "v_mfma_f32_16x16x32_bf16 v[0:3], a[192:195], a[128:131], v[0:3]\n"
    "buffer_load_dwordx4 a[232:235], v240, s[20:23], s53 offen offset:2048 sc1\n"
    "v_mfma_f32_16x16x32_bf16 v[0:3], a[196:199], a[160:163], v[0:3]\n"
    "s_waitcnt lgkmcnt(10)\n"
    "v_mfma_f32_16x16x32_bf16 v[32:35], a[196:199], a[164:167], v[32:35]\n"
    "buffer_load_dwordx4 a[236:239], v240, s[20:23], s53 offen offset:3072 sc1\n"
    "v_mfma_f32_16x16x32_bf16 v[32:35], a[192:195], a[132:135], v[32:35]\n"
    "s_waitcnt lgkmcnt(8)\n"
    "s_addk_i32 s53, 4096\n"
    "ds_read_b128 a[188:191], v241 offset:31744\n"
    "v_mfma_f32_16x16x32_bf16 v[64:67], a[192:195], a[136:139], v[64:67]\n"
    "ds_read_b128 a[156:159], v241 offset:30720\n"
    "v_mfma_f32_16x16x32_bf16 v[64:67], a[196:199], a[168:171], v[64:67]\n"
    "s_waitcnt lgkmcnt(8)\n"
    "v_mfma_f32_16x16x32_bf16 v[96:99], a[196:199], a[172:175], v[96:99]\n"
    "v_mfma_f32_16x16x32_bf16 v[96:99], a[192:195], a[140:143], v[96:99]\n"
    "s_waitcnt lgkmcnt(6)\n"
    "v_mfma_f32_16x16x32_bf16 v[128:131], a[192:195], a[144:147], v[128:131]\n"
    "v_mfma_f32_16x16x32_bf16 v[128:131], a[196:199], a[176:179], v[128:131]\n"
    "s_waitcnt lgkmcnt(4)\n"
    "v_mfma_f32_16x16x32_bf16 v[160:163], a[196:199], a[180:183], v[160:163]\n"
    "v_mfma_f32_16x16x32_bf16 v[160:163], a[192:195], a[148:151], v[160:163]\n"
    "s_waitcnt lgkmcnt(2)\n"
    "v_mfma_f32_16x16x32_bf16 v[192:195], a[192:195], a[152:155], v[192:195]\n"
    "v_mfma_f32_16x16x32_bf16 v[192:195], a[196:199], a[184:187], v[192:195]\n"
    "s_waitcnt lgkmcnt(0)\n"
    "v_mfma_f32_16x16x32_bf16 v[224:227], a[196:199], a[188:191], v[224:227]\n"
    "v_mfma_f32_16x16x32_bf16 v[224:227], a[192:195], a[156:159], v[224:227]\n"
    "s_waitcnt vmcnt(14)\n"
    "v_cmp_le_u32 vcc, s52, v242\n"
    "s_nop 1\n"
    "s_and_b32 s63, vcc_lo, 15\n"
    "s_cmp_eq_u32 s63, 15\n"
    "s_cbranch_scc1 LA6_%=\n"
    "s_mov_b32 s69, 0x400000\n"
    "LA7_%=:\n"
    "s_sleep 1\n"
    "buffer_load_dword v242, v240, s[48:51], 0 offen offset:0 sc1\n"
    "s_waitcnt vmcnt(0)\n"
    "v_cmp_le_u32 vcc, s52, v242\n"
    "s_sub_u32 s69, s69, 1\n"
    "s_cbranch_scc1 LA6_%=\n"
    "s_and_b32 s63, vcc_lo, 15\n"
    "s_cmp_eq_u32 s63, 15\n"
    "s_cbranch_scc0 LA7_%=\n"
    "LA6_%=:\n"
    "s_waitcnt vmcnt(12)\n"
    "v_mfma_f32_16x16x32_bf16 v[228:231], a[200:203], a[156:159], v[228:231]\n"
    "v_readfirstlane_b32 s90, v242\n"
    "s_and_b32 s90, s90, 1\n"
    "v_mfma_f32_16x16x32_bf16 v[228:231], a[204:207], a[188:191], v[228:231]\n"
    "s_lshl_b32 s90, s90, 13\n"
    "v_mfma_f32_16x16x32_bf16 v[196:199], a[204:207], a[184:187], v[196:199]\n"
    "buffer_load_dwordx4 a[192:195], v240, s[20:23], s53 offen offset:0 sc1\n"
    "v_mfma_f32_16x16x32_bf16 v[196:199], a[200:203], a[152:155], v[196:199]\n"
    "buffer_load_dwordx4 a[196:199], v240, s[20:23], s53 offen offset:1024 sc1\n"
    "v_mfma_f32_16x16x32_bf16 v[164:167], a[200:203], a[148:151], v[164:167]\n"
    "s_mov_b32 m0, s56\n"
    "s_xor_b32 s90, s90, s59\n"
    "v_mfma_f32_16x16x32_bf16 v[164:167], a[204:207], a[180:183], v[164:167]\n"
    "buffer_load_dwordx4 v240, s[32:35], s90 offen sc1 lds\n"
    "v_mfma_f32_16x16x32_bf16 v[132:135], a[204:207], a[176:179], v[132:135]\n"
    "v_mfma_f32_16x16x32_bf16 v[132:135], a[200:203], a[144:147], v[132:135]\n"
    "v_mfma_f32_16x16x32_bf16 v[100:103], a[200:203], a[140:143], v[100:103]\n"
    "v_mfma_f32_16x16x32_bf16 v[100:103], a[204:207], a[172:175], v[100:103]\n"
    "v_mfma_f32_16x16x32_bf16 v[68:71], a[204:207], a[168:171], v[68:71]\n"
    "v_mfma_f32_16x16x32_bf16 v[68:71], a[200:203], a[136:139], v[68:71]\n"
    "v_mfma_f32_16x16x32_bf16 v[36:39], a[200:203], a[132:135], v[36:39]\n"
    "v_mfma_f32_16x16x32_bf16 v[36:39], a[204:207], a[164:167], v[36:39]\n"
    "v_mfma_f32_16x16x32_bf16 v[4:7], a[204:207], a[160:163], v[4:7]\n"
    "v_mfma_f32_16x16x32_bf16 v[4:7], a[200:203], a[128:131], v[4:7]\n"
    "s_waitcnt vmcnt(12)\n"
    "v_mfma_f32_16x16x32_bf16 v[8:11], a[208:211], a[128:131], v[8:11]\n"
    "buffer_load_dwordx4 a[200:203], v240, s[20:23], s53 offen offset:2048 sc1\n"
    "v_mfma_f32_16x16x32_bf16 v[8:11], a[212:215], a[160:163], v[8:11]\n"
    "buffer_load_dwordx4 a[204:207], v240, s[20:23], s53 offen offset:3072 sc1\n"
    "v_mfma_f32_16x16x32_bf16 v[40:43], a[212:215], a[164:167], v[40:43]\n"
    "s_addk_i32 s53, 4096\n"
    "v_mfma_f32_16x16x32_bf16 v[40:43], a[208:211], a[132:135], v[40:43]\n"
    "v_mfma_f32_16x16x32_bf16 v[72:75], a[208:211], a[136:139], v[72:75]\n"
    "v_mfma_f32_16x16x32_bf16 v[72:75], a[212:215], a[168:171], v[72:75]\n"
    "v_mfma_f32_16x16x32_bf16 v[104:107], a[212:215], a[172:175], v[104:107]\n"
    "v_mfma_f32_16x16x32_bf16 v[104:107], a[208:211], a[140:143], v[104:107]\n"
    "v_mfma_f32_16x16x32_bf16 v[136:139], a[208:211], a[144:147], v[136:139]\n"
    "s_addk_i32 m0, 2048\n"
    "s_xor_b32 s90, s90, 0x800\n"
    "v_mfma_f32_16x16x32_bf16 v[136:139], a[212:215], a[176:179], v[136:139]\n"
    "buffer_load_dwordx4 v240, s[32:35], s90 offen sc1 lds\n"
    "v_mfma_f32_16x16x32_bf16 v[168:171], a[212:215], a[180:183], v[168:171]\n"
    "v_mfma_f32_16x16x32_bf16 v[168:171], a[208:211], a[148:151], v[168:171]\n"
    "v_mfma_f32_16x16x32_bf16 v[200:203], a[208:211], a[152:155], v[200:203]\n"
    "v_mfma_f32_16x16x32_bf16 v[200:203], a[212:215], a[184:187], v[200:203]\n"
    "v_mfma_f32_16x16x32_bf16 v[232:235], a[212:215], a[188:191], v[232:235]\n"
    "v_mfma_f32_16x16x32_bf16 v[232:235], a[208:211], a[156:159], v[232:235]\n"
    "s_waitcnt vmcnt(11)\n"
    "v_mfma_f32_16x16x32_bf16 v[236:239], a[216:219], a[156:159], v[236:239]\n"
    "buffer_load_dwordx4 a[208:211], v240, s[20:23], s53 offen offset:0 sc1\n"
    "v_mfma_f32_16x16x32_bf16 v[236:239], a[220:223], a[188:191], v[236:239]\n"
    "buffer_load_dwordx4 a[212:215], v240, s[20:23], s53 offen offset:1024 sc1\n"
    "v_mfma_f32_16x16x32_bf16 v[204:207], a[220:223], a[184:187], v[204:207]\n"
    "v_mfma_f32_16x16x32_bf16 v[204:207], a[216:219], a[152:155], v[204:207]\n"
    "v_mfma_f32_16x16x32_bf16 v[172:175], a[216:219], a[148:151], v[172:175]\n"
    "v_mfma_f32_16x16x32_bf16 v[172:175], a[220:223], a[180:183], v[172:175]\n"
    "v_mfma_f32_16x16x32_bf16 v[140:143], a[220:223], a[176:179], v[140:143]\n"
    "v_mfma_f32_16x16x32_bf16 v[140:143], a[216:219], a[144:147], v[140:143]\n"
    "v_mfma_f32_16x16x32_bf16 v[108:111], a[216:219], a[140:143], v[108:111]\n"
    "v_mfma_f32_16x16x32_bf16 v[108:111], a[220:223], a[172:175], v[108:111]\n"
    "v_mfma_f32_16x16x32_bf16 v[76:79], a[220:223], a[168:171], v[76:79]\n"
    "v_mfma_f32_16x16x32_bf16 v[76:79], a[216:219], a[136:139], v[76:79]\n"
    "v_mfma_f32_16x16x32_bf16 v[44:47], a[216:219], a[132:135], v[44:47]\n"
    "v_mfma_f32_16x16x32_bf16 v[44:47], a[220:223], a[164:167], v[44:47]\n"
    "v_mfma_f32_16x16x32_bf16 v[12:15], a[220:223], a[160:163], v[12:15]\n"
    "v_mfma_f32_16x16x32_bf16 v[12:15], a[216:219], a[128:131], v[12:15]\n"
    "s_waitcnt vmcnt(10)\n"
    "v_mfma_f32_16x16x32_bf16 v[16:19], a[224:227], a[128:131], v[16:19]\n"
    "buffer_load_dwordx4 a[216:219], v240, s[20:23], s53 offen offset:2048 sc1\n"
    "v_mfma_f32_16x16x32_bf16 v[16:19], a[228:231], a[160:163], v[16:19]\n"
    "buffer_load_dwordx4 a[220:223], v240, s[20:23], s53 offen offset:3072 sc1\n"
    "v_mfma_f32_16x16x32_bf16 v[48:51], a[228:231], a[164:167], v[48:51]\n"
    "s_addk_i32 s53, 4096\n"
    "s_addk_i32 m0, 2048\n"
    "s_xor_b32 s90, s90, 0x101800\n"
    "v_mfma_f32_16x16x32_bf16 v[48:51], a[224:227], a[132:135], v[48:51]\n"
    "buffer_load_dwordx4 v240, s[32:35], s90 offen sc1 lds\n"
    "v_mfma_f32_16x16x32_bf16 v[80:83], a[224:227], a[136:139], v[80:83]\n"
    "v_mfma_f32_16x16x32_bf16 v[80:83], a[228:231], a[168:171], v[80:83]\n"
    "v_mfma_f32_16x16x32_bf16 v[112:115], a[228:231], a[172:175], v[112:115]\n"
    "v_mfma_f32_16x16x32_bf16 v[112:115], a[224:227], a[140:143], v[112:115]\n"
    "v_mfma_f32_16x16x32_bf16 v[144:147], a[224:227], a[144:147], v[144:147]\n"
    "v_mfma_f32_16x16x32_bf16 v[144:147], a[228:231], a[176:179], v[144:147]\n"
    "v_mfma_f32_16x16x32_bf16 v[176:179], a[228:231], a[180:183], v[176:179]\n"
    "v_mfma_f32_16x16x32_bf16 v[176:179], a[224:227], a[148:151], v[176:179]\n"
    "v_mfma_f32_16x16x32_bf16 v[208:211], a[224:227], a[152:155], v[208:211]\n"
    "v_mfma_f32_16x16x32_bf16 v[208:211], a[228:231], a[184:187], v[208:211]\n"
    "v_mfma_f32_16x16x32_bf16 a[240:243], a[228:231], a[188:191], a[240:243]\n"
    "s_add_u32 s62, s52, s64\n"
    "v_mov_b32 v242, s62\n"
    "s_mov_b32 s46, 260\n"
    "v_mfma_f32_16x16x32_bf16 a[240:243], a[224:227], a[156:159], a[240:243]\n"
    "s_waitcnt vmcnt(11)\n"
    "v_mfma_f32_16x16x32_bf16 a[244:247], a[232:235], a[156:159], a[244:247]\n"
    "buffer_store_dword v242, v240, s[44:47], 0 offen offset:256 sc1\n"
    "v_mfma_f32_16x16x32_bf16 a[244:247], a[236:239], a[188:191], a[244:247]\n"
    "buffer_load_dwordx4 a[228:231], v240, s[20:23], s53 offen offset:0 sc1\n"
    "v_mfma_f32_16x16x32_bf16 v[212:215], a[236:239], a[184:187], v[212:215]\n"
    "v_mfma_f32_16x16x32_bf16 v[212:215], a[232:235], a[152:155], v[212:215]\n"
    "v_mfma_f32_16x16x32_bf16 v[180:183], a[232:235], a[148:151], v[180:183]\n"
    "v_mfma_f32_16x16x32_bf16 v[180:183], a[236:239], a[180:183], v[180:183]\n"
    "v_mfma_f32_16x16x32_bf16 v[148:151], a[236:239], a[176:179], v[148:151]\n"
    "v_mfma_f32_16x16x32_bf16 v[148:151], a[232:235], a[144:147], v[148:151]\n"
    "v_mfma_f32_16x16x32_bf16 v[116:119], a[232:235], a[140:143], v[116:119]\n"
    "s_addk_i32 m0, 2048\n"
    "s_xor_b32 s90, s90, 0x800\n"
    "v_mfma_f32_16x16x32_bf16 v[116:119], a[236:239], a[172:175], v[116:119]\n"
    "buffer_load_dwordx4 v240, s[32:35], s90 offen sc1 lds\n"
    "v_mfma_f32_16x16x32_bf16 v[84:87], a[236:239], a[168:171], v[84:87]\n"
    "v_mfma_f32_16x16x32_bf16 v[84:87], a[232:235], a[136:139], v[84:87]\n"
    "v_mfma_f32_16x16x32_bf16 v[52:55], a[232:235], a[132:135], v[52:55]\n"
    "v_mfma_f32_16x16x32_bf16 v[52:55], a[236:239], a[164:167], v[52:55]\n"
    "v_mfma_f32_16x16x32_bf16 v[20:23], a[236:239], a[160:163], v[20:23]\n"
    "v_mfma_f32_16x16x32_bf16 v[20:23], a[232:235], a[128:131], v[20:23]\n"
    "s_waitcnt vmcnt(12)\n"
    "v_mfma_f32_16x16x32_bf16 v[24:27], a[192:195], a[128:131], v[24:27]\n"
    "buffer_load_dwordx4 a[232:235], v240, s[20:23], s53 offen offset:1024 sc1\n"
    "v_mfma_f32_16x16x32_bf16 v[24:27], a[196:199], a[160:163], v[24:27]\n"
    "buffer_load_dwordx4 a[236:239], v240, s[20:23], s53 offen offset:2048 sc1\n"
    "v_mfma_f32_16x16x32_bf16 v[56:59], a[196:199], a[164:167], v[56:59]\n"
    "v_mfma_f32_16x16x32_bf16 v[56:59], a[192:195], a[132:135], v[56:59]\n"
    "v_mfma_f32_16x16x32_bf16 v[88:91], a[192:195], a[136:139], v[88:91]\n"
    "v_mfma_f32_16x16x32_bf16 v[88:91], a[196:199], a[168:171], v[88:91]\n"
    "v_mfma_f32_16x16x32_bf16 v[120:123], a[196:199], a[172:175], v[120:123]\n"
    "v_mfma_f32_16x16x32_bf16 v[120:123], a[192:195], a[140:143], v[120:123]\n"
    "v_mfma_f32_16x16x32_bf16 v[152:155], a[192:195], a[144:147], v[152:155]\n"
    "v_mfma_f32_16x16x32_bf16 v[152:155], a[196:199], a[176:179], v[152:155]\n"
    "v_mfma_f32_16x16x32_bf16 v[184:187], a[196:199], a[180:183], v[184:187]\n"
    "v_mfma_f32_16x16x32_bf16 v[184:187], a[192:195], a[148:151], v[184:187]\n"
    "v_mfma_f32_16x16x32_bf16 v[216:219], a[192:195], a[152:155], v[216:219]\n"
    "v_mfma_f32_16x16x32_bf16 v[216:219], a[196:199], a[184:187], v[216:219]\n"
    "s_barrier\n"
    "v_mfma_f32_16x16x32_bf16 a[248:251], a[196:199], a[188:191], a[248:251]\n"
    "v_mfma_f32_16x16x32_bf16 a[248:251], a[192:195], a[156:159], a[248:251]\n"
    "s_waitcnt vmcnt(11)\n"
    "v_mfma_f32_16x16x32_bf16 a[252:255], a[200:203], a[156:159], a[252:255]\n"
    "buffer_load_dwordx4 a[192:195], v240, s[20:23], s53 offen offset:3072 sc1\n"
    "v_mfma_f32_16x16x32_bf16 a[252:255], a[204:207], a[188:191], a[252:255]\n"
    "s_addk_i32 s53, 4096\n"
    "v_mfma_f32_16x16x32_bf16 v[220:223], a[204:207], a[184:187], v[220:223]\n"
    "buffer_load_dwordx4 a[196:199], v240, s[20:23], s53 offen offset:0 sc1\n"
    "v_mfma_f32_16x16x32_bf16 v[220:223], a[200:203], a[152:155], v[220:223]\n"
    "ds_read_b128 a[156:159], v241 offset:47104\n"
    "ds_read_b128 a[188:191], v241 offset:48128\n"
    "v_mfma_f32_16x16x32_bf16 v[188:191], a[200:203], a[148:151], v[188:191]\n"
    "s_addk_i32 m0, 2048\n"
    "s_xor_b32 s90, s90, 0x185800\n"
    "v_mfma_f32_16x16x32_bf16 v[188:191], a[204:207], a[180:183], v[188:191]\n"
    "buffer_load_dwordx4 v240, s[32:35], s90 offen sc1 lds\n"
    "v_mfma_f32_16x16x32_bf16 v[156:159], a[204:207], a[176:179], v[156:159]\n"
    "ds_read_b128 a[184:187], v241 offset:46080\n"
    "ds_read_b128 a[152:155], v241 offset:45056\n"
    "v_mfma_f32_16x16x32_bf16 v[156:159], a[200:203], a[144:147], v[156:159]\n"
    "ds_read_b128 a[148:151], v241 offset:43008\n"
    "ds_read_b128 a[180:183], v241 offset:44032\n"
    "v_mfma_f32_16x16x32_bf16 v[124:127], a[200:203], a[140:143], v[124:127]\n"
    "ds_read_b128 a[176:179], v241 offset:41984\n"
    "v_mfma_f32_16x16x32_bf16 v[124:127], a[204:207], a[172:175], v[124:127]\n"
    "ds_read_b128 a[144:147], v241 offset:40960\n"
    "v_mfma_f32_16x16x32_bf16 v[92:95], a[204:207], a[168:171], v[92:95]\n"
    "ds_read_b128 a[140:143], v241 offset:38912\n"
    "v_mfma_f32_16x16x32_bf16 v[92:95], a[200:203], a[136:139], v[92:95]\n"
    "ds_read_b128 a[172:175], v241 offset:39936\n"
    "v_mfma_f32_16x16x32_bf16 v[60:63], a[200:203], a[132:135], v[60:63]\n"
    "ds_read_b128 a[168:171], v241 offset:37888\n"
    "v_mfma_f32_16x16x32_bf16 v[60:63], a[204:207], a[164:167], v[60:63]\n"
    "ds_read_b128 a[136:139], v241 offset:36864\n"
    "v_mfma_f32_16x16x32_bf16 v[28:31], a[204:207], a[160:163], v[28:31]\n"
    "ds_read_b128 a[132:135], v241 offset:34816\n"
    "v_mfma_f32_16x16x32_bf16 v[28:31], a[200:203], a[128:131], v[28:31]\n"
    "s_waitcnt vmcnt(11) lgkmcnt(11)\n"
    "ds_read_b128 a[164:167], v241 offset:35840\n"
    "v_mfma_f32_16x16x32_bf16 v[224:227], a[208:211], a[156:159], v[224:227]\n"
    "buffer_load_dwordx4 a[200:203], v240, s[20:23], s53 offen offset:1024 sc1\n"
    "v_mfma_f32_16x16x32_bf16 v[224:227], a[212:215], a[188:191], v[224:227]\n"
    "s_waitcnt lgkmcnt(10)\n"
    "v_mfma_f32_16x16x32_bf16 v[192:195], a[212:215], a[184:187], v[192:195]\n"
    "buffer_load_dwordx4 a[204:207], v240, s[20:23], s53 offen offset:2048 sc1\n"
    "v_mfma_f32_16x16x32_bf16 v[192:195], a[208:211], a[152:155], v[192:195]\n"
    "s_waitcnt lgkmcnt(8)\n"
    "ds_read_b128 a[160:163], v241 offset:33792\n"
    "ds_read_b128 a[128:131], v241 offset:32768\n"
    "v_mfma_f32_16x16x32_bf16 v[160:163], a[208:211], a[148:151], v[160:163]\n"
    "v_mfma_f32_16x16x32_bf16 v[160:163], a[212:215], a[180:183], v[160:163]\n"
    "s_waitcnt lgkmcnt(8)\n"
    "v_mfma_f32_16x16x32_bf16 v[128:131], a[212:215], a[176:179], v[128:131]\n"
    "v_mfma_f32_16x16x32_bf16 v[128:131], a[208:211], a[144:147], v[128:131]\n"
    "s_waitcnt lgkmcnt(6)\n"
    "v_mfma_f32_16x16x32_bf16 v[96:99], a[208:211], a[140:143], v[96:99]\n"
    "v_mfma_f32_16x16x32_bf16 v[96:99], a[212:215], a[172:175], v[96:99]\n"
    "s_waitcnt lgkmcnt(4)\n"
    "v_mfma_f32_16x16x32_bf16 v[64:67], a[212:215], a[168:171], v[64:67]\n"
    "v_mfma_f32_16x16x32_bf16 v[64:67], a[208:211], a[136:139], v[64:67]\n"
    "s_waitcnt lgkmcnt(2)\n"
    "v_mfma_f32_16x16x32_bf16 v[32:35], a[208:211], a[132:135], v[32:35]\n"
    "s_addk_i32 m0, 2048\n"
    "s_xor_b32 s90, s90, 0x800\n"
    "v_mfma_f32_16x16x32_bf16 v[32:35], a[212:215], a[164:167], v[32:35]\n"
    "s_waitcnt lgkmcnt(0)\n"
    "v_mfma_f32_16x16x32_bf16 v[0:3], a[212:215], a[160:163], v[0:3]\n"
    "buffer_load_dwordx4 v240, s[32:35], s90 offen sc1 lds\n"
    "v_mfma_f32_16x16x32_bf16 v[0:3], a[208:211], a[128:131], v[0:3]\n"
    "s_waitcnt vmcnt(12)\n"
    "v_mfma_f32_16x16x32_bf16 v[4:7], a[216:219], a[128:131], v[4:7]\n"
    "buffer_load_dwordx4 a[208:211], v240, s[20:23], s53 offen offset:3072 sc1\n"
    "v_mfma_f32_16x16x32_bf16 v[4:7], a[220:223], a[160:163], v[4:7]\n"
    "s_addk_i32 s53, 4096\n"
    "v_mfma_f32_16x16x32_bf16 v[36:39], a[220:223], a[164:167], v[36:39]\n"
    "buffer_load_dwordx4 a[212:215], v240, s[20:23], s53 offen offset:0 sc1\n"
    "v_mfma_f32_16x16x32_bf16 v[36:39], a[216:219], a[132:135], v[36:39]\n"
    "v_mfma_f32_16x16x32_bf16 v[68:71], a[216:219], a[136:139], v[68:71]\n"
    "v_mfma_f32_16x16x32_bf16 v[68:71], a[220:223], a[168:171], v[68:71]\n"
    "v_mfma_f32_16x16x32_bf16 v[100:103], a[220:223], a[172:175], v[100:103]\n"
    "v_mfma_f32_16x16x32_bf16 v[100:103], a[216:219], a[140:143], v[100:103]\n"
    "v_mfma_f32_16x16x32_bf16 v[132:135], a[216:219], a[144:147], v[132:135]\n"
    "v_mfma_f32_16x16x32_bf16 v[132:135], a[220:223], a[176:179], v[132:135]\n"
    "v_mfma_f32_16x16x32_bf16 v[164:167], a[220:223], a[180:183], v[164:167]\n"
    "v_mfma_f32_16x16x32_bf16 v[164:167], a[216:219], a[148:151], v[164:167]\n"
    "v_mfma_f32_16x16x32_bf16 v[196:199], a[216:219], a[152:155], v[196:199]\n"
    "v_mfma_f32_16x16x32_bf16 v[196:199], a[220:223], a[184:187], v[196:199]\n"
    "v_mfma_f32_16x16x32_bf16 v[228:231], a[220:223], a[188:191], v[228:231]\n"
    "v_mfma_f32_16x16x32_bf16 v[228:231], a[216:219], a[156:159], v[228:231]\n"
    "s_waitcnt vmcnt(9)\n"
    "v_mfma_f32_16x16x32_bf16 v[232:235], a[228:231], a[156:159], v[232:235]\n"
    "buffer_load_dwordx4 a[216:219], v240, s[20:23], s53 offen offset:1024 sc1\n"
    "v_mfma_f32_16x16x32_bf16 v[232:235], a[232:235], a[188:191], v[232:235]\n"
    "buffer_load_dwordx4 a[220:223], v240, s[20:23], s53 offen offset:2048 sc1\n"
    "v_mfma_f32_16x16x32_bf16 v[200:203], a[232:235], a[184:187], v[200:203]\n"
    "buffer_load_dwordx4 a[224:227], v240, s[20:23], s53 offen offset:3072 sc1\n"
    "v_mfma_f32_16x16x32_bf16 v[200:203], a[228:231], a[152:155], v[200:203]\n"
    "s_addk_i32 s53, 4096\n"
    "v_mfma_f32_16x16x32_bf16 v[168:171], a[228:231], a[148:151], v[168:171]\n"
    "s_addk_i32 m0, 2048\n"
    "s_xor_b32 s90, s90, 0x101800\n"
    "v_mfma_f32_16x16x32_bf16 v[168:171], a[232:235], a[180:183], v[168:171]\n"
    "buffer_load_dwordx4 v240, s[32:35], s90 offen sc1 lds\n"
    "v_mfma_f32_16x16x32_bf16 v[136:139], a[232:235], a[176:179], v[136:139]\n"
    "v_mfma_f32_16x16x32_bf16 v[136:139], a[228:231], a[144:147], v[136:139]\n"
    "v_mfma_f32_16x16x32_bf16 v[104:107], a[228:231], a[140:143], v[104:107]\n"
    "v_mfma_f32_16x16x32_bf16 v[104:107], a[232:235], a[172:175], v[104:107]\n"
    "v_mfma_f32_16x16x32_bf16 v[72:75], a[232:235], a[168:171], v[72:75]\n"
    "v_mfma_f32_16x16x32_bf16 v[72:75], a[228:231], a[136:139], v[72:75]\n"
    "v_mfma_f32_16x16x32_bf16 v[40:43], a[228:231], a[132:135], v[40:43]\n"
    "v_mfma_f32_16x16x32_bf16 v[40:43], a[232:235], a[164:167], v[40:43]\n"
    "v_mfma_f32_16x16x32_bf16 v[8:11], a[232:235], a[160:163], v[8:11]\n"
    "v_mfma_f32_16x16x32_bf16 v[8:11], a[228:231], a[128:131], v[8:11]\n"
    "s_waitcnt vmcnt(11)\n"
    "v_mfma_f32_16x16x32_bf16 v[12:15], a[236:239], a[128:131], v[12:15]\n"
    "buffer_load_dwordx4 a[228:231], v240, s[20:23], s53 offen offset:0 sc1\n"
    "v_mfma_f32_16x16x32_bf16 v[12:15], a[192:195], a[160:163], v[12:15]\n"
    "buffer_load_dwordx4 a[232:235], v240, s[20:23], s53 offen offset:1024 sc1\n"
    "v_mfma_f32_16x16x32_bf16 v[44:47], a[192:195], a[164:167], v[44:47]\n"
    "v_mfma_f32_16x16x32_bf16 v[44:47], a[236:239], a[132:135], v[44:47]\n"
    "v_mfma_f32_16x16x32_bf16 v[76:79], a[236:239], a[136:139], v[76:79]\n"
    "v_mfma_f32_16x16x32_bf16 v[76:79], a[192:195], a[168:171], v[76:79]\n"
    "v_mfma_f32_16x16x32_bf16 v[108:111], a[192:195], a[172:175], v[108:111]\n"
    "v_mfma_f32_16x16x32_bf16 v[108:111], a[236:239], a[140:143], v[108:111]\n"
    "v_mfma_f32_16x16x32_bf16 v[140:143], a[236:239], a[144:147], v[140:143]\n"
    "v_mfma_f32_16x16x32_bf16 v[140:143], a[192:195], a[176:179], v[140:143]\n"
    "v_mfma_f32_16x16x32_bf16 v[172:175], a[192:195], a[180:183], v[172:175]\n"
    "v_mfma_f32_16x16x32_bf16 v[172:175], a[236:239], a[148:151], v[172:175]\n"
    "v_mfma_f32_16x16x32_bf16 v[204:207], a[236:239], a[152:155], v[204:207]\n"
    "s_addk_i32 m0, 2048\n"
    "s_xor_b32 s90, s90, 0x800\n"
    "v_mfma_f32_16x16x32_bf16 v[204:207], a[192:195], a[184:187], v[204:207]\n"
    "buffer_load_dwordx4 v240, s[32:35], s90 offen sc1 lds\n"
    "v_mfma_f32_16x16x32_bf16 v[236:239], a[192:195], a[188:191], v[236:239]\n"
    "v_mfma_f32_16x16x32_bf16 v[236:239], a[236:239], a[156:159], v[236:239]\n"
    "s_waitcnt vmcnt(11)\n"
    "v_mfma_f32_16x16x32_bf16 a[240:243], a[196:199], a[156:159], a[240:243]\n"
    "buffer_load_dwordx4 a[236:239], v240, s[20:23], s53 offen offset:2048 sc1\n"
    "v_mfma_f32_16x16x32_bf16 a[240:243], a[200:203], a[188:191], a[240:243]\n"
    "buffer_load_dwordx4 a[192:195], v240, s[20:23], s53 offen offset:3072 sc1\n"
    "v_mfma_f32_16x16x32_bf16 v[208:211], a[200:203], a[184:187], v[208:211]\n"
    "s_addk_i32 s53, 4096\n"
    "v_mfma_f32_16x16x32_bf16 v[208:211], a[196:199], a[152:155], v[208:211]\n"
    "v_mfma_f32_16x16x32_bf16 v[176:179], a[196:199], a[148:151], v[176:179]\n"
    "v_mfma_f32_16x16x32_bf16 v[176:179], a[200:203], a[180:183], v[176:179]\n"
    "v_mfma_f32_16x16x32_bf16 v[144:147], a[200:203], a[176:179], v[144:147]\n"
    "v_mfma_f32_16x16x32_bf16 v[144:147], a[196:199], a[144:147], v[144:147]\n"
    "v_mfma_f32_16x16x32_bf16 v[112:115], a[196:199], a[140:143], v[112:115]\n"
    "v_mfma_f32_16x16x32_bf16 v[112:115], a[200:203], a[172:175], v[112:115]\n"
    "s_bitcmp1_b32 0x404, s52\n"
    "s_cbranch_scc1 OA_%=\n"
    "OB_%=:\n"
    "v_mfma_f32_16x16x32_bf16 v[80:83], a[200:203], a[168:171], v[80:83]\n"
    "v_mfma_f32_16x16x32_bf16 v[80:83], a[196:199], a[136:139], v[80:83]\n"
    "v_mfma_f32_16x16x32_bf16 v[48:51], a[196:199], a[132:135], v[48:51]\n"
    "v_mfma_f32_16x16x32_bf16 v[48:51], a[200:203], a[164:167], v[48:51]\n"
    "v_mfma_f32_16x16x32_bf16 v[16:19], a[200:203], a[160:163], v[16:19]\n"
    "v_mfma_f32_16x16x32_bf16 v[16:19], a[196:199], a[128:131], v[16:19]\n"
    "s_waitcnt vmcnt(10)\n"
    "v_mfma_f32_16x16x32_bf16 v[20:23], a[204:207], a[128:131], v[20:23]\n"
    "buffer_load_dwordx4 a[200:203], v240, s[20:23], s53 offen offset:0 sc1\n"
    "v_mfma_f32_16x16x32_bf16 v[20:23], a[208:211], a[160:163], v[20:23]\n"
    "v_mfma_f32_16x16x32_bf16 v[52:55], a[208:211], a[164:167], v[52:55]\n"
    "v_mfma_f32_16x16x32_bf16 v[52:55], a[204:207], a[132:135], v[52:55]\n"
    "v_mfma_f32_16x16x32_bf16 v[84:87], a[204:207], a[136:139], v[84:87]\n"
    "v_mfma_f32_16x16x32_bf16 v[84:87], a[208:211], a[168:171], v[84:87]\n"
    "v_mfma_f32_16x16x32_bf16 v[116:119], a[208:211], a[172:175], v[116:119]\n"
    "v_mfma_f32_16x16x32_bf16 v[116:119], a[204:207], a[140:143], v[116:119]\n"
    "v_mfma_f32_16x16x32_bf16 v[148:151], a[204:207], a[144:147], v[148:151]\n"
    "s_mov_b32 m0, s71\n"
    "s_xor_b32 s90, s90, 0x195800\n"
    "v_mfma_f32_16x16x32_bf16 v[148:151], a[208:211], a[176:179], v[148:151]\n"
    "buffer_load_dwordx4 v240, s[32:35], s90 offen sc1 lds\n"
    "v_mfma_f32_16x16x32_bf16 v[180:183], a[208:211], a[180:183], v[180:183]\n"
    "v_mfma_f32_16x16x32_bf16 v[180:183], a[204:207], a[148:151], v[180:183]\n"
    "v_mfma_f32_16x16x32_bf16 v[212:215], a[204:207], a[152:155], v[212:215]\n"
    "v_mfma_f32_16x16x32_bf16 v[212:215], a[208:211], a[184:187], v[212:215]\n"
    "v_mfma_f32_16x16x32_bf16 a[244:247], a[208:211], a[188:191], a[244:247]\n"
    "v_mfma_f32_16x16x32_bf16 a[244:247], a[204:207], a[156:159], a[244:247]\n"
    "s_waitcnt vmcnt(10)\n"
    "v_mfma_f32_16x16x32_bf16 a[248:251], a[212:215], a[156:159], a[248:251]\n"
    "buffer_load_dwordx4 a[204:207], v240, s[20:23], s53 offen offset:1024 sc1\n"
    "v_mfma_f32_16x16x32_bf16 a[248:251], a[216:219], a[188:191], a[248:251]\n"
    "buffer_load_dwordx4 a[208:211], v240, s[20:23], s53 offen offset:2048 sc1\n"
    "v_mfma_f32_16x16x32_bf16 v[216:219], a[216:219], a[184:187], v[216:219]\n"
    "v_mfma_f32_16x16x32_bf16 v[216:219], a[212:215], a[152:155], v[216:219]\n"
    "v_mfma_f32_16x16x32_bf16 v[184:187], a[212:215], a[148:151], v[184:187]\n"
    "v_mfma_f32_16x16x32_bf16 v[184:187], a[216:219], a[180:183], v[184:187]\n"
    "v_mfma_f32_16x16x32_bf16 v[152:155], a[216:219], a[176:179], v[152:155]\n"
    "v_mfma_f32_16x16x32_bf16 v[152:155], a[212:215], a[144:147], v[152:155]\n"
    "v_mfma_f32_16x16x32_bf16 v[120:123], a[212:215], a[140:143], v[120:123]\n"
    "v_mfma_f32_16x16x32_bf16 v[120:123], a[216:219], a[172:175], v[120:123]\n"
    "v_mfma_f32_16x16x32_bf16 v[88:91], a[216:219], a[168:171], v[88:91]\n"
    "v_mfma_f32_16x16x32_bf16 v[88:91], a[212:215], a[136:139], v[88:91]\n"
    "s_addk_i32 m0, 2048\n"
    "s_xor_b32 s90, s90, 0x800\n"
    "v_mfma_f32_16x16x32_bf16 v[56:59], a[212:215], a[132:135], v[56:59]\n"
    "buffer_load_dwordx4 v240, s[32:35], s90 offen sc1 lds\n"
    "v_mfma_f32_16x16x32_bf16 v[56:59], a[216:219], a[164:167], v[56:59]\n"
    "v_mfma_f32_16x16x32_bf16 v[24:27], a[216:219], a[160:163], v[24:27]\n"
    "v_mfma_f32_16x16x32_bf16 v[24:27], a[212:215], a[128:131], v[24:27]\n"
    "s_waitcnt vmcnt(11)\n"
    "v_mfma_f32_16x16x32_bf16 v[28:31], a[220:223], a[128:131], v[28:31]\n"
    "buffer_load_dwordx4 a[212:215], v240, s[20:23], s53 offen offset:3072 sc1\n"
    "v_mfma_f32_16x16x32_bf16 v[28:31], a[224:227], a[160:163], v[28:31]\n"
    "s_addk_i32 s53, 4096\n"
    "v_mfma_f32_16x16x32_bf16 v[60:63], a[224:227], a[164:167], v[60:63]\n"
    "buffer_load_dwordx4 a[216:219], v240, s[20:23], s53 offen offset:0 sc1\n"
    "v_mfma_f32_16x16x32_bf16 v[60:63], a[220:223], a[132:135], v[60:63]\n"
    "ds_read_b128 a[128:131], v241 offset:49152\n"
    "ds_read_b128 a[160:163], v241 offset:50176\n"
    "v_mfma_f32_16x16x32_bf16 v[92:95], a[220:223], a[136:139], v[92:95]\n"
    "ds_read_b128 a[164:167], v241 offset:52224\n"
    "v_mfma_f32_16x16x32_bf16 v[92:95], a[224:227], a[168:171], v[92:95]\n"
    "ds_read_b128 a[132:135], v241 offset:51200\n"
    "v_mfma_f32_16x16x32_bf16 v[124:127], a[224:227], a[172:175], v[124:127]\n"
    "ds_read_b128 a[136:139], v241 offset:53248\n"
    "v_mfma_f32_16x16x32_bf16 v[124:127], a[220:223], a[140:143], v[124:127]\n"
    "ds_read_b128 a[168:171], v241 offset:54272\n"
    "v_mfma_f32_16x16x32_bf16 v[156:159], a[220:223], a[144:147], v[156:159]\n"
    "ds_read_b128 a[172:175], v241 offset:56320\n"
    "v_mfma_f32_16x16x32_bf16 v[156:159], a[224:227], a[176:179], v[156:159]\n"
    "ds_read_b128 a[140:143], v241 offset:55296\n"
    "v_mfma_f32_16x16x32_bf16 v[188:191], a[224:227], a[180:183], v[188:191]\n"
    "ds_read_b128 a[144:147], v241 offset:57344\n"
    "v_mfma_f32_16x16x32_bf16 v[188:191], a[220:223], a[148:151], v[188:191]\n"
    "ds_read_b128 a[176:179], v241 offset:58368\n"
    "v_mfma_f32_16x16x32_bf16 v[220:223], a[220:223], a[152:155], v[220:223]\n"
    "ds_read_b128 a[180:183], v241 offset:60416\n"
    "v_mfma_f32_16x16x32_bf16 v[220:223], a[224:227], a[184:187], v[220:223]\n"
    "ds_read_b128 a[148:151], v241 offset:59392\n"
    "v_mfma_f32_16x16x32_bf16 a[252:255], a[224:227], a[188:191], a[252:255]\n"
    "s_addk_i32 m0, 2048\n"
    "s_xor_b32 s90, s90, 0x101800\n"
    "v_mfma_f32_16x16x32_bf16 a[252:255], a[220:223], a[156:159], a[252:255]\n"
    "s_waitcnt vmcnt(10) lgkmcnt(10)\n"
    "v_mfma_f32_16x16x32_bf16 v[0:3], a[228:231], a[128:131], v[0:3]\n"
    "buffer_load_dwordx4 v240, s[32:35], s90 offen sc1 lds\n"
    "v_mfma_f32_16x16x32_bf16 v[0:3], a[232:235], a[160:163], v[0:3]\n"
    "s_waitcnt lgkmcnt(8)\n"
    "ds_read_b128 a[152:155], v241 offset:61440\n"
    "ds_read_b128 a[184:187], v241 offset:62464\n"
    "v_mfma_f32_16x16x32_bf16 v[32:35], a[232:235], a[164:167], v[32:35]\n"
    "buffer_load_dwordx4 a[220:223], v240, s[20:23], s53 offen offset:1024 sc1\n"
    "v_mfma_f32_16x16x32_bf16 v[32:35], a[228:231], a[132:135], v[32:35]\n"
    "s_waitcnt lgkmcnt(8)\n"
    "v_mfma_f32_16x16x32_bf16 v[64:67], a[228:231], a[136:139], v[64:67]\n"
    "buffer_load_dwordx4 a[224:227], v240, s[20:23], s53 offen offset:2048 sc1\n"
    "ds_read_b128 a[188:191], v241 offset:64512\n"
    "ds_read_b128 a[156:159], v241 offset:63488\n"
    "v_mfma_f32_16x16x32_bf16 v[64:67], a[232:235], a[168:171], v[64:67]\n"
    "s_waitcnt lgkmcnt(8)\n"
    "v_mfma_f32_16x16x32_bf16 v[96:99], a[232:235], a[172:175], v[96:99]\n"
    "v_mfma_f32_16x16x32_bf16 v[96:99], a[228:231], a[140:143], v[96:99]\n"
    "s_waitcnt lgkmcnt(6)\n"
    "v_mfma_f32_16x16x32_bf16 v[128:131], a[228:231], a[144:147], v[128:131]\n"
    "v_mfma_f32_16x16x32_bf16 v[128:131], a[232:235], a[176:179], v[128:131]\n"
    "s_waitcnt lgkmcnt(4)\n"
    "v_mfma_f32_16x16x32_bf16 v[160:163], a[232:235], a[180:183], v[160:163]\n"
    "v_mfma_f32_16x16x32_bf16 v[160:163], a[228:231], a[148:151], v[160:163]\n"
    "s_waitcnt lgkmcnt(2)\n"
    "v_mfma_f32_16x16x32_bf16 v[192:195], a[228:231], a[152:155], v[192:195]\n"
    "v_mfma_f32_16x16x32_bf16 v[192:195], a[232:235], a[184:187], v[192:195]\n"
    "s_waitcnt lgkmcnt(0)\n"
    "v_mfma_f32_16x16x32_bf16 v[224:227], a[232:235], a[188:191], v[224:227]\n"
    "v_mfma_f32_16x16x32_bf16 v[224:227], a[228:231], a[156:159], v[224:227]\n"
    "s_waitcnt vmcnt(10)\n"
    "v_mfma_f32_16x16x32_bf16 v[228:231], a[236:239], a[156:159], v[228:231]\n"
    "buffer_load_dwordx4 a[228:231], v240, s[20:23], s53 offen offset:3072 sc1\n"
    "v_mfma_f32_16x16x32_bf16 v[228:231], a[192:195], a[188:191], v[228:231]\n"
    "s_addk_i32 s53, 4096\n"
    "v_mfma_f32_16x16x32_bf16 v[196:199], a[192:195], a[184:187], v[196:199]\n"
    "buffer_load_dwordx4 a[232:235], v240, s[20:23], s53 offen offset:0 sc1\n"
    "v_mfma_f32_16x16x32_bf16 v[196:199], a[236:239], a[152:155], v[196:199]\n"
    "s_addk_i32 m0, 2048\n"
    "s_xor_b32 s90, s90, 0x800\n"
    "v_mfma_f32_16x16x32_bf16 v[164:167], a[236:239], a[148:151], v[164:167]\n"
    "buffer_load_dwordx4 v240, s[32:35], s90 offen sc1 lds\n"
    "v_mfma_f32_16x16x32_bf16 v[164:167], a[192:195], a[180:183], v[164:167]\n"
    "v_mfma_f32_16x16x32_bf16 v[132:135], a[192:195], a[176:179], v[132:135]\n"
    "v_mfma_f32_16x16x32_bf16 v[132:135], a[236:239], a[144:147], v[132:135]\n"
    "v_mfma_f32_16x16x32_bf16 v[100:103], a[236:239], a[140:143], v[100:103]\n"
    "v_mfma_f32_16x16x32_bf16 v[100:103], a[192:195], a[172:175], v[100:103]\n"
    "v_mfma_f32_16x16x32_bf16 v[68:71], a[192:195], a[168:171], v[68:71]\n"
    "v_mfma_f32_16x16x32_bf16 v[68:71], a[236:239], a[136:139], v[68:71]\n"
    "v_mfma_f32_16x16x32_bf16 v[36:39], a[236:239], a[132:135], v[36:39]\n"
    "v_mfma_f32_16x16x32_bf16 v[36:39], a[192:195], a[164:167], v[36:39]\n"
    "s_mov_b32 s50, 320\n"
    "v_mfma_f32_16x16x32_bf16 v[4:7], a[192:195], a[160:163], v[4:7]\n"
    "buffer_load_dword v242, v240, s[48:51], 0 offen offset:256 sc1\n"
    "v_mfma_f32_16x16x32_bf16 v[4:7], a[236:239], a[128:131], v[4:7]\n"
    "s_waitcnt vmcnt(11)\n"
    "v_mfma_f32_16x16x32_bf16 v[8:11], a[200:203], a[128:131], v[8:11]\n"
    "buffer_load_dwordx4 a[236:239], v240, s[20:23], s53 offen offset:1024 sc1\n"
    "v_mfma_f32_16x16x32_bf16 v[8:11], a[204:207], a[160:163], v[8:11]\n"
    "buffer_load_dwordx4 a[192:195], v240, s[20:23], s53 offen offset:2048 sc1\n"
    "v_mfma_f32_16x16x32_bf16 v[40:43], a[204:207], a[164:167], v[40:43]\n"
    "buffer_load_dwordx4 a[196:199], v240, s[20:23], s53 offen offset:3072 sc1\n"
    "v_mfma_f32_16x16x32_bf16 v[40:43], a[200:203], a[132:135], v[40:43]\n"
    "s_addk_i32 s53, 4096\n"
    "v_mfma_f32_16x16x32_bf16 v[72:75], a[200:203], a[136:139], v[72:75]\n"
    "v_mfma_f32_16x16x32_bf16 v[72:75], a[204:207], a[168:171], v[72:75]\n"
    "v_mfma_f32_16x16x32_bf16 v[104:107], a[204:207], a[172:175], v[104:107]\n"
    "v_mfma_f32_16x16x32_bf16 v[104:107], a[200:203], a[140:143], v[104:107]\n"
    "v_mfma_f32_16x16x32_bf16 v[136:139], a[200:203], a[144:147], v[136:139]\n"
    "s_addk_i32 m0, 2048\n"
    "s_xor_b32 s90, s90, 0x185800\n"
    "v_mfma_f32_16x16x32_bf16 v[136:139], a[204:207], a[176:179], v[136:139]\n"
    "buffer_load_dwordx4 v240, s[32:35], s90 offen sc1 lds\n"
    "v_mfma_f32_16x16x32_bf16 v[168:171], a[204:207], a[180:183], v[168:171]\n"
    "v_mfma_f32_16x16x32_bf16 v[168:171], a[200:203], a[148:151], v[168:171]\n"
    "v_mfma_f32_16x16x32_bf16 v[200:203], a[200:203], a[152:155], v[200:203]\n"
    "v_mfma_f32_16x16x32_bf16 v[200:203], a[204:207], a[184:187], v[200:203]\n"
    "v_mfma_f32_16x16x32_bf16 v[232:235], a[204:207], a[188:191], v[232:235]\n"
    "v_mfma_f32_16x16x32_bf16 v[232:235], a[200:203], a[156:159], v[232:235]\n"
    "s_waitcnt vmcnt(12)\n"
    "v_mfma_f32_16x16x32_bf16 v[236:239], a[208:211], a[156:159], v[236:239]\n"
    "buffer_load_dwordx4 a[200:203], v240, s[20:23], s53 offen offset:0 sc1\n"
    "v_mfma_f32_16x16x32_bf16 v[236:239], a[212:215], a[188:191], v[236:239]\n"
    "buffer_load_dwordx4 a[204:207], v240, s[20:23], s53 offen offset:1024 sc1\n"
    "v_mfma_f32_16x16x32_bf16 v[204:207], a[212:215], a[184:187], v[204:207]\n"
    "v_mfma_f32_16x16x32_bf16 v[204:207], a[208:211], a[152:155], v[204:207]\n"
    "v_mfma_f32_16x16x32_bf16 v[172:175], a[208:211], a[148:151], v[172:175]\n"
    "v_mfma_f32_16x16x32_bf16 v[172:175], a[212:215], a[180:183], v[172:175]\n"
    "v_mfma_f32_16x16x32_bf16 v[140:143], a[212:215], a[176:179], v[140:143]\n"
    "v_mfma_f32_16x16x32_bf16 v[140:143], a[208:211], a[144:147], v[140:143]\n"
    "v_mfma_f32_16x16x32_bf16 v[108:111], a[208:211], a[140:143], v[108:111]\n"
    "v_mfma_f32_16x16x32_bf16 v[108:111], a[212:215], a[172:175], v[108:111]\n"
    "v_mfma_f32_16x16x32_bf16 v[76:79], a[212:215], a[168:171], v[76:79]\n"
    "v_mfma_f32_16x16x32_bf16 v[76:79], a[208:211], a[136:139], v[76:79]\n"
    "s_addk_i32 m0, 2048\n"
    "s_xor_b32 s90, s90, 0x800\n"
    "v_mfma_f32_16x16x32_bf16 v[44:47], a[208:211], a[132:135], v[44:47]\n"
    "buffer_load_dwordx4 v240, s[32:35], s90 offen sc1 lds\n"
    "v_mfma_f32_16x16x32_bf16 v[44:47], a[212:215], a[164:167], v[44:47]\n"
    "v_mfma_f32_16x16x32_bf16 v[12:15], a[212:215], a[160:163], v[12:15]\n"
    "v_mfma_f32_16x16x32_bf16 v[12:15], a[208:211], a[128:131], v[12:15]\n"
    "s_waitcnt vmcnt(12)\n"
    "v_mfma_f32_16x16x32_bf16 v[16:19], a[216:219], a[128:131], v[16:19]\n"
    "buffer_load_dwordx4 a[208:211], v240, s[20:23], s53 offen offset:2048 sc1\n"
    "v_mfma_f32_16x16x32_bf16 v[16:19], a[220:223], a[160:163], v[16:19]\n"
    "buffer_load_dwordx4 a[212:215], v240, s[20:23], s53 offen offset:3072 sc1\n"
    "v_mfma_f32_16x16x32_bf16 v[48:51], a[220:223], a[164:167], v[48:51]\n"
    "s_addk_i32 s53, 4096\n"
    "v_mfma_f32_16x16x32_bf16 v[48:51], a[216:219], a[132:135], v[48:51]\n"
    "v_mfma_f32_16x16x32_bf16 v[80:83], a[216:219], a[136:139], v[80:83]\n"
    "v_mfma_f32_16x16x32_bf16 v[80:83], a[220:223], a[168:171], v[80:83]\n"
    "v_mfma_f32_16x16x32_bf16 v[112:115], a[220:223], a[172:175], v[112:115]\n"
    "v_mfma_f32_16x16x32_bf16 v[112:115], a[216:219], a[140:143], v[112:115]\n"
    "v_mfma_f32_16x16x32_bf16 v[144:147], a[216:219], a[144:147], v[144:147]\n"
    "v_mfma_f32_16x16x32_bf16 v[144:147], a[220:223], a[176:179], v[144:147]\n"
    "v_mfma_f32_16x16x32_bf16 v[176:179], a[220:223], a[180:183], v[176:179]\n"
    "v_mfma_f32_16x16x32_bf16 v[176:179], a[216:219], a[148:151], v[176:179]\n"
    "v_mfma_f32_16x16x32_bf16 v[208:211], a[216:219], a[152:155], v[208:211]\n"
    "v_mfma_f32_16x16x32_bf16 v[208:211], a[220:223], a[184:187], v[208:211]\n"
    "v_mfma_f32_16x16x32_bf16 a[240:243], a[220:223], a[188:191], a[240:243]\n"
    "s_addk_i32 m0, 2048\n"
    "s_xor_b32 s90, s90, 0x101800\n"
    "v_mfma_f32_16x16x32_bf16 a[240:243], a[216:219], a[156:159], a[240:243]\n"
    "s_waitcnt vmcnt(12)\n"
    "v_mfma_f32_16x16x32_bf16 a[244:247], a[224:227], a[156:159], a[244:247]\n"
    "buffer_load_dwordx4 v240, s[32:35], s90 offen sc1 lds\n"
    "v_mfma_f32_16x16x32_bf16 a[244:247], a[228:231], a[188:191], a[244:247]\n"
    "buffer_load_dwordx4 a[220:223], v240, s[20:23], s53 offen offset:0 sc1\n"
    "v_mfma_f32_16x16x32_bf16 v[212:215], a[228:231], a[184:187], v[212:215]\n"
    "v_mfma_f32_16x16x32_bf16 v[212:215], a[224:227], a[152:155], v[212:215]\n"
    "v_mfma_f32_16x16x32_bf16 v[180:183], a[224:227], a[148:151], v[180:183]\n"
    "v_mfma_f32_16x16x32_bf16 v[180:183], a[228:231], a[180:183], v[180:183]\n"
    "v_mfma_f32_16x16x32_bf16 v[148:151], a[228:231], a[176:179], v[148:151]\n"
    "v_mfma_f32_16x16x32_bf16 v[148:151], a[224:227], a[144:147], v[148:151]\n"
    "v_mfma_f32_16x16x32_bf16 v[116:119], a[224:227], a[140:143], v[116:119]\n"
    "v_mfma_f32_16x16x32_bf16 v[116:119], a[228:231], a[172:175], v[116:119]\n"
    "v_mfma_f32_16x16x32_bf16 v[84:87], a[228:231], a[168:171], v[84:87]\n"
    "v_mfma_f32_16x16x32_bf16 v[84:87], a[224:227], a[136:139], v[84:87]\n"
    "v_mfma_f32_16x16x32_bf16 v[52:55], a[224:227], a[132:135], v[52:55]\n"
    "v_mfma_f32_16x16x32_bf16 v[52:55], a[228:231], a[164:167], v[52:55]\n"
    "v_mfma_f32_16x16x32_bf16 v[20:23], a[228:231], a[160:163], v[20:23]\n"
    "v_mfma_f32_16x16x32_bf16 v[20:23], a[224:227], a[128:131], v[20:23]\n"
    "s_waitcnt vmcnt(10)\n"
    "v_mfma_f32_16x16x32_bf16 v[24:27], a[232:235], a[128:131], v[24:27]\n"
    "buffer_load_dwordx4 a[224:227], v240, s[20:23], s53 offen offset:1024 sc1\n"
    "v_mfma_f32_16x16x32_bf16 v[24:27], a[236:239], a[160:163], v[24:27]\n"
    "buffer_load_dwordx4 a[228:231], v240, s[20:23], s53 offen offset:2048 sc1\n"
    "v_mfma_f32_16x16x32_bf16 v[56:59], a[236:239], a[164:167], v[56:59]\n"
    "s_addk_i32 m0, 2048\n"
    "s_xor_b32 s90, s90, 0x800\n"
    "v_mfma_f32_16x16x32_bf16 v[56:59], a[232:235], a[132:135], v[56:59]\n"
    "buffer_load_dwordx4 v240, s[32:35], s90 offen sc1 lds\n"
    "v_mfma_f32_16x16x32_bf16 v[88:91], a[232:235], a[136:139], v[88:91]\n"
    "v_mfma_f32_16x16x32_bf16 v[88:91], a[236:239], a[168:171], v[88:91]\n"
    "v_mfma_f32_16x16x32_bf16 v[120:123], a[236:239], a[172:175], v[120:123]\n"
    "v_mfma_f32_16x16x32_bf16 v[120:123], a[232:235], a[140:143], v[120:123]\n"
    "v_mfma_f32_16x16x32_bf16 v[152:155], a[232:235], a[144:147], v[152:155]\n"
    "v_mfma_f32_16x16x32_bf16 v[152:155], a[236:239], a[176:179], v[152:155]\n"
    "v_mfma_f32_16x16x32_bf16 v[184:187], a[236:239], a[180:183], v[184:187]\n"
    "v_mfma_f32_16x16x32_bf16 v[184:187], a[232:235], a[148:151], v[184:187]\n"
    "v_mfma_f32_16x16x32_bf16 v[216:219], a[232:235], a[152:155], v[216:219]\n"
    "v_mfma_f32_16x16x32_bf16 v[216:219], a[236:239], a[184:187], v[216:219]\n"
    "s_barrier\n"
    "v_mfma_f32_16x16x32_bf16 a[248:251], a[236:239], a[188:191], a[248:251]\n"
    "v_mfma_f32_16x16x32_bf16 a[248:251], a[232:235], a[156:159], a[248:251]\n"
    "s_waitcnt vmcnt(11)\n"
    "v_mfma_f32_16x16x32_bf16 a[252:255], a[192:195], a[156:159], a[252:255]\n"
    "buffer_load_dwordx4 a[232:235], v240, s[20:23], s53 offen offset:3072 sc1\n"
    "v_mfma_f32_16x16x32_bf16 a[252:255], a[196:199], a[188:191], a[252:255]\n"
    "s_addk_i32 s53, 4096\n"
    "v_mfma_f32_16x16x32_bf16 v[220:223], a[196:199], a[184:187], v[220:223]\n"
    "buffer_load_dwordx4 a[236:239], v240, s[20:23], s53 offen offset:0 sc1\n"
    "v_mfma_f32_16x16x32_bf16 v[220:223], a[192:195], a[152:155], v[220:223]\n"
    "v_add_u32 v241, s67, v240\n"
    "ds_read_b128 a[156:159], v241 offset:14336\n"
    "v_mfma_f32_16x16x32_bf16 v[188:191], a[192:195], a[148:151], v[188:191]\n"
    "ds_read_b128 a[188:191], v241 offset:15360\n"
    "ds_read_b128 a[184:187], v241 offset:13312\n"
    "v_mfma_f32_16x16x32_bf16 v[188:191], a[196:199], a[180:183], v[188:191]\n"
    "ds_read_b128 a[152:155], v241 offset:12288\n"
    "v_mfma_f32_16x16x32_bf16 v[156:159], a[196:199], a[176:179], v[156:159]\n"
    "ds_read_b128 a[148:151], v241 offset:10240\n"
    "v_mfma_f32_16x16x32_bf16 v[156:159], a[192:195], a[144:147], v[156:159]\n"
    "v_cmp_le_u32 vcc, s52, v242\n"
    "s_nop 1\n"
    "s_and_b32 s63, vcc_lo, 15\n"
    "s_cmp_eq_u32 s63, 15\n"
    "s_cbranch_scc1 LA8_%=\n"
    "s_mov_b32 s69, 0x400000\n"
    "LA9_%=:\n"
    "s_sleep 1\n"
    "buffer_load_dword v242, v240, s[48:51], 0 offen offset:256 sc1\n"
    "s_waitcnt vmcnt(0)\n"
    "v_cmp_le_u32 vcc, s52, v242\n"
    "s_sub_u32 s69, s69, 1\n"
    "s_cbranch_scc1 LA8_%=\n"
    "s_and_b32 s63, vcc_lo, 15\n"
    "s_cmp_eq_u32 s63, 15\n"
    "s_cbranch_scc0 LA9_%=\n"
    "LA8_%=:\n"
    "v_mfma_f32_16x16x32_bf16 v[124:127], a[192:195], a[140:143], v[124:127]\n"
    "ds_read_b128 a[180:183], v241 offset:11264\n"
    "s_add_u32 m0, s71, 32768\n"
    "v_mfma_f32_16x16x32_bf16 v[124:127], a[196:199], a[172:175], v[124:127]\n"
    "s_xor_b32 s90, s90, 0x1b5800\n"
    "v_mfma_f32_16x16x32_bf16 v[92:95], a[196:199], a[168:171], v[92:95]\n"
    "buffer_load_dwordx4 v240, s[32:35], s90 offen sc1 lds\n"
    "v_mfma_f32_16x16x32_bf16 v[92:95], a[192:195], a[136:139], v[92:95]\n"
    "ds_read_b128 a[176:179], v241 offset:9216\n"
    "ds_read_b128 a[144:147], v241 offset:8192\n"
    "v_mfma_f32_16x16x32_bf16 v[60:63], a[192:195], a[132:135], v[60:63]\n"
    "ds_read_b128 a[140:143], v241 offset:6144\n"
    "ds_read_b128 a[172:175], v241 offset:7168\n"
    "v_mfma_f32_16x16x32_bf16 v[60:63], a[196:199], a[164:167], v[60:63]\n"
    "ds_read_b128 a[168:171], v241 offset:5120\n"
    "ds_read_b128 a[136:139], v241 offset:4096\n"
    "v_mfma_f32_16x16x32_bf16 v[28:31], a[196:199], a[160:163], v[28:31]\n"
    "ds_read_b128 a[132:135], v241 offset:2048\n"
    "v_mfma_f32_16x16x32_bf16 v[28:31], a[192:195], a[128:131], v[28:31]\n"
    "s_waitcnt vmcnt(11) lgkmcnt(11)\n"
    "ds_read_b128 a[164:167], v241 offset:3072\n"
    "v_mfma_f32_16x16x32_bf16 v[224:227], a[200:203], a[156:159], v[224:227]\n"
    "buffer_load_dwordx4 a[192:195], v240, s[20:23], s53 offen offset:1024 sc1\n"
    "v_mfma_f32_16x16x32_bf16 v[224:227], a[204:207], a[188:191], v[224:227]\n"
    "s_waitcnt lgkmcnt(10)\n"
    "v_mfma_f32_16x16x32_bf16 v[192:195], a[204:207], a[184:187], v[192:195]\n"
    "buffer_load_dwordx4 a[196:199], v240, s[20:23], s53 offen offset:2048 sc1\n"
    "v_mfma_f32_16x16x32_bf16 v[192:195], a[200:203], a[152:155], v[192:195]\n"
    "s_waitcnt lgkmcnt(8)\n"
    "ds_read_b128 a[160:163], v241 offset:1024\n"
    "ds_read_b128 a[128:131], v241 offset:0\n"
    "v_mfma_f32_16x16x32_bf16 v[160:163], a[200:203], a[148:151], v[160:163]\n"
    "v_mfma_f32_16x16x32_bf16 v[160:163], a[204:207], a[180:183], v[160:163]\n"
    "s_waitcnt lgkmcnt(8)\n"
    "v_mfma_f32_16x16x32_bf16 v[128:131], a[204:207], a[176:179], v[128:131]\n"
    "v_mfma_f32_16x16x32_bf16 v[128:131], a[200:203], a[144:147], v[128:131]\n"
    "s_waitcnt lgkmcnt(6)\n"
    "v_mfma_f32_16x16x32_bf16 v[96:99], a[200:203], a[140:143], v[96:99]\n"
    "s_addk_i32 m0, 2048\n"
    "s_xor_b32 s90, s90, 0x800\n"
    "v_mfma_f32_16x16x32_bf16 v[96:99], a[204:207], a[172:175], v[96:99]\n"
    "s_waitcnt lgkmcnt(4)\n"
    "v_mfma_f32_16x16x32_bf16 v[64:67], a[204:207], a[168:171], v[64:67]\n"
    "buffer_load_dwordx4 v240, s[32:35], s90 offen sc1 lds\n"
    "v_mfma_f32_16x16x32_bf16 v[64:67], a[200:203], a[136:139], v[64:67]\n"
    "s_waitcnt lgkmcnt(2)\n"
    "v_mfma_f32_16x16x32_bf16 v[32:35], a[200:203], a[132:135], v[32:35]\n"
    "v_mfma_f32_16x16x32_bf16 v[32:35], a[204:207], a[164:167], v[32:35]\n"
    "s_waitcnt lgkmcnt(0)\n"
    "v_mfma_f32_16x16x32_bf16 v[0:3], a[204:207], a[160:163], v[0:3]\n"
    "v_mfma_f32_16x16x32_bf16 v[0:3], a[200:203], a[128:131], v[0:3]\n"
    "s_waitcnt vmcnt(11)\n"
    "v_mfma_f32_16x16x32_bf16 v[4:7], a[208:211], a[128:131], v[4:7]\n"
    "buffer_load_dwordx4 a[200:203], v240, s[20:23], s53 offen offset:3072 sc1\n"
    "v_mfma_f32_16x16x32_bf16 v[4:7], a[212:215], a[160:163], v[4:7]\n"
    "s_addk_i32 s53, 4096\n"
    "v_mfma_f32_16x16x32_bf16 v[36:39], a[212:215], a[164:167], v[36:39]\n"
    "buffer_load_dwordx4 a[204:207], v240, s[20:23], s53 offen offset:0 sc1\n"
    "v_mfma_f32_16x16x32_bf16 v[36:39], a[208:211], a[132:135], v[36:39]\n"
    "v_mfma_f32_16x16x32_bf16 v[68:71], a[208:211], a[136:139], v[68:71]\n"
    "v_mfma_f32_16x16x32_bf16 v[68:71], a[212:215], a[168:171], v[68:71]\n"
    "v_mfma_f32_16x16x32_bf16 v[100:103], a[212:215], a[172:175], v[100:103]\n"
    "v_mfma_f32_16x16x32_bf16 v[100:103], a[208:211], a[140:143], v[100:103]\n"
    "v_mfma_f32_16x16x32_bf16 v[132:135], a[208:211], a[144:147], v[132:135]\n"
    "s_addk_i32 m0, 2048\n"
    "s_xor_b32 s90, s90, 0x101800\n"
    "v_mfma_f32_16x16x32_bf16 v[132:135], a[212:215], a[176:179], v[132:135]\n"
    "buffer_load_dwordx4 v240, s[32:35], s90 offen sc1 lds\n"
    "v_mfma_f32_16x16x32_bf16 v[164:167], a[212:215], a[180:183], v[164:167]\n"
    "v_mfma_f32_16x16x32_bf16 v[164:167], a[208:211], a[148:151], v[164:167]\n"
    "v_mfma_f32_16x16x32_bf16 v[196:199], a[208:211], a[152:155], v[196:199]\n"
    "v_mfma_f32_16x16x32_bf16 v[196:199], a[212:215], a[184:187], v[196:199]\n"
    "v_mfma_f32_16x16x32_bf16 v[228:231], a[212:215], a[188:191], v[228:231]\n"
    "v_mfma_f32_16x16x32_bf16 v[228:231], a[208:211], a[156:159], v[228:231]\n"
    "s_waitcnt vmcnt(11)\n"
    "v_mfma_f32_16x16x32_bf16 v[232:235], a[220:223], a[156:159], v[232:235]\n"
    "buffer_load_dwordx4 a[208:211], v240, s[20:23], s53 offen offset:1024 sc1\n"
    "v_mfma_f32_16x16x32_bf16 v[232:235], a[224:227], a[188:191], v[232:235]\n"
    "buffer_load_dwordx4 a[212:215], v240, s[20:23], s53 offen offset:2048 sc1\n"
    "v_mfma_f32_16x16x32_bf16 v[200:203], a[224:227], a[184:187], v[200:203]\n"
    "buffer_load_dwordx4 a[216:219], v240, s[20:23], s53 offen offset:3072 sc1\n"
    "v_mfma_f32_16x16x32_bf16 v[200:203], a[220:223], a[152:155], v[200:203]\n"
    "s_addk_i32 s53, 4096\n"
    "v_mfma_f32_16x16x32_bf16 v[168:171], a[220:223], a[148:151], v[168:171]\n"
    "v_mfma_f32_16x16x32_bf16 v[168:171], a[224:227], a[180:183], v[168:171]\n"
    "v_mfma_f32_16x16x32_bf16 v[136:139], a[224:227], a[176:179], v[136:139]\n"
    "v_mfma_f32_16x16x32_bf16 v[136:139], a[220:223], a[144:147], v[136:139]\n"
    "v_mfma_f32_16x16x32_bf16 v[104:107], a[220:223], a[140:143], v[104:107]\n"
    "s_addk_i32 m0, 2048\n"
    "s_xor_b32 s90, s90, 0x800\n"
    "v_mfma_f32_16x16x32_bf16 v[104:107], a[224:227], a[172:175], v[104:107]\n"
    "buffer_load_dwordx4 v240, s[32:35], s90 offen sc1 lds\n"
    "v_mfma_f32_16x16x32_bf16 v[72:75], a[224:227], a[168:171], v[72:75]\n"
    "v_mfma_f32_16x16x32_bf16 v[72:75], a[220:223], a[136:139], v[72:75]\n"
    "v_mfma_f32_16x16x32_bf16 v[40:43], a[220:223], a[132:135], v[40:43]\n"
    "v_mfma_f32_16x16x32_bf16 v[40:43], a[224:227], a[164:167], v[40:43]\n"
    "v_mfma_f32_16x16x32_bf16 v[8:11], a[224:227], a[160:163], v[8:11]\n"
    "v_mfma_f32_16x16x32_bf16 v[8:11], a[220:223], a[128:131], v[8:11]\n"
    "s_waitcnt vmcnt(12)\n"
    "v_mfma_f32_16x16x32_bf16 v[12:15], a[228:231], a[128:131], v[12:15]\n"
    "buffer_load_dwordx4 a[220:223], v240, s[20:23], s53 offen offset:0 sc1\n"
    "v_mfma_f32_16x16x32_bf16 v[12:15], a[232:235], a[160:163], v[12:15]\n"
    "buffer_load_dwordx4 a[224:227], v240, s[20:23], s53 offen offset:1024 sc1\n"
    "v_mfma_f32_16x16x32_bf16 v[44:47], a[232:235], a[164:167], v[44:47]\n"
    "v_mfma_f32_16x16x32_bf16 v[44:47], a[228:231], a[132:135], v[44:47]\n"
    "v_mfma_f32_16x16x32_bf16 v[76:79], a[228:231], a[136:139], v[76:79]\n"
    "v_mfma_f32_16x16x32_bf16 v[76:79], a[232:235], a[168:171], v[76:79]\n"
    "v_mfma_f32_16x16x32_bf16 v[108:111], a[232:235], a[172:175], v[108:111]\n"
    "v_mfma_f32_16x16x32_bf16 v[108:111], a[228:231], a[140:143], v[108:111]\n"
    "v_mfma_f32_16x16x32_bf16 v[140:143], a[228:231], a[144:147], v[140:143]\n"
    "v_mfma_f32_16x16x32_bf16 v[140:143], a[232:235], a[176:179], v[140:143]\n"
    "v_mfma_f32_16x16x32_bf16 v[172:175], a[232:235], a[180:183], v[172:175]\n"
    "v_mfma_f32_16x16x32_bf16 v[172:175], a[228:231], a[148:151], v[172:175]\n"
    "v_mfma_f32_16x16x32_bf16 v[204:207], a[228:231], a[152:155], v[204:207]\n"
    "v_mfma_f32_16x16x32_bf16 v[204:207], a[232:235], a[184:187], v[204:207]\n"
    "v_mfma_f32_16x16x32_bf16 v[236:239], a[232:235], a[188:191], v[236:239]\n"
    "s_addk_i32 m0, 2048\n"
    "s_xor_b32 s90, s90, 0x185800\n"
    "v_mfma_f32_16x16x32_bf16 v[236:239], a[228:231], a[156:159], v[236:239]\n"
    "s_waitcnt vmcnt(11)\n"
    "v_mfma_f32_16x16x32_bf16 a[240:243], a[236:239], a[156:159], a[240:243]\n"
    "buffer_load_dwordx4 v240, s[32:35], s90 offen sc1 lds\n"
    "v_mfma_f32_16x16x32_bf16 a[240:243], a[192:195], a[188:191], a[240:243]\n"
    "buffer_load_dwordx4 a[228:231], v240, s[20:23], s53 offen offset:2048 sc1\n"
    "v_mfma_f32_16x16x32_bf16 v[208:211], a[192:195], a[184:187], v[208:211]\n"
    "buffer_load_dwordx4 a[232:235], v240, s[20:23], s53 offen offset:3072 sc1\n"
    "v_mfma_f32_16x16x32_bf16 v[208:211], a[236:239], a[152:155], v[208:211]\n"
    "s_addk_i32 s53, 4096\n"
    "v_mfma_f32_16x16x32_bf16 v[176:179], a[236:239], a[148:151], v[176:179]\n"
    "v_mfma_f32_16x16x32_bf16 v[176:179], a[192:195], a[180:183], v[176:179]\n"
    "v_mfma_f32_16x16x32_bf16 v[144:147], a[192:195], a[176:179], v[144:147]\n"
    "v_mfma_f32_16x16x32_bf16 v[144:147], a[236:239], a[144:147], v[144:147]\n"
    "v_mfma_f32_16x16x32_bf16 v[112:115], a[236:239], a[140:143], v[112:115]\n"
    "v_mfma_f32_16x16x32_bf16 v[112:115], a[192:195], a[172:175], v[112:115]\n"
    "v_mfma_f32_16x16x32_bf16 v[80:83], a[192:195], a[168:171], v[80:83]\n"
    "v_mfma_f32_16x16x32_bf16 v[80:83], a[236:239], a[136:139], v[80:83]\n"
    "v_mfma_f32_16x16x32_bf16 v[48:51], a[236:239], a[132:135], v[48:51]\n"
    "v_mfma_f32_16x16x32_bf16 v[48:51], a[192:195], a[164:167], v[48:51]\n"
    "v_mfma_f32_16x16x32_bf16 v[16:19], a[192:195], a[160:163], v[16:19]\n"
    "s_addk_i32 m0, 2048\n"
    "s_xor_b32 s90, s90, 0x800\n"
    "v_mfma_f32_16x16x32_bf16 v[16:19], a[236:239], a[128:131], v[16:19]\n"
    "s_waitcnt vmcnt(11)\n"
    "v_mfma_f32_16x16x32_bf16 v[20:23], a[196:199], a[128:131], v[20:23]\n"
    "buffer_load_dwordx4 v240, s[32:35], s90 offen sc1 lds\n"
    "v_mfma_f32_16x16x32_bf16 v[20:23], a[200:203], a[160:163], v[20:23]\n"
    "buffer_load_dwordx4 a[192:195], v240, s[20:23], s53 offen offset:0 sc1\n"
    "v_mfma_f32_16x16x32_bf16 v[52:55], a[200:203], a[164:167], v[52:55]\n"
    "v_mfma_f32_16x16x32_bf16 v[52:55], a[196:199], a[132:135], v[52:55]\n"
    "v_mfma_f32_16x16x32_bf16 v[84:87], a[196:199], a[136:139], v[84:87]\n"
    "v_mfma_f32_16x16x32_bf16 v[84:87], a[200:203], a[168:171], v[84:87]\n"
    "v_mfma_f32_16x16x32_bf16 v[116:119], a[200:203], a[172:175], v[116:119]\n"
    "v_mfma_f32_16x16x32_bf16 v[116:119], a[196:199], a[140:143], v[116:119]\n"
    "v_mfma_f32_16x16x32_bf16 v[148:151], a[196:199], a[144:147], v[148:151]\n"
    "v_mfma_f32_16x16x32_bf16 v[148:151], a[200:203], a[176:179], v[148:151]\n"
    "v_mfma_f32_16x16x32_bf16 v[180:183], a[200:203], a[180:183], v[180:183]\n"
    "v_mfma_f32_16x16x32_bf16 v[180:183], a[196:199], a[148:151], v[180:183]\n"
    "v_mfma_f32_16x16x32_bf16 v[212:215], a[196:199], a[152:155], v[212:215]\n"
    "v_mfma_f32_16x16x32_bf16 v[212:215], a[200:203], a[184:187], v[212:215]\n"
    "v_mfma_f32_16x16x32_bf16 a[244:247], a[200:203], a[188:191], a[244:247]\n"
    "s_addk_i32 m0, 2048\n"
    "s_xor_b32 s90, s90, 0x101800\n"
    "v_mfma_f32_16x16x32_bf16 a[244:247], a[196:199], a[156:159], a[244:247]\n"
    "s_waitcnt vmcnt(10)\n"
    "v_mfma_f32_16x16x32_bf16 a[248:251], a[204:207], a[156:159], a[248:251]\n"
    "buffer_load_dwordx4 v240, s[32:35], s90 offen sc1 lds\n"
    "v_mfma_f32_16x16x32_bf16 a[248:251], a[208:211], a[188:191], a[248:251]\n"
    "buffer_load_dwordx4 a[196:199], v240, s[20:23], s53 offen offset:1024 sc1\n"
    "v_mfma_f32_16x16x32_bf16 v[216:219], a[208:211], a[184:187], v[216:219]\n"
    "buffer_load_dwordx4 a[200:203], v240, s[20:23], s53 offen offset:2048 sc1\n"
    "v_mfma_f32_16x16x32_bf16 v[216:219], a[204:207], a[152:155], v[216:219]\n"
    "v_mfma_f32_16x16x32_bf16 v[184:187], a[204:207], a[148:151], v[184:187]\n"
    "v_mfma_f32_16x16x32_bf16 v[184:187], a[208:211], a[180:183], v[184:187]\n"
    "v_mfma_f32_16x16x32_bf16 v[152:155], a[208:211], a[176:179], v[152:155]\n"
    "v_mfma_f32_16x16x32_bf16 v[152:155], a[204:207], a[144:147], v[152:155]\n"
    "v_mfma_f32_16x16x32_bf16 v[120:123], a[204:207], a[140:143], v[120:123]\n"
    "v_mfma_f32_16x16x32_bf16 v[120:123], a[208:211], a[172:175], v[120:123]\n"
    "v_mfma_f32_16x16x32_bf16 v[88:91], a[208:211], a[168:171], v[88:91]\n"
    "v_mfma_f32_16x16x32_bf16 v[88:91], a[204:207], a[136:139], v[88:91]\n"
    "v_mfma_f32_16x16x32_bf16 v[56:59], a[204:207], a[132:135], v[56:59]\n"
    "v_mfma_f32_16x16x32_bf16 v[56:59], a[208:211], a[164:167], v[56:59]\n"
    "v_mfma_f32_16x16x32_bf16 v[24:27], a[208:211], a[160:163], v[24:27]\n"
    "s_addk_i32 m0, 2048\n"
    "s_xor_b32 s90, s90, 0x800\n"
    "v_mfma_f32_16x16x32_bf16 v[24:27], a[204:207], a[128:131], v[24:27]\n"
    "s_waitcnt vmcnt(11)\n"
    "v_mfma_f32_16x16x32_bf16 v[28:31], a[212:215], a[128:131], v[28:31]\n"
    "buffer_load_dwordx4 v240, s[32:35], s90 offen sc1 lds\n"
    "v_mfma_f32_16x16x32_bf16 v[28:31], a[216:219], a[160:163], v[28:31]\n"
    "buffer_load_dwordx4 a[204:207], v240, s[20:23], s53 offen offset:3072 sc1\n"
    "v_mfma_f32_16x16x32_bf16 v[60:63], a[216:219], a[164:167], v[60:63]\n"
    "s_addk_i32 s53, 4096\n"
    "v_mfma_f32_16x16x32_bf16 v[60:63], a[212:215], a[132:135], v[60:63]\n"
    "buffer_load_dwordx4 a[208:211], v240, s[20:23], s53 offen offset:0 sc1\n"
    "v_mfma_f32_16x16x32_bf16 v[92:95], a[212:215], a[136:139], v[92:95]\n"
    "ds_read_b128 a[128:131], v241 offset:16384\n"
    "ds_read_b128 a[160:163], v241 offset:17408\n"
    "v_mfma_f32_16x16x32_bf16 v[92:95], a[216:219], a[168:171], v[92:95]\n"
    "ds_read_b128 a[164:167], v241 offset:19456\n"
    "ds_read_b128 a[132:135], v241 offset:18432\n"
    "v_mfma_f32_16x16x32_bf16 v[124:127], a[216:219], a[172:175], v[124:127]\n"
    "ds_read_b128 a[136:139], v241 offset:20480\n"
    "v_mfma_f32_16x16x32_bf16 v[124:127], a[212:215], a[140:143], v[124:127]\n"
    "ds_read_b128 a[168:171], v241 offset:21504\n"
    "v_mfma_f32_16x16x32_bf16 v[156:159], a[212:215], a[144:147], v[156:159]\n"
    "ds_read_b128 a[172:175], v241 offset:23552\n"
    "v_mfma_f32_16x16x32_bf16 v[156:159], a[216:219], a[176:179], v[156:159]\n"
    "ds_read_b128 a[140:143], v241 offset:22528\n"
    "v_mfma_f32_16x16x32_bf16 v[188:191], a[216:219], a[180:183], v[188:191]\n"
    "ds_read_b128 a[144:147], v241 offset:24576\n"
    "v_mfma_f32_16x16x32_bf16 v[188:191], a[212:215], a[148:151], v[188:191]\n"
    "ds_read_b128 a[176:179], v241 offset:25600\n"
    "v_mfma_f32_16x16x32_bf16 v[220:223], a[212:215], a[152:155], v[220:223]\n"
    "ds_read_b128 a[180:183], v241 offset:27648\n"
    "v_mfma_f32_16x16x32_bf16 v[220:223], a[216:219], a[184:187], v[220:223]\n"
    "ds_read_b128 a[148:151], v241 offset:26624\n"
    "v_mfma_f32_16x16x32_bf16 a[252:255], a[216:219], a[188:191], a[252:255]\n"
    "ds_read_b128 a[152:155], v241 offset:28672\n"
    "v_mfma_f32_16x16x32_bf16 a[252:255], a[212:215], a[156:159], a[252:255]\n"
    "s_waitcnt vmcnt(11) lgkmcnt(11)\n"
    "ds_read_b128 a[184:187], v241 offset:29696\n"
    "v_mfma_f32_16x16x32_bf16 v[0:3], a[220:223], a[128:131], v[0:3]\n"
    "buffer_load_dwordx4 a[212:215], v240, s[20:23], s53 offen offset:1024 sc1\n"
    "v_mfma_f32_16x16x32_bf16 v[0:3], a[224:227], a[160:163], v[0:3]\n"
    "s_waitcnt lgkmcnt(10)\n"
    "v_mfma_f32_16x16x32_bf16 v[32:35], a[224:227], a[164:167], v[32:35]\n"
    "buffer_load_dwordx4 a[216:219], v240, s[20:23], s53 offen offset:2048 sc1\n"
    "v_mfma_f32_16x16x32_bf16 v[32:35], a[220:223], a[132:135], v[32:35]\n"
    "s_waitcnt lgkmcnt(8)\n"
    "ds_read_b128 a[188:191], v241 offset:31744\n"
    "ds_read_b128 a[156:159], v241 offset:30720\n"
    "v_mfma_f32_16x16x32_bf16 v[64:67], a[220:223], a[136:139], v[64:67]\n"
    "v_mfma_f32_16x16x32_bf16 v[64:67], a[224:227], a[168:171], v[64:67]\n"
    "s_waitcnt lgkmcnt(8)\n"
    "v_mfma_f32_16x16x32_bf16 v[96:99], a[224:227], a[172:175], v[96:99]\n"
    "v_mfma_f32_16x16x32_bf16 v[96:99], a[220:223], a[140:143], v[96:99]\n"
    "s_waitcnt lgkmcnt(6)\n"
    "v_mfma_f32_16x16x32_bf16 v[128:131], a[220:223], a[144:147], v[128:131]\n"
    "v_mfma_f32_16x16x32_bf16 v[128:131], a[224:227], a[176:179], v[128:131]\n"
    "s_waitcnt lgkmcnt(4)\n"
    "v_mfma_f32_16x16x32_bf16 v[160:163], a[224:227], a[180:183], v[160:163]\n"
    "v_mfma_f32_16x16x32_bf16 v[160:163], a[220:223], a[148:151], v[160:163]\n"
    "s_waitcnt lgkmcnt(2)\n"
    "v_mfma_f32_16x16x32_bf16 v[192:195], a[220:223], a[152:155], v[192:195]\n"
    "v_mfma_f32_16x16x32_bf16 v[192:195], a[224:227], a[184:187], v[192:195]\n"
    "s_waitcnt lgkmcnt(0)\n"
    "v_mfma_f32_16x16x32_bf16 v[224:227], a[224:227], a[188:191], v[224:227]\n"
    "v_mfma_f32_16x16x32_bf16 v[224:227], a[220:223], a[156:159], v[224:227]\n"
    "s_waitcnt vmcnt(10)\n"
    "v_mfma_f32_16x16x32_bf16 v[228:231], a[228:231], a[156:159], v[228:231]\n"
    "buffer_load_dwordx4 a[220:223], v240, s[20:23], s53 offen offset:3072 sc1\n"
    "v_mfma_f32_16x16x32_bf16 v[228:231], a[232:235], a[188:191], v[228:231]\n"
    "s_addk_i32 s53, 4096\n"
    "v_mfma_f32_16x16x32_bf16 v[196:199], a[232:235], a[184:187], v[196:199]\n"
    "buffer_load_dwordx4 a[224:227], v240, s[20:23], s53 offen offset:0 sc1\n"
    "v_mfma_f32_16x16x32_bf16 v[196:199], a[228:231], a[152:155], v[196:199]\n"
    "v_mfma_f32_16x16x32_bf16 v[164:167], a[228:231], a[148:151], v[164:167]\n"
    "v_mfma_f32_16x16x32_bf16 v[164:167], a[232:235], a[180:183], v[164:167]\n"
    "v_mfma_f32_16x16x32_bf16 v[132:135], a[232:235], a[176:179], v[132:135]\n"
    "v_mfma_f32_16x16x32_bf16 v[132:135], a[228:231], a[144:147], v[132:135]\n"
    "v_mfma_f32_16x16x32_bf16 v[100:103], a[228:231], a[140:143], v[100:103]\n"
    "buffer_load_dword v253, v240, s[36:39], s55 offen offset:0 sc1\n"
    "v_mfma_f32_16x16x32_bf16 v[100:103], a[232:235], a[172:175], v[100:103]\n"
    "buffer_load_dword v252, v240, s[36:39], s55 offen offset:1024 sc1\n"
    "v_mfma_f32_16x16x32_bf16 v[68:71], a[232:235], a[168:171], v[68:71]\n"
    "v_mfma_f32_16x16x32_bf16 v[68:71], a[228:231], a[136:139], v[68:71]\n"
    "v_mfma_f32_16x16x32_bf16 v[36:39], a[228:231], a[132:135], v[36:39]\n"
    "v_mfma_f32_16x16x32_bf16 v[36:39], a[232:235], a[164:167], v[36:39]\n"
    "v_mfma_f32_16x16x32_bf16 v[4:7], a[232:235], a[160:163], v[4:7]\n"
    "v_mfma_f32_16x16x32_bf16 v[4:7], a[228:231], a[128:131], v[4:7]\n"
    "s_waitcnt vmcnt(10)\n"
    "v_mfma_f32_16x16x32_bf16 v[8:11], a[192:195], a[128:131], v[8:11]\n"
    "buffer_load_dwordx4 a[228:231], v240, s[20:23], s53 offen offset:1024 sc1\n"
    "v_mfma_f32_16x16x32_bf16 v[8:11], a[196:199], a[160:163], v[8:11]\n"
    "buffer_load_dwordx4 a[232:235], v240, s[20:23], s53 offen offset:2048 sc1\n"
    "v_mfma_f32_16x16x32_bf16 v[40:43], a[196:199], a[164:167], v[40:43]\n"
    "buffer_load_dwordx4 a[236:239], v240, s[20:23], s53 offen offset:3072 sc1\n"
    "v_mfma_f32_16x16x32_bf16 v[40:43], a[192:195], a[132:135], v[40:43]\n"
    "s_addk_i32 s53, 4096\n"
    "v_mfma_f32_16x16x32_bf16 v[72:75], a[192:195], a[136:139], v[72:75]\n"
    "v_mfma_f32_16x16x32_bf16 v[72:75], a[196:199], a[168:171], v[72:75]\n"
    "v_mfma_f32_16x16x32_bf16 v[104:107], a[196:199], a[172:175], v[104:107]\n"
    "v_mfma_f32_16x16x32_bf16 v[104:107], a[192:195], a[140:143], v[104:107]\n"
    "v_mfma_f32_16x16x32_bf16 v[136:139], a[192:195], a[144:147], v[136:139]\n"
    "v_mfma_f32_16x16x32_bf16 v[136:139], a[196:199], a[176:179], v[136:139]\n"
    "v_mfma_f32_16x16x32_bf16 v[168:171], a[196:199], a[180:183], v[168:171]\n"
    "v_mfma_f32_16x16x32_bf16 v[168:171], a[192:195], a[148:151], v[168:171]\n"
    "v_mfma_f32_16x16x32_bf16 v[200:203], a[192:195], a[152:155], v[200:203]\n"
    "v_mfma_f32_16x16x32_bf16 v[200:203], a[196:199], a[184:187], v[200:203]\n"
    "v_mfma_f32_16x16x32_bf16 v[232:235], a[196:199], a[188:191], v[232:235]\n"
    "v_mfma_f32_16x16x32_bf16 v[232:235], a[192:195], a[156:159], v[232:235]\n"
    "s_waitcnt vmcnt(10)\n"
    "v_mfma_f32_16x16x32_bf16 v[236:239], a[200:203], a[156:159], v[236:239]\n"
    "buffer_load_dwordx4 a[192:195], v240, s[20:23], s53 offen offset:0 sc1\n"
    "v_mfma_f32_16x16x32_bf16 v[236:239], a[204:207], a[188:191], v[236:239]\n"
    "buffer_load_dwordx4 a[196:199], v240, s[20:23], s53 offen offset:1024 sc1\n"
    "v_mfma_f32_16x16x32_bf16 v[204:207], a[204:207], a[184:187], v[204:207]\n"
    "v_mfma_f32_16x16x32_bf16 v[204:207], a[200:203], a[152:155], v[204:207]\n"
    "v_mfma_f32_16x16x32_bf16 v[172:175], a[200:203], a[148:151], v[172:175]\n"
    "v_mfma_f32_16x16x32_bf16 v[172:175], a[204:207], a[180:183], v[172:175]\n"
    "v_mfma_f32_16x16x32_bf16 v[140:143], a[204:207], a[176:179], v[140:143]\n"
    "v_mfma_f32_16x16x32_bf16 v[140:143], a[200:203], a[144:147], v[140:143]\n"
    "v_mfma_f32_16x16x32_bf16 v[108:111], a[200:203], a[140:143], v[108:111]\n"
    "v_mfma_f32_16x16x32_bf16 v[108:111], a[204:207], a[172:175], v[108:111]\n"
    "v_mfma_f32_16x16x32_bf16 v[76:79], a[204:207], a[168:171], v[76:79]\n"
    "v_mfma_f32_16x16x32_bf16 v[76:79], a[200:203], a[136:139], v[76:79]\n"
    "v_mfma_f32_16x16x32_bf16 v[44:47], a[200:203], a[132:135], v[44:47]\n"
    "v_mfma_f32_16x16x32_bf16 v[44:47], a[204:207], a[164:167], v[44:47]\n"
    "v_mfma_f32_16x16x32_bf16 v[12:15], a[204:207], a[160:163], v[12:15]\n"
    "v_mfma_f32_16x16x32_bf16 v[12:15], a[200:203], a[128:131], v[12:15]\n"
    "s_waitcnt vmcnt(10)\n"
    "v_mfma_f32_16x16x32_bf16 v[16:19], a[208:211], a[128:131], v[16:19]\n"
    "buffer_load_dwordx4 a[200:203], v240, s[20:23], s53 offen offset:2048 sc1\n"
    "v_mfma_f32_16x16x32_bf16 v[16:19], a[212:215], a[160:163], v[16:19]\n"
    "buffer_load_dwordx4 a[204:207], v240, s[20:23], s53 offen offset:3072 sc1\n"
    "v_mfma_f32_16x16x32_bf16 v[48:51], a[212:215], a[164:167], v[48:51]\n"
    "s_addk_i32 s53, 4096\n"
    "v_mfma_f32_16x16x32_bf16 v[48:51], a[208:211], a[132:135], v[48:51]\n"
    "v_mfma_f32_16x16x32_bf16 v[80:83], a[208:211], a[136:139], v[80:83]\n"
    "v_mfma_f32_16x16x32_bf16 v[80:83], a[212:215], a[168:171], v[80:83]\n"
    "v_mfma_f32_16x16x32_bf16 v[112:115], a[212:215], a[172:175], v[112:115]\n"
    "v_mfma_f32_16x16x32_bf16 v[112:115], a[208:211], a[140:143], v[112:115]\n"
    "v_mfma_f32_16x16x32_bf16 v[144:147], a[208:211], a[144:147], v[144:147]\n"
    "v_mfma_f32_16x16x32_bf16 v[144:147], a[212:215], a[176:179], v[144:147]\n"
    "v_mfma_f32_16x16x32_bf16 v[176:179], a[212:215], a[180:183], v[176:179]\n"
    "v_mfma_f32_16x16x32_bf16 v[176:179], a[208:211], a[148:151], v[176:179]\n"
    "v_mfma_f32_16x16x32_bf16 v[208:211], a[208:211], a[152:155], v[208:211]\n"
    "v_mfma_f32_16x16x32_bf16 v[208:211], a[212:215], a[184:187], v[208:211]\n"
    "v_mfma_f32_16x16x32_bf16 a[240:243], a[212:215], a[188:191], a[240:243]\n"
    "v_mfma_f32_16x16x32_bf16 a[240:243], a[208:211], a[156:159], a[240:243]\n"
    "s_waitcnt vmcnt(10)\n"
    "v_mfma_f32_16x16x32_bf16 a[244:247], a[216:219], a[156:159], a[244:247]\n"
    "buffer_load_dwordx4 a[208:211], v240, s[20:23], s53 offen offset:0 sc1\n"
    "v_mfma_f32_16x16x32_bf16 a[244:247], a[220:223], a[188:191], a[244:247]\n"
    "buffer_load_dwordx4 a[212:215], v240, s[20:23], s53 offen offset:1024 sc1\n"
    "v_mfma_f32_16x16x32_bf16 v[212:215], a[220:223], a[184:187], v[212:215]\n"
    "v_mfma_f32_16x16x32_bf16 v[212:215], a[216:219], a[152:155], v[212:215]\n"
    "v_mfma_f32_16x16x32_bf16 v[180:183], a[216:219], a[148:151], v[180:183]\n"
    "v_mfma_f32_16x16x32_bf16 v[180:183], a[220:223], a[180:183], v[180:183]\n"
    "v_mfma_f32_16x16x32_bf16 v[148:151], a[220:223], a[176:179], v[148:151]\n"
    "v_mfma_f32_16x16x32_bf16 v[148:151], a[216:219], a[144:147], v[148:151]\n"
    "v_mfma_f32_16x16x32_bf16 v[116:119], a[216:219], a[140:143], v[116:119]\n"
    "v_mfma_f32_16x16x32_bf16 v[116:119], a[220:223], a[172:175], v[116:119]\n"
    "v_mfma_f32_16x16x32_bf16 v[84:87], a[220:223], a[168:171], v[84:87]\n"
    "v_mfma_f32_16x16x32_bf16 v[84:87], a[216:219], a[136:139], v[84:87]\n"
    "v_mfma_f32_16x16x32_bf16 v[52:55], a[216:219], a[132:135], v[52:55]\n"
    "v_mfma_f32_16x16x32_bf16 v[52:55], a[220:223], a[164:167], v[52:55]\n"
    "v_mfma_f32_16x16x32_bf16 v[20:23], a[220:223], a[160:163], v[20:23]\n"
    "v_mfma_f32_16x16x32_bf16 v[20:23], a[216:219], a[128:131], v[20:23]\n"
    "s_waitcnt vmcnt(8)\n"
    "v_mfma_f32_16x16x32_bf16 v[24:27], a[224:227], a[128:131], v[24:27]\n"
    "buffer_load_dwordx4 a[216:219], v240, s[20:23], s53 offen offset:2048 sc1\n"
    "v_mfma_f32_16x16x32_bf16 v[24:27], a[228:231], a[160:163], v[24:27]\n"
    "buffer_load_dwordx4 a[220:223], v240, s[20:23], s53 offen offset:3072 sc1\n"
    "v_mfma_f32_16x16x32_bf16 v[56:59], a[228:231], a[164:167], v[56:59]\n"
    "s_addk_i32 s53, 4096\n"
    "v_mfma_f32_16x16x32_bf16 v[56:59], a[224:227], a[132:135], v[56:59]\n"
    "v_mfma_f32_16x16x32_bf16 v[88:91], a[224:227], a[136:139], v[88:91]\n"
    "v_mfma_f32_16x16x32_bf16 v[88:91], a[228:231], a[168:171], v[88:91]\n"
    "v_mfma_f32_16x16x32_bf16 v[120:123], a[228:231], a[172:175], v[120:123]\n"
    "v_mfma_f32_16x16x32_bf16 v[120:123], a[224:227], a[140:143], v[120:123]\n"
    "v_mfma_f32_16x16x32_bf16 v[152:155], a[224:227], a[144:147], v[152:155]\n"
    "v_mfma_f32_16x16x32_bf16 v[152:155], a[228:231], a[176:179], v[152:155]\n"
    "v_mfma_f32_16x16x32_bf16 v[184:187], a[228:231], a[180:183], v[184:187]\n"
    "v_mfma_f32_16x16x32_bf16 v[184:187], a[224:227], a[148:151], v[184:187]\n"
    "v_mfma_f32_16x16x32_bf16 v[216:219], a[224:227], a[152:155], v[216:219]\n"
    "v_mfma_f32_16x16x32_bf16 v[216:219], a[228:231], a[184:187], v[216:219]\n"
    "s_barrier\n"
    "v_mfma_f32_16x16x32_bf16 a[248:251], a[228:231], a[188:191], a[248:251]\n"
    "v_mfma_f32_16x16x32_bf16 a[248:251], a[224:227], a[156:159], a[248:251]\n"
    "s_waitcnt vmcnt(8)\n"
    "v_mfma_f32_16x16x32_bf16 a[252:255], a[232:235], a[156:159], a[252:255]\n"
    "buffer_load_dwordx4 a[224:227], v240, s[20:23], s53 offen offset:0 sc1\n"
    "v_mfma_f32_16x16x32_bf16 a[252:255], a[236:239], a[188:191], a[252:255]\n"
    "buffer_load_dwordx4 a[228:231], v240, s[20:23], s53 offen offset:1024 sc1\n"
    "v_mfma_f32_16x16x32_bf16 v[220:223], a[236:239], a[184:187], v[220:223]\n"
    "v_add_u32 v241, s66, v240\n"
    "ds_read_b128 a[156:159], v241 offset:14336\n"
    "v_mfma_f32_16x16x32_bf16 v[220:223], a[232:235], a[152:155], v[220:223]\n"
    "ds_read_b128 a[188:191], v241 offset:15360\n"
    "v_mfma_f32_16x16x32_bf16 v[188:191], a[232:235], a[148:151], v[188:191]\n"
    "ds_read_b128 a[184:187], v241 offset:13312\n"
    "v_mfma_f32_16x16x32_bf16 v[188:191], a[236:239], a[180:183], v[188:191]\n"
    "ds_read_b128 a[152:155], v241 offset:12288\n"
    "v_mfma_f32_16x16x32_bf16 v[156:159], a[236:239], a[176:179], v[156:159]\n"
    "ds_read_b128 a[148:151], v241 offset:10240\n"
    "v_mfma_f32_16x16x32_bf16 v[156:159], a[232:235], a[144:147], v[156:159]\n"
    "ds_read_b128 a[180:183], v241 offset:11264\n"
    "v_mfma_f32_16x16x32_bf16 v[124:127], a[232:235], a[140:143], v[124:127]\n"
    "ds_read_b128 a[176:179], v241 offset:9216\n"
    "v_mfma_f32_16x16x32_bf16 v[124:127], a[236:239], a[172:175], v[124:127]\n"
    "ds_read_b128 a[144:147], v241 offset:8192\n"
    "v_mfma_f32_16x16x32_bf16 v[92:95], a[236:239], a[168:171], v[92:95]\n"
    "ds_read_b128 a[140:143], v241 offset:6144\n"
    "v_mfma_f32_16x16x32_bf16 v[92:95], a[232:235], a[136:139], v[92:95]\n"
    "ds_read_b128 a[172:175], v241 offset:7168\n"
    "v_mfma_f32_16x16x32_bf16 v[60:63], a[232:235], a[132:135], v[60:63]\n"
    "ds_read_b128 a[168:171], v241 offset:5120\n"
    "v_mfma_f32_16x16x32_bf16 v[60:63], a[236:239], a[164:167], v[60:63]\n"
    "ds_read_b128 a[136:139], v241 offset:4096\n"
    "v_mfma_f32_16x16x32_bf16 v[28:31], a[236:239], a[160:163], v[28:31]\n"
    "ds_read_b128 a[132:135], v241 offset:2048\n"
    "v_mfma_f32_16x16x32_bf16 v[28:31], a[232:235], a[128:131], v[28:31]\n"
    "ds_read_b128 a[164:167], v241 offset:3072\n"
    "ds_read_b128 a[160:163], v241 offset:1024\n"
    "ds_read_b128 a[128:131], v241 offset:0\n"
    "buffer_load_dwordx4 a[232:235], v240, s[20:23], s53 offen offset:2048 sc1\n"
    "buffer_load_dwordx4 a[236:239], v240, s[20:23], s53 offen offset:3072 sc1\n"
    "s_addk_i32 s53, 4096\n"
    "s_bitcmp1_b32 0x404, s52\n"
    "s_cbranch_scc1 OD_%=\n"
    "OE_%=:\n"
    "s_waitcnt vmcnt(10) lgkmcnt(14)\n"
    "v_mfma_f32_16x16x32_bf16 v[224:227], a[192:195], a[156:159], v[224:227]\n"
    "v_mfma_f32_16x16x32_bf16 v[224:227], a[196:199], a[188:191], v[224:227]\n"
    "s_waitcnt lgkmcnt(12)\n"
    "v_mfma_f32_16x16x32_bf16 v[192:195], a[196:199], a[184:187], v[192:195]\n"
    "v_mfma_f32_16x16x32_bf16 v[192:195], a[192:195], a[152:155], v[192:195]\n"
    "s_waitcnt lgkmcnt(10)\n"
    "v_mfma_f32_16x16x32_bf16 v[160:163], a[192:195], a[148:151], v[160:163]\n"
    "v_mfma_f32_16x16x32_bf16 v[160:163], a[196:199], a[180:183], v[160:163]\n"
    "s_waitcnt lgkmcnt(8)\n"
    "v_mfma_f32_16x16x32_bf16 v[128:131], a[196:199], a[176:179], v[128:131]\n"
    "s_mov_b32 m0, s56\n"
    "s_xor_b32 s90, s90, 0x195800\n"
    "v_mfma_f32_16x16x32_bf16 v[128:131], a[192:195], a[144:147], v[128:131]\n"
    "s_waitcnt lgkmcnt(6)\n"
    "v_mfma_f32_16x16x32_bf16 v[96:99], a[192:195], a[140:143], v[96:99]\n"
    "buffer_load_dwordx4 v240, s[32:35], s90 offen sc1 lds\n"
    "v_mfma_f32_16x16x32_bf16 v[96:99], a[196:199], a[172:175], v[96:99]\n"
    "s_waitcnt lgkmcnt(4)\n"
    "v_mfma_f32_16x16x32_bf16 v[64:67], a[196:199], a[168:171], v[64:67]\n"
    "v_mfma_f32_16x16x32_bf16 v[64:67], a[192:195], a[136:139], v[64:67]\n"
    "s_waitcnt lgkmcnt(2)\n"
    "v_mfma_f32_16x16x32_bf16 v[32:35], a[192:195], a[132:135], v[32:35]\n"
    "v_mfma_f32_16x16x32_bf16 v[32:35], a[196:199], a[164:167], v[32:35]\n"
    "s_waitcnt lgkmcnt(0)\n"
    "v_mfma_f32_16x16x32_bf16 v[0:3], a[196:199], a[160:163], v[0:3]\n"
    "v_mfma_f32_16x16x32_bf16 v[0:3], a[192:195], a[128:131], v[0:3]\n"
    "s_waitcnt vmcnt(9)\n"
    "v_mfma_f32_16x16x32_bf16 v[4:7], a[200:203], a[128:131], v[4:7]\n"
    "buffer_load_dwordx4 a[192:195], v240, s[20:23], s53 offen offset:0 sc1\n"
    "v_mfma_f32_16x16x32_bf16 v[4:7], a[204:207], a[160:163], v[4:7]\n"
    "buffer_load_dwordx4 a[196:199], v240, s[20:23], s53 offen offset:1024 sc1\n"
    "v_mfma_f32_16x16x32_bf16 v[36:39], a[204:207], a[164:167], v[36:39]\n"
    "v_mfma_f32_16x16x32_bf16 v[36:39], a[200:203], a[132:135], v[36:39]\n"
    "v_mfma_f32_16x16x32_bf16 v[68:71], a[200:203], a[136:139], v[68:71]\n"
    "v_mfma_f32_16x16x32_bf16 v[68:71], a[204:207], a[168:171], v[68:71]\n"
    "v_mfma_f32_16x16x32_bf16 v[100:103], a[204:207], a[172:175], v[100:103]\n"
    "v_mfma_f32_16x16x32_bf16 v[100:103], a[200:203], a[140:143], v[100:103]\n"
    "v_mfma_f32_16x16x32_bf16 v[132:135], a[200:203], a[144:147], v[132:135]\n"
    "v_mfma_f32_16x16x32_bf16 v[132:135], a[204:207], a[176:179], v[132:135]\n"
    "v_mfma_f32_16x16x32_bf16 v[164:167], a[204:207], a[180:183], v[164:167]\n"
    "v_mfma_f32_16x16x32_bf16 v[164:167], a[200:203], a[148:151], v[164:167]\n"
    "v_mfma_f32_16x16x32_bf16 v[196:199], a[200:203], a[152:155], v[196:199]\n"
    "v_mfma_f32_16x16x32_bf16 v[196:199], a[204:207], a[184:187], v[196:199]\n"
    "v_mfma_f32_16x16x32_bf16 v[228:231], a[204:207], a[188:191], v[228:231]\n"
    "s_addk_i32 m0, 2048\n"
    "s_xor_b32 s90, s90, 0x800\n"
    "v_mfma_f32_16x16x32_bf16 v[228:231], a[200:203], a[156:159], v[228:231]\n"
    "s_waitcnt vmcnt(9)\n"
    "v_mfma_f32_16x16x32_bf16 v[232:235], a[208:211], a[156:159], v[232:235]\n"
    "buffer_load_dwordx4 v240, s[32:35], s90 offen sc1 lds\n"
    "v_mfma_f32_16x16x32_bf16 v[232:235], a[212:215], a[188:191], v[232:235]\n"
    "buffer_load_dwordx4 a[200:203], v240, s[20:23], s53 offen offset:2048 sc1\n"
    "v_mfma_f32_16x16x32_bf16 v[200:203], a[212:215], a[184:187], v[200:203]\n"
    "buffer_load_dwordx4 a[204:207], v240, s[20:23], s53 offen offset:3072 sc1\n"
    "v_mfma_f32_16x16x32_bf16 v[200:203], a[208:211], a[152:155], v[200:203]\n"
    "s_addk_i32 s53, 4096\n"
    "v_mfma_f32_16x16x32_bf16 v[168:171], a[208:211], a[148:151], v[168:171]\n"
    "v_mfma_f32_16x16x32_bf16 v[168:171], a[212:215], a[180:183], v[168:171]\n"
    "v_mfma_f32_16x16x32_bf16 v[136:139], a[212:215], a[176:179], v[136:139]\n"
    "v_mfma_f32_16x16x32_bf16 v[136:139], a[208:211], a[144:147], v[136:139]\n"
    "v_mfma_f32_16x16x32_bf16 v[104:107], a[208:211], a[140:143], v[104:107]\n"
    "v_mfma_f32_16x16x32_bf16 v[104:107], a[212:215], a[172:175], v[104:107]\n"
    "v_mfma_f32_16x16x32_bf16 v[72:75], a[212:215], a[168:171], v[72:75]\n"
    "v_mfma_f32_16x16x32_bf16 v[72:75], a[208:211], a[136:139], v[72:75]\n"
    "v_mfma_f32_16x16x32_bf16 v[40:43], a[208:211], a[132:135], v[40:43]\n"
    "v_mfma_f32_16x16x32_bf16 v[40:43], a[212:215], a[164:167], v[40:43]\n"
    "v_mfma_f32_16x16x32_bf16 v[8:11], a[212:215], a[160:163], v[8:11]\n"
    "v_mfma_f32_16x16x32_bf16 v[8:11], a[208:211], a[128:131], v[8:11]\n"
    "s_waitcnt vmcnt(10)\n"
    "v_mfma_f32_16x16x32_bf16 v[12:15], a[216:219], a[128:131], v[12:15]\n"
    "buffer_load_dwordx4 a[212:215], v240, s[20:23], s53 offen offset:0 sc1\n"
    "v_mfma_f32_16x16x32_bf16 v[12:15], a[220:223], a[160:163], v[12:15]\n"
    "v_mfma_f32_16x16x32_bf16 v[44:47], a[220:223], a[164:167], v[44:47]\n"
    "v_mfma_f32_16x16x32_bf16 v[44:47], a[216:219], a[132:135], v[44:47]\n"
    "v_mfma_f32_16x16x32_bf16 v[76:79], a[216:219], a[136:139], v[76:79]\n"
    "v_mfma_f32_16x16x32_bf16 v[76:79], a[220:223], a[168:171], v[76:79]\n"
    "v_mfma_f32_16x16x32_bf16 v[108:111], a[220:223], a[172:175], v[108:111]\n"
    "s_addk_i32 m0, 2048\n"
    "s_xor_b32 s90, s90, 0x101800\n"
    "v_mfma_f32_16x16x32_bf16 v[108:111], a[216:219], a[140:143], v[108:111]\n"
    "buffer_load_dwordx4 v240, s[32:35], s90 offen sc1 lds\n"
    "v_mfma_f32_16x16x32_bf16 v[140:143], a[216:219], a[144:147], v[140:143]\n"
    "v_mfma_f32_16x16x32_bf16 v[140:143], a[220:223], a[176:179], v[140:143]\n"
    "v_mfma_f32_16x16x32_bf16 v[172:175], a[220:223], a[180:183], v[172:175]\n"
    "v_mfma_f32_16x16x32_bf16 v[172:175], a[216:219], a[148:151], v[172:175]\n"
    "v_mfma_f32_16x16x32_bf16 v[204:207], a[216:219], a[152:155], v[204:207]\n"
    "v_mfma_f32_16x16x32_bf16 v[204:207], a[220:223], a[184:187], v[204:207]\n"
    "v_mfma_f32_16x16x32_bf16 v[236:239], a[220:223], a[188:191], v[236:239]\n"
    "v_mfma_f32_16x16x32_bf16 v[236:239], a[216:219], a[156:159], v[236:239]\n"
    "s_waitcnt vmcnt(10)\n"
    "v_mfma_f32_16x16x32_bf16 a[240:243], a[224:227], a[156:159], a[240:243]\n"
    "buffer_load_dwordx4 a[216:219], v240, s[20:23], s53 offen offset:1024 sc1\n"
    "v_mfma_f32_16x16x32_bf16 a[240:243], a[228:231], a[188:191], a[240:243]\n"
    "buffer_load_dwordx4 a[220:223], v240, s[20:23], s53 offen offset:2048 sc1\n"
    "v_mfma_f32_16x16x32_bf16 v[208:211], a[228:231], a[184:187], v[208:211]\n"
    "v_mfma_f32_16x16x32_bf16 v[208:211], a[224:227], a[152:155], v[208:211]\n"
    "v_mfma_f32_16x16x32_bf16 v[176:179], a[224:227], a[148:151], v[176:179]\n"
    "v_mfma_f32_16x16x32_bf16 v[176:179], a[228:231], a[180:183], v[176:179]\n"
    "v_mfma_f32_16x16x32_bf16 v[144:147], a[228:231], a[176:179], v[144:147]\n"
    "v_mfma_f32_16x16x32_bf16 v[144:147], a[224:227], a[144:147], v[144:147]\n"
    "v_mfma_f32_16x16x32_bf16 v[112:115], a[224:227], a[140:143], v[112:115]\n"
    "v_mfma_f32_16x16x32_bf16 v[112:115], a[228:231], a[172:175], v[112:115]\n"
    "v_mfma_f32_16x16x32_bf16 v[80:83], a[228:231], a[168:171], v[80:83]\n"
    "v_mfma_f32_16x16x32_bf16 v[80:83], a[224:227], a[136:139], v[80:83]\n"
    "v_mfma_f32_16x16x32_bf16 v[48:51], a[224:227], a[132:135], v[48:51]\n"
    "v_mfma_f32_16x16x32_bf16 v[48:51], a[228:231], a[164:167], v[48:51]\n"
    "v_mfma_f32_16x16x32_bf16 v[16:19], a[228:231], a[160:163], v[16:19]\n"
    "s_addk_i32 m0, 2048\n"
    "s_xor_b32 s90, s90, 0x800\n"
    "v_mfma_f32_16x16x32_bf16 v[16:19], a[224:227], a[128:131], v[16:19]\n"
    "s_waitcnt vmcnt(10)\n"
    "v_mfma_f32_16x16x32_bf16 v[20:23], a[232:235], a[128:131], v[20:23]\n"
    "buffer_load_dwordx4 v240, s[32:35], s90 offen sc1 lds\n"
    "v_mfma_f32_16x16x32_bf16 v[20:23], a[236:239], a[160:163], v[20:23]\n"
    "buffer_load_dwordx4 a[224:227], v240, s[20:23], s53 offen offset:3072 sc1\n"
    "v_mfma_f32_16x16x32_bf16 v[52:55], a[236:239], a[164:167], v[52:55]\n"
    "s_addk_i32 s53, 4096\n"
    "v_mfma_f32_16x16x32_bf16 v[52:55], a[232:235], a[132:135], v[52:55]\n"
    "buffer_load_dwordx4 a[228:231], v240, s[20:23], s53 offen offset:0 sc1\n"
    "v_mfma_f32_16x16x32_bf16 v[84:87], a[232:235], a[136:139], v[84:87]\n"
    "v_mfma_f32_16x16x32_bf16 v[84:87], a[236:239], a[168:171], v[84:87]\n"
    "v_mfma_f32_16x16x32_bf16 v[116:119], a[236:239], a[172:175], v[116:119]\n"
    "v_mfma_f32_16x16x32_bf16 v[116:119], a[232:235], a[140:143], v[116:119]\n"
    "v_mfma_f32_16x16x32_bf16 v[148:151], a[232:235], a[144:147], v[148:151]\n"
    "v_mfma_f32_16x16x32_bf16 v[148:151], a[236:239], a[176:179], v[148:151]\n"
    "v_mfma_f32_16x16x32_bf16 v[180:183], a[236:239], a[180:183], v[180:183]\n"
    "v_mfma_f32_16x16x32_bf16 v[180:183], a[232:235], a[148:151], v[180:183]\n"
    "v_mfma_f32_16x16x32_bf16 v[212:215], a[232:235], a[152:155], v[212:215]\n"
    "v_mfma_f32_16x16x32_bf16 v[212:215], a[236:239], a[184:187], v[212:215]\n"
    "v_mfma_f32_16x16x32_bf16 a[244:247], a[236:239], a[188:191], a[244:247]\n"
    "v_mfma_f32_16x16x32_bf16 a[244:247], a[232:235], a[156:159], a[244:247]\n"
    "s_waitcnt vmcnt(10)\n"
    "v_mfma_f32_16x16x32_bf16 a[248:251], a[192:195], a[156:159], a[248:251]\n"
    "buffer_load_dwordx4 a[232:235], v240, s[20:23], s53 offen offset:1024 sc1\n"
    "v_mfma_f32_16x16x32_bf16 a[248:251], a[196:199], a[188:191], a[248:251]\n"
    "buffer_load_dwordx4 a[236:239], v240, s[20:23], s53 offen offset:2048 sc1\n"
    "v_mfma_f32_16x16x32_bf16 v[216:219], a[196:199], a[184:187], v[216:219]\n"
    "v_mfma_f32_16x16x32_bf16 v[216:219], a[192:195], a[152:155], v[216:219]\n"
    "v_mfma_f32_16x16x32_bf16 v[184:187], a[192:195], a[148:151], v[184:187]\n"
    "v_mfma_f32_16x16x32_bf16 v[184:187], a[196:199], a[180:183], v[184:187]\n"
    "v_mfma_f32_16x16x32_bf16 v[152:155], a[196:199], a[176:179], v[152:155]\n"
    "v_mfma_f32_16x16x32_bf16 v[152:155], a[192:195], a[144:147], v[152:155]\n"
    "v_mfma_f32_16x16x32_bf16 v[120:123], a[192:195], a[140:143], v[120:123]\n"
    "v_mfma_f32_16x16x32_bf16 v[120:123], a[196:199], a[172:175], v[120:123]\n"
    "v_mfma_f32_16x16x32_bf16 v[88:91], a[196:199], a[168:171], v[88:91]\n"
    "s_addk_i32 m0, 2048\n"
    "s_xor_b32 s90, s90, 0x185800\n"
    "v_mfma_f32_16x16x32_bf16 v[88:91], a[192:195], a[136:139], v[88:91]\n"
    "buffer_load_dwordx4 v240, s[32:35], s90 offen sc1 lds\n"
    "v_mfma_f32_16x16x32_bf16 v[56:59], a[192:195], a[132:135], v[56:59]\n"
    "v_mfma_f32_16x16x32_bf16 v[56:59], a[196:199], a[164:167], v[56:59]\n"
    "v_mfma_f32_16x16x32_bf16 v[24:27], a[196:199], a[160:163], v[24:27]\n"
    "v_mfma_f32_16x16x32_bf16 v[24:27], a[192:195], a[128:131], v[24:27]\n"
    "s_waitcnt vmcnt(10)\n"
    "v_mfma_f32_16x16x32_bf16 v[28:31], a[200:203], a[128:131], v[28:31]\n"
    "buffer_load_dwordx4 a[192:195], v240, s[20:23], s53 offen offset:3072 sc1\n"
    "v_mfma_f32_16x16x32_bf16 v[28:31], a[204:207], a[160:163], v[28:31]\n"
    "s_addk_i32 s53, 4096\n"
    "v_mfma_f32_16x16x32_bf16 v[60:63], a[204:207], a[164:167], v[60:63]\n"
    "buffer_load_dwordx4 a[196:199], v240, s[20:23], s53 offen offset:0 sc1\n"
    "v_mfma_f32_16x16x32_bf16 v[60:63], a[200:203], a[132:135], v[60:63]\n"
    "ds_read_b128 a[128:131], v241 offset:16384\n"
    "ds_read_b128 a[160:163], v241 offset:17408\n"
    "v_mfma_f32_16x16x32_bf16 v[92:95], a[200:203], a[136:139], v[92:95]\n"
    "ds_read_b128 a[164:167], v241 offset:19456\n"
    "v_mfma_f32_16x16x32_bf16 v[92:95], a[204:207], a[168:171], v[92:95]\n"
    "ds_read_b128 a[132:135], v241 offset:18432\n"
    "v_mfma_f32_16x16x32_bf16 v[124:127], a[204:207], a[172:175], v[124:127]\n"
    "ds_read_b128 a[136:139], v241 offset:20480\n"
    "v_mfma_f32_16x16x32_bf16 v[124:127], a[200:203], a[140:143], v[124:127]\n"
    "ds_read_b128 a[168:171], v241 offset:21504\n"
    "v_mfma_f32_16x16x32_bf16 v[156:159], a[200:203], a[144:147], v[156:159]\n"
    "ds_read_b128 a[172:175], v241 offset:23552\n"
    "v_mfma_f32_16x16x32_bf16 v[156:159], a[204:207], a[176:179], v[156:159]\n"
    "ds_read_b128 a[140:143], v241 offset:22528\n"
    "v_mfma_f32_16x16x32_bf16 v[188:191], a[204:207], a[180:183], v[188:191]\n"
    "ds_read_b128 a[144:147], v241 offset:24576\n"
    "v_mfma_f32_16x16x32_bf16 v[188:191], a[200:203], a[148:151], v[188:191]\n"
    "ds_read_b128 a[176:179], v241 offset:25600\n"
    "v_mfma_f32_16x16x32_bf16 v[220:223], a[200:203], a[152:155], v[220:223]\n"
    "ds_read_b128 a[180:183], v241 offset:27648\n"
    "v_mfma_f32_16x16x32_bf16 v[220:223], a[204:207], a[184:187], v[220:223]\n"
    "ds_read_b128 a[148:151], v241 offset:26624\n"
    "v_mfma_f32_16x16x32_bf16 a[252:255], a[204:207], a[188:191], a[252:255]\n"
    "ds_read_b128 a[152:155], v241 offset:28672\n"
    "v_mfma_f32_16x16x32_bf16 a[252:255], a[200:203], a[156:159], a[252:255]\n"
    "s_waitcnt vmcnt(9) lgkmcnt(11)\n"
    "ds_read_b128 a[184:187], v241 offset:29696\n"
    "v_mfma_f32_16x16x32_bf16 v[0:3], a[212:215], a[128:131], v[0:3]\n"
    "buffer_load_dwordx4 a[200:203], v240, s[20:23], s53 offen offset:1024 sc1\n"
    "v_mfma_f32_16x16x32_bf16 v[0:3], a[216:219], a[160:163], v[0:3]\n"
    "s_waitcnt lgkmcnt(10)\n"
    "v_mfma_f32_16x16x32_bf16 v[32:35], a[216:219], a[164:167], v[32:35]\n"
    "buffer_load_dwordx4 a[204:207], v240, s[20:23], s53 offen offset:2048 sc1\n"
    "v_mfma_f32_16x16x32_bf16 v[32:35], a[212:215], a[132:135], v[32:35]\n"
    "s_waitcnt lgkmcnt(8)\n"
    "v_mfma_f32_16x16x32_bf16 v[64:67], a[212:215], a[136:139], v[64:67]\n"
    "buffer_load_dwordx4 a[208:211], v240, s[20:23], s53 offen offset:3072 sc1\n"
    "s_addk_i32 s53, 4096\n"
    "ds_read_b128 a[188:191], v241 offset:31744\n"
    "ds_read_b128 a[156:159], v241 offset:30720\n"
    "v_mfma_f32_16x16x32_bf16 v[64:67], a[216:219], a[168:171], v[64:67]\n"
    "s_waitcnt lgkmcnt(8)\n"
    "s_addk_i32 m0, 2048\n"
    "s_xor_b32 s90, s90, 0x800\n"
    "v_mfma_f32_16x16x32_bf16 v[96:99], a[216:219], a[172:175], v[96:99]\n"
    "buffer_load_dwordx4 v240, s[32:35], s90 offen sc1 lds\n"
    "v_mfma_f32_16x16x32_bf16 v[96:99], a[212:215], a[140:143], v[96:99]\n"
    "s_waitcnt lgkmcnt(6)\n"
    "v_mfma_f32_16x16x32_bf16 v[128:131], a[212:215], a[144:147], v[128:131]\n"
    "v_mfma_f32_16x16x32_bf16 v[128:131], a[216:219], a[176:179], v[128:131]\n"
    "s_waitcnt lgkmcnt(4)\n"
    "v_mfma_f32_16x16x32_bf16 v[160:163], a[216:219], a[180:183], v[160:163]\n"
    "v_mfma_f32_16x16x32_bf16 v[160:163], a[212:215], a[148:151], v[160:163]\n"
    "s_waitcnt lgkmcnt(2)\n"
    "v_mfma_f32_16x16x32_bf16 v[192:195], a[212:215], a[152:155], v[192:195]\n"
    "v_mfma_f32_16x16x32_bf16 v[192:195], a[216:219], a[184:187], v[192:195]\n"
    "s_waitcnt lgkmcnt(0)\n"
    "v_mfma_f32_16x16x32_bf16 v[224:227], a[216:219], a[188:191], v[224:227]\n"
    "v_mfma_f32_16x16x32_bf16 v[224:227], a[212:215], a[156:159], v[224:227]\n"
    "s_waitcnt vmcnt(10)\n"
    "v_mfma_f32_16x16x32_bf16 v[228:231], a[220:223], a[156:159], v[228:231]\n"
    "buffer_load_dwordx4 a[212:215], v240, s[20:23], s53 offen offset:0 sc1\n"
    "v_mfma_f32_16x16x32_bf16 v[228:231], a[224:227], a[188:191], v[228:231]\n"
    "buffer_load_dwordx4 a[216:219], v240, s[20:23], s53 offen offset:1024 sc1\n"
    "v_mfma_f32_16x16x32_bf16 v[196:199], a[224:227], a[184:187], v[196:199]\n"
    "v_mfma_f32_16x16x32_bf16 v[196:199], a[220:223], a[152:155], v[196:199]\n"
    "v_mfma_f32_16x16x32_bf16 v[164:167], a[220:223], a[148:151], v[164:167]\n"
    "v_mfma_f32_16x16x32_bf16 v[164:167], a[224:227], a[180:183], v[164:167]\n"
    "v_mfma_f32_16x16x32_bf16 v[132:135], a[224:227], a[176:179], v[132:135]\n"
    "v_mfma_f32_16x16x32_bf16 v[132:135], a[220:223], a[144:147], v[132:135]\n"
    "v_mfma_f32_16x16x32_bf16 v[100:103], a[220:223], a[140:143], v[100:103]\n"
    "v_mfma_f32_16x16x32_bf16 v[100:103], a[224:227], a[172:175], v[100:103]\n"
    "v_mfma_f32_16x16x32_bf16 v[68:71], a[224:227], a[168:171], v[68:71]\n"
    "s_addk_i32 m0, 2048\n"
    "s_xor_b32 s90, s90, 0x101800\n"
    "v_mfma_f32_16x16x32_bf16 v[68:71], a[220:223], a[136:139], v[68:71]\n"
    "buffer_load_dwordx4 v240, s[32:35], s90 offen sc1 lds\n"
    "v_mfma_f32_16x16x32_bf16 v[36:39], a[220:223], a[132:135], v[36:39]\n"
    "v_mfma_f32_16x16x32_bf16 v[36:39], a[224:227], a[164:167], v[36:39]\n"
    "v_mfma_f32_16x16x32_bf16 v[4:7], a[224:227], a[160:163], v[4:7]\n"
    "v_mfma_f32_16x16x32_bf16 v[4:7], a[220:223], a[128:131], v[4:7]\n"
    "s_waitcnt vmcnt(11)\n"
    "v_mfma_f32_16x16x32_bf16 v[8:11], a[228:231], a[128:131], v[8:11]\n"
    "buffer_load_dwordx4 a[220:223], v240, s[20:23], s53 offen offset:2048 sc1\n"
    "v_mfma_f32_16x16x32_bf16 v[8:11], a[232:235], a[160:163], v[8:11]\n"
    "buffer_load_dwordx4 a[224:227], v240, s[20:23], s53 offen offset:3072 sc1\n"
    "v_mfma_f32_16x16x32_bf16 v[40:43], a[232:235], a[164:167], v[40:43]\n"
    "s_addk_i32 s53, 4096\n"
    "v_mfma_f32_16x16x32_bf16 v[40:43], a[228:231], a[132:135], v[40:43]\n"
    "v_mfma_f32_16x16x32_bf16 v[72:75], a[228:231], a[136:139], v[72:75]\n"
    "v_mfma_f32_16x16x32_bf16 v[72:75], a[232:235], a[168:171], v[72:75]\n"
    "v_mfma_f32_16x16x32_bf16 v[104:107], a[232:235], a[172:175], v[104:107]\n"
    "v_mfma_f32_16x16x32_bf16 v[104:107], a[228:231], a[140:143], v[104:107]\n"
    "v_mfma_f32_16x16x32_bf16 v[136:139], a[228:231], a[144:147], v[136:139]\n"
    "v_mfma_f32_16x16x32_bf16 v[136:139], a[232:235], a[176:179], v[136:139]\n"
    "v_mfma_f32_16x16x32_bf16 v[168:171], a[232:235], a[180:183], v[168:171]\n"
    "v_mfma_f32_16x16x32_bf16 v[168:171], a[228:231], a[148:151], v[168:171]\n"
    "v_mfma_f32_16x16x32_bf16 v[200:203], a[228:231], a[152:155], v[200:203]\n"
    "v_mfma_f32_16x16x32_bf16 v[200:203], a[232:235], a[184:187], v[200:203]\n"
    "v_mfma_f32_16x16x32_bf16 v[232:235], a[232:235], a[188:191], v[232:235]\n"
    "v_mfma_f32_16x16x32_bf16 v[232:235], a[228:231], a[156:159], v[232:235]\n"
    "s_waitcnt vmcnt(10)\n"
    "v_mfma_f32_16x16x32_bf16 v[236:239], a[236:239], a[156:159], v[236:239]\n"
    "buffer_load_dwordx4 a[232:235], v240, s[20:23], s53 offen offset:0 sc1\n"
    "v_mfma_f32_16x16x32_bf16 v[236:239], a[192:195], a[188:191], v[236:239]\n"
    "v_mfma_f32_16x16x32_bf16 v[204:207], a[192:195], a[184:187], v[204:207]\n"
    "s_addk_i32 m0, 2048\n"
    "s_xor_b32 s90, s90, 0x800\n"
    "v_mfma_f32_16x16x32_bf16 v[204:207], a[236:239], a[152:155], v[204:207]\n"
    "buffer_load_dwordx4 v240, s[32:35], s90 offen sc1 lds\n"
    "v_mfma_f32_16x16x32_bf16 v[172:175], a[236:239], a[148:151], v[172:175]\n"
    "v_mfma_f32_16x16x32_bf16 v[172:175], a[192:195], a[180:183], v[172:175]\n"
    "v_mfma_f32_16x16x32_bf16 v[140:143], a[192:195], a[176:179], v[140:143]\n"
    "v_mfma_f32_16x16x32_bf16 v[140:143], a[236:239], a[144:147], v[140:143]\n"
    "v_mfma_f32_16x16x32_bf16 v[108:111], a[236:239], a[140:143], v[108:111]\n"
    "v_mfma_f32_16x16x32_bf16 v[108:111], a[192:195], a[172:175], v[108:111]\n"
    "v_mfma_f32_16x16x32_bf16 v[76:79], a[192:195], a[168:171], v[76:79]\n"
    "v_mfma_f32_16x16x32_bf16 v[76:79], a[236:239], a[136:139], v[76:79]\n"
    "v_mfma_f32_16x16x32_bf16 v[44:47], a[236:239], a[132:135], v[44:47]\n"
    "v_mfma_f32_16x16x32_bf16 v[44:47], a[192:195], a[164:167], v[44:47]\n"
    "v_mfma_f32_16x16x32_bf16 v[12:15], a[192:195], a[160:163], v[12:15]\n"
    "v_mfma_f32_16x16x32_bf16 v[12:15], a[236:239], a[128:131], v[12:15]\n"
    "s_waitcnt vmcnt(10)\n"
    "v_mfma_f32_16x16x32_bf16 v[16:19], a[196:199], a[128:131], v[16:19]\n"
    "buffer_load_dwordx4 a[236:239], v240, s[20:23], s53 offen offset:1024 sc1\n"
    "v_mfma_f32_16x16x32_bf16 v[16:19], a[200:203], a[160:163], v[16:19]\n"
    "buffer_load_dwordx4 a[192:195], v240, s[20:23], s53 offen offset:2048 sc1\n"
    "v_mfma_f32_16x16x32_bf16 v[48:51], a[200:203], a[164:167], v[48:51]\n"
    "v_mfma_f32_16x16x32_bf16 v[48:51], a[196:199], a[132:135], v[48:51]\n"
    "v_mfma_f32_16x16x32_bf16 v[80:83], a[196:199], a[136:139], v[80:83]\n"
    "v_mfma_f32_16x16x32_bf16 v[80:83], a[200:203], a[168:171], v[80:83]\n"
    "v_mfma_f32_16x16x32_bf16 v[112:115], a[200:203], a[172:175], v[112:115]\n"
    "v_mfma_f32_16x16x32_bf16 v[112:115], a[196:199], a[140:143], v[112:115]\n"
    "v_mfma_f32_16x16x32_bf16 v[144:147], a[196:199], a[144:147], v[144:147]\n"
    "v_mfma_f32_16x16x32_bf16 v[144:147], a[200:203], a[176:179], v[144:147]\n"
    "v_mfma_f32_16x16x32_bf16 v[176:179], a[200:203], a[180:183], v[176:179]\n"
    "v_mfma_f32_16x16x32_bf16 v[176:179], a[196:199], a[148:151], v[176:179]\n"
    "v_mfma_f32_16x16x32_bf16 v[208:211], a[196:199], a[152:155], v[208:211]\n"
    "v_mfma_f32_16x16x32_bf16 v[208:211], a[200:203], a[184:187], v[208:211]\n"
    "v_mfma_f32_16x16x32_bf16 a[240:243], a[200:203], a[188:191], a[240:243]\n"
    "v_mfma_f32_16x16x32_bf16 a[240:243], a[196:199], a[156:159], a[240:243]\n"
    "s_waitcnt vmcnt(10)\n"
    "v_mfma_f32_16x16x32_bf16 a[244:247], a[204:207], a[156:159], a[244:247]\n"
    "buffer_load_dwordx4 a[196:199], v240, s[20:23], s53 offen offset:3072 sc1\n"
    "v_mfma_f32_16x16x32_bf16 a[244:247], a[208:211], a[188:191], a[244:247]\n"
    "s_addk_i32 s53, 4096\n"
    "v_mfma_f32_16x16x32_bf16 v[212:215], a[208:211], a[184:187], v[212:215]\n"
    "buffer_load_dwordx4 a[200:203], v240, s[20:23], s53 offen offset:0 sc1\n"
    "v_mfma_f32_16x16x32_bf16 v[212:215], a[204:207], a[152:155], v[212:215]\n"
    "v_mfma_f32_16x16x32_bf16 v[180:183], a[204:207], a[148:151], v[180:183]\n"
    "v_mfma_f32_16x16x32_bf16 v[180:183], a[208:211], a[180:183], v[180:183]\n"
    "v_mfma_f32_16x16x32_bf16 v[148:151], a[208:211], a[176:179], v[148:151]\n"
    "v_mfma_f32_16x16x32_bf16 v[148:151], a[204:207], a[144:147], v[148:151]\n"
    "v_mfma_f32_16x16x32_bf16 v[116:119], a[204:207], a[140:143], v[116:119]\n"
    "v_mfma_f32_16x16x32_bf16 v[116:119], a[208:211], a[172:175], v[116:119]\n"
    "v_mfma_f32_16x16x32_bf16 v[84:87], a[208:211], a[168:171], v[84:87]\n"
    "v_mfma_f32_16x16x32_bf16 v[84:87], a[204:207], a[136:139], v[84:87]\n"
    "v_mfma_f32_16x16x32_bf16 v[52:55], a[204:207], a[132:135], v[52:55]\n"
    "v_mfma_f32_16x16x32_bf16 v[52:55], a[208:211], a[164:167], v[52:55]\n"
    "v_mfma_f32_16x16x32_bf16 v[20:23], a[208:211], a[160:163], v[20:23]\n"
    "v_mfma_f32_16x16x32_bf16 v[20:23], a[204:207], a[128:131], v[20:23]\n"
    "s_waitcnt vmcnt(9)\n"
    "v_mfma_f32_16x16x32_bf16 v[24:27], a[212:215], a[128:131], v[24:27]\n"
    "buffer_load_dwordx4 a[204:207], v240, s[20:23], s53 offen offset:1024 sc1\n"
    "v_mfma_f32_16x16x32_bf16 v[24:27], a[216:219], a[160:163], v[24:27]\n"
    "buffer_load_dwordx4 a[208:211], v240, s[20:23], s53 offen offset:2048 sc1\n"
    "v_mfma_f32_16x16x32_bf16 v[56:59], a[216:219], a[164:167], v[56:59]\n"
    "v_mfma_f32_16x16x32_bf16 v[56:59], a[212:215], a[132:135], v[56:59]\n"
    "v_mfma_f32_16x16x32_bf16 v[88:91], a[212:215], a[136:139], v[88:91]\n"
    "v_mfma_f32_16x16x32_bf16 v[88:91], a[216:219], a[168:171], v[88:91]\n"
    "v_mfma_f32_16x16x32_bf16 v[120:123], a[216:219], a[172:175], v[120:123]\n"
    "v_mfma_f32_16x16x32_bf16 v[120:123], a[212:215], a[140:143], v[120:123]\n"
    "v_mfma_f32_16x16x32_bf16 v[152:155], a[212:215], a[144:147], v[152:155]\n"
    "v_mfma_f32_16x16x32_bf16 v[152:155], a[216:219], a[176:179], v[152:155]\n"
    "v_mfma_f32_16x16x32_bf16 v[184:187], a[216:219], a[180:183], v[184:187]\n"
    "v_mfma_f32_16x16x32_bf16 v[184:187], a[212:215], a[148:151], v[184:187]\n"
    "v_mfma_f32_16x16x32_bf16 v[216:219], a[212:215], a[152:155], v[216:219]\n"
    "v_mfma_f32_16x16x32_bf16 v[216:219], a[216:219], a[184:187], v[216:219]\n"
    "v_mfma_f32_16x16x32_bf16 a[248:251], a[216:219], a[188:191], a[248:251]\n"
    "v_mfma_f32_16x16x32_bf16 a[248:251], a[212:215], a[156:159], a[248:251]\n"
    "s_waitcnt vmcnt(8)\n"
    "v_mfma_f32_16x16x32_bf16 a[252:255], a[220:223], a[156:159], a[252:255]\n"
    "buffer_load_dwordx4 a[212:215], v240, s[20:23], s53 offen offset:3072 sc1\n"
    "v_mfma_f32_16x16x32_bf16 a[252:255], a[224:227], a[188:191], a[252:255]\n"
    "s_addk_i32 s53, 4096\n"
    "v_mfma_f32_16x16x32_bf16 v[220:223], a[224:227], a[184:187], v[220:223]\n"
    "buffer_load_dwordx4 a[216:219], v240, s[20:23], s53 offen offset:0 sc1\n"
    "v_mfma_f32_16x16x32_bf16 v[220:223], a[220:223], a[152:155], v[220:223]\n"
    "ds_read_b128 a[156:159], v241 offset:47104\n"
    "ds_read_b128 a[188:191], v241 offset:48128\n"
    "v_mfma_f32_16x16x32_bf16 v[188:191], a[220:223], a[148:151], v[188:191]\n"
    "ds_read_b128 a[184:187], v241 offset:46080\n"
    "v_mfma_f32_16x16x32_bf16 v[188:191], a[224:227], a[180:183], v[188:191]\n"
    "ds_read_b128 a[152:155], v241 offset:45056\n"
    "v_mfma_f32_16x16x32_bf16 v[156:159], a[224:227], a[176:179], v[156:159]\n"
    "ds_read_b128 a[148:151], v241 offset:43008\n"
    "v_mfma_f32_16x16x32_bf16 v[156:159], a[220:223], a[144:147], v[156:159]\n"
    "ds_read_b128 a[180:183], v241 offset:44032\n"
    "v_mfma_f32_16x16x32_bf16 v[124:127], a[220:223], a[140:143], v[124:127]\n"
    "ds_read_b128 a[176:179], v241 offset:41984\n"
    "v_mfma_f32_16x16x32_bf16 v[124:127], a[224:227], a[172:175], v[124:127]\n"
    "ds_read_b128 a[144:147], v241 offset:40960\n"
    "v_mfma_f32_16x16x32_bf16 v[92:95], a[224:227], a[168:171], v[92:95]\n"
    "ds_read_b128 a[140:143], v241 offset:38912\n"
    "v_mfma_f32_16x16x32_bf16 v[92:95], a[220:223], a[136:139], v[92:95]\n"
    "ds_read_b128 a[172:175], v241 offset:39936\n"
    "v_mfma_f32_16x16x32_bf16 v[60:63], a[220:223], a[132:135], v[60:63]\n"
    "ds_read_b128 a[168:171], v241 offset:37888\n"
    "v_mfma_f32_16x16x32_bf16 v[60:63], a[224:227], a[164:167], v[60:63]\n"
    "ds_read_b128 a[136:139], v241 offset:36864\n"
    "v_mfma_f32_16x16x32_bf16 v[28:31], a[224:227], a[160:163], v[28:31]\n"
    "ds_read_b128 a[132:135], v241 offset:34816\n"
    "v_mfma_f32_16x16x32_bf16 v[28:31], a[220:223], a[128:131], v[28:31]\n"
    "s_waitcnt vmcnt(7) lgkmcnt(11)\n"
    "ds_read_b128 a[164:167], v241 offset:35840\n"
    "v_mfma_f32_16x16x32_bf16 v[224:227], a[232:235], a[156:159], v[224:227]\n"
    "buffer_load_dwordx4 a[220:223], v240, s[20:23], s53 offen offset:1024 sc1\n"
    "v_mfma_f32_16x16x32_bf16 v[224:227], a[236:239], a[188:191], v[224:227]\n"
    "s_waitcnt lgkmcnt(10)\n"
    "v_mfma_f32_16x16x32_bf16 v[192:195], a[236:239], a[184:187], v[192:195]\n"
    "buffer_load_dwordx4 a[224:227], v240, s[20:23], s53 offen offset:2048 sc1\n"
    "v_mfma_f32_16x16x32_bf16 v[192:195], a[232:235], a[152:155], v[192:195]\n"
    "s_waitcnt lgkmcnt(8)\n"
    "v_mfma_f32_16x16x32_bf16 v[160:163], a[232:235], a[148:151], v[160:163]\n"
    "buffer_load_dwordx4 a[228:231], v240, s[20:23], s53 offen offset:3072 sc1\n"
    "s_addk_i32 s53, 4096\n"
    "ds_read_b128 a[160:163], v241 offset:33792\n"
    "ds_read_b128 a[128:131], v241 offset:32768\n"
    "v_mfma_f32_16x16x32_bf16 v[160:163], a[236:239], a[180:183], v[160:163]\n"
    "s_waitcnt lgkmcnt(8)\n"
    "v_mfma_f32_16x16x32_bf16 v[128:131], a[236:239], a[176:179], v[128:131]\n"
    "v_mfma_f32_16x16x32_bf16 v[128:131], a[232:235], a[144:147], v[128:131]\n"
    "s_waitcnt lgkmcnt(6)\n"
    "v_mfma_f32_16x16x32_bf16 v[96:99], a[232:235], a[140:143], v[96:99]\n"
    "v_mfma_f32_16x16x32_bf16 v[96:99], a[236:239], a[172:175], v[96:99]\n"
    "s_waitcnt lgkmcnt(4)\n"
    "v_mfma_f32_16x16x32_bf16 v[64:67], a[236:239], a[168:171], v[64:67]\n"
    "v_mfma_f32_16x16x32_bf16 v[64:67], a[232:235], a[136:139], v[64:67]\n"
    "s_waitcnt lgkmcnt(2)\n"
    "v_mfma_f32_16x16x32_bf16 v[32:35], a[232:235], a[132:135], v[32:35]\n"
    "v_mfma_f32_16x16x32_bf16 v[32:35], a[236:239], a[164:167], v[32:35]\n"
    "s_waitcnt lgkmcnt(0)\n"
    "v_mfma_f32_16x16x32_bf16 v[0:3], a[236:239], a[160:163], v[0:3]\n"
    "v_mfma_f32_16x16x32_bf16 v[0:3], a[232:235], a[128:131], v[0:3]\n"
    "s_waitcnt vmcnt(8)\n"
    "v_mfma_f32_16x16x32_bf16 v[4:7], a[192:195], a[128:131], v[4:7]\n"
    "buffer_load_dwordx4 a[232:235], v240, s[20:23], s53 offen offset:0 sc1\n"
    "v_mfma_f32_16x16x32_bf16 v[4:7], a[196:199], a[160:163], v[4:7]\n"
    "buffer_load_dwordx4 a[236:239], v240, s[20:23], s53 offen offset:1024 sc1\n"
    "v_mfma_f32_16x16x32_bf16 v[36:39], a[196:199], a[164:167], v[36:39]\n"
    "v_mfma_f32_16x16x32_bf16 v[36:39], a[192:195], a[132:135], v[36:39]\n"
    "v_mfma_f32_16x16x32_bf16 v[68:71], a[192:195], a[136:139], v[68:71]\n"
    "v_mfma_f32_16x16x32_bf16 v[68:71], a[196:199], a[168:171], v[68:71]\n"
    "v_mfma_f32_16x16x32_bf16 v[100:103], a[196:199], a[172:175], v[100:103]\n"
    "v_mfma_f32_16x16x32_bf16 v[100:103], a[192:195], a[140:143], v[100:103]\n"
    "v_mfma_f32_16x16x32_bf16 v[132:135], a[192:195], a[144:147], v[132:135]\n"
    "v_mfma_f32_16x16x32_bf16 v[132:135], a[196:199], a[176:179], v[132:135]\n"
    "v_mfma_f32_16x16x32_bf16 v[164:167], a[196:199], a[180:183], v[164:167]\n"
    "v_mfma_f32_16x16x32_bf16 v[164:167], a[192:195], a[148:151], v[164:167]\n"
    "v_mfma_f32_16x16x32_bf16 v[196:199], a[192:195], a[152:155], v[196:199]\n"
    "v_mfma_f32_16x16x32_bf16 v[196:199], a[196:199], a[184:187], v[196:199]\n"
    "v_mfma_f32_16x16x32_bf16 v[228:231], a[196:199], a[188:191], v[228:231]\n"
    "v_mfma_f32_16x16x32_bf16 v[228:231], a[192:195], a[156:159], v[228:231]\n"
    "s_waitcnt vmcnt(8)\n"
    "v_mfma_f32_16x16x32_bf16 v[232:235], a[200:203], a[156:159], v[232:235]\n"
    "buffer_load_dwordx4 a[192:195], v240, s[20:23], s53 offen offset:2048 sc1\n"
    "v_mfma_f32_16x16x32_bf16 v[232:235], a[204:207], a[188:191], v[232:235]\n"
    "buffer_load_dwordx4 a[196:199], v240, s[20:23], s53 offen offset:3072 sc1\n"
    "v_mfma_f32_16x16x32_bf16 v[200:203], a[204:207], a[184:187], v[200:203]\n"
    "s_addk_i32 s53, 4096\n"
    "v_mfma_f32_16x16x32_bf16 v[200:203], a[200:203], a[152:155], v[200:203]\n"
    "v_mfma_f32_16x16x32_bf16 v[168:171], a[200:203], a[148:151], v[168:171]\n"
    "v_mfma_f32_16x16x32_bf16 v[168:171], a[204:207], a[180:183], v[168:171]\n"
    "v_mfma_f32_16x16x32_bf16 v[136:139], a[204:207], a[176:179], v[136:139]\n"
    "v_mfma_f32_16x16x32_bf16 v[136:139], a[200:203], a[144:147], v[136:139]\n"
    "v_mfma_f32_16x16x32_bf16 v[104:107], a[200:203], a[140:143], v[104:107]\n"
    "v_mfma_f32_16x16x32_bf16 v[104:107], a[204:207], a[172:175], v[104:107]\n"
    "v_mfma_f32_16x16x32_bf16 v[72:75], a[204:207], a[168:171], v[72:75]\n"
    "v_mfma_f32_16x16x32_bf16 v[72:75], a[200:203], a[136:139], v[72:75]\n"
    "v_mfma_f32_16x16x32_bf16 v[40:43], a[200:203], a[132:135], v[40:43]\n"
    "v_mfma_f32_16x16x32_bf16 v[40:43], a[204:207], a[164:167], v[40:43]\n"
    "v_mfma_f32_16x16x32_bf16 v[8:11], a[204:207], a[160:163], v[8:11]\n"
    "v_mfma_f32_16x16x32_bf16 v[8:11], a[200:203], a[128:131], v[8:11]\n"
    "s_waitcnt vmcnt(8)\n"
    "v_mfma_f32_16x16x32_bf16 v[12:15], a[208:211], a[128:131], v[12:15]\n"
    "buffer_load_dwordx4 a[204:207], v240, s[20:23], s53 offen offset:0 sc1\n"
    "v_mfma_f32_16x16x32_bf16 v[12:15], a[212:215], a[160:163], v[12:15]\n"
    "v_mfma_f32_16x16x32_bf16 v[44:47], a[212:215], a[164:167], v[44:47]\n"
    "v_mfma_f32_16x16x32_bf16 v[44:47], a[208:211], a[132:135], v[44:47]\n"
    "v_mfma_f32_16x16x32_bf16 v[76:79], a[208:211], a[136:139], v[76:79]\n"
    "v_mfma_f32_16x16x32_bf16 v[76:79], a[212:215], a[168:171], v[76:79]\n"
    "v_mfma_f32_16x16x32_bf16 v[108:111], a[212:215], a[172:175], v[108:111]\n"
    "v_mfma_f32_16x16x32_bf16 v[108:111], a[208:211], a[140:143], v[108:111]\n"
    "v_mfma_f32_16x16x32_bf16 v[140:143], a[208:211], a[144:147], v[140:143]\n"
    "v_mfma_f32_16x16x32_bf16 v[140:143], a[212:215], a[176:179], v[140:143]\n"
    "v_mfma_f32_16x16x32_bf16 v[172:175], a[212:215], a[180:183], v[172:175]\n"
    "v_mfma_f32_16x16x32_bf16 v[172:175], a[208:211], a[148:151], v[172:175]\n"
    "v_mfma_f32_16x16x32_bf16 v[204:207], a[208:211], a[152:155], v[204:207]\n"
    "v_mfma_f32_16x16x32_bf16 v[204:207], a[212:215], a[184:187], v[204:207]\n"
    "v_mfma_f32_16x16x32_bf16 v[236:239], a[212:215], a[188:191], v[236:239]\n"
    "v_mfma_f32_16x16x32_bf16 v[236:239], a[208:211], a[156:159], v[236:239]\n"
    "s_waitcnt vmcnt(7)\n"
    "v_mfma_f32_16x16x32_bf16 a[240:243], a[216:219], a[156:159], a[240:243]\n"
    "buffer_load_dwordx4 a[208:211], v240, s[20:23], s53 offen offset:1024 sc1\n"
    "v_mfma_f32_16x16x32_bf16 a[240:243], a[220:223], a[188:191], a[240:243]\n"
    "buffer_load_dwordx4 a[212:215], v240, s[20:23], s53 offen offset:2048 sc1\n"
    "v_mfma_f32_16x16x32_bf16 v[208:211], a[220:223], a[184:187], v[208:211]\n"
    "v_mfma_f32_16x16x32_bf16 v[208:211], a[216:219], a[152:155], v[208:211]\n"
    "v_mfma_f32_16x16x32_bf16 v[176:179], a[216:219], a[148:151], v[176:179]\n"
    "v_mfma_f32_16x16x32_bf16 v[176:179], a[220:223], a[180:183], v[176:179]\n"
    "v_mfma_f32_16x16x32_bf16 v[144:147], a[220:223], a[176:179], v[144:147]\n"
    "v_mfma_f32_16x16x32_bf16 v[144:147], a[216:219], a[144:147], v[144:147]\n"
    "v_mfma_f32_16x16x32_bf16 v[112:115], a[216:219], a[140:143], v[112:115]\n"
    "v_mfma_f32_16x16x32_bf16 v[112:115], a[220:223], a[172:175], v[112:115]\n"
    "v_mfma_f32_16x16x32_bf16 v[80:83], a[220:223], a[168:171], v[80:83]\n"
    "v_mfma_f32_16x16x32_bf16 v[80:83], a[216:219], a[136:139], v[80:83]\n"
    "v_mfma_f32_16x16x32_bf16 v[48:51], a[216:219], a[132:135], v[48:51]\n"
    "v_mfma_f32_16x16x32_bf16 v[48:51], a[220:223], a[164:167], v[48:51]\n"
    "v_mfma_f32_16x16x32_bf16 v[16:19], a[220:223], a[160:163], v[16:19]\n"
    "v_mfma_f32_16x16x32_bf16 v[16:19], a[216:219], a[128:131], v[16:19]\n"
    "s_waitcnt vmcnt(7)\n"
    "v_mfma_f32_16x16x32_bf16 v[20:23], a[224:227], a[128:131], v[20:23]\n"
    "buffer_load_dwordx4 a[216:219], v240, s[20:23], s53 offen offset:3072 sc1\n"
    "v_mfma_f32_16x16x32_bf16 v[20:23], a[228:231], a[160:163], v[20:23]\n"
    "s_addk_i32 s53, 4096\n"
    "v_mfma_f32_16x16x32_bf16 v[52:55], a[228:231], a[164:167], v[52:55]\n"
    "buffer_load_dwordx4 a[220:223], v240, s[20:23], s53 offen offset:0 sc1\n"
    "v_mfma_f32_16x16x32_bf16 v[52:55], a[224:227], a[132:135], v[52:55]\n"
    "v_mfma_f32_16x16x32_bf16 v[84:87], a[224:227], a[136:139], v[84:87]\n"
    "v_mfma_f32_16x16x32_bf16 v[84:87], a[228:231], a[168:171], v[84:87]\n"
    "v_mfma_f32_16x16x32_bf16 v[116:119], a[228:231], a[172:175], v[116:119]\n"
    "v_mfma_f32_16x16x32_bf16 v[116:119], a[224:227], a[140:143], v[116:119]\n"
    "v_mfma_f32_16x16x32_bf16 v[148:151], a[224:227], a[144:147], v[148:151]\n"
    "v_mfma_f32_16x16x32_bf16 v[148:151], a[228:231], a[176:179], v[148:151]\n"
    "v_mfma_f32_16x16x32_bf16 v[180:183], a[228:231], a[180:183], v[180:183]\n"
    "v_mfma_f32_16x16x32_bf16 v[180:183], a[224:227], a[148:151], v[180:183]\n"
    "v_mfma_f32_16x16x32_bf16 v[212:215], a[224:227], a[152:155], v[212:215]\n"
    "v_mfma_f32_16x16x32_bf16 v[212:215], a[228:231], a[184:187], v[212:215]\n"
    "v_mfma_f32_16x16x32_bf16 a[244:247], a[228:231], a[188:191], a[244:247]\n"
    "v_mfma_f32_16x16x32_bf16 a[244:247], a[224:227], a[156:159], a[244:247]\n"
    "s_waitcnt vmcnt(7)\n"
    "v_mfma_f32_16x16x32_bf16 a[248:251], a[232:235], a[156:159], a[248:251]\n"
    "buffer_load_dwordx4 a[224:227], v240, s[20:23], s53 offen offset:1024 sc1\n"
    "v_mfma_f32_16x16x32_bf16 a[248:251], a[236:239], a[188:191], a[248:251]\n"
    "buffer_load_dwordx4 a[228:231], v240, s[20:23], s53 offen offset:2048 sc1\n"
    "v_mfma_f32_16x16x32_bf16 v[216:219], a[236:239], a[184:187], v[216:219]\n"
    "v_mfma_f32_16x16x32_bf16 v[216:219], a[232:235], a[152:155], v[216:219]\n"
    "v_mfma_f32_16x16x32_bf16 v[184:187], a[232:235], a[148:151], v[184:187]\n"
    "v_mfma_f32_16x16x32_bf16 v[184:187], a[236:239], a[180:183], v[184:187]\n"
    "v_mfma_f32_16x16x32_bf16 v[152:155], a[236:239], a[176:179], v[152:155]\n"
    "v_mfma_f32_16x16x32_bf16 v[152:155], a[232:235], a[144:147], v[152:155]\n"
    "v_mfma_f32_16x16x32_bf16 v[120:123], a[232:235], a[140:143], v[120:123]\n"
    "v_mfma_f32_16x16x32_bf16 v[120:123], a[236:239], a[172:175], v[120:123]\n"
    "v_mfma_f32_16x16x32_bf16 v[88:91], a[236:239], a[168:171], v[88:91]\n"
    "v_mfma_f32_16x16x32_bf16 v[88:91], a[232:235], a[136:139], v[88:91]\n"
    "v_mfma_f32_16x16x32_bf16 v[56:59], a[232:235], a[132:135], v[56:59]\n"
    "v_mfma_f32_16x16x32_bf16 v[56:59], a[236:239], a[164:167], v[56:59]\n"
    "v_mfma_f32_16x16x32_bf16 v[24:27], a[236:239], a[160:163], v[24:27]\n"
    "v_mfma_f32_16x16x32_bf16 v[24:27], a[232:235], a[128:131], v[24:27]\n"
    "s_waitcnt vmcnt(7)\n"
    "v_mfma_f32_16x16x32_bf16 v[28:31], a[192:195], a[128:131], v[28:31]\n"
    "buffer_load_dwordx4 a[232:235], v240, s[20:23], s53 offen offset:3072 sc1\n"
    "v_mfma_f32_16x16x32_bf16 v[28:31], a[196:199], a[160:163], v[28:31]\n"
    "s_addk_i32 s53, 4096\n"
    "v_mfma_f32_16x16x32_bf16 v[60:63], a[196:199], a[164:167], v[60:63]\n"
    "buffer_load_dwordx4 a[236:239], v240, s[20:23], s53 offen offset:0 sc1\n"
    "v_mfma_f32_16x16x32_bf16 v[60:63], a[192:195], a[132:135], v[60:63]\n"
    "ds_read_b128 a[128:131], v241 offset:49152\n"
    "ds_read_b128 a[160:163], v241 offset:50176\n"
    "v_mfma_f32_16x16x32_bf16 v[92:95], a[192:195], a[136:139], v[92:95]\n"
    "ds_read_b128 a[164:167], v241 offset:52224\n"
    "v_mfma_f32_16x16x32_bf16 v[92:95], a[196:199], a[168:171], v[92:95]\n"
    "ds_read_b128 a[132:135], v241 offset:51200\n"
    "v_mfma_f32_16x16x32_bf16 v[124:127], a[196:199], a[172:175], v[124:127]\n"
    "ds_read_b128 a[136:139], v241 offset:53248\n"
    "v_mfma_f32_16x16x32_bf16 v[124:127], a[192:195], a[140:143], v[124:127]\n"
    "ds_read_b128 a[168:171], v241 offset:54272\n"
    "v_mfma_f32_16x16x32_bf16 v[156:159], a[192:195], a[144:147], v[156:159]\n"
    "ds_read_b128 a[172:175], v241 offset:56320\n"
    "v_mfma_f32_16x16x32_bf16 v[156:159], a[196:199], a[176:179], v[156:159]\n"
    "ds_read_b128 a[140:143], v241 offset:55296\n"
    "v_mfma_f32_16x16x32_bf16 v[188:191], a[196:199], a[180:183], v[188:191]\n"
    "ds_read_b128 a[144:147], v241 offset:57344\n"
    "v_mfma_f32_16x16x32_bf16 v[188:191], a[192:195], a[148:151], v[188:191]\n"
    "ds_read_b128 a[176:179], v241 offset:58368\n"
    "v_mfma_f32_16x16x32_bf16 v[220:223], a[192:195], a[152:155], v[220:223]\n"
    "ds_read_b128 a[180:183], v241 offset:60416\n"
    "v_mfma_f32_16x16x32_bf16 v[220:223], a[196:199], a[184:187], v[220:223]\n"
    "ds_read_b128 a[148:151], v241 offset:59392\n"
    "v_mfma_f32_16x16x32_bf16 a[252:255], a[196:199], a[188:191], a[252:255]\n"
    "ds_read_b128 a[152:155], v241 offset:61440\n"
    "v_mfma_f32_16x16x32_bf16 a[252:255], a[192:195], a[156:159], a[252:255]\n"
    "s_waitcnt vmcnt(7) lgkmcnt(11)\n"
    "ds_read_b128 a[184:187], v241 offset:62464\n"
    "v_mfma_f32_16x16x32_bf16 v[0:3], a[204:207], a[128:131], v[0:3]\n"
    "buffer_load_dwordx4 a[192:195], v240, s[20:23], s53 offen offset:1024 sc1\n"
    "v_mfma_f32_16x16x32_bf16 v[0:3], a[208:211], a[160:163], v[0:3]\n"
    "s_waitcnt lgkmcnt(10)\n"
    "v_mfma_f32_16x16x32_bf16 v[32:35], a[208:211], a[164:167], v[32:35]\n"
    "buffer_load_dwordx4 a[196:199], v240, s[20:23], s53 offen offset:2048 sc1\n"
    "v_mfma_f32_16x16x32_bf16 v[32:35], a[204:207], a[132:135], v[32:35]\n"
    "s_waitcnt lgkmcnt(8)\n"
    "v_mfma_f32_16x16x32_bf16 v[64:67], a[204:207], a[136:139], v[64:67]\n"
    "buffer_load_dwordx4 a[200:203], v240, s[20:23], s53 offen offset:3072 sc1\n"
    "s_addk_i32 s53, 4096\n"
    "ds_read_b128 a[188:191], v241 offset:64512\n"
    "ds_read_b128 a[156:159], v241 offset:63488\n"
    "v_mfma_f32_16x16x32_bf16 v[64:67], a[208:211], a[168:171], v[64:67]\n"
    "s_waitcnt lgkmcnt(8)\n"
    "v_mfma_f32_16x16x32_bf16 v[96:99], a[208:211], a[172:175], v[96:99]\n"
    "v_mfma_f32_16x16x32_bf16 v[96:99], a[204:207], a[140:143], v[96:99]\n"
    "s_waitcnt lgkmcnt(6)\n"
    "v_mfma_f32_16x16x32_bf16 v[128:131], a[204:207], a[144:147], v[128:131]\n"
    "v_mfma_f32_16x16x32_bf16 v[128:131], a[208:211], a[176:179], v[128:131]\n"
    "s_waitcnt lgkmcnt(4)\n"
    "v_mfma_f32_16x16x32_bf16 v[160:163], a[208:211], a[180:183], v[160:163]\n"
    "v_mfma_f32_16x16x32_bf16 v[160:163], a[204:207], a[148:151], v[160:163]\n"
    "s_waitcnt lgkmcnt(2)\n"
    "v_mfma_f32_16x16x32_bf16 v[192:195], a[204:207], a[152:155], v[192:195]\n"
    "v_mfma_f32_16x16x32_bf16 v[192:195], a[208:211], a[184:187], v[192:195]\n"
    "s_waitcnt lgkmcnt(0)\n"
    "v_mfma_f32_16x16x32_bf16 v[224:227], a[208:211], a[188:191], v[224:227]\n"
    "v_mfma_f32_16x16x32_bf16 v[224:227], a[204:207], a[156:159], v[224:227]\n"
    "s_waitcnt vmcnt(8)\n"
    "v_mfma_f32_16x16x32_bf16 v[228:231], a[212:215], a[156:159], v[228:231]\n"
    "buffer_load_dwordx4 a[204:207], v240, s[20:23], s53 offen offset:0 sc1\n"
    "v_mfma_f32_16x16x32_bf16 v[228:231], a[216:219], a[188:191], v[228:231]\n"
    "buffer_load_dwordx4 a[208:211], v240, s[20:23], s53 offen offset:1024 sc1\n"
    "v_mfma_f32_16x16x32_bf16 v[196:199], a[216:219], a[184:187], v[196:199]\n"
    "v_mfma_f32_16x16x32_bf16 v[196:199], a[212:215], a[152:155], v[196:199]\n"
    "v_mfma_f32_16x16x32_bf16 v[164:167], a[212:215], a[148:151], v[164:167]\n"
    "v_mfma_f32_16x16x32_bf16 v[164:167], a[216:219], a[180:183], v[164:167]\n"
    "v_mfma_f32_16x16x32_bf16 v[132:135], a[216:219], a[176:179], v[132:135]\n"
    "v_mfma_f32_16x16x32_bf16 v[132:135], a[212:215], a[144:147], v[132:135]\n"
    "v_mfma_f32_16x16x32_bf16 v[100:103], a[212:215], a[140:143], v[100:103]\n"
    "v_mfma_f32_16x16x32_bf16 v[100:103], a[216:219], a[172:175], v[100:103]\n"
    "v_mfma_f32_16x16x32_bf16 v[68:71], a[216:219], a[168:171], v[68:71]\n"
    "v_mfma_f32_16x16x32_bf16 v[68:71], a[212:215], a[136:139], v[68:71]\n"
    "v_mfma_f32_16x16x32_bf16 v[36:39], a[212:215], a[132:135], v[36:39]\n"
    "v_mfma_f32_16x16x32_bf16 v[36:39], a[216:219], a[164:167], v[36:39]\n"
    "v_mfma_f32_16x16x32_bf16 v[4:7], a[216:219], a[160:163], v[4:7]\n"
    "v_mfma_f32_16x16x32_bf16 v[4:7], a[212:215], a[128:131], v[4:7]\n"
    "s_waitcnt vmcnt(8)\n"
    "v_mfma_f32_16x16x32_bf16 v[8:11], a[220:223], a[128:131], v[8:11]\n"
    "buffer_load_dwordx4 a[212:215], v240, s[20:23], s53 offen offset:2048 sc1\n"
    "v_mfma_f32_16x16x32_bf16 v[8:11], a[224:227], a[160:163], v[8:11]\n"
    "buffer_load_dwordx4 a[216:219], v240, s[20:23], s53 offen offset:3072 sc1\n"
    "v_mfma_f32_16x16x32_bf16 v[40:43], a[224:227], a[164:167], v[40:43]\n"
    "s_addk_i32 s53, 4096\n"
    "v_mfma_f32_16x16x32_bf16 v[40:43], a[220:223], a[132:135], v[40:43]\n"
    "v_mfma_f32_16x16x32_bf16 v[72:75], a[220:223], a[136:139], v[72:75]\n"
    "v_mfma_f32_16x16x32_bf16 v[72:75], a[224:227], a[168:171], v[72:75]\n"
    "v_mfma_f32_16x16x32_bf16 v[104:107], a[224:227], a[172:175], v[104:107]\n"
    "v_mfma_f32_16x16x32_bf16 v[104:107], a[220:223], a[140:143], v[104:107]\n"
    "v_mfma_f32_16x16x32_bf16 v[136:139], a[220:223], a[144:147], v[136:139]\n"
    "v_mfma_f32_16x16x32_bf16 v[136:139], a[224:227], a[176:179], v[136:139]\n"
    "v_mfma_f32_16x16x32_bf16 v[168:171], a[224:227], a[180:183], v[168:171]\n"
    "v_mfma_f32_16x16x32_bf16 v[168:171], a[220:223], a[148:151], v[168:171]\n"
    "v_mfma_f32_16x16x32_bf16 v[200:203], a[220:223], a[152:155], v[200:203]\n"
    "v_mfma_f32_16x16x32_bf16 v[200:203], a[224:227], a[184:187], v[200:203]\n"
    "v_mfma_f32_16x16x32_bf16 v[232:235], a[224:227], a[188:191], v[232:235]\n"
    "v_mfma_f32_16x16x32_bf16 v[232:235], a[220:223], a[156:159], v[232:235]\n"
    "s_waitcnt vmcnt(8)\n"
    "v_mfma_f32_16x16x32_bf16 v[236:239], a[228:231], a[156:159], v[236:239]\n"
    "buffer_load_dwordx4 a[220:223], v240, s[20:23], s53 offen offset:0 sc1\n"
    "v_mfma_f32_16x16x32_bf16 v[236:239], a[232:235], a[188:191], v[236:239]\n"
    "buffer_load_dwordx4 a[224:227], v240, s[20:23], s53 offen offset:1024 sc1\n"
    "v_mfma_f32_16x16x32_bf16 v[204:207], a[232:235], a[184:187], v[204:207]\n"
    "v_mfma_f32_16x16x32_bf16 v[204:207], a[228:231], a[152:155], v[204:207]\n"
    "v_mfma_f32_16x16x32_bf16 v[172:175], a[228:231], a[148:151], v[172:175]\n"
    "v_mfma_f32_16x16x32_bf16 v[172:175], a[232:235], a[180:183], v[172:175]\n"
    "v_mfma_f32_16x16x32_bf16 v[140:143], a[232:235], a[176:179], v[140:143]\n"
    "v_mfma_f32_16x16x32_bf16 v[140:143], a[228:231], a[144:147], v[140:143]\n"
    "v_mfma_f32_16x16x32_bf16 v[108:111], a[228:231], a[140:143], v[108:111]\n"
    "v_mfma_f32_16x16x32_bf16 v[108:111], a[232:235], a[172:175], v[108:111]\n"
    "v_mfma_f32_16x16x32_bf16 v[76:79], a[232:235], a[168:171], v[76:79]\n"
    "v_mfma_f32_16x16x32_bf16 v[76:79], a[228:231], a[136:139], v[76:79]\n"
    "v_mfma_f32_16x16x32_bf16 v[44:47], a[228:231], a[132:135], v[44:47]\n"
    "v_mfma_f32_16x16x32_bf16 v[44:47], a[232:235], a[164:167], v[44:47]\n"
    "v_mfma_f32_16x16x32_bf16 v[12:15], a[232:235], a[160:163], v[12:15]\n"
    "v_mfma_f32_16x16x32_bf16 v[12:15], a[228:231], a[128:131], v[12:15]\n"
    "s_waitcnt vmcnt(8)\n"
    "v_mfma_f32_16x16x32_bf16 v[16:19], a[236:239], a[128:131], v[16:19]\n"
    "buffer_load_dwordx4 a[228:231], v240, s[20:23], s53 offen offset:2048 sc1\n"
    "v_mfma_f32_16x16x32_bf16 v[16:19], a[192:195], a[160:163], v[16:19]\n"
    "buffer_load_dwordx4 a[232:235], v240, s[20:23], s53 offen offset:3072 sc1\n"
    "v_mfma_f32_16x16x32_bf16 v[48:51], a[192:195], a[164:167], v[48:51]\n"
    "s_addk_i32 s53, 4096\n"
    "v_mfma_f32_16x16x32_bf16 v[48:51], a[236:239], a[132:135], v[48:51]\n"
    "v_mfma_f32_16x16x32_bf16 v[80:83], a[236:239], a[136:139], v[80:83]\n"
    "v_mfma_f32_16x16x32_bf16 v[80:83], a[192:195], a[168:171], v[80:83]\n"
    "v_mfma_f32_16x16x32_bf16 v[112:115], a[192:195], a[172:175], v[112:115]\n"
    "v_mfma_f32_16x16x32_bf16 v[112:115], a[236:239], a[140:143], v[112:115]\n"
    "v_mfma_f32_16x16x32_bf16 v[144:147], a[236:239], a[144:147], v[144:147]\n"
    "v_mfma_f32_16x16x32_bf16 v[144:147], a[192:195], a[176:179], v[144:147]\n"
    "v_mfma_f32_16x16x32_bf16 v[176:179], a[192:195], a[180:183], v[176:179]\n"
    "v_mfma_f32_16x16x32_bf16 v[176:179], a[236:239], a[148:151], v[176:179]\n"
    "v_mfma_f32_16x16x32_bf16 v[208:211], a[236:239], a[152:155], v[208:211]\n"
    "v_mfma_f32_16x16x32_bf16 v[208:211], a[192:195], a[184:187], v[208:211]\n"
    "v_mfma_f32_16x16x32_bf16 a[240:243], a[192:195], a[188:191], a[240:243]\n"
    "v_mfma_f32_16x16x32_bf16 a[240:243], a[236:239], a[156:159], a[240:243]\n"
    "s_waitcnt vmcnt(8)\n"
    "v_mfma_f32_16x16x32_bf16 a[244:247], a[196:199], a[156:159], a[244:247]\n"
    "buffer_load_dwordx4 a[192:195], v240, s[20:23], s53 offen offset:0 sc1\n"
    "v_mfma_f32_16x16x32_bf16 a[244:247], a[200:203], a[188:191], a[244:247]\n"
    "v_mfma_f32_16x16x32_bf16 v[212:215], a[200:203], a[184:187], v[212:215]\n"
    "v_mfma_f32_16x16x32_bf16 v[212:215], a[196:199], a[152:155], v[212:215]\n"
    "v_mfma_f32_16x16x32_bf16 v[180:183], a[196:199], a[148:151], v[180:183]\n"
    "v_mfma_f32_16x16x32_bf16 v[180:183], a[200:203], a[180:183], v[180:183]\n"
    "v_mfma_f32_16x16x32_bf16 v[148:151], a[200:203], a[176:179], v[148:151]\n"
    "v_mfma_f32_16x16x32_bf16 v[148:151], a[196:199], a[144:147], v[148:151]\n"
    "v_mfma_f32_16x16x32_bf16 v[116:119], a[196:199], a[140:143], v[116:119]\n"
    "v_mfma_f32_16x16x32_bf16 v[116:119], a[200:203], a[172:175], v[116:119]\n"
    "v_mfma_f32_16x16x32_bf16 v[84:87], a[200:203], a[168:171], v[84:87]\n"
    "v_mfma_f32_16x16x32_bf16 v[84:87], a[196:199], a[136:139], v[84:87]\n"
    "v_mfma_f32_16x16x32_bf16 v[52:55], a[196:199], a[132:135], v[52:55]\n"
    "v_mfma_f32_16x16x32_bf16 v[52:55], a[200:203], a[164:167], v[52:55]\n"
    "v_mfma_f32_16x16x32_bf16 v[20:23], a[200:203], a[160:163], v[20:23]\n"
    "v_mfma_f32_16x16x32_bf16 v[20:23], a[196:199], a[128:131], v[20:23]\n"
    "s_waitcnt vmcnt(7)\n"
    "v_mfma_f32_16x16x32_bf16 v[24:27], a[204:207], a[128:131], v[24:27]\n"
    "buffer_load_dwordx4 a[196:199], v240, s[20:23], s53 offen offset:1024 sc1\n"
    "v_mfma_f32_16x16x32_bf16 v[24:27], a[208:211], a[160:163], v[24:27]\n"
    "buffer_load_dwordx4 a[200:203], v240, s[20:23], s53 offen offset:2048 sc1\n"
    "v_mfma_f32_16x16x32_bf16 v[56:59], a[208:211], a[164:167], v[56:59]\n"
    "v_mfma_f32_16x16x32_bf16 v[56:59], a[204:207], a[132:135], v[56:59]\n"
    "v_mfma_f32_16x16x32_bf16 v[88:91], a[204:207], a[136:139], v[88:91]\n"
    "v_mfma_f32_16x16x32_bf16 v[88:91], a[208:211], a[168:171], v[88:91]\n"
    "v_mfma_f32_16x16x32_bf16 v[120:123], a[208:211], a[172:175], v[120:123]\n"
    "v_mfma_f32_16x16x32_bf16 v[120:123], a[204:207], a[140:143], v[120:123]\n"
    "v_mfma_f32_16x16x32_bf16 v[152:155], a[204:207], a[144:147], v[152:155]\n"
    "v_mfma_f32_16x16x32_bf16 v[152:155], a[208:211], a[176:179], v[152:155]\n"
    "v_mfma_f32_16x16x32_bf16 v[184:187], a[208:211], a[180:183], v[184:187]\n"
    "v_mfma_f32_16x16x32_bf16 v[184:187], a[204:207], a[148:151], v[184:187]\n"
    "v_mfma_f32_16x16x32_bf16 v[216:219], a[204:207], a[152:155], v[216:219]\n"
    "v_mfma_f32_16x16x32_bf16 v[216:219], a[208:211], a[184:187], v[216:219]\n"
    "s_barrier\n"
    "v_mfma_f32_16x16x32_bf16 a[248:251], a[208:211], a[188:191], a[248:251]\n"
    "v_mfma_f32_16x16x32_bf16 a[248:251], a[204:207], a[156:159], a[248:251]\n"
    "s_waitcnt vmcnt(7)\n"
    "v_mfma_f32_16x16x32_bf16 a[252:255], a[212:215], a[156:159], a[252:255]\n"
    "buffer_load_dwordx4 a[204:207], v240, s[20:23], s53 offen offset:3072 sc1\n"
    "v_mfma_f32_16x16x32_bf16 a[252:255], a[216:219], a[188:191], a[252:255]\n"
    "s_addk_i32 s53, 4096\n"
    "v_mfma_f32_16x16x32_bf16 v[220:223], a[216:219], a[184:187], v[220:223]\n"
    "buffer_load_dwordx4 a[208:211], v240, s[20:23], s53 offen offset:0 sc1\n"
    "v_mfma_f32_16x16x32_bf16 v[220:223], a[212:215], a[152:155], v[220:223]\n"
    "v_add_u32 v241, s67, v240\n"
    "ds_read_b128 a[156:159], v241 offset:14336\n"
    "v_mfma_f32_16x16x32_bf16 v[188:191], a[212:215], a[148:151], v[188:191]\n"
    "ds_read_b128 a[188:191], v241 offset:15360\n"
    "ds_read_b128 a[184:187], v241 offset:13312\n"
    "v_mfma_f32_16x16x32_bf16 v[188:191], a[216:219], a[180:183], v[188:191]\n"
    "ds_read_b128 a[152:155], v241 offset:12288\n"
    "v_mfma_f32_16x16x32_bf16 v[156:159], a[216:219], a[176:179], v[156:159]\n"
    "ds_read_b128 a[148:151], v241 offset:10240\n"
    "v_mfma_f32_16x16x32_bf16 v[156:159], a[212:215], a[144:147], v[156:159]\n"
    "ds_read_b128 a[180:183], v241 offset:11264\n"
    "v_mfma_f32_16x16x32_bf16 v[124:127], a[212:215], a[140:143], v[124:127]\n"
    "ds_read_b128 a[176:179], v241 offset:9216\n"
    "v_mfma_f32_16x16x32_bf16 v[124:127], a[216:219], a[172:175], v[124:127]\n"
    "ds_read_b128 a[144:147], v241 offset:8192\n"
    "v_mfma_f32_16x16x32_bf16 v[92:95], a[216:219], a[168:171], v[92:95]\n"
    "ds_read_b128 a[140:143], v241 offset:6144\n"
    "v_mfma_f32_16x16x32_bf16 v[92:95], a[212:215], a[136:139], v[92:95]\n"
    "ds_read_b128 a[172:175], v241 offset:7168\n"
    "v_mfma_f32_16x16x32_bf16 v[60:63], a[212:215], a[132:135], v[60:63]\n"
    "ds_read_b128 a[168:171], v241 offset:5120\n"
    "v_mfma_f32_16x16x32_bf16 v[60:63], a[216:219], a[164:167], v[60:63]\n"
    "ds_read_b128 a[136:139], v241 offset:4096\n"
    "v_mfma_f32_16x16x32_bf16 v[28:31], a[216:219], a[160:163], v[28:31]\n"
    "ds_read_b128 a[132:135], v241 offset:2048\n"
    "v_mfma_f32_16x16x32_bf16 v[28:31], a[212:215], a[128:131], v[28:31]\n"
    "s_waitcnt vmcnt(7) lgkmcnt(11)\n"
    "ds_read_b128 a[164:167], v241 offset:3072\n"
    "v_mfma_f32_16x16x32_bf16 v[224:227], a[220:223], a[156:159], v[224:227]\n"
    "buffer_load_dwordx4 a[212:215], v240, s[20:23], s53 offen offset:1024 sc1\n"
    "v_mfma_f32_16x16x32_bf16 v[224:227], a[224:227], a[188:191], v[224:227]\n"
    "s_waitcnt lgkmcnt(10)\n"
    "v_mfma_f32_16x16x32_bf16 v[192:195], a[224:227], a[184:187], v[192:195]\n"
    "buffer_load_dwordx4 a[216:219], v240, s[20:23], s53 offen offset:2048 sc1\n"
    "v_mfma_f32_16x16x32_bf16 v[192:195], a[220:223], a[152:155], v[192:195]\n"
    "s_waitcnt lgkmcnt(8)\n"
    "ds_read_b128 a[160:163], v241 offset:1024\n"
    "ds_read_b128 a[128:131], v241 offset:0\n"
    "v_mfma_f32_16x16x32_bf16 v[160:163], a[220:223], a[148:151], v[160:163]\n"
    "v_mfma_f32_16x16x32_bf16 v[160:163], a[224:227], a[180:183], v[160:163]\n"
    "s_waitcnt lgkmcnt(8)\n"
    "v_mfma_f32_16x16x32_bf16 v[128:131], a[224:227], a[176:179], v[128:131]\n"
    "v_mfma_f32_16x16x32_bf16 v[128:131], a[220:223], a[144:147], v[128:131]\n"
    "s_waitcnt lgkmcnt(6)\n"
    "v_mfma_f32_16x16x32_bf16 v[96:99], a[220:223], a[140:143], v[96:99]\n"
    "v_mfma_f32_16x16x32_bf16 v[96:99], a[224:227], a[172:175], v[96:99]\n"
    "s_waitcnt lgkmcnt(4)\n"
    "v_mfma_f32_16x16x32_bf16 v[64:67], a[224:227], a[168:171], v[64:67]\n"
    "v_mfma_f32_16x16x32_bf16 v[64:67], a[220:223], a[136:139], v[64:67]\n"
    "s_waitcnt lgkmcnt(2)\n"
    "v_mfma_f32_16x16x32_bf16 v[32:35], a[220:223], a[132:135], v[32:35]\n"
    "v_mfma_f32_16x16x32_bf16 v[32:35], a[224:227], a[164:167], v[32:35]\n"
    "s_waitcnt lgkmcnt(0)\n"
    "v_mfma_f32_16x16x32_bf16 v[0:3], a[224:227], a[160:163], v[0:3]\n"
    "v_mfma_f32_16x16x32_bf16 v[0:3], a[220:223], a[128:131], v[0:3]\n"
    "s_waitcnt vmcnt(7)\n"
    "v_mfma_f32_16x16x32_bf16 v[4:7], a[228:231], a[128:131], v[4:7]\n"
    "buffer_load_dwordx4 a[220:223], v240, s[20:23], s53 offen offset:3072 sc1\n"
    "v_mfma_f32_16x16x32_bf16 v[4:7], a[232:235], a[160:163], v[4:7]\n"
    "s_addk_i32 s53, 4096\n"
    "v_mfma_f32_16x16x32_bf16 v[36:39], a[232:235], a[164:167], v[36:39]\n"
    "buffer_load_dwordx4 a[224:227], v240, s[20:23], s53 offen offset:0 sc1\n"
    "v_mfma_f32_16x16x32_bf16 v[36:39], a[228:231], a[132:135], v[36:39]\n"
    "v_mfma_f32_16x16x32_bf16 v[68:71], a[228:231], a[136:139], v[68:71]\n"
    "v_mfma_f32_16x16x32_bf16 v[68:71], a[232:235], a[168:171], v[68:71]\n"
    "v_mfma_f32_16x16x32_bf16 v[100:103], a[232:235], a[172:175], v[100:103]\n"
    "v_mfma_f32_16x16x32_bf16 v[100:103], a[228:231], a[140:143], v[100:103]\n"
    "v_mfma_f32_16x16x32_bf16 v[132:135], a[228:231], a[144:147], v[132:135]\n"
    "v_mfma_f32_16x16x32_bf16 v[132:135], a[232:235], a[176:179], v[132:135]\n"
    "v_mfma_f32_16x16x32_bf16 v[164:167], a[232:235], a[180:183], v[164:167]\n"
    "v_mfma_f32_16x16x32_bf16 v[164:167], a[228:231], a[148:151], v[164:167]\n"
    "v_mfma_f32_16x16x32_bf16 v[196:199], a[228:231], a[152:155], v[196:199]\n"
    "v_mfma_f32_16x16x32_bf16 v[196:199], a[232:235], a[184:187], v[196:199]\n"
    "v_mfma_f32_16x16x32_bf16 v[228:231], a[232:235], a[188:191], v[228:231]\n"
    "v_mfma_f32_16x16x32_bf16 v[228:231], a[228:231], a[156:159], v[228:231]\n"
    "s_waitcnt vmcnt(7)\n"
    "v_mfma_f32_16x16x32_bf16 v[232:235], a[192:195], a[156:159], v[232:235]\n"
    "buffer_load_dwordx4 a[228:231], v240, s[20:23], s53 offen offset:1024 sc1\n"
    "v_mfma_f32_16x16x32_bf16 v[232:235], a[196:199], a[188:191], v[232:235]\n"
    "buffer_load_dwordx4 a[232:235], v240, s[20:23], s53 offen offset:2048 sc1\n"
    "v_mfma_f32_16x16x32_bf16 v[200:203], a[196:199], a[184:187], v[200:203]\n"
    "buffer_load_dwordx4 a[236:239], v240, s[20:23], s53 offen offset:3072 sc1\n"
    "v_mfma_f32_16x16x32_bf16 v[200:203], a[192:195], a[152:155], v[200:203]\n"
    "s_addk_i32 s53, 4096\n"
    "v_mfma_f32_16x16x32_bf16 v[168:171], a[192:195], a[148:151], v[168:171]\n"
    "v_mfma_f32_16x16x32_bf16 v[168:171], a[196:199], a[180:183], v[168:171]\n"
    "v_mfma_f32_16x16x32_bf16 v[136:139], a[196:199], a[176:179], v[136:139]\n"
    "v_mfma_f32_16x16x32_bf16 v[136:139], a[192:195], a[144:147], v[136:139]\n"
    "v_mfma_f32_16x16x32_bf16 v[104:107], a[192:195], a[140:143], v[104:107]\n"
    "v_mfma_f32_16x16x32_bf16 v[104:107], a[196:199], a[172:175], v[104:107]\n"
    "v_mfma_f32_16x16x32_bf16 v[72:75], a[196:199], a[168:171], v[72:75]\n"
    "v_mfma_f32_16x16x32_bf16 v[72:75], a[192:195], a[136:139], v[72:75]\n"
    "v_mfma_f32_16x16x32_bf16 v[40:43], a[192:195], a[132:135], v[40:43]\n"
    "v_mfma_f32_16x16x32_bf16 v[40:43], a[196:199], a[164:167], v[40:43]\n"
    "v_mfma_f32_16x16x32_bf16 v[8:11], a[196:199], a[160:163], v[8:11]\n"
    "v_mfma_f32_16x16x32_bf16 v[8:11], a[192:195], a[128:131], v[8:11]\n"
    "s_waitcnt vmcnt(8)\n"
    "v_mfma_f32_16x16x32_bf16 v[12:15], a[200:203], a[128:131], v[12:15]\n"
    "buffer_load_dwordx4 a[192:195], v240, s[20:23], s53 offen offset:0 sc1\n"
    "v_mfma_f32_16x16x32_bf16 v[12:15], a[204:207], a[160:163], v[12:15]\n"
    "buffer_load_dwordx4 a[196:199], v240, s[20:23], s53 offen offset:1024 sc1\n"
    "v_mfma_f32_16x16x32_bf16 v[44:47], a[204:207], a[164:167], v[44:47]\n"
    "v_mfma_f32_16x16x32_bf16 v[44:47], a[200:203], a[132:135], v[44:47]\n"
    "v_mfma_f32_16x16x32_bf16 v[76:79], a[200:203], a[136:139], v[76:79]\n"
    "v_mfma_f32_16x16x32_bf16 v[76:79], a[204:207], a[168:171], v[76:79]\n"
    "v_mfma_f32_16x16x32_bf16 v[108:111], a[204:207], a[172:175], v[108:111]\n"
    "v_mfma_f32_16x16x32_bf16 v[108:111], a[200:203], a[140:143], v[108:111]\n"
    "v_mfma_f32_16x16x32_bf16 v[140:143], a[200:203], a[144:147], v[140:143]\n"
    "v_mfma_f32_16x16x32_bf16 v[140:143], a[204:207], a[176:179], v[140:143]\n"
    "v_mfma_f32_16x16x32_bf16 v[172:175], a[204:207], a[180:183], v[172:175]\n"
    "v_mfma_f32_16x16x32_bf16 v[172:175], a[200:203], a[148:151], v[172:175]\n"
    "v_mfma_f32_16x16x32_bf16 v[204:207], a[200:203], a[152:155], v[204:207]\n"
    "v_mfma_f32_16x16x32_bf16 v[204:207], a[204:207], a[184:187], v[204:207]\n"
    "v_mfma_f32_16x16x32_bf16 v[236:239], a[204:207], a[188:191], v[236:239]\n"
    "v_mfma_f32_16x16x32_bf16 v[236:239], a[200:203], a[156:159], v[236:239]\n"
    "s_waitcnt vmcnt(8)\n"
    "v_mfma_f32_16x16x32_bf16 a[240:243], a[208:211], a[156:159], a[240:243]\n"
    "buffer_load_dwordx4 a[200:203], v240, s[20:23], s53 offen offset:2048 sc1\n"
    "v_mfma_f32_16x16x32_bf16 a[240:243], a[212:215], a[188:191], a[240:243]\n"
    "buffer_load_dwordx4 a[204:207], v240, s[20:23], s53 offen offset:3072 sc1\n"
    "v_mfma_f32_16x16x32_bf16 v[208:211], a[212:215], a[184:187], v[208:211]\n"
    "s_addk_i32 s53, 4096\n"
    "v_mfma_f32_16x16x32_bf16 v[208:211], a[208:211], a[152:155], v[208:211]\n"
    "v_mfma_f32_16x16x32_bf16 v[176:179], a[208:211], a[148:151], v[176:179]\n"
    "v_mfma_f32_16x16x32_bf16 v[176:179], a[212:215], a[180:183], v[176:179]\n"
    "v_mfma_f32_16x16x32_bf16 v[144:147], a[212:215], a[176:179], v[144:147]\n"
    "v_mfma_f32_16x16x32_bf16 v[144:147], a[208:211], a[144:147], v[144:147]\n"
    "v_mfma_f32_16x16x32_bf16 v[112:115], a[208:211], a[140:143], v[112:115]\n"
    "v_mfma_f32_16x16x32_bf16 v[112:115], a[212:215], a[172:175], v[112:115]\n"
    "v_mfma_f32_16x16x32_bf16 v[80:83], a[212:215], a[168:171], v[80:83]\n"
    "v_mfma_f32_16x16x32_bf16 v[80:83], a[208:211], a[136:139], v[80:83]\n"
    "v_mfma_f32_16x16x32_bf16 v[48:51], a[208:211], a[132:135], v[48:51]\n"
    "v_mfma_f32_16x16x32_bf16 v[48:51], a[212:215], a[164:167], v[48:51]\n"
    "v_mfma_f32_16x16x32_bf16 v[16:19], a[212:215], a[160:163], v[16:19]\n"
    "v_mfma_f32_16x16x32_bf16 v[16:19], a[208:211], a[128:131], v[16:19]\n"
    "s_waitcnt vmcnt(8)\n"
    "v_mfma_f32_16x16x32_bf16 v[20:23], a[216:219], a[128:131], v[20:23]\n"
    "buffer_load_dwordx4 a[208:211], v240, s[20:23], s53 offen offset:0 sc1\n"
    "v_mfma_f32_16x16x32_bf16 v[20:23], a[220:223], a[160:163], v[20:23]\n"
    "buffer_load_dwordx4 a[212:215], v240, s[20:23], s53 offen offset:1024 sc1\n"
    "v_mfma_f32_16x16x32_bf16 v[52:55], a[220:223], a[164:167], v[52:55]\n"
    "v_mfma_f32_16x16x32_bf16 v[52:55], a[216:219], a[132:135], v[52:55]\n"
    "v_mfma_f32_16x16x32_bf16 v[84:87], a[216:219], a[136:139], v[84:87]\n"
    "v_mfma_f32_16x16x32_bf16 v[84:87], a[220:223], a[168:171], v[84:87]\n"
    "v_mfma_f32_16x16x32_bf16 v[116:119], a[220:223], a[172:175], v[116:119]\n"
    "v_mfma_f32_16x16x32_bf16 v[116:119], a[216:219], a[140:143], v[116:119]\n"
    "v_mfma_f32_16x16x32_bf16 v[148:151], a[216:219], a[144:147], v[148:151]\n"
    "v_mfma_f32_16x16x32_bf16 v[148:151], a[220:223], a[176:179], v[148:151]\n"
    "v_mfma_f32_16x16x32_bf16 v[180:183], a[220:223], a[180:183], v[180:183]\n"
    "v_mfma_f32_16x16x32_bf16 v[180:183], a[216:219], a[148:151], v[180:183]\n"
    "v_mfma_f32_16x16x32_bf16 v[212:215], a[216:219], a[152:155], v[212:215]\n"
    "v_mfma_f32_16x16x32_bf16 v[212:215], a[220:223], a[184:187], v[212:215]\n"
    "v_mfma_f32_16x16x32_bf16 a[244:247], a[220:223], a[188:191], a[244:247]\n"
    "v_mfma_f32_16x16x32_bf16 a[244:247], a[216:219], a[156:159], a[244:247]\n"
    "s_waitcnt vmcnt(8)\n"
    "v_mfma_f32_16x16x32_bf16 a[248:251], a[224:227], a[156:159], a[248:251]\n"
    "buffer_load_dwordx4 a[216:219], v240, s[20:23], s53 offen offset:2048 sc1\n"
    "v_mfma_f32_16x16x32_bf16 a[248:251], a[228:231], a[188:191], a[248:251]\n"
    "buffer_load_dwordx4 a[220:223], v240, s[20:23], s53 offen offset:3072 sc1\n"
    "v_mfma_f32_16x16x32_bf16 v[216:219], a[228:231], a[184:187], v[216:219]\n"
    "s_addk_i32 s53, 4096\n"
    "v_mfma_f32_16x16x32_bf16 v[216:219], a[224:227], a[152:155], v[216:219]\n"
    "v_mfma_f32_16x16x32_bf16 v[184:187], a[224:227], a[148:151], v[184:187]\n"
    "v_mfma_f32_16x16x32_bf16 v[184:187], a[228:231], a[180:183], v[184:187]\n"
    "v_mfma_f32_16x16x32_bf16 v[152:155], a[228:231], a[176:179], v[152:155]\n"
    "v_mfma_f32_16x16x32_bf16 v[152:155], a[224:227], a[144:147], v[152:155]\n"
    "v_mfma_f32_16x16x32_bf16 v[120:123], a[224:227], a[140:143], v[120:123]\n"
    "v_mfma_f32_16x16x32_bf16 v[120:123], a[228:231], a[172:175], v[120:123]\n"
    "v_mfma_f32_16x16x32_bf16 v[88:91], a[228:231], a[168:171], v[88:91]\n"
    "v_mfma_f32_16x16x32_bf16 v[88:91], a[224:227], a[136:139], v[88:91]\n"
    "v_mfma_f32_16x16x32_bf16 v[56:59], a[224:227], a[132:135], v[56:59]\n"
    "v_mfma_f32_16x16x32_bf16 v[56:59], a[228:231], a[164:167], v[56:59]\n"
    "v_mfma_f32_16x16x32_bf16 v[24:27], a[228:231], a[160:163], v[24:27]\n"
    "v_mfma_f32_16x16x32_bf16 v[24:27], a[224:227], a[128:131], v[24:27]\n"
    "s_waitcnt vmcnt(8)\n"
    "v_mfma_f32_16x16x32_bf16 v[28:31], a[232:235], a[128:131], v[28:31]\n"
    "buffer_load_dwordx4 a[224:227], v240, s[20:23], s53 offen offset:0 sc1\n"
    "v_mfma_f32_16x16x32_bf16 v[28:31], a[236:239], a[160:163], v[28:31]\n"
    "buffer_load_dwordx4 a[228:231], v240, s[20:23], s53 offen offset:1024 sc1\n"
    "v_mfma_f32_16x16x32_bf16 v[60:63], a[236:239], a[164:167], v[60:63]\n"
    "ds_read_b128 a[128:131], v241 offset:16384\n"
    "v_mfma_f32_16x16x32_bf16 v[60:63], a[232:235], a[132:135], v[60:63]\n"
    "ds_read_b128 a[160:163], v241 offset:17408\n"
    "v_mfma_f32_16x16x32_bf16 v[92:95], a[232:235], a[136:139], v[92:95]\n"
    "ds_read_b128 a[164:167], v241 offset:19456\n"
    "v_mfma_f32_16x16x32_bf16 v[92:95], a[236:239], a[168:171], v[92:95]\n"
    "ds_read_b128 a[132:135], v241 offset:18432\n"
    "v_mfma_f32_16x16x32_bf16 v[124:127], a[236:239], a[172:175], v[124:127]\n"
    "ds_read_b128 a[136:139], v241 offset:20480\n"
    "v_mfma_f32_16x16x32_bf16 v[124:127], a[232:235], a[140:143], v[124:127]\n"
    "ds_read_b128 a[168:171], v241 offset:21504\n"
    "v_mfma_f32_16x16x32_bf16 v[156:159], a[232:235], a[144:147], v[156:159]\n"
    "ds_read_b128 a[172:175], v241 offset:23552\n"
    "v_mfma_f32_16x16x32_bf16 v[156:159], a[236:239], a[176:179], v[156:159]\n"
    "ds_read_b128 a[140:143], v241 offset:22528\n"
    "v_mfma_f32_16x16x32_bf16 v[188:191], a[236:239], a[180:183], v[188:191]\n"
    "ds_read_b128 a[144:147], v241 offset:24576\n"
    "v_mfma_f32_16x16x32_bf16 v[188:191], a[232:235], a[148:151], v[188:191]\n"
    "ds_read_b128 a[176:179], v241 offset:25600\n"
    "v_mfma_f32_16x16x32_bf16 v[220:223], a[232:235], a[152:155], v[220:223]\n"
    "ds_read_b128 a[180:183], v241 offset:27648\n"
    "v_mfma_f32_16x16x32_bf16 v[220:223], a[236:239], a[184:187], v[220:223]\n"
    "ds_read_b128 a[148:151], v241 offset:26624\n"
    "v_mfma_f32_16x16x32_bf16 a[252:255], a[236:239], a[188:191], a[252:255]\n"
    "ds_read_b128 a[152:155], v241 offset:28672\n"
    "v_mfma_f32_16x16x32_bf16 a[252:255], a[232:235], a[156:159], a[252:255]\n"
    "s_waitcnt vmcnt(8) lgkmcnt(11)\n"
    "ds_read_b128 a[184:187], v241 offset:29696\n"
    "v_mfma_f32_16x16x32_bf16 v[0:3], a[192:195], a[128:131], v[0:3]\n"
    "buffer_load_dwordx4 a[232:235], v240, s[20:23], s53 offen offset:2048 sc1\n"
    "v_mfma_f32_16x16x32_bf16 v[0:3], a[196:199], a[160:163], v[0:3]\n"
    "s_waitcnt lgkmcnt(10)\n"
    "v_accvgpr_read_b32 v245, a0\n"
    "v_accvgpr_read_b32 v247, a1\n"
    "v_mfma_f32_16x16x32_bf16 v[32:35], a[196:199], a[164:167], v[32:35]\n"
    "buffer_load_dwordx4 a[236:239], v240, s[20:23], s53 offen offset:3072 sc1\n"
    "v_mfma_f32_16x16x32_bf16 v[32:35], a[192:195], a[132:135], v[32:35]\n"
    "s_waitcnt lgkmcnt(8)\n"
    "s_addk_i32 s53, 4096\n"
    "ds_read_b128 a[188:191], v241 offset:31744\n"
    "v_mfma_f32_16x16x32_bf16 v[64:67], a[192:195], a[136:139], v[64:67]\n"
    "ds_read_b128 a[156:159], v241 offset:30720\n"
    "v_lshlrev_b32 v244, 16, v245\n"
    "v_mfma_f32_16x16x32_bf16 v[64:67], a[196:199], a[168:171], v[64:67]\n"
    "s_waitcnt lgkmcnt(8)\n"
    "v_lshlrev_b32 v246, 16, v247\n"
    "v_and_b32 v245, s58, v245\n"
    "v_mfma_f32_16x16x32_bf16 v[96:99], a[196:199], a[172:175], v[96:99]\n"
    "v_and_b32 v247, s58, v247\n"
    "v_add_f32_dpp v254, v253, v0 row_newbcast:0 row_mask:0xf bank_mask:0xf\n"
    "v_mfma_f32_16x16x32_bf16 v[96:99], a[192:195], a[140:143], v[96:99]\n"
    "s_waitcnt lgkmcnt(6)\n"
    "v_add_f32_dpp v255, v253, v2 row_newbcast:2 row_mask:0xf bank_mask:0xf\n"
    "v_mul_f32 v244, v244, v254\n"
    "v_mfma_f32_16x16x32_bf16 v[128:131], a[192:195], a[144:147], v[128:131]\n"
    "v_mul_f32 v246, v246, v255\n"
    "v_add_f32_dpp v254, v253, v1 row_newbcast:1 row_mask:0xf bank_mask:0xf\n"
    "v_mfma_f32_16x16x32_bf16 v[128:131], a[196:199], a[176:179], v[128:131]\n"
    "s_waitcnt lgkmcnt(4)\n"
    "v_add_f32_dpp v255, v253, v3 row_newbcast:3 row_mask:0xf bank_mask:0xf\n"
    "v_mul_f32 v245, v245, v254\n"
    "v_mfma_f32_16x16x32_bf16 v[160:163], a[196:199], a[180:183], v[160:163]\n"
    "v_mul_f32 v247, v247, v255\n"
    "v_cvt_pk_bf16_f32 v248, v244, v245\n"
    "v_mfma_f32_16x16x32_bf16 v[160:163], a[192:195], a[148:151], v[160:163]\n"
    "s_waitcnt lgkmcnt(2)\n"
    "v_cvt_pk_bf16_f32 v249, v246, v247\n"
    "v_accvgpr_read_b32 v245, a2\n"
    "v_mfma_f32_16x16x32_bf16 v[192:195], a[192:195], a[152:155], v[192:195]\n"
    "v_accvgpr_read_b32 v247, a3\n"
    "v_lshlrev_b32 v244, 16, v245\n"
    "v_mfma_f32_16x16x32_bf16 v[192:195], a[196:199], a[184:187], v[192:195]\n"
    "s_waitcnt lgkmcnt(0)\n"
    "v_lshlrev_b32 v246, 16, v247\n"
    "v_and_b32 v245, s58, v245\n"
    "v_mfma_f32_16x16x32_bf16 v[224:227], a[196:199], a[188:191], v[224:227]\n"
    "v_and_b32 v247, s58, v247\n"
    "v_mfma_f32_16x16x32_bf16 v[224:227], a[192:195], a[156:159], v[224:227]\n"
    "s_waitcnt vmcnt(8)\n"
    "v_mfma_f32_16x16x32_bf16 v[228:231], a[200:203], a[156:159], v[228:231]\n"
    "buffer_load_dwordx4 a[192:195], v240, s[20:23], s53 offen offset:0 sc1\n"
    "v_mfma_f32_16x16x32_bf16 v[228:231], a[204:207], a[188:191], v[228:231]\n"
    "buffer_load_dwordx4 a[196:199], v240, s[20:23], s53 offen offset:1024 sc1\n"
    "v_mfma_f32_16x16x32_bf16 v[196:199], a[204:207], a[184:187], v[196:199]\n"
    "v_mfma_f32_16x16x32_bf16 v[196:199], a[200:203], a[152:155], v[196:199]\n"
    "v_mfma_f32_16x16x32_bf16 v[164:167], a[200:203], a[148:151], v[164:167]\n"
    "v_mfma_f32_16x16x32_bf16 v[164:167], a[204:207], a[180:183], v[164:167]\n"
    "v_mfma_f32_16x16x32_bf16 v[132:135], a[204:207], a[176:179], v[132:135]\n"
    "v_mfma_f32_16x16x32_bf16 v[132:135], a[200:203], a[144:147], v[132:135]\n"
    "v_mfma_f32_16x16x32_bf16 v[100:103], a[200:203], a[140:143], v[100:103]\n"
    "v_mfma_f32_16x16x32_bf16 v[100:103], a[204:207], a[172:175], v[100:103]\n"
    "v_mfma_f32_16x16x32_bf16 v[68:71], a[204:207], a[168:171], v[68:71]\n"
    "v_mfma_f32_16x16x32_bf16 v[68:71], a[200:203], a[136:139], v[68:71]\n"
    "v_mfma_f32_16x16x32_bf16 v[36:39], a[200:203], a[132:135], v[36:39]\n"
    "v_mfma_f32_16x16x32_bf16 v[36:39], a[204:207], a[164:167], v[36:39]\n"
    "v_mfma_f32_16x16x32_bf16 v[4:7], a[204:207], a[160:163], v[4:7]\n"
    "v_mfma_f32_16x16x32_bf16 v[4:7], a[200:203], a[128:131], v[4:7]\n"
    "s_waitcnt vmcnt(8)\n"
    "v_mfma_f32_16x16x32_bf16 v[8:11], a[208:211], a[128:131], v[8:11]\n"
    "buffer_load_dwordx4 a[200:203], v240, s[20:23], s53 offen offset:2048 sc1\n"
    "v_mfma_f32_16x16x32_bf16 v[8:11], a[212:215], a[160:163], v[8:11]\n"
    "buffer_load_dwordx4 a[204:207], v240, s[20:23], s53 offen offset:3072 sc1\n"
    "v_mfma_f32_16x16x32_bf16 v[40:43], a[212:215], a[164:167], v[40:43]\n"
    "s_addk_i32 s53, 4096\n"
    "s_xor_b32 s91, s54, 0x40000\n"
    "v_add_f32_dpp v254, v253, v4 row_newbcast:4 row_mask:0xf bank_mask:0xf\n"
    "v_mfma_f32_16x16x32_bf16 v[40:43], a[208:211], a[132:135], v[40:43]\n"
    "v_add_f32_dpp v255, v253, v6 row_newbcast:6 row_mask:0xf bank_mask:0xf\n"
    "v_mul_f32 v244, v244, v254\n"
    "v_mfma_f32_16x16x32_bf16 v[72:75], a[208:211], a[136:139], v[72:75]\n"
    "v_mul_f32 v246, v246, v255\n"
    "v_add_f32_dpp v254, v253, v5 row_newbcast:5 row_mask:0xf bank_mask:0xf\n"
    "v_mfma_f32_16x16x32_bf16 v[72:75], a[212:215], a[168:171], v[72:75]\n"
    "v_add_f32_dpp v255, v253, v7 row_newbcast:7 row_mask:0xf bank_mask:0xf\n"
    "v_mul_f32 v245, v245, v254\n"
    "v_mfma_f32_16x16x32_bf16 v[104:107], a[212:215], a[172:175], v[104:107]\n"
    "v_mul_f32 v247, v247, v255\n"
    "v_cvt_pk_bf16_f32 v250, v244, v245\n"
    "v_mfma_f32_16x16x32_bf16 v[104:107], a[208:211], a[140:143], v[104:107]\n"
    "v_cvt_pk_bf16_f32 v251, v246, v247\n"
    "v_accvgpr_read_b32 v245, a4\n"
    "v_mfma_f32_16x16x32_bf16 v[136:139], a[208:211], a[144:147], v[136:139]\n"
    "v_accvgpr_read_b32 v247, a5\n"
    "v_lshlrev_b32 v244, 16, v245\n"
    "v_mfma_f32_16x16x32_bf16 v[136:139], a[212:215], a[176:179], v[136:139]\n"
    "v_lshlrev_b32 v246, 16, v247\n"
    "v_and_b32 v245, s58, v245\n"
    "v_mfma_f32_16x16x32_bf16 v[168:171], a[212:215], a[180:183], v[168:171]\n"
    "v_and_b32 v247, s58, v247\n"
    "v_add_f32_dpp v254, v253, v32 row_newbcast:0 row_mask:0xf bank_mask:0xf\n"
    "v_mfma_f32_16x16x32_bf16 v[168:171], a[208:211], a[148:151], v[168:171]\n"
    "v_add_f32_dpp v255, v253, v34 row_newbcast:2 row_mask:0xf bank_mask:0xf\n"
    "v_mul_f32 v244, v244, v254\n"
    "v_mfma_f32_16x16x32_bf16 v[200:203], a[208:211], a[152:155], v[200:203]\n"
    "v_mul_f32 v246, v246, v255\n"
    "v_add_f32_dpp v254, v253, v33 row_newbcast:1 row_mask:0xf bank_mask:0xf\n"
    "v_mfma_f32_16x16x32_bf16 v[200:203], a[212:215], a[184:187], v[200:203]\n"
    "v_add_f32_dpp v255, v253, v35 row_newbcast:3 row_mask:0xf bank_mask:0xf\n"
    "v_mul_f32 v245, v245, v254\n"
    "v_mfma_f32_16x16x32_bf16 v[232:235], a[212:215], a[188:191], v[232:235]\n"
    "v_mul_f32 v247, v247, v255\n"
    "v_mfma_f32_16x16x32_bf16 v[232:235], a[208:211], a[156:159], v[232:235]\n"
    "s_waitcnt vmcnt(8)\n"
    "v_mfma_f32_16x16x32_bf16 v[236:239], a[216:219], a[156:159], v[236:239]\n"
    "buffer_load_dwordx4 a[208:211], v240, s[20:23], s53 offen offset:0 sc1\n"
    "v_mfma_f32_16x16x32_bf16 v[236:239], a[220:223], a[188:191], v[236:239]\n"
    "buffer_load_dwordx4 a[212:215], v240, s[20:23], s53 offen offset:1024 sc1\n"
    "v_mfma_f32_16x16x32_bf16 v[204:207], a[220:223], a[184:187], v[204:207]\n"
    "v_mfma_f32_16x16x32_bf16 v[204:207], a[216:219], a[152:155], v[204:207]\n"
    "buffer_store_dwordx4 v[248:251], v240, s[32:35], s91 offen sc1\n"
    "ds_write_b128 v243, v[248:251] offset:0\n"
    "v_mfma_f32_16x16x32_bf16 v[172:175], a[216:219], a[148:151], v[172:175]\n"
    "v_cvt_pk_bf16_f32 v248, v244, v245\n"
    "v_cvt_pk_bf16_f32 v249, v246, v247\n"
    "v_mfma_f32_16x16x32_bf16 v[172:175], a[220:223], a[180:183], v[172:175]\n"
    "v_accvgpr_read_b32 v245, a6\n"
    "v_accvgpr_read_b32 v247, a7\n"
    "v_mfma_f32_16x16x32_bf16 v[140:143], a[220:223], a[176:179], v[140:143]\n"
    "v_lshlrev_b32 v244, 16, v245\n"
    "v_lshlrev_b32 v246, 16, v247\n"
    "v_mfma_f32_16x16x32_bf16 v[140:143], a[216:219], a[144:147], v[140:143]\n"
    "v_and_b32 v245, s58, v245\n"
    "v_and_b32 v247, s58, v247\n"
    "v_mfma_f32_16x16x32_bf16 v[108:111], a[216:219], a[140:143], v[108:111]\n"
    "s_xor_b32 s91, s91, 0x800\n"
    "v_add_f32_dpp v254, v253, v36 row_newbcast:4 row_mask:0xf bank_mask:0xf\n"
    "v_add_f32_dpp v255, v253, v38 row_newbcast:6 row_mask:0xf bank_mask:0xf\n"
    "v_mfma_f32_16x16x32_bf16 v[108:111], a[220:223], a[172:175], v[108:111]\n"
    "v_mul_f32 v244, v244, v254\n"
    "v_mul_f32 v246, v246, v255\n"
    "v_mfma_f32_16x16x32_bf16 v[76:79], a[220:223], a[168:171], v[76:79]\n"
    "v_add_f32_dpp v254, v253, v37 row_newbcast:5 row_mask:0xf bank_mask:0xf\n"
    "v_add_f32_dpp v255, v253, v39 row_newbcast:7 row_mask:0xf bank_mask:0xf\n"
    "v_mfma_f32_16x16x32_bf16 v[76:79], a[216:219], a[136:139], v[76:79]\n"
    "v_mul_f32 v245, v245, v254\n"
    "v_mul_f32 v247, v247, v255\n"
    "v_mfma_f32_16x16x32_bf16 v[44:47], a[216:219], a[132:135], v[44:47]\n"
    "v_cvt_pk_bf16_f32 v250, v244, v245\n"
    "v_cvt_pk_bf16_f32 v251, v246, v247\n"
    "v_mfma_f32_16x16x32_bf16 v[44:47], a[220:223], a[164:167], v[44:47]\n"
    "v_accvgpr_read_b32 v245, a8\n"
    "v_accvgpr_read_b32 v247, a9\n"
    "v_mfma_f32_16x16x32_bf16 v[12:15], a[220:223], a[160:163], v[12:15]\n"
    "v_lshlrev_b32 v244, 16, v245\n"
    "v_lshlrev_b32 v246, 16, v247\n"
    "v_mfma_f32_16x16x32_bf16 v[12:15], a[216:219], a[128:131], v[12:15]\n"
    "s_waitcnt vmcnt(9)\n"
    "v_and_b32 v245, s58, v245\n"
    "v_and_b32 v247, s58, v247\n"
    "v_mfma_f32_16x16x32_bf16 v[16:19], a[224:227], a[128:131], v[16:19]\n"
    "buffer_load_dwordx4 a[216:219], v240, s[20:23], s53 offen offset:2048 sc1\n"
    "v_mfma_f32_16x16x32_bf16 v[16:19], a[228:231], a[160:163], v[16:19]\n"
    "buffer_load_dwordx4 a[220:223], v240, s[20:23], s53 offen offset:3072 sc1\n"
    "v_mfma_f32_16x16x32_bf16 v[48:51], a[228:231], a[164:167], v[48:51]\n"
    "s_addk_i32 s53, 4096\n"
    "v_add_f32_dpp v254, v253, v64 row_newbcast:0 row_mask:0xf bank_mask:0xf\n"
    "v_add_f32_dpp v255, v253, v66 row_newbcast:2 row_mask:0xf bank_mask:0xf\n"
    "v_mfma_f32_16x16x32_bf16 v[48:51], a[224:227], a[132:135], v[48:51]\n"
    "v_mul_f32 v244, v244, v254\n"
    "v_mul_f32 v246, v246, v255\n"
    "v_mfma_f32_16x16x32_bf16 v[80:83], a[224:227], a[136:139], v[80:83]\n"
    "v_add_f32_dpp v254, v253, v65 row_newbcast:1 row_mask:0xf bank_mask:0xf\n"
    "v_add_f32_dpp v255, v253, v67 row_newbcast:3 row_mask:0xf bank_mask:0xf\n"
    "v_mfma_f32_16x16x32_bf16 v[80:83], a[228:231], a[168:171], v[80:83]\n"
    "v_mul_f32 v245, v245, v254\n"
    "v_mul_f32 v247, v247, v255\n"
    "v_mfma_f32_16x16x32_bf16 v[112:115], a[228:231], a[172:175], v[112:115]\n"
    "v_mfma_f32_16x16x32_bf16 v[112:115], a[224:227], a[140:143], v[112:115]\n"
    "buffer_store_dwordx4 v[248:251], v240, s[32:35], s91 offen sc1\n"
    "ds_write_b128 v243, v[248:251] offset:2048\n"
    "v_mfma_f32_16x16x32_bf16 v[144:147], a[224:227], a[144:147], v[144:147]\n"
    "v_cvt_pk_bf16_f32 v248, v244, v245\n"
    "v_cvt_pk_bf16_f32 v249, v246, v247\n"
    "v_mfma_f32_16x16x32_bf16 v[144:147], a[228:231], a[176:179], v[144:147]\n"
    "v_accvgpr_read_b32 v245, a10\n"
    "v_accvgpr_read_b32 v247, a11\n"
    "v_mfma_f32_16x16x32_bf16 v[176:179], a[228:231], a[180:183], v[176:179]\n"
    "v_lshlrev_b32 v244, 16, v245\n"
    "v_lshlrev_b32 v246, 16, v247\n"
    "v_mfma_f32_16x16x32_bf16 v[176:179], a[224:227], a[148:151], v[176:179]\n"
    "v_and_b32 v245, s58, v245\n"
    "v_and_b32 v247, s58, v247\n"
    "v_mfma_f32_16x16x32_bf16 v[208:211], a[224:227], a[152:155], v[208:211]\n"
    "s_xor_b32 s91, s91, 0x101800\n"
    "v_add_f32_dpp v254, v253, v68 row_newbcast:4 row_mask:0xf bank_mask:0xf\n"
    "v_add_f32_dpp v255, v253, v70 row_newbcast:6 row_mask:0xf bank_mask:0xf\n"
    "v_mfma_f32_16x16x32_bf16 v[208:211], a[228:231], a[184:187], v[208:211]\n"
    "v_mul_f32 v244, v244, v254\n"
    "v_mul_f32 v246, v246, v255\n"
    "v_mfma_f32_16x16x32_bf16 a[240:243], a[228:231], a[188:191], a[240:243]\n"
    "v_add_f32_dpp v254, v253, v69 row_newbcast:5 row_mask:0xf bank_mask:0xf\n"
    "v_add_f32_dpp v255, v253, v71 row_newbcast:7 row_mask:0xf bank_mask:0xf\n"
    "v_mfma_f32_16x16x32_bf16 a[240:243], a[224:227], a[156:159], a[240:243]\n"
    "s_waitcnt vmcnt(10)\n"
    "v_mul_f32 v245, v245, v254\n"
    "v_mul_f32 v247, v247, v255\n"
    "v_mfma_f32_16x16x32_bf16 a[244:247], a[232:235], a[156:159], a[244:247]\n"
    "buffer_load_dwordx4 a[224:227], v240, s[20:23], s53 offen offset:0 sc1\n"
    "v_mfma_f32_16x16x32_bf16 a[244:247], a[236:239], a[188:191], a[244:247]\n"
    "buffer_load_dwordx4 a[228:231], v240, s[20:23], s53 offen offset:1024 sc1\n"
    "v_mfma_f32_16x16x32_bf16 v[212:215], a[236:239], a[184:187], v[212:215]\n"
    "v_cvt_pk_bf16_f32 v250, v244, v245\n"
    "v_cvt_pk_bf16_f32 v251, v246, v247\n"
    "v_mfma_f32_16x16x32_bf16 v[212:215], a[232:235], a[152:155], v[212:215]\n"
    "v_accvgpr_read_b32 v245, a12\n"
    "v_accvgpr_read_b32 v247, a13\n"
    "v_mfma_f32_16x16x32_bf16 v[180:183], a[232:235], a[148:151], v[180:183]\n"
    "v_lshlrev_b32 v244, 16, v245\n"
    "v_lshlrev_b32 v246, 16, v247\n"
    "v_mfma_f32_16x16x32_bf16 v[180:183], a[236:239], a[180:183], v[180:183]\n"
    "v_and_b32 v245, s58, v245\n"
    "v_and_b32 v247, s58, v247\n"
    "v_mfma_f32_16x16x32_bf16 v[148:151], a[236:239], a[176:179], v[148:151]\n"
    "v_add_f32_dpp v254, v253, v96 row_newbcast:0 row_mask:0xf bank_mask:0xf\n"
    "v_add_f32_dpp v255, v253, v98 row_newbcast:2 row_mask:0xf bank_mask:0xf\n"
    "v_mfma_f32_16x16x32_bf16 v[148:151], a[232:235], a[144:147], v[148:151]\n"
    "v_mul_f32 v244, v244, v254\n"
    "v_mul_f32 v246, v246, v255\n"
    "v_mfma_f32_16x16x32_bf16 v[116:119], a[232:235], a[140:143], v[116:119]\n"
    "v_add_f32_dpp v254, v253, v97 row_newbcast:1 row_mask:0xf bank_mask:0xf\n"
    "v_add_f32_dpp v255, v253, v99 row_newbcast:3 row_mask:0xf bank_mask:0xf\n"
    "v_mfma_f32_16x16x32_bf16 v[116:119], a[236:239], a[172:175], v[116:119]\n"
    "v_mul_f32 v245, v245, v254\n"
    "v_mul_f32 v247, v247, v255\n"
    "v_mfma_f32_16x16x32_bf16 v[84:87], a[236:239], a[168:171], v[84:87]\n"
    "v_mfma_f32_16x16x32_bf16 v[84:87], a[232:235], a[136:139], v[84:87]\n"
    "buffer_store_dwordx4 v[248:251], v240, s[32:35], s91 offen sc1\n"
    "ds_write_b128 v243, v[248:251] offset:4096\n"
    "v_mfma_f32_16x16x32_bf16 v[52:55], a[232:235], a[132:135], v[52:55]\n"
    "v_cvt_pk_bf16_f32 v248, v244, v245\n"
    "v_cvt_pk_bf16_f32 v249, v246, v247\n"
    "v_mfma_f32_16x16x32_bf16 v[52:55], a[236:239], a[164:167], v[52:55]\n"
    "v_accvgpr_read_b32 v245, a14\n"
    "v_accvgpr_read_b32 v247, a15\n"
    "v_mfma_f32_16x16x32_bf16 v[20:23], a[236:239], a[160:163], v[20:23]\n"
    "v_lshlrev_b32 v244, 16, v245\n"
    "v_lshlrev_b32 v246, 16, v247\n"
    "v_mfma_f32_16x16x32_bf16 v[20:23], a[232:235], a[128:131], v[20:23]\n"
    "s_waitcnt vmcnt(11)\n"
    "v_and_b32 v245, s58, v245\n"
    "v_and_b32 v247, s58, v247\n"
    "v_mfma_f32_16x16x32_bf16 v[24:27], a[192:195], a[128:131], v[24:27]\n"
    "buffer_load_dwordx4 a[232:235], v240, s[20:23], s53 offen offset:2048 sc1\n"
    "v_mfma_f32_16x16x32_bf16 v[24:27], a[196:199], a[160:163], v[24:27]\n"
    "buffer_load_dwordx4 a[236:239], v240, s[20:23], s53 offen offset:3072 sc1\n"
    "v_mfma_f32_16x16x32_bf16 v[56:59], a[196:199], a[164:167], v[56:59]\n"
    "s_addk_i32 s53, 4096\n"
    "s_xor_b32 s91, s91, 0x800\n"
    "v_add_f32_dpp v254, v253, v100 row_newbcast:4 row_mask:0xf bank_mask:0xf\n"
    "v_mfma_f32_16x16x32_bf16 v[56:59], a[192:195], a[132:135], v[56:59]\n"
    "v_add_f32_dpp v255, v253, v102 row_newbcast:6 row_mask:0xf bank_mask:0xf\n"
    "v_mul_f32 v244, v244, v254\n"
    "v_mfma_f32_16x16x32_bf16 v[88:91], a[192:195], a[136:139], v[88:91]\n"
    "v_mul_f32 v246, v246, v255\n"
    "v_add_f32_dpp v254, v253, v101 row_newbcast:5 row_mask:0xf bank_mask:0xf\n"
    "v_mfma_f32_16x16x32_bf16 v[88:91], a[196:199], a[168:171], v[88:91]\n"
    "v_add_f32_dpp v255, v253, v103 row_newbcast:7 row_mask:0xf bank_mask:0xf\n"
    "v_mul_f32 v245, v245, v254\n"
    "v_mfma_f32_16x16x32_bf16 v[120:123], a[196:199], a[172:175], v[120:123]\n"
    "v_mul_f32 v247, v247, v255\n"
    "v_cvt_pk_bf16_f32 v250, v244, v245\n"
    "v_mfma_f32_16x16x32_bf16 v[120:123], a[192:195], a[140:143], v[120:123]\n"
    "v_cvt_pk_bf16_f32 v251, v246, v247\n"
    "v_accvgpr_read_b32 v245, a16\n"
    "v_mfma_f32_16x16x32_bf16 v[152:155], a[192:195], a[144:147], v[152:155]\n"
    "v_accvgpr_read_b32 v247, a17\n"
    "v_lshlrev_b32 v244, 16, v245\n"
    "v_mfma_f32_16x16x32_bf16 v[152:155], a[196:199], a[176:179], v[152:155]\n"
    "v_lshlrev_b32 v246, 16, v247\n"
    "v_and_b32 v245, s58, v245\n"
    "v_mfma_f32_16x16x32_bf16 v[184:187], a[196:199], a[180:183], v[184:187]\n"
    "v_and_b32 v247, s58, v247\n"
    "v_add_f32_dpp v254, v253, v128 row_newbcast:0 row_mask:0xf bank_mask:0xf\n"
    "v_mfma_f32_16x16x32_bf16 v[184:187], a[192:195], a[148:151], v[184:187]\n"
    "v_add_f32_dpp v255, v253, v130 row_newbcast:2 row_mask:0xf bank_mask:0xf\n"
    "v_mul_f32 v244, v244, v254\n"
    "v_mfma_f32_16x16x32_bf16 v[216:219], a[192:195], a[152:155], v[216:219]\n"
    "v_mul_f32 v246, v246, v255\n"
    "v_add_f32_dpp v254, v253, v129 row_newbcast:1 row_mask:0xf bank_mask:0xf\n"
    "v_mfma_f32_16x16x32_bf16 v[216:219], a[196:199], a[184:187], v[216:219]\n"
    "v_add_f32_dpp v255, v253, v131 row_newbcast:3 row_mask:0xf bank_mask:0xf\n"
    "v_mul_f32 v245, v245, v254\n"
    "v_mfma_f32_16x16x32_bf16 a[248:251], a[196:199], a[188:191], a[248:251]\n"
    "v_mul_f32 v247, v247, v255\n"
    "v_mfma_f32_16x16x32_bf16 a[248:251], a[192:195], a[156:159], a[248:251]\n"
    "s_waitcnt vmcnt(11)\n"
    "v_mfma_f32_16x16x32_bf16 a[252:255], a[200:203], a[156:159], a[252:255]\n"
    "buffer_load_dwordx4 a[192:195], v240, s[20:23], s53 offen offset:0 sc1\n"
    "v_mfma_f32_16x16x32_bf16 a[252:255], a[204:207], a[188:191], a[252:255]\n"
    "buffer_load_dwordx4 a[196:199], v240, s[20:23], s53 offen offset:1024 sc1\n"
    "v_mfma_f32_16x16x32_bf16 v[220:223], a[204:207], a[184:187], v[220:223]\n"
    "v_add_u32 v241, s57, v240\n"
    "ds_read_b128 a[156:159], v241 offset:14336\n"
    "v_mfma_f32_16x16x32_bf16 v[220:223], a[200:203], a[152:155], v[220:223]\n"
    "buffer_store_dwordx4 v[248:251], v240, s[32:35], s91 offen sc1\n"
    "ds_write_b128 v243, v[248:251] offset:6144\n"
    "v_mfma_f32_16x16x32_bf16 v[188:191], a[200:203], a[148:151], v[188:191]\n"
    "ds_read_b128 a[188:191], v241 offset:15360\n"
    "ds_read_b128 a[184:187], v241 offset:13312\n"
    "v_mfma_f32_16x16x32_bf16 v[188:191], a[204:207], a[180:183], v[188:191]\n"
    "ds_read_b128 a[152:155], v241 offset:12288\n"
    "v_cvt_pk_bf16_f32 v248, v244, v245\n"
    "v_mfma_f32_16x16x32_bf16 v[156:159], a[204:207], a[176:179], v[156:159]\n"
    "ds_read_b128 a[148:151], v241 offset:10240\n"
    "v_cvt_pk_bf16_f32 v249, v246, v247\n"
    "v_mfma_f32_16x16x32_bf16 v[156:159], a[200:203], a[144:147], v[156:159]\n"
    "ds_read_b128 a[180:183], v241 offset:11264\n"
    "v_accvgpr_read_b32 v245, a18\n"
    "v_mfma_f32_16x16x32_bf16 v[124:127], a[200:203], a[140:143], v[124:127]\n"
    "ds_read_b128 a[176:179], v241 offset:9216\n"
    "v_accvgpr_read_b32 v247, a19\n"
    "v_mfma_f32_16x16x32_bf16 v[124:127], a[204:207], a[172:175], v[124:127]\n"
    "ds_read_b128 a[144:147], v241 offset:8192\n"
    "v_lshlrev_b32 v244, 16, v245\n"
    "v_mfma_f32_16x16x32_bf16 v[92:95], a[204:207], a[168:171], v[92:95]\n"
    "ds_read_b128 a[140:143], v241 offset:6144\n"
    "v_lshlrev_b32 v246, 16, v247\n"
    "v_mfma_f32_16x16x32_bf16 v[92:95], a[200:203], a[136:139], v[92:95]\n"
    "ds_read_b128 a[172:175], v241 offset:7168\n"
    "v_and_b32 v245, s58, v245\n"
    "v_mfma_f32_16x16x32_bf16 v[60:63], a[200:203], a[132:135], v[60:63]\n"
    "ds_read_b128 a[168:171], v241 offset:5120\n"
    "v_and_b32 v247, s58, v247\n"
    "v_mfma_f32_16x16x32_bf16 v[60:63], a[204:207], a[164:167], v[60:63]\n"
    "ds_read_b128 a[136:139], v241 offset:4096\n"
    "v_add_f32_dpp v254, v253, v132 row_newbcast:4 row_mask:0xf bank_mask:0xf\n"
    "v_mfma_f32_16x16x32_bf16 v[28:31], a[204:207], a[160:163], v[28:31]\n"
    "ds_read_b128 a[132:135], v241 offset:2048\n"
    "s_xor_b32 s91, s91, 0x185800\n"
    "v_mfma_f32_16x16x32_bf16 v[28:31], a[200:203], a[128:131], v[28:31]\n"
    "s_waitcnt vmcnt(12) lgkmcnt(11)\n"
    "ds_read_b128 a[164:167], v241 offset:3072\n"
    "v_add_f32_dpp v255, v253, v134 row_newbcast:6 row_mask:0xf bank_mask:0xf\n"
    "v_mfma_f32_16x16x32_bf16 a[240:243], a[208:211], a[156:159], a[240:243]\n"
    "buffer_load_dwordx4 a[200:203], v240, s[20:23], s53 offen offset:2048 sc1\n"
    "v_mfma_f32_16x16x32_bf16 a[240:243], a[212:215], a[188:191], a[240:243]\n"
    "s_waitcnt lgkmcnt(10)\n"
    "v_mul_f32 v244, v244, v254\n"
    "v_mul_f32 v246, v246, v255\n"
    "v_mfma_f32_16x16x32_bf16 v[208:211], a[212:215], a[184:187], v[208:211]\n"
    "buffer_load_dwordx4 a[204:207], v240, s[20:23], s53 offen offset:3072 sc1\n"
    "v_mfma_f32_16x16x32_bf16 v[208:211], a[208:211], a[152:155], v[208:211]\n"
    "s_waitcnt lgkmcnt(8)\n"
    "s_addk_i32 s53, 4096\n"
    "ds_read_b128 a[160:163], v241 offset:1024\n"
    "v_mfma_f32_16x16x32_bf16 v[176:179], a[208:211], a[148:151], v[176:179]\n"
    "ds_read_b128 a[128:131], v241 offset:0\n"
    "v_add_f32_dpp v254, v253, v133 row_newbcast:5 row_mask:0xf bank_mask:0xf\n"
    "v_mfma_f32_16x16x32_bf16 v[176:179], a[212:215], a[180:183], v[176:179]\n"
    "s_waitcnt lgkmcnt(8)\n"
    "v_add_f32_dpp v255, v253, v135 row_newbcast:7 row_mask:0xf bank_mask:0xf\n"
    "v_mul_f32 v245, v245, v254\n"
    "v_mfma_f32_16x16x32_bf16 v[144:147], a[212:215], a[176:179], v[144:147]\n"
    "v_mul_f32 v247, v247, v255\n"
    "v_cvt_pk_bf16_f32 v250, v244, v245\n"
    "v_mfma_f32_16x16x32_bf16 v[144:147], a[208:211], a[144:147], v[144:147]\n"
    "s_waitcnt lgkmcnt(6)\n"
    "v_cvt_pk_bf16_f32 v251, v246, v247\n"
    "v_accvgpr_read_b32 v245, a20\n"
    "v_mfma_f32_16x16x32_bf16 v[112:115], a[208:211], a[140:143], v[112:115]\n"
    "v_accvgpr_read_b32 v247, a21\n"
    "v_lshlrev_b32 v244, 16, v245\n"
    "v_mfma_f32_16x16x32_bf16 v[112:115], a[212:215], a[172:175], v[112:115]\n"
    "s_waitcnt lgkmcnt(4)\n"
    "v_lshlrev_b32 v246, 16, v247\n"
    "v_and_b32 v245, s58, v245\n"
    "v_mfma_f32_16x16x32_bf16 v[80:83], a[212:215], a[168:171], v[80:83]\n"
    "v_and_b32 v247, s58, v247\n"
    "v_add_f32_dpp v254, v253, v160 row_newbcast:0 row_mask:0xf bank_mask:0xf\n"
    "v_mfma_f32_16x16x32_bf16 v[80:83], a[208:211], a[136:139], v[80:83]\n"
    "s_waitcnt lgkmcnt(2)\n"
    "v_add_f32_dpp v255, v253, v162 row_newbcast:2 row_mask:0xf bank_mask:0xf\n"
    "v_mul_f32 v244, v244, v254\n"
    "v_mfma_f32_16x16x32_bf16 v[48:51], a[208:211], a[132:135], v[48:51]\n"
    "v_mul_f32 v246, v246, v255\n"
    "v_add_f32_dpp v254, v253, v161 row_newbcast:1 row_mask:0xf bank_mask:0xf\n"
    "v_mfma_f32_16x16x32_bf16 v[48:51], a[212:215], a[164:167], v[48:51]\n"
    "s_waitcnt lgkmcnt(0)\n"
    "v_add_f32_dpp v255, v253, v163 row_newbcast:3 row_mask:0xf bank_mask:0xf\n"
    "v_mul_f32 v245, v245, v254\n"
    "v_mfma_f32_16x16x32_bf16 v[16:19], a[212:215], a[160:163], v[16:19]\n"
    "v_mul_f32 v247, v247, v255\n"
    "v_mfma_f32_16x16x32_bf16 v[16:19], a[208:211], a[128:131], v[16:19]\n"
    "s_waitcnt vmcnt(11)\n"
    "v_mfma_f32_16x16x32_bf16 v[20:23], a[216:219], a[128:131], v[20:23]\n"
    "buffer_load_dwordx4 a[208:211], v240, s[20:23], s53 offen offset:0 sc1\n"
    "v_mfma_f32_16x16x32_bf16 v[20:23], a[220:223], a[160:163], v[20:23]\n"
    "buffer_load_dwordx4 a[212:215], v240, s[20:23], s53 offen offset:1024 sc1\n"
    "v_mfma_f32_16x16x32_bf16 v[52:55], a[220:223], a[164:167], v[52:55]\n"
    "v_mfma_f32_16x16x32_bf16 v[52:55], a[216:219], a[132:135], v[52:55]\n"
    "buffer_store_dwordx4 v[248:251], v240, s[32:35], s91 offen sc1\n"
    "ds_write_b128 v243, v[248:251] offset:8192\n"
    "v_mfma_f32_16x16x32_bf16 v[84:87], a[216:219], a[136:139], v[84:87]\n"
    "v_cvt_pk_bf16_f32 v248, v244, v245\n"
    "v_cvt_pk_bf16_f32 v249, v246, v247\n"
    "v_mfma_f32_16x16x32_bf16 v[84:87], a[220:223], a[168:171], v[84:87]\n"
    "v_accvgpr_read_b32 v245, a22\n"
    "v_accvgpr_read_b32 v247, a23\n"
    "v_mfma_f32_16x16x32_bf16 v[116:119], a[220:223], a[172:175], v[116:119]\n"
    "v_lshlrev_b32 v244, 16, v245\n"
    "v_lshlrev_b32 v246, 16, v247\n"
    "v_mfma_f32_16x16x32_bf16 v[116:119], a[216:219], a[140:143], v[116:119]\n"
    "v_and_b32 v245, s58, v245\n"
    "v_and_b32 v247, s58, v247\n"
    "v_mfma_f32_16x16x32_bf16 v[148:151], a[216:219], a[144:147], v[148:151]\n"
    "s_xor_b32 s91, s91, 0x800\n"
    "v_add_f32_dpp v254, v253, v164 row_newbcast:4 row_mask:0xf bank_mask:0xf\n"
    "v_add_f32_dpp v255, v253, v166 row_newbcast:6 row_mask:0xf bank_mask:0xf\n"
    "v_mfma_f32_16x16x32_bf16 v[148:151], a[220:223], a[176:179], v[148:151]\n"
    "v_mul_f32 v244, v244, v254\n"
    "v_mul_f32 v246, v246, v255\n"
    "v_mfma_f32_16x16x32_bf16 v[180:183], a[220:223], a[180:183], v[180:183]\n"
    "v_add_f32_dpp v254, v253, v165 row_newbcast:5 row_mask:0xf bank_mask:0xf\n"
    "v_add_f32_dpp v255, v253, v167 row_newbcast:7 row_mask:0xf bank_mask:0xf\n"
    "v_mfma_f32_16x16x32_bf16 v[180:183], a[216:219], a[148:151], v[180:183]\n"
    "v_mul_f32 v245, v245, v254\n"
    "v_mul_f32 v247, v247, v255\n"
    "v_mfma_f32_16x16x32_bf16 v[212:215], a[216:219], a[152:155], v[212:215]\n"
    "v_cvt_pk_bf16_f32 v250, v244, v245\n"
    "v_cvt_pk_bf16_f32 v251, v246, v247\n"
    "v_mfma_f32_16x16x32_bf16 v[212:215], a[220:223], a[184:187], v[212:215]\n"
    "v_accvgpr_read_b32 v245, a24\n"
    "v_accvgpr_read_b32 v247, a25\n"
    "v_mfma_f32_16x16x32_bf16 a[244:247], a[220:223], a[188:191], a[244:247]\n"
    "v_lshlrev_b32 v244, 16, v245\n"
    "v_lshlrev_b32 v246, 16, v247\n"
    "v_mfma_f32_16x16x32_bf16 a[244:247], a[216:219], a[156:159], a[244:247]\n"
    "s_waitcnt vmcnt(11)\n"
    "v_and_b32 v245, s58, v245\n"
    "v_and_b32 v247, s58, v247\n"
    "v_mfma_f32_16x16x32_bf16 a[248:251], a[224:227], a[156:159], a[248:251]\n"
    "buffer_load_dwordx4 a[216:219], v240, s[20:23], s53 offen offset:2048 sc1\n"
    "v_mfma_f32_16x16x32_bf16 a[248:251], a[228:231], a[188:191], a[248:251]\n"
    "buffer_load_dwordx4 a[220:223], v240, s[20:23], s53 offen offset:3072 sc1\n"
    "v_mfma_f32_16x16x32_bf16 v[216:219], a[228:231], a[184:187], v[216:219]\n"
    "s_addk_i32 s53, 4096\n"
    "v_add_f32_dpp v254, v253, v192 row_newbcast:0 row_mask:0xf bank_mask:0xf\n"
    "v_add_f32_dpp v255, v253, v194 row_newbcast:2 row_mask:0xf bank_mask:0xf\n"
    "v_mfma_f32_16x16x32_bf16 v[216:219], a[224:227], a[152:155], v[216:219]\n"
    "v_mul_f32 v244, v244, v254\n"
    "v_mul_f32 v246, v246, v255\n"
    "v_mfma_f32_16x16x32_bf16 v[184:187], a[224:227], a[148:151], v[184:187]\n"
    "v_add_f32_dpp v254, v253, v193 row_newbcast:1 row_mask:0xf bank_mask:0xf\n"
    "v_add_f32_dpp v255, v253, v195 row_newbcast:3 row_mask:0xf bank_mask:0xf\n"
    "v_mfma_f32_16x16x32_bf16 v[184:187], a[228:231], a[180:183], v[184:187]\n"
    "v_mul_f32 v245, v245, v254\n"
    "v_mul_f32 v247, v247, v255\n"
    "v_mfma_f32_16x16x32_bf16 v[152:155], a[228:231], a[176:179], v[152:155]\n"
    "buffer_store_dwordx4 v[248:251], v240, s[32:35], s91 offen sc1\n"
    "ds_write_b128 v243, v[248:251] offset:10240\n"
    "v_mfma_f32_16x16x32_bf16 v[152:155], a[224:227], a[144:147], v[152:155]\n"
    "v_cvt_pk_bf16_f32 v248, v244, v245\n"
    "v_cvt_pk_bf16_f32 v249, v246, v247\n"
    "v_mfma_f32_16x16x32_bf16 v[120:123], a[224:227], a[140:143], v[120:123]\n"
    "v_accvgpr_read_b32 v245, a26\n"
    "v_accvgpr_read_b32 v247, a27\n"
    "v_mfma_f32_16x16x32_bf16 v[120:123], a[228:231], a[172:175], v[120:123]\n"
    "v_lshlrev_b32 v244, 16, v245\n"
    "v_lshlrev_b32 v246, 16, v247\n"
    "v_mfma_f32_16x16x32_bf16 v[88:91], a[228:231], a[168:171], v[88:91]\n"
    "s_xor_b32 s91, s91, 0x101800\n"
    "v_and_b32 v245, s58, v245\n"
    "v_and_b32 v247, s58, v247\n"
    "v_mfma_f32_16x16x32_bf16 v[88:91], a[224:227], a[136:139], v[88:91]\n"
    "v_add_f32_dpp v254, v253, v196 row_newbcast:4 row_mask:0xf bank_mask:0xf\n"
    "v_add_f32_dpp v255, v253, v198 row_newbcast:6 row_mask:0xf bank_mask:0xf\n"
    "v_mfma_f32_16x16x32_bf16 v[56:59], a[224:227], a[132:135], v[56:59]\n"
    "v_mul_f32 v244, v244, v254\n"
    "v_mul_f32 v246, v246, v255\n"
    "v_mfma_f32_16x16x32_bf16 v[56:59], a[228:231], a[164:167], v[56:59]\n"
    "v_add_f32_dpp v254, v253, v197 row_newbcast:5 row_mask:0xf bank_mask:0xf\n"
    "v_add_f32_dpp v255, v253, v199 row_newbcast:7 row_mask:0xf bank_mask:0xf\n"
    "v_mfma_f32_16x16x32_bf16 v[24:27], a[228:231], a[160:163], v[24:27]\n"
    "v_mul_f32 v245, v245, v254\n"
    "v_mul_f32 v247, v247, v255\n"
    "v_mfma_f32_16x16x32_bf16 v[24:27], a[224:227], a[128:131], v[24:27]\n"
    "s_waitcnt vmcnt(11)\n"
    "v_cvt_pk_bf16_f32 v250, v244, v245\n"
    "v_cvt_pk_bf16_f32 v251, v246, v247\n"
    "v_mfma_f32_16x16x32_bf16 v[28:31], a[232:235], a[128:131], v[28:31]\n"
    "buffer_load_dwordx4 a[224:227], v240, s[20:23], s53 offen offset:0 sc1\n"
    "v_mfma_f32_16x16x32_bf16 v[28:31], a[236:239], a[160:163], v[28:31]\n"
    "buffer_load_dwordx4 a[228:231], v240, s[20:23], s53 offen offset:1024 sc1\n"
    "v_mfma_f32_16x16x32_bf16 v[60:63], a[236:239], a[164:167], v[60:63]\n"
    "ds_read_b128 a[128:131], v241 offset:16384\n"
    "v_accvgpr_read_b32 v245, a28\n"
    "v_mfma_f32_16x16x32_bf16 v[60:63], a[232:235], a[132:135], v[60:63]\n"
    "ds_read_b128 a[160:163], v241 offset:17408\n"
    "v_accvgpr_read_b32 v247, a29\n"
    "v_mfma_f32_16x16x32_bf16 v[92:95], a[232:235], a[136:139], v[92:95]\n"
    "ds_read_b128 a[164:167], v241 offset:19456\n"
    "v_lshlrev_b32 v244, 16, v245\n"
    "v_mfma_f32_16x16x32_bf16 v[92:95], a[236:239], a[168:171], v[92:95]\n"
    "ds_read_b128 a[132:135], v241 offset:18432\n"
    "v_lshlrev_b32 v246, 16, v247\n"
    "v_mfma_f32_16x16x32_bf16 v[124:127], a[236:239], a[172:175], v[124:127]\n"
    "ds_read_b128 a[136:139], v241 offset:20480\n"
    "v_and_b32 v245, s58, v245\n"
    "v_mfma_f32_16x16x32_bf16 v[124:127], a[232:235], a[140:143], v[124:127]\n"
    "ds_read_b128 a[168:171], v241 offset:21504\n"
    "v_and_b32 v247, s58, v247\n"
    "v_mfma_f32_16x16x32_bf16 v[156:159], a[232:235], a[144:147], v[156:159]\n"
    "ds_read_b128 a[172:175], v241 offset:23552\n"
    "v_add_f32_dpp v254, v253, v224 row_newbcast:0 row_mask:0xf bank_mask:0xf\n"
    "v_mfma_f32_16x16x32_bf16 v[156:159], a[236:239], a[176:179], v[156:159]\n"
    "ds_read_b128 a[140:143], v241 offset:22528\n"
    "v_add_f32_dpp v255, v253, v226 row_newbcast:2 row_mask:0xf bank_mask:0xf\n"
    "v_mfma_f32_16x16x32_bf16 v[188:191], a[236:239], a[180:183], v[188:191]\n"
    "ds_read_b128 a[144:147], v241 offset:24576\n"
    "v_mul_f32 v244, v244, v254\n"
    "v_mfma_f32_16x16x32_bf16 v[188:191], a[232:235], a[148:151], v[188:191]\n"
    "ds_read_b128 a[176:179], v241 offset:25600\n"
    "v_mul_f32 v246, v246, v255\n"
    "v_mfma_f32_16x16x32_bf16 v[220:223], a[232:235], a[152:155], v[220:223]\n"
    "ds_read_b128 a[180:183], v241 offset:27648\n"
    "v_add_f32_dpp v254, v253, v225 row_newbcast:1 row_mask:0xf bank_mask:0xf\n"
    "v_mfma_f32_16x16x32_bf16 v[220:223], a[236:239], a[184:187], v[220:223]\n"
    "ds_read_b128 a[148:151], v241 offset:26624\n"
    "v_add_f32_dpp v255, v253, v227 row_newbcast:3 row_mask:0xf bank_mask:0xf\n"
    "v_mfma_f32_16x16x32_bf16 a[252:255], a[236:239], a[188:191], a[252:255]\n"
    "ds_read_b128 a[152:155], v241 offset:28672\n"
    "v_mul_f32 v245, v245, v254\n"
    "v_mfma_f32_16x16x32_bf16 a[252:255], a[232:235], a[156:159], a[252:255]\n"
    "s_waitcnt vmcnt(11) lgkmcnt(11)\n"
    "ds_read_b128 a[184:187], v241 offset:29696\n"
    "v_mul_f32 v247, v247, v255\n"
    "v_mfma_f32_16x16x32_bf16 v[16:19], a[192:195], a[128:131], v[16:19]\n"
    "buffer_load_dwordx4 a[232:235], v240, s[20:23], s53 offen offset:2048 sc1\n"
    "v_mfma_f32_16x16x32_bf16 v[16:19], a[196:199], a[160:163], v[16:19]\n"
    "s_waitcnt lgkmcnt(10)\n"
    "v_mfma_f32_16x16x32_bf16 v[48:51], a[196:199], a[164:167], v[48:51]\n"
    "buffer_load_dwordx4 a[236:239], v240, s[20:23], s53 offen offset:3072 sc1\n"
    "v_mfma_f32_16x16x32_bf16 v[48:51], a[192:195], a[132:135], v[48:51]\n"
    "s_waitcnt lgkmcnt(8)\n"
    "s_addk_i32 s53, 4096\n"
    "ds_read_b128 a[188:191], v241 offset:31744\n"
    "v_mfma_f32_16x16x32_bf16 v[80:83], a[192:195], a[136:139], v[80:83]\n"
    "ds_read_b128 a[156:159], v241 offset:30720\n"
    "v_mfma_f32_16x16x32_bf16 v[80:83], a[196:199], a[168:171], v[80:83]\n"
    "s_waitcnt lgkmcnt(8)\n"
    "v_mfma_f32_16x16x32_bf16 v[112:115], a[196:199], a[172:175], v[112:115]\n"
    "buffer_store_dwordx4 v[248:251], v240, s[32:35], s91 offen sc1\n"
    "ds_write_b128 v243, v[248:251] offset:12288\n"
    "v_mfma_f32_16x16x32_bf16 v[112:115], a[192:195], a[140:143], v[112:115]\n"
    "s_waitcnt lgkmcnt(7)\n"
    "v_cvt_pk_bf16_f32 v248, v244, v245\n"
    "v_cvt_pk_bf16_f32 v249, v246, v247\n"
    "v_mfma_f32_16x16x32_bf16 v[144:147], a[192:195], a[144:147], v[144:147]\n"
    "v_accvgpr_read_b32 v245, a30\n"
    "v_accvgpr_read_b32 v247, a31\n"
    "v_mfma_f32_16x16x32_bf16 v[144:147], a[196:199], a[176:179], v[144:147]\n"
    "s_waitcnt lgkmcnt(5)\n"
    "v_lshlrev_b32 v244, 16, v245\n"
    "v_lshlrev_b32 v246, 16, v247\n"
    "v_mfma_f32_16x16x32_bf16 v[176:179], a[196:199], a[180:183], v[176:179]\n"
    "v_and_b32 v245, s58, v245\n"
    "v_and_b32 v247, s58, v247\n"
    "v_mfma_f32_16x16x32_bf16 v[176:179], a[192:195], a[148:151], v[176:179]\n"
    "s_waitcnt lgkmcnt(3)\n"
    "s_xor_b32 s91, s91, 0x800\n"
    "v_add_f32_dpp v254, v253, v228 row_newbcast:4 row_mask:0xf bank_mask:0xf\n"
    "v_mfma_f32_16x16x32_bf16 v[208:211], a[192:195], a[152:155], v[208:211]\n"
    "v_add_f32_dpp v255, v253, v230 row_newbcast:6 row_mask:0xf bank_mask:0xf\n"
    "v_mul_f32 v244, v244, v254\n"
    "v_mfma_f32_16x16x32_bf16 v[208:211], a[196:199], a[184:187], v[208:211]\n"
    "s_waitcnt lgkmcnt(1)\n"
    "v_mul_f32 v246, v246, v255\n"
    "v_add_f32_dpp v254, v253, v229 row_newbcast:5 row_mask:0xf bank_mask:0xf\n"
    "v_mfma_f32_16x16x32_bf16 a[240:243], a[196:199], a[188:191], a[240:243]\n"
    "v_add_f32_dpp v255, v253, v231 row_newbcast:7 row_mask:0xf bank_mask:0xf\n"
    "v_mul_f32 v245, v245, v254\n"
    "v_mfma_f32_16x16x32_bf16 a[240:243], a[192:195], a[156:159], a[240:243]\n"
    "s_waitcnt vmcnt(11)\n"
    "v_mul_f32 v247, v247, v255\n"
    "v_cvt_pk_bf16_f32 v250, v244, v245\n"
    "v_mfma_f32_16x16x32_bf16 a[244:247], a[200:203], a[156:159], a[244:247]\n"
    "buffer_load_dwordx4 a[192:195], v240, s[20:23], s53 offen offset:0 sc1\n"
    "v_mfma_f32_16x16x32_bf16 a[244:247], a[204:207], a[188:191], a[244:247]\n"
    "buffer_load_dwordx4 a[196:199], v240, s[20:23], s53 offen offset:1024 sc1\n"
    "v_mfma_f32_16x16x32_bf16 v[212:215], a[204:207], a[184:187], v[212:215]\n"
    "v_cvt_pk_bf16_f32 v251, v246, v247\n"
    "v_accvgpr_read_b32 v245, a32\n"
    "v_mfma_f32_16x16x32_bf16 v[212:215], a[200:203], a[152:155], v[212:215]\n"
    "buffer_store_dwordx4 v[248:251], v240, s[32:35], s91 offen sc1\n"
    "ds_write_b128 v243, v[248:251] offset:14336\n"
    "v_mfma_f32_16x16x32_bf16 v[180:183], a[200:203], a[148:151], v[180:183]\n"
    "v_accvgpr_read_b32 v247, a33\n"
    "v_lshlrev_b32 v244, 16, v245\n"
    "v_mfma_f32_16x16x32_bf16 v[180:183], a[204:207], a[180:183], v[180:183]\n"
    "v_lshlrev_b32 v246, 16, v247\n"
    "v_and_b32 v245, s58, v245\n"
    "v_mfma_f32_16x16x32_bf16 v[148:151], a[204:207], a[176:179], v[148:151]\n"
    "v_and_b32 v247, s58, v247\n"
    "v_add_f32_dpp v254, v253, v8 row_newbcast:8 row_mask:0xf bank_mask:0xf\n"
    "v_mfma_f32_16x16x32_bf16 v[148:151], a[200:203], a[144:147], v[148:151]\n"
    "v_add_f32_dpp v255, v253, v10 row_newbcast:10 row_mask:0xf bank_mask:0xf\n"
    "v_mul_f32 v244, v244, v254\n"
    "v_mfma_f32_16x16x32_bf16 v[116:119], a[200:203], a[140:143], v[116:119]\n"
    "v_mul_f32 v246, v246, v255\n"
    "v_add_f32_dpp v254, v253, v9 row_newbcast:9 row_mask:0xf bank_mask:0xf\n"
    "v_mfma_f32_16x16x32_bf16 v[116:119], a[204:207], a[172:175], v[116:119]\n"
    "v_add_f32_dpp v255, v253, v11 row_newbcast:11 row_mask:0xf bank_mask:0xf\n"
    "v_mul_f32 v245, v245, v254\n"
    "v_mfma_f32_16x16x32_bf16 v[84:87], a[204:207], a[168:171], v[84:87]\n"
    "s_xor_b32 s91, s91, 0x195800\n"
    "v_mul_f32 v247, v247, v255\n"
    "v_cvt_pk_bf16_f32 v248, v244, v245\n"
    "v_mfma_f32_16x16x32_bf16 v[84:87], a[200:203], a[136:139], v[84:87]\n"
    "v_cvt_pk_bf16_f32 v249, v246, v247\n"
    "v_accvgpr_read_b32 v245, a34\n"
    "v_mfma_f32_16x16x32_bf16 v[52:55], a[200:203], a[132:135], v[52:55]\n"
    "v_accvgpr_read_b32 v247, a35\n"
    "v_lshlrev_b32 v244, 16, v245\n"
    "v_mfma_f32_16x16x32_bf16 v[52:55], a[204:207], a[164:167], v[52:55]\n"
    "v_lshlrev_b32 v246, 16, v247\n"
    "v_and_b32 v245, s58, v245\n"
    "v_mfma_f32_16x16x32_bf16 v[20:23], a[204:207], a[160:163], v[20:23]\n"
    "v_and_b32 v247, s58, v247\n"
    "v_add_f32_dpp v254, v253, v12 row_newbcast:12 row_mask:0xf bank_mask:0xf\n"
    "v_mfma_f32_16x16x32_bf16 v[20:23], a[200:203], a[128:131], v[20:23]\n"
    "s_waitcnt vmcnt(12)\n"
    "v_add_f32_dpp v255, v253, v14 row_newbcast:14 row_mask:0xf bank_mask:0xf\n"
    "v_mul_f32 v244, v244, v254\n"
    "v_mfma_f32_16x16x32_bf16 v[24:27], a[208:211], a[128:131], v[24:27]\n"
    "buffer_load_dwordx4 a[200:203], v240, s[20:23], s53 offen offset:2048 sc1\n"
    "v_mfma_f32_16x16x32_bf16 v[24:27], a[212:215], a[160:163], v[24:27]\n"
    "buffer_load_dwordx4 a[204:207], v240, s[20:23], s53 offen offset:3072 sc1\n"
    "v_mfma_f32_16x16x32_bf16 v[56:59], a[212:215], a[164:167], v[56:59]\n"
    "s_addk_i32 s53, 4096\n"
    "v_mul_f32 v246, v246, v255\n"
    "v_add_f32_dpp v254, v253, v13 row_newbcast:13 row_mask:0xf bank_mask:0xf\n"
    "v_mfma_f32_16x16x32_bf16 v[56:59], a[208:211], a[132:135], v[56:59]\n"
    "v_add_f32_dpp v255, v253, v15 row_newbcast:15 row_mask:0xf bank_mask:0xf\n"
    "v_mul_f32 v245, v245, v254\n"
    "v_mfma_f32_16x16x32_bf16 v[88:91], a[208:211], a[136:139], v[88:91]\n"
    "v_mul_f32 v247, v247, v255\n"
    "v_cvt_pk_bf16_f32 v250, v244, v245\n"
    "v_mfma_f32_16x16x32_bf16 v[88:91], a[212:215], a[168:171], v[88:91]\n"
    "v_cvt_pk_bf16_f32 v251, v246, v247\n"
    "v_accvgpr_read_b32 v245, a36\n"
    "v_mfma_f32_16x16x32_bf16 v[120:123], a[212:215], a[172:175], v[120:123]\n"
    "buffer_store_dwordx4 v[248:251], v240, s[32:35], s91 offen sc1\n"
    "ds_write_b128 v243, v[248:251] offset:32768\n"
    "v_accvgpr_read_b32 v247, a37\n"
    "v_lshlrev_b32 v244, 16, v245\n"
    "v_lshlrev_b32 v246, 16, v247\n"
    "v_mfma_f32_16x16x32_bf16 v[120:123], a[208:211], a[140:143], v[120:123]\n"
    "s_xor_b32 s91, s91, 0x800\n"
    "v_and_b32 v245, s58, v245\n"
    "v_and_b32 v247, s58, v247\n"
    "v_add_f32_dpp v254, v253, v40 row_newbcast:8 row_mask:0xf bank_mask:0xf\n"
    "v_mfma_f32_16x16x32_bf16 v[152:155], a[208:211], a[144:147], v[152:155]\n"
    "v_add_f32_dpp v255, v253, v42 row_newbcast:10 row_mask:0xf bank_mask:0xf\n"
    "v_mul_f32 v244, v244, v254\n"
    "v_mfma_f32_16x16x32_bf16 v[152:155], a[212:215], a[176:179], v[152:155]\n"
    "v_mul_f32 v246, v246, v255\n"
    "v_add_f32_dpp v254, v253, v41 row_newbcast:9 row_mask:0xf bank_mask:0xf\n"
    "v_add_f32_dpp v255, v253, v43 row_newbcast:11 row_mask:0xf bank_mask:0xf\n"
    "v_mfma_f32_16x16x32_bf16 v[184:187], a[212:215], a[180:183], v[184:187]\n"
    "v_mul_f32 v245, v245, v254\n"
    "v_mul_f32 v247, v247, v255\n"
    "v_mfma_f32_16x16x32_bf16 v[184:187], a[208:211], a[148:151], v[184:187]\n"
    "v_cvt_pk_bf16_f32 v248, v244, v245\n"
    "v_cvt_pk_bf16_f32 v249, v246, v247\n"
    "v_mfma_f32_16x16x32_bf16 v[216:219], a[208:211], a[152:155], v[216:219]\n"
    "v_accvgpr_read_b32 v245, a38\n"
    "v_accvgpr_read_b32 v247, a39\n"
    "v_mfma_f32_16x16x32_bf16 v[216:219], a[212:215], a[184:187], v[216:219]\n"
    "v_lshlrev_b32 v244, 16, v245\n"
    "v_lshlrev_b32 v246, 16, v247\n"
    "v_and_b32 v245, s58, v245\n"
    "v_mfma_f32_16x16x32_bf16 a[248:251], a[212:215], a[188:191], a[248:251]\n"
    "v_and_b32 v247, s58, v247\n"
    "v_add_f32_dpp v254, v253, v44 row_newbcast:12 row_mask:0xf bank_mask:0xf\n"
    "v_mfma_f32_16x16x32_bf16 a[248:251], a[208:211], a[156:159], a[248:251]\n"
    "s_waitcnt vmcnt(12)\n"
    "v_add_f32_dpp v255, v253, v46 row_newbcast:14 row_mask:0xf bank_mask:0xf\n"
    "v_mul_f32 v244, v244, v254\n"
    "v_mfma_f32_16x16x32_bf16 a[252:255], a[216:219], a[156:159], a[252:255]\n"
    "buffer_load_dwordx4 a[208:211], v240, s[20:23], s53 offen offset:0 sc1\n"
    "v_mul_f32 v246, v246, v255\n"
    "v_add_f32_dpp v254, v253, v45 row_newbcast:13 row_mask:0xf bank_mask:0xf\n"
    "v_mfma_f32_16x16x32_bf16 a[252:255], a[220:223], a[188:191], a[252:255]\n"
    "buffer_load_dwordx4 a[212:215], v240, s[20:23], s53 offen offset:1024 sc1\n"
    "v_add_f32_dpp v255, v253, v47 row_newbcast:15 row_mask:0xf bank_mask:0xf\n"
    "v_mul_f32 v245, v245, v254\n"
    "v_mul_f32 v247, v247, v255\n"
    "v_mfma_f32_16x16x32_bf16 v[220:223], a[220:223], a[184:187], v[220:223]\n"
    "ds_read_b128 a[156:159], v241 offset:47104\n"
    "v_cvt_pk_bf16_f32 v250, v244, v245\n"
    "v_cvt_pk_bf16_f32 v251, v246, v247\n"
    "v_accvgpr_read_b32 v245, a40\n"
    "v_mfma_f32_16x16x32_bf16 v[220:223], a[216:219], a[152:155], v[220:223]\n"
    "ds_read_b128 a[188:191], v241 offset:48128\n"
    "v_accvgpr_read_b32 v247, a41\n"
    "v_lshlrev_b32 v244, 16, v245\n"
    "v_mfma_f32_16x16x32_bf16 v[188:191], a[216:219], a[148:151], v[188:191]\n"
    "ds_read_b128 a[184:187], v241 offset:46080\n"
    "v_lshlrev_b32 v246, 16, v247\n"
    "v_and_b32 v245, s58, v245\n"
    "v_and_b32 v247, s58, v247\n"
    "v_mfma_f32_16x16x32_bf16 v[188:191], a[220:223], a[180:183], v[188:191]\n"
    "ds_read_b128 a[152:155], v241 offset:45056\n"
    "v_add_f32_dpp v254, v253, v72 row_newbcast:8 row_mask:0xf bank_mask:0xf\n"
    "v_add_f32_dpp v255, v253, v74 row_newbcast:10 row_mask:0xf bank_mask:0xf\n"
    "v_mfma_f32_16x16x32_bf16 v[156:159], a[220:223], a[176:179], v[156:159]\n"
    "ds_read_b128 a[148:151], v241 offset:43008\n"
    "v_mul_f32 v244, v244, v254\n"
    "v_mul_f32 v246, v246, v255\n"
    "v_add_f32_dpp v254, v253, v73 row_newbcast:9 row_mask:0xf bank_mask:0xf\n"
    "v_mfma_f32_16x16x32_bf16 v[156:159], a[216:219], a[144:147], v[156:159]\n"
    "ds_read_b128 a[180:183], v241 offset:44032\n"
    "v_add_f32_dpp v255, v253, v75 row_newbcast:11 row_mask:0xf bank_mask:0xf\n"
    "v_mul_f32 v245, v245, v254\n"
    "v_mfma_f32_16x16x32_bf16 v[124:127], a[216:219], a[140:143], v[124:127]\n"
    "ds_read_b128 a[176:179], v241 offset:41984\n"
    "v_mul_f32 v247, v247, v255\n"
    "v_mfma_f32_16x16x32_bf16 v[124:127], a[220:223], a[172:175], v[124:127]\n"
    "buffer_store_dwordx4 v[248:251], v240, s[32:35], s91 offen sc1\n"
    "ds_write_b128 v243, v[248:251] offset:34816\n"
    "v_mfma_f32_16x16x32_bf16 v[92:95], a[220:223], a[168:171], v[92:95]\n"
    "ds_read_b128 a[144:147], v241 offset:40960\n"
    "ds_read_b128 a[140:143], v241 offset:38912\n"
    "v_mfma_f32_16x16x32_bf16 v[92:95], a[216:219], a[136:139], v[92:95]\n"
    "ds_read_b128 a[172:175], v241 offset:39936\n"
    "v_cvt_pk_bf16_f32 v248, v244, v245\n"
    "v_mfma_f32_16x16x32_bf16 v[60:63], a[216:219], a[132:135], v[60:63]\n"
    "ds_read_b128 a[168:171], v241 offset:37888\n"
    "v_cvt_pk_bf16_f32 v249, v246, v247\n"
    "v_mfma_f32_16x16x32_bf16 v[60:63], a[220:223], a[164:167], v[60:63]\n"
    "ds_read_b128 a[136:139], v241 offset:36864\n"
    "v_accvgpr_read_b32 v245, a42\n"
    "v_mfma_f32_16x16x32_bf16 v[28:31], a[220:223], a[160:163], v[28:31]\n"
    "ds_read_b128 a[132:135], v241 offset:34816\n"
    "v_accvgpr_read_b32 v247, a43\n"
    "v_mfma_f32_16x16x32_bf16 v[28:31], a[216:219], a[128:131], v[28:31]\n"
    "s_waitcnt vmcnt(12) lgkmcnt(12)\n"
    "ds_read_b128 a[164:167], v241 offset:35840\n"
    "v_lshlrev_b32 v244, 16, v245\n"
    "v_mfma_f32_16x16x32_bf16 a[240:243], a[224:227], a[156:159], a[240:243]\n"
    "buffer_load_dwordx4 a[216:219], v240, s[20:23], s53 offen offset:2048 sc1\n"
    "v_mfma_f32_16x16x32_bf16 a[240:243], a[228:231], a[188:191], a[240:243]\n"
    "s_waitcnt lgkmcnt(11)\n"
    "v_lshlrev_b32 v246, 16, v247\n"
    "v_and_b32 v245, s58, v245\n"
    "v_mfma_f32_16x16x32_bf16 v[208:211], a[228:231], a[184:187], v[208:211]\n"
    "buffer_load_dwordx4 a[220:223], v240, s[20:23], s53 offen offset:3072 sc1\n"
    "v_mfma_f32_16x16x32_bf16 v[208:211], a[224:227], a[152:155], v[208:211]\n"
    "s_waitcnt lgkmcnt(9)\n"
    "s_addk_i32 s53, 4096\n"
    "ds_read_b128 a[160:163], v241 offset:33792\n"
    "v_mfma_f32_16x16x32_bf16 v[176:179], a[224:227], a[148:151], v[176:179]\n"
    "ds_read_b128 a[128:131], v241 offset:32768\n"
    "v_and_b32 v247, s58, v247\n"
    "v_mfma_f32_16x16x32_bf16 v[176:179], a[228:231], a[180:183], v[176:179]\n"
    "s_waitcnt lgkmcnt(8)\n"
    "s_xor_b32 s91, s91, 0x101800\n"
    "v_add_f32_dpp v254, v253, v76 row_newbcast:12 row_mask:0xf bank_mask:0xf\n"
    "v_mfma_f32_16x16x32_bf16 v[144:147], a[228:231], a[176:179], v[144:147]\n"
    "v_add_f32_dpp v255, v253, v78 row_newbcast:14 row_mask:0xf bank_mask:0xf\n"
    "v_mul_f32 v244, v244, v254\n"
    "v_mfma_f32_16x16x32_bf16 v[144:147], a[224:227], a[144:147], v[144:147]\n"
    "s_waitcnt lgkmcnt(6)\n"
    "v_mul_f32 v246, v246, v255\n"
    "v_add_f32_dpp v254, v253, v77 row_newbcast:13 row_mask:0xf bank_mask:0xf\n"
    "v_mfma_f32_16x16x32_bf16 v[112:115], a[224:227], a[140:143], v[112:115]\n"
    "v_add_f32_dpp v255, v253, v79 row_newbcast:15 row_mask:0xf bank_mask:0xf\n"
    "v_mul_f32 v245, v245, v254\n"
    "v_mfma_f32_16x16x32_bf16 v[112:115], a[228:231], a[172:175], v[112:115]\n"
    "s_waitcnt lgkmcnt(4)\n"
    "v_mul_f32 v247, v247, v255\n"
    "v_cvt_pk_bf16_f32 v250, v244, v245\n"
    "v_mfma_f32_16x16x32_bf16 v[80:83], a[228:231], a[168:171], v[80:83]\n"
    "v_cvt_pk_bf16_f32 v251, v246, v247\n"
    "v_accvgpr_read_b32 v245, a44\n"
    "v_mfma_f32_16x16x32_bf16 v[80:83], a[224:227], a[136:139], v[80:83]\n"
    "s_waitcnt lgkmcnt(2)\n"
    "v_accvgpr_read_b32 v247, a45\n"
    "v_lshlrev_b32 v244, 16, v245\n"
    "v_mfma_f32_16x16x32_bf16 v[48:51], a[224:227], a[132:135], v[48:51]\n"
    "v_lshlrev_b32 v246, 16, v247\n"
    "v_and_b32 v245, s58, v245\n"
    "v_mfma_f32_16x16x32_bf16 v[48:51], a[228:231], a[164:167], v[48:51]\n"
    "s_waitcnt lgkmcnt(0)\n"
    "v_and_b32 v247, s58, v247\n"
    "v_add_f32_dpp v254, v253, v104 row_newbcast:8 row_mask:0xf bank_mask:0xf\n"
    "v_mfma_f32_16x16x32_bf16 v[16:19], a[228:231], a[160:163], v[16:19]\n"
    "v_add_f32_dpp v255, v253, v106 row_newbcast:10 row_mask:0xf bank_mask:0xf\n"
    "v_mul_f32 v244, v244, v254\n"
    "v_mfma_f32_16x16x32_bf16 v[16:19], a[224:227], a[128:131], v[16:19]\n"
    "s_waitcnt vmcnt(12)\n"
    "v_mul_f32 v246, v246, v255\n"
    "v_add_f32_dpp v254, v253, v105 row_newbcast:9 row_mask:0xf bank_mask:0xf\n"
    "v_mfma_f32_16x16x32_bf16 v[20:23], a[232:235], a[128:131], v[20:23]\n"
    "buffer_load_dwordx4 a[224:227], v240, s[20:23], s53 offen offset:0 sc1\n"
    "v_mfma_f32_16x16x32_bf16 v[20:23], a[236:239], a[160:163], v[20:23]\n"
    "buffer_load_dwordx4 a[228:231], v240, s[20:23], s53 offen offset:1024 sc1\n"
    "v_mfma_f32_16x16x32_bf16 v[52:55], a[236:239], a[164:167], v[52:55]\n"
    "buffer_store_dwordx4 v[248:251], v240, s[32:35], s91 offen sc1\n"
    "ds_write_b128 v243, v[248:251] offset:36864\n"
    "v_mfma_f32_16x16x32_bf16 v[52:55], a[232:235], a[132:135], v[52:55]\n"
    "v_add_f32_dpp v255, v253, v107 row_newbcast:11 row_mask:0xf bank_mask:0xf\n"
    "v_mul_f32 v245, v245, v254\n"
    "v_mfma_f32_16x16x32_bf16 v[84:87], a[232:235], a[136:139], v[84:87]\n"
    "v_mul_f32 v247, v247, v255\n"
    "v_cvt_pk_bf16_f32 v248, v244, v245\n"
    "v_mfma_f32_16x16x32_bf16 v[84:87], a[236:239], a[168:171], v[84:87]\n"
    "v_cvt_pk_bf16_f32 v249, v246, v247\n"
    "v_accvgpr_read_b32 v245, a46\n"
    "v_mfma_f32_16x16x32_bf16 v[116:119], a[236:239], a[172:175], v[116:119]\n"
    "s_xor_b32 s91, s91, 0x800\n"
    "v_accvgpr_read_b32 v247, a47\n"
    "v_lshlrev_b32 v244, 16, v245\n"
    "v_mfma_f32_16x16x32_bf16 v[116:119], a[232:235], a[140:143], v[116:119]\n"
    "v_lshlrev_b32 v246, 16, v247\n"
    "v_and_b32 v245, s58, v245\n"
    "v_mfma_f32_16x16x32_bf16 v[148:151], a[232:235], a[144:147], v[148:151]\n"
    "v_and_b32 v247, s58, v247\n"
    "v_add_f32_dpp v254, v253, v108 row_newbcast:12 row_mask:0xf bank_mask:0xf\n"
    "v_mfma_f32_16x16x32_bf16 v[148:151], a[236:239], a[176:179], v[148:151]\n"
    "v_add_f32_dpp v255, v253, v110 row_newbcast:14 row_mask:0xf bank_mask:0xf\n"
    "v_mul_f32 v244, v244, v254\n"
    "v_mfma_f32_16x16x32_bf16 v[180:183], a[236:239], a[180:183], v[180:183]\n"
    "v_mul_f32 v246, v246, v255\n"
    "v_add_f32_dpp v254, v253, v109 row_newbcast:13 row_mask:0xf bank_mask:0xf\n"
    "v_mfma_f32_16x16x32_bf16 v[180:183], a[232:235], a[148:151], v[180:183]\n"
    "v_add_f32_dpp v255, v253, v111 row_newbcast:15 row_mask:0xf bank_mask:0xf\n"
    "v_mul_f32 v245, v245, v254\n"
    "v_mfma_f32_16x16x32_bf16 v[212:215], a[232:235], a[152:155], v[212:215]\n"
    "v_mul_f32 v247, v247, v255\n"
    "v_cvt_pk_bf16_f32 v250, v244, v245\n"
    "v_mfma_f32_16x16x32_bf16 v[212:215], a[236:239], a[184:187], v[212:215]\n"
    "v_cvt_pk_bf16_f32 v251, v246, v247\n"
    "v_accvgpr_read_b32 v245, a48\n"
    "v_mfma_f32_16x16x32_bf16 a[244:247], a[236:239], a[188:191], a[244:247]\n"
    "v_accvgpr_read_b32 v247, a49\n"
    "v_lshlrev_b32 v244, 16, v245\n"
    "v_mfma_f32_16x16x32_bf16 a[244:247], a[232:235], a[156:159], a[244:247]\n"
    "s_waitcnt vmcnt(12)\n"
    "v_lshlrev_b32 v246, 16, v247\n"
    "v_and_b32 v245, s58, v245\n"
    "v_mfma_f32_16x16x32_bf16 a[248:251], a[192:195], a[156:159], a[248:251]\n"
    "buffer_store_dwordx4 v[248:251], v240, s[32:35], s91 offen sc1\n"
    "ds_write_b128 v243, v[248:251] offset:38912\n"
    "v_mfma_f32_16x16x32_bf16 a[248:251], a[196:199], a[188:191], a[248:251]\n"
    "buffer_load_dwordx4 a[232:235], v240, s[20:23], s53 offen offset:2048 sc1\n"
    "v_mfma_f32_16x16x32_bf16 v[216:219], a[196:199], a[184:187], v[216:219]\n"
    "buffer_load_dwordx4 a[236:239], v240, s[20:23], s53 offen offset:3072 sc1\n"
    "v_mfma_f32_16x16x32_bf16 v[216:219], a[192:195], a[152:155], v[216:219]\n"
    "s_addk_i32 s53, 4096\n"
    "v_and_b32 v247, s58, v247\n"
    "v_add_f32_dpp v254, v253, v136 row_newbcast:8 row_mask:0xf bank_mask:0xf\n"
    "v_mfma_f32_16x16x32_bf16 v[184:187], a[192:195], a[148:151], v[184:187]\n"
    "s_xor_b32 s91, s91, 0x185800\n"
    "v_add_f32_dpp v255, v253, v138 row_newbcast:10 row_mask:0xf bank_mask:0xf\n"
    "v_mul_f32 v244, v244, v254\n"
    "v_mfma_f32_16x16x32_bf16 v[184:187], a[196:199], a[180:183], v[184:187]\n"
    "v_mul_f32 v246, v246, v255\n"
    "v_add_f32_dpp v254, v253, v137 row_newbcast:9 row_mask:0xf bank_mask:0xf\n"
    "v_mfma_f32_16x16x32_bf16 v[152:155], a[196:199], a[176:179], v[152:155]\n"
    "v_add_f32_dpp v255, v253, v139 row_newbcast:11 row_mask:0xf bank_mask:0xf\n"
    "v_mul_f32 v245, v245, v254\n"
    "v_mfma_f32_16x16x32_bf16 v[152:155], a[192:195], a[144:147], v[152:155]\n"
    "v_mul_f32 v247, v247, v255\n"
    "v_cvt_pk_bf16_f32 v248, v244, v245\n"
    "v_mfma_f32_16x16x32_bf16 v[120:123], a[192:195], a[140:143], v[120:123]\n"
    "v_cvt_pk_bf16_f32 v249, v246, v247\n"
    "v_accvgpr_read_b32 v245, a50\n"
    "v_mfma_f32_16x16x32_bf16 v[120:123], a[196:199], a[172:175], v[120:123]\n"
    "v_accvgpr_read_b32 v247, a51\n"
    "v_lshlrev_b32 v244, 16, v245\n"
    "v_mfma_f32_16x16x32_bf16 v[88:91], a[196:199], a[168:171], v[88:91]\n"
    "v_lshlrev_b32 v246, 16, v247\n"
    "v_and_b32 v245, s58, v245\n"
    "v_mfma_f32_16x16x32_bf16 v[88:91], a[192:195], a[136:139], v[88:91]\n"
    "v_and_b32 v247, s58, v247\n"
    "v_add_f32_dpp v254, v253, v140 row_newbcast:12 row_mask:0xf bank_mask:0xf\n"
    "v_add_f32_dpp v255, v253, v142 row_newbcast:14 row_mask:0xf bank_mask:0xf\n"
    "v_mfma_f32_16x16x32_bf16 v[56:59], a[192:195], a[132:135], v[56:59]\n"
    "v_mul_f32 v244, v244, v254\n"
    "v_mul_f32 v246, v246, v255\n"
    "v_mfma_f32_16x16x32_bf16 v[56:59], a[196:199], a[164:167], v[56:59]\n"
    "v_add_f32_dpp v254, v253, v141 row_newbcast:13 row_mask:0xf bank_mask:0xf\n"
    "v_add_f32_dpp v255, v253, v143 row_newbcast:15 row_mask:0xf bank_mask:0xf\n"
    "v_mfma_f32_16x16x32_bf16 v[24:27], a[196:199], a[160:163], v[24:27]\n"
    "v_mul_f32 v245, v245, v254\n"
    "v_mul_f32 v247, v247, v255\n"
    "v_mfma_f32_16x16x32_bf16 v[24:27], a[192:195], a[128:131], v[24:27]\n"
    "s_waitcnt vmcnt(12)\n"
    "v_cvt_pk_bf16_f32 v250, v244, v245\n"
    "v_cvt_pk_bf16_f32 v251, v246, v247\n"
    "v_accvgpr_read_b32 v245, a52\n"
    "v_mfma_f32_16x16x32_bf16 v[28:31], a[200:203], a[128:131], v[28:31]\n"
    "buffer_store_dwordx4 v[248:251], v240, s[32:35], s91 offen sc1\n"
    "ds_write_b128 v243, v[248:251] offset:40960\n"
    "v_accvgpr_read_b32 v247, a53\n"
    "v_lshlrev_b32 v244, 16, v245\n"
    "v_lshlrev_b32 v246, 16, v247\n"
    "v_mfma_f32_16x16x32_bf16 v[28:31], a[204:207], a[160:163], v[28:31]\n"
    "buffer_load_dwordx4 a[192:195], v240, s[20:23], s53 offen offset:0 sc1\n"
    "v_and_b32 v245, s58, v245\n"
    "v_and_b32 v247, s58, v247\n"
    "v_mfma_f32_16x16x32_bf16 v[60:63], a[204:207], a[164:167], v[60:63]\n"
    "buffer_load_dwordx4 a[196:199], v240, s[20:23], s53 offen offset:1024 sc1\n"
    "v_add_f32_dpp v254, v253, v168 row_newbcast:8 row_mask:0xf bank_mask:0xf\n"
    "v_add_f32_dpp v255, v253, v170 row_newbcast:10 row_mask:0xf bank_mask:0xf\n"
    "v_mul_f32 v244, v244, v254\n"
    "v_mfma_f32_16x16x32_bf16 v[60:63], a[200:203], a[132:135], v[60:63]\n"
    "ds_read_b128 a[128:131], v241 offset:49152\n"
    "ds_read_b128 a[160:163], v241 offset:50176\n"
    "v_mul_f32 v246, v246, v255\n"
    "v_add_f32_dpp v254, v253, v169 row_newbcast:9 row_mask:0xf bank_mask:0xf\n"
    "v_mfma_f32_16x16x32_bf16 v[92:95], a[200:203], a[136:139], v[92:95]\n"
    "ds_read_b128 a[164:167], v241 offset:52224\n"
    "v_add_f32_dpp v255, v253, v171 row_newbcast:11 row_mask:0xf bank_mask:0xf\n"
    "v_mul_f32 v245, v245, v254\n"
    "v_mul_f32 v247, v247, v255\n"
    "v_mfma_f32_16x16x32_bf16 v[92:95], a[204:207], a[168:171], v[92:95]\n"
    "ds_read_b128 a[132:135], v241 offset:51200\n"
    "v_cvt_pk_bf16_f32 v248, v244, v245\n"
    "v_mfma_f32_16x16x32_bf16 v[124:127], a[204:207], a[172:175], v[124:127]\n"
    "ds_read_b128 a[136:139], v241 offset:53248\n"
    "v_cvt_pk_bf16_f32 v249, v246, v247\n"
    "v_accvgpr_read_b32 v245, a54\n"
    "v_accvgpr_read_b32 v247, a55\n"
    "v_mfma_f32_16x16x32_bf16 v[124:127], a[200:203], a[140:143], v[124:127]\n"
    "ds_read_b128 a[168:171], v241 offset:54272\n"
    "v_lshlrev_b32 v244, 16, v245\n"
    "v_lshlrev_b32 v246, 16, v247\n"
    "v_mfma_f32_16x16x32_bf16 v[156:159], a[200:203], a[144:147], v[156:159]\n"
    "ds_read_b128 a[172:175], v241 offset:56320\n"
    "v_and_b32 v245, s58, v245\n"
    "v_mfma_f32_16x16x32_bf16 v[156:159], a[204:207], a[176:179], v[156:159]\n"
    "ds_read_b128 a[140:143], v241 offset:55296\n"
    "v_and_b32 v247, s58, v247\n"
    "v_mfma_f32_16x16x32_bf16 v[188:191], a[204:207], a[180:183], v[188:191]\n"
    "ds_read_b128 a[144:147], v241 offset:57344\n"
    "v_add_f32_dpp v254, v253, v172 row_newbcast:12 row_mask:0xf bank_mask:0xf\n"
    "v_mfma_f32_16x16x32_bf16 v[188:191], a[200:203], a[148:151], v[188:191]\n"
    "ds_read_b128 a[176:179], v241 offset:58368\n"
    "s_xor_b32 s91, s91, 0x800\n"
    "v_mfma_f32_16x16x32_bf16 v[220:223], a[200:203], a[152:155], v[220:223]\n"
    "ds_read_b128 a[180:183], v241 offset:60416\n"
    "v_add_f32_dpp v255, v253, v174 row_newbcast:14 row_mask:0xf bank_mask:0xf\n"
    "v_mfma_f32_16x16x32_bf16 v[220:223], a[204:207], a[184:187], v[220:223]\n"
    "ds_read_b128 a[148:151], v241 offset:59392\n"
    "v_mul_f32 v244, v244, v254\n"
    "v_mfma_f32_16x16x32_bf16 a[252:255], a[204:207], a[188:191], a[252:255]\n"
    "ds_read_b128 a[152:155], v241 offset:61440\n"
    "v_mul_f32 v246, v246, v255\n"
    "v_mfma_f32_16x16x32_bf16 a[252:255], a[200:203], a[156:159], a[252:255]\n"
    "s_waitcnt vmcnt(12) lgkmcnt(11)\n"
    "ds_read_b128 a[184:187], v241 offset:62464\n"
    "v_add_f32_dpp v254, v253, v173 row_newbcast:13 row_mask:0xf bank_mask:0xf\n"
    "v_mfma_f32_16x16x32_bf16 v[16:19], a[208:211], a[128:131], v[16:19]\n"
    "buffer_load_dwordx4 a[200:203], v240, s[20:23], s53 offen offset:2048 sc1\n"
    "v_mfma_f32_16x16x32_bf16 v[16:19], a[212:215], a[160:163], v[16:19]\n"
    "s_waitcnt lgkmcnt(10)\n"
    "v_add_f32_dpp v255, v253, v175 row_newbcast:15 row_mask:0xf bank_mask:0xf\n"
    "v_mul_f32 v245, v245, v254\n"
    "v_mfma_f32_16x16x32_bf16 v[48:51], a[212:215], a[164:167], v[48:51]\n"
    "buffer_load_dwordx4 a[204:207], v240, s[20:23], s53 offen offset:3072 sc1\n"
    "v_mfma_f32_16x16x32_bf16 v[48:51], a[208:211], a[132:135], v[48:51]\n"
    "s_waitcnt lgkmcnt(8)\n"
    "s_addk_i32 s53, 4096\n"
    "ds_read_b128 a[188:191], v241 offset:64512\n"
    "v_mfma_f32_16x16x32_bf16 v[80:83], a[208:211], a[136:139], v[80:83]\n"
    "ds_read_b128 a[156:159], v241 offset:63488\n"
    "v_mul_f32 v247, v247, v255\n"
    "v_mfma_f32_16x16x32_bf16 v[80:83], a[212:215], a[168:171], v[80:83]\n"
    "s_waitcnt lgkmcnt(8)\n"
    "v_cvt_pk_bf16_f32 v250, v244, v245\n"
    "v_cvt_pk_bf16_f32 v251, v246, v247\n"
    "v_mfma_f32_16x16x32_bf16 v[112:115], a[212:215], a[172:175], v[112:115]\n"
    "v_accvgpr_read_b32 v245, a56\n"
    "v_accvgpr_read_b32 v247, a57\n"
    "v_mfma_f32_16x16x32_bf16 v[112:115], a[208:211], a[140:143], v[112:115]\n"
    "s_waitcnt lgkmcnt(6)\n"
    "v_lshlrev_b32 v244, 16, v245\n"
    "v_lshlrev_b32 v246, 16, v247\n"
    "v_mfma_f32_16x16x32_bf16 v[144:147], a[208:211], a[144:147], v[144:147]\n"
    "v_and_b32 v245, s58, v245\n"
    "v_and_b32 v247, s58, v247\n"
    "v_mfma_f32_16x16x32_bf16 v[144:147], a[212:215], a[176:179], v[144:147]\n"
    "s_waitcnt lgkmcnt(4)\n"
    "v_add_f32_dpp v254, v253, v200 row_newbcast:8 row_mask:0xf bank_mask:0xf\n"
    "v_add_f32_dpp v255, v253, v202 row_newbcast:10 row_mask:0xf bank_mask:0xf\n"
    "v_mfma_f32_16x16x32_bf16 v[176:179], a[212:215], a[180:183], v[176:179]\n"
    "v_mul_f32 v244, v244, v254\n"
    "v_mul_f32 v246, v246, v255\n"
    "v_mfma_f32_16x16x32_bf16 v[176:179], a[208:211], a[148:151], v[176:179]\n"
    "s_waitcnt lgkmcnt(2)\n"
    "v_add_f32_dpp v254, v253, v201 row_newbcast:9 row_mask:0xf bank_mask:0xf\n"
    "v_add_f32_dpp v255, v253, v203 row_newbcast:11 row_mask:0xf bank_mask:0xf\n"
    "v_mfma_f32_16x16x32_bf16 v[208:211], a[208:211], a[152:155], v[208:211]\n"
    "v_mul_f32 v245, v245, v254\n"
    "v_mul_f32 v247, v247, v255\n"
    "v_mfma_f32_16x16x32_bf16 v[208:211], a[212:215], a[184:187], v[208:211]\n"
    "s_waitcnt lgkmcnt(0)\n"
    "v_mfma_f32_16x16x32_bf16 a[240:243], a[212:215], a[188:191], a[240:243]\n"
    "buffer_store_dwordx4 v[248:251], v240, s[32:35], s91 offen sc1\n"
    "ds_write_b128 v243, v[248:251] offset:43008\n"
    "v_mfma_f32_16x16x32_bf16 a[240:243], a[208:211], a[156:159], a[240:243]\n"
    "s_waitcnt vmcnt(12)\n"
    "v_cvt_pk_bf16_f32 v248, v244, v245\n"
    "v_cvt_pk_bf16_f32 v249, v246, v247\n"
    "v_mfma_f32_16x16x32_bf16 a[244:247], a[216:219], a[156:159], a[244:247]\n"
    "buffer_load_dwordx4 a[208:211], v240, s[20:23], s53 offen offset:0 sc1\n"
    "v_mfma_f32_16x16x32_bf16 a[244:247], a[220:223], a[188:191], a[244:247]\n"
    "buffer_load_dwordx4 a[212:215], v240, s[20:23], s53 offen offset:1024 sc1\n"
    "v_mfma_f32_16x16x32_bf16 v[212:215], a[220:223], a[184:187], v[212:215]\n"
    "v_accvgpr_read_b32 v245, a58\n"
    "v_accvgpr_read_b32 v247, a59\n"
    "v_mfma_f32_16x16x32_bf16 v[212:215], a[216:219], a[152:155], v[212:215]\n"
    "s_xor_b32 s91, s91, 0x101800\n"
    "v_lshlrev_b32 v244, 16, v245\n"
    "v_lshlrev_b32 v246, 16, v247\n"
    "v_mfma_f32_16x16x32_bf16 v[180:183], a[216:219], a[148:151], v[180:183]\n"
    "v_and_b32 v245, s58, v245\n"
    "v_and_b32 v247, s58, v247\n"
    "v_mfma_f32_16x16x32_bf16 v[180:183], a[220:223], a[180:183], v[180:183]\n"
    "v_add_f32_dpp v254, v253, v204 row_newbcast:12 row_mask:0xf bank_mask:0xf\n"
    "v_add_f32_dpp v255, v253, v206 row_newbcast:14 row_mask:0xf bank_mask:0xf\n"
    "v_mfma_f32_16x16x32_bf16 v[148:151], a[220:223], a[176:179], v[148:151]\n"
    "v_mul_f32 v244, v244, v254\n"
    "v_mul_f32 v246, v246, v255\n"
    "v_mfma_f32_16x16x32_bf16 v[148:151], a[216:219], a[144:147], v[148:151]\n"
    "v_add_f32_dpp v254, v253, v205 row_newbcast:13 row_mask:0xf bank_mask:0xf\n"
    "v_add_f32_dpp v255, v253, v207 row_newbcast:15 row_mask:0xf bank_mask:0xf\n"
    "v_mfma_f32_16x16x32_bf16 v[116:119], a[216:219], a[140:143], v[116:119]\n"
    "v_mul_f32 v245, v245, v254\n"
    "v_mul_f32 v247, v247, v255\n"
    "v_mfma_f32_16x16x32_bf16 v[116:119], a[220:223], a[172:175], v[116:119]\n"
    "v_cvt_pk_bf16_f32 v250, v244, v245\n"
    "v_cvt_pk_bf16_f32 v251, v246, v247\n"
    "v_mfma_f32_16x16x32_bf16 v[84:87], a[220:223], a[168:171], v[84:87]\n"
    "v_accvgpr_read_b32 v245, a60\n"
    "v_accvgpr_read_b32 v247, a61\n"
    "v_mfma_f32_16x16x32_bf16 v[84:87], a[216:219], a[136:139], v[84:87]\n"
    "v_lshlrev_b32 v244, 16, v245\n"
    "v_lshlrev_b32 v246, 16, v247\n"
    "v_mfma_f32_16x16x32_bf16 v[52:55], a[216:219], a[132:135], v[52:55]\n"
    "buffer_store_dwordx4 v[248:251], v240, s[32:35], s91 offen sc1\n"
    "ds_write_b128 v243, v[248:251] offset:45056\n"
    "v_mfma_f32_16x16x32_bf16 v[52:55], a[220:223], a[164:167], v[52:55]\n"
    "v_and_b32 v245, s58, v245\n"
    "v_and_b32 v247, s58, v247\n"
    "v_mfma_f32_16x16x32_bf16 v[20:23], a[220:223], a[160:163], v[20:23]\n"
    "v_add_f32_dpp v254, v253, v232 row_newbcast:8 row_mask:0xf bank_mask:0xf\n"
    "v_add_f32_dpp v255, v253, v234 row_newbcast:10 row_mask:0xf bank_mask:0xf\n"
    "v_mfma_f32_16x16x32_bf16 v[20:23], a[216:219], a[128:131], v[20:23]\n"
    "s_waitcnt vmcnt(13)\n"
    "v_mul_f32 v244, v244, v254\n"
    "v_mul_f32 v246, v246, v255\n"
    "v_mfma_f32_16x16x32_bf16 v[24:27], a[224:227], a[128:131], v[24:27]\n"
    "buffer_load_dwordx4 a[216:219], v240, s[20:23], s53 offen offset:2048 sc1\n"
    "v_mfma_f32_16x16x32_bf16 v[24:27], a[228:231], a[160:163], v[24:27]\n"
    "buffer_load_dwordx4 a[220:223], v240, s[20:23], s53 offen offset:3072 sc1\n"
    "v_mfma_f32_16x16x32_bf16 v[56:59], a[228:231], a[164:167], v[56:59]\n"
    "s_addk_i32 s53, 4096\n"
    "s_xor_b32 s91, s91, 0x800\n"
    "v_add_f32_dpp v254, v253, v233 row_newbcast:9 row_mask:0xf bank_mask:0xf\n"
    "v_mfma_f32_16x16x32_bf16 v[56:59], a[224:227], a[132:135], v[56:59]\n"
    "v_add_f32_dpp v255, v253, v235 row_newbcast:11 row_mask:0xf bank_mask:0xf\n"
    "v_mul_f32 v245, v245, v254\n"
    "v_mfma_f32_16x16x32_bf16 v[88:91], a[224:227], a[136:139], v[88:91]\n"
    "v_mul_f32 v247, v247, v255\n"
    "v_cvt_pk_bf16_f32 v248, v244, v245\n"
    "v_mfma_f32_16x16x32_bf16 v[88:91], a[228:231], a[168:171], v[88:91]\n"
    "v_cvt_pk_bf16_f32 v249, v246, v247\n"
    "v_accvgpr_read_b32 v245, a62\n"
    "v_mfma_f32_16x16x32_bf16 v[120:123], a[228:231], a[172:175], v[120:123]\n"
    "v_accvgpr_read_b32 v247, a63\n"
    "v_lshlrev_b32 v244, 16, v245\n"
    "v_mfma_f32_16x16x32_bf16 v[120:123], a[224:227], a[140:143], v[120:123]\n"
    "v_lshlrev_b32 v246, 16, v247\n"
    "v_and_b32 v245, s58, v245\n"
    "v_and_b32 v247, s58, v247\n"
    "v_mfma_f32_16x16x32_bf16 v[152:155], a[224:227], a[144:147], v[152:155]\n"
    "v_add_f32_dpp v254, v253, v236 row_newbcast:12 row_mask:0xf bank_mask:0xf\n"
    "v_add_f32_dpp v255, v253, v238 row_newbcast:14 row_mask:0xf bank_mask:0xf\n"
    "v_mfma_f32_16x16x32_bf16 v[152:155], a[228:231], a[176:179], v[152:155]\n"
    "v_mul_f32 v244, v244, v254\n"
    "v_mul_f32 v246, v246, v255\n"
    "v_mfma_f32_16x16x32_bf16 v[184:187], a[228:231], a[180:183], v[184:187]\n"
    "v_add_f32_dpp v254, v253, v237 row_newbcast:13 row_mask:0xf bank_mask:0xf\n"
    "v_add_f32_dpp v255, v253, v239 row_newbcast:15 row_mask:0xf bank_mask:0xf\n"
    "v_mfma_f32_16x16x32_bf16 v[184:187], a[224:227], a[148:151], v[184:187]\n"
    "v_mul_f32 v245, v245, v254\n"
    "v_mul_f32 v247, v247, v255\n"
    "v_cvt_pk_bf16_f32 v250, v244, v245\n"
    "v_mfma_f32_16x16x32_bf16 v[216:219], a[224:227], a[152:155], v[216:219]\n"
    "v_cvt_pk_bf16_f32 v251, v246, v247\n"
    "v_mfma_f32_16x16x32_bf16 v[216:219], a[228:231], a[184:187], v[216:219]\n"
    "buffer_store_dwordx4 v[248:251], v240, s[32:35], s91 offen sc1\n"
    "ds_write_b128 v243, v[248:251] offset:47104\n"
    "s_waitcnt lgkmcnt(0)\n"
    "s_barrier\n"
    "v_mfma_f32_16x16x32_bf16 a[248:251], a[228:231], a[188:191], a[248:251]\n"
    "v_mfma_f32_16x16x32_bf16 a[248:251], a[224:227], a[156:159], a[248:251]\n"
    "s_waitcnt vmcnt(12)\n"
    "v_mfma_f32_16x16x32_bf16 a[252:255], a[232:235], a[156:159], a[252:255]\n"
    "buffer_load_dwordx4 a[224:227], v240, s[20:23], s53 offen offset:0 sc1\n"
    "v_mfma_f32_16x16x32_bf16 a[252:255], a[236:239], a[188:191], a[252:255]\n"
    "buffer_load_dwordx4 a[228:231], v240, s[20:23], s53 offen offset:1024 sc1\n"
    "v_mfma_f32_16x16x32_bf16 v[220:223], a[236:239], a[184:187], v[220:223]\n"
    "v_add_u32 v241, s66, v240\n"
    "ds_read_b128 a[156:159], v241 offset:14336\n"
    "v_mfma_f32_16x16x32_bf16 v[220:223], a[232:235], a[152:155], v[220:223]\n"
    "ds_read_b128 a[188:191], v241 offset:15360\n"
    "v_mfma_f32_16x16x32_bf16 v[188:191], a[232:235], a[148:151], v[188:191]\n"
    "ds_read_b128 a[184:187], v241 offset:13312\n"
    "v_mfma_f32_16x16x32_bf16 v[188:191], a[236:239], a[180:183], v[188:191]\n"
    "ds_read_b128 a[152:155], v241 offset:12288\n"
    "v_mfma_f32_16x16x32_bf16 v[156:159], a[236:239], a[176:179], v[156:159]\n"
    "ds_read_b128 a[148:151], v241 offset:10240\n"
    "v_mfma_f32_16x16x32_bf16 v[156:159], a[232:235], a[144:147], v[156:159]\n"
    "ds_read_b128 a[180:183], v241 offset:11264\n"
    "v_mfma_f32_16x16x32_bf16 v[124:127], a[232:235], a[140:143], v[124:127]\n"
    "ds_read_b128 a[176:179], v241 offset:9216\n"
    "v_mfma_f32_16x16x32_bf16 v[124:127], a[236:239], a[172:175], v[124:127]\n"
    "ds_read_b128 a[144:147], v241 offset:8192\n"
    "v_mfma_f32_16x16x32_bf16 v[92:95], a[236:239], a[168:171], v[92:95]\n"
    "ds_read_b128 a[140:143], v241 offset:6144\n"
    "v_mfma_f32_16x16x32_bf16 v[92:95], a[232:235], a[136:139], v[92:95]\n"
    "ds_read_b128 a[172:175], v241 offset:7168\n"
    "v_mfma_f32_16x16x32_bf16 v[60:63], a[232:235], a[132:135], v[60:63]\n"
    "ds_read_b128 a[168:171], v241 offset:5120\n"
    "v_mfma_f32_16x16x32_bf16 v[60:63], a[236:239], a[164:167], v[60:63]\n"
    "ds_read_b128 a[136:139], v241 offset:4096\n"
    "v_mfma_f32_16x16x32_bf16 v[28:31], a[236:239], a[160:163], v[28:31]\n"
    "ds_read_b128 a[132:135], v241 offset:2048\n"
    "v_mfma_f32_16x16x32_bf16 v[28:31], a[232:235], a[128:131], v[28:31]\n"
    "ds_read_b128 a[164:167], v241 offset:3072\n"
    "ds_read_b128 a[160:163], v241 offset:1024\n"
    "ds_read_b128 a[128:131], v241 offset:0\n"
    "buffer_load_dwordx4 a[232:235], v240, s[20:23], s53 offen offset:2048 sc1\n"
    "buffer_load_dwordx4 a[236:239], v240, s[20:23], s53 offen offset:3072 sc1\n"
    "s_addk_i32 s53, 4096\n"
    "s_branch LOOP_%=\n"
    "LOOPE_%=:\n"
    "CP0_%=:\n"
    "s_cmp_lg_u32 s84, 0\n"
    "s_cbranch_scc1 CQ0_%=\n"
    "ds_read_b128 a[0:3], v241 offset:0\n"
    "ds_read_b128 a[4:7], v241 offset:2048\n"
    "ds_read_b128 a[8:11], v241 offset:4096\n"
    "ds_read_b128 a[12:15], v241 offset:6144\n"
    "ds_read_b128 a[16:19], v241 offset:8192\n"
    "ds_read_b128 a[20:23], v241 offset:10240\n"
    "ds_read_b128 a[24:27], v241 offset:12288\n"
    "ds_read_b128 a[28:31], v241 offset:14336\n"
    "ds_read_b128 a[32:35], v242 offset:0\n"
    "ds_read_b128 a[36:39], v242 offset:2048\n"
    "ds_read_b128 a[40:43], v242 offset:4096\n"
    "ds_read_b128 a[44:47], v242 offset:6144\n"
    "ds_read_b128 a[48:51], v242 offset:8192\n"
    "ds_read_b128 a[52:55], v242 offset:10240\n"
    "ds_read_b128 a[56:59], v242 offset:12288\n"
    "ds_read_b128 a[60:63], v242 offset:14336\n"
    "s_branch CR0_%=\n"
    "CQ0_%=:\n"
    "ds_read_b128 a[64:67], v241 offset:0\n"
    "ds_read_b128 a[68:71], v241 offset:2048\n"
    "ds_read_b128 a[72:75], v241 offset:4096\n"
    "ds_read_b128 a[76:79], v241 offset:6144\n"
    "ds_read_b128 a[80:83], v241 offset:8192\n"
    "ds_read_b128 a[84:87], v241 offset:10240\n"
    "ds_read_b128 a[88:91], v241 offset:12288\n"
    "ds_read_b128 a[92:95], v241 offset:14336\n"
    "ds_read_b128 a[96:99], v242 offset:0\n"
    "ds_read_b128 a[100:103], v242 offset:2048\n"
    "ds_read_b128 a[104:107], v242 offset:4096\n"
    "ds_read_b128 a[108:111], v242 offset:6144\n"
    "ds_read_b128 a[112:115], v242 offset:8192\n"
    "ds_read_b128 a[116:119], v242 offset:10240\n"
    "ds_read_b128 a[120:123], v242 offset:12288\n"
    "ds_read_b128 a[124:127], v242 offset:14336\n"
    "s_branch CR0_%=\n"
    "CP1_%=:\n"
    "s_cmp_lg_u32 s84, 1\n"
    "s_cbranch_scc1 CQ1_%=\n"
    "ds_read_b128 a[0:3], v241 offset:16384\n"
    "ds_read_b128 a[4:7], v241 offset:18432\n"
    "ds_read_b128 a[8:11], v241 offset:20480\n"
    "ds_read_b128 a[12:15], v241 offset:22528\n"
    "ds_read_b128 a[16:19], v241 offset:24576\n"
    "ds_read_b128 a[20:23], v241 offset:26624\n"
    "ds_read_b128 a[24:27], v241 offset:28672\n"
    "ds_read_b128 a[28:31], v241 offset:30720\n"
    "ds_read_b128 a[32:35], v242 offset:16384\n"
    "ds_read_b128 a[36:39], v242 offset:18432\n"
    "ds_read_b128 a[40:43], v242 offset:20480\n"
    "ds_read_b128 a[44:47], v242 offset:22528\n"
    "ds_read_b128 a[48:51], v242 offset:24576\n"
    "ds_read_b128 a[52:55], v242 offset:26624\n"
    "ds_read_b128 a[56:59], v242 offset:28672\n"
    "ds_read_b128 a[60:63], v242 offset:30720\n"
    "s_branch CR1_%=\n"
    "CQ1_%=:\n"
    "ds_read_b128 a[64:67], v241 offset:16384\n"
    "ds_read_b128 a[68:71], v241 offset:18432\n"
    "ds_read_b128 a[72:75], v241 offset:20480\n"
    "ds_read_b128 a[76:79], v241 offset:22528\n"
    "ds_read_b128 a[80:83], v241 offset:24576\n"
    "ds_read_b128 a[84:87], v241 offset:26624\n"
    "ds_read_b128 a[88:91], v241 offset:28672\n"
    "ds_read_b128 a[92:95], v241 offset:30720\n"
    "ds_read_b128 a[96:99], v242 offset:16384\n"
    "ds_read_b128 a[100:103], v242 offset:18432\n"
    "ds_read_b128 a[104:107], v242 offset:20480\n"
    "ds_read_b128 a[108:111], v242 offset:22528\n"
    "ds_read_b128 a[112:115], v242 offset:24576\n"
    "ds_read_b128 a[116:119], v242 offset:26624\n"
    "ds_read_b128 a[120:123], v242 offset:28672\n"
    "ds_read_b128 a[124:127], v242 offset:30720\n"
    "s_branch CR1_%=\n"
    "CP2_%=:\n"
    "s_cmp_lg_u32 s84, 2\n"
    "s_cbranch_scc1 CQ2_%=\n"
    "ds_read_b128 a[0:3], v241 offset:32768\n"
    "ds_read_b128 a[4:7], v241 offset:34816\n"
    "ds_read_b128 a[8:11], v241 offset:36864\n"
    "ds_read_b128 a[12:15], v241 offset:38912\n"
    "ds_read_b128 a[16:19], v241 offset:40960\n"
    "ds_read_b128 a[20:23], v241 offset:43008\n"
    "ds_read_b128 a[24:27], v241 offset:45056\n"
    "ds_read_b128 a[28:31], v241 offset:47104\n"
    "ds_read_b128 a[32:35], v242 offset:32768\n"
    "ds_read_b128 a[36:39], v242 offset:34816\n"
    "ds_read_b128 a[40:43], v242 offset:36864\n"
    "ds_read_b128 a[44:47], v242 offset:38912\n"
    "ds_read_b128 a[48:51], v242 offset:40960\n"
    "ds_read_b128 a[52:55], v242 offset:43008\n"
    "ds_read_b128 a[56:59], v242 offset:45056\n"
    "ds_read_b128 a[60:63], v242 offset:47104\n"
    "s_branch CR2_%=\n"
    "CQ2_%=:\n"
    "ds_read_b128 a[64:67], v241 offset:32768\n"
    "ds_read_b128 a[68:71], v241 offset:34816\n"
    "ds_read_b128 a[72:75], v241 offset:36864\n"
    "ds_read_b128 a[76:79], v241 offset:38912\n"
    "ds_read_b128 a[80:83], v241 offset:40960\n"
    "ds_read_b128 a[84:87], v241 offset:43008\n"
    "ds_read_b128 a[88:91], v241 offset:45056\n"
    "ds_read_b128 a[92:95], v241 offset:47104\n"
    "ds_read_b128 a[96:99], v242 offset:32768\n"
    "ds_read_b128 a[100:103], v242 offset:34816\n"
    "ds_read_b128 a[104:107], v242 offset:36864\n"
    "ds_read_b128 a[108:111], v242 offset:38912\n"
    "ds_read_b128 a[112:115], v242 offset:40960\n"
    "ds_read_b128 a[116:119], v242 offset:43008\n"
    "ds_read_b128 a[120:123], v242 offset:45056\n"
    "ds_read_b128 a[124:127], v242 offset:47104\n"
    "s_branch CR2_%=\n"
    "CP3_%=:\n"
    "s_cmp_lg_u32 s84, 3\n"
    "s_cbranch_scc1 CQ3_%=\n"
    "ds_read_b128 a[0:3], v241 offset:0\n"
    "ds_read_b128 a[4:7], v241 offset:2048\n"
    "ds_read_b128 a[8:11], v241 offset:4096\n"
    "ds_read_b128 a[12:15], v241 offset:6144\n"
    "ds_read_b128 a[16:19], v241 offset:8192\n"
    "ds_read_b128 a[20:23], v241 offset:10240\n"
    "ds_read_b128 a[24:27], v241 offset:12288\n"
    "ds_read_b128 a[28:31], v241 offset:14336\n"
    "ds_read_b128 a[32:35], v242 offset:0\n"
    "ds_read_b128 a[36:39], v242 offset:2048\n"
    "ds_read_b128 a[40:43], v242 offset:4096\n"
    "ds_read_b128 a[44:47], v242 offset:6144\n"
    "ds_read_b128 a[48:51], v242 offset:8192\n"
    "ds_read_b128 a[52:55], v242 offset:10240\n"
    "ds_read_b128 a[56:59], v242 offset:12288\n"
    "ds_read_b128 a[60:63], v242 offset:14336\n"
    "s_branch CR3_%=\n"
    "CQ3_%=:\n"
    "ds_read_b128 a[64:67], v241 offset:0\n"
    "ds_read_b128 a[68:71], v241 offset:2048\n"
    "ds_read_b128 a[72:75], v241 offset:4096\n"
    "ds_read_b128 a[76:79], v241 offset:6144\n"
    "ds_read_b128 a[80:83], v241 offset:8192\n"
    "ds_read_b128 a[84:87], v241 offset:10240\n"
    "ds_read_b128 a[88:91], v241 offset:12288\n"
    "ds_read_b128 a[92:95], v241 offset:14336\n"
    "ds_read_b128 a[96:99], v242 offset:0\n"
    "ds_read_b128 a[100:103], v242 offset:2048\n"
    "ds_read_b128 a[104:107], v242 offset:4096\n"
    "ds_read_b128 a[108:111], v242 offset:6144\n"
    "ds_read_b128 a[112:115], v242 offset:8192\n"
    "ds_read_b128 a[116:119], v242 offset:10240\n"
    "ds_read_b128 a[120:123], v242 offset:12288\n"
    "ds_read_b128 a[124:127], v242 offset:14336\n"
    "s_branch CR3_%=\n"
    "CP4_%=:\n"
    "s_cmp_lg_u32 s84, 4\n"
    "s_cbranch_scc1 CQ4_%=\n"
    "ds_read_b128 a[0:3], v241 offset:16384\n"
    "ds_read_b128 a[4:7], v241 offset:18432\n"
    "ds_read_b128 a[8:11], v241 offset:20480\n"
    "ds_read_b128 a[12:15], v241 offset:22528\n"
    "ds_read_b128 a[16:19], v241 offset:24576\n"
    "ds_read_b128 a[20:23], v241 offset:26624\n"
    "ds_read_b128 a[24:27], v241 offset:28672\n"
    "ds_read_b128 a[28:31], v241 offset:30720\n"
    "ds_read_b128 a[32:35], v242 offset:16384\n"
    "ds_read_b128 a[36:39], v242 offset:18432\n"
    "ds_read_b128 a[40:43], v242 offset:20480\n"
    "ds_read_b128 a[44:47], v242 offset:22528\n"
    "ds_read_b128 a[48:51], v242 offset:24576\n"
    "ds_read_b128 a[52:55], v242 offset:26624\n"
    "ds_read_b128 a[56:59], v242 offset:28672\n"
    "ds_read_b128 a[60:63], v242 offset:30720\n"
    "s_branch CR4_%=\n"
    "CQ4_%=:\n"
    "ds_read_b128 a[64:67], v241 offset:16384\n"
    "ds_read_b128 a[68:71], v241 offset:18432\n"
    "ds_read_b128 a[72:75], v241 offset:20480\n"
    "ds_read_b128 a[76:79], v241 offset:22528\n"
    "ds_read_b128 a[80:83], v241 offset:24576\n"
    "ds_read_b128 a[84:87], v241 offset:26624\n"
    "ds_read_b128 a[88:91], v241 offset:28672\n"
    "ds_read_b128 a[92:95], v241 offset:30720\n"
    "ds_read_b128 a[96:99], v242 offset:16384\n"
    "ds_read_b128 a[100:103], v242 offset:18432\n"
    "ds_read_b128 a[104:107], v242 offset:20480\n"
    "ds_read_b128 a[108:111], v242 offset:22528\n"
    "ds_read_b128 a[112:115], v242 offset:24576\n"
    "ds_read_b128 a[116:119], v242 offset:26624\n"
    "ds_read_b128 a[120:123], v242 offset:28672\n"
    "ds_read_b128 a[124:127], v242 offset:30720\n"
    "s_branch CR4_%=\n"
    "CP5_%=:\n"
    "s_cmp_lg_u32 s84, 5\n"
    "s_cbranch_scc1 CQ5_%=\n"
    "ds_read_b128 a[0:3], v241 offset:32768\n"
    "ds_read_b128 a[4:7], v241 offset:34816\n"
    "ds_read_b128 a[8:11], v241 offset:36864\n"
    "ds_read_b128 a[12:15], v241 offset:38912\n"
    "ds_read_b128 a[16:19], v241 offset:40960\n"
    "ds_read_b128 a[20:23], v241 offset:43008\n"
    "ds_read_b128 a[24:27], v241 offset:45056\n"
    "ds_read_b128 a[28:31], v241 offset:47104\n"
    "ds_read_b128 a[32:35], v242 offset:32768\n"
    "ds_read_b128 a[36:39], v242 offset:34816\n"
    "ds_read_b128 a[40:43], v242 offset:36864\n"
    "ds_read_b128 a[44:47], v242 offset:38912\n"
    "ds_read_b128 a[48:51], v242 offset:40960\n"
    "ds_read_b128 a[52:55], v242 offset:43008\n"
    "ds_read_b128 a[56:59], v242 offset:45056\n"
    "ds_read_b128 a[60:63], v242 offset:47104\n"
    "s_branch CR5_%=\n"
    "CQ5_%=:\n"
    "ds_read_b128 a[64:67], v241 offset:32768\n"
    "ds_read_b128 a[68:71], v241 offset:34816\n"
    "ds_read_b128 a[72:75], v241 offset:36864\n"
    "ds_read_b128 a[76:79], v241 offset:38912\n"
    "ds_read_b128 a[80:83], v241 offset:40960\n"
    "ds_read_b128 a[84:87], v241 offset:43008\n"
    "ds_read_b128 a[88:91], v241 offset:45056\n"
    "ds_read_b128 a[92:95], v241 offset:47104\n"
    "ds_read_b128 a[96:99], v242 offset:32768\n"
    "ds_read_b128 a[100:103], v242 offset:34816\n"
    "ds_read_b128 a[104:107], v242 offset:36864\n"
    "ds_read_b128 a[108:111], v242 offset:38912\n"
    "ds_read_b128 a[112:115], v242 offset:40960\n"
    "ds_read_b128 a[116:119], v242 offset:43008\n"
    "ds_read_b128 a[120:123], v242 offset:45056\n"
    "ds_read_b128 a[124:127], v242 offset:47104\n"
    "s_branch CR5_%=\n"
    "CP6_%=:\n"
    "s_cmp_lg_u32 s84, 6\n"
    "s_cbranch_scc1 CQ6_%=\n"
    "ds_read_b128 a[0:3], v241 offset:0\n"
    "ds_read_b128 a[4:7], v241 offset:2048\n"
    "ds_read_b128 a[8:11], v241 offset:4096\n"
    "ds_read_b128 a[12:15], v241 offset:6144\n"
    "ds_read_b128 a[16:19], v241 offset:8192\n"
    "ds_read_b128 a[20:23], v241 offset:10240\n"
    "ds_read_b128 a[24:27], v241 offset:12288\n"
    "ds_read_b128 a[28:31], v241 offset:14336\n"
    "ds_read_b128 a[32:35], v242 offset:0\n"
    "ds_read_b128 a[36:39], v242 offset:2048\n"
    "ds_read_b128 a[40:43], v242 offset:4096\n"
    "ds_read_b128 a[44:47], v242 offset:6144\n"
    "ds_read_b128 a[48:51], v242 offset:8192\n"
    "ds_read_b128 a[52:55], v242 offset:10240\n"
    "ds_read_b128 a[56:59], v242 offset:12288\n"
    "ds_read_b128 a[60:63], v242 offset:14336\n"
    "s_branch CR6_%=\n"
    "CQ6_%=:\n"
    "ds_read_b128 a[64:67], v241 offset:0\n"
    "ds_read_b128 a[68:71], v241 offset:2048\n"
    "ds_read_b128 a[72:75], v241 offset:4096\n"
    "ds_read_b128 a[76:79], v241 offset:6144\n"
    "ds_read_b128 a[80:83], v241 offset:8192\n"
    "ds_read_b128 a[84:87], v241 offset:10240\n"
    "ds_read_b128 a[88:91], v241 offset:12288\n"
    "ds_read_b128 a[92:95], v241 offset:14336\n"
    "ds_read_b128 a[96:99], v242 offset:0\n"
    "ds_read_b128 a[100:103], v242 offset:2048\n"
    "ds_read_b128 a[104:107], v242 offset:4096\n"
    "ds_read_b128 a[108:111], v242 offset:6144\n"
    "ds_read_b128 a[112:115], v242 offset:8192\n"
    "ds_read_b128 a[116:119], v242 offset:10240\n"
    "ds_read_b128 a[120:123], v242 offset:12288\n"
    "ds_read_b128 a[124:127], v242 offset:14336\n"
    "s_branch CR6_%=\n"
    "CP7_%=:\n"
    "s_cmp_lg_u32 s84, 7\n"
    "s_cbranch_scc1 CQ7_%=\n"
    "ds_read_b128 a[0:3], v241 offset:16384\n"
    "ds_read_b128 a[4:7], v241 offset:18432\n"
    "ds_read_b128 a[8:11], v241 offset:20480\n"
    "ds_read_b128 a[12:15], v241 offset:22528\n"
    "ds_read_b128 a[16:19], v241 offset:24576\n"
    "ds_read_b128 a[20:23], v241 offset:26624\n"
    "ds_read_b128 a[24:27], v241 offset:28672\n"
    "ds_read_b128 a[28:31], v241 offset:30720\n"
    "ds_read_b128 a[32:35], v242 offset:16384\n"
    "ds_read_b128 a[36:39], v242 offset:18432\n"
    "ds_read_b128 a[40:43], v242 offset:20480\n"
    "ds_read_b128 a[44:47], v242 offset:22528\n"
    "ds_read_b128 a[48:51], v242 offset:24576\n"
    "ds_read_b128 a[52:55], v242 offset:26624\n"
    "ds_read_b128 a[56:59], v242 offset:28672\n"
    "ds_read_b128 a[60:63], v242 offset:30720\n"
    "s_branch CR7_%=\n"
    "CQ7_%=:\n"
    "ds_read_b128 a[64:67], v241 offset:16384\n"
    "ds_read_b128 a[68:71], v241 offset:18432\n"
    "ds_read_b128 a[72:75], v241 offset:20480\n"
    "ds_read_b128 a[76:79], v241 offset:22528\n"
    "ds_read_b128 a[80:83], v241 offset:24576\n"
    "ds_read_b128 a[84:87], v241 offset:26624\n"
    "ds_read_b128 a[88:91], v241 offset:28672\n"
    "ds_read_b128 a[92:95], v241 offset:30720\n"
    "ds_read_b128 a[96:99], v242 offset:16384\n"
    "ds_read_b128 a[100:103], v242 offset:18432\n"
    "ds_read_b128 a[104:107], v242 offset:20480\n"
    "ds_read_b128 a[108:111], v242 offset:22528\n"
    "ds_read_b128 a[112:115], v242 offset:24576\n"
    "ds_read_b128 a[116:119], v242 offset:26624\n"
    "ds_read_b128 a[120:123], v242 offset:28672\n"
    "ds_read_b128 a[124:127], v242 offset:30720\n"
    "s_branch CR7_%=\n"
    "CP8_%=:\n"
    "s_cmp_lg_u32 s84, 8\n"
    "s_cbranch_scc1 CQ8_%=\n"
    "ds_read_b128 a[0:3], v241 offset:32768\n"
    "ds_read_b128 a[4:7], v241 offset:34816\n"
    "ds_read_b128 a[8:11], v241 offset:36864\n"
    "ds_read_b128 a[12:15], v241 offset:38912\n"
    "ds_read_b128 a[16:19], v241 offset:40960\n"
    "ds_read_b128 a[20:23], v241 offset:43008\n"
    "ds_read_b128 a[24:27], v241 offset:45056\n"
    "ds_read_b128 a[28:31], v241 offset:47104\n"
    "ds_read_b128 a[32:35], v242 offset:32768\n"
    "ds_read_b128 a[36:39], v242 offset:34816\n"
    "ds_read_b128 a[40:43], v242 offset:36864\n"
    "ds_read_b128 a[44:47], v242 offset:38912\n"
    "ds_read_b128 a[48:51], v242 offset:40960\n"
    "ds_read_b128 a[52:55], v242 offset:43008\n"
    "ds_read_b128 a[56:59], v242 offset:45056\n"
    "ds_read_b128 a[60:63], v242 offset:47104\n"
    "s_branch CR8_%=\n"
    "CQ8_%=:\n"
    "ds_read_b128 a[64:67], v241 offset:32768\n"
    "ds_read_b128 a[68:71], v241 offset:34816\n"
    "ds_read_b128 a[72:75], v241 offset:36864\n"
    "ds_read_b128 a[76:79], v241 offset:38912\n"
    "ds_read_b128 a[80:83], v241 offset:40960\n"
    "ds_read_b128 a[84:87], v241 offset:43008\n"
    "ds_read_b128 a[88:91], v241 offset:45056\n"
    "ds_read_b128 a[92:95], v241 offset:47104\n"
    "ds_read_b128 a[96:99], v242 offset:32768\n"
    "ds_read_b128 a[100:103], v242 offset:34816\n"
    "ds_read_b128 a[104:107], v242 offset:36864\n"
    "ds_read_b128 a[108:111], v242 offset:38912\n"
    "ds_read_b128 a[112:115], v242 offset:40960\n"
    "ds_read_b128 a[116:119], v242 offset:43008\n"
    "ds_read_b128 a[120:123], v242 offset:45056\n"
    "ds_read_b128 a[124:127], v242 offset:47104\n"
    "s_branch CR8_%=\n"
    "CP9_%=:\n"
    "s_cmp_lg_u32 s84, 9\n"
    "s_cbranch_scc1 CQ9_%=\n"
    "ds_read_b128 a[0:3], v241 offset:0\n"
    "ds_read_b128 a[4:7], v241 offset:2048\n"
    "ds_read_b128 a[8:11], v241 offset:4096\n"
    "ds_read_b128 a[12:15], v241 offset:6144\n"
    "ds_read_b128 a[16:19], v241 offset:8192\n"
    "ds_read_b128 a[20:23], v241 offset:10240\n"
    "ds_read_b128 a[24:27], v241 offset:12288\n"
    "ds_read_b128 a[28:31], v241 offset:14336\n"
    "ds_read_b128 a[32:35], v242 offset:0\n"
    "ds_read_b128 a[36:39], v242 offset:2048\n"
    "ds_read_b128 a[40:43], v242 offset:4096\n"
    "ds_read_b128 a[44:47], v242 offset:6144\n"
    "ds_read_b128 a[48:51], v242 offset:8192\n"
    "ds_read_b128 a[52:55], v242 offset:10240\n"
    "ds_read_b128 a[56:59], v242 offset:12288\n"
    "ds_read_b128 a[60:63], v242 offset:14336\n"
    "s_branch CR9_%=\n"
    "CQ9_%=:\n"
    "ds_read_b128 a[64:67], v241 offset:0\n"
    "ds_read_b128 a[68:71], v241 offset:2048\n"
    "ds_read_b128 a[72:75], v241 offset:4096\n"
    "ds_read_b128 a[76:79], v241 offset:6144\n"
    "ds_read_b128 a[80:83], v241 offset:8192\n"
    "ds_read_b128 a[84:87], v241 offset:10240\n"
    "ds_read_b128 a[88:91], v241 offset:12288\n"
    "ds_read_b128 a[92:95], v241 offset:14336\n"
    "ds_read_b128 a[96:99], v242 offset:0\n"
    "ds_read_b128 a[100:103], v242 offset:2048\n"
    "ds_read_b128 a[104:107], v242 offset:4096\n"
    "ds_read_b128 a[108:111], v242 offset:6144\n"
    "ds_read_b128 a[112:115], v242 offset:8192\n"
    "ds_read_b128 a[116:119], v242 offset:10240\n"
    "ds_read_b128 a[120:123], v242 offset:12288\n"
    "ds_read_b128 a[124:127], v242 offset:14336\n"
    "s_branch CR9_%=\n"
    "CP10_%=:\n"
    "s_cmp_lg_u32 s84, 10\n"
    "s_cbranch_scc1 CQ10_%=\n"
    "ds_read_b128 a[0:3], v241 offset:16384\n"
    "ds_read_b128 a[4:7], v241 offset:18432\n"
    "ds_read_b128 a[8:11], v241 offset:20480\n"
    "ds_read_b128 a[12:15], v241 offset:22528\n"
    "ds_read_b128 a[16:19], v241 offset:24576\n"
    "ds_read_b128 a[20:23], v241 offset:26624\n"
    "ds_read_b128 a[24:27], v241 offset:28672\n"
    "ds_read_b128 a[28:31], v241 offset:30720\n"
    "ds_read_b128 a[32:35], v242 offset:16384\n"
    "ds_read_b128 a[36:39], v242 offset:18432\n"
    "ds_read_b128 a[40:43], v242 offset:20480\n"
    "ds_read_b128 a[44:47], v242 offset:22528\n"
    "ds_read_b128 a[48:51], v242 offset:24576\n"
    "ds_read_b128 a[52:55], v242 offset:26624\n"
    "ds_read_b128 a[56:59], v242 offset:28672\n"
    "ds_read_b128 a[60:63], v242 offset:30720\n"
    "s_branch CR10_%=\n"
    "CQ10_%=:\n"
    "ds_read_b128 a[64:67], v241 offset:16384\n"
    "ds_read_b128 a[68:71], v241 offset:18432\n"
    "ds_read_b128 a[72:75], v241 offset:20480\n"
    "ds_read_b128 a[76:79], v241 offset:22528\n"
    "ds_read_b128 a[80:83], v241 offset:24576\n"
    "ds_read_b128 a[84:87], v241 offset:26624\n"
    "ds_read_b128 a[88:91], v241 offset:28672\n"
    "ds_read_b128 a[92:95], v241 offset:30720\n"
    "ds_read_b128 a[96:99], v242 offset:16384\n"
    "ds_read_b128 a[100:103], v242 offset:18432\n"
    "ds_read_b128 a[104:107], v242 offset:20480\n"
    "ds_read_b128 a[108:111], v242 offset:22528\n"
    "ds_read_b128 a[112:115], v242 offset:24576\n"
    "ds_read_b128 a[116:119], v242 offset:26624\n"
    "ds_read_b128 a[120:123], v242 offset:28672\n"
    "ds_read_b128 a[124:127], v242 offset:30720\n"
    "s_branch CR10_%=\n"
    "CP11_%=:\n"
    "s_cmp_lg_u32 s84, 11\n"
    "s_cbranch_scc1 CQ11_%=\n"
    "ds_read_b128 a[0:3], v241 offset:32768\n"
    "ds_read_b128 a[4:7], v241 offset:34816\n"
    "ds_read_b128 a[8:11], v241 offset:36864\n"
    "ds_read_b128 a[12:15], v241 offset:38912\n"
    "ds_read_b128 a[16:19], v241 offset:40960\n"
    "ds_read_b128 a[20:23], v241 offset:43008\n"
    "ds_read_b128 a[24:27], v241 offset:45056\n"
    "ds_read_b128 a[28:31], v241 offset:47104\n"
    "ds_read_b128 a[32:35], v242 offset:32768\n"
    "ds_read_b128 a[36:39], v242 offset:34816\n"
    "ds_read_b128 a[40:43], v242 offset:36864\n"
    "ds_read_b128 a[44:47], v242 offset:38912\n"
    "ds_read_b128 a[48:51], v242 offset:40960\n"
    "ds_read_b128 a[52:55], v242 offset:43008\n"
    "ds_read_b128 a[56:59], v242 offset:45056\n"
    "ds_read_b128 a[60:63], v242 offset:47104\n"
    "s_branch CR11_%=\n"
    "CQ11_%=:\n"
    "ds_read_b128 a[64:67], v241 offset:32768\n"
    "ds_read_b128 a[68:71], v241 offset:34816\n"
    "ds_read_b128 a[72:75], v241 offset:36864\n"
    "ds_read_b128 a[76:79], v241 offset:38912\n"
    "ds_read_b128 a[80:83], v241 offset:40960\n"
    "ds_read_b128 a[84:87], v241 offset:43008\n"
    "ds_read_b128 a[88:91], v241 offset:45056\n"
    "ds_read_b128 a[92:95], v241 offset:47104\n"
    "ds_read_b128 a[96:99], v242 offset:32768\n"
    "ds_read_b128 a[100:103], v242 offset:34816\n"
    "ds_read_b128 a[104:107], v242 offset:36864\n"
    "ds_read_b128 a[108:111], v242 offset:38912\n"
    "ds_read_b128 a[112:115], v242 offset:40960\n"
    "ds_read_b128 a[116:119], v242 offset:43008\n"
    "ds_read_b128 a[120:123], v242 offset:45056\n"
    "ds_read_b128 a[124:127], v242 offset:47104\n"
    "s_branch CR11_%=\n"
    "CP12_%=:\n"
    "s_cmp_lg_u32 s84, 12\n"
    "s_cbranch_scc1 CQ12_%=\n"
    "ds_read_b128 a[0:3], v241 offset:0\n"
    "ds_read_b128 a[4:7], v241 offset:2048\n"
    "ds_read_b128 a[8:11], v241 offset:4096\n"
    "ds_read_b128 a[12:15], v241 offset:6144\n"
    "ds_read_b128 a[16:19], v241 offset:8192\n"
    "ds_read_b128 a[20:23], v241 offset:10240\n"
    "ds_read_b128 a[24:27], v241 offset:12288\n"
    "ds_read_b128 a[28:31], v241 offset:14336\n"
    "ds_read_b128 a[32:35], v242 offset:0\n"
    "ds_read_b128 a[36:39], v242 offset:2048\n"
    "ds_read_b128 a[40:43], v242 offset:4096\n"
    "ds_read_b128 a[44:47], v242 offset:6144\n"
    "ds_read_b128 a[48:51], v242 offset:8192\n"
    "ds_read_b128 a[52:55], v242 offset:10240\n"
    "ds_read_b128 a[56:59], v242 offset:12288\n"
    "ds_read_b128 a[60:63], v242 offset:14336\n"
    "s_branch CR12_%=\n"
    "CQ12_%=:\n"
    "ds_read_b128 a[64:67], v241 offset:0\n"
    "ds_read_b128 a[68:71], v241 offset:2048\n"
    "ds_read_b128 a[72:75], v241 offset:4096\n"
    "ds_read_b128 a[76:79], v241 offset:6144\n"
    "ds_read_b128 a[80:83], v241 offset:8192\n"
    "ds_read_b128 a[84:87], v241 offset:10240\n"
    "ds_read_b128 a[88:91], v241 offset:12288\n"
    "ds_read_b128 a[92:95], v241 offset:14336\n"
    "ds_read_b128 a[96:99], v242 offset:0\n"
    "ds_read_b128 a[100:103], v242 offset:2048\n"
    "ds_read_b128 a[104:107], v242 offset:4096\n"
    "ds_read_b128 a[108:111], v242 offset:6144\n"
    "ds_read_b128 a[112:115], v242 offset:8192\n"
    "ds_read_b128 a[116:119], v242 offset:10240\n"
    "ds_read_b128 a[120:123], v242 offset:12288\n"
    "ds_read_b128 a[124:127], v242 offset:14336\n"
    "s_branch CR12_%=\n"
    "CP13_%=:\n"
    "s_cmp_lg_u32 s84, 13\n"
    "s_cbranch_scc1 CQ13_%=\n"
    "ds_read_b128 a[0:3], v241 offset:16384\n"
    "ds_read_b128 a[4:7], v241 offset:18432\n"
    "ds_read_b128 a[8:11], v241 offset:20480\n"
    "ds_read_b128 a[12:15], v241 offset:22528\n"
    "ds_read_b128 a[16:19], v241 offset:24576\n"
    "ds_read_b128 a[20:23], v241 offset:26624\n"
    "ds_read_b128 a[24:27], v241 offset:28672\n"
    "ds_read_b128 a[28:31], v241 offset:30720\n"
    "ds_read_b128 a[32:35], v242 offset:16384\n"
    "ds_read_b128 a[36:39], v242 offset:18432\n"
    "ds_read_b128 a[40:43], v242 offset:20480\n"
    "ds_read_b128 a[44:47], v242 offset:22528\n"
    "ds_read_b128 a[48:51], v242 offset:24576\n"
    "ds_read_b128 a[52:55], v242 offset:26624\n"
    "ds_read_b128 a[56:59], v242 offset:28672\n"
    "ds_read_b128 a[60:63], v242 offset:30720\n"
    "s_branch CR13_%=\n"
    "CQ13_%=:\n"
    "ds_read_b128 a[64:67], v241 offset:16384\n"
    "ds_read_b128 a[68:71], v241 offset:18432\n"
    "ds_read_b128 a[72:75], v241 offset:20480\n"
    "ds_read_b128 a[76:79], v241 offset:22528\n"
    "ds_read_b128 a[80:83], v241 offset:24576\n"
    "ds_read_b128 a[84:87], v241 offset:26624\n"
    "ds_read_b128 a[88:91], v241 offset:28672\n"
    "ds_read_b128 a[92:95], v241 offset:30720\n"
    "ds_read_b128 a[96:99], v242 offset:16384\n"
    "ds_read_b128 a[100:103], v242 offset:18432\n"
    "ds_read_b128 a[104:107], v242 offset:20480\n"
    "ds_read_b128 a[108:111], v242 offset:22528\n"
    "ds_read_b128 a[112:115], v242 offset:24576\n"
    "ds_read_b128 a[116:119], v242 offset:26624\n"
    "ds_read_b128 a[120:123], v242 offset:28672\n"
    "ds_read_b128 a[124:127], v242 offset:30720\n"
    "s_branch CR13_%=\n"
    "CP14_%=:\n"
    "s_cmp_lg_u32 s84, 14\n"
    "s_cbranch_scc1 CQ14_%=\n"
    "ds_read_b128 a[0:3], v241 offset:49152\n"
    "ds_read_b128 a[4:7], v241 offset:51200\n"
    "ds_read_b128 a[8:11], v241 offset:53248\n"
    "ds_read_b128 a[12:15], v241 offset:55296\n"
    "ds_read_b128 a[16:19], v241 offset:57344\n"
    "ds_read_b128 a[20:23], v241 offset:59392\n"
    "ds_read_b128 a[24:27], v241 offset:61440\n"
    "ds_read_b128 a[28:31], v241 offset:63488\n"
    "ds_read_b128 a[32:35], v242 offset:49152\n"
    "ds_read_b128 a[36:39], v242 offset:51200\n"
    "ds_read_b128 a[40:43], v242 offset:53248\n"
    "ds_read_b128 a[44:47], v242 offset:55296\n"
    "ds_read_b128 a[48:51], v242 offset:57344\n"
    "ds_read_b128 a[52:55], v242 offset:59392\n"
    "ds_read_b128 a[56:59], v242 offset:61440\n"
    "ds_read_b128 a[60:63], v242 offset:63488\n"
    "s_branch CR14_%=\n"
    "CQ14_%=:\n"
    "ds_read_b128 a[64:67], v241 offset:49152\n"
    "ds_read_b128 a[68:71], v241 offset:51200\n"
    "ds_read_b128 a[72:75], v241 offset:53248\n"
    "ds_read_b128 a[76:79], v241 offset:55296\n"
    "ds_read_b128 a[80:83], v241 offset:57344\n"
    "ds_read_b128 a[84:87], v241 offset:59392\n"
    "ds_read_b128 a[88:91], v241 offset:61440\n"
    "ds_read_b128 a[92:95], v241 offset:63488\n"
    "ds_read_b128 a[96:99], v242 offset:49152\n"
    "ds_read_b128 a[100:103], v242 offset:51200\n"
    "ds_read_b128 a[104:107], v242 offset:53248\n"
    "ds_read_b128 a[108:111], v242 offset:55296\n"
    "ds_read_b128 a[112:115], v242 offset:57344\n"
    "ds_read_b128 a[116:119], v242 offset:59392\n"
    "ds_read_b128 a[120:123], v242 offset:61440\n"
    "ds_read_b128 a[124:127], v242 offset:63488\n"
    "s_branch CR14_%=\n"
    "CP15_%=:\n"
    "s_cmp_lg_u32 s84, 15\n"
    "s_cbranch_scc1 CQ15_%=\n"
    "ds_read_b128 a[0:3], v241 offset:32768\n"
    "ds_read_b128 a[4:7], v241 offset:34816\n"
    "ds_read_b128 a[8:11], v241 offset:36864\n"
    "ds_read_b128 a[12:15], v241 offset:38912\n"
    "ds_read_b128 a[16:19], v241 offset:40960\n"
    "ds_read_b128 a[20:23], v241 offset:43008\n"
    "ds_read_b128 a[24:27], v241 offset:45056\n"
    "ds_read_b128 a[28:31], v241 offset:47104\n"
    "ds_read_b128 a[32:35], v242 offset:32768\n"
    "ds_read_b128 a[36:39], v242 offset:34816\n"
    "ds_read_b128 a[40:43], v242 offset:36864\n"
    "ds_read_b128 a[44:47], v242 offset:38912\n"
    "ds_read_b128 a[48:51], v242 offset:40960\n"
    "ds_read_b128 a[52:55], v242 offset:43008\n"
    "ds_read_b128 a[56:59], v242 offset:45056\n"
    "ds_read_b128 a[60:63], v242 offset:47104\n"
    "s_branch CR15_%=\n"
    "CQ15_%=:\n"
    "ds_read_b128 a[64:67], v241 offset:32768\n"
    "ds_read_b128 a[68:71], v241 offset:34816\n"
    "ds_read_b128 a[72:75], v241 offset:36864\n"
    "ds_read_b128 a[76:79], v241 offset:38912\n"
    "ds_read_b128 a[80:83], v241 offset:40960\n"
    "ds_read_b128 a[84:87], v241 offset:43008\n"
    "ds_read_b128 a[88:91], v241 offset:45056\n"
    "ds_read_b128 a[92:95], v241 offset:47104\n"
    "ds_read_b128 a[96:99], v242 offset:32768\n"
    "ds_read_b128 a[100:103], v242 offset:34816\n"
    "ds_read_b128 a[104:107], v242 offset:36864\n"
    "ds_read_b128 a[108:111], v242 offset:38912\n"
    "ds_read_b128 a[112:115], v242 offset:40960\n"
    "ds_read_b128 a[116:119], v242 offset:43008\n"
    "ds_read_b128 a[120:123], v242 offset:45056\n"
    "ds_read_b128 a[124:127], v242 offset:47104\n"
    "s_branch CR15_%=\n"
    "OA_%=:\n"
    "s_cmp_eq_u32 s52, 2\n"
    "s_cbranch_scc1 OC_%=\n"
    "s_lshr_b32 s60, %3, 3\n"
    "s_lshl_b32 s60, s60, 2\n"
    "s_add_u32 s60, s60, s65\n"
    "s_lshl_b32 s60, s60, 10\n"
    "s_min_u32 s60, s60, CODE_END_%= - LAST_%= - 1024\n"
    "s_getpc_b64 s[62:63]\n"
    "LA9001_%=:\n"
    "s_add_u32 s62, s62, LAST_%= - LA9001_%=\n"
    "s_addc_u32 s63, s63, 0\n"
    "s_add_u32 s62, s62, s60\n"
    "s_addc_u32 s63, s63, 0\n"
    "global_load_dwordx4 v[244:247], v240, s[62:63]\n"
    "s_branch OB_%=\n"
    "OC_%=:\n"
    "buffer_load_dwordx4 v[248:251], v240, s[24:27], 0 offen sc1\n"
    "s_branch OB_%=\n"
    "OD_%=:\n"
    "s_cmp_eq_u32 s52, 10\n"
    "s_cbranch_scc1 LAST_%=\n"
    "v_and_b32 v244, v248, v249\n"
    "v_and_b32 v245, v250, v251\n"
    "v_and_b32 v244, v244, v245\n"
    "v_and_b32 v244, 1, v244\n"
    "v_cmp_ne_u32 vcc, 1, v244\n"
    "s_nop 1\n"
    "s_cmp_eq_u64 vcc, 0\n"
    "s_cbranch_scc1 LA9002_%=\n"
    "s_mov_b32 s69, 0x400000\n"
    "LA9003_%=:\n"
    "s_sleep 2\n"
    "buffer_load_dwordx4 v[248:251], v240, s[24:27], 0 offen sc1\n"
    "s_waitcnt vmcnt(0)\n"
    "s_sub_u32 s69, s69, 1\n"
    "s_cbranch_scc1 LA9002_%=\n"
    "v_and_b32 v244, v248, v249\n"
    "v_and_b32 v245, v250, v251\n"
    "v_and_b32 v244, v244, v245\n"
    "v_and_b32 v244, 1, v244\n"
    "v_cmp_ne_u32 vcc, 1, v244\n"
    "s_nop 1\n"
    "s_cmp_eq_u64 vcc, 0\n"
    "s_cbranch_scc0 LA9003_%=\n"
    "LA9002_%=:\n"
    "v_add_u32 v248, v248, v249\n"
    "v_add3_u32 v248, v248, v250, v251\n"
    "s_lshr_b32 s60, %0, 2\n"
    "s_xor_b32 s61, s60, 2\n"
    "v_and_b32 v249, 0xfffff, v248\n"
    "v_readlane_b32 s62, v248, s60\n"
    "v_readlane_b32 s63, v248, s61\n"
    "v_add_u32_dpp v249, v249, v249 row_ror:8 row_mask:0xf bank_mask:0xf\n"
    "s_nop 1\n"
    "v_add_u32_dpp v249, v249, v249 row_ror:4 row_mask:0xf bank_mask:0xf\n"
    "s_nop 1\n"
    "v_add_u32_dpp v249, v249, v249 row_ror:2 row_mask:0xf bank_mask:0xf\n"
    "s_nop 1\n"
    "v_add_u32_dpp v249, v249, v249 row_ror:1 row_mask:0xf bank_mask:0xf\n"
    "s_nop 1\n"
    "s_add_u32 s62, s62, s63\n"
    "s_lshr_b32 s62, s62, 20\n"
    "s_cmp_gt_u32 s62, 2048\n"
    "s_cselect_b32 s64, 1, 0\n"
    "v_readlane_b32 s62, v249, s60\n"
    "s_cmp_gt_u32 s62, 262208\n"
    "s_cselect_b32 s62, 524288, 0\n"
    "s_lshl_b32 s54, %3, 19\n"
    "s_xor_b32 s54, s54, s62\n"
    "s_xor_b32 s59, s54, 4194304\n"
    "s_lshl_b32 s62, s64, 13\n"
    "s_xor_b32 s54, s54, s62\n"
    "s_branch OE_%=\n"
    "LAST_%=:\n"
    "s_waitcnt vmcnt(10) lgkmcnt(14)\n"
    "v_mfma_f32_16x16x32_bf16 v[224:227], a[192:195], a[156:159], v[224:227]\n"
    "v_mfma_f32_16x16x32_bf16 v[224:227], a[196:199], a[188:191], v[224:227]\n"
    "s_waitcnt lgkmcnt(12)\n"
    "v_mfma_f32_16x16x32_bf16 v[192:195], a[196:199], a[184:187], v[192:195]\n"
    "v_mfma_f32_16x16x32_bf16 v[192:195], a[192:195], a[152:155], v[192:195]\n"
    "s_waitcnt lgkmcnt(10)\n"
    "v_mfma_f32_16x16x32_bf16 v[160:163], a[192:195], a[148:151], v[160:163]\n"
    "v_mfma_f32_16x16x32_bf16 v[160:163], a[196:199], a[180:183], v[160:163]\n"
    "s_waitcnt lgkmcnt(8)\n"
    "v_mfma_f32_16x16x32_bf16 v[128:131], a[196:199], a[176:179], v[128:131]\n"
    "s_mov_b32 m0, s56\n"
    "s_xor_b32 s90, s90, 0x195800\n"
    "v_mfma_f32_16x16x32_bf16 v[128:131], a[192:195], a[144:147], v[128:131]\n"
    "s_waitcnt lgkmcnt(6)\n"
    "v_mfma_f32_16x16x32_bf16 v[96:99], a[192:195], a[140:143], v[96:99]\n"
    "buffer_load_dwordx4 v240, s[32:35], s90 offen sc1 lds\n"
    "v_mfma_f32_16x16x32_bf16 v[96:99], a[196:199], a[172:175], v[96:99]\n"
    "s_waitcnt lgkmcnt(4)\n"
    "v_mfma_f32_16x16x32_bf16 v[64:67], a[196:199], a[168:171], v[64:67]\n"
    "v_mfma_f32_16x16x32_bf16 v[64:67], a[192:195], a[136:139], v[64:67]\n"
    "s_waitcnt lgkmcnt(2)\n"
    "v_mfma_f32_16x16x32_bf16 v[32:35], a[192:195], a[132:135], v[32:35]\n"
    "v_mfma_f32_16x16x32_bf16 v[32:35], a[196:199], a[164:167], v[32:35]\n"
    "s_waitcnt lgkmcnt(0)\n"
    "v_mfma_f32_16x16x32_bf16 v[0:3], a[196:199], a[160:163], v[0:3]\n"
    "v_mfma_f32_16x16x32_bf16 v[0:3], a[192:195], a[128:131], v[0:3]\n"
    "s_waitcnt vmcnt(9)\n"
    "v_mfma_f32_16x16x32_bf16 v[4:7], a[200:203], a[128:131], v[4:7]\n"
    "buffer_load_dwordx4 a[192:195], v240, s[20:23], s53 offen offset:0 sc1\n"
    "v_mfma_f32_16x16x32_bf16 v[4:7], a[204:207], a[160:163], v[4:7]\n"
    "buffer_load_dwordx4 a[196:199], v240, s[20:23], s53 offen offset:1024 sc1\n"
    "v_mfma_f32_16x16x32_bf16 v[36:39], a[204:207], a[164:167], v[36:39]\n"
    "v_mfma_f32_16x16x32_bf16 v[36:39], a[200:203], a[132:135], v[36:39]\n"
    "v_mfma_f32_16x16x32_bf16 v[68:71], a[200:203], a[136:139], v[68:71]\n"
    "v_mfma_f32_16x16x32_bf16 v[68:71], a[204:207], a[168:171], v[68:71]\n"
    "v_mfma_f32_16x16x32_bf16 v[100:103], a[204:207], a[172:175], v[100:103]\n"
    "s_addk_i32 m0, 2048\n"
    "s_xor_b32 s90, s90, 0x800\n"
    "v_mfma_f32_16x16x32_bf16 v[100:103], a[200:203], a[140:143], v[100:103]\n"
    "buffer_load_dwordx4 v240, s[32:35], s90 offen sc1 lds\n"
    "v_mfma_f32_16x16x32_bf16 v[132:135], a[200:203], a[144:147], v[132:135]\n"
    "v_mfma_f32_16x16x32_bf16 v[132:135], a[204:207], a[176:179], v[132:135]\n"
    "v_mfma_f32_16x16x32_bf16 v[164:167], a[204:207], a[180:183], v[164:167]\n"
    "v_mfma_f32_16x16x32_bf16 v[164:167], a[200:203], a[148:151], v[164:167]\n"
    "v_mfma_f32_16x16x32_bf16 v[196:199], a[200:203], a[152:155], v[196:199]\n"
    "v_mfma_f32_16x16x32_bf16 v[196:199], a[204:207], a[184:187], v[196:199]\n"
    "v_mfma_f32_16x16x32_bf16 v[228:231], a[204:207], a[188:191], v[228:231]\n"
    "v_mfma_f32_16x16x32_bf16 v[228:231], a[200:203], a[156:159], v[228:231]\n"
    "s_waitcnt vmcnt(10)\n"
    "v_mfma_f32_16x16x32_bf16 v[232:235], a[208:211], a[156:159], v[232:235]\n"
    "buffer_load_dwordx4 a[200:203], v240, s[20:23], s53 offen offset:2048 sc1\n"
    "v_mfma_f32_16x16x32_bf16 v[232:235], a[212:215], a[188:191], v[232:235]\n"
    "buffer_load_dwordx4 a[204:207], v240, s[20:23], s53 offen offset:3072 sc1\n"
    "v_mfma_f32_16x16x32_bf16 v[200:203], a[212:215], a[184:187], v[200:203]\n"
    "v_mfma_f32_16x16x32_bf16 v[200:203], a[208:211], a[152:155], v[200:203]\n"
    "v_mfma_f32_16x16x32_bf16 v[168:171], a[208:211], a[148:151], v[168:171]\n"
    "v_mfma_f32_16x16x32_bf16 v[168:171], a[212:215], a[180:183], v[168:171]\n"
    "v_mfma_f32_16x16x32_bf16 v[136:139], a[212:215], a[176:179], v[136:139]\n"
    "s_addk_i32 m0, 2048\n"
    "s_xor_b32 s90, s90, 0x101800\n"
    "v_mfma_f32_16x16x32_bf16 v[136:139], a[208:211], a[144:147], v[136:139]\n"
    "buffer_load_dwordx4 v240, s[32:35], s90 offen sc1 lds\n"
    "v_mfma_f32_16x16x32_bf16 v[104:107], a[208:211], a[140:143], v[104:107]\n"
    "v_mfma_f32_16x16x32_bf16 v[104:107], a[212:215], a[172:175], v[104:107]\n"
    "v_mfma_f32_16x16x32_bf16 v[72:75], a[212:215], a[168:171], v[72:75]\n"
    "v_mfma_f32_16x16x32_bf16 v[72:75], a[208:211], a[136:139], v[72:75]\n"
    "v_mfma_f32_16x16x32_bf16 v[40:43], a[208:211], a[132:135], v[40:43]\n"
    "v_mfma_f32_16x16x32_bf16 v[40:43], a[212:215], a[164:167], v[40:43]\n"
    "v_mfma_f32_16x16x32_bf16 v[8:11], a[212:215], a[160:163], v[8:11]\n"
    "v_mfma_f32_16x16x32_bf16 v[8:11], a[208:211], a[128:131], v[8:11]\n"
    "s_waitcnt vmcnt(11)\n"
    "s_addk_i32 s53, 4096\n"
    "v_mfma_f32_16x16x32_bf16 v[12:15], a[216:219], a[128:131], v[12:15]\n"
    "buffer_load_dwordx4 a[208:211], v240, s[20:23], s53 offen offset:0 sc1\n"
    "v_mfma_f32_16x16x32_bf16 v[12:15], a[220:223], a[160:163], v[12:15]\n"
    "buffer_load_dwordx4 a[212:215], v240, s[20:23], s53 offen offset:1024 sc1\n"
    "v_mfma_f32_16x16x32_bf16 v[44:47], a[220:223], a[164:167], v[44:47]\n"
    "v_mfma_f32_16x16x32_bf16 v[44:47], a[216:219], a[132:135], v[44:47]\n"
    "v_mfma_f32_16x16x32_bf16 v[76:79], a[216:219], a[136:139], v[76:79]\n"
    "v_mfma_f32_16x16x32_bf16 v[76:79], a[220:223], a[168:171], v[76:79]\n"
    "v_mfma_f32_16x16x32_bf16 v[108:111], a[220:223], a[172:175], v[108:111]\n"
    "s_addk_i32 m0, 2048\n"
    "s_xor_b32 s90, s90, 0x800\n"
    "v_mfma_f32_16x16x32_bf16 v[108:111], a[216:219], a[140:143], v[108:111]\n"
    "buffer_load_dwordx4 v240, s[32:35], s90 offen sc1 lds\n"
    "v_mfma_f32_16x16x32_bf16 v[140:143], a[216:219], a[144:147], v[140:143]\n"
    "v_mfma_f32_16x16x32_bf16 v[140:143], a[220:223], a[176:179], v[140:143]\n"
    "v_mfma_f32_16x16x32_bf16 v[172:175], a[220:223], a[180:183], v[172:175]\n"
    "v_mfma_f32_16x16x32_bf16 v[172:175], a[216:219], a[148:151], v[172:175]\n"
    "v_mfma_f32_16x16x32_bf16 v[204:207], a[216:219], a[152:155], v[204:207]\n"
    "v_mfma_f32_16x16x32_bf16 v[204:207], a[220:223], a[184:187], v[204:207]\n"
    "v_mfma_f32_16x16x32_bf16 v[236:239], a[220:223], a[188:191], v[236:239]\n"
    "v_mfma_f32_16x16x32_bf16 v[236:239], a[216:219], a[156:159], v[236:239]\n"
    "s_waitcnt vmcnt(12)\n"
    "v_mfma_f32_16x16x32_bf16 a[240:243], a[224:227], a[156:159], a[240:243]\n"
    "buffer_load_dwordx4 a[216:219], v240, s[20:23], s53 offen offset:2048 sc1\n"
    "v_mfma_f32_16x16x32_bf16 a[240:243], a[228:231], a[188:191], a[240:243]\n"
    "buffer_load_dwordx4 a[220:223], v240, s[20:23], s53 offen offset:3072 sc1\n"
    "v_mfma_f32_16x16x32_bf16 v[208:211], a[228:231], a[184:187], v[208:211]\n"
    "v_mfma_f32_16x16x32_bf16 v[208:211], a[224:227], a[152:155], v[208:211]\n"
    "v_mfma_f32_16x16x32_bf16 v[176:179], a[224:227], a[148:151], v[176:179]\n"
    "v_mfma_f32_16x16x32_bf16 v[176:179], a[228:231], a[180:183], v[176:179]\n"
    "v_mfma_f32_16x16x32_bf16 v[144:147], a[228:231], a[176:179], v[144:147]\n"
    "v_mfma_f32_16x16x32_bf16 v[144:147], a[224:227], a[144:147], v[144:147]\n"
    "v_mfma_f32_16x16x32_bf16 v[112:115], a[224:227], a[140:143], v[112:115]\n"
    "v_mfma_f32_16x16x32_bf16 v[112:115], a[228:231], a[172:175], v[112:115]\n"
    "v_mfma_f32_16x16x32_bf16 v[80:83], a[228:231], a[168:171], v[80:83]\n"
    "v_mfma_f32_16x16x32_bf16 v[80:83], a[224:227], a[136:139], v[80:83]\n"
    "v_mfma_f32_16x16x32_bf16 v[48:51], a[224:227], a[132:135], v[48:51]\n"
    "s_addk_i32 m0, 2048\n"
    "s_xor_b32 s90, s90, 0x185800\n"
    "v_mfma_f32_16x16x32_bf16 v[48:51], a[228:231], a[164:167], v[48:51]\n"
    "buffer_load_dwordx4 v240, s[32:35], s90 offen sc1 lds\n"
    "v_mfma_f32_16x16x32_bf16 v[16:19], a[228:231], a[160:163], v[16:19]\n"
    "v_mfma_f32_16x16x32_bf16 v[16:19], a[224:227], a[128:131], v[16:19]\n"
    "s_waitcnt vmcnt(13)\n"
    "s_addk_i32 s53, 4096\n"
    "v_mfma_f32_16x16x32_bf16 v[20:23], a[232:235], a[128:131], v[20:23]\n"
    "buffer_load_dwordx4 a[224:227], v240, s[20:23], s53 offen offset:0 sc1\n"
    "v_mfma_f32_16x16x32_bf16 v[20:23], a[236:239], a[160:163], v[20:23]\n"
    "buffer_load_dwordx4 a[228:231], v240, s[20:23], s53 offen offset:1024 sc1\n"
    "v_mfma_f32_16x16x32_bf16 v[52:55], a[236:239], a[164:167], v[52:55]\n"
    "v_mfma_f32_16x16x32_bf16 v[52:55], a[232:235], a[132:135], v[52:55]\n"
    "v_mfma_f32_16x16x32_bf16 v[84:87], a[232:235], a[136:139], v[84:87]\n"
    "v_mfma_f32_16x16x32_bf16 v[84:87], a[236:239], a[168:171], v[84:87]\n"
    "v_mfma_f32_16x16x32_bf16 v[116:119], a[236:239], a[172:175], v[116:119]\n"
    "v_mfma_f32_16x16x32_bf16 v[116:119], a[232:235], a[140:143], v[116:119]\n"
    "v_mfma_f32_16x16x32_bf16 v[148:151], a[232:235], a[144:147], v[148:151]\n"
    "v_mfma_f32_16x16x32_bf16 v[148:151], a[236:239], a[176:179], v[148:151]\n"
    "v_mfma_f32_16x16x32_bf16 v[180:183], a[236:239], a[180:183], v[180:183]\n"
    "v_mfma_f32_16x16x32_bf16 v[180:183], a[232:235], a[148:151], v[180:183]\n"
    "v_mfma_f32_16x16x32_bf16 v[212:215], a[232:235], a[152:155], v[212:215]\n"
    "s_addk_i32 m0, 2048\n"
    "s_xor_b32 s90, s90, 0x800\n"
    "v_mfma_f32_16x16x32_bf16 v[212:215], a[236:239], a[184:187], v[212:215]\n"
    "buffer_load_dwordx4 v240, s[32:35], s90 offen sc1 lds\n"
    "v_mfma_f32_16x16x32_bf16 a[244:247], a[236:239], a[188:191], a[244:247]\n"
    "v_mfma_f32_16x16x32_bf16 a[244:247], a[232:235], a[156:159], a[244:247]\n"
    "s_waitcnt vmcnt(13)\n"
    "v_mfma_f32_16x16x32_bf16 a[248:251], a[192:195], a[156:159], a[248:251]\n"
    "buffer_load_dwordx4 a[232:235], v240, s[20:23], s53 offen offset:2048 sc1\n"
    "v_mfma_f32_16x16x32_bf16 a[248:251], a[196:199], a[188:191], a[248:251]\n"
    "buffer_load_dwordx4 a[236:239], v240, s[20:23], s53 offen offset:3072 sc1\n"
    "v_mfma_f32_16x16x32_bf16 v[216:219], a[196:199], a[184:187], v[216:219]\n"
    "v_mfma_f32_16x16x32_bf16 v[216:219], a[192:195], a[152:155], v[216:219]\n"
    "v_mfma_f32_16x16x32_bf16 v[184:187], a[192:195], a[148:151], v[184:187]\n"
    "v_mfma_f32_16x16x32_bf16 v[184:187], a[196:199], a[180:183], v[184:187]\n"
    "v_mfma_f32_16x16x32_bf16 v[152:155], a[196:199], a[176:179], v[152:155]\n"
    "v_mfma_f32_16x16x32_bf16 v[152:155], a[192:195], a[144:147], v[152:155]\n"
    "v_mfma_f32_16x16x32_bf16 v[120:123], a[192:195], a[140:143], v[120:123]\n"
    "v_mfma_f32_16x16x32_bf16 v[120:123], a[196:199], a[172:175], v[120:123]\n"
    "v_mfma_f32_16x16x32_bf16 v[88:91], a[196:199], a[168:171], v[88:91]\n"
    "v_mfma_f32_16x16x32_bf16 v[88:91], a[192:195], a[136:139], v[88:91]\n"
    "v_mfma_f32_16x16x32_bf16 v[56:59], a[192:195], a[132:135], v[56:59]\n"
    "s_addk_i32 m0, 2048\n"
    "s_xor_b32 s90, s90, 0x101800\n"
    "v_mfma_f32_16x16x32_bf16 v[56:59], a[196:199], a[164:167], v[56:59]\n"
    "buffer_load_dwordx4 v240, s[32:35], s90 offen sc1 lds\n"
    "v_mfma_f32_16x16x32_bf16 v[24:27], a[196:199], a[160:163], v[24:27]\n"
    "v_mfma_f32_16x16x32_bf16 v[24:27], a[192:195], a[128:131], v[24:27]\n"
    "s_waitcnt vmcnt(13)\n"
    "s_addk_i32 s53, 12288\n"
    "v_mfma_f32_16x16x32_bf16 v[28:31], a[200:203], a[128:131], v[28:31]\n"
    "buffer_load_dwordx4 a[192:195], v240, s[20:23], s53 offen offset:0 sc1\n"
    "v_mfma_f32_16x16x32_bf16 v[28:31], a[204:207], a[160:163], v[28:31]\n"
    "buffer_load_dwordx4 a[196:199], v240, s[20:23], s53 offen offset:1024 sc1\n"
    "v_mfma_f32_16x16x32_bf16 v[60:63], a[204:207], a[164:167], v[60:63]\n"
    "ds_read_b128 a[128:131], v241 offset:16384\n"
    "v_mfma_f32_16x16x32_bf16 v[60:63], a[200:203], a[132:135], v[60:63]\n"
    "ds_read_b128 a[160:163], v241 offset:17408\n"
    "v_mfma_f32_16x16x32_bf16 v[92:95], a[200:203], a[136:139], v[92:95]\n"
    "ds_read_b128 a[164:167], v241 offset:19456\n"
    "v_mfma_f32_16x16x32_bf16 v[92:95], a[204:207], a[168:171], v[92:95]\n"
    "ds_read_b128 a[132:135], v241 offset:18432\n"
    "v_mfma_f32_16x16x32_bf16 v[124:127], a[204:207], a[172:175], v[124:127]\n"
    "ds_read_b128 a[136:139], v241 offset:20480\n"
    "v_mfma_f32_16x16x32_bf16 v[124:127], a[200:203], a[140:143], v[124:127]\n"
    "ds_read_b128 a[168:171], v241 offset:21504\n"
    "v_mfma_f32_16x16x32_bf16 v[156:159], a[200:203], a[144:147], v[156:159]\n"
    "ds_read_b128 a[172:175], v241 offset:23552\n"
    "v_mfma_f32_16x16x32_bf16 v[156:159], a[204:207], a[176:179], v[156:159]\n"
    "ds_read_b128 a[140:143], v241 offset:22528\n"
    "v_mfma_f32_16x16x32_bf16 v[188:191], a[204:207], a[180:183], v[188:191]\n"
    "ds_read_b128 a[144:147], v241 offset:24576\n"
    "v_mfma_f32_16x16x32_bf16 v[188:191], a[200:203], a[148:151], v[188:191]\n"
    "ds_read_b128 a[176:179], v241 offset:25600\n"
    "v_mfma_f32_16x16x32_bf16 v[220:223], a[200:203], a[152:155], v[220:223]\n"
    "s_addk_i32 m0, 2048\n"
    "s_xor_b32 s90, s90, 0x800\n"
    "v_mfma_f32_16x16x32_bf16 v[220:223], a[204:207], a[184:187], v[220:223]\n"
    "buffer_load_dwordx4 v240, s[32:35], s90 offen sc1 lds\n"
    "v_mfma_f32_16x16x32_bf16 a[252:255], a[204:207], a[188:191], a[252:255]\n"
    "ds_read_b128 a[180:183], v241 offset:27648\n"
    "ds_read_b128 a[148:151], v241 offset:26624\n"
    "v_mfma_f32_16x16x32_bf16 a[252:255], a[200:203], a[156:159], a[252:255]\n"
    "s_waitcnt vmcnt(13) lgkmcnt(10)\n"
    "ds_read_b128 a[152:155], v241 offset:28672\n"
    "ds_read_b128 a[184:187], v241 offset:29696\n"
    "v_mfma_f32_16x16x32_bf16 v[0:3], a[208:211], a[128:131], v[0:3]\n"
    "buffer_load_dwordx4 a[200:203], v240, s[20:23], s53 offen offset:2048 sc1\n"
    "v_mfma_f32_16x16x32_bf16 v[0:3], a[212:215], a[160:163], v[0:3]\n"
    "s_waitcnt lgkmcnt(10)\n"
    "v_mfma_f32_16x16x32_bf16 v[32:35], a[212:215], a[164:167], v[32:35]\n"
    "buffer_load_dwordx4 a[204:207], v240, s[20:23], s53 offen offset:3072 sc1\n"
    "v_mfma_f32_16x16x32_bf16 v[32:35], a[208:211], a[132:135], v[32:35]\n"
    "s_waitcnt lgkmcnt(8)\n"
    "ds_read_b128 a[188:191], v241 offset:31744\n"
    "ds_read_b128 a[156:159], v241 offset:30720\n"
    "v_mfma_f32_16x16x32_bf16 v[64:67], a[208:211], a[136:139], v[64:67]\n"
    "v_add_f32_dpp v16, v252, v16 row_newbcast:0 row_mask:0xf bank_mask:0xf\n"
    "v_add_f32_dpp v17, v252, v17 row_newbcast:1 row_mask:0xf bank_mask:0xf\n"
    "v_mfma_f32_16x16x32_bf16 v[64:67], a[212:215], a[168:171], v[64:67]\n"
    "s_waitcnt lgkmcnt(8)\n"
    "v_add_f32_dpp v18, v252, v18 row_newbcast:2 row_mask:0xf bank_mask:0xf\n"
    "v_mfma_f32_16x16x32_bf16 v[96:99], a[212:215], a[172:175], v[96:99]\n"
    "v_add_f32_dpp v19, v252, v19 row_newbcast:3 row_mask:0xf bank_mask:0xf\n"
    "v_mfma_f32_16x16x32_bf16 v[96:99], a[208:211], a[140:143], v[96:99]\n"
    "s_waitcnt lgkmcnt(6)\n"
    "v_add_f32_dpp v20, v252, v20 row_newbcast:4 row_mask:0xf bank_mask:0xf\n"
    "v_add_f32_dpp v21, v252, v21 row_newbcast:5 row_mask:0xf bank_mask:0xf\n"
    "v_mfma_f32_16x16x32_bf16 v[128:131], a[208:211], a[144:147], v[128:131]\n"
    "v_add_f32_dpp v22, v252, v22 row_newbcast:6 row_mask:0xf bank_mask:0xf\n"
    "v_mfma_f32_16x16x32_bf16 v[128:131], a[212:215], a[176:179], v[128:131]\n"
    "s_waitcnt lgkmcnt(4)\n"
    "v_add_f32_dpp v23, v252, v23 row_newbcast:7 row_mask:0xf bank_mask:0xf\n"
    "v_mfma_f32_16x16x32_bf16 v[160:163], a[212:215], a[180:183], v[160:163]\n"
    "v_add_f32_dpp v24, v252, v24 row_newbcast:8 row_mask:0xf bank_mask:0xf\n"
    "v_add_f32_dpp v25, v252, v25 row_newbcast:9 row_mask:0xf bank_mask:0xf\n"
    "v_mfma_f32_16x16x32_bf16 v[160:163], a[208:211], a[148:151], v[160:163]\n"
    "s_waitcnt lgkmcnt(2)\n"
    "v_add_f32_dpp v26, v252, v26 row_newbcast:10 row_mask:0xf bank_mask:0xf\n"
    "v_mfma_f32_16x16x32_bf16 v[192:195], a[208:211], a[152:155], v[192:195]\n"
    "v_add_f32_dpp v27, v252, v27 row_newbcast:11 row_mask:0xf bank_mask:0xf\n"
    "v_mfma_f32_16x16x32_bf16 v[192:195], a[212:215], a[184:187], v[192:195]\n"
    "s_waitcnt lgkmcnt(0)\n"
    "v_add_f32_dpp v28, v252, v28 row_newbcast:12 row_mask:0xf bank_mask:0xf\n"
    "v_add_f32_dpp v29, v252, v29 row_newbcast:13 row_mask:0xf bank_mask:0xf\n"
    "v_mfma_f32_16x16x32_bf16 v[224:227], a[212:215], a[188:191], v[224:227]\n"
    "v_add_f32_dpp v30, v252, v30 row_newbcast:14 row_mask:0xf bank_mask:0xf\n"
    "v_mfma_f32_16x16x32_bf16 v[224:227], a[208:211], a[156:159], v[224:227]\n"
    "s_waitcnt vmcnt(12)\n"
    "s_addk_i32 s53, 4096\n"
    "v_add_f32_dpp v31, v252, v31 row_newbcast:15 row_mask:0xf bank_mask:0xf\n"
    "v_mfma_f32_16x16x32_bf16 v[228:231], a[216:219], a[156:159], v[228:231]\n"
    "buffer_load_dwordx4 a[208:211], v240, s[20:23], s53 offen offset:0 sc1\n"
    "v_mfma_f32_16x16x32_bf16 v[228:231], a[220:223], a[188:191], v[228:231]\n"
    "buffer_load_dwordx4 a[212:215], v240, s[20:23], s53 offen offset:1024 sc1\n"
    "v_mfma_f32_16x16x32_bf16 v[196:199], a[220:223], a[184:187], v[196:199]\n"
    "v_add_f32_dpp v48, v252, v48 row_newbcast:0 row_mask:0xf bank_mask:0xf\n"
    "v_add_f32_dpp v49, v252, v49 row_newbcast:1 row_mask:0xf bank_mask:0xf\n"
    "v_mfma_f32_16x16x32_bf16 v[196:199], a[216:219], a[152:155], v[196:199]\n"
    "v_add_f32_dpp v50, v252, v50 row_newbcast:2 row_mask:0xf bank_mask:0xf\n"
    "v_add_f32_dpp v51, v252, v51 row_newbcast:3 row_mask:0xf bank_mask:0xf\n"
    "v_mfma_f32_16x16x32_bf16 v[164:167], a[216:219], a[148:151], v[164:167]\n"
    "v_add_f32_dpp v52, v252, v52 row_newbcast:4 row_mask:0xf bank_mask:0xf\n"
    "v_add_f32_dpp v53, v252, v53 row_newbcast:5 row_mask:0xf bank_mask:0xf\n"
    "v_mfma_f32_16x16x32_bf16 v[164:167], a[220:223], a[180:183], v[164:167]\n"
    "v_add_f32_dpp v54, v252, v54 row_newbcast:6 row_mask:0xf bank_mask:0xf\n"
    "v_add_f32_dpp v55, v252, v55 row_newbcast:7 row_mask:0xf bank_mask:0xf\n"
    "v_mfma_f32_16x16x32_bf16 v[132:135], a[220:223], a[176:179], v[132:135]\n"
    "v_add_f32_dpp v56, v252, v56 row_newbcast:8 row_mask:0xf bank_mask:0xf\n"
    "v_mfma_f32_16x16x32_bf16 v[132:135], a[216:219], a[144:147], v[132:135]\n"
    "v_add_f32_dpp v57, v252, v57 row_newbcast:9 row_mask:0xf bank_mask:0xf\n"
    "v_add_f32_dpp v58, v252, v58 row_newbcast:10 row_mask:0xf bank_mask:0xf\n"
    "v_mfma_f32_16x16x32_bf16 v[100:103], a[216:219], a[140:143], v[100:103]\n"
    "v_add_f32_dpp v59, v252, v59 row_newbcast:11 row_mask:0xf bank_mask:0xf\n"
    "v_mfma_f32_16x16x32_bf16 v[100:103], a[220:223], a[172:175], v[100:103]\n"
    "v_add_f32_dpp v60, v252, v60 row_newbcast:12 row_mask:0xf bank_mask:0xf\n"
    "v_mfma_f32_16x16x32_bf16 v[68:71], a[220:223], a[168:171], v[68:71]\n"
    "s_movk_i32 s60, 0x70\n"
    "v_add_f32_dpp v61, v252, v61 row_newbcast:13 row_mask:0xf bank_mask:0xf\n"
    "v_add_f32_dpp v62, v252, v62 row_newbcast:14 row_mask:0xf bank_mask:0xf\n"
    "v_mfma_f32_16x16x32_bf16 v[68:71], a[216:219], a[136:139], v[68:71]\n"
    "v_add_f32_dpp v63, v252, v63 row_newbcast:15 row_mask:0xf bank_mask:0xf\n"
    "v_mfma_f32_16x16x32_bf16 v[36:39], a[216:219], a[132:135], v[36:39]\n"
    "v_lshrrev_b32 v248, 4, v240\n"
    "v_xor_b32 v248, v248, v240\n"
    "v_mfma_f32_16x16x32_bf16 v[36:39], a[220:223], a[164:167], v[36:39]\n"
    "v_and_b32 v248, 0x70, v248\n"
    "v_and_b32 v249, 0xf0, v240\n"
    "v_mfma_f32_16x16x32_bf16 v[4:7], a[220:223], a[160:163], v[4:7]\n"
    "v_lshl_add_u32 v248, v249, 3, v248\n"
    "s_lshl_b32 s61, s65, 12\n"
    "v_mfma_f32_16x16x32_bf16 v[4:7], a[216:219], a[128:131], v[4:7]\n"
    "s_waitcnt vmcnt(11)\n"
    "s_add_u32 s61, s61, s66\n"
    "v_add_u32 v248, s61, v248\n"
    "v_xor_b32 v249, 64, v248\n"
    "v_lshrrev_b32 v242, 3, v240\n"
    "v_and_b32 v242, 0x70, v242\n"
    "v_xor_b32 v242, v242, v240\n"
    "v_add_u32 v242, s61, v242\n"
    "v_and_b32 v243, 0x380, v240\n"
    "v_lshlrev_b32 v243, 4, v243\n"
    "v_and_or_b32 v243, v240, s60, v243\n"
    "v_mfma_f32_16x16x32_bf16 v[8:11], a[224:227], a[128:131], v[8:11]\n"
    "s_add_u32 s91, s53, 0xf000\n"
    "v_add_f32_dpp v80, v252, v80 row_newbcast:0 row_mask:0xf bank_mask:0xf\n"
    "v_add_f32_dpp v81, v252, v81 row_newbcast:1 row_mask:0xf bank_mask:0xf\n"
    "v_mfma_f32_16x16x32_bf16 v[8:11], a[228:231], a[160:163], v[8:11]\n"
    "buffer_load_dwordx4 a[216:219], v240, s[20:23], s53 offen offset:2048 sc1\n"
    "v_mfma_f32_16x16x32_bf16 v[40:43], a[228:231], a[164:167], v[40:43]\n"
    "buffer_load_dwordx4 a[220:223], v240, s[20:23], s53 offen offset:3072 sc1\n"
    "v_mfma_f32_16x16x32_bf16 v[40:43], a[224:227], a[132:135], v[40:43]\n"
    "v_add_f32_dpp v82, v252, v82 row_newbcast:2 row_mask:0xf bank_mask:0xf\n"
    "v_add_f32_dpp v83, v252, v83 row_newbcast:3 row_mask:0xf bank_mask:0xf\n"
    "v_mfma_f32_16x16x32_bf16 v[72:75], a[224:227], a[136:139], v[72:75]\n"
    "v_add_f32_dpp v84, v252, v84 row_newbcast:4 row_mask:0xf bank_mask:0xf\n"
    "v_add_f32_dpp v85, v252, v85 row_newbcast:5 row_mask:0xf bank_mask:0xf\n"
    "v_mfma_f32_16x16x32_bf16 v[72:75], a[228:231], a[168:171], v[72:75]\n"
    "v_add_f32_dpp v86, v252, v86 row_newbcast:6 row_mask:0xf bank_mask:0xf\n"
    "v_add_f32_dpp v87, v252, v87 row_newbcast:7 row_mask:0xf bank_mask:0xf\n"
    "v_mfma_f32_16x16x32_bf16 v[104:107], a[228:231], a[172:175], v[104:107]\n"
    "v_add_f32_dpp v88, v252, v88 row_newbcast:8 row_mask:0xf bank_mask:0xf\n"
    "v_add_f32_dpp v89, v252, v89 row_newbcast:9 row_mask:0xf bank_mask:0xf\n"
    "v_mfma_f32_16x16x32_bf16 v[104:107], a[224:227], a[140:143], v[104:107]\n"
    "v_add_f32_dpp v90, v252, v90 row_newbcast:10 row_mask:0xf bank_mask:0xf\n"
    "v_add_f32_dpp v91, v252, v91 row_newbcast:11 row_mask:0xf bank_mask:0xf\n"
    "v_mfma_f32_16x16x32_bf16 v[136:139], a[224:227], a[144:147], v[136:139]\n"
    "v_add_f32_dpp v92, v252, v92 row_newbcast:12 row_mask:0xf bank_mask:0xf\n"
    "v_add_f32_dpp v93, v252, v93 row_newbcast:13 row_mask:0xf bank_mask:0xf\n"
    "v_mfma_f32_16x16x32_bf16 v[136:139], a[228:231], a[176:179], v[136:139]\n"
    "v_add_f32_dpp v94, v252, v94 row_newbcast:14 row_mask:0xf bank_mask:0xf\n"
    "v_add_f32_dpp v95, v252, v95 row_newbcast:15 row_mask:0xf bank_mask:0xf\n"
    "v_mfma_f32_16x16x32_bf16 v[168:171], a[228:231], a[180:183], v[168:171]\n"
    "v_add_f32_dpp v112, v252, v112 row_newbcast:0 row_mask:0xf bank_mask:0xf\n"
    "v_add_f32_dpp v113, v252, v113 row_newbcast:1 row_mask:0xf bank_mask:0xf\n"
    "v_mfma_f32_16x16x32_bf16 v[168:171], a[224:227], a[148:151], v[168:171]\n"
    "v_add_f32_dpp v114, v252, v114 row_newbcast:2 row_mask:0xf bank_mask:0xf\n"
    "v_add_f32_dpp v115, v252, v115 row_newbcast:3 row_mask:0xf bank_mask:0xf\n"
    "v_mfma_f32_16x16x32_bf16 v[200:203], a[224:227], a[152:155], v[200:203]\n"
    "v_add_f32_dpp v116, v252, v116 row_newbcast:4 row_mask:0xf bank_mask:0xf\n"
    "v_add_f32_dpp v117, v252, v117 row_newbcast:5 row_mask:0xf bank_mask:0xf\n"
    "v_mfma_f32_16x16x32_bf16 v[200:203], a[228:231], a[184:187], v[200:203]\n"
    "v_add_f32_dpp v118, v252, v118 row_newbcast:6 row_mask:0xf bank_mask:0xf\n"
    "v_add_f32_dpp v119, v252, v119 row_newbcast:7 row_mask:0xf bank_mask:0xf\n"
    "v_mfma_f32_16x16x32_bf16 v[232:235], a[228:231], a[188:191], v[232:235]\n"
    "v_add_f32_dpp v120, v252, v120 row_newbcast:8 row_mask:0xf bank_mask:0xf\n"
    "v_mfma_f32_16x16x32_bf16 v[232:235], a[224:227], a[156:159], v[232:235]\n"
    "s_waitcnt vmcnt(10)\n"
    "s_addk_i32 s53, 12288\n"
    "v_add_f32_dpp v121, v252, v121 row_newbcast:9 row_mask:0xf bank_mask:0xf\n"
    "v_mfma_f32_16x16x32_bf16 v[236:239], a[232:235], a[156:159], v[236:239]\n"
    "buffer_load_dwordx4 a[224:227], v240, s[20:23], s53 offen offset:0 sc1\n"
    "v_mfma_f32_16x16x32_bf16 v[236:239], a[236:239], a[188:191], v[236:239]\n"
    "buffer_load_dwordx4 a[228:231], v240, s[20:23], s53 offen offset:1024 sc1\n"
    "v_mfma_f32_16x16x32_bf16 v[204:207], a[236:239], a[184:187], v[204:207]\n"
    "ds_read_b128 a[156:159], v241 offset:47104\n"
    "v_add_f32_dpp v122, v252, v122 row_newbcast:10 row_mask:0xf bank_mask:0xf\n"
    "v_mfma_f32_16x16x32_bf16 v[204:207], a[232:235], a[152:155], v[204:207]\n"
    "ds_read_b128 a[188:191], v241 offset:48128\n"
    "v_add_f32_dpp v123, v252, v123 row_newbcast:11 row_mask:0xf bank_mask:0xf\n"
    "v_mfma_f32_16x16x32_bf16 v[172:175], a[232:235], a[148:151], v[172:175]\n"
    "ds_read_b128 a[184:187], v241 offset:46080\n"
    "v_add_f32_dpp v124, v252, v124 row_newbcast:12 row_mask:0xf bank_mask:0xf\n"
    "v_mfma_f32_16x16x32_bf16 v[172:175], a[236:239], a[180:183], v[172:175]\n"
    "ds_read_b128 a[152:155], v241 offset:45056\n"
    "v_add_f32_dpp v125, v252, v125 row_newbcast:13 row_mask:0xf bank_mask:0xf\n"
    "v_mfma_f32_16x16x32_bf16 v[140:143], a[236:239], a[176:179], v[140:143]\n"
    "ds_read_b128 a[148:151], v241 offset:43008\n"
    "v_add_f32_dpp v126, v252, v126 row_newbcast:14 row_mask:0xf bank_mask:0xf\n"
    "v_mfma_f32_16x16x32_bf16 v[140:143], a[232:235], a[144:147], v[140:143]\n"
    "ds_read_b128 a[180:183], v241 offset:44032\n"
    "v_add_f32_dpp v127, v252, v127 row_newbcast:15 row_mask:0xf bank_mask:0xf\n"
    "v_mfma_f32_16x16x32_bf16 v[108:111], a[232:235], a[140:143], v[108:111]\n"
    "ds_read_b128 a[176:179], v241 offset:41984\n"
    "v_add_f32_dpp v144, v252, v144 row_newbcast:0 row_mask:0xf bank_mask:0xf\n"
    "v_mfma_f32_16x16x32_bf16 v[108:111], a[236:239], a[172:175], v[108:111]\n"
    "ds_read_b128 a[144:147], v241 offset:40960\n"
    "v_add_f32_dpp v145, v252, v145 row_newbcast:1 row_mask:0xf bank_mask:0xf\n"
    "v_mfma_f32_16x16x32_bf16 v[76:79], a[236:239], a[168:171], v[76:79]\n"
    "ds_read_b128 a[140:143], v241 offset:38912\n"
    "v_add_f32_dpp v146, v252, v146 row_newbcast:2 row_mask:0xf bank_mask:0xf\n"
    "v_mfma_f32_16x16x32_bf16 v[76:79], a[232:235], a[136:139], v[76:79]\n"
    "ds_read_b128 a[172:175], v241 offset:39936\n"
    "v_add_f32_dpp v147, v252, v147 row_newbcast:3 row_mask:0xf bank_mask:0xf\n"
    "v_mfma_f32_16x16x32_bf16 v[44:47], a[232:235], a[132:135], v[44:47]\n"
    "ds_read_b128 a[168:171], v241 offset:37888\n"
    "v_add_f32_dpp v148, v252, v148 row_newbcast:4 row_mask:0xf bank_mask:0xf\n"
    "v_mfma_f32_16x16x32_bf16 v[44:47], a[236:239], a[164:167], v[44:47]\n"
    "ds_read_b128 a[136:139], v241 offset:36864\n"
    "v_add_f32_dpp v149, v252, v149 row_newbcast:5 row_mask:0xf bank_mask:0xf\n"
    "v_mfma_f32_16x16x32_bf16 v[12:15], a[236:239], a[160:163], v[12:15]\n"
    "ds_read_b128 a[132:135], v241 offset:34816\n"
    "v_add_f32_dpp v150, v252, v150 row_newbcast:6 row_mask:0xf bank_mask:0xf\n"
    "v_mfma_f32_16x16x32_bf16 v[12:15], a[232:235], a[128:131], v[12:15]\n"
    "s_waitcnt vmcnt(9) lgkmcnt(11)\n"
    "ds_read_b128 a[164:167], v241 offset:35840\n"
    "v_add_f32_dpp v151, v252, v151 row_newbcast:7 row_mask:0xf bank_mask:0xf\n"
    "v_mfma_f32_16x16x32_bf16 v[224:227], a[192:195], a[156:159], v[224:227]\n"
    "buffer_load_dwordx4 a[232:235], v240, s[20:23], s53 offen offset:2048 sc1\n"
    "v_mfma_f32_16x16x32_bf16 v[224:227], a[196:199], a[188:191], v[224:227]\n"
    "s_waitcnt lgkmcnt(10)\n"
    "v_add_f32_dpp v152, v252, v152 row_newbcast:8 row_mask:0xf bank_mask:0xf\n"
    "v_add_f32_dpp v153, v252, v153 row_newbcast:9 row_mask:0xf bank_mask:0xf\n"
    "v_mfma_f32_16x16x32_bf16 v[192:195], a[196:199], a[184:187], v[192:195]\n"
    "buffer_load_dwordx4 a[236:239], v240, s[20:23], s53 offen offset:3072 sc1\n"
    "v_mfma_f32_16x16x32_bf16 v[192:195], a[192:195], a[152:155], v[192:195]\n"
    "s_waitcnt lgkmcnt(8)\n"
    "ds_read_b128 a[160:163], v241 offset:33792\n"
    "ds_read_b128 a[128:131], v241 offset:32768\n"
    "v_mfma_f32_16x16x32_bf16 v[160:163], a[192:195], a[148:151], v[160:163]\n"
    "v_add_f32_dpp v154, v252, v154 row_newbcast:10 row_mask:0xf bank_mask:0xf\n"
    "v_add_f32_dpp v155, v252, v155 row_newbcast:11 row_mask:0xf bank_mask:0xf\n"
    "v_mfma_f32_16x16x32_bf16 v[160:163], a[196:199], a[180:183], v[160:163]\n"
    "s_waitcnt lgkmcnt(8)\n"
    "v_add_f32_dpp v156, v252, v156 row_newbcast:12 row_mask:0xf bank_mask:0xf\n"
    "v_add_f32_dpp v157, v252, v157 row_newbcast:13 row_mask:0xf bank_mask:0xf\n"
    "v_mfma_f32_16x16x32_bf16 v[128:131], a[196:199], a[176:179], v[128:131]\n"
    "v_add_f32_dpp v158, v252, v158 row_newbcast:14 row_mask:0xf bank_mask:0xf\n"
    "v_add_f32_dpp v159, v252, v159 row_newbcast:15 row_mask:0xf bank_mask:0xf\n"
    "v_mfma_f32_16x16x32_bf16 v[128:131], a[192:195], a[144:147], v[128:131]\n"
    "s_waitcnt lgkmcnt(6)\n"
    "v_add_f32_dpp v176, v252, v176 row_newbcast:0 row_mask:0xf bank_mask:0xf\n"
    "v_add_f32_dpp v177, v252, v177 row_newbcast:1 row_mask:0xf bank_mask:0xf\n"
    "v_mfma_f32_16x16x32_bf16 v[96:99], a[192:195], a[140:143], v[96:99]\n"
    "v_add_f32_dpp v178, v252, v178 row_newbcast:2 row_mask:0xf bank_mask:0xf\n"
    "v_add_f32_dpp v179, v252, v179 row_newbcast:3 row_mask:0xf bank_mask:0xf\n"
    "v_mfma_f32_16x16x32_bf16 v[96:99], a[196:199], a[172:175], v[96:99]\n"
    "s_waitcnt lgkmcnt(4)\n"
    "v_add_f32_dpp v180, v252, v180 row_newbcast:4 row_mask:0xf bank_mask:0xf\n"
    "v_add_f32_dpp v181, v252, v181 row_newbcast:5 row_mask:0xf bank_mask:0xf\n"
    "v_mfma_f32_16x16x32_bf16 v[64:67], a[196:199], a[168:171], v[64:67]\n"
    "v_add_f32_dpp v182, v252, v182 row_newbcast:6 row_mask:0xf bank_mask:0xf\n"
    "v_add_f32_dpp v183, v252, v183 row_newbcast:7 row_mask:0xf bank_mask:0xf\n"
    "v_mfma_f32_16x16x32_bf16 v[64:67], a[192:195], a[136:139], v[64:67]\n"
    "s_waitcnt lgkmcnt(2)\n"
    "v_add_f32_dpp v184, v252, v184 row_newbcast:8 row_mask:0xf bank_mask:0xf\n"
    "v_add_f32_dpp v185, v252, v185 row_newbcast:9 row_mask:0xf bank_mask:0xf\n"
    "v_mfma_f32_16x16x32_bf16 v[32:35], a[192:195], a[132:135], v[32:35]\n"
    "v_add_f32_dpp v186, v252, v186 row_newbcast:10 row_mask:0xf bank_mask:0xf\n"
    "v_add_f32_dpp v187, v252, v187 row_newbcast:11 row_mask:0xf bank_mask:0xf\n"
    "v_mfma_f32_16x16x32_bf16 v[32:35], a[196:199], a[164:167], v[32:35]\n"
    "s_waitcnt lgkmcnt(0)\n"
    "v_add_f32_dpp v188, v252, v188 row_newbcast:12 row_mask:0xf bank_mask:0xf\n"
    "v_add_f32_dpp v189, v252, v189 row_newbcast:13 row_mask:0xf bank_mask:0xf\n"
    "v_mfma_f32_16x16x32_bf16 v[0:3], a[196:199], a[160:163], v[0:3]\n"
    "v_add_f32_dpp v190, v252, v190 row_newbcast:14 row_mask:0xf bank_mask:0xf\n"
    "v_add_f32_dpp v191, v252, v191 row_newbcast:15 row_mask:0xf bank_mask:0xf\n"
    "v_mfma_f32_16x16x32_bf16 v[0:3], a[192:195], a[128:131], v[0:3]\n"
    "s_waitcnt vmcnt(8)\n"
    "s_addk_i32 s53, 4096\n"
    "v_add_f32_dpp v208, v252, v208 row_newbcast:0 row_mask:0xf bank_mask:0xf\n"
    "v_mfma_f32_16x16x32_bf16 v[4:7], a[200:203], a[128:131], v[4:7]\n"
    "buffer_load_dwordx4 a[192:195], v240, s[20:23], s53 offen offset:0 sc1\n"
    "v_mfma_f32_16x16x32_bf16 v[4:7], a[204:207], a[160:163], v[4:7]\n"
    "buffer_load_dwordx4 a[196:199], v240, s[20:23], s53 offen offset:1024 sc1\n"
    "v_mfma_f32_16x16x32_bf16 v[36:39], a[204:207], a[164:167], v[36:39]\n"
    "v_add_f32_dpp v209, v252, v209 row_newbcast:1 row_mask:0xf bank_mask:0xf\n"
    "v_add_f32_dpp v210, v252, v210 row_newbcast:2 row_mask:0xf bank_mask:0xf\n"
    "v_mfma_f32_16x16x32_bf16 v[36:39], a[200:203], a[132:135], v[36:39]\n"
    "v_add_f32_dpp v211, v252, v211 row_newbcast:3 row_mask:0xf bank_mask:0xf\n"
    "v_add_f32_dpp v212, v252, v212 row_newbcast:4 row_mask:0xf bank_mask:0xf\n"
    "v_mfma_f32_16x16x32_bf16 v[68:71], a[200:203], a[136:139], v[68:71]\n"
    "v_add_f32_dpp v213, v252, v213 row_newbcast:5 row_mask:0xf bank_mask:0xf\n"
    "v_add_f32_dpp v214, v252, v214 row_newbcast:6 row_mask:0xf bank_mask:0xf\n"
    "v_mfma_f32_16x16x32_bf16 v[68:71], a[204:207], a[168:171], v[68:71]\n"
    "v_add_f32_dpp v215, v252, v215 row_newbcast:7 row_mask:0xf bank_mask:0xf\n"
    "v_add_f32_dpp v216, v252, v216 row_newbcast:8 row_mask:0xf bank_mask:0xf\n"
    "v_mfma_f32_16x16x32_bf16 v[100:103], a[204:207], a[172:175], v[100:103]\n"
    "v_add_f32_dpp v217, v252, v217 row_newbcast:9 row_mask:0xf bank_mask:0xf\n"
    "v_add_f32_dpp v218, v252, v218 row_newbcast:10 row_mask:0xf bank_mask:0xf\n"
    "v_mfma_f32_16x16x32_bf16 v[100:103], a[200:203], a[140:143], v[100:103]\n"
    "v_add_f32_dpp v219, v252, v219 row_newbcast:11 row_mask:0xf bank_mask:0xf\n"
    "v_add_f32_dpp v220, v252, v220 row_newbcast:12 row_mask:0xf bank_mask:0xf\n"
    "v_mfma_f32_16x16x32_bf16 v[132:135], a[200:203], a[144:147], v[132:135]\n"
    "v_add_f32_dpp v221, v252, v221 row_newbcast:13 row_mask:0xf bank_mask:0xf\n"
    "v_add_f32_dpp v222, v252, v222 row_newbcast:14 row_mask:0xf bank_mask:0xf\n"
    "v_mfma_f32_16x16x32_bf16 v[132:135], a[204:207], a[176:179], v[132:135]\n"
    "v_add_f32_dpp v223, v252, v223 row_newbcast:15 row_mask:0xf bank_mask:0xf\n"
    "v_accvgpr_read_b32 v244, a240\n"
    "v_mfma_f32_16x16x32_bf16 v[164:167], a[204:207], a[180:183], v[164:167]\n"
    "v_accvgpr_read_b32 v245, a241\n"
    "v_accvgpr_read_b32 v246, a242\n"
    "v_mfma_f32_16x16x32_bf16 v[164:167], a[200:203], a[148:151], v[164:167]\n"
    "v_accvgpr_read_b32 v247, a243\n"
    "v_mfma_f32_16x16x32_bf16 v[196:199], a[200:203], a[152:155], v[196:199]\n"
    "v_add_f32_dpp v244, v252, v244 row_newbcast:0 row_mask:0xf bank_mask:0xf\n"
    "v_mfma_f32_16x16x32_bf16 v[196:199], a[204:207], a[184:187], v[196:199]\n"
    "v_add_f32_dpp v245, v252, v245 row_newbcast:1 row_mask:0xf bank_mask:0xf\n"
    "v_add_f32_dpp v246, v252, v246 row_newbcast:2 row_mask:0xf bank_mask:0xf\n"
    "v_mfma_f32_16x16x32_bf16 v[228:231], a[204:207], a[188:191], v[228:231]\n"
    "v_add_f32_dpp v247, v252, v247 row_newbcast:3 row_mask:0xf bank_mask:0xf\n"
    "v_mfma_f32_16x16x32_bf16 v[228:231], a[200:203], a[156:159], v[228:231]\n"
    "s_waitcnt vmcnt(8)\n"
    "v_accvgpr_write_b32 a240, v244\n"
    "v_mfma_f32_16x16x32_bf16 v[232:235], a[208:211], a[156:159], v[232:235]\n"
    "buffer_load_dwordx4 a[200:203], v240, s[20:23], s53 offen offset:2048 sc1\n"
    "v_mfma_f32_16x16x32_bf16 v[232:235], a[212:215], a[188:191], v[232:235]\n"
    "buffer_load_dwordx4 a[204:207], v240, s[20:23], s53 offen offset:3072 sc1\n"
    "v_mfma_f32_16x16x32_bf16 v[200:203], a[212:215], a[184:187], v[200:203]\n"
    "v_accvgpr_write_b32 a241, v245\n"
    "v_accvgpr_write_b32 a242, v246\n"
    "v_mfma_f32_16x16x32_bf16 v[200:203], a[208:211], a[152:155], v[200:203]\n"
    "v_accvgpr_write_b32 a243, v247\n"
    "v_accvgpr_read_b32 v244, a244\n"
    "v_mfma_f32_16x16x32_bf16 v[168:171], a[208:211], a[148:151], v[168:171]\n"
    "v_accvgpr_read_b32 v245, a245\n"
    "v_accvgpr_read_b32 v246, a246\n"
    "v_mfma_f32_16x16x32_bf16 v[168:171], a[212:215], a[180:183], v[168:171]\n"
    "v_accvgpr_read_b32 v247, a247\n"
    "v_add_f32_dpp v244, v252, v244 row_newbcast:4 row_mask:0xf bank_mask:0xf\n"
    "v_mfma_f32_16x16x32_bf16 v[136:139], a[212:215], a[176:179], v[136:139]\n"
    "v_add_f32_dpp v245, v252, v245 row_newbcast:5 row_mask:0xf bank_mask:0xf\n"
    "v_mfma_f32_16x16x32_bf16 v[136:139], a[208:211], a[144:147], v[136:139]\n"
    "v_add_f32_dpp v246, v252, v246 row_newbcast:6 row_mask:0xf bank_mask:0xf\n"
    "v_add_f32_dpp v247, v252, v247 row_newbcast:7 row_mask:0xf bank_mask:0xf\n"
    "v_mfma_f32_16x16x32_bf16 v[104:107], a[208:211], a[140:143], v[104:107]\n"
    "v_accvgpr_write_b32 a244, v244\n"
    "v_mfma_f32_16x16x32_bf16 v[104:107], a[212:215], a[172:175], v[104:107]\n"
    "v_accvgpr_write_b32 a245, v245\n"
    "v_mfma_f32_16x16x32_bf16 v[72:75], a[212:215], a[168:171], v[72:75]\n"
    "v_accvgpr_write_b32 a246, v246\n"
    "v_accvgpr_write_b32 a247, v247\n"
    "v_mfma_f32_16x16x32_bf16 v[72:75], a[208:211], a[136:139], v[72:75]\n"
    "v_accvgpr_read_b32 v244, a248\n"
    "v_mfma_f32_16x16x32_bf16 v[40:43], a[208:211], a[132:135], v[40:43]\n"
    "v_accvgpr_read_b32 v245, a249\n"
    "v_mfma_f32_16x16x32_bf16 v[40:43], a[212:215], a[164:167], v[40:43]\n"
    "v_accvgpr_read_b32 v246, a250\n"
    "v_accvgpr_read_b32 v247, a251\n"
    "v_mfma_f32_16x16x32_bf16 v[8:11], a[212:215], a[160:163], v[8:11]\n"
    "v_add_f32_dpp v244, v252, v244 row_newbcast:8 row_mask:0xf bank_mask:0xf\n"
    "v_mfma_f32_16x16x32_bf16 v[8:11], a[208:211], a[128:131], v[8:11]\n"
    "s_waitcnt vmcnt(8)\n"
    "s_addk_i32 s53, 12288\n"
    "v_add_f32_dpp v245, v252, v245 row_newbcast:9 row_mask:0xf bank_mask:0xf\n"
    "v_mfma_f32_16x16x32_bf16 v[12:15], a[216:219], a[128:131], v[12:15]\n"
    "buffer_load_dwordx4 a[208:211], v240, s[20:23], s53 offen offset:0 sc1\n"
    "v_mfma_f32_16x16x32_bf16 v[12:15], a[220:223], a[160:163], v[12:15]\n"
    "buffer_load_dwordx4 a[212:215], v240, s[20:23], s53 offen offset:1024 sc1\n"
    "v_mfma_f32_16x16x32_bf16 v[44:47], a[220:223], a[164:167], v[44:47]\n"
    "ds_read_b128 a[128:131], v241 offset:49152\n"
    "v_add_f32_dpp v246, v252, v246 row_newbcast:10 row_mask:0xf bank_mask:0xf\n"
    "v_mfma_f32_16x16x32_bf16 v[44:47], a[216:219], a[132:135], v[44:47]\n"
    "ds_read_b128 a[160:163], v241 offset:50176\n"
    "v_add_f32_dpp v247, v252, v247 row_newbcast:11 row_mask:0xf bank_mask:0xf\n"
    "v_mfma_f32_16x16x32_bf16 v[76:79], a[216:219], a[136:139], v[76:79]\n"
    "ds_read_b128 a[164:167], v241 offset:52224\n"
    "v_accvgpr_write_b32 a248, v244\n"
    "v_mfma_f32_16x16x32_bf16 v[76:79], a[220:223], a[168:171], v[76:79]\n"
    "ds_read_b128 a[132:135], v241 offset:51200\n"
    "v_accvgpr_write_b32 a249, v245\n"
    "v_mfma_f32_16x16x32_bf16 v[108:111], a[220:223], a[172:175], v[108:111]\n"
    "ds_read_b128 a[136:139], v241 offset:53248\n"
    "v_accvgpr_write_b32 a250, v246\n"
    "v_mfma_f32_16x16x32_bf16 v[108:111], a[216:219], a[140:143], v[108:111]\n"
    "ds_read_b128 a[168:171], v241 offset:54272\n"
    "v_accvgpr_write_b32 a251, v247\n"
    "v_mfma_f32_16x16x32_bf16 v[140:143], a[216:219], a[144:147], v[140:143]\n"
    "ds_read_b128 a[172:175], v241 offset:56320\n"
    "v_accvgpr_read_b32 v244, a252\n"
    "v_mfma_f32_16x16x32_bf16 v[140:143], a[220:223], a[176:179], v[140:143]\n"
    "ds_read_b128 a[140:143], v241 offset:55296\n"
    "v_accvgpr_read_b32 v245, a253\n"
    "v_mfma_f32_16x16x32_bf16 v[172:175], a[220:223], a[180:183], v[172:175]\n"
    "ds_read_b128 a[144:147], v241 offset:57344\n"
    "v_accvgpr_read_b32 v246, a254\n"
    "v_mfma_f32_16x16x32_bf16 v[172:175], a[216:219], a[148:151], v[172:175]\n"
    "ds_read_b128 a[176:179], v241 offset:58368\n"
    "v_accvgpr_read_b32 v247, a255\n"
    "v_mfma_f32_16x16x32_bf16 v[204:207], a[216:219], a[152:155], v[204:207]\n"
    "ds_read_b128 a[180:183], v241 offset:60416\n"
    "v_add_f32_dpp v244, v252, v244 row_newbcast:12 row_mask:0xf bank_mask:0xf\n"
    "v_mfma_f32_16x16x32_bf16 v[204:207], a[220:223], a[184:187], v[204:207]\n"
    "ds_read_b128 a[148:151], v241 offset:59392\n"
    "v_add_f32_dpp v245, v252, v245 row_newbcast:13 row_mask:0xf bank_mask:0xf\n"
    "v_mfma_f32_16x16x32_bf16 v[236:239], a[220:223], a[188:191], v[236:239]\n"
    "ds_read_b128 a[152:155], v241 offset:61440\n"
    "v_add_f32_dpp v246, v252, v246 row_newbcast:14 row_mask:0xf bank_mask:0xf\n"
    "v_mfma_f32_16x16x32_bf16 v[236:239], a[216:219], a[156:159], v[236:239]\n"
    "s_waitcnt vmcnt(8) lgkmcnt(11)\n"
    "ds_read_b128 a[184:187], v241 offset:62464\n"
    "v_add_f32_dpp v247, v252, v247 row_newbcast:15 row_mask:0xf bank_mask:0xf\n"
    "v_mfma_f32_16x16x32_bf16 v[0:3], a[224:227], a[128:131], v[0:3]\n"
    "buffer_load_dwordx4 a[216:219], v240, s[20:23], s53 offen offset:2048 sc1\n"
    "v_mfma_f32_16x16x32_bf16 v[0:3], a[228:231], a[160:163], v[0:3]\n"
    "s_waitcnt lgkmcnt(10)\n"
    "v_accvgpr_write_b32 a252, v244\n"
    "v_accvgpr_write_b32 a253, v245\n"
    "v_mfma_f32_16x16x32_bf16 v[32:35], a[228:231], a[164:167], v[32:35]\n"
    "buffer_load_dwordx4 a[220:223], v240, s[20:23], s53 offen offset:3072 sc1\n"
    "v_mfma_f32_16x16x32_bf16 v[32:35], a[224:227], a[132:135], v[32:35]\n"
    "s_waitcnt lgkmcnt(8)\n"
    "ds_read_b128 a[188:191], v241 offset:64512\n"
    "ds_read_b128 a[156:159], v241 offset:63488\n"
    "v_mfma_f32_16x16x32_bf16 v[64:67], a[224:227], a[136:139], v[64:67]\n"
    "v_accvgpr_write_b32 a254, v246\n"
    "v_add_f32_dpp v8, v253, v8 row_newbcast:8 row_mask:0xf bank_mask:0xf\n"
    "v_mfma_f32_16x16x32_bf16 v[64:67], a[228:231], a[168:171], v[64:67]\n"
    "s_waitcnt lgkmcnt(8)\n"
    "v_accvgpr_write_b32 a255, v247\n"
    "v_add_f32_dpp v9, v253, v9 row_newbcast:9 row_mask:0xf bank_mask:0xf\n"
    "v_mfma_f32_16x16x32_bf16 v[96:99], a[228:231], a[172:175], v[96:99]\n"
    "v_add_f32_dpp v10, v253, v10 row_newbcast:10 row_mask:0xf bank_mask:0xf\n"
    "v_add_f32_dpp v11, v253, v11 row_newbcast:11 row_mask:0xf bank_mask:0xf\n"
    "v_mfma_f32_16x16x32_bf16 v[96:99], a[224:227], a[140:143], v[96:99]\n"
    "s_waitcnt lgkmcnt(6)\n"
    "v_add_f32_dpp v40, v253, v40 row_newbcast:8 row_mask:0xf bank_mask:0xf\n"
    "v_add_f32_dpp v41, v253, v41 row_newbcast:9 row_mask:0xf bank_mask:0xf\n"
    "v_mfma_f32_16x16x32_bf16 v[128:131], a[224:227], a[144:147], v[128:131]\n"
    "v_add_f32_dpp v42, v253, v42 row_newbcast:10 row_mask:0xf bank_mask:0xf\n"
    "v_add_f32_dpp v43, v253, v43 row_newbcast:11 row_mask:0xf bank_mask:0xf\n"
    "v_mfma_f32_16x16x32_bf16 v[128:131], a[228:231], a[176:179], v[128:131]\n"
    "s_waitcnt lgkmcnt(4)\n"
    "v_add_f32_dpp v72, v253, v72 row_newbcast:8 row_mask:0xf bank_mask:0xf\n"
    "v_add_f32_dpp v73, v253, v73 row_newbcast:9 row_mask:0xf bank_mask:0xf\n"
    "v_mfma_f32_16x16x32_bf16 v[160:163], a[228:231], a[180:183], v[160:163]\n"
    "v_add_f32_dpp v74, v253, v74 row_newbcast:10 row_mask:0xf bank_mask:0xf\n"
    "v_add_f32_dpp v75, v253, v75 row_newbcast:11 row_mask:0xf bank_mask:0xf\n"
    "v_mfma_f32_16x16x32_bf16 v[160:163], a[224:227], a[148:151], v[160:163]\n"
    "s_waitcnt lgkmcnt(2)\n"
    "v_add_f32_dpp v104, v253, v104 row_newbcast:8 row_mask:0xf bank_mask:0xf\n"
    "v_add_f32_dpp v105, v253, v105 row_newbcast:9 row_mask:0xf bank_mask:0xf\n"
    "v_mfma_f32_16x16x32_bf16 v[192:195], a[224:227], a[152:155], v[192:195]\n"
    "v_add_f32_dpp v106, v253, v106 row_newbcast:10 row_mask:0xf bank_mask:0xf\n"
    "v_add_f32_dpp v107, v253, v107 row_newbcast:11 row_mask:0xf bank_mask:0xf\n"
    "v_mfma_f32_16x16x32_bf16 v[192:195], a[228:231], a[184:187], v[192:195]\n"
    "s_waitcnt lgkmcnt(0)\n"
    "v_add_f32_dpp v12, v253, v12 row_newbcast:12 row_mask:0xf bank_mask:0xf\n"
    "v_add_f32_dpp v13, v253, v13 row_newbcast:13 row_mask:0xf bank_mask:0xf\n"
    "v_mfma_f32_16x16x32_bf16 v[224:227], a[228:231], a[188:191], v[224:227]\n"
    "v_add_f32_dpp v14, v253, v14 row_newbcast:14 row_mask:0xf bank_mask:0xf\n"
    "v_mfma_f32_16x16x32_bf16 v[224:227], a[224:227], a[156:159], v[224:227]\n"
    "s_waitcnt vmcnt(8)\n"
    "s_addk_i32 s53, 4096\n"
    "v_add_f32_dpp v15, v253, v15 row_newbcast:15 row_mask:0xf bank_mask:0xf\n"
    "v_mfma_f32_16x16x32_bf16 v[228:231], a[232:235], a[156:159], v[228:231]\n"
    "buffer_load_dwordx4 a[224:227], v240, s[20:23], s53 offen offset:0 sc1\n"
    "v_mfma_f32_16x16x32_bf16 v[228:231], a[236:239], a[188:191], v[228:231]\n"
    "buffer_load_dwordx4 a[228:231], v240, s[20:23], s53 offen offset:1024 sc1\n"
    "v_mfma_f32_16x16x32_bf16 v[196:199], a[236:239], a[184:187], v[196:199]\n"
    "v_add_f32_dpp v44, v253, v44 row_newbcast:12 row_mask:0xf bank_mask:0xf\n"
    "v_add_f32_dpp v45, v253, v45 row_newbcast:13 row_mask:0xf bank_mask:0xf\n"
    "v_mfma_f32_16x16x32_bf16 v[196:199], a[232:235], a[152:155], v[196:199]\n"
    "v_add_f32_dpp v46, v253, v46 row_newbcast:14 row_mask:0xf bank_mask:0xf\n"
    "v_add_f32_dpp v47, v253, v47 row_newbcast:15 row_mask:0xf bank_mask:0xf\n"
    "v_mfma_f32_16x16x32_bf16 v[164:167], a[232:235], a[148:151], v[164:167]\n"
    "v_add_f32_dpp v76, v253, v76 row_newbcast:12 row_mask:0xf bank_mask:0xf\n"
    "v_mfma_f32_16x16x32_bf16 v[164:167], a[236:239], a[180:183], v[164:167]\n"
    "v_add_f32_dpp v77, v253, v77 row_newbcast:13 row_mask:0xf bank_mask:0xf\n"
    "v_mfma_f32_16x16x32_bf16 v[132:135], a[236:239], a[176:179], v[132:135]\n"
    "v_add_f32_dpp v78, v253, v78 row_newbcast:14 row_mask:0xf bank_mask:0xf\n"
    "v_mfma_f32_16x16x32_bf16 v[132:135], a[232:235], a[144:147], v[132:135]\n"
    "v_add_f32_dpp v79, v253, v79 row_newbcast:15 row_mask:0xf bank_mask:0xf\n"
    "v_mfma_f32_16x16x32_bf16 v[100:103], a[232:235], a[140:143], v[100:103]\n"
    "v_add_f32_dpp v108, v253, v108 row_newbcast:12 row_mask:0xf bank_mask:0xf\n"
    "v_mfma_f32_16x16x32_bf16 v[100:103], a[236:239], a[172:175], v[100:103]\n"
    "v_add_f32_dpp v109, v253, v109 row_newbcast:13 row_mask:0xf bank_mask:0xf\n"
    "v_mfma_f32_16x16x32_bf16 v[68:71], a[236:239], a[168:171], v[68:71]\n"
    "v_add_f32_dpp v110, v253, v110 row_newbcast:14 row_mask:0xf bank_mask:0xf\n"
    "v_mfma_f32_16x16x32_bf16 v[68:71], a[232:235], a[136:139], v[68:71]\n"
    "v_add_f32_dpp v111, v253, v111 row_newbcast:15 row_mask:0xf bank_mask:0xf\n"
    "v_mfma_f32_16x16x32_bf16 v[36:39], a[232:235], a[132:135], v[36:39]\n"
    "v_add_f32_dpp v0, v253, v0 row_newbcast:0 row_mask:0xf bank_mask:0xf\n"
    "v_mfma_f32_16x16x32_bf16 v[36:39], a[236:239], a[164:167], v[36:39]\n"
    "v_add_f32_dpp v1, v253, v1 row_newbcast:1 row_mask:0xf bank_mask:0xf\n"
    "v_mfma_f32_16x16x32_bf16 v[4:7], a[236:239], a[160:163], v[4:7]\n"
    "v_add_f32_dpp v2, v253, v2 row_newbcast:2 row_mask:0xf bank_mask:0xf\n"
    "v_mfma_f32_16x16x32_bf16 v[4:7], a[232:235], a[128:131], v[4:7]\n"
    "s_waitcnt vmcnt(8)\n"
    "v_add_f32_dpp v3, v253, v3 row_newbcast:3 row_mask:0xf bank_mask:0xf\n"
    "v_mfma_f32_16x16x32_bf16 v[8:11], a[192:195], a[128:131], v[8:11]\n"
    "buffer_load_dwordx4 a[232:235], v240, s[20:23], s53 offen offset:2048 sc1\n"
    "v_mfma_f32_16x16x32_bf16 v[8:11], a[196:199], a[160:163], v[8:11]\n"
    "buffer_load_dwordx4 a[236:239], v240, s[20:23], s53 offen offset:3072 sc1\n"
    "v_mfma_f32_16x16x32_bf16 v[40:43], a[196:199], a[164:167], v[40:43]\n"
    "v_add_f32_dpp v32, v253, v32 row_newbcast:0 row_mask:0xf bank_mask:0xf\n"
    "v_add_f32_dpp v33, v253, v33 row_newbcast:1 row_mask:0xf bank_mask:0xf\n"
    "v_mfma_f32_16x16x32_bf16 v[40:43], a[192:195], a[132:135], v[40:43]\n"
    "v_add_f32_dpp v34, v253, v34 row_newbcast:2 row_mask:0xf bank_mask:0xf\n"
    "v_add_f32_dpp v35, v253, v35 row_newbcast:3 row_mask:0xf bank_mask:0xf\n"
    "v_mfma_f32_16x16x32_bf16 v[72:75], a[192:195], a[136:139], v[72:75]\n"
    "v_add_f32_dpp v64, v253, v64 row_newbcast:0 row_mask:0xf bank_mask:0xf\n"
    "v_mfma_f32_16x16x32_bf16 v[72:75], a[196:199], a[168:171], v[72:75]\n"
    "v_add_f32_dpp v65, v253, v65 row_newbcast:1 row_mask:0xf bank_mask:0xf\n"
    "v_mfma_f32_16x16x32_bf16 v[104:107], a[196:199], a[172:175], v[104:107]\n"
    "v_add_f32_dpp v66, v253, v66 row_newbcast:2 row_mask:0xf bank_mask:0xf\n"
    "v_mfma_f32_16x16x32_bf16 v[104:107], a[192:195], a[140:143], v[104:107]\n"
    "v_add_f32_dpp v67, v253, v67 row_newbcast:3 row_mask:0xf bank_mask:0xf\n"
    "v_mfma_f32_16x16x32_bf16 v[136:139], a[192:195], a[144:147], v[136:139]\n"
    "v_add_f32_dpp v96, v253, v96 row_newbcast:0 row_mask:0xf bank_mask:0xf\n"
    "v_mfma_f32_16x16x32_bf16 v[136:139], a[196:199], a[176:179], v[136:139]\n"
    "v_add_f32_dpp v97, v253, v97 row_newbcast:1 row_mask:0xf bank_mask:0xf\n"
    "v_mfma_f32_16x16x32_bf16 v[168:171], a[196:199], a[180:183], v[168:171]\n"
    "v_add_f32_dpp v98, v253, v98 row_newbcast:2 row_mask:0xf bank_mask:0xf\n"
    "v_mfma_f32_16x16x32_bf16 v[168:171], a[192:195], a[148:151], v[168:171]\n"
    "v_add_f32_dpp v99, v253, v99 row_newbcast:3 row_mask:0xf bank_mask:0xf\n"
    "v_mfma_f32_16x16x32_bf16 v[200:203], a[192:195], a[152:155], v[200:203]\n"
    "v_add_f32_dpp v4, v253, v4 row_newbcast:4 row_mask:0xf bank_mask:0xf\n"
    "v_mfma_f32_16x16x32_bf16 v[200:203], a[196:199], a[184:187], v[200:203]\n"
    "s_barrier\n"
    "v_add_f32_dpp v5, v253, v5 row_newbcast:5 row_mask:0xf bank_mask:0xf\n"
    "v_mfma_f32_16x16x32_bf16 v[232:235], a[196:199], a[188:191], v[232:235]\n"
    "v_add_f32_dpp v6, v253, v6 row_newbcast:6 row_mask:0xf bank_mask:0xf\n"
    "v_mfma_f32_16x16x32_bf16 v[232:235], a[192:195], a[156:159], v[232:235]\n"
    "s_waitcnt vmcnt(8)\n"
    "s_addk_i32 s53, 12288\n"
    "v_add_f32_dpp v7, v253, v7 row_newbcast:7 row_mask:0xf bank_mask:0xf\n"
    "v_mfma_f32_16x16x32_bf16 v[236:239], a[200:203], a[156:159], v[236:239]\n"
    "buffer_load_dwordx4 a[192:195], v240, s[20:23], s53 offen offset:0 sc1\n"
    "v_mfma_f32_16x16x32_bf16 v[236:239], a[204:207], a[188:191], v[236:239]\n"
    "buffer_load_dwordx4 a[196:199], v240, s[20:23], s53 offen offset:1024 sc1\n"
    "v_mfma_f32_16x16x32_bf16 v[204:207], a[204:207], a[184:187], v[204:207]\n"
    "v_add_u32 v241, s67, v240\n"
    "ds_read_b128 a[156:159], v241 offset:14336\n"
    "v_mfma_f32_16x16x32_bf16 v[204:207], a[200:203], a[152:155], v[204:207]\n"
    "ds_read_b128 a[188:191], v241 offset:15360\n"
    "v_add_f32_dpp v36, v253, v36 row_newbcast:4 row_mask:0xf bank_mask:0xf\n"
    "v_mfma_f32_16x16x32_bf16 v[172:175], a[200:203], a[148:151], v[172:175]\n"
    "ds_read_b128 a[184:187], v241 offset:13312\n"
    "v_add_f32_dpp v37, v253, v37 row_newbcast:5 row_mask:0xf bank_mask:0xf\n"
    "v_mfma_f32_16x16x32_bf16 v[172:175], a[204:207], a[180:183], v[172:175]\n"
    "ds_read_b128 a[152:155], v241 offset:12288\n"
    "v_add_f32_dpp v38, v253, v38 row_newbcast:6 row_mask:0xf bank_mask:0xf\n"
    "v_mfma_f32_16x16x32_bf16 v[140:143], a[204:207], a[176:179], v[140:143]\n"
    "ds_read_b128 a[148:151], v241 offset:10240\n"
    "v_add_f32_dpp v39, v253, v39 row_newbcast:7 row_mask:0xf bank_mask:0xf\n"
    "v_mfma_f32_16x16x32_bf16 v[140:143], a[200:203], a[144:147], v[140:143]\n"
    "ds_read_b128 a[180:183], v241 offset:11264\n"
    "v_add_f32_dpp v68, v253, v68 row_newbcast:4 row_mask:0xf bank_mask:0xf\n"
    "v_mfma_f32_16x16x32_bf16 v[108:111], a[200:203], a[140:143], v[108:111]\n"
    "ds_read_b128 a[176:179], v241 offset:9216\n"
    "v_add_f32_dpp v69, v253, v69 row_newbcast:5 row_mask:0xf bank_mask:0xf\n"
    "v_mfma_f32_16x16x32_bf16 v[108:111], a[204:207], a[172:175], v[108:111]\n"
    "ds_read_b128 a[144:147], v241 offset:8192\n"
    "v_add_f32_dpp v70, v253, v70 row_newbcast:6 row_mask:0xf bank_mask:0xf\n"
    "v_mfma_f32_16x16x32_bf16 v[76:79], a[204:207], a[168:171], v[76:79]\n"
    "ds_read_b128 a[140:143], v241 offset:6144\n"
    "v_add_f32_dpp v71, v253, v71 row_newbcast:7 row_mask:0xf bank_mask:0xf\n"
    "v_mfma_f32_16x16x32_bf16 v[76:79], a[200:203], a[136:139], v[76:79]\n"
    "ds_read_b128 a[172:175], v241 offset:7168\n"
    "v_add_f32_dpp v100, v253, v100 row_newbcast:4 row_mask:0xf bank_mask:0xf\n"
    "v_mfma_f32_16x16x32_bf16 v[44:47], a[200:203], a[132:135], v[44:47]\n"
    "ds_read_b128 a[168:171], v241 offset:5120\n"
    "v_add_f32_dpp v101, v253, v101 row_newbcast:5 row_mask:0xf bank_mask:0xf\n"
    "v_mfma_f32_16x16x32_bf16 v[44:47], a[204:207], a[164:167], v[44:47]\n"
    "ds_read_b128 a[136:139], v241 offset:4096\n"
    "v_add_f32_dpp v102, v253, v102 row_newbcast:6 row_mask:0xf bank_mask:0xf\n"
    "v_mfma_f32_16x16x32_bf16 v[12:15], a[204:207], a[160:163], v[12:15]\n"
    "ds_read_b128 a[132:135], v241 offset:2048\n"
    "v_add_f32_dpp v103, v253, v103 row_newbcast:7 row_mask:0xf bank_mask:0xf\n"
    "v_mfma_f32_16x16x32_bf16 v[12:15], a[200:203], a[128:131], v[12:15]\n"
    "s_waitcnt vmcnt(8) lgkmcnt(11)\n"
    "ds_read_b128 a[164:167], v241 offset:3072\n"
    "v_add_f32_dpp v136, v253, v136 row_newbcast:8 row_mask:0xf bank_mask:0xf\n"
    "v_mfma_f32_16x16x32_bf16 v[224:227], a[208:211], a[156:159], v[224:227]\n"
    "buffer_load_dwordx4 a[200:203], v240, s[20:23], s53 offen offset:2048 sc1\n"
    "v_mfma_f32_16x16x32_bf16 v[224:227], a[212:215], a[188:191], v[224:227]\n"
    "s_waitcnt lgkmcnt(10)\n"
    "v_add_f32_dpp v137, v253, v137 row_newbcast:9 row_mask:0xf bank_mask:0xf\n"
    "v_add_f32_dpp v138, v253, v138 row_newbcast:10 row_mask:0xf bank_mask:0xf\n"
    "v_mfma_f32_16x16x32_bf16 v[192:195], a[212:215], a[184:187], v[192:195]\n"
    "buffer_load_dwordx4 a[204:207], v240, s[20:23], s53 offen offset:3072 sc1\n"
    "v_mfma_f32_16x16x32_bf16 v[192:195], a[208:211], a[152:155], v[192:195]\n"
    "s_waitcnt lgkmcnt(8)\n"
    "ds_read_b128 a[160:163], v241 offset:1024\n"
    "ds_read_b128 a[128:131], v241 offset:0\n"
    "v_mfma_f32_16x16x32_bf16 v[160:163], a[208:211], a[148:151], v[160:163]\n"
    "v_add_f32_dpp v139, v253, v139 row_newbcast:11 row_mask:0xf bank_mask:0xf\n"
    "v_add_f32_dpp v168, v253, v168 row_newbcast:8 row_mask:0xf bank_mask:0xf\n"
    "v_mfma_f32_16x16x32_bf16 v[160:163], a[212:215], a[180:183], v[160:163]\n"
    "s_waitcnt lgkmcnt(8)\n"
    "v_add_f32_dpp v169, v253, v169 row_newbcast:9 row_mask:0xf bank_mask:0xf\n"
    "v_add_f32_dpp v170, v253, v170 row_newbcast:10 row_mask:0xf bank_mask:0xf\n"
    "v_mfma_f32_16x16x32_bf16 v[128:131], a[212:215], a[176:179], v[128:131]\n"
    "v_add_f32_dpp v171, v253, v171 row_newbcast:11 row_mask:0xf bank_mask:0xf\n"
    "v_add_f32_dpp v200, v253, v200 row_newbcast:8 row_mask:0xf bank_mask:0xf\n"
    "v_mfma_f32_16x16x32_bf16 v[128:131], a[208:211], a[144:147], v[128:131]\n"
    "s_waitcnt lgkmcnt(6)\n"
    "v_add_f32_dpp v201, v253, v201 row_newbcast:9 row_mask:0xf bank_mask:0xf\n"
    "v_add_f32_dpp v202, v253, v202 row_newbcast:10 row_mask:0xf bank_mask:0xf\n"
    "v_mfma_f32_16x16x32_bf16 v[96:99], a[208:211], a[140:143], v[96:99]\n"
    "v_add_f32_dpp v203, v253, v203 row_newbcast:11 row_mask:0xf bank_mask:0xf\n"
    "v_add_f32_dpp v232, v253, v232 row_newbcast:8 row_mask:0xf bank_mask:0xf\n"
    "v_mfma_f32_16x16x32_bf16 v[96:99], a[212:215], a[172:175], v[96:99]\n"
    "s_waitcnt lgkmcnt(4)\n"
    "v_add_f32_dpp v233, v253, v233 row_newbcast:9 row_mask:0xf bank_mask:0xf\n"
    "v_mfma_f32_16x16x32_bf16 v[64:67], a[212:215], a[168:171], v[64:67]\n"
    "v_add_f32_dpp v234, v253, v234 row_newbcast:10 row_mask:0xf bank_mask:0xf\n"
    "v_mfma_f32_16x16x32_bf16 v[64:67], a[208:211], a[136:139], v[64:67]\n"
    "s_waitcnt lgkmcnt(2)\n"
    "v_add_f32_dpp v235, v253, v235 row_newbcast:11 row_mask:0xf bank_mask:0xf\n"
    "v_mfma_f32_16x16x32_bf16 v[32:35], a[208:211], a[132:135], v[32:35]\n"
    "v_add_f32_dpp v140, v253, v140 row_newbcast:12 row_mask:0xf bank_mask:0xf\n"
    "v_mfma_f32_16x16x32_bf16 v[32:35], a[212:215], a[164:167], v[32:35]\n"
    "s_waitcnt lgkmcnt(0)\n"
    "v_add_f32_dpp v141, v253, v141 row_newbcast:13 row_mask:0xf bank_mask:0xf\n"
    "v_mfma_f32_16x16x32_bf16 v[0:3], a[212:215], a[160:163], v[0:3]\n"
    "v_add_f32_dpp v142, v253, v142 row_newbcast:14 row_mask:0xf bank_mask:0xf\n"
    "v_mfma_f32_16x16x32_bf16 v[0:3], a[208:211], a[128:131], v[0:3]\n"
    "s_waitcnt vmcnt(8)\n"
    "s_addk_i32 s53, 4096\n"
    "v_add_f32_dpp v143, v253, v143 row_newbcast:15 row_mask:0xf bank_mask:0xf\n"
    "v_mfma_f32_16x16x32_bf16 v[4:7], a[216:219], a[128:131], v[4:7]\n"
    "buffer_load_dwordx4 a[208:211], v240, s[20:23], s53 offen offset:0 sc1\n"
    "v_mfma_f32_16x16x32_bf16 v[4:7], a[220:223], a[160:163], v[4:7]\n"
    "buffer_load_dwordx4 a[212:215], v240, s[20:23], s53 offen offset:1024 sc1\n"
    "v_mfma_f32_16x16x32_bf16 v[36:39], a[220:223], a[164:167], v[36:39]\n"
    "v_add_f32_dpp v172, v253, v172 row_newbcast:12 row_mask:0xf bank_mask:0xf\n"
    "v_add_f32_dpp v173, v253, v173 row_newbcast:13 row_mask:0xf bank_mask:0xf\n"
    "v_mfma_f32_16x16x32_bf16 v[36:39], a[216:219], a[132:135], v[36:39]\n"
    "v_add_f32_dpp v174, v253, v174 row_newbcast:14 row_mask:0xf bank_mask:0xf\n"
    "v_add_f32_dpp v175, v253, v175 row_newbcast:15 row_mask:0xf bank_mask:0xf\n"
    "v_mfma_f32_16x16x32_bf16 v[68:71], a[216:219], a[136:139], v[68:71]\n"
    "v_add_f32_dpp v204, v253, v204 row_newbcast:12 row_mask:0xf bank_mask:0xf\n"
    "v_mfma_f32_16x16x32_bf16 v[68:71], a[220:223], a[168:171], v[68:71]\n"
    "v_add_f32_dpp v205, v253, v205 row_newbcast:13 row_mask:0xf bank_mask:0xf\n"
    "v_mfma_f32_16x16x32_bf16 v[100:103], a[220:223], a[172:175], v[100:103]\n"
    "v_add_f32_dpp v206, v253, v206 row_newbcast:14 row_mask:0xf bank_mask:0xf\n"
    "v_mfma_f32_16x16x32_bf16 v[100:103], a[216:219], a[140:143], v[100:103]\n"
    "v_add_f32_dpp v207, v253, v207 row_newbcast:15 row_mask:0xf bank_mask:0xf\n"
    "v_mfma_f32_16x16x32_bf16 v[132:135], a[216:219], a[144:147], v[132:135]\n"
    "v_add_f32_dpp v236, v253, v236 row_newbcast:12 row_mask:0xf bank_mask:0xf\n"
    "v_mfma_f32_16x16x32_bf16 v[132:135], a[220:223], a[176:179], v[132:135]\n"
    "v_add_f32_dpp v237, v253, v237 row_newbcast:13 row_mask:0xf bank_mask:0xf\n"
    "v_mfma_f32_16x16x32_bf16 v[164:167], a[220:223], a[180:183], v[164:167]\n"
    "v_add_f32_dpp v238, v253, v238 row_newbcast:14 row_mask:0xf bank_mask:0xf\n"
    "v_mfma_f32_16x16x32_bf16 v[164:167], a[216:219], a[148:151], v[164:167]\n"
    "v_add_f32_dpp v239, v253, v239 row_newbcast:15 row_mask:0xf bank_mask:0xf\n"
    "v_mfma_f32_16x16x32_bf16 v[196:199], a[216:219], a[152:155], v[196:199]\n"
    "v_add_f32_dpp v128, v253, v128 row_newbcast:0 row_mask:0xf bank_mask:0xf\n"
    "v_mfma_f32_16x16x32_bf16 v[196:199], a[220:223], a[184:187], v[196:199]\n"
    "v_add_f32_dpp v129, v253, v129 row_newbcast:1 row_mask:0xf bank_mask:0xf\n"
    "v_mfma_f32_16x16x32_bf16 v[228:231], a[220:223], a[188:191], v[228:231]\n"
    "v_add_f32_dpp v130, v253, v130 row_newbcast:2 row_mask:0xf bank_mask:0xf\n"
    "v_mfma_f32_16x16x32_bf16 v[228:231], a[216:219], a[156:159], v[228:231]\n"
    "s_waitcnt vmcnt(8)\n"
    "v_add_f32_dpp v131, v253, v131 row_newbcast:3 row_mask:0xf bank_mask:0xf\n"
    "v_mfma_f32_16x16x32_bf16 v[232:235], a[224:227], a[156:159], v[232:235]\n"
    "buffer_load_dwordx4 a[216:219], v240, s[20:23], s53 offen offset:2048 sc1\n"
    "v_mfma_f32_16x16x32_bf16 v[232:235], a[228:231], a[188:191], v[232:235]\n"
    "buffer_load_dwordx4 a[220:223], v240, s[20:23], s53 offen offset:3072 sc1\n"
    "v_mfma_f32_16x16x32_bf16 v[200:203], a[228:231], a[184:187], v[200:203]\n"
    "v_add_f32_dpp v160, v253, v160 row_newbcast:0 row_mask:0xf bank_mask:0xf\n"
    "v_add_f32_dpp v161, v253, v161 row_newbcast:1 row_mask:0xf bank_mask:0xf\n"
    "v_mfma_f32_16x16x32_bf16 v[200:203], a[224:227], a[152:155], v[200:203]\n"
    "v_add_f32_dpp v162, v253, v162 row_newbcast:2 row_mask:0xf bank_mask:0xf\n"
    "v_add_f32_dpp v163, v253, v163 row_newbcast:3 row_mask:0xf bank_mask:0xf\n"
    "v_mfma_f32_16x16x32_bf16 v[168:171], a[224:227], a[148:151], v[168:171]\n"
    "v_add_f32_dpp v192, v253, v192 row_newbcast:0 row_mask:0xf bank_mask:0xf\n"
    "v_mfma_f32_16x16x32_bf16 v[168:171], a[228:231], a[180:183], v[168:171]\n"
    "v_add_f32_dpp v193, v253, v193 row_newbcast:1 row_mask:0xf bank_mask:0xf\n"
    "v_mfma_f32_16x16x32_bf16 v[136:139], a[228:231], a[176:179], v[136:139]\n"
    "v_add_f32_dpp v194, v253, v194 row_newbcast:2 row_mask:0xf bank_mask:0xf\n"
    "v_mfma_f32_16x16x32_bf16 v[136:139], a[224:227], a[144:147], v[136:139]\n"
    "v_add_f32_dpp v195, v253, v195 row_newbcast:3 row_mask:0xf bank_mask:0xf\n"
    "v_mfma_f32_16x16x32_bf16 v[104:107], a[224:227], a[140:143], v[104:107]\n"
    "v_add_f32_dpp v224, v253, v224 row_newbcast:0 row_mask:0xf bank_mask:0xf\n"
    "v_mfma_f32_16x16x32_bf16 v[104:107], a[228:231], a[172:175], v[104:107]\n"
    "v_add_f32_dpp v225, v253, v225 row_newbcast:1 row_mask:0xf bank_mask:0xf\n"
    "v_mfma_f32_16x16x32_bf16 v[72:75], a[228:231], a[168:171], v[72:75]\n"
    "v_add_f32_dpp v226, v253, v226 row_newbcast:2 row_mask:0xf bank_mask:0xf\n"
    "v_mfma_f32_16x16x32_bf16 v[72:75], a[224:227], a[136:139], v[72:75]\n"
    "v_add_f32_dpp v227, v253, v227 row_newbcast:3 row_mask:0xf bank_mask:0xf\n"
    "v_mfma_f32_16x16x32_bf16 v[40:43], a[224:227], a[132:135], v[40:43]\n"
    "v_add_f32_dpp v132, v253, v132 row_newbcast:4 row_mask:0xf bank_mask:0xf\n"
    "v_mfma_f32_16x16x32_bf16 v[40:43], a[228:231], a[164:167], v[40:43]\n"
    "v_add_f32_dpp v133, v253, v133 row_newbcast:5 row_mask:0xf bank_mask:0xf\n"
    "v_mfma_f32_16x16x32_bf16 v[8:11], a[228:231], a[160:163], v[8:11]\n"
    "v_add_f32_dpp v134, v253, v134 row_newbcast:6 row_mask:0xf bank_mask:0xf\n"
    "v_mfma_f32_16x16x32_bf16 v[8:11], a[224:227], a[128:131], v[8:11]\n"
    "s_waitcnt vmcnt(8)\n"
    "s_addk_i32 s53, 4096\n"
    "v_add_f32_dpp v135, v253, v135 row_newbcast:7 row_mask:0xf bank_mask:0xf\n"
    "v_mfma_f32_16x16x32_bf16 v[12:15], a[232:235], a[128:131], v[12:15]\n"
    "buffer_load_dwordx4 a[224:227], v240, s[20:23], s53 offen offset:0 sc1\n"
    "v_mfma_f32_16x16x32_bf16 v[12:15], a[236:239], a[160:163], v[12:15]\n"
    "buffer_load_dwordx4 a[228:231], v240, s[20:23], s53 offen offset:1024 sc1\n"
    "v_mfma_f32_16x16x32_bf16 v[44:47], a[236:239], a[164:167], v[44:47]\n"
    "ds_read_b128 a[128:131], v241 offset:16384\n"
    "v_add_f32_dpp v164, v253, v164 row_newbcast:4 row_mask:0xf bank_mask:0xf\n"
    "v_mfma_f32_16x16x32_bf16 v[44:47], a[232:235], a[132:135], v[44:47]\n"
    "ds_read_b128 a[160:163], v241 offset:17408\n"
    "v_add_f32_dpp v165, v253, v165 row_newbcast:5 row_mask:0xf bank_mask:0xf\n"
    "v_mfma_f32_16x16x32_bf16 v[76:79], a[232:235], a[136:139], v[76:79]\n"
    "ds_read_b128 a[164:167], v241 offset:19456\n"
    "v_add_f32_dpp v166, v253, v166 row_newbcast:6 row_mask:0xf bank_mask:0xf\n"
    "v_mfma_f32_16x16x32_bf16 v[76:79], a[236:239], a[168:171], v[76:79]\n"
    "ds_read_b128 a[132:135], v241 offset:18432\n"
    "v_add_f32_dpp v167, v253, v167 row_newbcast:7 row_mask:0xf bank_mask:0xf\n"
    "v_mfma_f32_16x16x32_bf16 v[108:111], a[236:239], a[172:175], v[108:111]\n"
    "ds_read_b128 a[136:139], v241 offset:20480\n"
    "v_add_f32_dpp v196, v253, v196 row_newbcast:4 row_mask:0xf bank_mask:0xf\n"
    "v_mfma_f32_16x16x32_bf16 v[108:111], a[232:235], a[140:143], v[108:111]\n"
    "ds_read_b128 a[168:171], v241 offset:21504\n"
    "v_add_f32_dpp v197, v253, v197 row_newbcast:5 row_mask:0xf bank_mask:0xf\n"
    "v_mfma_f32_16x16x32_bf16 v[140:143], a[232:235], a[144:147], v[140:143]\n"
    "ds_read_b128 a[172:175], v241 offset:23552\n"
    "v_add_f32_dpp v198, v253, v198 row_newbcast:6 row_mask:0xf bank_mask:0xf\n"
    "v_mfma_f32_16x16x32_bf16 v[140:143], a[236:239], a[176:179], v[140:143]\n"
    "ds_read_b128 a[140:143], v241 offset:22528\n"
    "v_add_f32_dpp v199, v253, v199 row_newbcast:7 row_mask:0xf bank_mask:0xf\n"
    "v_mfma_f32_16x16x32_bf16 v[172:175], a[236:239], a[180:183], v[172:175]\n"
    "ds_read_b128 a[144:147], v241 offset:24576\n"
    "v_add_f32_dpp v228, v253, v228 row_newbcast:4 row_mask:0xf bank_mask:0xf\n"
    "v_mfma_f32_16x16x32_bf16 v[172:175], a[232:235], a[148:151], v[172:175]\n"
    "ds_read_b128 a[176:179], v241 offset:25600\n"
    "v_add_f32_dpp v229, v253, v229 row_newbcast:5 row_mask:0xf bank_mask:0xf\n"
    "v_mfma_f32_16x16x32_bf16 v[204:207], a[232:235], a[152:155], v[204:207]\n"
    "ds_read_b128 a[180:183], v241 offset:27648\n"
    "v_add_f32_dpp v230, v253, v230 row_newbcast:6 row_mask:0xf bank_mask:0xf\n"
    "v_mfma_f32_16x16x32_bf16 v[204:207], a[236:239], a[184:187], v[204:207]\n"
    "ds_read_b128 a[148:151], v241 offset:26624\n"
    "v_add_f32_dpp v231, v253, v231 row_newbcast:7 row_mask:0xf bank_mask:0xf\n"
    "v_mfma_f32_16x16x32_bf16 v[236:239], a[236:239], a[188:191], v[236:239]\n"
    "ds_read_b128 a[152:155], v241 offset:28672\n"
    "v_mfma_f32_16x16x32_bf16 v[236:239], a[232:235], a[156:159], v[236:239]\n"
    "s_waitcnt vmcnt(8) lgkmcnt(11)\n"
    "ds_read_b128 a[184:187], v241 offset:29696\n"
    "v_mfma_f32_16x16x32_bf16 v[0:3], a[192:195], a[128:131], v[0:3]\n"
    "buffer_load_dwordx4 a[232:235], v240, s[20:23], s53 offen offset:2048 sc1\n"
    "v_mfma_f32_16x16x32_bf16 v[0:3], a[196:199], a[160:163], v[0:3]\n"
    "s_waitcnt lgkmcnt(10)\n"
    "v_mfma_f32_16x16x32_bf16 v[32:35], a[196:199], a[164:167], v[32:35]\n"
    "buffer_load_dwordx4 a[236:239], v240, s[20:23], s53 offen offset:3072 sc1\n"
    "v_mfma_f32_16x16x32_bf16 v[32:35], a[192:195], a[132:135], v[32:35]\n"
    "s_waitcnt lgkmcnt(8)\n"
    "ds_read_b128 a[188:191], v241 offset:31744\n"
    "ds_read_b128 a[156:159], v241 offset:30720\n"
    "v_mfma_f32_16x16x32_bf16 v[64:67], a[192:195], a[136:139], v[64:67]\n"
    "v_mfma_f32_16x16x32_bf16 v[64:67], a[196:199], a[168:171], v[64:67]\n"
    "s_waitcnt lgkmcnt(8)\n"
    "v_mfma_f32_16x16x32_bf16 v[96:99], a[196:199], a[172:175], v[96:99]\n"
    "v_mfma_f32_16x16x32_bf16 v[96:99], a[192:195], a[140:143], v[96:99]\n"
    "s_waitcnt lgkmcnt(6)\n"
    "v_mfma_f32_16x16x32_bf16 v[128:131], a[192:195], a[144:147], v[128:131]\n"
    "v_mfma_f32_16x16x32_bf16 v[128:131], a[196:199], a[176:179], v[128:131]\n"
    "s_waitcnt lgkmcnt(4)\n"
    "v_mfma_f32_16x16x32_bf16 v[160:163], a[196:199], a[180:183], v[160:163]\n"
    "v_mfma_f32_16x16x32_bf16 v[160:163], a[192:195], a[148:151], v[160:163]\n"
    "s_waitcnt lgkmcnt(2)\n"
    "v_mfma_f32_16x16x32_bf16 v[192:195], a[192:195], a[152:155], v[192:195]\n"
    "v_mfma_f32_16x16x32_bf16 v[192:195], a[196:199], a[184:187], v[192:195]\n"
    "s_waitcnt lgkmcnt(0)\n"
    "v_mfma_f32_16x16x32_bf16 v[224:227], a[196:199], a[188:191], v[224:227]\n"
    "v_mfma_f32_16x16x32_bf16 v[224:227], a[192:195], a[156:159], v[224:227]\n"
    "s_waitcnt vmcnt(8)\n"
    "s_addk_i32 s53, 4096\n"
    "v_mfma_f32_16x16x32_bf16 v[228:231], a[200:203], a[156:159], v[228:231]\n"
    "buffer_load_dwordx4 a[192:195], v240, s[20:23], s53 offen offset:0 sc1\n"
    "v_mfma_f32_16x16x32_bf16 v[228:231], a[204:207], a[188:191], v[228:231]\n"
    "buffer_load_dwordx4 a[196:199], v240, s[20:23], s53 offen offset:1024 sc1\n"
    "v_mfma_f32_16x16x32_bf16 v[196:199], a[204:207], a[184:187], v[196:199]\n"
    "v_accvgpr_read_b32 v245, a28\n"
    "v_accvgpr_read_b32 v247, a29\n"
    "v_mfma_f32_16x16x32_bf16 v[196:199], a[200:203], a[152:155], v[196:199]\n"
    "v_lshlrev_b32 v244, 16, v245\n"
    "v_lshlrev_b32 v246, 16, v247\n"
    "v_mfma_f32_16x16x32_bf16 v[164:167], a[200:203], a[148:151], v[164:167]\n"
    "v_and_b32 v245, s58, v245\n"
    "v_and_b32 v247, s58, v247\n"
    "v_mfma_f32_16x16x32_bf16 v[164:167], a[204:207], a[180:183], v[164:167]\n"
    "v_mul_f32 v244, v244, v224\n"
    "v_mul_f32 v246, v246, v226\n"
    "v_mfma_f32_16x16x32_bf16 v[132:135], a[204:207], a[176:179], v[132:135]\n"
    "v_mul_f32 v245, v245, v225\n"
    "v_mul_f32 v247, v247, v227\n"
    "v_mfma_f32_16x16x32_bf16 v[132:135], a[200:203], a[144:147], v[132:135]\n"
    "v_cvt_pk_bf16_f32 v224, v244, v245\n"
    "v_cvt_pk_bf16_f32 v225, v246, v247\n"
    "v_mfma_f32_16x16x32_bf16 v[100:103], a[200:203], a[140:143], v[100:103]\n"
    "v_accvgpr_read_b32 v245, a30\n"
    "v_accvgpr_read_b32 v247, a31\n"
    "v_mfma_f32_16x16x32_bf16 v[100:103], a[204:207], a[172:175], v[100:103]\n"
    "v_lshlrev_b32 v244, 16, v245\n"
    "v_lshlrev_b32 v246, 16, v247\n"
    "v_mfma_f32_16x16x32_bf16 v[68:71], a[204:207], a[168:171], v[68:71]\n"
    "v_and_b32 v245, s58, v245\n"
    "v_and_b32 v247, s58, v247\n"
    "v_mfma_f32_16x16x32_bf16 v[68:71], a[200:203], a[136:139], v[68:71]\n"
    "v_mul_f32 v244, v244, v228\n"
    "v_mul_f32 v246, v246, v230\n"
    "v_mfma_f32_16x16x32_bf16 v[36:39], a[200:203], a[132:135], v[36:39]\n"
    "v_mul_f32 v245, v245, v229\n"
    "v_mul_f32 v247, v247, v231\n"
    "v_mfma_f32_16x16x32_bf16 v[36:39], a[204:207], a[164:167], v[36:39]\n"
    "v_cvt_pk_bf16_f32 v226, v244, v245\n"
    "v_cvt_pk_bf16_f32 v227, v246, v247\n"
    "v_mfma_f32_16x16x32_bf16 v[4:7], a[204:207], a[160:163], v[4:7]\n"
    "ds_write_b128 v248, v[224:227] offset:2048\n"
    "v_mfma_f32_16x16x32_bf16 v[4:7], a[200:203], a[128:131], v[4:7]\n"
    "s_waitcnt vmcnt(8)\n"
    "v_accvgpr_read_b32 v245, a24\n"
    "v_accvgpr_read_b32 v247, a25\n"
    "v_mfma_f32_16x16x32_bf16 v[8:11], a[208:211], a[128:131], v[8:11]\n"
    "buffer_load_dwordx4 a[200:203], v240, s[20:23], s53 offen offset:2048 sc1\n"
    "v_mfma_f32_16x16x32_bf16 v[8:11], a[212:215], a[160:163], v[8:11]\n"
    "buffer_load_dwordx4 a[204:207], v240, s[20:23], s53 offen offset:3072 sc1\n"
    "v_mfma_f32_16x16x32_bf16 v[40:43], a[212:215], a[164:167], v[40:43]\n"
    "v_lshlrev_b32 v244, 16, v245\n"
    "v_lshlrev_b32 v246, 16, v247\n"
    "v_mfma_f32_16x16x32_bf16 v[40:43], a[208:211], a[132:135], v[40:43]\n"
    "v_and_b32 v245, s58, v245\n"
    "v_and_b32 v247, s58, v247\n"
    "v_mfma_f32_16x16x32_bf16 v[72:75], a[208:211], a[136:139], v[72:75]\n"
    "v_mul_f32 v244, v244, v192\n"
    "v_mul_f32 v246, v246, v194\n"
    "v_mfma_f32_16x16x32_bf16 v[72:75], a[212:215], a[168:171], v[72:75]\n"
    "v_mul_f32 v245, v245, v193\n"
    "v_mul_f32 v247, v247, v195\n"
    "v_mfma_f32_16x16x32_bf16 v[104:107], a[212:215], a[172:175], v[104:107]\n"
    "v_cvt_pk_bf16_f32 v192, v244, v245\n"
    "v_cvt_pk_bf16_f32 v193, v246, v247\n"
    "v_mfma_f32_16x16x32_bf16 v[104:107], a[208:211], a[140:143], v[104:107]\n"
    "v_accvgpr_read_b32 v245, a26\n"
    "v_accvgpr_read_b32 v247, a27\n"
    "v_mfma_f32_16x16x32_bf16 v[136:139], a[208:211], a[144:147], v[136:139]\n"
    "buffer_load_dwordx4 a[28:31], v240, s[20:23], s91 offen offset:0 sc1\n"
    "v_mfma_f32_16x16x32_bf16 v[136:139], a[212:215], a[176:179], v[136:139]\n"
    "v_lshlrev_b32 v244, 16, v245\n"
    "v_lshlrev_b32 v246, 16, v247\n"
    "v_mfma_f32_16x16x32_bf16 v[168:171], a[212:215], a[180:183], v[168:171]\n"
    "v_and_b32 v245, s58, v245\n"
    "v_and_b32 v247, s58, v247\n"
    "v_mfma_f32_16x16x32_bf16 v[168:171], a[208:211], a[148:151], v[168:171]\n"
    "v_mul_f32 v244, v244, v196\n"
    "v_mul_f32 v246, v246, v198\n"
    "v_mfma_f32_16x16x32_bf16 v[200:203], a[208:211], a[152:155], v[200:203]\n"
    "v_mul_f32 v245, v245, v197\n"
    "v_mul_f32 v247, v247, v199\n"
    "v_mfma_f32_16x16x32_bf16 v[200:203], a[212:215], a[184:187], v[200:203]\n"
    "v_cvt_pk_bf16_f32 v194, v244, v245\n"
    "v_cvt_pk_bf16_f32 v195, v246, v247\n"
    "v_mfma_f32_16x16x32_bf16 v[232:235], a[212:215], a[188:191], v[232:235]\n"
    "ds_write_b128 v248, v[192:195] offset:0\n"
    "v_mfma_f32_16x16x32_bf16 v[232:235], a[208:211], a[156:159], v[232:235]\n"
    "s_waitcnt vmcnt(9)\n"
    "s_addk_i32 s53, -20480\n"
    "v_mfma_f32_16x16x32_bf16 v[236:239], a[216:219], a[156:159], v[236:239]\n"
    "buffer_load_dwordx4 a[208:211], v240, s[20:23], s53 offen offset:0 sc1\n"
    "v_mfma_f32_16x16x32_bf16 v[236:239], a[220:223], a[188:191], v[236:239]\n"
    "buffer_load_dwordx4 a[212:215], v240, s[20:23], s53 offen offset:1024 sc1\n"
    "v_mfma_f32_16x16x32_bf16 v[204:207], a[220:223], a[184:187], v[204:207]\n"
    "v_accvgpr_read_b32 v245, a60\n"
    "v_accvgpr_read_b32 v247, a61\n"
    "v_mfma_f32_16x16x32_bf16 v[204:207], a[216:219], a[152:155], v[204:207]\n"
    "v_lshlrev_b32 v244, 16, v245\n"
    "v_lshlrev_b32 v246, 16, v247\n"
    "v_mfma_f32_16x16x32_bf16 v[172:175], a[216:219], a[148:151], v[172:175]\n"
    "v_and_b32 v245, s58, v245\n"
    "v_and_b32 v247, s58, v247\n"
    "v_mfma_f32_16x16x32_bf16 v[172:175], a[220:223], a[180:183], v[172:175]\n"
    "v_mul_f32 v244, v244, v232\n"
    "v_mul_f32 v246, v246, v234\n"
    "v_mfma_f32_16x16x32_bf16 v[140:143], a[220:223], a[176:179], v[140:143]\n"
    "v_mul_f32 v245, v245, v233\n"
    "v_mul_f32 v247, v247, v235\n"
    "v_mfma_f32_16x16x32_bf16 v[140:143], a[216:219], a[144:147], v[140:143]\n"
    "v_cvt_pk_bf16_f32 v232, v244, v245\n"
    "v_cvt_pk_bf16_f32 v233, v246, v247\n"
    "v_mfma_f32_16x16x32_bf16 v[108:111], a[216:219], a[140:143], v[108:111]\n"
    "buffer_load_dwordx4 v[224:227], v240, s[20:23], s91 offen offset:1024 sc1\n"
    "v_mfma_f32_16x16x32_bf16 v[108:111], a[220:223], a[172:175], v[108:111]\n"
    "v_accvgpr_read_b32 v245, a62\n"
    "v_accvgpr_read_b32 v247, a63\n"
    "v_mfma_f32_16x16x32_bf16 v[76:79], a[220:223], a[168:171], v[76:79]\n"
    "v_lshlrev_b32 v244, 16, v245\n"
    "v_lshlrev_b32 v246, 16, v247\n"
    "v_mfma_f32_16x16x32_bf16 v[76:79], a[216:219], a[136:139], v[76:79]\n"
    "v_and_b32 v245, s58, v245\n"
    "v_and_b32 v247, s58, v247\n"
    "v_mfma_f32_16x16x32_bf16 v[44:47], a[216:219], a[132:135], v[44:47]\n"
    "v_mul_f32 v244, v244, v236\n"
    "v_mul_f32 v246, v246, v238\n"
    "v_mfma_f32_16x16x32_bf16 v[44:47], a[220:223], a[164:167], v[44:47]\n"
    "v_mul_f32 v245, v245, v237\n"
    "v_mul_f32 v247, v247, v239\n"
    "v_mfma_f32_16x16x32_bf16 v[12:15], a[220:223], a[160:163], v[12:15]\n"
    "v_cvt_pk_bf16_f32 v234, v244, v245\n"
    "v_cvt_pk_bf16_f32 v235, v246, v247\n"
    "v_mfma_f32_16x16x32_bf16 v[12:15], a[216:219], a[128:131], v[12:15]\n"
    "s_waitcnt vmcnt(10)\n"
    "v_mfma_f32_16x16x32_bf16 v[16:19], a[224:227], a[128:131], v[16:19]\n"
    "ds_write_b128 v249, v[232:235] offset:2048\n"
    "v_mfma_f32_16x16x32_bf16 v[16:19], a[228:231], a[160:163], v[16:19]\n"
    "buffer_load_dwordx4 a[216:219], v240, s[20:23], s53 offen offset:2048 sc1\n"
    "v_mfma_f32_16x16x32_bf16 v[48:51], a[228:231], a[164:167], v[48:51]\n"
    "buffer_load_dwordx4 a[220:223], v240, s[20:23], s53 offen offset:3072 sc1\n"
    "v_mfma_f32_16x16x32_bf16 v[48:51], a[224:227], a[132:135], v[48:51]\n"
    "ds_read_b128 v[228:231], v242 offset:2048\n"
    "ds_read_b128 v[236:239], v242 offset:3072\n"
    "v_mfma_f32_16x16x32_bf16 v[80:83], a[224:227], a[136:139], v[80:83]\n"
    "v_accvgpr_read_b32 v245, a56\n"
    "v_accvgpr_read_b32 v247, a57\n"
    "v_mfma_f32_16x16x32_bf16 v[80:83], a[228:231], a[168:171], v[80:83]\n"
    "v_lshlrev_b32 v244, 16, v245\n"
    "v_lshlrev_b32 v246, 16, v247\n"
    "v_mfma_f32_16x16x32_bf16 v[112:115], a[228:231], a[172:175], v[112:115]\n"
    "v_and_b32 v245, s58, v245\n"
    "v_and_b32 v247, s58, v247\n"
    "v_mfma_f32_16x16x32_bf16 v[112:115], a[224:227], a[140:143], v[112:115]\n"
    "s_add_u32 s63, s70, 229376\n"
    "s_waitcnt lgkmcnt(1)\n"
    "v_mul_f32 v244, v244, v200\n"
    "v_mfma_f32_16x16x32_bf16 v[144:147], a[224:227], a[144:147], v[144:147]\n"
    "buffer_store_dwordx4 v[228:231], v243, s[40:43], s63 offen nt\n"
    "buffer_load_dwordx4 a[24:27], v240, s[20:23], s91 offen offset:2048 sc1\n"
    "v_mfma_f32_16x16x32_bf16 v[144:147], a[228:231], a[176:179], v[144:147]\n"
    "v_mul_f32 v246, v246, v202\n"
    "v_mul_f32 v245, v245, v201\n"
    "v_mfma_f32_16x16x32_bf16 v[176:179], a[228:231], a[180:183], v[176:179]\n"
    "v_mul_f32 v247, v247, v203\n"
    "v_cvt_pk_bf16_f32 v200, v244, v245\n"
    "v_mfma_f32_16x16x32_bf16 v[176:179], a[224:227], a[148:151], v[176:179]\n"
    "v_cvt_pk_bf16_f32 v201, v246, v247\n"
    "v_accvgpr_read_b32 v245, a58\n"
    "v_mfma_f32_16x16x32_bf16 v[208:211], a[224:227], a[152:155], v[208:211]\n"
    "v_accvgpr_read_b32 v247, a59\n"
    "v_lshlrev_b32 v244, 16, v245\n"
    "v_mfma_f32_16x16x32_bf16 v[208:211], a[228:231], a[184:187], v[208:211]\n"
    "v_lshlrev_b32 v246, 16, v247\n"
    "v_and_b32 v245, s58, v245\n"
    "v_mfma_f32_16x16x32_bf16 a[240:243], a[228:231], a[188:191], a[240:243]\n"
    "v_and_b32 v247, s58, v247\n"
    "v_mul_f32 v244, v244, v204\n"
    "v_mfma_f32_16x16x32_bf16 a[240:243], a[224:227], a[156:159], a[240:243]\n"
    "s_waitcnt vmcnt(12)\n"
    "s_addk_i32 s53, 4096\n"
    "v_mul_f32 v246, v246, v206\n"
    "v_mfma_f32_16x16x32_bf16 a[244:247], a[232:235], a[156:159], a[244:247]\n"
    "buffer_load_dwordx4 a[224:227], v240, s[20:23], s53 offen offset:0 sc1\n"
    "v_mfma_f32_16x16x32_bf16 a[244:247], a[236:239], a[188:191], a[244:247]\n"
    "buffer_load_dwordx4 a[228:231], v240, s[20:23], s53 offen offset:1024 sc1\n"
    "v_mfma_f32_16x16x32_bf16 v[212:215], a[236:239], a[184:187], v[212:215]\n"
    "v_mul_f32 v245, v245, v205\n"
    "v_mul_f32 v247, v247, v207\n"
    "v_mfma_f32_16x16x32_bf16 v[212:215], a[232:235], a[152:155], v[212:215]\n"
    "s_add_u32 s63, s70, 245760\n"
    "s_waitcnt lgkmcnt(0)\n"
    "v_cvt_pk_bf16_f32 v202, v244, v245\n"
    "v_mfma_f32_16x16x32_bf16 v[180:183], a[232:235], a[148:151], v[180:183]\n"
    "buffer_store_dwordx4 v[236:239], v243, s[40:43], s63 offen nt\n"
    "v_cvt_pk_bf16_f32 v203, v246, v247\n"
    "v_mfma_f32_16x16x32_bf16 v[180:183], a[236:239], a[180:183], v[180:183]\n"
    "ds_write_b128 v249, v[200:203] offset:0\n"
    "v_mfma_f32_16x16x32_bf16 v[148:151], a[236:239], a[176:179], v[148:151]\n"
    "ds_read_b128 v[196:199], v242 offset:0\n"
    "ds_read_b128 v[204:207], v242 offset:1024\n"
    "v_mfma_f32_16x16x32_bf16 v[148:151], a[232:235], a[144:147], v[148:151]\n"
    "v_accvgpr_read_b32 v245, a20\n"
    "v_accvgpr_read_b32 v247, a21\n"
    "v_mfma_f32_16x16x32_bf16 v[116:119], a[232:235], a[140:143], v[116:119]\n"
    "buffer_load_dwordx4 v[192:195], v240, s[20:23], s91 offen offset:3072 sc1\n"
    "v_mfma_f32_16x16x32_bf16 v[116:119], a[236:239], a[172:175], v[116:119]\n"
    "s_addk_i32 s91, 4096\n"
    "v_lshlrev_b32 v244, 16, v245\n"
    "v_lshlrev_b32 v246, 16, v247\n"
    "v_mfma_f32_16x16x32_bf16 v[84:87], a[236:239], a[168:171], v[84:87]\n"
    "v_and_b32 v245, s58, v245\n"
    "v_and_b32 v247, s58, v247\n"
    "v_mfma_f32_16x16x32_bf16 v[84:87], a[232:235], a[136:139], v[84:87]\n"
    "v_mul_f32 v244, v244, v160\n"
    "v_mul_f32 v246, v246, v162\n"
    "v_mfma_f32_16x16x32_bf16 v[52:55], a[232:235], a[132:135], v[52:55]\n"
    "v_mul_f32 v245, v245, v161\n"
    "v_mul_f32 v247, v247, v163\n"
    "v_mfma_f32_16x16x32_bf16 v[52:55], a[236:239], a[164:167], v[52:55]\n"
    "v_cvt_pk_bf16_f32 v160, v244, v245\n"
    "v_cvt_pk_bf16_f32 v161, v246, v247\n"
    "v_mfma_f32_16x16x32_bf16 v[20:23], a[236:239], a[160:163], v[20:23]\n"
    "v_accvgpr_read_b32 v245, a22\n"
    "v_accvgpr_read_b32 v247, a23\n"
    "v_mfma_f32_16x16x32_bf16 v[20:23], a[232:235], a[128:131], v[20:23]\n"
    "s_waitcnt vmcnt(14)\n"
    "v_lshlrev_b32 v244, 16, v245\n"
    "v_lshlrev_b32 v246, 16, v247\n"
    "v_mfma_f32_16x16x32_bf16 v[24:27], a[192:195], a[128:131], v[24:27]\n"
    "buffer_load_dwordx4 a[232:235], v240, s[20:23], s53 offen offset:2048 sc1\n"
    "v_mfma_f32_16x16x32_bf16 v[24:27], a[196:199], a[160:163], v[24:27]\n"
    "buffer_load_dwordx4 a[236:239], v240, s[20:23], s53 offen offset:3072 sc1\n"
    "v_mfma_f32_16x16x32_bf16 v[56:59], a[196:199], a[164:167], v[56:59]\n"
    "v_and_b32 v245, s58, v245\n"
    "v_and_b32 v247, s58, v247\n"
    "v_mfma_f32_16x16x32_bf16 v[56:59], a[192:195], a[132:135], v[56:59]\n"
    "s_add_u32 s63, s70, 196608\n"
    "s_waitcnt lgkmcnt(1)\n"
    "v_mul_f32 v244, v244, v164\n"
    "v_mfma_f32_16x16x32_bf16 v[88:91], a[192:195], a[136:139], v[88:91]\n"
    "buffer_store_dwordx4 v[196:199], v243, s[40:43], s63 offen nt\n"
    "v_mul_f32 v246, v246, v166\n"
    "v_mul_f32 v245, v245, v165\n"
    "v_mul_f32 v247, v247, v167\n"
    "v_mfma_f32_16x16x32_bf16 v[88:91], a[196:199], a[168:171], v[88:91]\n"
    "v_cvt_pk_bf16_f32 v162, v244, v245\n"
    "v_cvt_pk_bf16_f32 v163, v246, v247\n"
    "v_mfma_f32_16x16x32_bf16 v[120:123], a[196:199], a[172:175], v[120:123]\n"
    "ds_write_b128 v248, v[160:163] offset:2048\n"
    "v_mfma_f32_16x16x32_bf16 v[120:123], a[192:195], a[140:143], v[120:123]\n"
    "v_accvgpr_read_b32 v245, a52\n"
    "v_accvgpr_read_b32 v247, a53\n"
    "v_mfma_f32_16x16x32_bf16 v[152:155], a[192:195], a[144:147], v[152:155]\n"
    "buffer_load_dwordx4 a[60:63], v240, s[20:23], s91 offen offset:0 sc1\n"
    "v_mfma_f32_16x16x32_bf16 v[152:155], a[196:199], a[176:179], v[152:155]\n"
    "v_lshlrev_b32 v244, 16, v245\n"
    "v_lshlrev_b32 v246, 16, v247\n"
    "v_mfma_f32_16x16x32_bf16 v[184:187], a[196:199], a[180:183], v[184:187]\n"
    "v_and_b32 v245, s58, v245\n"
    "v_and_b32 v247, s58, v247\n"
    "v_mfma_f32_16x16x32_bf16 v[184:187], a[192:195], a[148:151], v[184:187]\n"
    "v_mul_f32 v244, v244, v168\n"
    "v_mul_f32 v246, v246, v170\n"
    "v_mfma_f32_16x16x32_bf16 v[216:219], a[192:195], a[152:155], v[216:219]\n"
    "v_mul_f32 v245, v245, v169\n"
    "v_mul_f32 v247, v247, v171\n"
    "v_mfma_f32_16x16x32_bf16 v[216:219], a[196:199], a[184:187], v[216:219]\n"
    "v_cvt_pk_bf16_f32 v168, v244, v245\n"
    "v_cvt_pk_bf16_f32 v169, v246, v247\n"
    "v_mfma_f32_16x16x32_bf16 a[248:251], a[196:199], a[188:191], a[248:251]\n"
    "v_accvgpr_read_b32 v245, a54\n"
    "v_accvgpr_read_b32 v247, a55\n"
    "v_mfma_f32_16x16x32_bf16 a[248:251], a[192:195], a[156:159], a[248:251]\n"
    "s_waitcnt vmcnt(16)\n"
    "s_addk_i32 s53, -20480\n"
    "v_lshlrev_b32 v244, 16, v245\n"
    "v_mfma_f32_16x16x32_bf16 a[252:255], a[200:203], a[156:159], a[252:255]\n"
    "buffer_load_dwordx4 a[192:195], v240, s[20:23], s53 offen offset:0 sc1\n"
    "v_mfma_f32_16x16x32_bf16 a[252:255], a[204:207], a[188:191], a[252:255]\n"
    "buffer_load_dwordx4 a[196:199], v240, s[20:23], s53 offen offset:1024 sc1\n"
    "v_mfma_f32_16x16x32_bf16 v[220:223], a[204:207], a[184:187], v[220:223]\n"
    "ds_read_b128 a[156:159], v241 offset:14336\n"
    "v_lshlrev_b32 v246, 16, v247\n"
    "v_mfma_f32_16x16x32_bf16 v[220:223], a[200:203], a[152:155], v[220:223]\n"
    "s_add_u32 s63, s70, 212992\n"
    "s_waitcnt lgkmcnt(2)\n"
    "v_and_b32 v245, s58, v245\n"
    "v_mfma_f32_16x16x32_bf16 v[188:191], a[200:203], a[148:151], v[188:191]\n"
    "buffer_store_dwordx4 v[204:207], v243, s[40:43], s63 offen nt\n"
    "ds_read_b128 a[188:191], v241 offset:15360\n"
    "ds_read_b128 a[184:187], v241 offset:13312\n"
    "v_and_b32 v247, s58, v247\n"
    "v_mfma_f32_16x16x32_bf16 v[188:191], a[204:207], a[180:183], v[188:191]\n"
    "ds_read_b128 a[152:155], v241 offset:12288\n"
    "v_mul_f32 v244, v244, v172\n"
    "v_mfma_f32_16x16x32_bf16 v[156:159], a[204:207], a[176:179], v[156:159]\n"
    "ds_read_b128 a[148:151], v241 offset:10240\n"
    "v_mul_f32 v246, v246, v174\n"
    "v_mfma_f32_16x16x32_bf16 v[156:159], a[200:203], a[144:147], v[156:159]\n"
    "ds_read_b128 a[180:183], v241 offset:11264\n"
    "v_mul_f32 v245, v245, v173\n"
    "v_mfma_f32_16x16x32_bf16 v[124:127], a[200:203], a[140:143], v[124:127]\n"
    "buffer_load_dwordx4 v[232:235], v240, s[20:23], s91 offen offset:1024 sc1\n"
    "v_mfma_f32_16x16x32_bf16 v[124:127], a[204:207], a[172:175], v[124:127]\n"
    "ds_read_b128 a[176:179], v241 offset:9216\n"
    "ds_read_b128 a[144:147], v241 offset:8192\n"
    "v_mfma_f32_16x16x32_bf16 v[92:95], a[204:207], a[168:171], v[92:95]\n"
    "ds_read_b128 a[140:143], v241 offset:6144\n"
    "v_mul_f32 v247, v247, v175\n"
    "v_mfma_f32_16x16x32_bf16 v[92:95], a[200:203], a[136:139], v[92:95]\n"
    "ds_read_b128 a[172:175], v241 offset:7168\n"
    "v_cvt_pk_bf16_f32 v170, v244, v245\n"
    "v_mfma_f32_16x16x32_bf16 v[60:63], a[200:203], a[132:135], v[60:63]\n"
    "ds_read_b128 a[168:171], v241 offset:5120\n"
    "v_cvt_pk_bf16_f32 v171, v246, v247\n"
    "v_mfma_f32_16x16x32_bf16 v[60:63], a[204:207], a[164:167], v[60:63]\n"
    "ds_write_b128 v249, v[168:171] offset:2048\n"
    "v_mfma_f32_16x16x32_bf16 v[28:31], a[204:207], a[160:163], v[28:31]\n"
    "ds_read_b128 a[136:139], v241 offset:4096\n"
    "ds_read_b128 v[164:167], v242 offset:2048\n"
    "v_mfma_f32_16x16x32_bf16 v[28:31], a[200:203], a[128:131], v[28:31]\n"
    "s_waitcnt vmcnt(17) lgkmcnt(12)\n"
    "ds_read_b128 v[172:175], v242 offset:3072\n"
    "ds_read_b128 a[132:135], v241 offset:2048\n"
    "v_mfma_f32_16x16x32_bf16 a[240:243], a[208:211], a[156:159], a[240:243]\n"
    "ds_read_b128 a[164:167], v241 offset:3072\n"
    "v_accvgpr_read_b32 v245, a16\n"
    "v_mfma_f32_16x16x32_bf16 a[240:243], a[212:215], a[188:191], a[240:243]\n"
    "s_waitcnt lgkmcnt(13)\n"
    "v_accvgpr_read_b32 v247, a17\n"
    "v_lshlrev_b32 v244, 16, v245\n"
    "v_mfma_f32_16x16x32_bf16 v[208:211], a[212:215], a[184:187], v[208:211]\n"
    "buffer_load_dwordx4 a[200:203], v240, s[20:23], s53 offen offset:2048 sc1\n"
    "v_mfma_f32_16x16x32_bf16 v[208:211], a[208:211], a[152:155], v[208:211]\n"
    "s_waitcnt lgkmcnt(11)\n"
    "v_lshlrev_b32 v246, 16, v247\n"
    "v_and_b32 v245, s58, v245\n"
    "v_mfma_f32_16x16x32_bf16 v[176:179], a[208:211], a[148:151], v[176:179]\n"
    "buffer_load_dwordx4 a[204:207], v240, s[20:23], s53 offen offset:3072 sc1\n"
    "ds_read_b128 a[160:163], v241 offset:1024\n"
    "ds_read_b128 a[128:131], v241 offset:0\n"
    "v_mfma_f32_16x16x32_bf16 v[176:179], a[212:215], a[180:183], v[176:179]\n"
    "v_and_b32 v247, s58, v247\n"
    "s_add_u32 s63, s70, 163840\n"
    "s_waitcnt lgkmcnt(5)\n"
    "v_mfma_f32_16x16x32_bf16 v[144:147], a[212:215], a[176:179], v[144:147]\n"
    "buffer_store_dwordx4 v[164:167], v243, s[40:43], s63 offen nt\n"
    "v_mul_f32 v244, v244, v128\n"
    "v_mul_f32 v246, v246, v130\n"
    "v_mul_f32 v245, v245, v129\n"
    "v_mfma_f32_16x16x32_bf16 v[144:147], a[208:211], a[144:147], v[144:147]\n"
    "v_mul_f32 v247, v247, v131\n"
    "v_cvt_pk_bf16_f32 v128, v244, v245\n"
    "v_mfma_f32_16x16x32_bf16 v[112:115], a[208:211], a[140:143], v[112:115]\n"
    "buffer_load_dwordx4 v[228:231], v240, s[20:23], s91 offen offset:2048 sc1\n"
    "v_mfma_f32_16x16x32_bf16 v[112:115], a[212:215], a[172:175], v[112:115]\n"
    "v_cvt_pk_bf16_f32 v129, v246, v247\n"
    "v_accvgpr_read_b32 v245, a18\n"
    "v_mfma_f32_16x16x32_bf16 v[80:83], a[212:215], a[168:171], v[80:83]\n"
    "v_accvgpr_read_b32 v247, a19\n"
    "v_lshlrev_b32 v244, 16, v245\n"
    "v_mfma_f32_16x16x32_bf16 v[80:83], a[208:211], a[136:139], v[80:83]\n"
    "s_waitcnt lgkmcnt(2)\n"
    "v_lshlrev_b32 v246, 16, v247\n"
    "v_and_b32 v245, s58, v245\n"
    "v_mfma_f32_16x16x32_bf16 v[48:51], a[208:211], a[132:135], v[48:51]\n"
    "v_and_b32 v247, s58, v247\n"
    "v_mul_f32 v244, v244, v132\n"
    "v_mfma_f32_16x16x32_bf16 v[48:51], a[212:215], a[164:167], v[48:51]\n"
    "s_waitcnt lgkmcnt(0)\n"
    "v_mul_f32 v246, v246, v134\n"
    "v_mul_f32 v245, v245, v133\n"
    "v_mfma_f32_16x16x32_bf16 v[16:19], a[212:215], a[160:163], v[16:19]\n"
    "v_mul_f32 v247, v247, v135\n"
    "v_cvt_pk_bf16_f32 v130, v244, v245\n"
    "v_mfma_f32_16x16x32_bf16 v[16:19], a[208:211], a[128:131], v[16:19]\n"
    "s_waitcnt vmcnt(18)\n"
    "s_addk_i32 s53, 4096\n"
    "v_cvt_pk_bf16_f32 v131, v246, v247\n"
    "v_mfma_f32_16x16x32_bf16 v[20:23], a[216:219], a[128:131], v[20:23]\n"
    "buffer_load_dwordx4 a[208:211], v240, s[20:23], s53 offen offset:0 sc1\n"
    "v_mfma_f32_16x16x32_bf16 v[20:23], a[220:223], a[160:163], v[20:23]\n"
    "buffer_load_dwordx4 a[212:215], v240, s[20:23], s53 offen offset:1024 sc1\n"
    "v_mfma_f32_16x16x32_bf16 v[52:55], a[220:223], a[164:167], v[52:55]\n"
    "ds_write_b128 v248, v[128:131] offset:0\n"
    "v_mfma_f32_16x16x32_bf16 v[52:55], a[216:219], a[132:135], v[52:55]\n"
    "s_add_u32 s63, s70, 180224\n"
    "v_accvgpr_read_b32 v245, a48\n"
    "v_mfma_f32_16x16x32_bf16 v[84:87], a[216:219], a[136:139], v[84:87]\n"
    "buffer_store_dwordx4 v[172:175], v243, s[40:43], s63 offen nt\n"
    "v_accvgpr_read_b32 v247, a49\n"
    "v_lshlrev_b32 v244, 16, v245\n"
    "v_lshlrev_b32 v246, 16, v247\n"
    "v_mfma_f32_16x16x32_bf16 v[84:87], a[220:223], a[168:171], v[84:87]\n"
    "v_and_b32 v245, s58, v245\n"
    "v_and_b32 v247, s58, v247\n"
    "v_mfma_f32_16x16x32_bf16 v[116:119], a[220:223], a[172:175], v[116:119]\n"
    "v_mul_f32 v244, v244, v136\n"
    "v_mul_f32 v246, v246, v138\n"
    "v_mfma_f32_16x16x32_bf16 v[116:119], a[216:219], a[140:143], v[116:119]\n"
    "v_mul_f32 v245, v245, v137\n"
    "v_mul_f32 v247, v247, v139\n"
    "v_mfma_f32_16x16x32_bf16 v[148:151], a[216:219], a[144:147], v[148:151]\n"
    "buffer_load_dwordx4 v[236:239], v240, s[20:23], s91 offen offset:3072 sc1\n"
    "v_mfma_f32_16x16x32_bf16 v[148:151], a[220:223], a[176:179], v[148:151]\n"
    "s_addk_i32 s91, 4096\n"
    "v_cvt_pk_bf16_f32 v136, v244, v245\n"
    "v_cvt_pk_bf16_f32 v137, v246, v247\n"
    "v_mfma_f32_16x16x32_bf16 v[180:183], a[220:223], a[180:183], v[180:183]\n"
    "v_accvgpr_read_b32 v245, a50\n"
    "v_accvgpr_read_b32 v247, a51\n"
    "v_mfma_f32_16x16x32_bf16 v[180:183], a[216:219], a[148:151], v[180:183]\n"
    "v_lshlrev_b32 v244, 16, v245\n"
    "v_lshlrev_b32 v246, 16, v247\n"
    "v_mfma_f32_16x16x32_bf16 v[212:215], a[216:219], a[152:155], v[212:215]\n"
    "v_and_b32 v245, s58, v245\n"
    "v_and_b32 v247, s58, v247\n"
    "v_mfma_f32_16x16x32_bf16 v[212:215], a[220:223], a[184:187], v[212:215]\n"
    "v_mul_f32 v244, v244, v140\n"
    "v_mul_f32 v246, v246, v142\n"
    "v_mfma_f32_16x16x32_bf16 a[244:247], a[220:223], a[188:191], a[244:247]\n"
    "v_mul_f32 v245, v245, v141\n"
    "v_mul_f32 v247, v247, v143\n"
    "v_mfma_f32_16x16x32_bf16 a[244:247], a[216:219], a[156:159], a[244:247]\n"
    "s_waitcnt vmcnt(18)\n"
    "v_cvt_pk_bf16_f32 v138, v244, v245\n"
    "v_cvt_pk_bf16_f32 v139, v246, v247\n"
    "v_mfma_f32_16x16x32_bf16 a[248:251], a[224:227], a[156:159], a[248:251]\n"
    "buffer_load_dwordx4 a[216:219], v240, s[20:23], s53 offen offset:2048 sc1\n"
    "v_mfma_f32_16x16x32_bf16 a[248:251], a[228:231], a[188:191], a[248:251]\n"
    "buffer_load_dwordx4 a[220:223], v240, s[20:23], s53 offen offset:3072 sc1\n"
    "v_mfma_f32_16x16x32_bf16 v[216:219], a[228:231], a[184:187], v[216:219]\n"
    "ds_write_b128 v249, v[136:139] offset:0\n"
    "v_mfma_f32_16x16x32_bf16 v[216:219], a[224:227], a[152:155], v[216:219]\n"
    "ds_read_b128 v[132:135], v242 offset:0\n"
    "ds_read_b128 v[140:143], v242 offset:1024\n"
    "v_mfma_f32_16x16x32_bf16 v[184:187], a[224:227], a[148:151], v[184:187]\n"
    "v_accvgpr_read_b32 v245, a12\n"
    "v_accvgpr_read_b32 v247, a13\n"
    "v_mfma_f32_16x16x32_bf16 v[184:187], a[228:231], a[180:183], v[184:187]\n"
    "v_lshlrev_b32 v244, 16, v245\n"
    "v_lshlrev_b32 v246, 16, v247\n"
    "v_mfma_f32_16x16x32_bf16 v[152:155], a[228:231], a[176:179], v[152:155]\n"
    "v_and_b32 v245, s58, v245\n"
    "v_and_b32 v247, s58, v247\n"
    "v_mfma_f32_16x16x32_bf16 v[152:155], a[224:227], a[144:147], v[152:155]\n"
    "v_mul_f32 v244, v244, v96\n"
    "v_mul_f32 v246, v246, v98\n"
    "v_mfma_f32_16x16x32_bf16 v[120:123], a[224:227], a[140:143], v[120:123]\n"
    "buffer_load_dwordx4 a[56:59], v240, s[20:23], s91 offen offset:0 sc1\n"
    "v_mfma_f32_16x16x32_bf16 v[120:123], a[228:231], a[172:175], v[120:123]\n"
    "v_mul_f32 v245, v245, v97\n"
    "v_mul_f32 v247, v247, v99\n"
    "v_mfma_f32_16x16x32_bf16 v[88:91], a[228:231], a[168:171], v[88:91]\n"
    "v_cvt_pk_bf16_f32 v96, v244, v245\n"
    "v_cvt_pk_bf16_f32 v97, v246, v247\n"
    "v_mfma_f32_16x16x32_bf16 v[88:91], a[224:227], a[136:139], v[88:91]\n"
    "v_accvgpr_read_b32 v245, a14\n"
    "v_accvgpr_read_b32 v247, a15\n"
    "v_mfma_f32_16x16x32_bf16 v[56:59], a[224:227], a[132:135], v[56:59]\n"
    "v_lshlrev_b32 v244, 16, v245\n"
    "v_lshlrev_b32 v246, 16, v247\n"
    "v_mfma_f32_16x16x32_bf16 v[56:59], a[228:231], a[164:167], v[56:59]\n"
    "v_and_b32 v245, s58, v245\n"
    "v_and_b32 v247, s58, v247\n"
    "v_mfma_f32_16x16x32_bf16 v[24:27], a[228:231], a[160:163], v[24:27]\n"
    "v_mul_f32 v244, v244, v100\n"
    "v_mul_f32 v246, v246, v102\n"
    "v_mfma_f32_16x16x32_bf16 v[24:27], a[224:227], a[128:131], v[24:27]\n"
    "s_waitcnt vmcnt(17)\n"
    "s_addk_i32 s53, -20480\n"
    "v_mul_f32 v245, v245, v101\n"
    "v_mfma_f32_16x16x32_bf16 v[28:31], a[232:235], a[128:131], v[28:31]\n"
    "buffer_load_dwordx4 a[224:227], v240, s[20:23], s53 offen offset:0 sc1\n"
    "v_mfma_f32_16x16x32_bf16 v[28:31], a[236:239], a[160:163], v[28:31]\n"
    "buffer_load_dwordx4 a[228:231], v240, s[20:23], s53 offen offset:1024 sc1\n"
    "v_mfma_f32_16x16x32_bf16 v[60:63], a[236:239], a[164:167], v[60:63]\n"
    "v_add_u32 v241, s66, v240\n"
    "ds_read_b128 a[128:131], v241 offset:49152\n"
    "v_mfma_f32_16x16x32_bf16 v[60:63], a[232:235], a[132:135], v[60:63]\n"
    "s_add_u32 s63, s70, 131072\n"
    "s_waitcnt lgkmcnt(2)\n"
    "v_mul_f32 v247, v247, v103\n"
    "v_mfma_f32_16x16x32_bf16 v[92:95], a[232:235], a[136:139], v[92:95]\n"
    "buffer_store_dwordx4 v[132:135], v243, s[40:43], s63 offen nt\n"
    "ds_read_b128 a[160:163], v241 offset:50176\n"
    "ds_read_b128 a[164:167], v241 offset:52224\n"
    "v_cvt_pk_bf16_f32 v98, v244, v245\n"
    "v_mfma_f32_16x16x32_bf16 v[92:95], a[236:239], a[168:171], v[92:95]\n"
    "ds_read_b128 a[132:135], v241 offset:51200\n"
    "v_cvt_pk_bf16_f32 v99, v246, v247\n"
    "v_mfma_f32_16x16x32_bf16 v[124:127], a[236:239], a[172:175], v[124:127]\n"
    "ds_write_b128 v248, v[96:99] offset:2048\n"
    "v_mfma_f32_16x16x32_bf16 v[124:127], a[232:235], a[140:143], v[124:127]\n"
    "ds_read_b128 a[136:139], v241 offset:53248\n"
    "ds_read_b128 a[168:171], v241 offset:54272\n"
    "v_mfma_f32_16x16x32_bf16 v[156:159], a[232:235], a[144:147], v[156:159]\n"
    "buffer_load_dwordx4 v[200:203], v240, s[20:23], s91 offen offset:1024 sc1\n"
    "v_mfma_f32_16x16x32_bf16 v[156:159], a[236:239], a[176:179], v[156:159]\n"
    "ds_read_b128 a[172:175], v241 offset:56320\n"
    "ds_read_b128 a[140:143], v241 offset:55296\n"
    "v_mfma_f32_16x16x32_bf16 v[188:191], a[236:239], a[180:183], v[188:191]\n"
    "ds_read_b128 a[144:147], v241 offset:57344\n"
    "v_accvgpr_read_b32 v245, a44\n"
    "v_mfma_f32_16x16x32_bf16 v[188:191], a[232:235], a[148:151], v[188:191]\n"
    "ds_read_b128 a[176:179], v241 offset:58368\n"
    "v_accvgpr_read_b32 v247, a45\n"
    "v_mfma_f32_16x16x32_bf16 v[220:223], a[232:235], a[152:155], v[220:223]\n"
    "ds_read_b128 a[180:183], v241 offset:60416\n"
    "v_lshlrev_b32 v244, 16, v245\n"
    "v_mfma_f32_16x16x32_bf16 v[220:223], a[236:239], a[184:187], v[220:223]\n"
    "ds_read_b128 a[148:151], v241 offset:59392\n"
    "v_lshlrev_b32 v246, 16, v247\n"
    "v_mfma_f32_16x16x32_bf16 a[252:255], a[236:239], a[188:191], a[252:255]\n"
    "ds_read_b128 a[152:155], v241 offset:61440\n"
    "v_and_b32 v245, s58, v245\n"
    "v_mfma_f32_16x16x32_bf16 a[252:255], a[232:235], a[156:159], a[252:255]\n"
    "s_waitcnt vmcnt(17) lgkmcnt(12)\n"
    "ds_read_b128 a[184:187], v241 offset:62464\n"
    "v_and_b32 v247, s58, v247\n"
    "v_mfma_f32_16x16x32_bf16 v[16:19], a[192:195], a[128:131], v[16:19]\n"
    "buffer_load_dwordx4 a[232:235], v240, s[20:23], s53 offen offset:2048 sc1\n"
    "v_mfma_f32_16x16x32_bf16 v[16:19], a[196:199], a[160:163], v[16:19]\n"
    "s_waitcnt lgkmcnt(11)\n"
    "v_mul_f32 v244, v244, v104\n"
    "v_mul_f32 v246, v246, v106\n"
    "v_mfma_f32_16x16x32_bf16 v[48:51], a[196:199], a[164:167], v[48:51]\n"
    "buffer_load_dwordx4 a[236:239], v240, s[20:23], s53 offen offset:3072 sc1\n"
    "v_mfma_f32_16x16x32_bf16 v[48:51], a[192:195], a[132:135], v[48:51]\n"
    "s_waitcnt lgkmcnt(8)\n"
    "ds_read_b128 a[188:191], v241 offset:64512\n"
    "ds_read_b128 a[156:159], v241 offset:63488\n"
    "v_mfma_f32_16x16x32_bf16 v[80:83], a[192:195], a[136:139], v[80:83]\n"
    "s_add_u32 s63, s70, 147456\n"
    "v_mul_f32 v245, v245, v105\n"
    "v_mfma_f32_16x16x32_bf16 v[80:83], a[196:199], a[168:171], v[80:83]\n"
    "s_waitcnt lgkmcnt(8)\n"
    "v_mfma_f32_16x16x32_bf16 v[112:115], a[196:199], a[172:175], v[112:115]\n"
    "buffer_store_dwordx4 v[140:143], v243, s[40:43], s63 offen nt\n"
    "v_mul_f32 v247, v247, v107\n"
    "v_cvt_pk_bf16_f32 v104, v244, v245\n"
    "v_cvt_pk_bf16_f32 v105, v246, v247\n"
    "v_mfma_f32_16x16x32_bf16 v[112:115], a[192:195], a[140:143], v[112:115]\n"
    "s_waitcnt lgkmcnt(6)\n"
    "v_accvgpr_read_b32 v245, a46\n"
    "v_accvgpr_read_b32 v247, a47\n"
    "v_mfma_f32_16x16x32_bf16 v[144:147], a[192:195], a[144:147], v[144:147]\n"
    "buffer_load_dwordx4 v[196:199], v240, s[20:23], s91 offen offset:2048 sc1\n"
    "v_mfma_f32_16x16x32_bf16 v[144:147], a[196:199], a[176:179], v[144:147]\n"
    "s_waitcnt lgkmcnt(4)\n"
    "v_lshlrev_b32 v244, 16, v245\n"
    "v_lshlrev_b32 v246, 16, v247\n"
    "v_mfma_f32_16x16x32_bf16 v[176:179], a[196:199], a[180:183], v[176:179]\n"
    "v_and_b32 v245, s58, v245\n"
    "v_and_b32 v247, s58, v247\n"
    "v_mfma_f32_16x16x32_bf16 v[176:179], a[192:195], a[148:151], v[176:179]\n"
    "s_waitcnt lgkmcnt(2)\n"
    "v_mul_f32 v244, v244, v108\n"
    "v_mul_f32 v246, v246, v110\n"
    "v_mfma_f32_16x16x32_bf16 v[208:211], a[192:195], a[152:155], v[208:211]\n"
    "v_mul_f32 v245, v245, v109\n"
    "v_mul_f32 v247, v247, v111\n"
    "v_mfma_f32_16x16x32_bf16 v[208:211], a[196:199], a[184:187], v[208:211]\n"
    "s_waitcnt lgkmcnt(0)\n"
    "v_cvt_pk_bf16_f32 v106, v244, v245\n"
    "v_cvt_pk_bf16_f32 v107, v246, v247\n"
    "v_mfma_f32_16x16x32_bf16 a[240:243], a[196:199], a[188:191], a[240:243]\n"
    "ds_write_b128 v249, v[104:107] offset:2048\n"
    "v_mfma_f32_16x16x32_bf16 a[240:243], a[192:195], a[156:159], a[240:243]\n"
    "s_waitcnt vmcnt(17)\n"
    "ds_read_b128 v[100:103], v242 offset:2048\n"
    "ds_read_b128 v[108:111], v242 offset:3072\n"
    "v_mfma_f32_16x16x32_bf16 a[244:247], a[200:203], a[156:159], a[244:247]\n"
    "s_addk_i32 s53, 4096\n"
    "v_accvgpr_read_b32 v245, a8\n"
    "v_accvgpr_read_b32 v247, a9\n"
    "v_mfma_f32_16x16x32_bf16 a[244:247], a[204:207], a[188:191], a[244:247]\n"
    "buffer_load_dwordx4 a[192:195], v240, s[20:23], s53 offen offset:0 sc1\n"
    "v_mfma_f32_16x16x32_bf16 v[212:215], a[204:207], a[184:187], v[212:215]\n"
    "buffer_load_dwordx4 a[196:199], v240, s[20:23], s53 offen offset:1024 sc1\n"
    "v_mfma_f32_16x16x32_bf16 v[212:215], a[200:203], a[152:155], v[212:215]\n"
    "v_lshlrev_b32 v244, 16, v245\n"
    "v_lshlrev_b32 v246, 16, v247\n"
    "v_mfma_f32_16x16x32_bf16 v[180:183], a[200:203], a[148:151], v[180:183]\n"
    "v_and_b32 v245, s58, v245\n"
    "v_and_b32 v247, s58, v247\n"
    "v_mfma_f32_16x16x32_bf16 v[180:183], a[204:207], a[180:183], v[180:183]\n"
    "s_add_u32 s63, s70, 98304\n"
    "s_waitcnt lgkmcnt(1)\n"
    "v_mul_f32 v244, v244, v64\n"
    "v_mfma_f32_16x16x32_bf16 v[148:151], a[204:207], a[176:179], v[148:151]\n"
    "buffer_store_dwordx4 v[100:103], v243, s[40:43], s63 offen nt\n"
    "v_mul_f32 v246, v246, v66\n"
    "v_mul_f32 v245, v245, v65\n"
    "v_mul_f32 v247, v247, v67\n"
    "v_mfma_f32_16x16x32_bf16 v[148:151], a[200:203], a[144:147], v[148:151]\n"
    "v_cvt_pk_bf16_f32 v64, v244, v245\n"
    "v_cvt_pk_bf16_f32 v65, v246, v247\n"
    "v_mfma_f32_16x16x32_bf16 v[116:119], a[200:203], a[140:143], v[116:119]\n"
    "buffer_load_dwordx4 a[20:23], v240, s[20:23], s91 offen offset:3072 sc1\n"
    "v_mfma_f32_16x16x32_bf16 v[116:119], a[204:207], a[172:175], v[116:119]\n"
    "s_addk_i32 s91, 4096\n"
    "v_accvgpr_read_b32 v245, a10\n"
    "v_accvgpr_read_b32 v247, a11\n"
    "v_mfma_f32_16x16x32_bf16 v[84:87], a[204:207], a[168:171], v[84:87]\n"
    "v_lshlrev_b32 v244, 16, v245\n"
    "v_lshlrev_b32 v246, 16, v247\n"
    "v_mfma_f32_16x16x32_bf16 v[84:87], a[200:203], a[136:139], v[84:87]\n"
    "v_and_b32 v245, s58, v245\n"
    "v_and_b32 v247, s58, v247\n"
    "v_mfma_f32_16x16x32_bf16 v[52:55], a[200:203], a[132:135], v[52:55]\n"
    "v_mul_f32 v244, v244, v68\n"
    "v_mul_f32 v246, v246, v70\n"
    "v_mfma_f32_16x16x32_bf16 v[52:55], a[204:207], a[164:167], v[52:55]\n"
    "v_mul_f32 v245, v245, v69\n"
    "v_mul_f32 v247, v247, v71\n"
    "v_mfma_f32_16x16x32_bf16 v[20:23], a[204:207], a[160:163], v[20:23]\n"
    "v_cvt_pk_bf16_f32 v66, v244, v245\n"
    "v_cvt_pk_bf16_f32 v67, v246, v247\n"
    "v_mfma_f32_16x16x32_bf16 v[20:23], a[200:203], a[128:131], v[20:23]\n"
    "s_waitcnt vmcnt(17)\n"
    "v_mfma_f32_16x16x32_bf16 v[24:27], a[208:211], a[128:131], v[24:27]\n"
    "ds_write_b128 v248, v[64:67] offset:0\n"
    "v_mfma_f32_16x16x32_bf16 v[24:27], a[212:215], a[160:163], v[24:27]\n"
    "buffer_load_dwordx4 a[200:203], v240, s[20:23], s53 offen offset:2048 sc1\n"
    "v_mfma_f32_16x16x32_bf16 v[56:59], a[212:215], a[164:167], v[56:59]\n"
    "buffer_load_dwordx4 a[204:207], v240, s[20:23], s53 offen offset:3072 sc1\n"
    "v_mfma_f32_16x16x32_bf16 v[56:59], a[208:211], a[132:135], v[56:59]\n"
    "s_add_u32 s63, s70, 114688\n"
    "s_waitcnt lgkmcnt(1)\n"
    "v_accvgpr_read_b32 v245, a40\n"
    "v_mfma_f32_16x16x32_bf16 v[88:91], a[208:211], a[136:139], v[88:91]\n"
    "buffer_store_dwordx4 v[108:111], v243, s[40:43], s63 offen nt\n"
    "v_accvgpr_read_b32 v247, a41\n"
    "v_lshlrev_b32 v244, 16, v245\n"
    "v_lshlrev_b32 v246, 16, v247\n"
    "v_mfma_f32_16x16x32_bf16 v[88:91], a[212:215], a[168:171], v[88:91]\n"
    "v_and_b32 v245, s58, v245\n"
    "v_and_b32 v247, s58, v247\n"
    "v_mfma_f32_16x16x32_bf16 v[120:123], a[212:215], a[172:175], v[120:123]\n"
    "v_mul_f32 v244, v244, v72\n"
    "v_mul_f32 v246, v246, v74\n"
    "v_mfma_f32_16x16x32_bf16 v[120:123], a[208:211], a[140:143], v[120:123]\n"
    "v_mul_f32 v245, v245, v73\n"
    "v_mul_f32 v247, v247, v75\n"
    "v_mfma_f32_16x16x32_bf16 v[152:155], a[208:211], a[144:147], v[152:155]\n"
    "buffer_load_dwordx4 v[160:163], v240, s[20:23], s91 offen offset:0 sc1\n"
    "v_mfma_f32_16x16x32_bf16 v[152:155], a[212:215], a[176:179], v[152:155]\n"
    "v_cvt_pk_bf16_f32 v72, v244, v245\n"
    "v_cvt_pk_bf16_f32 v73, v246, v247\n"
    "v_mfma_f32_16x16x32_bf16 v[184:187], a[212:215], a[180:183], v[184:187]\n"
    "v_accvgpr_read_b32 v245, a42\n"
    "v_accvgpr_read_b32 v247, a43\n"
    "v_mfma_f32_16x16x32_bf16 v[184:187], a[208:211], a[148:151], v[184:187]\n"
    "v_lshlrev_b32 v244, 16, v245\n"
    "v_lshlrev_b32 v246, 16, v247\n"
    "v_mfma_f32_16x16x32_bf16 v[216:219], a[208:211], a[152:155], v[216:219]\n"
    "v_and_b32 v245, s58, v245\n"
    "v_and_b32 v247, s58, v247\n"
    "v_mfma_f32_16x16x32_bf16 v[216:219], a[212:215], a[184:187], v[216:219]\n"
    "v_mul_f32 v244, v244, v76\n"
    "v_mul_f32 v246, v246, v78\n"
    "v_mfma_f32_16x16x32_bf16 a[248:251], a[212:215], a[188:191], a[248:251]\n"
    "v_mul_f32 v245, v245, v77\n"
    "v_mul_f32 v247, v247, v79\n"
    "v_mfma_f32_16x16x32_bf16 a[248:251], a[208:211], a[156:159], a[248:251]\n"
    "s_waitcnt vmcnt(17)\n"
    "s_addk_i32 s53, -20480\n"
    "v_cvt_pk_bf16_f32 v74, v244, v245\n"
    "v_mfma_f32_16x16x32_bf16 a[252:255], a[216:219], a[156:159], a[252:255]\n"
    "buffer_load_dwordx4 a[208:211], v240, s[20:23], s53 offen offset:0 sc1\n"
    "v_mfma_f32_16x16x32_bf16 a[252:255], a[220:223], a[188:191], a[252:255]\n"
    "buffer_load_dwordx4 a[212:215], v240, s[20:23], s53 offen offset:1024 sc1\n"
    "v_mfma_f32_16x16x32_bf16 v[220:223], a[220:223], a[184:187], v[220:223]\n"
    "ds_read_b128 a[156:159], v241 offset:47104\n"
    "v_cvt_pk_bf16_f32 v75, v246, v247\n"
    "v_mfma_f32_16x16x32_bf16 v[220:223], a[216:219], a[152:155], v[220:223]\n"
    "ds_write_b128 v249, v[72:75] offset:0\n"
    "v_mfma_f32_16x16x32_bf16 v[188:191], a[216:219], a[148:151], v[188:191]\n"
    "ds_read_b128 a[188:191], v241 offset:48128\n"
    "ds_read_b128 v[68:71], v242 offset:0\n"
    "v_mfma_f32_16x16x32_bf16 v[188:191], a[220:223], a[180:183], v[188:191]\n"
    "ds_read_b128 v[76:79], v242 offset:1024\n"
    "ds_read_b128 a[184:187], v241 offset:46080\n"
    "v_mfma_f32_16x16x32_bf16 v[156:159], a[220:223], a[176:179], v[156:159]\n"
    "ds_read_b128 a[152:155], v241 offset:45056\n"
    "ds_read_b128 a[148:151], v241 offset:43008\n"
    "v_mfma_f32_16x16x32_bf16 v[156:159], a[216:219], a[144:147], v[156:159]\n"
    "ds_read_b128 a[180:183], v241 offset:44032\n"
    "v_accvgpr_read_b32 v245, a4\n"
    "v_mfma_f32_16x16x32_bf16 v[124:127], a[216:219], a[140:143], v[124:127]\n"
    "buffer_load_dwordx4 v[204:207], v240, s[20:23], s91 offen offset:1024 sc1\n"
    "v_mfma_f32_16x16x32_bf16 v[124:127], a[220:223], a[172:175], v[124:127]\n"
    "ds_read_b128 a[176:179], v241 offset:41984\n"
    "ds_read_b128 a[144:147], v241 offset:40960\n"
    "v_mfma_f32_16x16x32_bf16 v[92:95], a[220:223], a[168:171], v[92:95]\n"
    "ds_read_b128 a[140:143], v241 offset:38912\n"
    "v_accvgpr_read_b32 v247, a5\n"
    "v_mfma_f32_16x16x32_bf16 v[92:95], a[216:219], a[136:139], v[92:95]\n"
    "ds_read_b128 a[172:175], v241 offset:39936\n"
    "v_lshlrev_b32 v244, 16, v245\n"
    "v_mfma_f32_16x16x32_bf16 v[60:63], a[216:219], a[132:135], v[60:63]\n"
    "ds_read_b128 a[168:171], v241 offset:37888\n"
    "v_lshlrev_b32 v246, 16, v247\n"
    "v_mfma_f32_16x16x32_bf16 v[60:63], a[220:223], a[164:167], v[60:63]\n"
    "ds_read_b128 a[136:139], v241 offset:36864\n"
    "v_and_b32 v245, s58, v245\n"
    "v_mfma_f32_16x16x32_bf16 v[28:31], a[220:223], a[160:163], v[28:31]\n"
    "ds_read_b128 a[132:135], v241 offset:34816\n"
    "v_and_b32 v247, s58, v247\n"
    "v_mfma_f32_16x16x32_bf16 v[28:31], a[216:219], a[128:131], v[28:31]\n"
    "s_waitcnt vmcnt(17) lgkmcnt(13)\n"
    "ds_read_b128 a[164:167], v241 offset:35840\n"
    "v_mul_f32 v244, v244, v32\n"
    "v_mfma_f32_16x16x32_bf16 a[240:243], a[224:227], a[156:159], a[240:243]\n"
    "buffer_load_dwordx4 a[216:219], v240, s[20:23], s53 offen offset:2048 sc1\n"
    "v_mfma_f32_16x16x32_bf16 a[240:243], a[228:231], a[188:191], a[240:243]\n"
    "s_waitcnt lgkmcnt(10)\n"
    "v_mul_f32 v246, v246, v34\n"
    "v_mul_f32 v245, v245, v33\n"
    "v_mfma_f32_16x16x32_bf16 v[208:211], a[228:231], a[184:187], v[208:211]\n"
    "buffer_load_dwordx4 a[220:223], v240, s[20:23], s53 offen offset:3072 sc1\n"
    "v_mfma_f32_16x16x32_bf16 v[208:211], a[224:227], a[152:155], v[208:211]\n"
    "s_waitcnt lgkmcnt(8)\n"
    "ds_read_b128 a[160:163], v241 offset:33792\n"
    "ds_read_b128 a[128:131], v241 offset:32768\n"
    "v_mfma_f32_16x16x32_bf16 v[176:179], a[224:227], a[148:151], v[176:179]\n"
    "s_add_u32 s63, s70, 65536\n"
    "v_mul_f32 v247, v247, v35\n"
    "v_mfma_f32_16x16x32_bf16 v[176:179], a[228:231], a[180:183], v[176:179]\n"
    "s_waitcnt lgkmcnt(8)\n"
    "v_mfma_f32_16x16x32_bf16 v[144:147], a[228:231], a[176:179], v[144:147]\n"
    "buffer_store_dwordx4 v[68:71], v243, s[40:43], s63 offen nt\n"
    "v_cvt_pk_bf16_f32 v32, v244, v245\n"
    "v_cvt_pk_bf16_f32 v33, v246, v247\n"
    "v_accvgpr_read_b32 v245, a6\n"
    "v_mfma_f32_16x16x32_bf16 v[144:147], a[224:227], a[144:147], v[144:147]\n"
    "s_waitcnt lgkmcnt(6)\n"
    "v_accvgpr_read_b32 v247, a7\n"
    "v_lshlrev_b32 v244, 16, v245\n"
    "v_mfma_f32_16x16x32_bf16 v[112:115], a[224:227], a[140:143], v[112:115]\n"
    "buffer_load_dwordx4 a[52:55], v240, s[20:23], s91 offen offset:2048 sc1\n"
    "v_mfma_f32_16x16x32_bf16 v[112:115], a[228:231], a[172:175], v[112:115]\n"
    "s_waitcnt lgkmcnt(4)\n"
    "v_lshlrev_b32 v246, 16, v247\n"
    "v_and_b32 v245, s58, v245\n"
    "v_mfma_f32_16x16x32_bf16 v[80:83], a[228:231], a[168:171], v[80:83]\n"
    "v_and_b32 v247, s58, v247\n"
    "v_mul_f32 v244, v244, v36\n"
    "v_mfma_f32_16x16x32_bf16 v[80:83], a[224:227], a[136:139], v[80:83]\n"
    "s_waitcnt lgkmcnt(2)\n"
    "v_mul_f32 v246, v246, v38\n"
    "v_mul_f32 v245, v245, v37\n"
    "v_mfma_f32_16x16x32_bf16 v[48:51], a[224:227], a[132:135], v[48:51]\n"
    "v_mul_f32 v247, v247, v39\n"
    "v_cvt_pk_bf16_f32 v34, v244, v245\n"
    "v_mfma_f32_16x16x32_bf16 v[48:51], a[228:231], a[164:167], v[48:51]\n"
    "s_waitcnt lgkmcnt(0)\n"
    "v_cvt_pk_bf16_f32 v35, v246, v247\n"
    "v_mfma_f32_16x16x32_bf16 v[16:19], a[228:231], a[160:163], v[16:19]\n"
    "ds_write_b128 v248, v[32:35] offset:2048\n"
    "v_mfma_f32_16x16x32_bf16 v[16:19], a[224:227], a[128:131], v[16:19]\n"
    "s_waitcnt vmcnt(17)\n"
    "s_addk_i32 s53, 4096\n"
    "v_accvgpr_read_b32 v245, a36\n"
    "v_mfma_f32_16x16x32_bf16 v[20:23], a[232:235], a[128:131], v[20:23]\n"
    "buffer_load_dwordx4 a[224:227], v240, s[20:23], s53 offen offset:0 sc1\n"
    "v_mfma_f32_16x16x32_bf16 v[20:23], a[236:239], a[160:163], v[20:23]\n"
    "buffer_load_dwordx4 a[228:231], v240, s[20:23], s53 offen offset:1024 sc1\n"
    "v_mfma_f32_16x16x32_bf16 v[52:55], a[236:239], a[164:167], v[52:55]\n"
    "v_accvgpr_read_b32 v247, a37\n"
    "v_lshlrev_b32 v244, 16, v245\n"
    "v_mfma_f32_16x16x32_bf16 v[52:55], a[232:235], a[132:135], v[52:55]\n"
    "s_add_u32 s63, s70, 81920\n"
    "v_lshlrev_b32 v246, 16, v247\n"
    "v_mfma_f32_16x16x32_bf16 v[84:87], a[232:235], a[136:139], v[84:87]\n"
    "buffer_store_dwordx4 v[76:79], v243, s[40:43], s63 offen nt\n"
    "v_and_b32 v245, s58, v245\n"
    "v_and_b32 v247, s58, v247\n"
    "v_mul_f32 v244, v244, v40\n"
    "v_mfma_f32_16x16x32_bf16 v[84:87], a[236:239], a[168:171], v[84:87]\n"
    "v_mul_f32 v246, v246, v42\n"
    "v_mul_f32 v245, v245, v41\n"
    "v_mfma_f32_16x16x32_bf16 v[116:119], a[236:239], a[172:175], v[116:119]\n"
    "v_mul_f32 v247, v247, v43\n"
    "v_cvt_pk_bf16_f32 v40, v244, v245\n"
    "v_mfma_f32_16x16x32_bf16 v[116:119], a[232:235], a[140:143], v[116:119]\n"
    "v_cvt_pk_bf16_f32 v41, v246, v247\n"
    "v_accvgpr_read_b32 v245, a38\n"
    "v_mfma_f32_16x16x32_bf16 v[148:151], a[232:235], a[144:147], v[148:151]\n"
    "buffer_load_dwordx4 v[168:171], v240, s[20:23], s91 offen offset:3072 sc1\n"
    "v_mfma_f32_16x16x32_bf16 v[148:151], a[236:239], a[176:179], v[148:151]\n"
    "s_addk_i32 s91, 4096\n"
    "v_accvgpr_read_b32 v247, a39\n"
    "v_lshlrev_b32 v244, 16, v245\n"
    "v_mfma_f32_16x16x32_bf16 v[180:183], a[236:239], a[180:183], v[180:183]\n"
    "v_lshlrev_b32 v246, 16, v247\n"
    "v_and_b32 v245, s58, v245\n"
    "v_mfma_f32_16x16x32_bf16 v[180:183], a[232:235], a[148:151], v[180:183]\n"
    "v_and_b32 v247, s58, v247\n"
    "v_mul_f32 v244, v244, v44\n"
    "v_mfma_f32_16x16x32_bf16 v[212:215], a[232:235], a[152:155], v[212:215]\n"
    "v_mul_f32 v246, v246, v46\n"
    "v_mul_f32 v245, v245, v45\n"
    "v_mfma_f32_16x16x32_bf16 v[212:215], a[236:239], a[184:187], v[212:215]\n"
    "v_mul_f32 v247, v247, v47\n"
    "v_cvt_pk_bf16_f32 v42, v244, v245\n"
    "v_mfma_f32_16x16x32_bf16 a[244:247], a[236:239], a[188:191], a[244:247]\n"
    "v_cvt_pk_bf16_f32 v43, v246, v247\n"
    "v_mfma_f32_16x16x32_bf16 a[244:247], a[232:235], a[156:159], a[244:247]\n"
    "s_waitcnt vmcnt(17)\n"
    "v_mfma_f32_16x16x32_bf16 a[248:251], a[192:195], a[156:159], a[248:251]\n"
    "ds_write_b128 v249, v[40:43] offset:2048\n"
    "v_mfma_f32_16x16x32_bf16 a[248:251], a[196:199], a[188:191], a[248:251]\n"
    "buffer_load_dwordx4 a[232:235], v240, s[20:23], s53 offen offset:2048 sc1\n"
    "v_mfma_f32_16x16x32_bf16 v[216:219], a[196:199], a[184:187], v[216:219]\n"
    "buffer_load_dwordx4 a[236:239], v240, s[20:23], s53 offen offset:3072 sc1\n"
    "v_mfma_f32_16x16x32_bf16 v[216:219], a[192:195], a[152:155], v[216:219]\n"
    "buffer_load_dwordx4 v[164:167], v240, s[20:23], s91 offen offset:0 sc1\n"
    "ds_read_b128 v[36:39], v242 offset:2048\n"
    "ds_read_b128 v[44:47], v242 offset:3072\n"
    "v_mfma_f32_16x16x32_bf16 v[184:187], a[192:195], a[148:151], v[184:187]\n"
    "buffer_load_dwordx4 a[16:19], v240, s[20:23], s91 offen offset:1024 sc1\n"
    "v_mfma_f32_16x16x32_bf16 v[184:187], a[196:199], a[180:183], v[184:187]\n"
    "v_accvgpr_read_b32 v245, a0\n"
    "v_accvgpr_read_b32 v247, a1\n"
    "v_mfma_f32_16x16x32_bf16 v[152:155], a[196:199], a[176:179], v[152:155]\n"
    "v_lshlrev_b32 v244, 16, v245\n"
    "v_lshlrev_b32 v246, 16, v247\n"
    "v_mfma_f32_16x16x32_bf16 v[152:155], a[192:195], a[144:147], v[152:155]\n"
    "s_add_u32 s63, s70, 32768\n"
    "s_waitcnt lgkmcnt(1)\n"
    "v_and_b32 v245, s58, v245\n"
    "v_mfma_f32_16x16x32_bf16 v[120:123], a[192:195], a[140:143], v[120:123]\n"
    "buffer_store_dwordx4 v[36:39], v243, s[40:43], s63 offen nt\n"
    "buffer_load_dwordx4 v[128:131], v240, s[20:23], s91 offen offset:2048 sc1\n"
    "v_mfma_f32_16x16x32_bf16 v[120:123], a[196:199], a[172:175], v[120:123]\n"
    "v_and_b32 v247, s58, v247\n"
    "v_mul_f32 v244, v244, v0\n"
    "v_mfma_f32_16x16x32_bf16 v[88:91], a[196:199], a[168:171], v[88:91]\n"
    "v_mul_f32 v246, v246, v2\n"
    "v_mul_f32 v245, v245, v1\n"
    "v_mfma_f32_16x16x32_bf16 v[88:91], a[192:195], a[136:139], v[88:91]\n"
    "v_mul_f32 v247, v247, v3\n"
    "v_cvt_pk_bf16_f32 v0, v244, v245\n"
    "v_mfma_f32_16x16x32_bf16 v[56:59], a[192:195], a[132:135], v[56:59]\n"
    "buffer_load_dwordx4 v[172:175], v240, s[20:23], s91 offen offset:3072 sc1\n"
    "v_mfma_f32_16x16x32_bf16 v[56:59], a[196:199], a[164:167], v[56:59]\n"
    "s_addk_i32 s91, 4096\n"
    "v_cvt_pk_bf16_f32 v1, v246, v247\n"
    "v_accvgpr_read_b32 v245, a2\n"
    "v_mfma_f32_16x16x32_bf16 v[24:27], a[196:199], a[160:163], v[24:27]\n"
    "v_accvgpr_read_b32 v247, a3\n"
    "v_lshlrev_b32 v244, 16, v245\n"
    "v_mfma_f32_16x16x32_bf16 v[24:27], a[192:195], a[128:131], v[24:27]\n"
    "s_waitcnt vmcnt(20)\n"
    "v_lshlrev_b32 v246, 16, v247\n"
    "v_and_b32 v245, s58, v245\n"
    "v_mfma_f32_16x16x32_bf16 v[28:31], a[200:203], a[128:131], v[28:31]\n"
    "buffer_load_dwordx4 a[48:51], v240, s[20:23], s91 offen offset:0 sc1\n"
    "v_mfma_f32_16x16x32_bf16 v[28:31], a[204:207], a[160:163], v[28:31]\n"
    "s_add_u32 s63, s70, 49152\n"
    "s_waitcnt lgkmcnt(0)\n"
    "v_and_b32 v247, s58, v247\n"
    "v_mfma_f32_16x16x32_bf16 v[60:63], a[204:207], a[164:167], v[60:63]\n"
    "buffer_store_dwordx4 v[44:47], v243, s[40:43], s63 offen nt\n"
    "ds_read_b128 a[128:131], v241 offset:16384\n"
    "v_mul_f32 v244, v244, v4\n"
    "v_mul_f32 v246, v246, v6\n"
    "v_mfma_f32_16x16x32_bf16 v[60:63], a[200:203], a[132:135], v[60:63]\n"
    "ds_read_b128 a[160:163], v241 offset:17408\n"
    "v_mul_f32 v245, v245, v5\n"
    "v_mfma_f32_16x16x32_bf16 v[92:95], a[200:203], a[136:139], v[92:95]\n"
    "buffer_load_dwordx4 v[136:139], v240, s[20:23], s91 offen offset:1024 sc1\n"
    "v_mfma_f32_16x16x32_bf16 v[92:95], a[204:207], a[168:171], v[92:95]\n"
    "ds_read_b128 a[164:167], v241 offset:19456\n"
    "ds_read_b128 a[132:135], v241 offset:18432\n"
    "v_mfma_f32_16x16x32_bf16 v[124:127], a[204:207], a[172:175], v[124:127]\n"
    "ds_read_b128 a[136:139], v241 offset:20480\n"
    "v_mul_f32 v247, v247, v7\n"
    "v_mfma_f32_16x16x32_bf16 v[124:127], a[200:203], a[140:143], v[124:127]\n"
    "ds_read_b128 a[168:171], v241 offset:21504\n"
    "v_cvt_pk_bf16_f32 v2, v244, v245\n"
    "v_mfma_f32_16x16x32_bf16 v[156:159], a[200:203], a[144:147], v[156:159]\n"
    "buffer_load_dwordx4 v[132:135], v240, s[20:23], s91 offen offset:2048 sc1\n"
    "v_mfma_f32_16x16x32_bf16 v[156:159], a[204:207], a[176:179], v[156:159]\n"
    "ds_read_b128 a[172:175], v241 offset:23552\n"
    "ds_read_b128 a[140:143], v241 offset:22528\n"
    "v_mfma_f32_16x16x32_bf16 v[188:191], a[204:207], a[180:183], v[188:191]\n"
    "ds_read_b128 a[144:147], v241 offset:24576\n"
    "v_cvt_pk_bf16_f32 v3, v246, v247\n"
    "v_mfma_f32_16x16x32_bf16 v[188:191], a[200:203], a[148:151], v[188:191]\n"
    "ds_write_b128 v248, v[0:3] offset:0\n"
    "v_mfma_f32_16x16x32_bf16 v[220:223], a[200:203], a[152:155], v[220:223]\n"
    "ds_read_b128 a[176:179], v241 offset:25600\n"
    "v_accvgpr_read_b32 v245, a32\n"
    "v_mfma_f32_16x16x32_bf16 v[220:223], a[204:207], a[184:187], v[220:223]\n"
    "buffer_load_dwordx4 a[12:15], v240, s[20:23], s91 offen offset:3072 sc1\n"
    "v_mfma_f32_16x16x32_bf16 a[252:255], a[204:207], a[188:191], a[252:255]\n"
    "s_addk_i32 s91, 4096\n"
    "ds_read_b128 a[180:183], v241 offset:27648\n"
    "ds_read_b128 a[148:151], v241 offset:26624\n"
    "v_mfma_f32_16x16x32_bf16 a[252:255], a[200:203], a[156:159], a[252:255]\n"
    "s_waitcnt vmcnt(21) lgkmcnt(11)\n"
    "ds_read_b128 a[152:155], v241 offset:28672\n"
    "ds_read_b128 a[184:187], v241 offset:29696\n"
    "v_mfma_f32_16x16x32_bf16 v[16:19], a[208:211], a[128:131], v[16:19]\n"
    "buffer_load_dwordx4 v[96:99], v240, s[20:23], s91 offen offset:0 sc1\n"
    "v_mfma_f32_16x16x32_bf16 v[16:19], a[212:215], a[160:163], v[16:19]\n"
    "s_waitcnt lgkmcnt(11)\n"
    "ds_read_b128 a[188:191], v241 offset:31744\n"
    "ds_read_b128 a[156:159], v241 offset:30720\n"
    "v_mfma_f32_16x16x32_bf16 v[48:51], a[212:215], a[164:167], v[48:51]\n"
    "v_accvgpr_read_b32 v247, a33\n"
    "v_lshlrev_b32 v244, 16, v245\n"
    "v_mfma_f32_16x16x32_bf16 v[48:51], a[208:211], a[132:135], v[48:51]\n"
    "s_waitcnt lgkmcnt(11)\n"
    "v_lshlrev_b32 v246, 16, v247\n"
    "v_and_b32 v245, s58, v245\n"
    "v_mfma_f32_16x16x32_bf16 v[80:83], a[208:211], a[136:139], v[80:83]\n"
    "buffer_load_dwordx4 v[140:143], v240, s[20:23], s91 offen offset:1024 sc1\n"
    "v_mfma_f32_16x16x32_bf16 v[80:83], a[212:215], a[168:171], v[80:83]\n"
    "s_waitcnt lgkmcnt(9)\n"
    "v_and_b32 v247, s58, v247\n"
    "v_mul_f32 v244, v244, v8\n"
    "v_mfma_f32_16x16x32_bf16 v[112:115], a[212:215], a[172:175], v[112:115]\n"
    "v_mul_f32 v246, v246, v10\n"
    "v_mul_f32 v245, v245, v9\n"
    "v_mfma_f32_16x16x32_bf16 v[112:115], a[208:211], a[140:143], v[112:115]\n"
    "s_waitcnt lgkmcnt(6)\n"
    "v_mul_f32 v247, v247, v11\n"
    "v_cvt_pk_bf16_f32 v8, v244, v245\n"
    "v_mfma_f32_16x16x32_bf16 v[144:147], a[208:211], a[144:147], v[144:147]\n"
    "buffer_load_dwordx4 a[44:47], v240, s[20:23], s91 offen offset:2048 sc1\n"
    "v_mfma_f32_16x16x32_bf16 v[144:147], a[212:215], a[176:179], v[144:147]\n"
    "s_waitcnt lgkmcnt(4)\n"
    "v_cvt_pk_bf16_f32 v9, v246, v247\n"
    "v_accvgpr_read_b32 v245, a34\n"
    "v_mfma_f32_16x16x32_bf16 v[176:179], a[212:215], a[180:183], v[176:179]\n"
    "v_accvgpr_read_b32 v247, a35\n"
    "v_lshlrev_b32 v244, 16, v245\n"
    "v_mfma_f32_16x16x32_bf16 v[176:179], a[208:211], a[148:151], v[176:179]\n"
    "s_waitcnt lgkmcnt(2)\n"
    "v_lshlrev_b32 v246, 16, v247\n"
    "v_and_b32 v245, s58, v245\n"
    "v_mfma_f32_16x16x32_bf16 v[208:211], a[208:211], a[152:155], v[208:211]\n"
    "buffer_load_dwordx4 v[104:107], v240, s[20:23], s91 offen offset:3072 sc1\n"
    "v_mfma_f32_16x16x32_bf16 v[208:211], a[212:215], a[184:187], v[208:211]\n"
    "s_waitcnt lgkmcnt(0)\n"
    "s_addk_i32 s91, 4096\n"
    "v_and_b32 v247, s58, v247\n"
    "v_mfma_f32_16x16x32_bf16 a[240:243], a[212:215], a[188:191], a[240:243]\n"
    "v_mul_f32 v244, v244, v12\n"
    "v_mul_f32 v246, v246, v14\n"
    "v_mfma_f32_16x16x32_bf16 a[240:243], a[208:211], a[156:159], a[240:243]\n"
    "s_waitcnt vmcnt(22)\n"
    "v_mul_f32 v245, v245, v13\n"
    "v_mul_f32 v247, v247, v15\n"
    "v_mfma_f32_16x16x32_bf16 a[244:247], a[216:219], a[156:159], a[244:247]\n"
    "buffer_load_dwordx4 v[100:103], v240, s[20:23], s91 offen offset:0 sc1\n"
    "v_mfma_f32_16x16x32_bf16 a[244:247], a[220:223], a[188:191], a[244:247]\n"
    "v_cvt_pk_bf16_f32 v10, v244, v245\n"
    "v_cvt_pk_bf16_f32 v11, v246, v247\n"
    "v_mfma_f32_16x16x32_bf16 v[212:215], a[220:223], a[184:187], v[212:215]\n"
    "ds_write_b128 v249, v[8:11] offset:0\n"
    "v_mfma_f32_16x16x32_bf16 v[212:215], a[216:219], a[152:155], v[212:215]\n"
    "ds_read_b128 v[4:7], v242 offset:0\n"
    "ds_read_b128 v[12:15], v242 offset:1024\n"
    "v_mfma_f32_16x16x32_bf16 v[180:183], a[216:219], a[148:151], v[180:183]\n"
    "buffer_load_dwordx4 a[8:11], v240, s[20:23], s91 offen offset:1024 sc1\n"
    "v_mfma_f32_16x16x32_bf16 v[180:183], a[220:223], a[180:183], v[180:183]\n"
    "v_mfma_f32_16x16x32_bf16 v[148:151], a[220:223], a[176:179], v[148:151]\n"
    "v_mfma_f32_16x16x32_bf16 v[148:151], a[216:219], a[144:147], v[148:151]\n"
    "v_mfma_f32_16x16x32_bf16 v[116:119], a[216:219], a[140:143], v[116:119]\n"
    "buffer_load_dwordx4 v[64:67], v240, s[20:23], s91 offen offset:2048 sc1\n"
    "v_mfma_f32_16x16x32_bf16 v[116:119], a[220:223], a[172:175], v[116:119]\n"
    "v_mfma_f32_16x16x32_bf16 v[84:87], a[220:223], a[168:171], v[84:87]\n"
    "v_mfma_f32_16x16x32_bf16 v[84:87], a[216:219], a[136:139], v[84:87]\n"
    "v_mfma_f32_16x16x32_bf16 v[52:55], a[216:219], a[132:135], v[52:55]\n"
    "buffer_load_dwordx4 v[108:111], v240, s[20:23], s91 offen offset:3072 sc1\n"
    "v_mfma_f32_16x16x32_bf16 v[52:55], a[220:223], a[164:167], v[52:55]\n"
    "s_addk_i32 s91, 4096\n"
    "s_add_u32 s63, s70, 0\n"
    "s_waitcnt lgkmcnt(1)\n"
    "v_mfma_f32_16x16x32_bf16 v[20:23], a[220:223], a[160:163], v[20:23]\n"
    "buffer_store_dwordx4 v[4:7], v243, s[40:43], s63 offen nt\n"
    "v_mfma_f32_16x16x32_bf16 v[20:23], a[216:219], a[128:131], v[20:23]\n"
    "s_waitcnt vmcnt(23)\n"
    "v_mfma_f32_16x16x32_bf16 v[24:27], a[224:227], a[128:131], v[24:27]\n"
    "v_mfma_f32_16x16x32_bf16 v[24:27], a[228:231], a[160:163], v[24:27]\n"
    "s_add_u32 s63, s70, 16384\n"
    "s_waitcnt lgkmcnt(0)\n"
    "v_mfma_f32_16x16x32_bf16 v[56:59], a[228:231], a[164:167], v[56:59]\n"
    "buffer_store_dwordx4 v[12:15], v243, s[40:43], s63 offen nt\n"
    "v_mfma_f32_16x16x32_bf16 v[56:59], a[224:227], a[132:135], v[56:59]\n"
    "v_mfma_f32_16x16x32_bf16 v[88:91], a[224:227], a[136:139], v[88:91]\n"
    "v_mfma_f32_16x16x32_bf16 v[88:91], a[228:231], a[168:171], v[88:91]\n"
    "v_mfma_f32_16x16x32_bf16 v[120:123], a[228:231], a[172:175], v[120:123]\n"
    "v_mfma_f32_16x16x32_bf16 v[120:123], a[224:227], a[140:143], v[120:123]\n"
    "v_mfma_f32_16x16x32_bf16 v[152:155], a[224:227], a[144:147], v[152:155]\n"
    "v_mfma_f32_16x16x32_bf16 v[152:155], a[228:231], a[176:179], v[152:155]\n"
    "v_mfma_f32_16x16x32_bf16 v[184:187], a[228:231], a[180:183], v[184:187]\n"
    "v_mfma_f32_16x16x32_bf16 v[184:187], a[224:227], a[148:151], v[184:187]\n"
    "v_mfma_f32_16x16x32_bf16 v[216:219], a[224:227], a[152:155], v[216:219]\n"
    "v_mfma_f32_16x16x32_bf16 v[216:219], a[228:231], a[184:187], v[216:219]\n"
    "v_mfma_f32_16x16x32_bf16 a[248:251], a[228:231], a[188:191], a[248:251]\n"
    "v_mfma_f32_16x16x32_bf16 a[248:251], a[224:227], a[156:159], a[248:251]\n"
    "s_waitcnt vmcnt(20)\n"
    "v_mfma_f32_16x16x32_bf16 a[252:255], a[232:235], a[156:159], a[252:255]\n"
    "v_mfma_f32_16x16x32_bf16 a[252:255], a[236:239], a[188:191], a[252:255]\n"
    "v_mfma_f32_16x16x32_bf16 v[220:223], a[236:239], a[184:187], v[220:223]\n"
    "v_add_u32 v241, s57, v240\n"
    "ds_read_b128 a[156:159], v241 offset:0\n"
    "v_mfma_f32_16x16x32_bf16 v[220:223], a[232:235], a[152:155], v[220:223]\n"
    "ds_read_b128 a[188:191], v241 offset:1024\n"
    "v_mfma_f32_16x16x32_bf16 v[188:191], a[232:235], a[148:151], v[188:191]\n"
    "ds_read_b128 a[184:187], v241 offset:16384\n"
    "v_mfma_f32_16x16x32_bf16 v[188:191], a[236:239], a[180:183], v[188:191]\n"
    "ds_read_b128 a[152:155], v241 offset:17408\n"
    "v_mfma_f32_16x16x32_bf16 v[156:159], a[236:239], a[176:179], v[156:159]\n"
    "ds_read_b128 a[148:151], v241 offset:32768\n"
    "v_mfma_f32_16x16x32_bf16 v[156:159], a[232:235], a[144:147], v[156:159]\n"
    "ds_read_b128 a[180:183], v241 offset:33792\n"
    "v_mfma_f32_16x16x32_bf16 v[124:127], a[232:235], a[140:143], v[124:127]\n"
    "ds_read_b128 a[176:179], v241 offset:49152\n"
    "v_mfma_f32_16x16x32_bf16 v[124:127], a[236:239], a[172:175], v[124:127]\n"
    "ds_read_b128 a[144:147], v241 offset:50176\n"
    "v_mfma_f32_16x16x32_bf16 v[92:95], a[236:239], a[168:171], v[92:95]\n"
    "v_mfma_f32_16x16x32_bf16 v[92:95], a[232:235], a[136:139], v[92:95]\n"
    "v_mfma_f32_16x16x32_bf16 v[60:63], a[232:235], a[132:135], v[60:63]\n"
    "v_mfma_f32_16x16x32_bf16 v[60:63], a[236:239], a[164:167], v[60:63]\n"
    "v_mfma_f32_16x16x32_bf16 v[28:31], a[236:239], a[160:163], v[28:31]\n"
    "v_mfma_f32_16x16x32_bf16 v[28:31], a[232:235], a[128:131], v[28:31]\n"
    "s_waitcnt lgkmcnt(7)\n"
    "v_mfma_f32_16x16x32_bf16 v[16:19], a[28:31], a[156:159], v[16:19]\n"
    "s_waitcnt lgkmcnt(6)\n"
    "v_mfma_f32_16x16x32_bf16 v[16:19], v[224:227], a[188:191], v[16:19]\n"
    "s_waitcnt lgkmcnt(5)\n"
    "ds_read_b128 a[140:143], v241 offset:2048\n"
    "v_mfma_f32_16x16x32_bf16 v[16:19], a[56:59], a[184:187], v[16:19]\n"
    "s_waitcnt lgkmcnt(5)\n"
    "v_mfma_f32_16x16x32_bf16 v[16:19], v[200:203], a[152:155], v[16:19]\n"
    "s_waitcnt vmcnt(19) lgkmcnt(4)\n"
    "v_mfma_f32_16x16x32_bf16 v[16:19], v[164:167], a[148:151], v[16:19]\n"
    "s_waitcnt vmcnt(18) lgkmcnt(3)\n"
    "ds_read_b128 a[172:175], v241 offset:3072\n"
    "v_mfma_f32_16x16x32_bf16 v[16:19], a[16:19], a[180:183], v[16:19]\n"
    "s_waitcnt vmcnt(9) lgkmcnt(3)\n"
    "v_mfma_f32_16x16x32_bf16 v[16:19], v[96:99], a[176:179], v[16:19]\n"
    "s_waitcnt vmcnt(8) lgkmcnt(2)\n"
    "v_mfma_f32_16x16x32_bf16 v[16:19], v[140:143], a[144:147], v[16:19]\n"
    "ds_read_b128 a[168:171], v241 offset:18432\n"
    "v_mfma_f32_16x16x32_bf16 v[20:23], a[24:27], a[156:159], v[20:23]\n"
    "v_mfma_f32_16x16x32_bf16 v[20:23], v[192:195], a[188:191], v[20:23]\n"
    "v_accvgpr_read_b32 v245, a64\n"
    "v_accvgpr_read_b32 v247, a65\n"
    "v_mfma_f32_16x16x32_bf16 v[20:23], v[196:199], a[184:187], v[20:23]\n"
    "ds_read_b128 a[136:139], v241 offset:19456\n"
    "v_lshlrev_b32 v244, 16, v245\n"
    "v_mfma_f32_16x16x32_bf16 v[20:23], a[20:23], a[152:155], v[20:23]\n"
    "v_lshlrev_b32 v246, 16, v247\n"
    "v_and_b32 v245, s58, v245\n"
    "v_mfma_f32_16x16x32_bf16 v[20:23], v[128:131], a[148:151], v[20:23]\n"
    "v_and_b32 v247, s58, v247\n"
    "v_mul_f32 v244, v244, v16\n"
    "v_mfma_f32_16x16x32_bf16 v[20:23], v[172:175], a[180:183], v[20:23]\n"
    "s_waitcnt vmcnt(7)\n"
    "ds_read_b128 a[132:135], v241 offset:34816\n"
    "v_mul_f32 v246, v246, v18\n"
    "v_mfma_f32_16x16x32_bf16 v[20:23], a[44:47], a[176:179], v[20:23]\n"
    "s_waitcnt vmcnt(6)\n"
    "v_mul_f32 v245, v245, v17\n"
    "v_mul_f32 v247, v247, v19\n"
    "v_mfma_f32_16x16x32_bf16 v[20:23], v[104:107], a[144:147], v[20:23]\n"
    "v_cvt_pk_bf16_f32 v16, v244, v245\n"
    "v_cvt_pk_bf16_f32 v17, v246, v247\n"
    "v_mfma_f32_16x16x32_bf16 v[24:27], a[60:63], a[156:159], v[24:27]\n"
    "ds_read_b128 a[164:167], v241 offset:35840\n"
    "v_mfma_f32_16x16x32_bf16 v[24:27], v[232:235], a[188:191], v[24:27]\n"
    "v_accvgpr_read_b32 v245, a66\n"
    "v_accvgpr_read_b32 v247, a67\n"
    "v_mfma_f32_16x16x32_bf16 v[24:27], v[160:163], a[184:187], v[24:27]\n"
    "v_lshlrev_b32 v244, 16, v245\n"
    "v_lshlrev_b32 v246, 16, v247\n"
    "v_mfma_f32_16x16x32_bf16 v[24:27], v[204:207], a[152:155], v[24:27]\n"
    "ds_read_b128 a[160:163], v241 offset:51200\n"
    "v_and_b32 v245, s58, v245\n"
    "v_mfma_f32_16x16x32_bf16 v[24:27], a[48:51], a[148:151], v[24:27]\n"
    "v_and_b32 v247, s58, v247\n"
    "v_mul_f32 v244, v244, v20\n"
    "v_mfma_f32_16x16x32_bf16 v[24:27], v[136:139], a[180:183], v[24:27]\n"
    "s_waitcnt vmcnt(5)\n"
    "v_mul_f32 v246, v246, v22\n"
    "v_mul_f32 v245, v245, v21\n"
    "v_mfma_f32_16x16x32_bf16 v[24:27], v[100:103], a[176:179], v[24:27]\n"
    "s_waitcnt vmcnt(4)\n"
    "ds_read_b128 a[128:131], v241 offset:52224\n"
    "v_mul_f32 v247, v247, v23\n"
    "v_mfma_f32_16x16x32_bf16 v[24:27], a[8:11], a[144:147], v[24:27]\n"
    "v_cvt_pk_bf16_f32 v18, v244, v245\n"
    "v_cvt_pk_bf16_f32 v19, v246, v247\n"
    "v_mfma_f32_16x16x32_bf16 v[28:31], v[228:231], a[156:159], v[28:31]\n"
    "ds_write_b128 v248, v[16:19] offset:0\n"
    "v_mfma_f32_16x16x32_bf16 v[28:31], v[236:239], a[188:191], v[28:31]\n"
    "v_accvgpr_read_b32 v245, a96\n"
    "v_accvgpr_read_b32 v247, a97\n"
    "v_mfma_f32_16x16x32_bf16 v[28:31], a[52:55], a[184:187], v[28:31]\n"
    "v_lshlrev_b32 v244, 16, v245\n"
    "v_lshlrev_b32 v246, 16, v247\n"
    "v_mfma_f32_16x16x32_bf16 v[28:31], v[168:171], a[152:155], v[28:31]\n"
    "v_and_b32 v245, s58, v245\n"
    "v_and_b32 v247, s58, v247\n"
    "v_mfma_f32_16x16x32_bf16 v[28:31], v[132:135], a[148:151], v[28:31]\n"
    "v_mul_f32 v244, v244, v24\n"
    "v_mul_f32 v246, v246, v26\n"
    "v_mfma_f32_16x16x32_bf16 v[28:31], a[12:15], a[180:183], v[28:31]\n"
    "s_waitcnt vmcnt(3)\n"
    "v_mul_f32 v245, v245, v25\n"
    "v_mul_f32 v247, v247, v27\n"
    "v_mfma_f32_16x16x32_bf16 v[28:31], v[64:67], a[176:179], v[28:31]\n"
    "s_waitcnt vmcnt(2)\n"
    "v_cvt_pk_bf16_f32 v24, v244, v245\n"
    "v_cvt_pk_bf16_f32 v25, v246, v247\n"
    "v_mfma_f32_16x16x32_bf16 v[28:31], v[108:111], a[144:147], v[28:31]\n"
    "s_waitcnt lgkmcnt(8)\n"
    "v_mfma_f32_16x16x32_bf16 v[48:51], a[28:31], a[140:143], v[48:51]\n"
    "s_waitcnt lgkmcnt(7)\n"
    "v_mfma_f32_16x16x32_bf16 v[48:51], v[224:227], a[172:175], v[48:51]\n"
    "s_waitcnt lgkmcnt(6)\n"
    "ds_read_b128 a[156:159], v241 offset:4096\n"
    "v_accvgpr_read_b32 v245, a98\n"
    "v_mfma_f32_16x16x32_bf16 v[48:51], a[56:59], a[168:171], v[48:51]\n"
    "s_waitcnt lgkmcnt(6)\n"
    "v_accvgpr_read_b32 v247, a99\n"
    "v_lshlrev_b32 v244, 16, v245\n"
    "v_mfma_f32_16x16x32_bf16 v[48:51], v[200:203], a[136:139], v[48:51]\n"
    "s_waitcnt lgkmcnt(5)\n"
    "v_lshlrev_b32 v246, 16, v247\n"
    "v_and_b32 v245, s58, v245\n"
    "v_mfma_f32_16x16x32_bf16 v[48:51], v[164:167], a[132:135], v[48:51]\n"
    "s_waitcnt lgkmcnt(4)\n"
    "ds_read_b128 a[188:191], v241 offset:5120\n"
    "v_and_b32 v247, s58, v247\n"
    "v_mfma_f32_16x16x32_bf16 v[48:51], a[16:19], a[164:167], v[48:51]\n"
    "s_waitcnt lgkmcnt(4)\n"
    "v_mul_f32 v244, v244, v28\n"
    "v_mul_f32 v246, v246, v30\n"
    "v_mfma_f32_16x16x32_bf16 v[48:51], v[96:99], a[160:163], v[48:51]\n"
    "s_waitcnt lgkmcnt(3)\n"
    "v_mul_f32 v245, v245, v29\n"
    "v_mul_f32 v247, v247, v31\n"
    "v_mfma_f32_16x16x32_bf16 v[48:51], v[140:143], a[128:131], v[48:51]\n"
    "ds_read_b128 a[184:187], v241 offset:20480\n"
    "v_cvt_pk_bf16_f32 v26, v244, v245\n"
    "v_mfma_f32_16x16x32_bf16 v[52:55], a[24:27], a[140:143], v[52:55]\n"
    "v_cvt_pk_bf16_f32 v27, v246, v247\n"
    "v_mfma_f32_16x16x32_bf16 v[52:55], v[192:195], a[172:175], v[52:55]\n"
    "ds_write_b128 v249, v[24:27] offset:0\n"
    "v_mfma_f32_16x16x32_bf16 v[52:55], v[196:199], a[168:171], v[52:55]\n"
    "ds_read_b128 v[20:23], v242 offset:0\n"
    "ds_read_b128 v[28:31], v242 offset:1024\n"
    "v_mfma_f32_16x16x32_bf16 v[52:55], a[20:23], a[136:139], v[52:55]\n"
    "ds_read_b128 a[152:155], v241 offset:21504\n"
    "v_accvgpr_read_b32 v245, a68\n"
    "v_mfma_f32_16x16x32_bf16 v[52:55], v[128:131], a[132:135], v[52:55]\n"
    "v_accvgpr_read_b32 v247, a69\n"
    "v_lshlrev_b32 v244, 16, v245\n"
    "v_mfma_f32_16x16x32_bf16 v[52:55], v[172:175], a[164:167], v[52:55]\n"
    "ds_read_b128 a[148:151], v241 offset:36864\n"
    "v_lshlrev_b32 v246, 16, v247\n"
    "v_mfma_f32_16x16x32_bf16 v[52:55], a[44:47], a[160:163], v[52:55]\n"
    "v_and_b32 v245, s58, v245\n"
    "v_and_b32 v247, s58, v247\n"
    "v_mfma_f32_16x16x32_bf16 v[52:55], v[104:107], a[128:131], v[52:55]\n"
    "s_add_u32 s63, s70, 512\n"
    "s_waitcnt lgkmcnt(3)\n"
    "v_mul_f32 v244, v244, v48\n"
    "v_mfma_f32_16x16x32_bf16 v[56:59], a[60:63], a[140:143], v[56:59]\n"
    "buffer_store_dwordx4 v[20:23], v243, s[40:43], s63 offen sc1\n"
    "ds_read_b128 a[180:183], v241 offset:37888\n"
    "v_mul_f32 v246, v246, v50\n"
    "v_mul_f32 v245, v245, v49\n"
    "v_mfma_f32_16x16x32_bf16 v[56:59], v[232:235], a[172:175], v[56:59]\n"
    "s_add_u32 s63, s70, 16896\n"
    "s_waitcnt lgkmcnt(3)\n"
    "v_mul_f32 v247, v247, v51\n"
    "v_mfma_f32_16x16x32_bf16 v[56:59], v[160:163], a[168:171], v[56:59]\n"
    "buffer_store_dwordx4 v[28:31], v243, s[40:43], s63 offen sc1\n"
    "v_cvt_pk_bf16_f32 v48, v244, v245\n"
    "v_cvt_pk_bf16_f32 v49, v246, v247\n"
    "v_accvgpr_read_b32 v245, a70\n"
    "v_mfma_f32_16x16x32_bf16 v[56:59], v[204:207], a[136:139], v[56:59]\n"
    "ds_read_b128 a[176:179], v241 offset:53248\n"
    "v_accvgpr_read_b32 v247, a71\n"
    "v_mfma_f32_16x16x32_bf16 v[56:59], a[48:51], a[132:135], v[56:59]\n"
    "v_lshlrev_b32 v244, 16, v245\n"
    "v_lshlrev_b32 v246, 16, v247\n"
    "v_mfma_f32_16x16x32_bf16 v[56:59], v[136:139], a[164:167], v[56:59]\n"
    "v_and_b32 v245, s58, v245\n"
    "v_and_b32 v247, s58, v247\n"
    "v_mfma_f32_16x16x32_bf16 v[56:59], v[100:103], a[160:163], v[56:59]\n"
    "ds_read_b128 a[144:147], v241 offset:54272\n"
    "v_mul_f32 v244, v244, v52\n"
    "v_mfma_f32_16x16x32_bf16 v[56:59], a[8:11], a[128:131], v[56:59]\n"
    "v_mul_f32 v246, v246, v54\n"
    "v_mul_f32 v245, v245, v53\n"
    "v_mfma_f32_16x16x32_bf16 v[60:63], v[228:231], a[140:143], v[60:63]\n"
    "v_mul_f32 v247, v247, v55\n"
    "v_cvt_pk_bf16_f32 v50, v244, v245\n"
    "v_mfma_f32_16x16x32_bf16 v[60:63], v[236:239], a[172:175], v[60:63]\n"
    "v_cvt_pk_bf16_f32 v51, v246, v247\n"
    "v_mfma_f32_16x16x32_bf16 v[60:63], a[52:55], a[168:171], v[60:63]\n"
    "ds_write_b128 v248, v[48:51] offset:2048\n"
    "v_mfma_f32_16x16x32_bf16 v[60:63], v[168:171], a[136:139], v[60:63]\n"
    "v_accvgpr_read_b32 v245, a100\n"
    "v_accvgpr_read_b32 v247, a101\n"
    "v_mfma_f32_16x16x32_bf16 v[60:63], v[132:135], a[132:135], v[60:63]\n"
    "v_lshlrev_b32 v244, 16, v245\n"
    "v_lshlrev_b32 v246, 16, v247\n"
    "v_mfma_f32_16x16x32_bf16 v[60:63], a[12:15], a[164:167], v[60:63]\n"
    "v_and_b32 v245, s58, v245\n"
    "v_and_b32 v247, s58, v247\n"
    "v_mfma_f32_16x16x32_bf16 v[60:63], v[64:67], a[160:163], v[60:63]\n"
    "v_mul_f32 v244, v244, v56\n"
    "v_mul_f32 v246, v246, v58\n"
    "v_mfma_f32_16x16x32_bf16 v[60:63], v[108:111], a[128:131], v[60:63]\n"
    "v_mul_f32 v245, v245, v57\n"
    "v_mul_f32 v247, v247, v59\n"
    "v_mfma_f32_16x16x32_bf16 v[80:83], a[28:31], a[156:159], v[80:83]\n"
    "v_cvt_pk_bf16_f32 v56, v244, v245\n"
    "v_cvt_pk_bf16_f32 v57, v246, v247\n"
    "v_mfma_f32_16x16x32_bf16 v[80:83], v[224:227], a[188:191], v[80:83]\n"
    "ds_read_b128 a[140:143], v241 offset:14336\n"
    "v_accvgpr_read_b32 v245, a102\n"
    "v_mfma_f32_16x16x32_bf16 v[80:83], a[56:59], a[184:187], v[80:83]\n"
    "s_waitcnt lgkmcnt(6)\n"
    "v_accvgpr_read_b32 v247, a103\n"
    "v_lshlrev_b32 v244, 16, v245\n"
    "v_mfma_f32_16x16x32_bf16 v[80:83], v[200:203], a[152:155], v[80:83]\n"
    "s_waitcnt lgkmcnt(5)\n"
    "v_lshlrev_b32 v246, 16, v247\n"
    "v_and_b32 v245, s58, v245\n"
    "v_mfma_f32_16x16x32_bf16 v[80:83], v[164:167], a[148:151], v[80:83]\n"
    "s_waitcnt lgkmcnt(4)\n"
    "ds_read_b128 a[172:175], v241 offset:15360\n"
    "v_and_b32 v247, s58, v247\n"
    "v_mfma_f32_16x16x32_bf16 v[80:83], a[16:19], a[180:183], v[80:83]\n"
    "s_waitcnt lgkmcnt(4)\n"
    "v_mul_f32 v244, v244, v60\n"
    "v_mul_f32 v246, v246, v62\n"
    "v_mfma_f32_16x16x32_bf16 v[80:83], v[96:99], a[176:179], v[80:83]\n"
    "s_waitcnt lgkmcnt(3)\n"
    "v_mul_f32 v245, v245, v61\n"
    "v_mul_f32 v247, v247, v63\n"
    "v_mfma_f32_16x16x32_bf16 v[80:83], v[140:143], a[144:147], v[80:83]\n"
    "ds_read_b128 a[168:171], v241 offset:30720\n"
    "v_cvt_pk_bf16_f32 v58, v244, v245\n"
    "v_mfma_f32_16x16x32_bf16 v[84:87], a[24:27], a[156:159], v[84:87]\n"
    "v_cvt_pk_bf16_f32 v59, v246, v247\n"
    "v_mfma_f32_16x16x32_bf16 v[84:87], v[192:195], a[188:191], v[84:87]\n"
    "ds_write_b128 v249, v[56:59] offset:2048\n"
    "v_mfma_f32_16x16x32_bf16 v[84:87], v[196:199], a[184:187], v[84:87]\n"
    "ds_read_b128 v[52:55], v242 offset:2048\n"
    "ds_read_b128 v[60:63], v242 offset:3072\n"
    "v_mfma_f32_16x16x32_bf16 v[84:87], a[20:23], a[152:155], v[84:87]\n"
    "ds_read_b128 a[136:139], v241 offset:31744\n"
    "v_accvgpr_read_b32 v245, a72\n"
    "v_mfma_f32_16x16x32_bf16 v[84:87], v[128:131], a[148:151], v[84:87]\n"
    "v_accvgpr_read_b32 v247, a73\n"
    "v_lshlrev_b32 v244, 16, v245\n"
    "v_mfma_f32_16x16x32_bf16 v[84:87], v[172:175], a[180:183], v[84:87]\n"
    "ds_read_b128 a[132:135], v241 offset:47104\n"
    "v_lshlrev_b32 v246, 16, v247\n"
    "v_mfma_f32_16x16x32_bf16 v[84:87], a[44:47], a[176:179], v[84:87]\n"
    "v_and_b32 v245, s58, v245\n"
    "v_and_b32 v247, s58, v247\n"
    "v_mfma_f32_16x16x32_bf16 v[84:87], v[104:107], a[144:147], v[84:87]\n"
    "s_add_u32 s63, s70, 33280\n"
    "s_waitcnt lgkmcnt(3)\n"
    "v_mul_f32 v244, v244, v80\n"
    "v_mfma_f32_16x16x32_bf16 v[88:91], a[60:63], a[156:159], v[88:91]\n"
    "buffer_store_dwordx4 v[52:55], v243, s[40:43], s63 offen sc1\n"
    "ds_read_b128 a[164:167], v241 offset:48128\n"
    "v_mul_f32 v246, v246, v82\n"
    "v_mul_f32 v245, v245, v81\n"
    "v_mfma_f32_16x16x32_bf16 v[88:91], v[232:235], a[188:191], v[88:91]\n"
    "s_add_u32 s63, s70, 49664\n"
    "s_waitcnt lgkmcnt(3)\n"
    "v_mul_f32 v247, v247, v83\n"
    "v_mfma_f32_16x16x32_bf16 v[88:91], v[160:163], a[184:187], v[88:91]\n"
    "buffer_store_dwordx4 v[60:63], v243, s[40:43], s63 offen sc1\n"
    "v_cvt_pk_bf16_f32 v80, v244, v245\n"
    "v_cvt_pk_bf16_f32 v81, v246, v247\n"
    "v_accvgpr_read_b32 v245, a74\n"
    "v_mfma_f32_16x16x32_bf16 v[88:91], v[204:207], a[152:155], v[88:91]\n"
    "ds_read_b128 a[160:163], v241 offset:63488\n"
    "v_accvgpr_read_b32 v247, a75\n"
    "v_mfma_f32_16x16x32_bf16 v[88:91], a[48:51], a[148:151], v[88:91]\n"
    "v_lshlrev_b32 v244, 16, v245\n"
    "v_lshlrev_b32 v246, 16, v247\n"
    "v_mfma_f32_16x16x32_bf16 v[88:91], v[136:139], a[180:183], v[88:91]\n"
    "v_and_b32 v245, s58, v245\n"
    "v_and_b32 v247, s58, v247\n"
    "v_mfma_f32_16x16x32_bf16 v[88:91], v[100:103], a[176:179], v[88:91]\n"
    "ds_read_b128 a[128:131], v241 offset:64512\n"
    "v_mul_f32 v244, v244, v84\n"
    "v_mfma_f32_16x16x32_bf16 v[88:91], a[8:11], a[144:147], v[88:91]\n"
    "v_mul_f32 v246, v246, v86\n"
    "v_mul_f32 v245, v245, v85\n"
    "v_mfma_f32_16x16x32_bf16 v[92:95], v[228:231], a[156:159], v[92:95]\n"
    "v_mul_f32 v247, v247, v87\n"
    "v_cvt_pk_bf16_f32 v82, v244, v245\n"
    "v_mfma_f32_16x16x32_bf16 v[92:95], v[236:239], a[188:191], v[92:95]\n"
    "v_cvt_pk_bf16_f32 v83, v246, v247\n"
    "v_mfma_f32_16x16x32_bf16 v[92:95], a[52:55], a[184:187], v[92:95]\n"
    "ds_write_b128 v248, v[80:83] offset:0\n"
    "v_mfma_f32_16x16x32_bf16 v[92:95], v[168:171], a[152:155], v[92:95]\n"
    "v_accvgpr_read_b32 v245, a104\n"
    "v_accvgpr_read_b32 v247, a105\n"
    "v_mfma_f32_16x16x32_bf16 v[92:95], v[132:135], a[148:151], v[92:95]\n"
    "v_lshlrev_b32 v244, 16, v245\n"
    "v_lshlrev_b32 v246, 16, v247\n"
    "v_mfma_f32_16x16x32_bf16 v[92:95], a[12:15], a[180:183], v[92:95]\n"
    "v_and_b32 v245, s58, v245\n"
    "v_and_b32 v247, s58, v247\n"
    "v_mfma_f32_16x16x32_bf16 v[92:95], v[64:67], a[176:179], v[92:95]\n"
    "v_mul_f32 v244, v244, v88\n"
    "v_mul_f32 v246, v246, v90\n"
    "v_mfma_f32_16x16x32_bf16 v[92:95], v[108:111], a[144:147], v[92:95]\n"
    "v_mul_f32 v245, v245, v89\n"
    "v_mul_f32 v247, v247, v91\n"
    "v_mfma_f32_16x16x32_bf16 a[240:243], a[28:31], a[140:143], a[240:243]\n"
    "v_cvt_pk_bf16_f32 v88, v244, v245\n"
    "v_cvt_pk_bf16_f32 v89, v246, v247\n"
    "v_mfma_f32_16x16x32_bf16 a[240:243], v[224:227], a[172:175], a[240:243]\n"
    "ds_read_b128 a[156:159], v241 offset:6144\n"
    "v_accvgpr_read_b32 v245, a106\n"
    "v_mfma_f32_16x16x32_bf16 a[240:243], a[56:59], a[168:171], a[240:243]\n"
    "s_waitcnt lgkmcnt(6)\n"
    "v_accvgpr_read_b32 v247, a107\n"
    "v_lshlrev_b32 v244, 16, v245\n"
    "v_mfma_f32_16x16x32_bf16 a[240:243], v[200:203], a[136:139], a[240:243]\n"
    "s_waitcnt lgkmcnt(5)\n"
    "v_lshlrev_b32 v246, 16, v247\n"
    "v_and_b32 v245, s58, v245\n"
    "v_mfma_f32_16x16x32_bf16 a[240:243], v[164:167], a[132:135], a[240:243]\n"
    "s_waitcnt lgkmcnt(4)\n"
    "ds_read_b128 a[188:191], v241 offset:7168\n"
    "v_and_b32 v247, s58, v247\n"
    "v_mfma_f32_16x16x32_bf16 a[240:243], a[16:19], a[164:167], a[240:243]\n"
    "s_waitcnt lgkmcnt(4)\n"
    "v_mul_f32 v244, v244, v92\n"
    "v_mul_f32 v246, v246, v94\n"
    "v_mfma_f32_16x16x32_bf16 a[240:243], v[96:99], a[160:163], a[240:243]\n"
    "s_waitcnt lgkmcnt(3)\n"
    "v_mul_f32 v245, v245, v93\n"
    "v_mul_f32 v247, v247, v95\n"
    "v_mfma_f32_16x16x32_bf16 a[240:243], v[140:143], a[128:131], a[240:243]\n"
    "ds_read_b128 a[184:187], v241 offset:22528\n"
    "v_cvt_pk_bf16_f32 v90, v244, v245\n"
    "v_mfma_f32_16x16x32_bf16 a[244:247], a[24:27], a[140:143], a[244:247]\n"
    "v_cvt_pk_bf16_f32 v91, v246, v247\n"
    "v_mfma_f32_16x16x32_bf16 a[244:247], v[192:195], a[172:175], a[244:247]\n"
    "ds_write_b128 v249, v[88:91] offset:0\n"
    "v_mfma_f32_16x16x32_bf16 a[244:247], v[196:199], a[168:171], a[244:247]\n"
    "ds_read_b128 v[84:87], v242 offset:0\n"
    "ds_read_b128 v[92:95], v242 offset:1024\n"
    "v_mfma_f32_16x16x32_bf16 a[244:247], a[20:23], a[136:139], a[244:247]\n"
    "ds_read_b128 a[152:155], v241 offset:23552\n"
    "v_accvgpr_read_b32 v245, a92\n"
    "v_mfma_f32_16x16x32_bf16 a[244:247], v[128:131], a[132:135], a[244:247]\n"
    "v_accvgpr_read_b32 v247, a93\n"
    "v_lshlrev_b32 v244, 16, v245\n"
    "v_mfma_f32_16x16x32_bf16 a[244:247], v[172:175], a[164:167], a[244:247]\n"
    "ds_read_b128 a[148:151], v241 offset:38912\n"
    "v_lshlrev_b32 v246, 16, v247\n"
    "v_mfma_f32_16x16x32_bf16 a[244:247], a[44:47], a[160:163], a[244:247]\n"
    "v_and_b32 v245, s58, v245\n"
    "v_and_b32 v247, s58, v247\n"
    "v_mfma_f32_16x16x32_bf16 a[244:247], v[104:107], a[128:131], a[244:247]\n"
    "s_add_u32 s63, s70, 66048\n"
    "s_waitcnt lgkmcnt(3)\n"
    "v_accvgpr_read_b32 v254, a240\n"
    "v_mfma_f32_16x16x32_bf16 a[248:251], a[60:63], a[140:143], a[248:251]\n"
    "buffer_store_dwordx4 v[84:87], v243, s[40:43], s63 offen sc1\n"
    "ds_read_b128 a[180:183], v241 offset:39936\n"
    "v_accvgpr_read_b32 v255, a242\n"
    "v_mul_f32 v244, v244, v254\n"
    "v_mfma_f32_16x16x32_bf16 a[248:251], v[232:235], a[172:175], a[248:251]\n"
    "s_add_u32 s63, s70, 82432\n"
    "s_waitcnt lgkmcnt(3)\n"
    "v_mul_f32 v246, v246, v255\n"
    "v_mfma_f32_16x16x32_bf16 a[248:251], v[160:163], a[168:171], a[248:251]\n"
    "buffer_store_dwordx4 v[92:95], v243, s[40:43], s63 offen sc1\n"
    "v_accvgpr_read_b32 v254, a241\n"
    "v_accvgpr_read_b32 v255, a243\n"
    "v_mul_f32 v245, v245, v254\n"
    "v_mfma_f32_16x16x32_bf16 a[248:251], v[204:207], a[136:139], a[248:251]\n"
    "ds_read_b128 a[176:179], v241 offset:55296\n"
    "v_mul_f32 v247, v247, v255\n"
    "v_mfma_f32_16x16x32_bf16 a[248:251], a[48:51], a[132:135], a[248:251]\n"
    "v_cvt_pk_bf16_f32 v16, v244, v245\n"
    "v_cvt_pk_bf16_f32 v17, v246, v247\n"
    "v_mfma_f32_16x16x32_bf16 a[248:251], v[136:139], a[164:167], a[248:251]\n"
    "v_accvgpr_read_b32 v245, a94\n"
    "v_accvgpr_read_b32 v247, a95\n"
    "v_mfma_f32_16x16x32_bf16 a[248:251], v[100:103], a[160:163], a[248:251]\n"
    "ds_read_b128 a[144:147], v241 offset:56320\n"
    "v_lshlrev_b32 v244, 16, v245\n"
    "v_mfma_f32_16x16x32_bf16 a[248:251], a[8:11], a[128:131], a[248:251]\n"
    "v_lshlrev_b32 v246, 16, v247\n"
    "v_and_b32 v245, s58, v245\n"
    "v_mfma_f32_16x16x32_bf16 a[252:255], v[228:231], a[140:143], a[252:255]\n"
    "v_and_b32 v247, s58, v247\n"
    "v_accvgpr_read_b32 v254, a244\n"
    "v_mfma_f32_16x16x32_bf16 a[252:255], v[236:239], a[172:175], a[252:255]\n"
    "v_accvgpr_read_b32 v255, a246\n"
    "v_mul_f32 v244, v244, v254\n"
    "v_mfma_f32_16x16x32_bf16 a[252:255], a[52:55], a[168:171], a[252:255]\n"
    "v_mul_f32 v246, v246, v255\n"
    "v_accvgpr_read_b32 v254, a245\n"
    "v_mfma_f32_16x16x32_bf16 a[252:255], v[168:171], a[136:139], a[252:255]\n"
    "v_accvgpr_read_b32 v255, a247\n"
    "v_mul_f32 v245, v245, v254\n"
    "v_mfma_f32_16x16x32_bf16 a[252:255], v[132:135], a[132:135], a[252:255]\n"
    "v_mul_f32 v247, v247, v255\n"
    "v_cvt_pk_bf16_f32 v18, v244, v245\n"
    "v_mfma_f32_16x16x32_bf16 a[252:255], a[12:15], a[164:167], a[252:255]\n"
    "v_cvt_pk_bf16_f32 v19, v246, v247\n"
    "v_mfma_f32_16x16x32_bf16 a[252:255], v[64:67], a[160:163], a[252:255]\n"
    "ds_write_b128 v248, v[16:19] offset:2048\n"
    "v_mfma_f32_16x16x32_bf16 a[252:255], v[108:111], a[128:131], a[252:255]\n"
    "v_accvgpr_read_b32 v245, a124\n"
    "v_accvgpr_read_b32 v247, a125\n"
    "v_mfma_f32_16x16x32_bf16 v[112:115], a[28:31], a[156:159], v[112:115]\n"
    "v_lshlrev_b32 v244, 16, v245\n"
    "v_lshlrev_b32 v246, 16, v247\n"
    "v_mfma_f32_16x16x32_bf16 v[112:115], v[224:227], a[188:191], v[112:115]\n"
    "ds_read_b128 a[140:143], v241 offset:8192\n"
    "v_and_b32 v245, s58, v245\n"
    "v_mfma_f32_16x16x32_bf16 v[112:115], a[56:59], a[184:187], v[112:115]\n"
    "s_waitcnt lgkmcnt(6)\n"
    "v_and_b32 v247, s58, v247\n"
    "v_accvgpr_read_b32 v254, a248\n"
    "v_mfma_f32_16x16x32_bf16 v[112:115], v[200:203], a[152:155], v[112:115]\n"
    "s_waitcnt lgkmcnt(5)\n"
    "v_accvgpr_read_b32 v255, a250\n"
    "v_mul_f32 v244, v244, v254\n"
    "v_mfma_f32_16x16x32_bf16 v[112:115], v[164:167], a[148:151], v[112:115]\n"
    "s_waitcnt lgkmcnt(4)\n"
    "ds_read_b128 a[172:175], v241 offset:9216\n"
    "v_mul_f32 v246, v246, v255\n"
    "v_mfma_f32_16x16x32_bf16 v[112:115], a[16:19], a[180:183], v[112:115]\n"
    "s_waitcnt lgkmcnt(4)\n"
    "v_accvgpr_read_b32 v254, a249\n"
    "v_accvgpr_read_b32 v255, a251\n"
    "v_mfma_f32_16x16x32_bf16 v[112:115], v[96:99], a[176:179], v[112:115]\n"
    "s_waitcnt lgkmcnt(3)\n"
    "v_mul_f32 v245, v245, v254\n"
    "v_mul_f32 v247, v247, v255\n"
    "v_mfma_f32_16x16x32_bf16 v[112:115], v[140:143], a[144:147], v[112:115]\n"
    "ds_read_b128 a[168:171], v241 offset:24576\n"
    "v_cvt_pk_bf16_f32 v24, v244, v245\n"
    "v_mfma_f32_16x16x32_bf16 v[116:119], a[24:27], a[156:159], v[116:119]\n"
    "v_cvt_pk_bf16_f32 v25, v246, v247\n"
    "v_accvgpr_read_b32 v245, a126\n"
    "v_mfma_f32_16x16x32_bf16 v[116:119], v[192:195], a[188:191], v[116:119]\n"
    "v_accvgpr_read_b32 v247, a127\n"
    "v_lshlrev_b32 v244, 16, v245\n"
    "v_mfma_f32_16x16x32_bf16 v[116:119], v[196:199], a[184:187], v[116:119]\n"
    "ds_read_b128 a[136:139], v241 offset:25600\n"
    "v_lshlrev_b32 v246, 16, v247\n"
    "v_mfma_f32_16x16x32_bf16 v[116:119], a[20:23], a[152:155], v[116:119]\n"
    "v_and_b32 v245, s58, v245\n"
    "v_and_b32 v247, s58, v247\n"
    "v_mfma_f32_16x16x32_bf16 v[116:119], v[128:131], a[148:151], v[116:119]\n"
    "v_accvgpr_read_b32 v254, a252\n"
    "v_accvgpr_read_b32 v255, a254\n"
    "v_mfma_f32_16x16x32_bf16 v[116:119], v[172:175], a[180:183], v[116:119]\n"
    "ds_read_b128 a[132:135], v241 offset:40960\n"
    "v_mul_f32 v244, v244, v254\n"
    "v_mfma_f32_16x16x32_bf16 v[116:119], a[44:47], a[176:179], v[116:119]\n"
    "v_mul_f32 v246, v246, v255\n"
    "v_accvgpr_read_b32 v254, a253\n"
    "v_mfma_f32_16x16x32_bf16 v[116:119], v[104:107], a[144:147], v[116:119]\n"
    "v_accvgpr_read_b32 v255, a255\n"
    "v_mul_f32 v245, v245, v254\n"
    "v_mfma_f32_16x16x32_bf16 v[120:123], a[60:63], a[156:159], v[120:123]\n"
    "ds_read_b128 a[164:167], v241 offset:41984\n"
    "v_mul_f32 v247, v247, v255\n"
    "v_mfma_f32_16x16x32_bf16 v[120:123], v[232:235], a[188:191], v[120:123]\n"
    "v_cvt_pk_bf16_f32 v26, v244, v245\n"
    "v_cvt_pk_bf16_f32 v27, v246, v247\n"
    "v_mfma_f32_16x16x32_bf16 v[120:123], v[160:163], a[184:187], v[120:123]\n"
    "ds_write_b128 v249, v[24:27] offset:2048\n"
    "v_mfma_f32_16x16x32_bf16 v[120:123], v[204:207], a[152:155], v[120:123]\n"
    "ds_read_b128 a[244:247], v242 offset:2048\n"
    "ds_read_b128 a[252:255], v242 offset:3072\n"
    "v_mfma_f32_16x16x32_bf16 v[120:123], a[48:51], a[148:151], v[120:123]\n"
    "ds_read_b128 a[160:163], v241 offset:57344\n"
    "v_accvgpr_read_b32 v245, a76\n"
    "v_mfma_f32_16x16x32_bf16 v[120:123], v[136:139], a[180:183], v[120:123]\n"
    "v_accvgpr_read_b32 v247, a77\n"
    "v_lshlrev_b32 v244, 16, v245\n"
    "v_mfma_f32_16x16x32_bf16 v[120:123], v[100:103], a[176:179], v[120:123]\n"
    "ds_read_b128 a[128:131], v241 offset:58368\n"
    "v_lshlrev_b32 v246, 16, v247\n"
    "v_mfma_f32_16x16x32_bf16 v[120:123], a[8:11], a[144:147], v[120:123]\n"
    "v_and_b32 v245, s58, v245\n"
    "v_and_b32 v247, s58, v247\n"
    "v_mfma_f32_16x16x32_bf16 v[124:127], v[228:231], a[156:159], v[124:127]\n"
    "v_mul_f32 v244, v244, v112\n"
    "v_mul_f32 v246, v246, v114\n"
    "v_mfma_f32_16x16x32_bf16 v[124:127], v[236:239], a[188:191], v[124:127]\n"
    "s_add_u32 s63, s70, 229888\n"
    "s_waitcnt lgkmcnt(3)\n"
    "v_mul_f32 v245, v245, v113\n"
    "v_mfma_f32_16x16x32_bf16 v[124:127], a[52:55], a[184:187], v[124:127]\n"
    "buffer_store_dwordx4 a[244:247], v243, s[40:43], s63 offen sc1\n"
    "v_mul_f32 v247, v247, v115\n"
    "v_cvt_pk_bf16_f32 v112, v244, v245\n"
    "v_cvt_pk_bf16_f32 v113, v246, v247\n"
    "v_mfma_f32_16x16x32_bf16 v[124:127], v[168:171], a[152:155], v[124:127]\n"
    "v_accvgpr_read_b32 v245, a78\n"
    "v_accvgpr_read_b32 v247, a79\n"
    "v_mfma_f32_16x16x32_bf16 v[124:127], v[132:135], a[148:151], v[124:127]\n"
    "v_lshlrev_b32 v244, 16, v245\n"
    "v_lshlrev_b32 v246, 16, v247\n"
    "v_mfma_f32_16x16x32_bf16 v[124:127], a[12:15], a[180:183], v[124:127]\n"
    "v_and_b32 v245, s58, v245\n"
    "v_and_b32 v247, s58, v247\n"
    "v_mfma_f32_16x16x32_bf16 v[124:127], v[64:67], a[176:179], v[124:127]\n"
    "v_mul_f32 v244, v244, v116\n"
    "v_mul_f32 v246, v246, v118\n"
    "v_mfma_f32_16x16x32_bf16 v[124:127], v[108:111], a[144:147], v[124:127]\n"
    "s_add_u32 s63, s70, 246272\n"
    "s_waitcnt lgkmcnt(2)\n"
    "v_mul_f32 v245, v245, v117\n"
    "v_mfma_f32_16x16x32_bf16 v[144:147], a[28:31], a[140:143], v[144:147]\n"
    "buffer_store_dwordx4 a[252:255], v243, s[40:43], s63 offen sc1\n"
    "v_mul_f32 v247, v247, v119\n"
    "v_cvt_pk_bf16_f32 v114, v244, v245\n"
    "v_cvt_pk_bf16_f32 v115, v246, v247\n"
    "v_mfma_f32_16x16x32_bf16 v[144:147], v[224:227], a[172:175], v[144:147]\n"
    "ds_write_b128 v248, v[112:115] offset:2048\n"
    "v_mfma_f32_16x16x32_bf16 v[144:147], a[56:59], a[168:171], v[144:147]\n"
    "ds_read_b128 a[156:159], v241 offset:10240\n"
    "v_accvgpr_read_b32 v245, a108\n"
    "v_mfma_f32_16x16x32_bf16 v[144:147], v[200:203], a[136:139], v[144:147]\n"
    "v_accvgpr_read_b32 v247, a109\n"
    "v_lshlrev_b32 v244, 16, v245\n"
    "v_mfma_f32_16x16x32_bf16 v[144:147], v[164:167], a[132:135], v[144:147]\n"
    "ds_read_b128 a[188:191], v241 offset:11264\n"
    "v_lshlrev_b32 v246, 16, v247\n"
    "v_mfma_f32_16x16x32_bf16 v[144:147], a[16:19], a[164:167], v[144:147]\n"
    "s_waitcnt lgkmcnt(4)\n"
    "v_and_b32 v245, s58, v245\n"
    "v_and_b32 v247, s58, v247\n"
    "v_mfma_f32_16x16x32_bf16 v[144:147], v[96:99], a[160:163], v[144:147]\n"
    "s_waitcnt lgkmcnt(3)\n"
    "v_mul_f32 v244, v244, v120\n"
    "v_mul_f32 v246, v246, v122\n"
    "v_mfma_f32_16x16x32_bf16 v[144:147], v[140:143], a[128:131], v[144:147]\n"
    "ds_read_b128 a[184:187], v241 offset:26624\n"
    "v_mul_f32 v245, v245, v121\n"
    "v_mfma_f32_16x16x32_bf16 v[148:151], a[24:27], a[140:143], v[148:151]\n"
    "v_mul_f32 v247, v247, v123\n"
    "v_cvt_pk_bf16_f32 v120, v244, v245\n"
    "v_mfma_f32_16x16x32_bf16 v[148:151], v[192:195], a[172:175], v[148:151]\n"
    "v_cvt_pk_bf16_f32 v121, v246, v247\n"
    "v_accvgpr_read_b32 v245, a110\n"
    "v_mfma_f32_16x16x32_bf16 v[148:151], v[196:199], a[168:171], v[148:151]\n"
    "ds_read_b128 a[152:155], v241 offset:27648\n"
    "v_accvgpr_read_b32 v247, a111\n"
    "v_mfma_f32_16x16x32_bf16 v[148:151], a[20:23], a[136:139], v[148:151]\n"
    "v_lshlrev_b32 v244, 16, v245\n"
    "v_lshlrev_b32 v246, 16, v247\n"
    "v_mfma_f32_16x16x32_bf16 v[148:151], v[128:131], a[132:135], v[148:151]\n"
    "v_and_b32 v245, s58, v245\n"
    "v_and_b32 v247, s58, v247\n"
    "v_mfma_f32_16x16x32_bf16 v[148:151], v[172:175], a[164:167], v[148:151]\n"
    "ds_read_b128 a[148:151], v241 offset:43008\n"
    "v_mul_f32 v244, v244, v124\n"
    "v_mfma_f32_16x16x32_bf16 v[148:151], a[44:47], a[160:163], v[148:151]\n"
    "v_mul_f32 v246, v246, v126\n"
    "v_mul_f32 v245, v245, v125\n"
    "v_mfma_f32_16x16x32_bf16 v[148:151], v[104:107], a[128:131], v[148:151]\n"
    "v_mul_f32 v247, v247, v127\n"
    "v_cvt_pk_bf16_f32 v122, v244, v245\n"
    "v_mfma_f32_16x16x32_bf16 v[152:155], a[60:63], a[140:143], v[152:155]\n"
    "ds_read_b128 a[180:183], v241 offset:44032\n"
    "v_cvt_pk_bf16_f32 v123, v246, v247\n"
    "v_mfma_f32_16x16x32_bf16 v[152:155], v[232:235], a[172:175], v[152:155]\n"
    "ds_write_b128 v249, v[120:123] offset:2048\n"
    "v_mfma_f32_16x16x32_bf16 v[152:155], v[160:163], a[168:171], v[152:155]\n"
    "ds_read_b128 v[116:119], v242 offset:2048\n"
    "ds_read_b128 v[124:127], v242 offset:3072\n"
    "v_mfma_f32_16x16x32_bf16 v[152:155], v[204:207], a[136:139], v[152:155]\n"
    "ds_read_b128 a[176:179], v241 offset:59392\n"
    "v_accvgpr_read_b32 v245, a80\n"
    "v_mfma_f32_16x16x32_bf16 v[152:155], a[48:51], a[132:135], v[152:155]\n"
    "v_accvgpr_read_b32 v247, a81\n"
    "v_lshlrev_b32 v244, 16, v245\n"
    "v_mfma_f32_16x16x32_bf16 v[152:155], v[136:139], a[164:167], v[152:155]\n"
    "v_lshlrev_b32 v246, 16, v247\n"
    "v_and_b32 v245, s58, v245\n"
    "v_mfma_f32_16x16x32_bf16 v[152:155], v[100:103], a[160:163], v[152:155]\n"
    "ds_read_b128 a[144:147], v241 offset:60416\n"
    "v_and_b32 v247, s58, v247\n"
    "v_mfma_f32_16x16x32_bf16 v[152:155], a[8:11], a[128:131], v[152:155]\n"
    "s_add_u32 s63, s70, 98816\n"
    "s_waitcnt lgkmcnt(3)\n"
    "v_mul_f32 v244, v244, v144\n"
    "v_mfma_f32_16x16x32_bf16 v[156:159], v[228:231], a[140:143], v[156:159]\n"
    "buffer_store_dwordx4 v[116:119], v243, s[40:43], s63 offen sc1\n"
    "v_mul_f32 v246, v246, v146\n"
    "v_mul_f32 v245, v245, v145\n"
    "v_mul_f32 v247, v247, v147\n"
    "v_mfma_f32_16x16x32_bf16 v[156:159], v[236:239], a[172:175], v[156:159]\n"
    "v_cvt_pk_bf16_f32 v144, v244, v245\n"
    "v_cvt_pk_bf16_f32 v145, v246, v247\n"
    "v_mfma_f32_16x16x32_bf16 v[156:159], a[52:55], a[168:171], v[156:159]\n"
    "v_accvgpr_read_b32 v245, a82\n"
    "v_accvgpr_read_b32 v247, a83\n"
    "v_mfma_f32_16x16x32_bf16 v[156:159], v[168:171], a[136:139], v[156:159]\n"
    "v_lshlrev_b32 v244, 16, v245\n"
    "v_lshlrev_b32 v246, 16, v247\n"
    "v_mfma_f32_16x16x32_bf16 v[156:159], v[132:135], a[132:135], v[156:159]\n"
    "v_and_b32 v245, s58, v245\n"
    "v_and_b32 v247, s58, v247\n"
    "v_mfma_f32_16x16x32_bf16 v[156:159], a[12:15], a[164:167], v[156:159]\n"
    "s_add_u32 s63, s70, 115200\n"
    "s_waitcnt lgkmcnt(2)\n"
    "v_mul_f32 v244, v244, v148\n"
    "v_mfma_f32_16x16x32_bf16 v[156:159], v[64:67], a[160:163], v[156:159]\n"
    "buffer_store_dwordx4 v[124:127], v243, s[40:43], s63 offen sc1\n"
    "v_mul_f32 v246, v246, v150\n"
    "v_mul_f32 v245, v245, v149\n"
    "v_mul_f32 v247, v247, v151\n"
    "v_mfma_f32_16x16x32_bf16 v[156:159], v[108:111], a[128:131], v[156:159]\n"
    "v_cvt_pk_bf16_f32 v146, v244, v245\n"
    "v_cvt_pk_bf16_f32 v147, v246, v247\n"
    "v_mfma_f32_16x16x32_bf16 v[176:179], a[28:31], a[156:159], v[176:179]\n"
    "ds_write_b128 v248, v[144:147] offset:0\n"
    "v_mfma_f32_16x16x32_bf16 v[176:179], v[224:227], a[188:191], v[176:179]\n"
    "ds_read_b128 a[140:143], v241 offset:12288\n"
    "v_accvgpr_read_b32 v245, a112\n"
    "v_mfma_f32_16x16x32_bf16 v[176:179], a[56:59], a[184:187], v[176:179]\n"
    "v_accvgpr_read_b32 v247, a113\n"
    "v_lshlrev_b32 v244, 16, v245\n"
    "v_mfma_f32_16x16x32_bf16 v[176:179], v[200:203], a[152:155], v[176:179]\n"
    "v_lshlrev_b32 v246, 16, v247\n"
    "v_and_b32 v245, s58, v245\n"
    "v_mfma_f32_16x16x32_bf16 v[176:179], v[164:167], a[148:151], v[176:179]\n"
    "ds_read_b128 a[172:175], v241 offset:13312\n"
    "v_and_b32 v247, s58, v247\n"
    "v_mfma_f32_16x16x32_bf16 v[176:179], a[16:19], a[180:183], v[176:179]\n"
    "s_waitcnt lgkmcnt(4)\n"
    "v_mul_f32 v244, v244, v152\n"
    "v_mul_f32 v246, v246, v154\n"
    "v_mfma_f32_16x16x32_bf16 v[176:179], v[96:99], a[176:179], v[176:179]\n"
    "s_waitcnt lgkmcnt(3)\n"
    "v_mul_f32 v245, v245, v153\n"
    "v_mul_f32 v247, v247, v155\n"
    "v_mfma_f32_16x16x32_bf16 v[176:179], v[140:143], a[144:147], v[176:179]\n"
    "ds_read_b128 a[168:171], v241 offset:28672\n"
    "v_cvt_pk_bf16_f32 v152, v244, v245\n"
    "v_mfma_f32_16x16x32_bf16 v[180:183], a[24:27], a[156:159], v[180:183]\n"
    "v_cvt_pk_bf16_f32 v153, v246, v247\n"
    "v_accvgpr_read_b32 v245, a114\n"
    "v_mfma_f32_16x16x32_bf16 v[180:183], v[192:195], a[188:191], v[180:183]\n"
    "v_accvgpr_read_b32 v247, a115\n"
    "v_lshlrev_b32 v244, 16, v245\n"
    "v_mfma_f32_16x16x32_bf16 v[180:183], v[196:199], a[184:187], v[180:183]\n"
    "ds_read_b128 a[136:139], v241 offset:29696\n"
    "v_lshlrev_b32 v246, 16, v247\n"
    "v_mfma_f32_16x16x32_bf16 v[180:183], a[20:23], a[152:155], v[180:183]\n"
    "v_and_b32 v245, s58, v245\n"
    "v_and_b32 v247, s58, v247\n"
    "v_mfma_f32_16x16x32_bf16 v[180:183], v[128:131], a[148:151], v[180:183]\n"
    "v_mul_f32 v244, v244, v156\n"
    "v_mul_f32 v246, v246, v158\n"
    "v_mfma_f32_16x16x32_bf16 v[180:183], v[172:175], a[180:183], v[180:183]\n"
    "ds_read_b128 a[132:135], v241 offset:45056\n"
    "v_mul_f32 v245, v245, v157\n"
    "v_mfma_f32_16x16x32_bf16 v[180:183], a[44:47], a[176:179], v[180:183]\n"
    "v_mul_f32 v247, v247, v159\n"
    "v_cvt_pk_bf16_f32 v154, v244, v245\n"
    "v_mfma_f32_16x16x32_bf16 v[180:183], v[104:107], a[144:147], v[180:183]\n"
    "v_cvt_pk_bf16_f32 v155, v246, v247\n"
    "v_mfma_f32_16x16x32_bf16 v[184:187], a[60:63], a[156:159], v[184:187]\n"
    "ds_write_b128 v249, v[152:155] offset:0\n"
    "v_mfma_f32_16x16x32_bf16 v[184:187], v[232:235], a[188:191], v[184:187]\n"
    "ds_read_b128 a[164:167], v241 offset:46080\n"
    "ds_read_b128 v[148:151], v242 offset:0\n"
    "v_mfma_f32_16x16x32_bf16 v[184:187], v[160:163], a[184:187], v[184:187]\n"
    "ds_read_b128 v[156:159], v242 offset:1024\n"
    "v_accvgpr_read_b32 v245, a84\n"
    "v_mfma_f32_16x16x32_bf16 v[184:187], v[204:207], a[152:155], v[184:187]\n"
    "ds_read_b128 a[160:163], v241 offset:61440\n"
    "v_accvgpr_read_b32 v247, a85\n"
    "v_mfma_f32_16x16x32_bf16 v[184:187], a[48:51], a[148:151], v[184:187]\n"
    "v_lshlrev_b32 v244, 16, v245\n"
    "v_lshlrev_b32 v246, 16, v247\n"
    "v_mfma_f32_16x16x32_bf16 v[184:187], v[136:139], a[180:183], v[184:187]\n"
    "v_and_b32 v245, s58, v245\n"
    "v_and_b32 v247, s58, v247\n"
    "v_mfma_f32_16x16x32_bf16 v[184:187], v[100:103], a[176:179], v[184:187]\n"
    "ds_read_b128 a[128:131], v241 offset:62464\n"
    "v_mul_f32 v244, v244, v176\n"
    "v_mfma_f32_16x16x32_bf16 v[184:187], a[8:11], a[144:147], v[184:187]\n"
    "s_add_u32 s63, s70, 131584\n"
    "s_waitcnt lgkmcnt(3)\n"
    "v_mul_f32 v246, v246, v178\n"
    "v_mfma_f32_16x16x32_bf16 v[188:191], v[228:231], a[156:159], v[188:191]\n"
    "buffer_store_dwordx4 v[148:151], v243, s[40:43], s63 offen sc1\n"
    "v_mul_f32 v245, v245, v177\n"
    "v_mul_f32 v247, v247, v179\n"
    "v_cvt_pk_bf16_f32 v176, v244, v245\n"
    "v_mfma_f32_16x16x32_bf16 v[188:191], v[236:239], a[188:191], v[188:191]\n"
    "v_cvt_pk_bf16_f32 v177, v246, v247\n"
    "v_accvgpr_read_b32 v245, a86\n"
    "v_mfma_f32_16x16x32_bf16 v[188:191], a[52:55], a[184:187], v[188:191]\n"
    "v_accvgpr_read_b32 v247, a87\n"
    "v_lshlrev_b32 v244, 16, v245\n"
    "v_mfma_f32_16x16x32_bf16 v[188:191], v[168:171], a[152:155], v[188:191]\n"
    "v_lshlrev_b32 v246, 16, v247\n"
    "v_and_b32 v245, s58, v245\n"
    "v_mfma_f32_16x16x32_bf16 v[188:191], v[132:135], a[148:151], v[188:191]\n"
    "v_and_b32 v247, s58, v247\n"
    "v_mul_f32 v244, v244, v180\n"
    "v_mfma_f32_16x16x32_bf16 v[188:191], a[12:15], a[180:183], v[188:191]\n"
    "s_add_u32 s63, s70, 147968\n"
    "s_waitcnt lgkmcnt(2)\n"
    "v_mul_f32 v246, v246, v182\n"
    "v_mfma_f32_16x16x32_bf16 v[188:191], v[64:67], a[176:179], v[188:191]\n"
    "buffer_store_dwordx4 v[156:159], v243, s[40:43], s63 offen sc1\n"
    "v_mul_f32 v245, v245, v181\n"
    "v_mul_f32 v247, v247, v183\n"
    "v_cvt_pk_bf16_f32 v178, v244, v245\n"
    "v_mfma_f32_16x16x32_bf16 v[188:191], v[108:111], a[144:147], v[188:191]\n"
    "v_cvt_pk_bf16_f32 v179, v246, v247\n"
    "v_mfma_f32_16x16x32_bf16 v[208:211], a[28:31], a[140:143], v[208:211]\n"
    "ds_write_b128 v248, v[176:179] offset:2048\n"
    "v_mfma_f32_16x16x32_bf16 v[208:211], v[224:227], a[172:175], v[208:211]\n"
    "v_accvgpr_read_b32 v245, a116\n"
    "v_accvgpr_read_b32 v247, a117\n"
    "v_mfma_f32_16x16x32_bf16 v[208:211], a[56:59], a[168:171], v[208:211]\n"
    "v_lshlrev_b32 v244, 16, v245\n"
    "v_lshlrev_b32 v246, 16, v247\n"
    "v_mfma_f32_16x16x32_bf16 v[208:211], v[200:203], a[136:139], v[208:211]\n"
    "v_and_b32 v245, s58, v245\n"
    "v_and_b32 v247, s58, v247\n"
    "v_mfma_f32_16x16x32_bf16 v[208:211], v[164:167], a[132:135], v[208:211]\n"
    "v_mul_f32 v244, v244, v184\n"
    "v_mul_f32 v246, v246, v186\n"
    "v_mfma_f32_16x16x32_bf16 v[208:211], a[16:19], a[164:167], v[208:211]\n"
    "s_waitcnt lgkmcnt(2)\n"
    "v_mul_f32 v245, v245, v185\n"
    "v_mul_f32 v247, v247, v187\n"
    "v_mfma_f32_16x16x32_bf16 v[208:211], v[96:99], a[160:163], v[208:211]\n"
    "s_waitcnt lgkmcnt(1)\n"
    "v_cvt_pk_bf16_f32 v184, v244, v245\n"
    "v_cvt_pk_bf16_f32 v185, v246, v247\n"
    "v_mfma_f32_16x16x32_bf16 v[208:211], v[140:143], a[128:131], v[208:211]\n"
    "v_accvgpr_read_b32 v245, a118\n"
    "v_accvgpr_read_b32 v247, a119\n"
    "v_mfma_f32_16x16x32_bf16 v[212:215], a[24:27], a[140:143], v[212:215]\n"
    "v_lshlrev_b32 v244, 16, v245\n"
    "v_lshlrev_b32 v246, 16, v247\n"
    "v_mfma_f32_16x16x32_bf16 v[212:215], v[192:195], a[172:175], v[212:215]\n"
    "v_and_b32 v245, s58, v245\n"
    "v_and_b32 v247, s58, v247\n"
    "v_mfma_f32_16x16x32_bf16 v[212:215], v[196:199], a[168:171], v[212:215]\n"
    "v_mul_f32 v244, v244, v188\n"
    "v_mul_f32 v246, v246, v190\n"
    "v_mfma_f32_16x16x32_bf16 v[212:215], a[20:23], a[136:139], v[212:215]\n"
    "v_mul_f32 v245, v245, v189\n"
    "v_mul_f32 v247, v247, v191\n"
    "v_mfma_f32_16x16x32_bf16 v[212:215], v[128:131], a[132:135], v[212:215]\n"
    "v_cvt_pk_bf16_f32 v186, v244, v245\n"
    "v_cvt_pk_bf16_f32 v187, v246, v247\n"
    "v_mfma_f32_16x16x32_bf16 v[212:215], v[172:175], a[164:167], v[212:215]\n"
    "ds_write_b128 v249, v[184:187] offset:2048\n"
    "v_mfma_f32_16x16x32_bf16 v[212:215], a[44:47], a[160:163], v[212:215]\n"
    "ds_read_b128 v[180:183], v242 offset:2048\n"
    "ds_read_b128 v[188:191], v242 offset:3072\n"
    "v_mfma_f32_16x16x32_bf16 v[212:215], v[104:107], a[128:131], v[212:215]\n"
    "v_accvgpr_read_b32 v245, a88\n"
    "v_accvgpr_read_b32 v247, a89\n"
    "v_mfma_f32_16x16x32_bf16 v[216:219], a[60:63], a[140:143], v[216:219]\n"
    "v_lshlrev_b32 v244, 16, v245\n"
    "v_lshlrev_b32 v246, 16, v247\n"
    "v_mfma_f32_16x16x32_bf16 v[216:219], v[232:235], a[172:175], v[216:219]\n"
    "v_and_b32 v245, s58, v245\n"
    "v_and_b32 v247, s58, v247\n"
    "v_mfma_f32_16x16x32_bf16 v[216:219], v[160:163], a[168:171], v[216:219]\n"
    "v_mul_f32 v244, v244, v208\n"
    "v_mul_f32 v246, v246, v210\n"
    "v_mfma_f32_16x16x32_bf16 v[216:219], v[204:207], a[136:139], v[216:219]\n"
    "s_add_u32 s63, s70, 164352\n"
    "s_waitcnt lgkmcnt(1)\n"
    "v_mul_f32 v245, v245, v209\n"
    "v_mfma_f32_16x16x32_bf16 v[216:219], a[48:51], a[132:135], v[216:219]\n"
    "buffer_store_dwordx4 v[180:183], v243, s[40:43], s63 offen sc1\n"
    "v_mul_f32 v247, v247, v211\n"
    "v_cvt_pk_bf16_f32 v208, v244, v245\n"
    "v_cvt_pk_bf16_f32 v209, v246, v247\n"
    "v_mfma_f32_16x16x32_bf16 v[216:219], v[136:139], a[164:167], v[216:219]\n"
    "v_accvgpr_read_b32 v245, a90\n"
    "v_accvgpr_read_b32 v247, a91\n"
    "v_mfma_f32_16x16x32_bf16 v[216:219], v[100:103], a[160:163], v[216:219]\n"
    "v_lshlrev_b32 v244, 16, v245\n"
    "v_lshlrev_b32 v246, 16, v247\n"
    "v_mfma_f32_16x16x32_bf16 v[216:219], a[8:11], a[128:131], v[216:219]\n"
    "v_and_b32 v245, s58, v245\n"
    "v_and_b32 v247, s58, v247\n"
    "v_mfma_f32_16x16x32_bf16 v[220:223], v[228:231], a[140:143], v[220:223]\n"
    "v_mul_f32 v244, v244, v212\n"
    "v_mul_f32 v246, v246, v214\n"
    "v_mfma_f32_16x16x32_bf16 v[220:223], v[236:239], a[172:175], v[220:223]\n"
    "s_add_u32 s63, s70, 180736\n"
    "s_waitcnt lgkmcnt(0)\n"
    "v_mul_f32 v245, v245, v213\n"
    "v_mfma_f32_16x16x32_bf16 v[220:223], a[52:55], a[168:171], v[220:223]\n"
    "buffer_store_dwordx4 v[188:191], v243, s[40:43], s63 offen sc1\n"
    "v_mul_f32 v247, v247, v215\n"
    "v_cvt_pk_bf16_f32 v210, v244, v245\n"
    "v_cvt_pk_bf16_f32 v211, v246, v247\n"
    "v_mfma_f32_16x16x32_bf16 v[220:223], v[168:171], a[136:139], v[220:223]\n"
    "ds_write_b128 v248, v[208:211] offset:0\n"
    "v_mfma_f32_16x16x32_bf16 v[220:223], v[132:135], a[132:135], v[220:223]\n"
    "v_accvgpr_read_b32 v245, a120\n"
    "v_accvgpr_read_b32 v247, a121\n"
    "v_mfma_f32_16x16x32_bf16 v[220:223], a[12:15], a[164:167], v[220:223]\n"
    "v_lshlrev_b32 v244, 16, v245\n"
    "v_lshlrev_b32 v246, 16, v247\n"
    "v_mfma_f32_16x16x32_bf16 v[220:223], v[64:67], a[160:163], v[220:223]\n"
    "v_and_b32 v245, s58, v245\n"
    "v_and_b32 v247, s58, v247\n"
    "v_mfma_f32_16x16x32_bf16 v[220:223], v[108:111], a[128:131], v[220:223]\n"
    "v_mul_f32 v244, v244, v216\n"
    "s_nop 7\n"
    "v_mul_f32 v246, v246, v218\n"
    "v_mul_f32 v245, v245, v217\n"
    "v_mul_f32 v247, v247, v219\n"
    "v_cvt_pk_bf16_f32 v216, v244, v245\n"
    "v_cvt_pk_bf16_f32 v217, v246, v247\n"
    "v_accvgpr_read_b32 v245, a122\n"
    "v_accvgpr_read_b32 v247, a123\n"
    "v_lshlrev_b32 v244, 16, v245\n"
    "v_lshlrev_b32 v246, 16, v247\n"
    "v_and_b32 v245, s58, v245\n"
    "v_and_b32 v247, s58, v247\n"
    "v_mul_f32 v244, v244, v220\n"
    "v_mul_f32 v246, v246, v222\n"
    "v_mul_f32 v245, v245, v221\n"
    "v_mul_f32 v247, v247, v223\n"
    "v_cvt_pk_bf16_f32 v218, v244, v245\n"
    "v_cvt_pk_bf16_f32 v219, v246, v247\n"
    "ds_write_b128 v249, v[216:219] offset:0\n"
    "ds_read_b128 v[212:215], v242 offset:0\n"
    "ds_read_b128 v[220:223], v242 offset:1024\n"
    "s_add_u32 s63, s70, 197120\n"
    "s_waitcnt lgkmcnt(1)\n"
    "buffer_store_dwordx4 v[212:215], v243, s[40:43], s63 offen sc1\n"
    "s_add_u32 s63, s70, 213504\n"
    "s_waitcnt lgkmcnt(0)\n"
    "buffer_store_dwordx4 v[220:223], v243, s[40:43], s63 offen sc1\n"
    "CODE_END_%=:\n"
    :
    : "s"(vb), "s"(wave), "s"(ldsb), "s"(pb), "s"(__builtin_amdgcn_kernarg_segment_ptr())
    : "v0", "v1", "v2", "v3", "v4", "v5", "v6", "v7", "v8", "v9", "v10", "v11", "v12", "v13", "v14", "v15", "v16", "v17", "v18", "v19", "v20", "v21", "v22", "v23", "v24", "v25", "v26", "v27", "v28", "v29", "v30", "v31", "v32", "v33", "v34", "v35", "v36", "v37", "v38", "v39", "v40", "v41", "v42", "v43", "v44", "v45", "v46", "v47", "v48", "v49", "v50", "v51", "v52", "v53", "v54", "v55", "v56", "v57", "v58", "v59", "v60", "v61", "v62", "v63", "v64", "v65", "v66", "v67", "v68", "v69", "v70", "v71", "v72", "v73", "v74", "v75", "v76", "v77", "v78", "v79", "v80", "v81", "v82", "v83", "v84", "v85", "v86", "v87", "v88", "v89", "v90", "v91", "v92", "v93", "v94", "v95", "v96", "v97", "v98", "v99", "v100", "v101", "v102", "v103", "v104", "v105", "v106", "v107", "v108", "v109", "v110", "v111", "v112", "v113", "v114", "v115", "v116", "v117", "v118", "v119", "v120", "v121", "v122", "v123", "v124", "v125", "v126", "v127", "v128", "v129", "v130", "v131", "v132", "v133", "v134", "v135", "v136", "v137", "v138", "v139", "v140", "v141", "v142", "v143", "v144", "v145", "v146", "v147", "v148", "v149", "v150", "v151", "v152", "v153", "v154", "v155", "v156", "v157", "v158", "v159", "v160", "v161", "v162", "v163", "v164", "v165", "v166", "v167", "v168", "v169", "v170", "v171", "v172", "v173", "v174", "v175", "v176", "v177", "v178", "v179", "v180", "v181", "v182", "v183", "v184", "v185", "v186", "v187", "v188", "v189", "v190", "v191", "v192", "v193", "v194", "v195", "v196", "v197", "v198", "v199", "v200", "v201", "v202", "v203", "v204", "v205", "v206", "v207", "v208", "v209", "v210", "v211", "v212", "v213", "v214", "v215", "v216", "v217", "v218", "v219", "v220", "v221", "v222", "v223", "v224", "v225", "v226", "v227", "v228", "v229", "v230", "v231", "v232", "v233", "v234", "v235", "v236", "v237", "v238", "v239", "v240", "v241", "v242", "v243", "v244", "v245", "v246", "v247", "v248", "v249", "v250", "v251", "v252", "v253", "v254", "v255", "a0", "a1", "a2", "a3", "a4", "a5", "a6", "a7", "a8", "a9", "a10", "a11", "a12", "a13", "a14", "a15", "a16", "a17", "a18", "a19", "a20", "a21", "a22", "a23", "a24", "a25", "a26", "a27", "a28", "a29", "a30", "a31", "a32", "a33", "a34", "a35", "a36", "a37", "a38", "a39", "a40", "a41", "a42", "a43", "a44", "a45", "a46", "a47", "a48", "a49", "a50", "a51", "a52", "a53", "a54", "a55", "a56", "a57", "a58", "a59", "a60", "a61", "a62", "a63", "a64", "a65", "a66", "a67", "a68", "a69", "a70", "a71", "a72", "a73", "a74", "a75", "a76", "a77", "a78", "a79", "a80", "a81", "a82", "a83", "a84", "a85", "a86", "a87", "a88", "a89", "a90", "a91", "a92", "a93", "a94", "a95", "a96", "a97", "a98", "a99", "a100", "a101", "a102", "a103", "a104", "a105", "a106", "a107", "a108", "a109", "a110", "a111", "a112", "a113", "a114", "a115", "a116", "a117", "a118", "a119", "a120", "a121", "a122", "a123", "a124", "a125", "a126", "a127", "a128", "a129", "a130", "a131", "a132", "a133", "a134", "a135", "a136", "a137", "a138", "a139", "a140", "a141", "a142", "a143", "a144", "a145", "a146", "a147", "a148", "a149", "a150", "a151", "a152", "a153", "a154", "a155", "a156", "a157", "a158", "a159", "a160", "a161", "a162", "a163", "a164", "a165", "a166", "a167", "a168", "a169", "a170", "a171", "a172", "a173", "a174", "a175", "a176", "a177", "a178", "a179", "a180", "a181", "a182", "a183", "a184", "a185", "a186", "a187", "a188", "a189", "a190", "a191", "a192", "a193", "a194", "a195", "a196", "a197", "a198", "a199", "a200", "a201", "a202", "a203", "a204", "a205", "a206", "a207", "a208", "a209", "a210", "a211", "a212", "a213", "a214", "a215", "a216", "a217", "a218", "a219", "a220", "a221", "a222", "a223", "a224", "a225", "a226", "a227", "a228", "a229", "a230", "a231", "a232", "a233", "a234", "a235", "a236", "a237", "a238", "a239", "a240", "a241", "a242", "a243", "a244", "a245", "a246", "a247", "a248", "a249", "a250", "a251", "a252", "a253", "a254", "a255", "s4", "s5", "s6", "s7", "s8", "s9", "s10", "s11", "s12", "s13", "s14", "s15", "s20", "s21", "s22", "s23", "s24", "s25", "s26", "s27", "s28", "s29", "s30", "s31", "s32", "s33", "s34", "s35", "s36", "s37", "s38", "s39", "s40", "s41", "s42", "s43", "s44", "s45", "s46", "s47", "s48", "s49", "s50", "s51", "s52", "s53", "s54", "s55", "s56", "s57", "s58", "s59", "s60", "s61", "s62", "s63", "s64", "s65", "s66", "s67", "s68", "s69", "s70", "s71", "s72", "s73", "s74", "s75", "s76", "s77", "s78", "s79", "s80", "s81", "s82", "s83", "s84", "s85", "s86", "s87", "s88", "s89", "s90", "s91", "s92", "s93", "s94", "s95", "s96", "s97", "s98", "s99", "vcc", "scc", "memory");
}

extern "C" void kernel_launch(void* const* d_in, const int* in_sizes, int n_in,
                              void* d_out, int out_size, void* d_ws, size_t ws_size,
                              hipStream_t stream) {
  const u16* x0 = (const u16*)d_in[0];
  const u16* ws = (const u16*)d_in[1];
  const u16* bs = (const u16*)d_in[2];
  char* base = (char*)d_ws + 0;
  unsigned* flags = (unsigned*)base;
  u16* wt = (u16*)(base + FLAG_BYTES + ((size_t)226 << 20));
  u16* ex = (u16*)(base + ((size_t)32 << 20));
  dcn_pair<<<256, 256, 0, stream>>>(x0, wt, bs, ex, flags, (u16*)d_out, ws);
}
```

```cpp
#include <hip/hip_runtime.h>
#include <stdint.h>
typedef unsigned short u16;
constexpr size_t WT_BYTES = (size_t)6 * 1024 * 1024 * 2 + 98304;
constexpr size_t FLAG_BYTES = 256 * 1024 + 8192;

__global__ __launch_bounds__(256, 1) void dcn_pair(const u16* __restrict__ x0, u16* __restrict__ wt, const u16* __restrict__ bs,
                                                   u16* __restrict__ ex0, unsigned* __restrict__ flags, u16* __restrict__ out,
                                                   const u16* __restrict__ w) {
  __shared__ __attribute__((aligned(16))) unsigned char lds[163840];
  const int wave = __builtin_amdgcn_readfirstlane(threadIdx.x >> 6);
  const unsigned ldsb = (unsigned)(size_t)lds;
  const unsigned pb = blockIdx.x, pk = 3;
  const unsigned vb = ((pb & ~(8u | (1u << pk))) | (((pb >> pk) & 1u) << 3) | (((pb >> 3) & 1u) << pk)) ^ 0u;
  asm volatile(
    "s_load_dwordx2 s[72:73], %4, 0x30\n"
    "s_load_dwordx2 s[24:25], %4, 0\n"
    "s_load_dwordx8 s[4:11], %4, 0\n"
    "s_load_dwordx4 s[12:15], %4, 0x20\n"
    "s_mov_b32 s65, %1\n"
    "v_mbcnt_lo_u32_b32 v0, -1, 0\n"
    "v_mbcnt_hi_u32_b32 v0, -1, v0\n"
    "v_lshlrev_b32 v240, 4, v0\n"
    "s_getpc_b64 s[48:49]\n"
    "s_and_b32 s49, s49, 0xffff\n"
    "s_mov_b32 s50, CODE_END_%= - . - 64\n"
    "s_mov_b32 s51, 0x20000\n"
    "s_lshr_b32 s60, %3, 3\n"
    "s_lshl_b32 s60, s60, 2\n"
    "s_add_u32 s60, s60, s65\n"
    "s_lshl_b32 s60, s60, 10\n"
    "buffer_load_dwordx4 v[248:251], v240, s[48:51], s60 offen\n"
    "s_bfe_u32 s64, %0, 0x10003\n"
    "s_lshr_b32 s62, %0, 4\n"
    "s_and_b32 s61, %0, 7\n"
    "s_lshl_b32 s60, s62, 3\n"
    "s_add_u32 s60, s60, s61\n"
    "s_lshl_b32 s40, s60, 18\n"
    "s_mov_b32 s26, 262144\n"
    "s_mov_b32 s27, 0x20000\n"
    "s_mov_b32 s74, 12582912\n"
    "s_mov_b32 s75, s27\n"
    "s_lshl_b32 s60, s65, 12\n"
    "s_add_u32 s68, %2, s60\n"
    "s_add_u32 s79, s68, s60\n"
    "s_lshl_b32 s61, s64, 10\n"
    "s_lshl_b32 s60, s65, 7\n"
    "s_add_u32 s70, s61, s60\n"
    "s_lshl_b32 s66, s65, 16\n"
    "s_mov_b32 s42, 0x20a86420\n"
    "s_mov_b32 s43, 0x6420a864\n"
    "s_lshl_b32 s63, s62, 2\n"
    "s_lshr_b64 s[42:43], s[42:43], s63\n"
    "s_and_b32 s41, s42, 15\n"
    "s_lshl_b32 s67, s41, 7\n"
    "s_lshl_b32 s57, s41, 17\n"
    "s_add_u32 s57, s57, s70\n"
    "v_lshrrev_b32 v1, 3, v0\n"
    "v_lshlrev_b32 v1, 11, v1\n"
    "v_lshrrev_b32 v2, 4, v0\n"
    "v_and_b32 v3, 7, v0\n"
    "v_xor_b32 v4, v3, v2\n"
    "v_lshl_add_u32 v255, v4, 4, v1\n"
    "v_xor_b32 v243, 64, v255\n"
    "v_and_b32 v4, 1, v2\n"
    "v_lshlrev_b32 v4, 2, v4\n"
    "v_xor_b32 v4, v4, v3\n"
    "v_lshl_add_u32 v254, v4, 4, v1\n"
    "v_and_b32 v8, 3, v0\n"
    "v_lshlrev_b32 v8, 4, v8\n"
    "v_lshrrev_b32 v9, 2, v0\n"
    "v_and_b32 v9, 3, v9\n"
    "v_lshl_add_u32 v10, v2, 3, v9\n"
    "v_lshrrev_b32 v11, 1, v9\n"
    "v_lshlrev_b32 v11, 6, v11\n"
    "v_lshlrev_b32 v12, 7, v10\n"
    "v_add3_u32 v252, v12, v11, v8\n"
    "v_add_u32 v252, s79, v252\n"
    "v_xor_b32 v253, 64, v252\n"
    "v_and_b32 v5, 15, v0\n"
    "v_lshrrev_b32 v6, 1, v5\n"
    "v_xor_b32 v6, v6, v2\n"
    "v_lshlrev_b32 v5, 7, v5\n"
    "v_lshl_add_u32 v5, v6, 4, v5\n"
    "s_add_u32 s60, %2, 65536\n"
    "v_add_u32 v241, s60, v5\n"
    "v_xor_b32 v242, 64, v241\n"
    "v_mov_b32 v244, 0x3f803f80\n"
    "v_mov_b32 v245, 0x3f80\n"
    "v_mov_b32 v246, 0x3eaa4400\n"
    "v_mov_b32 v247, 0x0\n"
    "s_waitcnt lgkmcnt(0)\n"
    "s_and_b32 s73, s73, 0xffff\n"
    "s_add_u32 m0, s79, 0\n"
    "s_add_u32 s54, s57, 0\n"
    "buffer_load_dwordx4 v254, s[72:75], s54 offen  lds\n"
    "s_addk_i32 m0, 1024\n"
    "s_addk_i32 s54, 16384\n"
    "buffer_load_dwordx4 v254, s[72:75], s54 offen  lds\n"
    "s_addk_i32 m0, 1024\n"
    "s_addk_i32 s54, 16384\n"
    "buffer_load_dwordx4 v254, s[72:75], s54 offen  lds\n"
    "s_addk_i32 m0, 1024\n"
    "s_addk_i32 s54, 16384\n"
    "buffer_load_dwordx4 v254, s[72:75], s54 offen  lds\n"
    "s_add_u32 s24, s24, s40\n"
    "s_addc_u32 s25, s25, 0\n"
    "s_and_b32 s25, s25, 0xffff\n"
    "s_xor_b32 s59, s66, s67\n"
    "s_addk_i32 s67, 128\n"
    "s_cmpk_eq_u32 s67, 1536\n"
    "s_cselect_b32 s67, 0, s67\n"
    "s_add_u32 m0, s68, 65536\n"
    "s_nop 0\n"
    "buffer_load_dwordx4 v255, s[24:27], s59 offen nt lds\n"
    "s_add_u32 m0, s68, 66560\n"
    "s_addk_i32 s59, 16384\n"
    "buffer_load_dwordx4 v243, s[24:27], s59 offen nt lds\n"
    "s_add_u32 m0, s68, 67584\n"
    "s_addk_i32 s59, 16384\n"
    "buffer_load_dwordx4 v255, s[24:27], s59 offen nt lds\n"
    "s_add_u32 m0, s68, 68608\n"
    "s_addk_i32 s59, 16384\n"
    "buffer_load_dwordx4 v243, s[24:27], s59 offen nt lds\n"
    "s_lshl_b32 s84, s64, 3\n"
    "s_add_u32 s84, s84, s65\n"
    "s_add_u32 s85, s84, 4\n"
    "s_sub_u32 s60, s84, s41\n"
    "s_add_u32 s61, s60, 12\n"
    "s_cmp_lt_u32 s84, s41\n"
    "s_cselect_b32 s60, s61, s60\n"
    "s_cmp_lt_u32 s84, 12\n"
    "s_cselect_b32 s84, s60, s84\n"
    "s_sub_u32 s60, s85, s41\n"
    "s_add_u32 s61, s60, 12\n"
    "s_cmp_lt_u32 s85, s41\n"
    "s_cselect_b32 s60, s61, s60\n"
    "s_cmp_lt_u32 s85, 12\n"
    "s_cselect_b32 s85, s60, s85\n"
    "s_cmp_ge_u32 s84, 4\n"
    "s_cselect_b32 s60, s64, 0\n"
    "s_xor_b32 s84, s84, s60\n"
    "s_xor_b32 s85, s85, s64\n"
    "s_lshl_b32 s85, 1, s85\n"
    "s_lshl_b32 s60, 1, s84\n"
    "s_or_b32 s85, s85, s60\n"
    "s_cmp_eq_u32 s65, 0\n"
    "s_cselect_b32 s86, -1, 0\n"
    "s_cmp_eq_u32 %0, 0\n"
    "s_cselect_b32 s87, 4096, 0\n"
    "s_mov_b64 s[62:63], s[12:13]\n"
    "s_add_u32 s62, s62, 262144\n"
    "s_addc_u32 s63, s63, 0\n"
    "s_mov_b32 s80, s62\n"
    "s_and_b32 s81, s63, 0xffff\n"
    "s_mov_b32 s82, 0\n"
    "s_mov_b32 s83, 0x20000\n"
    "s_lshl_b32 s76, %0, 3\n"
    "s_lshl_b32 s77, %0, 2\n"
    "s_lshl_b32 s61, %0, 10\n"
    "s_lshl_b32 s60, s65, 8\n"
    "s_add_u32 s61, s61, s60\n"
    "s_mov_b64 s[62:63], s[12:13]\n"
    "s_add_u32 s62, s62, s61\n"
    "s_addc_u32 s63, s63, 0\n"
    "s_mov_b32 s44, s62\n"
    "s_and_b32 s45, s63, 0xffff\n"
    "s_mov_b32 s46, 256\n"
    "s_mov_b32 s47, 0x20000\n"
    "s_sub_u32 s90, s68, s79\n"
    "s_add_u32 s90, s90, 114688\n"
    "s_mov_b32 s60, s40\n"
    "s_mov_b64 s[62:63], s[14:15]\n"
    "s_add_u32 s62, s62, s60\n"
    "s_addc_u32 s63, s63, 0\n"
    "s_mov_b32 s40, s62\n"
    "s_and_b32 s41, s63, 0xffff\n"
    "s_mov_b32 s42, 262144\n"
    "s_mov_b32 s43, 0x20000\n"
    "s_add_u32 s60, s77, s65\n"
    "s_lshr_b32 s20, s60, 9\n"
    "s_bfe_u32 s21, s60, 0x50004\n"
    "s_and_b32 s22, s60, 15\n"
    "s_lshr_b32 s23, s22, 3\n"
    "s_bfe_u32 s32, s22, 0x10002\n"
    "s_and_b32 s33, s22, 3\n"
    "s_and_b32 s34, s21, 1\n"
    "s_lshl_b32 s34, s34, 1\n"
    "s_bfe_u32 s35, s21, 0x10002\n"
    "s_or_b32 s34, s34, s35\n"
    "s_lshl_b32 s35, s34, 4\n"
    "s_lshl_b32 s36, s32, 3\n"
    "s_add_u32 s35, s35, s36\n"
    "s_bfe_u32 s36, s21, 0x10003\n"
    "s_lshl_b32 s36, s36, 6\n"
    "s_add_u32 s36, s36, s35\n"
    "s_add_u32 s36, s36, 96\n"
    "s_add_u32 s37, s35, 32\n"
    "s_mul_i32 s39, s32, 224\n"
    "s_lshl_b32 s34, s34, 3\n"
    "s_add_u32 s39, s39, s34\n"
    "s_bitcmp1_b32 s21, 3\n"
    "s_cselect_b32 s39, s37, s39\n"
    "s_lshr_b32 s37, s21, 4\n"
    "s_cmp_eq_u32 s37, s23\n"
    "s_cselect_b32 s39, s39, s36\n"
    "s_bfe_u32 s36, s21, 0x10001\n"
    "s_add_u32 s39, s39, s36\n"
    "s_lshr_b32 s34, s21, 1\n"
    "s_xor_b32 s34, s34, s23\n"
    "s_lshl_b32 s35, s34, 3\n"
    "s_add_u32 s36, s35, 96\n"
    "s_sub_u32 s37, 344, s35\n"
    "s_cmp_eq_u32 s32, 0\n"
    "s_cselect_b32 s38, s36, s37\n"
    "s_cmp_lt_u32 s34, 12\n"
    "s_cselect_b32 s71, 1, 0\n"
    "s_andn2_b32 s71, s71, s20\n"
    "s_lshl_b32 s71, s71, 30\n"
    "s_and_b32 s36, s21, 1\n"
    "s_add_u32 s38, s38, s36\n"
    "s_cmp_eq_u32 s20, 0\n"
    "s_cselect_b32 s38, s38, s39\n"
    "s_lshl_b32 s35, s23, 2\n"
    "s_add_u32 s35, s35, s33\n"
    "s_mul_i32 s35, s35, 6\n"
    "s_add_u32 s35, s35, s20\n"
    "s_lshl_b32 s35, s35, 18\n"
    "s_lshl_b32 s38, s38, 10\n"
    "s_add_u32 s89, s35, s38\n"
    "s_or_b32 s89, s89, s71\n"
    "s_lshl_b32 s39, s39, 10\n"
    "s_add_u32 s91, s35, s39\n"
    "s_lshl_b32 s35, s20, 21\n"
    "s_lshl_b32 s36, s21, 16\n"
    "s_add_u32 s35, s35, s36\n"
    "s_lshl_b32 s36, s22, 7\n"
    "s_add_u32 s78, s35, s36\n"
    "s_mov_b64 s[62:63], s[6:7]\n"
    "s_add_u32 s62, s62, 0\n"
    "s_addc_u32 s63, s63, 0\n"
    "s_mov_b32 s28, s62\n"
    "s_and_b32 s29, s63, 0xffff\n"
    "s_mov_b32 s30, 12582912\n"
    "s_mov_b32 s31, 0x20000\n"
    "s_mov_b64 s[62:63], s[8:9]\n"
    "s_add_u32 s62, s62, 0\n"
    "s_addc_u32 s63, s63, 0\n"
    "s_mov_b32 s20, s62\n"
    "s_and_b32 s21, s63, 0xffff\n"
    "s_mov_b32 s22, 12288\n"
    "s_mov_b32 s23, 0x20000\n"
    "s_mov_b64 s[62:63], s[6:7]\n"
    "s_add_u32 s62, s62, 12582912\n"
    "s_addc_u32 s63, s63, 0\n"
    "s_mov_b32 s36, s62\n"
    "s_and_b32 s37, s63, 0xffff\n"
    "s_mov_b32 s38, 98304\n"
    "s_mov_b32 s39, 0x20000\n"
    "s_mov_b64 s[62:63], s[10:11]\n"
    "s_add_u32 s62, s62, 0x1fffff\n"
    "s_addc_u32 s63, s63, 0\n"
    "s_mov_b32 s32, s62\n"
    "s_and_b32 s33, s63, 0xffff\n"
    "s_mov_b32 s34, 201326592\n"
    "s_mov_b32 s35, 0x20000\n"
    "s_and_b32 s32, s32, 0xffe00000\n"
    "s_mov_b32 s58, 0xffff0000\n"
    "s_mov_b32 s52, 0\n"
    "s_mov_b32 s53, 196608\n"
    "s_xor_b32 s60, %0, 8\n"
    "s_lshl_b32 s61, s60, 10\n"
    "s_mov_b64 s[62:63], s[12:13]\n"
    "s_add_u32 s62, s62, s61\n"
    "s_addc_u32 s63, s63, 0\n"
    "s_mov_b32 s48, s62\n"
    "s_and_b32 s49, s63, 0xffff\n"
    "s_mov_b32 s50, 64\n"
    "s_mov_b32 s51, 0x20000\n"
    "s_add_u32 m0, s79, 4096\n"
    "s_addk_i32 s54, 16384\n"
    "buffer_load_dwordx4 v254, s[72:75], s54 offen  lds\n"
    "s_addk_i32 m0, 1024\n"
    "s_addk_i32 s54, 16384\n"
    "buffer_load_dwordx4 v254, s[72:75], s54 offen  lds\n"
    "s_addk_i32 m0, 1024\n"
    "s_addk_i32 s54, 16384\n"
    "buffer_load_dwordx4 v254, s[72:75], s54 offen  lds\n"
    "s_addk_i32 m0, 1024\n"
    "s_addk_i32 s54, 16384\n"
    "buffer_load_dwordx4 v254, s[72:75], s54 offen  lds\n"
    "v_mfma_f32_16x16x4_4b_bf16 v[0:15], v[244:245], v[246:247], 0\n"
    "v_mfma_f32_16x16x4_4b_bf16 v[32:47], v[244:245], v[246:247], 0\n"
    "s_add_u32 m0, s79, 32768\n"
    "s_add_u32 s54, s57, 512\n"
    "buffer_load_dwordx4 v254, s[72:75], s54 offen  lds\n"
    "v_mfma_f32_16x16x4_4b_bf16 v[64:79], v[244:245], v[246:247], 0\n"
    "v_mfma_f32_16x16x4_4b_bf16 v[96:111], v[244:245], v[246:247], 0\n"
    "s_addk_i32 m0, 1024\n"
    "s_addk_i32 s54, 16384\n"
    "buffer_load_dwordx4 v254, s[72:75], s54 offen  lds\n"
    "v_mfma_f32_16x16x4_4b_bf16 v[128:143], v[244:245], v[246:247], 0\n"
    "v_mfma_f32_16x16x4_4b_bf16 v[160:175], v[244:245], v[246:247], 0\n"
    "s_addk_i32 m0, 1024\n"
    "s_addk_i32 s54, 16384\n"
    "buffer_load_dwordx4 v254, s[72:75], s54 offen  lds\n"
    "v_mfma_f32_16x16x4_4b_bf16 v[192:207], v[244:245], v[246:247], 0\n"
    "v_mfma_f32_16x16x4_4b_bf16 v[224:239], v[244:245], v[246:247], 0\n"
    "s_addk_i32 m0, 1024\n"
    "s_addk_i32 s54, 16384\n"
    "buffer_load_dwordx4 v254, s[72:75], s54 offen  lds\n"
    "v_mfma_f32_16x16x4_4b_bf16 v[16:31], v[244:245], v[246:247], 0\n"
    "v_mfma_f32_16x16x4_4b_bf16 v[48:63], v[244:245], v[246:247], 0\n"
    "s_addk_i32 m0, 1024\n"
    "s_addk_i32 s54, 16384\n"
    "buffer_load_dwordx4 v254, s[72:75], s54 offen  lds\n"
    "v_mfma_f32_16x16x4_4b_bf16 v[80:95], v[244:245], v[246:247], 0\n"
    "v_mfma_f32_16x16x4_4b_bf16 v[112:127], v[244:245], v[246:247], 0\n"
    "s_addk_i32 m0, 1024\n"
    "s_addk_i32 s54, 16384\n"
    "buffer_load_dwordx4 v254, s[72:75], s54 offen  lds\n"
    "v_mfma_f32_16x16x4_4b_bf16 v[144:159], v[244:245], v[246:247], 0\n"
    "v_mfma_f32_16x16x4_4b_bf16 v[176:191], v[244:245], v[246:247], 0\n"
    "s_addk_i32 m0, 1024\n"
    "s_addk_i32 s54, 16384\n"
    "buffer_load_dwordx4 v254, s[72:75], s54 offen  lds\n"
    "v_mfma_f32_16x16x4_4b_bf16 v[208:223], v[244:245], v[246:247], 0\n"
    "v_mfma_f32_16x16x4_4b_bf16 a[240:255], v[244:245], v[246:247], 0\n"
    "s_addk_i32 m0, 1024\n"
    "s_addk_i32 s54, 16384\n"
    "buffer_load_dwordx4 v254, s[72:75], s54 offen  lds\n"
    "s_add_u32 s57, s57, 131072\n"
    "s_cmp_ge_u32 s57, 1572864\n"
    "s_cselect_b32 s57, s70, s57\n"
    "s_waitcnt vmcnt(16)\n"
    "ds_read_b64_tr_b16 a[224:225], v252 offset:0\n"
    "ds_read_b64_tr_b16 a[226:227], v252 offset:512\n"
    "ds_read_b64_tr_b16 a[228:229], v252 offset:8\n"
    "ds_read_b64_tr_b16 a[230:231], v252 offset:520\n"
    "ds_read_b64_tr_b16 a[232:233], v253 offset:0\n"
    "ds_read_b64_tr_b16 a[234:235], v253 offset:512\n"
    "ds_read_b64_tr_b16 a[236:237], v253 offset:8\n"
    "ds_read_b64_tr_b16 a[238:239], v253 offset:520\n"
    "s_waitcnt vmcnt(12)\n"
    "s_barrier\n"
    "ds_read_b128 a[128:131], v241 offset:0\n"
    "s_waitcnt lgkmcnt(0)\n"
    "v_mfma_f32_16x16x32_bf16 v[0:3], a[224:227], a[128:131], v[0:3]\n"
    "ds_read_b128 a[132:135], v241 offset:2048\n"
    "s_waitcnt lgkmcnt(0)\n"
    "v_mfma_f32_16x16x32_bf16 v[32:35], a[224:227], a[132:135], v[32:35]\n"
    "ds_read_b128 a[136:139], v241 offset:4096\n"
    "s_waitcnt lgkmcnt(0)\n"
    "v_mfma_f32_16x16x32_bf16 v[64:67], a[224:227], a[136:139], v[64:67]\n"
    "ds_read_b128 a[140:143], v241 offset:6144\n"
    "s_waitcnt lgkmcnt(0)\n"
    "ds_read_b128 a[144:147], v241 offset:8192\n"
    "ds_read_b128 a[148:151], v241 offset:10240\n"
    "ds_read_b128 a[152:155], v241 offset:12288\n"
    "ds_read_b128 a[156:159], v241 offset:14336\n"
    "v_mfma_f32_16x16x32_bf16 v[96:99], a[224:227], a[140:143], v[96:99]\n"
    "s_waitcnt lgkmcnt(3)\n"
    "v_mfma_f32_16x16x32_bf16 v[128:131], a[224:227], a[144:147], v[128:131]\n"
    "s_waitcnt lgkmcnt(2)\n"
    "v_mfma_f32_16x16x32_bf16 v[160:163], a[224:227], a[148:151], v[160:163]\n"
    "s_waitcnt lgkmcnt(1)\n"
    "v_mfma_f32_16x16x32_bf16 v[192:195], a[224:227], a[152:155], v[192:195]\n"
    "s_waitcnt lgkmcnt(0)\n"
    "ds_read_b128 a[160:163], v242 offset:0\n"
    "v_mfma_f32_16x16x32_bf16 v[224:227], a[224:227], a[156:159], v[224:227]\n"
    "ds_read_b128 a[164:167], v242 offset:2048\n"
    "v_mfma_f32_16x16x32_bf16 v[228:231], a[228:231], a[156:159], v[228:231]\n"
    "ds_read_b128 a[168:171], v242 offset:4096\n"
    "v_mfma_f32_16x16x32_bf16 v[196:199], a[228:231], a[152:155], v[196:199]\n"
    "ds_read_b128 a[172:175], v242 offset:6144\n"
    "v_mfma_f32_16x16x32_bf16 v[164:167], a[228:231], a[148:151], v[164:167]\n"
    "ds_read_b128 a[176:179], v242 offset:8192\n"
    "v_mfma_f32_16x16x32_bf16 v[132:135], a[228:231], a[144:147], v[132:135]\n"
    "ds_read_b128 a[180:183], v242 offset:10240\n"
    "v_mfma_f32_16x16x32_bf16 v[100:103], a[228:231], a[140:143], v[100:103]\n"
    "ds_read_b128 a[184:187], v242 offset:12288\n"
    "v_mfma_f32_16x16x32_bf16 v[68:71], a[228:231], a[136:139], v[68:71]\n"
    "ds_read_b128 a[188:191], v242 offset:14336\n"
    "s_add_u32 m0, s79, 131072\n"
    "v_mfma_f32_16x16x32_bf16 v[36:39], a[228:231], a[132:135], v[36:39]\n"
    "s_add_u32 s54, s57, 0\n"
    "v_mfma_f32_16x16x32_bf16 v[4:7], a[228:231], a[128:131], v[4:7]\n"
    "buffer_load_dwordx4 v254, s[72:75], s54 offen  lds\n"
    "v_mfma_f32_16x16x32_bf16 v[8:11], a[232:235], a[128:131], v[8:11]\n"
    "s_addk_i32 m0, 1024\n"
    "s_addk_i32 s54, 16384\n"
    "v_mfma_f32_16x16x32_bf16 v[40:43], a[232:235], a[132:135], v[40:43]\n"
    "buffer_load_dwordx4 v254, s[72:75], s54 offen  lds\n"
    "v_mfma_f32_16x16x32_bf16 v[72:75], a[232:235], a[136:139], v[72:75]\n"
    "s_addk_i32 m0, 1024\n"
    "s_addk_i32 s54, 16384\n"
    "v_mfma_f32_16x16x32_bf16 v[104:107], a[232:235], a[140:143], v[104:107]\n"
    "buffer_load_dwordx4 v254, s[72:75], s54 offen  lds\n"
    "v_mfma_f32_16x16x32_bf16 v[136:139], a[232:235], a[144:147], v[136:139]\n"
    "s_addk_i32 m0, 1024\n"
    "s_addk_i32 s54, 16384\n"
    "v_mfma_f32_16x16x32_bf16 v[168:171], a[232:235], a[148:151], v[168:171]\n"
    "buffer_load_dwordx4 v254, s[72:75], s54 offen  lds\n"
    "v_mfma_f32_16x16x32_bf16 v[200:203], a[232:235], a[152:155], v[200:203]\n"
    "s_addk_i32 m0, 1024\n"
    "s_addk_i32 s54, 16384\n"
    "v_mfma_f32_16x16x32_bf16 v[232:235], a[232:235], a[156:159], v[232:235]\n"
    "buffer_load_dwordx4 v254, s[72:75], s54 offen  lds\n"
    "v_mfma_f32_16x16x32_bf16 v[236:239], a[236:239], a[156:159], v[236:239]\n"
    "s_waitcnt vmcnt(13)\n"
    "ds_read_b64_tr_b16 a[192:193], v252 offset:4096\n"
    "ds_read_b64_tr_b16 a[194:195], v252 offset:4608\n"
    "s_addk_i32 m0, 1024\n"
    "s_addk_i32 s54, 16384\n"
    "buffer_load_dwordx4 v254, s[72:75], s54 offen  lds\n"
    "ds_read_b64_tr_b16 a[196:197], v252 offset:4104\n"
    "ds_read_b64_tr_b16 a[198:199], v252 offset:4616\n"
    "s_bitcmp1_b32 s85, 0\n"
    "s_cbranch_scc1 CP0_%=\n"
    "CR0_%=:\n"
    "v_mfma_f32_16x16x32_bf16 v[204:207], a[236:239], a[152:155], v[204:207]\n"
    "s_addk_i32 m0, 1024\n"
    "s_addk_i32 s54, 16384\n"
    "v_mfma_f32_16x16x32_bf16 v[172:175], a[236:239], a[148:151], v[172:175]\n"
    "buffer_load_dwordx4 v254, s[72:75], s54 offen  lds\n"
    "v_mfma_f32_16x16x32_bf16 v[140:143], a[236:239], a[144:147], v[140:143]\n"
    "ds_read_b64_tr_b16 a[200:201], v253 offset:4096\n"
    "ds_read_b64_tr_b16 a[202:203], v253 offset:4608\n"
    "v_mfma_f32_16x16x32_bf16 v[108:111], a[236:239], a[140:143], v[108:111]\n"
    "s_addk_i32 m0, 1024\n"
    "s_addk_i32 s54, 16384\n"
    "v_mfma_f32_16x16x32_bf16 v[76:79], a[236:239], a[136:139], v[76:79]\n"
    "buffer_load_dwordx4 v254, s[72:75], s54 offen  lds\n"
    "v_mfma_f32_16x16x32_bf16 v[44:47], a[236:239], a[132:135], v[44:47]\n"
    "ds_read_b64_tr_b16 a[204:205], v253 offset:4104\n"
    "ds_read_b64_tr_b16 a[206:207], v253 offset:4616\n"
    "v_mfma_f32_16x16x32_bf16 v[12:15], a[236:239], a[128:131], v[12:15]\n"
    "s_waitcnt lgkmcnt(6)\n"
    "s_xor_b32 s59, s66, s67\n"
    "s_addk_i32 s67, 128\n"
    "v_mfma_f32_16x16x32_bf16 v[0:3], a[192:195], a[160:163], v[0:3]\n"
    "s_cmpk_eq_u32 s67, 1536\n"
    "s_cselect_b32 s67, 0, s67\n"
    "s_add_u32 m0, s68, 81920\n"
    "v_mfma_f32_16x16x32_bf16 v[32:35], a[192:195], a[164:167], v[32:35]\n"
    "s_nop 0\n"
    "v_mfma_f32_16x16x32_bf16 v[64:67], a[192:195], a[168:171], v[64:67]\n"
    "buffer_load_dwordx4 v255, s[24:27], s59 offen nt lds\n"
    "v_mfma_f32_16x16x32_bf16 v[96:99], a[192:195], a[172:175], v[96:99]\n"
    "s_add_u32 m0, s68, 82944\n"
    "s_addk_i32 s59, 16384\n"
    "v_mfma_f32_16x16x32_bf16 v[128:131], a[192:195], a[176:179], v[128:131]\n"
    "buffer_load_dwordx4 v243, s[24:27], s59 offen nt lds\n"
    "s_add_u32 s82, s77, 3076\n"
    "s_and_b32 s82, s82, s86\n"
    "v_mfma_f32_16x16x32_bf16 v[160:163], a[192:195], a[180:183], v[160:163]\n"
    "buffer_load_dword v245, v240, s[80:83], s77 offen offset:3072 sc1\n"
    "v_lshrrev_b32 v246, 1, v240\n"
    "s_lshl_b32 s61, s65, 9\n"
    "s_mov_b32 s82, s87\n"
    "v_mfma_f32_16x16x32_bf16 v[192:195], a[192:195], a[184:187], v[192:195]\n"
    "buffer_load_dwordx2 v[246:247], v246, s[80:83], s61 offen sc1\n"
    "v_mov_b32 v248, 0\n"
    "v_mov_b32 v249, 0\n"
    "v_mov_b32 v250, 0\n"
    "v_mov_b32 v251, 0\n"
    "v_mfma_f32_16x16x32_bf16 v[224:227], a[192:195], a[188:191], v[224:227]\n"
    "s_waitcnt lgkmcnt(4)\n"
    "v_mfma_f32_16x16x32_bf16 v[228:231], a[196:199], a[188:191], v[228:231]\n"
    "buffer_store_dwordx4 v[248:251], v240, s[44:47], 0 offen sc1\n"
    "s_lshl_b32 s60, s65, 8\n"
    "s_sub_u32 s44, s44, s60\n"
    "s_subb_u32 s45, s45, 0\n"
    "s_lshl_b32 s60, s65, 4\n"
    "s_add_u32 s44, s44, s60\n"
    "s_addc_u32 s45, s45, 0\n"
    "s_mov_b32 s46, 4\n"
    "s_add_u32 m0, s68, 83968\n"
    "s_addk_i32 s59, 16384\n"
    "v_mfma_f32_16x16x32_bf16 v[196:199], a[196:199], a[184:187], v[196:199]\n"
    "buffer_load_dwordx4 v255, s[24:27], s59 offen nt lds\n"
    "s_add_u32 m0, s68, 84992\n"
    "s_addk_i32 s59, 16384\n"
    "v_mfma_f32_16x16x32_bf16 v[164:167], a[196:199], a[180:183], v[164:167]\n"
    "buffer_load_dwordx4 v243, s[24:27], s59 offen nt lds\n"
    "v_mfma_f32_16x16x32_bf16 v[132:135], a[196:199], a[176:179], v[132:135]\n"
    "v_mfma_f32_16x16x32_bf16 v[100:103], a[196:199], a[172:175], v[100:103]\n"
    "v_mfma_f32_16x16x32_bf16 v[68:71], a[196:199], a[168:171], v[68:71]\n"
    "v_mfma_f32_16x16x32_bf16 v[36:39], a[196:199], a[164:167], v[36:39]\n"
    "v_mfma_f32_16x16x32_bf16 v[4:7], a[196:199], a[160:163], v[4:7]\n"
    "s_waitcnt lgkmcnt(2)\n"
    "s_waitcnt vmcnt(15)\n"
    "ds_read_b64_tr_b16 a[208:209], v252 offset:32768\n"
    "v_mfma_f32_16x16x32_bf16 v[8:11], a[200:203], a[160:163], v[8:11]\n"
    "ds_read_b64_tr_b16 a[210:211], v252 offset:33280\n"
    "s_add_u32 s60, s77, 4096\n"
    "v_mfma_f32_16x16x32_bf16 v[40:43], a[200:203], a[164:167], v[40:43]\n"
    "s_add_u32 s82, s60, 4\n"
    "s_and_b32 s82, s82, s86\n"
    "v_mfma_f32_16x16x32_bf16 v[72:75], a[200:203], a[168:171], v[72:75]\n"
    "buffer_store_dword v248, v240, s[80:83], s60 offen sc1\n"
    "v_mfma_f32_16x16x32_bf16 v[104:107], a[200:203], a[172:175], v[104:107]\n"
    "ds_read_b64_tr_b16 a[212:213], v252 offset:36864\n"
    "ds_read_b64_tr_b16 a[214:215], v252 offset:37376\n"
    "v_mfma_f32_16x16x32_bf16 v[136:139], a[200:203], a[176:179], v[136:139]\n"
    "ds_read_b64_tr_b16 a[216:217], v252 offset:32776\n"
    "ds_read_b64_tr_b16 a[218:219], v252 offset:33288\n"
    "v_mfma_f32_16x16x32_bf16 v[168:171], a[200:203], a[180:183], v[168:171]\n"
    "ds_read_b64_tr_b16 a[220:221], v252 offset:36872\n"
    "ds_read_b64_tr_b16 a[222:223], v252 offset:37384\n"
    "v_mfma_f32_16x16x32_bf16 v[200:203], a[200:203], a[184:187], v[200:203]\n"
    "v_mfma_f32_16x16x32_bf16 v[232:235], a[200:203], a[188:191], v[232:235]\n"
    "s_waitcnt lgkmcnt(8)\n"
    "ds_read_b64_tr_b16 a[224:225], v253 offset:32768\n"
    "ds_read_b64_tr_b16 a[226:227], v253 offset:33280\n"
    "v_mfma_f32_16x16x32_bf16 v[236:239], a[204:207], a[188:191], v[236:239]\n"
    "v_mfma_f32_16x16x32_bf16 v[204:207], a[204:207], a[184:187], v[204:207]\n"
    "ds_read_b64_tr_b16 a[228:229], v253 offset:36864\n"
    "ds_read_b64_tr_b16 a[230:231], v253 offset:37376\n"
    "v_mfma_f32_16x16x32_bf16 v[172:175], a[204:207], a[180:183], v[172:175]\n"
    "v_mfma_f32_16x16x32_bf16 v[140:143], a[204:207], a[176:179], v[140:143]\n"
    "ds_read_b64_tr_b16 a[232:233], v253 offset:32776\n"
    "ds_read_b64_tr_b16 a[234:235], v253 offset:33288\n"
    "v_mfma_f32_16x16x32_bf16 v[108:111], a[204:207], a[172:175], v[108:111]\n"
    "v_mfma_f32_16x16x32_bf16 v[76:79], a[204:207], a[168:171], v[76:79]\n"
    "ds_read_b64_tr_b16 a[236:237], v253 offset:36872\n"
    "ds_read_b64_tr_b16 a[238:239], v253 offset:37384\n"
    "v_mfma_f32_16x16x32_bf16 v[44:47], a[204:207], a[164:167], v[44:47]\n"
    "v_mfma_f32_16x16x32_bf16 v[12:15], a[204:207], a[160:163], v[12:15]\n"
    "s_waitcnt lgkmcnt(12)\n"
    "v_lshrrev_b32 v250, 3, v240\n"
    "v_and_b32 v250, 14, v250\n"
    "v_mfma_f32_16x16x32_bf16 v[16:19], a[208:211], a[128:131], v[16:19]\n"
    "v_lshrrev_b32 v248, 4, v240\n"
    "v_and_b32 v248, 0x30, v248\n"
    "v_mfma_f32_16x16x32_bf16 v[16:19], a[212:215], a[160:163], v[16:19]\n"
    "v_or_b32 v250, v248, v250\n"
    "v_lshrrev_b32 v248, 1, v240\n"
    "v_mfma_f32_16x16x32_bf16 v[48:51], a[212:215], a[164:167], v[48:51]\n"
    "v_and_b32 v248, 64, v248\n"
    "v_or_b32 v250, v248, v250\n"
    "v_mfma_f32_16x16x32_bf16 v[48:51], a[208:211], a[132:135], v[48:51]\n"
    "s_add_u32 s60, s77, s65\n"
    "s_lshr_b32 s61, s60, 4\n"
    "s_min_u32 s61, s61, 5\n"
    "s_lshl_b32 s61, s61, 11\n"
    "s_add_u32 s22, s61, 2048\n"
    "s_bfe_u32 s61, s60, 0x10003\n"
    "s_lshl_b32 s61, s61, 10\n"
    "s_and_b32 s62, s60, 1\n"
    "s_lshl_b32 s62, s62, 9\n"
    "s_add_u32 s61, s61, s62\n"
    "s_bfe_u32 s62, s60, 0x20001\n"
    "s_lshl_b32 s62, s62, 7\n"
    "s_add_u32 s61, s61, s62\n"
    "v_add_u32 v250, s61, v250\n"
    "v_mfma_f32_16x16x32_bf16 v[80:83], a[208:211], a[136:139], v[80:83]\n"
    "buffer_load_ushort v248, v250, s[20:23], 0 offen\n"
    "v_mfma_f32_16x16x32_bf16 v[80:83], a[212:215], a[168:171], v[80:83]\n"
    "buffer_load_ushort v249, v250, s[20:23], 0 offen offset:2048\n"
    "v_add_u32 v250, 0x1000, v250\n"
    "v_mfma_f32_16x16x32_bf16 v[112:115], a[212:215], a[172:175], v[112:115]\n"
    "buffer_load_ushort v251, v250, s[20:23], 0 offen\n"
    "v_mfma_f32_16x16x32_bf16 v[112:115], a[208:211], a[140:143], v[112:115]\n"
    "buffer_load_ushort v244, v250, s[20:23], 0 offen offset:2048\n"
    "v_add_u32 v250, 0x1000, v250\n"
    "v_mfma_f32_16x16x32_bf16 v[144:147], a[208:211], a[144:147], v[144:147]\n"
    "v_mfma_f32_16x16x32_bf16 v[144:147], a[212:215], a[176:179], v[144:147]\n"
    "v_mfma_f32_16x16x32_bf16 v[176:179], a[212:215], a[180:183], v[176:179]\n"
    "v_mfma_f32_16x16x32_bf16 v[176:179], a[208:211], a[148:151], v[176:179]\n"
    "v_mfma_f32_16x16x32_bf16 v[208:211], a[208:211], a[152:155], v[208:211]\n"
    "v_mfma_f32_16x16x32_bf16 v[208:211], a[212:215], a[184:187], v[208:211]\n"
    "v_mfma_f32_16x16x32_bf16 a[240:243], a[212:215], a[188:191], a[240:243]\n"
    "v_mfma_f32_16x16x32_bf16 a[240:243], a[208:211], a[156:159], a[240:243]\n"
    "s_waitcnt lgkmcnt(8)\n"
    "v_mfma_f32_16x16x32_bf16 a[244:247], a[216:219], a[156:159], a[244:247]\n"
    "v_mfma_f32_16x16x32_bf16 a[244:247], a[220:223], a[188:191], a[244:247]\n"
    "v_mfma_f32_16x16x32_bf16 v[212:215], a[220:223], a[184:187], v[212:215]\n"
    "v_mfma_f32_16x16x32_bf16 v[212:215], a[216:219], a[152:155], v[212:215]\n"
    "v_mfma_f32_16x16x32_bf16 v[180:183], a[216:219], a[148:151], v[180:183]\n"
    "v_mfma_f32_16x16x32_bf16 v[180:183], a[220:223], a[180:183], v[180:183]\n"
    "v_mfma_f32_16x16x32_bf16 v[148:151], a[220:223], a[176:179], v[148:151]\n"
    "v_mfma_f32_16x16x32_bf16 v[148:151], a[216:219], a[144:147], v[148:151]\n"
    "s_xor_b32 s59, s66, s67\n"
    "s_addk_i32 s67, 128\n"
    "s_cmpk_eq_u32 s67, 1536\n"
    "v_mfma_f32_16x16x32_bf16 v[116:119], a[216:219], a[140:143], v[116:119]\n"
    "s_cselect_b32 s67, 0, s67\n"
    "s_add_u32 m0, s68, 98304\n"
    "s_nop 0\n"
    "v_mfma_f32_16x16x32_bf16 v[116:119], a[220:223], a[172:175], v[116:119]\n"
    "buffer_load_dwordx4 v255, s[24:27], s59 offen  lds\n"
    "v_mfma_f32_16x16x32_bf16 v[84:87], a[220:223], a[168:171], v[84:87]\n"
    "v_mfma_f32_16x16x32_bf16 v[84:87], a[216:219], a[136:139], v[84:87]\n"
    "s_add_u32 m0, s68, 99328\n"
    "s_addk_i32 s59, 16384\n"
    "v_mfma_f32_16x16x32_bf16 v[52:55], a[216:219], a[132:135], v[52:55]\n"
    "buffer_load_dwordx4 v243, s[24:27], s59 offen  lds\n"
    "v_mfma_f32_16x16x32_bf16 v[52:55], a[220:223], a[164:167], v[52:55]\n"
    "v_mfma_f32_16x16x32_bf16 v[20:23], a[220:223], a[160:163], v[20:23]\n"
    "v_mfma_f32_16x16x32_bf16 v[20:23], a[216:219], a[128:131], v[20:23]\n"
    "s_waitcnt lgkmcnt(4)\n"
    "v_add_u32 v252, 0x20000, v252\n"
    "v_add_u32 v253, 0x20000, v253\n"
    "v_mfma_f32_16x16x32_bf16 v[24:27], a[224:227], a[128:131], v[24:27]\n"
    "s_waitcnt vmcnt(14)\n"
    "ds_read_b64_tr_b16 a[192:193], v252 offset:0\n"
    "ds_read_b64_tr_b16 a[194:195], v252 offset:512\n"
    "v_mfma_f32_16x16x32_bf16 v[24:27], a[228:231], a[160:163], v[24:27]\n"
    "s_add_u32 m0, s68, 100352\n"
    "s_addk_i32 s59, 16384\n"
    "v_mfma_f32_16x16x32_bf16 v[56:59], a[228:231], a[164:167], v[56:59]\n"
    "buffer_load_dwordx4 v255, s[24:27], s59 offen  lds\n"
    "v_mfma_f32_16x16x32_bf16 v[56:59], a[224:227], a[132:135], v[56:59]\n"
    "ds_read_b64_tr_b16 a[196:197], v252 offset:4096\n"
    "ds_read_b64_tr_b16 a[198:199], v252 offset:4608\n"
    "v_mfma_f32_16x16x32_bf16 v[88:91], a[224:227], a[136:139], v[88:91]\n"
    "ds_read_b64_tr_b16 a[200:201], v252 offset:8\n"
    "ds_read_b64_tr_b16 a[202:203], v252 offset:520\n"
    "v_mfma_f32_16x16x32_bf16 v[88:91], a[228:231], a[168:171], v[88:91]\n"
    "s_add_u32 m0, s68, 101376\n"
    "s_addk_i32 s59, 16384\n"
    "v_mfma_f32_16x16x32_bf16 v[120:123], a[228:231], a[172:175], v[120:123]\n"
    "buffer_load_dwordx4 v243, s[24:27], s59 offen  lds\n"
    "v_mfma_f32_16x16x32_bf16 v[120:123], a[224:227], a[140:143], v[120:123]\n"
    "ds_read_b64_tr_b16 a[204:205], v252 offset:4104\n"
    "ds_read_b64_tr_b16 a[206:207], v252 offset:4616\n"
    "v_mfma_f32_16x16x32_bf16 v[152:155], a[224:227], a[144:147], v[152:155]\n"
    "ds_read_b64_tr_b16 a[208:209], v253 offset:0\n"
    "ds_read_b64_tr_b16 a[210:211], v253 offset:512\n"
    "v_mfma_f32_16x16x32_bf16 v[152:155], a[228:231], a[176:179], v[152:155]\n"
    "s_waitcnt lgkmcnt(2)\n"
    "s_add_u32 m0, s79, 0\n"
    "s_add_u32 s54, s57, 512\n"
    "v_mfma_f32_16x16x32_bf16 v[184:187], a[228:231], a[180:183], v[184:187]\n"
    "buffer_load_dwordx4 v254, s[72:75], s54 offen  lds\n"
    "v_mfma_f32_16x16x32_bf16 v[184:187], a[224:227], a[148:151], v[184:187]\n"
    "ds_read_b64_tr_b16 a[212:213], v253 offset:4096\n"
    "ds_read_b64_tr_b16 a[214:215], v253 offset:4608\n"
    "v_mfma_f32_16x16x32_bf16 v[216:219], a[224:227], a[152:155], v[216:219]\n"
    "ds_read_b64_tr_b16 a[216:217], v253 offset:8\n"
    "ds_read_b64_tr_b16 a[218:219], v253 offset:520\n"
    "v_mfma_f32_16x16x32_bf16 v[216:219], a[228:231], a[184:187], v[216:219]\n"
    "s_addk_i32 m0, 1024\n"
    "s_addk_i32 s54, 16384\n"
    "buffer_load_dwordx4 v254, s[72:75], s54 offen  lds\n"
    "s_waitcnt vmcnt(11)\n"
    "s_barrier\n"
    "v_mfma_f32_16x16x32_bf16 a[248:251], a[228:231], a[188:191], a[248:251]\n"
    "ds_read_b64_tr_b16 a[220:221], v253 offset:4104\n"
    "ds_read_b64_tr_b16 a[222:223], v253 offset:4616\n"
    "v_mfma_f32_16x16x32_bf16 a[248:251], a[224:227], a[156:159], a[248:251]\n"
    "s_addk_i32 m0, 1024\n"
    "s_addk_i32 s54, 16384\n"
    "v_mfma_f32_16x16x32_bf16 a[252:255], a[232:235], a[156:159], a[252:255]\n"
    "buffer_load_dwordx4 v254, s[72:75], s54 offen  lds\n"
    "v_mfma_f32_16x16x32_bf16 a[252:255], a[236:239], a[188:191], a[252:255]\n"
    "v_mfma_f32_16x16x32_bf16 v[220:223], a[236:239], a[184:187], v[220:223]\n"
    "ds_read_b128 a[156:159], v241 offset:30720\n"
    "v_mfma_f32_16x16x32_bf16 v[220:223], a[232:235], a[152:155], v[220:223]\n"
    "s_addk_i32 m0, 1024\n"
    "s_addk_i32 s54, 16384\n"
    "v_mfma_f32_16x16x32_bf16 v[188:191], a[232:235], a[148:151], v[188:191]\n"
    "buffer_load_dwordx4 v254, s[72:75], s54 offen  lds\n"
    "v_mfma_f32_16x16x32_bf16 v[188:191], a[236:239], a[180:183], v[188:191]\n"
    "ds_read_b128 a[188:191], v242 offset:30720\n"
    "ds_read_b128 a[184:187], v242 offset:28672\n"
    "v_mfma_f32_16x16x32_bf16 v[156:159], a[236:239], a[176:179], v[156:159]\n"
    "ds_read_b128 a[152:155], v241 offset:28672\n"
    "ds_read_b128 a[148:151], v241 offset:26624\n"
    "v_mfma_f32_16x16x32_bf16 v[156:159], a[232:235], a[144:147], v[156:159]\n"
    "s_addk_i32 m0, 1024\n"
    "s_addk_i32 s54, 16384\n"
    "v_mfma_f32_16x16x32_bf16 v[124:127], a[232:235], a[140:143], v[124:127]\n"
    "buffer_load_dwordx4 v254, s[72:75], s54 offen  lds\n"
    "v_mfma_f32_16x16x32_bf16 v[124:127], a[236:239], a[172:175], v[124:127]\n"
    "ds_read_b128 a[180:183], v242 offset:26624\n"
    "ds_read_b128 a[176:179], v242 offset:24576\n"
    "v_mfma_f32_16x16x32_bf16 v[92:95], a[236:239], a[168:171], v[92:95]\n"
    "ds_read_b128 a[144:147], v241 offset:24576\n"
    "ds_read_b128 a[140:143], v241 offset:22528\n"
    "v_mfma_f32_16x16x32_bf16 v[92:95], a[232:235], a[136:139], v[92:95]\n"
    "s_addk_i32 m0, 1024\n"
    "s_addk_i32 s54, 16384\n"
    "v_mfma_f32_16x16x32_bf16 v[60:63], a[232:235], a[132:135], v[60:63]\n"
    "buffer_load_dwordx4 v254, s[72:75], s54 offen  lds\n"
    "v_mfma_f32_16x16x32_bf16 v[60:63], a[236:239], a[164:167], v[60:63]\n"
    "ds_read_b128 a[172:175], v242 offset:22528\n"
    "ds_read_b128 a[168:171], v242 offset:20480\n"
    "v_mfma_f32_16x16x32_bf16 v[28:31], a[236:239], a[160:163], v[28:31]\n"
    "ds_read_b128 a[136:139], v241 offset:20480\n"
    "ds_read_b128 a[132:135], v241 offset:18432\n"
    "v_mfma_f32_16x16x32_bf16 v[28:31], a[232:235], a[128:131], v[28:31]\n"
    "s_waitcnt lgkmcnt(11)\n"
    "s_addk_i32 m0, 1024\n"
    "s_addk_i32 s54, 16384\n"
    "v_mfma_f32_16x16x32_bf16 v[224:227], a[192:195], a[156:159], v[224:227]\n"
    "buffer_load_dwordx4 v254, s[72:75], s54 offen  lds\n"
    "v_mfma_f32_16x16x32_bf16 v[224:227], a[196:199], a[188:191], v[224:227]\n"
    "s_waitcnt lgkmcnt(9)\n"
    "ds_read_b128 a[164:167], v242 offset:18432\n"
    "ds_read_b128 a[160:163], v242 offset:16384\n"
    "v_mfma_f32_16x16x32_bf16 v[192:195], a[196:199], a[184:187], v[192:195]\n"
    "ds_read_b128 a[128:131], v241 offset:16384\n"
    "v_mfma_f32_16x16x32_bf16 v[192:195], a[192:195], a[152:155], v[192:195]\n"
    "s_waitcnt lgkmcnt(10)\n"
    "s_addk_i32 m0, 1024\n"
    "s_addk_i32 s54, 16384\n"
    "v_mfma_f32_16x16x32_bf16 v[160:163], a[192:195], a[148:151], v[160:163]\n"
    "buffer_load_dwordx4 v254, s[72:75], s54 offen  lds\n"
    "v_mfma_f32_16x16x32_bf16 v[160:163], a[196:199], a[180:183], v[160:163]\n"
    "s_waitcnt lgkmcnt(8)\n"
    "s_add_u32 s57, s57, 131072\n"
    "s_cmp_ge_u32 s57, 1572864\n"
    "v_mfma_f32_16x16x32_bf16 v[128:131], a[196:199], a[176:179], v[128:131]\n"
    "s_cselect_b32 s57, s70, s57\n"
    "v_mfma_f32_16x16x32_bf16 v[128:131], a[192:195], a[144:147], v[128:131]\n"
    "s_waitcnt lgkmcnt(6)\n"
    "s_add_u32 m0, s79, 32768\n"
    "s_add_u32 s54, s57, 0\n"
    "v_mfma_f32_16x16x32_bf16 v[96:99], a[192:195], a[140:143], v[96:99]\n"
    "buffer_load_dwordx4 v254, s[72:75], s54 offen  lds\n"
    "v_mfma_f32_16x16x32_bf16 v[96:99], a[196:199], a[172:175], v[96:99]\n"
    "s_waitcnt lgkmcnt(4)\n"
    "v_mfma_f32_16x16x32_bf16 v[64:67], a[196:199], a[168:171], v[64:67]\n"
    "v_mfma_f32_16x16x32_bf16 v[64:67], a[192:195], a[136:139], v[64:67]\n"
    "s_waitcnt lgkmcnt(2)\n"
    "s_addk_i32 m0, 1024\n"
    "s_addk_i32 s54, 16384\n"
    "v_mfma_f32_16x16x32_bf16 v[32:35], a[192:195], a[132:135], v[32:35]\n"
    "buffer_load_dwordx4 v254, s[72:75], s54 offen  lds\n"
    "v_mfma_f32_16x16x32_bf16 v[32:35], a[196:199], a[164:167], v[32:35]\n"
    "s_waitcnt lgkmcnt(0)\n"
    "v_mfma_f32_16x16x32_bf16 v[0:3], a[196:199], a[160:163], v[0:3]\n"
    "v_mfma_f32_16x16x32_bf16 v[0:3], a[192:195], a[128:131], v[0:3]\n"
    "s_addk_i32 m0, 1024\n"
    "s_addk_i32 s54, 16384\n"
    "v_mfma_f32_16x16x32_bf16 v[4:7], a[200:203], a[128:131], v[4:7]\n"
    "buffer_load_dwordx4 v254, s[72:75], s54 offen  lds\n"
    "v_mfma_f32_16x16x32_bf16 v[4:7], a[204:207], a[160:163], v[4:7]\n"
    "v_mfma_f32_16x16x32_bf16 v[36:39], a[204:207], a[164:167], v[36:39]\n"
    "v_mfma_f32_16x16x32_bf16 v[36:39], a[200:203], a[132:135], v[36:39]\n"
    "s_addk_i32 m0, 1024\n"
    "s_addk_i32 s54, 16384\n"
    "v_mfma_f32_16x16x32_bf16 v[68:71], a[200:203], a[136:139], v[68:71]\n"
    "buffer_load_dwordx4 v254, s[72:75], s54 offen  lds\n"
    "v_mfma_f32_16x16x32_bf16 v[68:71], a[204:207], a[168:171], v[68:71]\n"
    "v_mfma_f32_16x16x32_bf16 v[100:103], a[204:207], a[172:175], v[100:103]\n"
    "v_mfma_f32_16x16x32_bf16 v[100:103], a[200:203], a[140:143], v[100:103]\n"
    "s_addk_i32 m0, 1024\n"
    "s_addk_i32 s54, 16384\n"
    "v_mfma_f32_16x16x32_bf16 v[132:135], a[200:203], a[144:147], v[132:135]\n"
    "buffer_load_dwordx4 v254, s[72:75], s54 offen  lds\n"
    "s_bitcmp1_b32 s85, 1\n"
    "s_cbranch_scc1 CP1_%=\n"
    "CR1_%=:\n"
    "v_mfma_f32_16x16x32_bf16 v[132:135], a[204:207], a[176:179], v[132:135]\n"
    "v_mfma_f32_16x16x32_bf16 v[164:167], a[204:207], a[180:183], v[164:167]\n"
    "v_mfma_f32_16x16x32_bf16 v[164:167], a[200:203], a[148:151], v[164:167]\n"
    "s_addk_i32 m0, 1024\n"
    "s_addk_i32 s54, 16384\n"
    "v_mfma_f32_16x16x32_bf16 v[196:199], a[200:203], a[152:155], v[196:199]\n"
    "buffer_load_dwordx4 v254, s[72:75], s54 offen  lds\n"
    "v_mfma_f32_16x16x32_bf16 v[196:199], a[204:207], a[184:187], v[196:199]\n"
    "v_mfma_f32_16x16x32_bf16 v[228:231], a[204:207], a[188:191], v[228:231]\n"
    "v_mfma_f32_16x16x32_bf16 v[228:231], a[200:203], a[156:159], v[228:231]\n"
    "v_add_u32 v252, 0xfffe0000, v252\n"
    "v_add_u32 v253, 0xfffe0000, v253\n"
    "v_mfma_f32_16x16x32_bf16 v[232:235], a[208:211], a[156:159], v[232:235]\n"
    "s_waitcnt vmcnt(6)\n"
    "ds_read_b64_tr_b16 a[224:225], v252 offset:0\n"
    "ds_read_b64_tr_b16 a[226:227], v252 offset:512\n"
    "v_mfma_f32_16x16x32_bf16 v[232:235], a[212:215], a[188:191], v[232:235]\n"
    "s_addk_i32 m0, 1024\n"
    "s_addk_i32 s54, 16384\n"
    "v_mfma_f32_16x16x32_bf16 v[200:203], a[212:215], a[184:187], v[200:203]\n"
    "buffer_load_dwordx4 v254, s[72:75], s54 offen  lds\n"
    "v_lshlrev_b32 v248, 16, v248\n"
    "v_lshlrev_b32 v249, 16, v249\n"
    "v_add_f32 v248, v249, v248\n"
    "v_lshlrev_b32 v251, 16, v251\n"
    "v_add_f32 v248, v251, v248\n"
    "v_lshlrev_b32 v244, 16, v244\n"
    "v_add_f32 v248, v244, v248\n"
    "v_mfma_f32_16x16x32_bf16 v[200:203], a[208:211], a[152:155], v[200:203]\n"
    "buffer_load_ushort v249, v250, s[20:23], 0 offen\n"
    "v_mfma_f32_16x16x32_bf16 v[168:171], a[208:211], a[148:151], v[168:171]\n"
    "buffer_load_ushort v251, v250, s[20:23], 0 offen offset:2048\n"
    "v_add_u32 v250, 0x1000, v250\n"
    "ds_read_b64_tr_b16 a[228:229], v252 offset:4096\n"
    "ds_read_b64_tr_b16 a[230:231], v252 offset:4608\n"
    "v_mfma_f32_16x16x32_bf16 v[168:171], a[212:215], a[180:183], v[168:171]\n"
    "ds_read_b64_tr_b16 a[232:233], v252 offset:8\n"
    "ds_read_b64_tr_b16 a[234:235], v252 offset:520\n"
    "v_mfma_f32_16x16x32_bf16 v[136:139], a[212:215], a[176:179], v[136:139]\n"
    "s_addk_i32 m0, 1024\n"
    "s_addk_i32 s54, 16384\n"
    "v_mfma_f32_16x16x32_bf16 v[136:139], a[208:211], a[144:147], v[136:139]\n"
    "buffer_load_dwordx4 v254, s[72:75], s54 offen  lds\n"
    "v_mfma_f32_16x16x32_bf16 v[104:107], a[208:211], a[140:143], v[104:107]\n"
    "ds_read_b64_tr_b16 a[236:237], v252 offset:4104\n"
    "ds_read_b64_tr_b16 a[238:239], v252 offset:4616\n"
    "v_mfma_f32_16x16x32_bf16 v[104:107], a[212:215], a[172:175], v[104:107]\n"
    "ds_read_b64_tr_b16 a[192:193], v253 offset:0\n"
    "ds_read_b64_tr_b16 a[194:195], v253 offset:512\n"
    "v_mfma_f32_16x16x32_bf16 v[72:75], a[212:215], a[168:171], v[72:75]\n"
    "s_add_u32 m0, s68, 114688\n"
    "s_add_u32 s60, s78, 0\n"
    "s_or_b32 s60, s60, s71\n"
    "v_mfma_f32_16x16x32_bf16 v[72:75], a[208:211], a[136:139], v[72:75]\n"
    "buffer_load_dwordx4 v254, s[72:75], s60 offen nt lds\n"
    "v_mfma_f32_16x16x32_bf16 v[40:43], a[208:211], a[132:135], v[40:43]\n"
    "ds_read_b64_tr_b16 a[196:197], v253 offset:4096\n"
    "ds_read_b64_tr_b16 a[198:199], v253 offset:4608\n"
    "v_mfma_f32_16x16x32_bf16 v[40:43], a[212:215], a[164:167], v[40:43]\n"
    "ds_read_b64_tr_b16 a[200:201], v253 offset:8\n"
    "ds_read_b64_tr_b16 a[202:203], v253 offset:520\n"
    "v_mfma_f32_16x16x32_bf16 v[8:11], a[212:215], a[160:163], v[8:11]\n"
    "s_add_u32 m0, s68, 115712\n"
    "s_add_u32 s60, s78, 16384\n"
    "s_or_b32 s60, s60, s71\n"
    "v_mfma_f32_16x16x32_bf16 v[8:11], a[208:211], a[128:131], v[8:11]\n"
    "buffer_load_dwordx4 v254, s[72:75], s60 offen nt lds\n"
    "v_mfma_f32_16x16x32_bf16 v[12:15], a[216:219], a[128:131], v[12:15]\n"
    "ds_read_b64_tr_b16 a[204:205], v253 offset:4104\n"
    "ds_read_b64_tr_b16 a[206:207], v253 offset:4616\n"
    "v_mfma_f32_16x16x32_bf16 v[12:15], a[220:223], a[160:163], v[12:15]\n"
    "s_add_u32 m0, s68, 116736\n"
    "s_add_u32 s60, s78, 32768\n"
    "s_or_b32 s60, s60, s71\n"
    "v_mfma_f32_16x16x32_bf16 v[44:47], a[220:223], a[164:167], v[44:47]\n"
    "buffer_load_dwordx4 v254, s[72:75], s60 offen nt lds\n"
    "v_mfma_f32_16x16x32_bf16 v[44:47], a[216:219], a[132:135], v[44:47]\n"
    "s_add_u32 m0, s68, 117760\n"
    "s_add_u32 s60, s78, 49152\n"
    "s_or_b32 s60, s60, s71\n"
    "v_mfma_f32_16x16x32_bf16 v[76:79], a[216:219], a[136:139], v[76:79]\n"
    "buffer_load_dwordx4 v254, s[72:75], s60 offen nt lds\n"
    "v_mfma_f32_16x16x32_bf16 v[76:79], a[220:223], a[168:171], v[76:79]\n"
    "v_mfma_f32_16x16x32_bf16 v[108:111], a[220:223], a[172:175], v[108:111]\n"
    "v_mfma_f32_16x16x32_bf16 v[108:111], a[216:219], a[140:143], v[108:111]\n"
    "s_xor_b32 s59, s66, s67\n"
    "s_addk_i32 s67, 128\n"
    "s_cmpk_eq_u32 s67, 1536\n"
    "v_mfma_f32_16x16x32_bf16 v[140:143], a[216:219], a[144:147], v[140:143]\n"
    "s_cselect_b32 s67, 0, s67\n"
    "s_add_u32 m0, s68, 65536\n"
    "s_nop 0\n"
    "v_mfma_f32_16x16x32_bf16 v[140:143], a[220:223], a[176:179], v[140:143]\n"
    "buffer_load_dwordx4 v255, s[24:27], s59 offen  lds\n"
    "v_mfma_f32_16x16x32_bf16 v[172:175], a[220:223], a[180:183], v[172:175]\n"
    "v_mfma_f32_16x16x32_bf16 v[172:175], a[216:219], a[148:151], v[172:175]\n"
    "s_add_u32 m0, s68, 66560\n"
    "s_addk_i32 s59, 16384\n"
    "v_mfma_f32_16x16x32_bf16 v[204:207], a[216:219], a[152:155], v[204:207]\n"
    "buffer_load_dwordx4 v243, s[24:27], s59 offen  lds\n"
    "v_mfma_f32_16x16x32_bf16 v[204:207], a[220:223], a[184:187], v[204:207]\n"
    "v_mfma_f32_16x16x32_bf16 v[236:239], a[220:223], a[188:191], v[236:239]\n"
    "v_mfma_f32_16x16x32_bf16 v[236:239], a[216:219], a[156:159], v[236:239]\n"
    "s_waitcnt lgkmcnt(12)\n"
    "s_add_u32 m0, s68, 67584\n"
    "s_addk_i32 s59, 16384\n"
    "v_mfma_f32_16x16x32_bf16 a[240:243], a[224:227], a[156:159], a[240:243]\n"
    "buffer_load_dwordx4 v255, s[24:27], s59 offen  lds\n"
    "v_mfma_f32_16x16x32_bf16 a[240:243], a[228:231], a[188:191], a[240:243]\n"
    "v_add_u32 v245, 1, v245\n"
    "v_add_u32 v246, 1, v246\n"
    "v_mfma_f32_16x16x32_bf16 v[208:211], a[228:231], a[184:187], v[208:211]\n"
    "v_readfirstlane_b32 s88, v245\n"
    "v_lshrrev_b32 v247, 2, v240\n"
    "v_mfma_f32_16x16x32_bf16 v[208:211], a[224:227], a[152:155], v[208:211]\n"
    "s_lshl_b32 s61, s65, 8\n"
    "s_mov_b32 s82, s87\n"
    "v_mfma_f32_16x16x32_bf16 v[176:179], a[224:227], a[148:151], v[176:179]\n"
    "buffer_store_dword v246, v247, s[80:83], s61 offen offset:2048 sc1\n"
    "v_mfma_f32_16x16x32_bf16 v[176:179], a[228:231], a[180:183], v[176:179]\n"
    "s_add_u32 m0, s68, 68608\n"
    "s_addk_i32 s59, 16384\n"
    "v_mfma_f32_16x16x32_bf16 v[144:147], a[228:231], a[176:179], v[144:147]\n"
    "buffer_load_dwordx4 v243, s[24:27], s59 offen  lds\n"
    "v_mfma_f32_16x16x32_bf16 v[144:147], a[224:227], a[144:147], v[144:147]\n"
    "s_add_u32 m0, s79, 131072\n"
    "s_add_u32 s54, s57, 512\n"
    "v_mfma_f32_16x16x32_bf16 v[112:115], a[224:227], a[140:143], v[112:115]\n"
    "buffer_load_dwordx4 v254, s[72:75], s54 offen  lds\n"
    "v_mfma_f32_16x16x32_bf16 v[112:115], a[228:231], a[172:175], v[112:115]\n"
    "v_mfma_f32_16x16x32_bf16 v[80:83], a[228:231], a[168:171], v[80:83]\n"
    "v_mfma_f32_16x16x32_bf16 v[80:83], a[224:227], a[136:139], v[80:83]\n"
    "s_addk_i32 m0, 1024\n"
    "s_addk_i32 s54, 16384\n"
    "v_mfma_f32_16x16x32_bf16 v[48:51], a[224:227], a[132:135], v[48:51]\n"
    "buffer_load_dwordx4 v254, s[72:75], s54 offen  lds\n"
    "v_mfma_f32_16x16x32_bf16 v[48:51], a[228:231], a[164:167], v[48:51]\n"
    "v_mfma_f32_16x16x32_bf16 v[16:19], a[228:231], a[160:163], v[16:19]\n"
    "v_mfma_f32_16x16x32_bf16 v[16:19], a[224:227], a[128:131], v[16:19]\n"
    "s_waitcnt lgkmcnt(8)\n"
    "s_addk_i32 m0, 1024\n"
    "s_addk_i32 s54, 16384\n"
    "v_mfma_f32_16x16x32_bf16 v[20:23], a[232:235], a[128:131], v[20:23]\n"
    "buffer_load_dwordx4 v254, s[72:75], s54 offen  lds\n"
    "v_mfma_f32_16x16x32_bf16 v[20:23], a[236:239], a[160:163], v[20:23]\n"
    "v_mfma_f32_16x16x32_bf16 v[52:55], a[236:239], a[164:167], v[52:55]\n"
    "v_mfma_f32_16x16x32_bf16 v[52:55], a[232:235], a[132:135], v[52:55]\n"
    "s_addk_i32 m0, 1024\n"
    "s_addk_i32 s54, 16384\n"
    "v_mfma_f32_16x16x32_bf16 v[84:87], a[232:235], a[136:139], v[84:87]\n"
    "buffer_load_dwordx4 v254, s[72:75], s54 offen  lds\n"
    "v_mfma_f32_16x16x32_bf16 v[84:87], a[236:239], a[168:171], v[84:87]\n"
    "v_mfma_f32_16x16x32_bf16 v[116:119], a[236:239], a[172:175], v[116:119]\n"
    "v_mfma_f32_16x16x32_bf16 v[116:119], a[232:235], a[140:143], v[116:119]\n"
    "s_addk_i32 m0, 1024\n"
    "s_addk_i32 s54, 16384\n"
    "v_mfma_f32_16x16x32_bf16 v[148:151], a[232:235], a[144:147], v[148:151]\n"
    "buffer_load_dwordx4 v254, s[72:75], s54 offen  lds\n"
    "v_mfma_f32_16x16x32_bf16 v[148:151], a[236:239], a[176:179], v[148:151]\n"
    "v_mfma_f32_16x16x32_bf16 v[180:183], a[236:239], a[180:183], v[180:183]\n"
    "v_mfma_f32_16x16x32_bf16 v[180:183], a[232:235], a[148:151], v[180:183]\n"
    "s_addk_i32 m0, 1024\n"
    "s_addk_i32 s54, 16384\n"
    "v_mfma_f32_16x16x32_bf16 v[212:215], a[232:235], a[152:155], v[212:215]\n"
    "buffer_load_dwordx4 v254, s[72:75], s54 offen  lds\n"
    "v_mfma_f32_16x16x32_bf16 v[212:215], a[236:239], a[184:187], v[212:215]\n"
    "v_mfma_f32_16x16x32_bf16 a[244:247], a[236:239], a[188:191], a[244:247]\n"
    "v_mfma_f32_16x16x32_bf16 a[244:247], a[232:235], a[156:159], a[244:247]\n"
    "s_waitcnt lgkmcnt(4)\n"
    "s_waitcnt vmcnt(15)\n"
    "ds_read_b64_tr_b16 a[208:209], v252 offset:32768\n"
    "v_mfma_f32_16x16x32_bf16 a[248:251], a[192:195], a[156:159], a[248:251]\n"
    "ds_read_b64_tr_b16 a[210:211], v252 offset:33280\n"
    "s_addk_i32 m0, 1024\n"
    "v_mfma_f32_16x16x32_bf16 a[248:251], a[196:199], a[188:191], a[248:251]\n"
    "s_addk_i32 s54, 16384\n"
    "v_mfma_f32_16x16x32_bf16 v[216:219], a[196:199], a[184:187], v[216:219]\n"
    "buffer_load_dwordx4 v254, s[72:75], s54 offen  lds\n"
    "v_mfma_f32_16x16x32_bf16 v[216:219], a[192:195], a[152:155], v[216:219]\n"
    "ds_read_b64_tr_b16 a[212:213], v252 offset:36864\n"
    "ds_read_b64_tr_b16 a[214:215], v252 offset:37376\n"
    "v_mfma_f32_16x16x32_bf16 v[184:187], a[192:195], a[148:151], v[184:187]\n"
    "ds_read_b64_tr_b16 a[216:217], v252 offset:32776\n"
    "ds_read_b64_tr_b16 a[218:219], v252 offset:33288\n"
    "v_mfma_f32_16x16x32_bf16 v[184:187], a[196:199], a[180:183], v[184:187]\n"
    "s_addk_i32 m0, 1024\n"
    "s_addk_i32 s54, 16384\n"
    "v_mfma_f32_16x16x32_bf16 v[152:155], a[196:199], a[176:179], v[152:155]\n"
    "buffer_load_dwordx4 v254, s[72:75], s54 offen  lds\n"
    "v_mfma_f32_16x16x32_bf16 v[152:155], a[192:195], a[144:147], v[152:155]\n"
    "s_add_u32 s57, s57, 131072\n"
    "s_cmp_ge_u32 s57, 1572864\n"
    "s_cselect_b32 s57, s70, s57\n"
    "v_mfma_f32_16x16x32_bf16 v[120:123], a[192:195], a[140:143], v[120:123]\n"
    "ds_read_b64_tr_b16 a[220:221], v252 offset:36872\n"
    "ds_read_b64_tr_b16 a[222:223], v252 offset:37384\n"
    "v_mfma_f32_16x16x32_bf16 v[120:123], a[196:199], a[172:175], v[120:123]\n"
    "ds_read_b64_tr_b16 a[224:225], v253 offset:32768\n"
    "ds_read_b64_tr_b16 a[226:227], v253 offset:33280\n"
    "v_mfma_f32_16x16x32_bf16 v[88:91], a[196:199], a[168:171], v[88:91]\n"
    "s_waitcnt lgkmcnt(10)\n"
    "s_add_u32 m0, s79, 0\n"
    "s_add_u32 s54, s57, 0\n"
    "v_mfma_f32_16x16x32_bf16 v[88:91], a[192:195], a[136:139], v[88:91]\n"
    "buffer_load_dwordx4 v254, s[72:75], s54 offen  lds\n"
    "v_mfma_f32_16x16x32_bf16 v[56:59], a[192:195], a[132:135], v[56:59]\n"
    "ds_read_b64_tr_b16 a[228:229], v253 offset:36864\n"
    "ds_read_b64_tr_b16 a[230:231], v253 offset:37376\n"
    "v_mfma_f32_16x16x32_bf16 v[56:59], a[196:199], a[164:167], v[56:59]\n"
    "ds_read_b64_tr_b16 a[232:233], v253 offset:32776\n"
    "ds_read_b64_tr_b16 a[234:235], v253 offset:33288\n"
    "s_addk_i32 m0, 1024\n"
    "s_addk_i32 s54, 16384\n"
    "buffer_load_dwordx4 v254, s[72:75], s54 offen  lds\n"
    "s_barrier\n"
    "v_mfma_f32_16x16x32_bf16 v[24:27], a[196:199], a[160:163], v[24:27]\n"
    "ds_read_b64_tr_b16 a[236:237], v253 offset:36872\n"
    "ds_read_b64_tr_b16 a[238:239], v253 offset:37384\n"
    "v_mfma_f32_16x16x32_bf16 v[24:27], a[192:195], a[128:131], v[24:27]\n"
    "s_addk_i32 m0, 1024\n"
    "s_addk_i32 s54, 16384\n"
    "v_mfma_f32_16x16x32_bf16 v[28:31], a[200:203], a[128:131], v[28:31]\n"
    "buffer_load_dwordx4 v254, s[72:75], s54 offen  lds\n"
    "v_mfma_f32_16x16x32_bf16 v[28:31], a[204:207], a[160:163], v[28:31]\n"
    "s_add_u32 s60, s77, s65\n"
    "s_bfe_u32 s61, s60, 0x30001\n"
    "s_mul_i32 s61, s61, 6\n"
    "v_mfma_f32_16x16x32_bf16 v[60:63], a[204:207], a[164:167], v[60:63]\n"
    "s_lshr_b32 s62, s60, 4\n"
    "s_add_u32 s61, s61, s62\n"
    "s_lshl_b32 s61, s61, 1\n"
    "v_mfma_f32_16x16x32_bf16 v[60:63], a[200:203], a[132:135], v[60:63]\n"
    "s_and_b32 s62, s60, 1\n"
    "s_add_u32 s61, s61, s62\n"
    "s_lshl_b32 s61, s61, 10\n"
    "s_cmp_lt_u32 s60, 96\n"
    "s_cselect_b32 s61, s61, 98304\n"
    "v_lshlrev_b32 v249, 16, v249\n"
    "v_add_f32 v248, v249, v248\n"
    "v_lshlrev_b32 v251, 16, v251\n"
    "v_add_f32 v248, v251, v248\n"
    "v_add_f32 v248, 0xc3ffaa80, v248\n"
    "v_mfma_f32_16x16x32_bf16 v[92:95], a[200:203], a[136:139], v[92:95]\n"
    "buffer_store_dwordx4 v[248:251], v240, s[36:39], s61 offen sc1\n"
    "s_lshl_b32 s61, s64, 2\n"
    "s_add_u32 s61, s61, s65\n"
    "s_mul_i32 s55, s61, 12288\n"
    "s_mul_i32 s61, s61, 1572864\n"
    "s_mov_b64 s[20:21], s[6:7]\n"
    "s_add_u32 s20, s20, s61\n"
    "s_addc_u32 s21, s21, 0\n"
    "s_and_b32 s21, s21, 0xffff\n"
    "s_mov_b32 s22, 1572864\n"
    "v_mfma_f32_16x16x32_bf16 v[92:95], a[204:207], a[168:171], v[92:95]\n"
    "ds_read_b128 a[128:131], v241 offset:32768\n"
    "s_addk_i32 m0, 1024\n"
    "v_mfma_f32_16x16x32_bf16 v[124:127], a[204:207], a[172:175], v[124:127]\n"
    "s_addk_i32 s54, 16384\n"
    "v_mfma_f32_16x16x32_bf16 v[124:127], a[200:203], a[140:143], v[124:127]\n"
    "buffer_load_dwordx4 v254, s[72:75], s54 offen  lds\n"
    "ds_read_b128 a[160:163], v242 offset:32768\n"
    "ds_read_b128 a[164:167], v242 offset:34816\n"
    "v_mfma_f32_16x16x32_bf16 v[156:159], a[200:203], a[144:147], v[156:159]\n"
    "ds_read_b128 a[132:135], v241 offset:34816\n"
    "ds_read_b128 a[136:139], v241 offset:36864\n"
    "v_mfma_f32_16x16x32_bf16 v[156:159], a[204:207], a[176:179], v[156:159]\n"
    "s_addk_i32 m0, 1024\n"
    "s_addk_i32 s54, 16384\n"
    "v_mfma_f32_16x16x32_bf16 v[188:191], a[204:207], a[180:183], v[188:191]\n"
    "buffer_load_dwordx4 v254, s[72:75], s54 offen  lds\n"
    "ds_read_b128 a[168:171], v242 offset:36864\n"
    "v_mfma_f32_16x16x32_bf16 v[188:191], a[200:203], a[148:151], v[188:191]\n"
    "ds_read_b128 a[172:175], v242 offset:38912\n"
    "ds_read_b128 a[140:143], v241 offset:38912\n"
    "v_mfma_f32_16x16x32_bf16 v[220:223], a[200:203], a[152:155], v[220:223]\n"
    "ds_read_b128 a[144:147], v241 offset:40960\n"
    "s_addk_i32 m0, 1024\n"
    "v_mfma_f32_16x16x32_bf16 v[220:223], a[204:207], a[184:187], v[220:223]\n"
    "s_addk_i32 s54, 16384\n"
    "v_mfma_f32_16x16x32_bf16 a[252:255], a[204:207], a[188:191], a[252:255]\n"
    "buffer_load_dwordx4 v254, s[72:75], s54 offen  lds\n"
    "ds_read_b128 a[176:179], v242 offset:40960\n"
    "v_mfma_f32_16x16x32_bf16 a[252:255], a[200:203], a[156:159], a[252:255]\n"
    "s_waitcnt lgkmcnt(8)\n"
    "ds_read_b128 a[180:183], v242 offset:43008\n"
    "ds_read_b128 a[148:151], v241 offset:43008\n"
    "v_mfma_f32_16x16x32_bf16 v[0:3], a[208:211], a[128:131], v[0:3]\n"
    "ds_read_b128 a[152:155], v241 offset:45056\n"
    "s_addk_i32 m0, 1024\n"
    "v_mfma_f32_16x16x32_bf16 v[0:3], a[212:215], a[160:163], v[0:3]\n"
    "s_waitcnt lgkmcnt(9)\n"
    "s_addk_i32 s54, 16384\n"
    "v_mfma_f32_16x16x32_bf16 v[32:35], a[212:215], a[164:167], v[32:35]\n"
    "buffer_load_dwordx4 v254, s[72:75], s54 offen  lds\n"
    "ds_read_b128 a[184:187], v242 offset:45056\n"
    "ds_read_b128 a[188:191], v242 offset:47104\n"
    "ds_read_b128 a[156:159], v241 offset:47104\n"
    "v_mfma_f32_16x16x32_bf16 v[32:35], a[208:211], a[132:135], v[32:35]\n"
    "s_waitcnt lgkmcnt(10)\n"
    "s_addk_i32 m0, 1024\n"
    "s_addk_i32 s54, 16384\n"
    "v_mfma_f32_16x16x32_bf16 v[64:67], a[208:211], a[136:139], v[64:67]\n"
    "buffer_load_dwordx4 v254, s[72:75], s54 offen  lds\n"
    "v_mfma_f32_16x16x32_bf16 v[64:67], a[212:215], a[168:171], v[64:67]\n"
    "s_waitcnt lgkmcnt(8)\n"
    "v_mfma_f32_16x16x32_bf16 v[96:99], a[212:215], a[172:175], v[96:99]\n"
    "v_mfma_f32_16x16x32_bf16 v[96:99], a[208:211], a[140:143], v[96:99]\n"
    "s_waitcnt lgkmcnt(6)\n"
    "s_add_u32 m0, s79, 32768\n"
    "s_add_u32 s54, s57, 512\n"
    "v_mfma_f32_16x16x32_bf16 v[128:131], a[208:211], a[144:147], v[128:131]\n"
    "buffer_load_dwordx4 v254, s[72:75], s54 offen  lds\n"
    "v_mfma_f32_16x16x32_bf16 v[128:131], a[212:215], a[176:179], v[128:131]\n"
    "s_waitcnt lgkmcnt(4)\n"
    "v_mfma_f32_16x16x32_bf16 v[160:163], a[212:215], a[180:183], v[160:163]\n"
    "v_mfma_f32_16x16x32_bf16 v[160:163], a[208:211], a[148:151], v[160:163]\n"
    "s_waitcnt lgkmcnt(2)\n"
    "s_addk_i32 m0, 1024\n"
    "s_addk_i32 s54, 16384\n"
    "v_mfma_f32_16x16x32_bf16 v[192:195], a[208:211], a[152:155], v[192:195]\n"
    "buffer_load_dwordx4 v254, s[72:75], s54 offen  lds\n"
    "v_mfma_f32_16x16x32_bf16 v[192:195], a[212:215], a[184:187], v[192:195]\n"
    "s_waitcnt lgkmcnt(0)\n"
    "v_mfma_f32_16x16x32_bf16 v[224:227], a[212:215], a[188:191], v[224:227]\n"
    "v_mfma_f32_16x16x32_bf16 v[224:227], a[208:211], a[156:159], v[224:227]\n"
    "s_addk_i32 m0, 1024\n"
    "s_addk_i32 s54, 16384\n"
    "v_mfma_f32_16x16x32_bf16 v[228:231], a[216:219], a[156:159], v[228:231]\n"
    "buffer_load_dwordx4 v254, s[72:75], s54 offen  lds\n"
    "v_mfma_f32_16x16x32_bf16 v[228:231], a[220:223], a[188:191], v[228:231]\n"
    "v_mfma_f32_16x16x32_bf16 v[196:199], a[220:223], a[184:187], v[196:199]\n"
    "v_mfma_f32_16x16x32_bf16 v[196:199], a[216:219], a[152:155], v[196:199]\n"
    "s_addk_i32 m0, 1024\n"
    "s_addk_i32 s54, 16384\n"
    "v_mfma_f32_16x16x32_bf16 v[164:167], a[216:219], a[148:151], v[164:167]\n"
    "buffer_load_dwordx4 v254, s[72:75], s54 offen  lds\n"
    "v_mfma_f32_16x16x32_bf16 v[164:167], a[220:223], a[180:183], v[164:167]\n"
    "s_lshl_b32 s59, s64, 7\n"
    "s_or_b32 s66, s66, s59\n"
    "s_xor_b32 s59, s66, s67\n"
    "v_mfma_f32_16x16x32_bf16 v[132:135], a[220:223], a[176:179], v[132:135]\n"
    "s_addk_i32 s67, 128\n"
    "s_cmpk_eq_u32 s67, 1536\n"
    "s_cselect_b32 s67, 0, s67\n"
    "v_mfma_f32_16x16x32_bf16 v[132:135], a[216:219], a[144:147], v[132:135]\n"
    "s_add_u32 m0, s68, 81920\n"
    "s_nop 0\n"
    "v_mfma_f32_16x16x32_bf16 v[100:103], a[216:219], a[140:143], v[100:103]\n"
    "buffer_load_dwordx4 v255, s[24:27], s59 offen  lds\n"
    "s_add_u32 m0, s68, 82944\n"
    "s_addk_i32 s59, 16384\n"
    "buffer_load_dwordx4 v243, s[24:27], s59 offen  lds\n"
    "s_bitcmp1_b32 s85, 2\n"
    "s_cbranch_scc1 CP2_%=\n"
    "CR2_%=:\n"
    "v_mfma_f32_16x16x32_bf16 v[100:103], a[220:223], a[172:175], v[100:103]\n"
    "v_mfma_f32_16x16x32_bf16 v[68:71], a[220:223], a[168:171], v[68:71]\n"
    "s_add_u32 m0, s68, 83968\n"
    "s_addk_i32 s59, 16384\n"
    "v_mfma_f32_16x16x32_bf16 v[68:71], a[216:219], a[136:139], v[68:71]\n"
    "buffer_load_dwordx4 v255, s[24:27], s59 offen  lds\n"
    "v_mfma_f32_16x16x32_bf16 v[36:39], a[216:219], a[132:135], v[36:39]\n"
    "v_mfma_f32_16x16x32_bf16 v[36:39], a[220:223], a[164:167], v[36:39]\n"
    "s_add_u32 m0, s68, 84992\n"
    "s_addk_i32 s59, 16384\n"
    "v_mfma_f32_16x16x32_bf16 v[4:7], a[220:223], a[160:163], v[4:7]\n"
    "buffer_load_dwordx4 v243, s[24:27], s59 offen  lds\n"
    "v_mfma_f32_16x16x32_bf16 v[4:7], a[216:219], a[128:131], v[4:7]\n"
    "v_add_u32 v252, 0x20000, v252\n"
    "v_add_u32 v253, 0x20000, v253\n"
    "v_mfma_f32_16x16x32_bf16 v[8:11], a[224:227], a[128:131], v[8:11]\n"
    "s_waitcnt vmcnt(17)\n"
    "ds_read_b64_tr_b16 a[192:193], v252 offset:0\n"
    "ds_read_b64_tr_b16 a[194:195], v252 offset:512\n"
    "v_mfma_f32_16x16x32_bf16 v[8:11], a[228:231], a[160:163], v[8:11]\n"
    "s_add_u32 m0, s79, 36864\n"
    "s_addk_i32 s54, 16384\n"
    "v_mfma_f32_16x16x32_bf16 v[40:43], a[228:231], a[164:167], v[40:43]\n"
    "buffer_load_dwordx4 v254, s[72:75], s54 offen  lds\n"
    "v_mfma_f32_16x16x32_bf16 v[40:43], a[224:227], a[132:135], v[40:43]\n"
    "ds_read_b64_tr_b16 a[196:197], v252 offset:4096\n"
    "ds_read_b64_tr_b16 a[198:199], v252 offset:4608\n"
    "v_mfma_f32_16x16x32_bf16 v[72:75], a[224:227], a[136:139], v[72:75]\n"
    "v_add_u32 v246, s90, v252\n"
    "v_add_u32 v246, 0xfffe0000, v246\n"
    "v_mfma_f32_16x16x32_bf16 v[72:75], a[228:231], a[168:171], v[72:75]\n"
    "ds_read_b64_tr_b16 v[248:249], v246 offset:0\n"
    "ds_read_b64_tr_b16 v[250:251], v246 offset:512\n"
    "v_mfma_f32_16x16x32_bf16 v[104:107], a[228:231], a[172:175], v[104:107]\n"
    "ds_read_b64_tr_b16 a[200:201], v252 offset:8\n"
    "ds_read_b64_tr_b16 a[202:203], v252 offset:520\n"
    "v_mfma_f32_16x16x32_bf16 v[104:107], a[224:227], a[140:143], v[104:107]\n"
    "s_addk_i32 m0, 1024\n"
    "s_addk_i32 s54, 16384\n"
    "v_mfma_f32_16x16x32_bf16 v[136:139], a[224:227], a[144:147], v[136:139]\n"
    "buffer_load_dwordx4 v254, s[72:75], s54 offen  lds\n"
    "ds_read_b64_tr_b16 a[204:205], v252 offset:4104\n"
    "ds_read_b64_tr_b16 a[206:207], v252 offset:4616\n"
    "v_mfma_f32_16x16x32_bf16 v[136:139], a[228:231], a[176:179], v[136:139]\n"
    "ds_read_b64_tr_b16 a[208:209], v253 offset:0\n"
    "ds_read_b64_tr_b16 a[210:211], v253 offset:512\n"
    "v_mfma_f32_16x16x32_bf16 v[168:171], a[228:231], a[180:183], v[168:171]\n"
    "ds_read_b64_tr_b16 a[212:213], v253 offset:4096\n"
    "ds_read_b64_tr_b16 a[214:215], v253 offset:4608\n"
    "v_mfma_f32_16x16x32_bf16 v[168:171], a[224:227], a[148:151], v[168:171]\n"
    "ds_read_b64_tr_b16 a[216:217], v253 offset:8\n"
    "ds_read_b64_tr_b16 a[218:219], v253 offset:520\n"
    "v_mfma_f32_16x16x32_bf16 v[200:203], a[224:227], a[152:155], v[200:203]\n"
    "s_add_u32 s62, s89, 0\n"
    "s_waitcnt lgkmcnt(10)\n"
    "v_mfma_f32_16x16x32_bf16 v[200:203], a[228:231], a[184:187], v[200:203]\n"
    "buffer_store_dwordx4 v[248:251], v240, s[28:31], s62 offen offset:0 sc1\n"
    "s_nop 0\n"
    "ds_read_b64_tr_b16 a[220:221], v253 offset:4104\n"
    "ds_read_b64_tr_b16 a[222:223], v253 offset:4616\n"
    "v_mfma_f32_16x16x32_bf16 v[232:235], a[228:231], a[188:191], v[232:235]\n"
    "v_mfma_f32_16x16x32_bf16 v[232:235], a[224:227], a[156:159], v[232:235]\n"
    "v_mfma_f32_16x16x32_bf16 v[236:239], a[232:235], a[156:159], v[236:239]\n"
    "v_mfma_f32_16x16x32_bf16 v[236:239], a[236:239], a[188:191], v[236:239]\n"
    "v_mfma_f32_16x16x32_bf16 v[204:207], a[236:239], a[184:187], v[204:207]\n"
    "v_add_u32 v246, s90, v252\n"
    "v_add_u32 v246, 0xfffe0000, v246\n"
    "v_mfma_f32_16x16x32_bf16 v[204:207], a[232:235], a[152:155], v[204:207]\n"
    "ds_read_b64_tr_b16 v[248:249], v246 offset:8\n"
    "ds_read_b64_tr_b16 v[250:251], v246 offset:520\n"
    "v_mfma_f32_16x16x32_bf16 v[172:175], a[232:235], a[148:151], v[172:175]\n"
    "v_mfma_f32_16x16x32_bf16 v[172:175], a[236:239], a[180:183], v[172:175]\n"
    "v_mfma_f32_16x16x32_bf16 v[140:143], a[236:239], a[176:179], v[140:143]\n"
    "v_mfma_f32_16x16x32_bf16 v[140:143], a[232:235], a[144:147], v[140:143]\n"
    "v_mfma_f32_16x16x32_bf16 v[108:111], a[232:235], a[140:143], v[108:111]\n"
    "v_mfma_f32_16x16x32_bf16 v[108:111], a[236:239], a[172:175], v[108:111]\n"
    "v_mfma_f32_16x16x32_bf16 v[76:79], a[236:239], a[168:171], v[76:79]\n"
    "v_mfma_f32_16x16x32_bf16 v[76:79], a[232:235], a[136:139], v[76:79]\n"
    "v_mfma_f32_16x16x32_bf16 v[44:47], a[232:235], a[132:135], v[44:47]\n"
    "s_add_u32 s62, s89, 0\n"
    "s_waitcnt lgkmcnt(0)\n"
    "v_mfma_f32_16x16x32_bf16 v[44:47], a[236:239], a[164:167], v[44:47]\n"
    "buffer_store_dwordx4 v[248:251], v240, s[28:31], s62 offen offset:2048 sc1\n"
    "s_nop 0\n"
    "v_mfma_f32_16x16x32_bf16 v[12:15], a[236:239], a[160:163], v[12:15]\n"
    "v_mfma_f32_16x16x32_bf16 v[12:15], a[232:235], a[128:131], v[12:15]\n"
    "v_mfma_f32_16x16x32_bf16 v[16:19], a[192:195], a[128:131], v[16:19]\n"
    "v_mfma_f32_16x16x32_bf16 v[16:19], a[196:199], a[160:163], v[16:19]\n"
    "v_mfma_f32_16x16x32_bf16 v[48:51], a[196:199], a[164:167], v[48:51]\n"
    "v_add_u32 v246, s90, v253\n"
    "v_add_u32 v246, 0xfffe0000, v246\n"
    "v_mfma_f32_16x16x32_bf16 v[48:51], a[192:195], a[132:135], v[48:51]\n"
    "ds_read_b64_tr_b16 v[248:249], v246 offset:0\n"
    "ds_read_b64_tr_b16 v[250:251], v246 offset:512\n"
    "v_mfma_f32_16x16x32_bf16 v[80:83], a[192:195], a[136:139], v[80:83]\n"
    "v_mfma_f32_16x16x32_bf16 v[80:83], a[196:199], a[168:171], v[80:83]\n"
    "v_mfma_f32_16x16x32_bf16 v[112:115], a[196:199], a[172:175], v[112:115]\n"
    "v_mfma_f32_16x16x32_bf16 v[112:115], a[192:195], a[140:143], v[112:115]\n"
    "v_mfma_f32_16x16x32_bf16 v[144:147], a[192:195], a[144:147], v[144:147]\n"
    "v_mfma_f32_16x16x32_bf16 v[144:147], a[196:199], a[176:179], v[144:147]\n"
    "v_mfma_f32_16x16x32_bf16 v[176:179], a[196:199], a[180:183], v[176:179]\n"
    "v_mfma_f32_16x16x32_bf16 v[176:179], a[192:195], a[148:151], v[176:179]\n"
    "v_mfma_f32_16x16x32_bf16 v[208:211], a[192:195], a[152:155], v[208:211]\n"
    "s_add_u32 s62, s89, 4096\n"
    "s_waitcnt lgkmcnt(0)\n"
    "v_mfma_f32_16x16x32_bf16 v[208:211], a[196:199], a[184:187], v[208:211]\n"
    "buffer_store_dwordx4 v[248:251], v240, s[28:31], s62 offen offset:0 sc1\n"
    "s_nop 0\n"
    "v_mfma_f32_16x16x32_bf16 a[240:243], a[196:199], a[188:191], a[240:243]\n"
    "v_mfma_f32_16x16x32_bf16 a[240:243], a[192:195], a[156:159], a[240:243]\n"
    "v_mfma_f32_16x16x32_bf16 a[244:247], a[200:203], a[156:159], a[244:247]\n"
    "v_mfma_f32_16x16x32_bf16 a[244:247], a[204:207], a[188:191], a[244:247]\n"
    "v_mfma_f32_16x16x32_bf16 v[212:215], a[204:207], a[184:187], v[212:215]\n"
    "v_add_u32 v246, s90, v253\n"
    "v_add_u32 v246, 0xfffe0000, v246\n"
    "v_mfma_f32_16x16x32_bf16 v[212:215], a[200:203], a[152:155], v[212:215]\n"
    "ds_read_b64_tr_b16 v[248:249], v246 offset:8\n"
    "ds_read_b64_tr_b16 v[250:251], v246 offset:520\n"
    "v_mfma_f32_16x16x32_bf16 v[180:183], a[200:203], a[148:151], v[180:183]\n"
    "v_mfma_f32_16x16x32_bf16 v[180:183], a[204:207], a[180:183], v[180:183]\n"
    "v_mfma_f32_16x16x32_bf16 v[148:151], a[204:207], a[176:179], v[148:151]\n"
    "v_mfma_f32_16x16x32_bf16 v[148:151], a[200:203], a[144:147], v[148:151]\n"
    "v_mfma_f32_16x16x32_bf16 v[116:119], a[200:203], a[140:143], v[116:119]\n"
    "v_mfma_f32_16x16x32_bf16 v[116:119], a[204:207], a[172:175], v[116:119]\n"
    "v_mfma_f32_16x16x32_bf16 v[84:87], a[204:207], a[168:171], v[84:87]\n"
    "v_mfma_f32_16x16x32_bf16 v[84:87], a[200:203], a[136:139], v[84:87]\n"
    "v_mfma_f32_16x16x32_bf16 v[52:55], a[200:203], a[132:135], v[52:55]\n"
    "s_add_u32 s62, s89, 4096\n"
    "s_waitcnt lgkmcnt(0)\n"
    "v_mfma_f32_16x16x32_bf16 v[52:55], a[204:207], a[164:167], v[52:55]\n"
    "buffer_store_dwordx4 v[248:251], v240, s[28:31], s62 offen offset:2048 sc1\n"
    "s_nop 0\n"
    "v_mfma_f32_16x16x32_bf16 v[20:23], a[204:207], a[160:163], v[20:23]\n"
    "v_mfma_f32_16x16x32_bf16 v[20:23], a[200:203], a[128:131], v[20:23]\n"
    "v_add_u32 v252, 0xfffe0000, v252\n"
    "v_add_u32 v253, 0xfffe0000, v253\n"
    "v_mfma_f32_16x16x32_bf16 v[24:27], a[208:211], a[128:131], v[24:27]\n"
    "s_waitcnt vmcnt(14)\n"
    "ds_read_b64_tr_b16 a[224:225], v252 offset:0\n"
    "ds_read_b64_tr_b16 a[226:227], v252 offset:512\n"
    "v_mfma_f32_16x16x32_bf16 v[24:27], a[212:215], a[160:163], v[24:27]\n"
    "ds_read_b64_tr_b16 a[228:229], v252 offset:4096\n"
    "ds_read_b64_tr_b16 a[230:231], v252 offset:4608\n"
    "v_mfma_f32_16x16x32_bf16 v[56:59], a[212:215], a[164:167], v[56:59]\n"
    "v_mfma_f32_16x16x32_bf16 v[56:59], a[208:211], a[132:135], v[56:59]\n"
    "ds_read_b64_tr_b16 a[232:233], v252 offset:8\n"
    "ds_read_b64_tr_b16 a[234:235], v252 offset:520\n"
    "v_mfma_f32_16x16x32_bf16 v[88:91], a[208:211], a[136:139], v[88:91]\n"
    "v_mfma_f32_16x16x32_bf16 v[88:91], a[212:215], a[168:171], v[88:91]\n"
    "ds_read_b64_tr_b16 a[236:237], v252 offset:4104\n"
    "ds_read_b64_tr_b16 a[238:239], v252 offset:4616\n"
    "v_mfma_f32_16x16x32_bf16 v[120:123], a[212:215], a[172:175], v[120:123]\n"
    "v_mfma_f32_16x16x32_bf16 v[120:123], a[208:211], a[140:143], v[120:123]\n"
    "ds_read_b64_tr_b16 a[192:193], v253 offset:0\n"
    "ds_read_b64_tr_b16 a[194:195], v253 offset:512\n"
    "v_mfma_f32_16x16x32_bf16 v[152:155], a[208:211], a[144:147], v[152:155]\n"
    "v_mfma_f32_16x16x32_bf16 v[152:155], a[212:215], a[176:179], v[152:155]\n"
    "ds_read_b64_tr_b16 a[196:197], v253 offset:4096\n"
    "ds_read_b64_tr_b16 a[198:199], v253 offset:4608\n"
    "v_mfma_f32_16x16x32_bf16 v[184:187], a[212:215], a[180:183], v[184:187]\n"
    "v_mfma_f32_16x16x32_bf16 v[184:187], a[208:211], a[148:151], v[184:187]\n"
    "ds_read_b64_tr_b16 a[200:201], v253 offset:8\n"
    "ds_read_b64_tr_b16 a[202:203], v253 offset:520\n"
    "v_mfma_f32_16x16x32_bf16 v[216:219], a[208:211], a[152:155], v[216:219]\n"
    "v_mfma_f32_16x16x32_bf16 v[216:219], a[212:215], a[184:187], v[216:219]\n"
    "s_barrier\n"
    "ds_read_b64_tr_b16 a[204:205], v253 offset:4104\n"
    "ds_read_b64_tr_b16 a[206:207], v253 offset:4616\n"
    "v_mfma_f32_16x16x32_bf16 a[248:251], a[212:215], a[188:191], a[248:251]\n"
    "v_mfma_f32_16x16x32_bf16 a[248:251], a[208:211], a[156:159], a[248:251]\n"
    "v_mfma_f32_16x16x32_bf16 a[252:255], a[216:219], a[156:159], a[252:255]\n"
    "v_mfma_f32_16x16x32_bf16 a[252:255], a[220:223], a[188:191], a[252:255]\n"
    "v_mfma_f32_16x16x32_bf16 v[220:223], a[220:223], a[184:187], v[220:223]\n"
    "ds_read_b128 a[156:159], v241 offset:14336\n"
    "v_mfma_f32_16x16x32_bf16 v[220:223], a[216:219], a[152:155], v[220:223]\n"
    "ds_read_b128 a[188:191], v242 offset:14336\n"
    "v_mfma_f32_16x16x32_bf16 v[188:191], a[216:219], a[148:151], v[188:191]\n"
    "ds_read_b128 a[184:187], v242 offset:12288\n"
    "v_mfma_f32_16x16x32_bf16 v[188:191], a[220:223], a[180:183], v[188:191]\n"
    "ds_read_b128 a[152:155], v241 offset:12288\n"
    "v_mfma_f32_16x16x32_bf16 v[156:159], a[220:223], a[176:179], v[156:159]\n"
    "ds_read_b128 a[148:151], v241 offset:10240\n"
    "v_mfma_f32_16x16x32_bf16 v[156:159], a[216:219], a[144:147], v[156:159]\n"
    "ds_read_b128 a[180:183], v242 offset:10240\n"
    "v_mfma_f32_16x16x32_bf16 v[124:127], a[216:219], a[140:143], v[124:127]\n"
    "ds_read_b128 a[176:179], v242 offset:8192\n"
    "v_mfma_f32_16x16x32_bf16 v[124:127], a[220:223], a[172:175], v[124:127]\n"
    "ds_read_b128 a[144:147], v241 offset:8192\n"
    "v_mfma_f32_16x16x32_bf16 v[92:95], a[220:223], a[168:171], v[92:95]\n"
    "ds_read_b128 a[140:143], v241 offset:6144\n"
    "v_mfma_f32_16x16x32_bf16 v[92:95], a[216:219], a[136:139], v[92:95]\n"
    "s_addk_i32 m0, 1024\n"
    "s_addk_i32 s54, 16384\n"
    "v_mfma_f32_16x16x32_bf16 v[60:63], a[216:219], a[132:135], v[60:63]\n"
    "buffer_load_dwordx4 v254, s[72:75], s54 offen  lds\n"
    "v_mfma_f32_16x16x32_bf16 v[60:63], a[220:223], a[164:167], v[60:63]\n"
    "ds_read_b128 a[172:175], v242 offset:6144\n"
    "ds_read_b128 a[168:171], v242 offset:4096\n"
    "v_mfma_f32_16x16x32_bf16 v[28:31], a[220:223], a[160:163], v[28:31]\n"
    "ds_read_b128 a[136:139], v241 offset:4096\n"
    "ds_read_b128 a[132:135], v241 offset:2048\n"
    "v_mfma_f32_16x16x32_bf16 v[28:31], a[216:219], a[128:131], v[28:31]\n"
    "s_waitcnt lgkmcnt(11)\n"
    "s_addk_i32 m0, 1024\n"
    "s_addk_i32 s54, 16384\n"
    "v_mfma_f32_16x16x32_bf16 v[224:227], a[224:227], a[156:159], v[224:227]\n"
    "buffer_load_dwordx4 v254, s[72:75], s54 offen  lds\n"
    "v_mfma_f32_16x16x32_bf16 v[224:227], a[228:231], a[188:191], v[224:227]\n"
    "s_waitcnt lgkmcnt(9)\n"
    "s_add_u32 s57, s57, 131072\n"
    "s_cmp_ge_u32 s57, 1572864\n"
    "v_mfma_f32_16x16x32_bf16 v[192:195], a[228:231], a[184:187], v[192:195]\n"
    "s_cselect_b32 s57, s70, s57\n"
    "ds_read_b128 a[164:167], v242 offset:2048\n"
    "ds_read_b128 a[160:163], v242 offset:0\n"
    "ds_read_b128 a[128:131], v241 offset:0\n"
    "v_mfma_f32_16x16x32_bf16 v[192:195], a[224:227], a[152:155], v[192:195]\n"
    "s_waitcnt lgkmcnt(10)\n"
    "s_add_u32 m0, s79, 131072\n"
    "s_lshl_b32 s54, s64, 17\n"
    "v_mfma_f32_16x16x32_bf16 v[160:163], a[224:227], a[148:151], v[160:163]\n"
    "s_xor_b32 s54, s54, s57\n"
    "v_mfma_f32_16x16x32_bf16 v[160:163], a[228:231], a[180:183], v[160:163]\n"
    "s_waitcnt lgkmcnt(8)\n"
    "v_mfma_f32_16x16x32_bf16 v[128:131], a[228:231], a[176:179], v[128:131]\n"
    "buffer_load_dwordx4 v254, s[72:75], s54 offen  lds\n"
    "v_mfma_f32_16x16x32_bf16 v[128:131], a[224:227], a[144:147], v[128:131]\n"
    "s_waitcnt lgkmcnt(6)\n"
    "s_addk_i32 m0, 1024\n"
    "s_addk_i32 s54, 16384\n"
    "v_mfma_f32_16x16x32_bf16 v[96:99], a[224:227], a[140:143], v[96:99]\n"
    "buffer_load_dwordx4 v254, s[72:75], s54 offen  lds\n"
    "v_mfma_f32_16x16x32_bf16 v[96:99], a[228:231], a[172:175], v[96:99]\n"
    "s_waitcnt lgkmcnt(4)\n"
    "v_mfma_f32_16x16x32_bf16 v[64:67], a[228:231], a[168:171], v[64:67]\n"
    "v_mfma_f32_16x16x32_bf16 v[64:67], a[224:227], a[136:139], v[64:67]\n"
    "s_waitcnt lgkmcnt(2)\n"
    "s_addk_i32 m0, 1024\n"
    "s_addk_i32 s54, 16384\n"
    "v_mfma_f32_16x16x32_bf16 v[32:35], a[224:227], a[132:135], v[32:35]\n"
    "buffer_load_dwordx4 v254, s[72:75], s54 offen  lds\n"
    "v_mfma_f32_16x16x32_bf16 v[32:35], a[228:231], a[164:167], v[32:35]\n"
    "s_waitcnt lgkmcnt(0)\n"
    "v_mfma_f32_16x16x32_bf16 v[0:3], a[228:231], a[160:163], v[0:3]\n"
    "v_mfma_f32_16x16x32_bf16 v[0:3], a[224:227], a[128:131], v[0:3]\n"
    "s_addk_i32 m0, 1024\n"
    "s_addk_i32 s54, 16384\n"
    "v_mfma_f32_16x16x32_bf16 v[4:7], a[232:235], a[128:131], v[4:7]\n"
    "buffer_load_dwordx4 v254, s[72:75], s54 offen  lds\n"
    "v_mfma_f32_16x16x32_bf16 v[4:7], a[236:239], a[160:163], v[4:7]\n"
    "s_add_u32 s82, s77, 2052\n"
    "s_and_b32 s82, s82, s86\n"
    "v_mfma_f32_16x16x32_bf16 v[36:39], a[236:239], a[164:167], v[36:39]\n"
    "buffer_load_dword v244, v240, s[80:83], s77 offen offset:2048 sc1\n"
    "v_mfma_f32_16x16x32_bf16 v[36:39], a[232:235], a[132:135], v[36:39]\n"
    "s_addk_i32 m0, 1024\n"
    "s_addk_i32 s54, 16384\n"
    "v_mfma_f32_16x16x32_bf16 v[68:71], a[232:235], a[136:139], v[68:71]\n"
    "buffer_load_dwordx4 v254, s[72:75], s54 offen  lds\n"
    "v_mfma_f32_16x16x32_bf16 v[68:71], a[236:239], a[168:171], v[68:71]\n"
    "v_mfma_f32_16x16x32_bf16 v[100:103], a[236:239], a[172:175], v[100:103]\n"
    "v_mfma_f32_16x16x32_bf16 v[100:103], a[232:235], a[140:143], v[100:103]\n"
    "s_addk_i32 m0, 1024\n"
    "s_addk_i32 s54, 16384\n"
    "v_mfma_f32_16x16x32_bf16 v[132:135], a[232:235], a[144:147], v[132:135]\n"
    "buffer_load_dwordx4 v254, s[72:75], s54 offen  lds\n"
    "s_bitcmp1_b32 s85, 3\n"
    "s_cbranch_scc1 CP3_%=\n"
    "CR3_%=:\n"
    "v_mfma_f32_16x16x32_bf16 v[132:135], a[236:239], a[176:179], v[132:135]\n"
    "v_mfma_f32_16x16x32_bf16 v[164:167], a[236:239], a[180:183], v[164:167]\n"
    "v_mfma_f32_16x16x32_bf16 v[164:167], a[232:235], a[148:151], v[164:167]\n"
    "s_addk_i32 m0, 1024\n"
    "s_addk_i32 s54, 16384\n"
    "v_mfma_f32_16x16x32_bf16 v[196:199], a[232:235], a[152:155], v[196:199]\n"
    "buffer_load_dwordx4 v254, s[72:75], s54 offen  lds\n"
    "v_mfma_f32_16x16x32_bf16 v[196:199], a[236:239], a[184:187], v[196:199]\n"
    "v_mfma_f32_16x16x32_bf16 v[228:231], a[236:239], a[188:191], v[228:231]\n"
    "v_mfma_f32_16x16x32_bf16 v[228:231], a[232:235], a[156:159], v[228:231]\n"
    "s_waitcnt vmcnt(8)\n"
    "ds_read_b64_tr_b16 a[208:209], v252 offset:32768\n"
    "ds_read_b64_tr_b16 a[210:211], v252 offset:33280\n"
    "v_mfma_f32_16x16x32_bf16 v[232:235], a[192:195], a[156:159], v[232:235]\n"
    "s_addk_i32 m0, 1024\n"
    "s_addk_i32 s54, 16384\n"
    "v_mfma_f32_16x16x32_bf16 v[232:235], a[196:199], a[188:191], v[232:235]\n"
    "buffer_load_dwordx4 v254, s[72:75], s54 offen  lds\n"
    "v_mfma_f32_16x16x32_bf16 v[200:203], a[196:199], a[184:187], v[200:203]\n"
    "ds_read_b64_tr_b16 a[212:213], v252 offset:36864\n"
    "ds_read_b64_tr_b16 a[214:215], v252 offset:37376\n"
    "v_mfma_f32_16x16x32_bf16 v[200:203], a[192:195], a[152:155], v[200:203]\n"
    "ds_read_b64_tr_b16 a[216:217], v252 offset:32776\n"
    "ds_read_b64_tr_b16 a[218:219], v252 offset:33288\n"
    "v_mfma_f32_16x16x32_bf16 v[168:171], a[192:195], a[148:151], v[168:171]\n"
    "s_xor_b32 s59, s66, s67\n"
    "s_addk_i32 s67, 128\n"
    "s_cmpk_eq_u32 s67, 1536\n"
    "v_mfma_f32_16x16x32_bf16 v[168:171], a[196:199], a[180:183], v[168:171]\n"
    "s_cselect_b32 s67, 0, s67\n"
    "s_add_u32 m0, s68, 98304\n"
    "s_nop 0\n"
    "v_mfma_f32_16x16x32_bf16 v[136:139], a[196:199], a[176:179], v[136:139]\n"
    "buffer_load_dwordx4 v255, s[24:27], s59 offen  lds\n"
    "v_mfma_f32_16x16x32_bf16 v[136:139], a[192:195], a[144:147], v[136:139]\n"
    "ds_read_b64_tr_b16 a[220:221], v252 offset:36872\n"
    "ds_read_b64_tr_b16 a[222:223], v252 offset:37384\n"
    "v_mfma_f32_16x16x32_bf16 v[104:107], a[192:195], a[140:143], v[104:107]\n"
    "ds_read_b64_tr_b16 a[224:225], v253 offset:32768\n"
    "ds_read_b64_tr_b16 a[226:227], v253 offset:33280\n"
    "v_mfma_f32_16x16x32_bf16 v[104:107], a[196:199], a[172:175], v[104:107]\n"
    "s_add_u32 m0, s68, 99328\n"
    "s_addk_i32 s59, 16384\n"
    "v_mfma_f32_16x16x32_bf16 v[72:75], a[196:199], a[168:171], v[72:75]\n"
    "buffer_load_dwordx4 v243, s[24:27], s59 offen  lds\n"
    "v_mfma_f32_16x16x32_bf16 v[72:75], a[192:195], a[136:139], v[72:75]\n"
    "ds_read_b64_tr_b16 a[228:229], v253 offset:36864\n"
    "ds_read_b64_tr_b16 a[230:231], v253 offset:37376\n"
    "v_mfma_f32_16x16x32_bf16 v[40:43], a[192:195], a[132:135], v[40:43]\n"
    "ds_read_b64_tr_b16 a[232:233], v253 offset:32776\n"
    "ds_read_b64_tr_b16 a[234:235], v253 offset:33288\n"
    "v_mfma_f32_16x16x32_bf16 v[40:43], a[196:199], a[164:167], v[40:43]\n"
    "s_add_u32 m0, s68, 100352\n"
    "s_addk_i32 s59, 16384\n"
    "v_mfma_f32_16x16x32_bf16 v[8:11], a[196:199], a[160:163], v[8:11]\n"
    "buffer_load_dwordx4 v255, s[24:27], s59 offen  lds\n"
    "v_mfma_f32_16x16x32_bf16 v[8:11], a[192:195], a[128:131], v[8:11]\n"
    "ds_read_b64_tr_b16 a[236:237], v253 offset:36872\n"
    "ds_read_b64_tr_b16 a[238:239], v253 offset:37384\n"
    "v_mfma_f32_16x16x32_bf16 v[12:15], a[200:203], a[128:131], v[12:15]\n"
    "s_add_u32 m0, s68, 101376\n"
    "s_addk_i32 s59, 16384\n"
    "v_mfma_f32_16x16x32_bf16 v[12:15], a[204:207], a[160:163], v[12:15]\n"
    "buffer_load_dwordx4 v243, s[24:27], s59 offen  lds\n"
    "v_mfma_f32_16x16x32_bf16 v[44:47], a[204:207], a[164:167], v[44:47]\n"
    "v_mfma_f32_16x16x32_bf16 v[44:47], a[200:203], a[132:135], v[44:47]\n"
    "s_add_u32 m0, s79, 0\n"
    "s_lshl_b32 s54, s64, 17\n"
    "s_xor_b32 s54, s54, s57\n"
    "v_mfma_f32_16x16x32_bf16 v[76:79], a[200:203], a[136:139], v[76:79]\n"
    "s_addk_i32 s54, 512\n"
    "v_mfma_f32_16x16x32_bf16 v[76:79], a[204:207], a[168:171], v[76:79]\n"
    "buffer_load_dwordx4 v254, s[72:75], s54 offen  lds\n"
    "v_mfma_f32_16x16x32_bf16 v[108:111], a[204:207], a[172:175], v[108:111]\n"
    "v_mfma_f32_16x16x32_bf16 v[108:111], a[200:203], a[140:143], v[108:111]\n"
    "s_addk_i32 m0, 1024\n"
    "s_addk_i32 s54, 16384\n"
    "v_mfma_f32_16x16x32_bf16 v[140:143], a[200:203], a[144:147], v[140:143]\n"
    "buffer_load_dwordx4 v254, s[72:75], s54 offen  lds\n"
    "v_mfma_f32_16x16x32_bf16 v[140:143], a[204:207], a[176:179], v[140:143]\n"
    "v_mfma_f32_16x16x32_bf16 v[172:175], a[204:207], a[180:183], v[172:175]\n"
    "v_mfma_f32_16x16x32_bf16 v[172:175], a[200:203], a[148:151], v[172:175]\n"
    "s_addk_i32 m0, 1024\n"
    "s_addk_i32 s54, 16384\n"
    "v_mfma_f32_16x16x32_bf16 v[204:207], a[200:203], a[152:155], v[204:207]\n"
    "buffer_load_dwordx4 v254, s[72:75], s54 offen  lds\n"
    "v_mfma_f32_16x16x32_bf16 v[204:207], a[204:207], a[184:187], v[204:207]\n"
    "v_mfma_f32_16x16x32_bf16 v[236:239], a[204:207], a[188:191], v[236:239]\n"
    "v_mfma_f32_16x16x32_bf16 v[236:239], a[200:203], a[156:159], v[236:239]\n"
    "s_waitcnt lgkmcnt(12)\n"
    "s_addk_i32 m0, 1024\n"
    "s_addk_i32 s54, 16384\n"
    "v_mfma_f32_16x16x32_bf16 a[240:243], a[208:211], a[156:159], a[240:243]\n"
    "buffer_load_dwordx4 v254, s[72:75], s54 offen  lds\n"
    "v_mfma_f32_16x16x32_bf16 a[240:243], a[212:215], a[188:191], a[240:243]\n"
    "v_mfma_f32_16x16x32_bf16 v[208:211], a[212:215], a[184:187], v[208:211]\n"
    "v_mfma_f32_16x16x32_bf16 v[208:211], a[208:211], a[152:155], v[208:211]\n"
    "s_addk_i32 m0, 1024\n"
    "s_addk_i32 s54, 16384\n"
    "v_mfma_f32_16x16x32_bf16 v[176:179], a[208:211], a[148:151], v[176:179]\n"
    "buffer_load_dwordx4 v254, s[72:75], s54 offen  lds\n"
    "v_mfma_f32_16x16x32_bf16 v[176:179], a[212:215], a[180:183], v[176:179]\n"
    "v_mfma_f32_16x16x32_bf16 v[144:147], a[212:215], a[176:179], v[144:147]\n"
    "v_mfma_f32_16x16x32_bf16 v[144:147], a[208:211], a[144:147], v[144:147]\n"
    "s_addk_i32 m0, 1024\n"
    "s_addk_i32 s54, 16384\n"
    "v_mfma_f32_16x16x32_bf16 v[112:115], a[208:211], a[140:143], v[112:115]\n"
    "buffer_load_dwordx4 v254, s[72:75], s54 offen  lds\n"
    "v_mfma_f32_16x16x32_bf16 v[112:115], a[212:215], a[172:175], v[112:115]\n"
    "v_mfma_f32_16x16x32_bf16 v[80:83], a[212:215], a[168:171], v[80:83]\n"
    "v_mfma_f32_16x16x32_bf16 v[80:83], a[208:211], a[136:139], v[80:83]\n"
    "s_addk_i32 m0, 1024\n"
    "s_addk_i32 s54, 16384\n"
    "v_mfma_f32_16x16x32_bf16 v[48:51], a[208:211], a[132:135], v[48:51]\n"
    "buffer_load_dwordx4 v254, s[72:75], s54 offen  lds\n"
    "v_mfma_f32_16x16x32_bf16 v[48:51], a[212:215], a[164:167], v[48:51]\n"
    "v_mfma_f32_16x16x32_bf16 v[16:19], a[212:215], a[160:163], v[16:19]\n"
    "v_mfma_f32_16x16x32_bf16 v[16:19], a[208:211], a[128:131], v[16:19]\n"
    "s_waitcnt lgkmcnt(8)\n"
    "s_addk_i32 m0, 1024\n"
    "s_addk_i32 s54, 16384\n"
    "v_mfma_f32_16x16x32_bf16 v[20:23], a[216:219], a[128:131], v[20:23]\n"
    "buffer_load_dwordx4 v254, s[72:75], s54 offen  lds\n"
    "v_mfma_f32_16x16x32_bf16 v[20:23], a[220:223], a[160:163], v[20:23]\n"
    "s_add_u32 s57, s57, 131072\n"
    "s_cmp_ge_u32 s57, 1572864\n"
    "s_cselect_b32 s57, s70, s57\n"
    "v_mfma_f32_16x16x32_bf16 v[52:55], a[220:223], a[164:167], v[52:55]\n"
    "v_mfma_f32_16x16x32_bf16 v[52:55], a[216:219], a[132:135], v[52:55]\n"
    "s_waitcnt lgkmcnt(0)\n"
    "s_add_u32 m0, s79, 32768\n"
    "s_lshl_b32 s54, s64, 17\n"
    "v_mfma_f32_16x16x32_bf16 v[84:87], a[216:219], a[136:139], v[84:87]\n"
    "s_xor_b32 s54, s54, s57\n"
    "v_mfma_f32_16x16x32_bf16 v[84:87], a[220:223], a[168:171], v[84:87]\n"
    "buffer_load_dwordx4 v254, s[72:75], s54 offen  lds\n"
    "v_mfma_f32_16x16x32_bf16 v[116:119], a[220:223], a[172:175], v[116:119]\n"
    "v_mfma_f32_16x16x32_bf16 v[116:119], a[216:219], a[140:143], v[116:119]\n"
    "s_addk_i32 m0, 1024\n"
    "s_addk_i32 s54, 16384\n"
    "v_mfma_f32_16x16x32_bf16 v[148:151], a[216:219], a[144:147], v[148:151]\n"
    "buffer_load_dwordx4 v254, s[72:75], s54 offen  lds\n"
    "v_mfma_f32_16x16x32_bf16 v[148:151], a[220:223], a[176:179], v[148:151]\n"
    "v_mfma_f32_16x16x32_bf16 v[180:183], a[220:223], a[180:183], v[180:183]\n"
    "v_mfma_f32_16x16x32_bf16 v[180:183], a[216:219], a[148:151], v[180:183]\n"
    "s_addk_i32 m0, 1024\n"
    "s_addk_i32 s54, 16384\n"
    "v_mfma_f32_16x16x32_bf16 v[212:215], a[216:219], a[152:155], v[212:215]\n"
    "buffer_load_dwordx4 v254, s[72:75], s54 offen  lds\n"
    "v_mfma_f32_16x16x32_bf16 v[212:215], a[220:223], a[184:187], v[212:215]\n"
    "v_mfma_f32_16x16x32_bf16 a[244:247], a[220:223], a[188:191], a[244:247]\n"
    "v_mfma_f32_16x16x32_bf16 a[244:247], a[216:219], a[156:159], a[244:247]\n"
    "v_add_u32 v252, 0x20000, v252\n"
    "v_add_u32 v253, 0x20000, v253\n"
    "v_mfma_f32_16x16x32_bf16 a[248:251], a[224:227], a[156:159], a[248:251]\n"
    "s_waitcnt vmcnt(15)\n"
    "ds_read_b64_tr_b16 a[192:193], v252 offset:0\n"
    "ds_read_b64_tr_b16 a[194:195], v252 offset:512\n"
    "v_mfma_f32_16x16x32_bf16 a[248:251], a[228:231], a[188:191], a[248:251]\n"
    "s_addk_i32 m0, 1024\n"
    "s_addk_i32 s54, 16384\n"
    "v_mfma_f32_16x16x32_bf16 v[216:219], a[228:231], a[184:187], v[216:219]\n"
    "buffer_load_dwordx4 v254, s[72:75], s54 offen  lds\n"
    "v_mfma_f32_16x16x32_bf16 v[216:219], a[224:227], a[152:155], v[216:219]\n"
    "ds_read_b64_tr_b16 a[196:197], v252 offset:4096\n"
    "ds_read_b64_tr_b16 a[198:199], v252 offset:4608\n"
    "v_mfma_f32_16x16x32_bf16 v[184:187], a[224:227], a[148:151], v[184:187]\n"
    "ds_read_b64_tr_b16 a[200:201], v252 offset:8\n"
    "ds_read_b64_tr_b16 a[202:203], v252 offset:520\n"
    "v_mfma_f32_16x16x32_bf16 v[184:187], a[228:231], a[180:183], v[184:187]\n"
    "s_addk_i32 m0, 1024\n"
    "s_addk_i32 s54, 16384\n"
    "v_mfma_f32_16x16x32_bf16 v[152:155], a[228:231], a[176:179], v[152:155]\n"
    "buffer_load_dwordx4 v254, s[72:75], s54 offen  lds\n"
    "v_mfma_f32_16x16x32_bf16 v[152:155], a[224:227], a[144:147], v[152:155]\n"
    "ds_read_b64_tr_b16 a[204:205], v252 offset:4104\n"
    "ds_read_b64_tr_b16 a[206:207], v252 offset:4616\n"
    "v_mfma_f32_16x16x32_bf16 v[120:123], a[224:227], a[140:143], v[120:123]\n"
    "ds_read_b64_tr_b16 a[208:209], v253 offset:0\n"
    "ds_read_b64_tr_b16 a[210:211], v253 offset:512\n"
    "v_mfma_f32_16x16x32_bf16 v[120:123], a[228:231], a[172:175], v[120:123]\n"
    "s_addk_i32 m0, 1024\n"
    "s_addk_i32 s54, 16384\n"
    "v_mfma_f32_16x16x32_bf16 v[88:91], a[228:231], a[168:171], v[88:91]\n"
    "buffer_load_dwordx4 v254, s[72:75], s54 offen  lds\n"
    "v_mfma_f32_16x16x32_bf16 v[88:91], a[224:227], a[136:139], v[88:91]\n"
    "ds_read_b64_tr_b16 a[212:213], v253 offset:4096\n"
    "ds_read_b64_tr_b16 a[214:215], v253 offset:4608\n"
    "v_mfma_f32_16x16x32_bf16 v[56:59], a[224:227], a[132:135], v[56:59]\n"
    "ds_read_b64_tr_b16 a[216:217], v253 offset:8\n"
    "ds_read_b64_tr_b16 a[218:219], v253 offset:520\n"
    "v_mfma_f32_16x16x32_bf16 v[56:59], a[228:231], a[164:167], v[56:59]\n"
    "s_addk_i32 m0, 1024\n"
    "s_addk_i32 s54, 16384\n"
    "buffer_load_dwordx4 v254, s[72:75], s54 offen  lds\n"
    "s_barrier\n"
    "v_mfma_f32_16x16x32_bf16 v[24:27], a[228:231], a[160:163], v[24:27]\n"
    "ds_read_b64_tr_b16 a[220:221], v253 offset:4104\n"
    "ds_read_b64_tr_b16 a[222:223], v253 offset:4616\n"
    "v_mfma_f32_16x16x32_bf16 v[24:27], a[224:227], a[128:131], v[24:27]\n"
    "s_addk_i32 m0, 1024\n"
    "s_addk_i32 s54, 16384\n"
    "v_mfma_f32_16x16x32_bf16 v[28:31], a[232:235], a[128:131], v[28:31]\n"
    "buffer_load_dwordx4 v254, s[72:75], s54 offen  lds\n"
    "v_mfma_f32_16x16x32_bf16 v[28:31], a[236:239], a[160:163], v[28:31]\n"
    "v_mfma_f32_16x16x32_bf16 v[60:63], a[236:239], a[164:167], v[60:63]\n"
    "ds_read_b128 a[128:131], v241 offset:16384\n"
    "v_mfma_f32_16x16x32_bf16 v[60:63], a[232:235], a[132:135], v[60:63]\n"
    "s_waitcnt lgkmcnt(1)\n"
    "s_add_u32 m0, s79, 131072\n"
    "s_lshl_b32 s54, s64, 17\n"
    "v_mfma_f32_16x16x32_bf16 v[92:95], a[232:235], a[136:139], v[92:95]\n"
    "s_xor_b32 s54, s54, s57\n"
    "s_addk_i32 s54, 512\n"
    "v_mfma_f32_16x16x32_bf16 v[92:95], a[236:239], a[168:171], v[92:95]\n"
    "buffer_load_dwordx4 v254, s[72:75], s54 offen  lds\n"
    "v_mfma_f32_16x16x32_bf16 v[124:127], a[236:239], a[172:175], v[124:127]\n"
    "ds_read_b128 a[160:163], v242 offset:16384\n"
    "ds_read_b128 a[164:167], v242 offset:18432\n"
    "v_mfma_f32_16x16x32_bf16 v[124:127], a[232:235], a[140:143], v[124:127]\n"
    "ds_read_b128 a[132:135], v241 offset:18432\n"
    "ds_read_b128 a[136:139], v241 offset:20480\n"
    "v_mfma_f32_16x16x32_bf16 v[156:159], a[232:235], a[144:147], v[156:159]\n"
    "s_addk_i32 m0, 1024\n"
    "s_addk_i32 s54, 16384\n"
    "v_mfma_f32_16x16x32_bf16 v[156:159], a[236:239], a[176:179], v[156:159]\n"
    "buffer_load_dwordx4 v254, s[72:75], s54 offen  lds\n"
    "v_mfma_f32_16x16x32_bf16 v[188:191], a[236:239], a[180:183], v[188:191]\n"
    "ds_read_b128 a[168:171], v242 offset:20480\n"
    "ds_read_b128 a[172:175], v242 offset:22528\n"
    "v_mfma_f32_16x16x32_bf16 v[188:191], a[232:235], a[148:151], v[188:191]\n"
    "ds_read_b128 a[140:143], v241 offset:22528\n"
    "ds_read_b128 a[144:147], v241 offset:24576\n"
    "v_mfma_f32_16x16x32_bf16 v[220:223], a[232:235], a[152:155], v[220:223]\n"
    "s_addk_i32 m0, 1024\n"
    "s_addk_i32 s54, 16384\n"
    "v_mfma_f32_16x16x32_bf16 v[220:223], a[236:239], a[184:187], v[220:223]\n"
    "buffer_load_dwordx4 v254, s[72:75], s54 offen  lds\n"
    "v_mfma_f32_16x16x32_bf16 a[252:255], a[236:239], a[188:191], a[252:255]\n"
    "ds_read_b128 a[176:179], v242 offset:24576\n"
    "ds_read_b128 a[180:183], v242 offset:26624\n"
    "v_mfma_f32_16x16x32_bf16 a[252:255], a[232:235], a[156:159], a[252:255]\n"
    "s_waitcnt lgkmcnt(9)\n"
    "ds_read_b128 a[148:151], v241 offset:26624\n"
    "ds_read_b128 a[152:155], v241 offset:28672\n"
    "v_mfma_f32_16x16x32_bf16 v[0:3], a[192:195], a[128:131], v[0:3]\n"
    "s_addk_i32 m0, 1024\n"
    "s_addk_i32 s54, 16384\n"
    "v_mfma_f32_16x16x32_bf16 v[0:3], a[196:199], a[160:163], v[0:3]\n"
    "s_waitcnt lgkmcnt(9)\n"
    "v_mfma_f32_16x16x32_bf16 v[32:35], a[196:199], a[164:167], v[32:35]\n"
    "buffer_load_dwordx4 v254, s[72:75], s54 offen  lds\n"
    "ds_read_b128 a[184:187], v242 offset:28672\n"
    "ds_read_b128 a[188:191], v242 offset:30720\n"
    "ds_read_b128 a[156:159], v241 offset:30720\n"
    "v_mfma_f32_16x16x32_bf16 v[32:35], a[192:195], a[132:135], v[32:35]\n"
    "s_waitcnt lgkmcnt(10)\n"
    "s_add_u32 s82, s76, 8\n"
    "s_and_b32 s82, s82, s86\n"
    "v_mfma_f32_16x16x32_bf16 v[64:67], a[192:195], a[136:139], v[64:67]\n"
    "buffer_store_dwordx2 v[244:245], v240, s[80:83], s76 offen sc1\n"
    "v_mfma_f32_16x16x32_bf16 v[64:67], a[196:199], a[168:171], v[64:67]\n"
    "s_waitcnt lgkmcnt(8)\n"
    "s_addk_i32 m0, 1024\n"
    "s_addk_i32 s54, 16384\n"
    "v_mfma_f32_16x16x32_bf16 v[96:99], a[196:199], a[172:175], v[96:99]\n"
    "buffer_load_dwordx4 v254, s[72:75], s54 offen  lds\n"
    "v_mfma_f32_16x16x32_bf16 v[96:99], a[192:195], a[140:143], v[96:99]\n"
    "s_waitcnt lgkmcnt(6)\n"
    "s_addk_i32 m0, 1024\n"
    "s_addk_i32 s54, 16384\n"
    "v_mfma_f32_16x16x32_bf16 v[128:131], a[192:195], a[144:147], v[128:131]\n"
    "buffer_load_dwordx4 v254, s[72:75], s54 offen  lds\n"
    "v_mfma_f32_16x16x32_bf16 v[128:131], a[196:199], a[176:179], v[128:131]\n"
    "s_waitcnt lgkmcnt(4)\n"
    "v_mfma_f32_16x16x32_bf16 v[160:163], a[196:199], a[180:183], v[160:163]\n"
    "v_mfma_f32_16x16x32_bf16 v[160:163], a[192:195], a[148:151], v[160:163]\n"
    "s_waitcnt lgkmcnt(2)\n"
    "s_addk_i32 m0, 1024\n"
    "s_addk_i32 s54, 16384\n"
    "v_mfma_f32_16x16x32_bf16 v[192:195], a[192:195], a[152:155], v[192:195]\n"
    "buffer_load_dwordx4 v254, s[72:75], s54 offen  lds\n"
    "v_mfma_f32_16x16x32_bf16 v[192:195], a[196:199], a[184:187], v[192:195]\n"
    "s_waitcnt lgkmcnt(0)\n"
    "v_mfma_f32_16x16x32_bf16 v[224:227], a[196:199], a[188:191], v[224:227]\n"
    "v_mfma_f32_16x16x32_bf16 v[224:227], a[192:195], a[156:159], v[224:227]\n"
    "s_addk_i32 m0, 1024\n"
    "s_addk_i32 s54, 16384\n"
    "v_mfma_f32_16x16x32_bf16 v[228:231], a[200:203], a[156:159], v[228:231]\n"
    "buffer_load_dwordx4 v254, s[72:75], s54 offen  lds\n"
    "v_mfma_f32_16x16x32_bf16 v[228:231], a[204:207], a[188:191], v[228:231]\n"
    "s_add_u32 s57, s57, 131072\n"
    "s_cmp_ge_u32 s57, 1572864\n"
    "s_cselect_b32 s57, s70, s57\n"
    "v_mfma_f32_16x16x32_bf16 v[196:199], a[204:207], a[184:187], v[196:199]\n"
    "v_mfma_f32_16x16x32_bf16 v[196:199], a[200:203], a[152:155], v[196:199]\n"
    "v_mfma_f32_16x16x32_bf16 v[164:167], a[200:203], a[148:151], v[164:167]\n"
    "v_mfma_f32_16x16x32_bf16 v[164:167], a[204:207], a[180:183], v[164:167]\n"
    "v_mfma_f32_16x16x32_bf16 v[132:135], a[204:207], a[176:179], v[132:135]\n"
    "s_xor_b32 s59, s66, s67\n"
    "s_addk_i32 s67, 128\n"
    "s_cmpk_eq_u32 s67, 1536\n"
    "v_mfma_f32_16x16x32_bf16 v[132:135], a[200:203], a[144:147], v[132:135]\n"
    "s_cselect_b32 s67, 0, s67\n"
    "s_add_u32 m0, s68, 65536\n"
    "s_nop 0\n"
    "v_mfma_f32_16x16x32_bf16 v[100:103], a[200:203], a[140:143], v[100:103]\n"
    "buffer_load_dwordx4 v255, s[24:27], s59 offen  lds\n"
    "s_bitcmp1_b32 s85, 4\n"
    "s_cbranch_scc1 CP4_%=\n"
    "CR4_%=:\n"
    "v_mfma_f32_16x16x32_bf16 v[100:103], a[204:207], a[172:175], v[100:103]\n"
    "s_add_u32 m0, s68, 66560\n"
    "s_addk_i32 s59, 16384\n"
    "v_mfma_f32_16x16x32_bf16 v[68:71], a[204:207], a[168:171], v[68:71]\n"
    "buffer_load_dwordx4 v243, s[24:27], s59 offen  lds\n"
    "v_mfma_f32_16x16x32_bf16 v[68:71], a[200:203], a[136:139], v[68:71]\n"
    "v_mfma_f32_16x16x32_bf16 v[36:39], a[200:203], a[132:135], v[36:39]\n"
    "s_add_u32 m0, s68, 67584\n"
    "s_addk_i32 s59, 16384\n"
    "v_mfma_f32_16x16x32_bf16 v[36:39], a[204:207], a[164:167], v[36:39]\n"
    "buffer_load_dwordx4 v255, s[24:27], s59 offen  lds\n"
    "v_mfma_f32_16x16x32_bf16 v[4:7], a[204:207], a[160:163], v[4:7]\n"
    "v_mfma_f32_16x16x32_bf16 v[4:7], a[200:203], a[128:131], v[4:7]\n"
    "v_add_u32 v252, 0xfffe0000, v252\n"
    "v_add_u32 v253, 0xfffe0000, v253\n"
    "v_mfma_f32_16x16x32_bf16 v[8:11], a[208:211], a[128:131], v[8:11]\n"
    "s_waitcnt vmcnt(20)\n"
    "ds_read_b64_tr_b16 a[224:225], v252 offset:0\n"
    "ds_read_b64_tr_b16 a[226:227], v252 offset:512\n"
    "v_mfma_f32_16x16x32_bf16 v[8:11], a[212:215], a[160:163], v[8:11]\n"
    "s_add_u32 m0, s68, 68608\n"
    "s_addk_i32 s59, 16384\n"
    "v_mfma_f32_16x16x32_bf16 v[40:43], a[212:215], a[164:167], v[40:43]\n"
    "buffer_load_dwordx4 v243, s[24:27], s59 offen  lds\n"
    "v_mfma_f32_16x16x32_bf16 v[40:43], a[208:211], a[132:135], v[40:43]\n"
    "ds_read_b64_tr_b16 a[228:229], v252 offset:4096\n"
    "ds_read_b64_tr_b16 a[230:231], v252 offset:4608\n"
    "v_mfma_f32_16x16x32_bf16 v[72:75], a[208:211], a[136:139], v[72:75]\n"
    "s_add_u32 m0, s68, 114688\n"
    "s_add_u32 s60, s78, 4194304\n"
    "v_mfma_f32_16x16x32_bf16 v[72:75], a[212:215], a[168:171], v[72:75]\n"
    "buffer_load_dwordx4 v254, s[72:75], s60 offen nt lds\n"
    "v_mfma_f32_16x16x32_bf16 v[104:107], a[212:215], a[172:175], v[104:107]\n"
    "ds_read_b64_tr_b16 a[232:233], v252 offset:8\n"
    "ds_read_b64_tr_b16 a[234:235], v252 offset:520\n"
    "v_mfma_f32_16x16x32_bf16 v[104:107], a[208:211], a[140:143], v[104:107]\n"
    "ds_read_b64_tr_b16 a[236:237], v252 offset:4104\n"
    "ds_read_b64_tr_b16 a[238:239], v252 offset:4616\n"
    "v_mfma_f32_16x16x32_bf16 v[136:139], a[208:211], a[144:147], v[136:139]\n"
    "s_add_u32 m0, s68, 115712\n"
    "s_add_u32 s60, s78, 4210688\n"
    "v_mfma_f32_16x16x32_bf16 v[136:139], a[212:215], a[176:179], v[136:139]\n"
    "buffer_load_dwordx4 v254, s[72:75], s60 offen nt lds\n"
    "v_mfma_f32_16x16x32_bf16 v[168:171], a[212:215], a[180:183], v[168:171]\n"
    "ds_read_b64_tr_b16 a[192:193], v253 offset:0\n"
    "ds_read_b64_tr_b16 a[194:195], v253 offset:512\n"
    "v_mfma_f32_16x16x32_bf16 v[168:171], a[208:211], a[148:151], v[168:171]\n"
    "s_add_u32 m0, s68, 116736\n"
    "s_add_u32 s60, s78, 4227072\n"
    "v_mfma_f32_16x16x32_bf16 v[200:203], a[208:211], a[152:155], v[200:203]\n"
    "buffer_load_dwordx4 v254, s[72:75], s60 offen nt lds\n"
    "ds_read_b64_tr_b16 a[196:197], v253 offset:4096\n"
    "ds_read_b64_tr_b16 a[198:199], v253 offset:4608\n"
    "v_mfma_f32_16x16x32_bf16 v[200:203], a[212:215], a[184:187], v[200:203]\n"
    "ds_read_b64_tr_b16 a[200:201], v253 offset:8\n"
    "ds_read_b64_tr_b16 a[202:203], v253 offset:520\n"
    "v_mfma_f32_16x16x32_bf16 v[232:235], a[212:215], a[188:191], v[232:235]\n"
    "s_add_u32 m0, s68, 117760\n"
    "s_add_u32 s60, s78, 4243456\n"
    "v_mfma_f32_16x16x32_bf16 v[232:235], a[208:211], a[156:159], v[232:235]\n"
    "buffer_load_dwordx4 v254, s[72:75], s60 offen nt lds\n"
    "v_mfma_f32_16x16x32_bf16 v[236:239], a[216:219], a[156:159], v[236:239]\n"
    "ds_read_b64_tr_b16 a[204:205], v253 offset:4104\n"
    "ds_read_b64_tr_b16 a[206:207], v253 offset:4616\n"
    "v_mfma_f32_16x16x32_bf16 v[236:239], a[220:223], a[188:191], v[236:239]\n"
    "v_mfma_f32_16x16x32_bf16 v[204:207], a[220:223], a[184:187], v[204:207]\n"
    "v_mfma_f32_16x16x32_bf16 v[204:207], a[216:219], a[152:155], v[204:207]\n"
    "v_mfma_f32_16x16x32_bf16 v[172:175], a[216:219], a[148:151], v[172:175]\n"
    "v_mfma_f32_16x16x32_bf16 v[172:175], a[220:223], a[180:183], v[172:175]\n"
    "v_mfma_f32_16x16x32_bf16 v[140:143], a[220:223], a[176:179], v[140:143]\n"
    "v_mfma_f32_16x16x32_bf16 v[140:143], a[216:219], a[144:147], v[140:143]\n"
    "v_mfma_f32_16x16x32_bf16 v[108:111], a[216:219], a[140:143], v[108:111]\n"
    "v_mfma_f32_16x16x32_bf16 v[108:111], a[220:223], a[172:175], v[108:111]\n"
    "v_mfma_f32_16x16x32_bf16 v[76:79], a[220:223], a[168:171], v[76:79]\n"
    "v_mfma_f32_16x16x32_bf16 v[76:79], a[216:219], a[136:139], v[76:79]\n"
    "v_mfma_f32_16x16x32_bf16 v[44:47], a[216:219], a[132:135], v[44:47]\n"
    "v_mfma_f32_16x16x32_bf16 v[44:47], a[220:223], a[164:167], v[44:47]\n"
    "v_mfma_f32_16x16x32_bf16 v[12:15], a[220:223], a[160:163], v[12:15]\n"
    "v_mfma_f32_16x16x32_bf16 v[12:15], a[216:219], a[128:131], v[12:15]\n"
    "s_waitcnt lgkmcnt(12)\n"
    "s_lshr_b32 s60, %3, 2\n"
    "s_lshl_b32 s60, s60, 21\n"
    "v_mfma_f32_16x16x32_bf16 v[16:19], a[224:227], a[128:131], v[16:19]\n"
    "s_and_b32 s61, %3, 3\n"
    "s_lshl_b32 s61, s61, 15\n"
    "s_add_u32 s60, s60, s61\n"
    "v_mfma_f32_16x16x32_bf16 v[16:19], a[228:231], a[160:163], v[16:19]\n"
    "s_mul_i32 s61, s65, 0x2400\n"
    "s_and_b32 s61, s61, 0x6400\n"
    "s_add_u32 s60, s60, s61\n"
    "v_mfma_f32_16x16x32_bf16 v[48:51], a[228:231], a[164:167], v[48:51]\n"
    "buffer_store_dwordx4 v[240:243], v240, s[32:35], s60 offen sc1\n"
    "s_xor_b32 s60, s60, 8192\n"
    "v_mfma_f32_16x16x32_bf16 v[48:51], a[224:227], a[132:135], v[48:51]\n"
    "buffer_store_dwordx4 v[240:243], v240, s[32:35], s60 offen sc1\n"
    "v_mfma_f32_16x16x32_bf16 v[80:83], a[224:227], a[136:139], v[80:83]\n"
    "v_mfma_f32_16x16x32_bf16 v[80:83], a[228:231], a[168:171], v[80:83]\n"
    "v_mfma_f32_16x16x32_bf16 v[112:115], a[228:231], a[172:175], v[112:115]\n"
    "v_mfma_f32_16x16x32_bf16 v[112:115], a[224:227], a[140:143], v[112:115]\n"
    "v_mfma_f32_16x16x32_bf16 v[144:147], a[224:227], a[144:147], v[144:147]\n"
    "v_mfma_f32_16x16x32_bf16 v[144:147], a[228:231], a[176:179], v[144:147]\n"
    "v_mfma_f32_16x16x32_bf16 v[176:179], a[228:231], a[180:183], v[176:179]\n"
    "v_mfma_f32_16x16x32_bf16 v[176:179], a[224:227], a[148:151], v[176:179]\n"
    "v_mfma_f32_16x16x32_bf16 v[208:211], a[224:227], a[152:155], v[208:211]\n"
    "v_mfma_f32_16x16x32_bf16 v[208:211], a[228:231], a[184:187], v[208:211]\n"
    "v_mfma_f32_16x16x32_bf16 a[240:243], a[228:231], a[188:191], a[240:243]\n"
    "v_mfma_f32_16x16x32_bf16 a[240:243], a[224:227], a[156:159], a[240:243]\n"
    "s_waitcnt lgkmcnt(8)\n"
    "v_mfma_f32_16x16x32_bf16 a[244:247], a[232:235], a[156:159], a[244:247]\n"
    "v_mfma_f32_16x16x32_bf16 a[244:247], a[236:239], a[188:191], a[244:247]\n"
    "v_mfma_f32_16x16x32_bf16 v[212:215], a[236:239], a[184:187], v[212:215]\n"
    "v_mfma_f32_16x16x32_bf16 v[212:215], a[232:235], a[152:155], v[212:215]\n"
    "v_mfma_f32_16x16x32_bf16 v[180:183], a[232:235], a[148:151], v[180:183]\n"
    "v_mfma_f32_16x16x32_bf16 v[180:183], a[236:239], a[180:183], v[180:183]\n"
    "v_mfma_f32_16x16x32_bf16 v[148:151], a[236:239], a[176:179], v[148:151]\n"
    "v_mfma_f32_16x16x32_bf16 v[148:151], a[232:235], a[144:147], v[148:151]\n"
    "v_mfma_f32_16x16x32_bf16 v[116:119], a[232:235], a[140:143], v[116:119]\n"
    "v_mfma_f32_16x16x32_bf16 v[116:119], a[236:239], a[172:175], v[116:119]\n"
    "v_mfma_f32_16x16x32_bf16 v[84:87], a[236:239], a[168:171], v[84:87]\n"
    "v_mfma_f32_16x16x32_bf16 v[84:87], a[232:235], a[136:139], v[84:87]\n"
    "v_mfma_f32_16x16x32_bf16 v[52:55], a[232:235], a[132:135], v[52:55]\n"
    "v_mfma_f32_16x16x32_bf16 v[52:55], a[236:239], a[164:167], v[52:55]\n"
    "v_mfma_f32_16x16x32_bf16 v[20:23], a[236:239], a[160:163], v[20:23]\n"
    "v_mfma_f32_16x16x32_bf16 v[20:23], a[232:235], a[128:131], v[20:23]\n"
    "s_waitcnt lgkmcnt(4)\n"
    "s_waitcnt vmcnt(19)\n"
    "ds_read_b64_tr_b16 a[208:209], v252 offset:32768\n"
    "v_mfma_f32_16x16x32_bf16 v[24:27], a[192:195], a[128:131], v[24:27]\n"
    "ds_read_b64_tr_b16 a[210:211], v252 offset:33280\n"
    "v_mfma_f32_16x16x32_bf16 v[24:27], a[196:199], a[160:163], v[24:27]\n"
    "ds_read_b64_tr_b16 a[212:213], v252 offset:36864\n"
    "ds_read_b64_tr_b16 a[214:215], v252 offset:37376\n"
    "v_mfma_f32_16x16x32_bf16 v[56:59], a[196:199], a[164:167], v[56:59]\n"
    "v_mfma_f32_16x16x32_bf16 v[56:59], a[192:195], a[132:135], v[56:59]\n"
    "ds_read_b64_tr_b16 a[216:217], v252 offset:32776\n"
    "ds_read_b64_tr_b16 a[218:219], v252 offset:33288\n"
    "v_mfma_f32_16x16x32_bf16 v[88:91], a[192:195], a[136:139], v[88:91]\n"
    "v_mfma_f32_16x16x32_bf16 v[88:91], a[196:199], a[168:171], v[88:91]\n"
    "ds_read_b64_tr_b16 a[220:221], v252 offset:36872\n"
    "ds_read_b64_tr_b16 a[222:223], v252 offset:37384\n"
    "v_mfma_f32_16x16x32_bf16 v[120:123], a[196:199], a[172:175], v[120:123]\n"
    "v_mfma_f32_16x16x32_bf16 v[120:123], a[192:195], a[140:143], v[120:123]\n"
    "ds_read_b64_tr_b16 a[224:225], v253 offset:32768\n"
    "ds_read_b64_tr_b16 a[226:227], v253 offset:33280\n"
    "v_mfma_f32_16x16x32_bf16 v[152:155], a[192:195], a[144:147], v[152:155]\n"
    "v_mfma_f32_16x16x32_bf16 v[152:155], a[196:199], a[176:179], v[152:155]\n"
    "ds_read_b64_tr_b16 a[228:229], v253 offset:36864\n"
    "ds_read_b64_tr_b16 a[230:231], v253 offset:37376\n"
    "v_mfma_f32_16x16x32_bf16 v[184:187], a[196:199], a[180:183], v[184:187]\n"
    "v_mfma_f32_16x16x32_bf16 v[184:187], a[192:195], a[148:151], v[184:187]\n"
    "ds_read_b64_tr_b16 a[232:233], v253 offset:32776\n"
    "ds_read_b64_tr_b16 a[234:235], v253 offset:33288\n"
    "v_mfma_f32_16x16x32_bf16 v[216:219], a[192:195], a[152:155], v[216:219]\n"
    "v_mfma_f32_16x16x32_bf16 v[216:219], a[196:199], a[184:187], v[216:219]\n"
    "s_barrier\n"
    "ds_read_b64_tr_b16 a[236:237], v253 offset:36872\n"
    "ds_read_b64_tr_b16 a[238:239], v253 offset:37384\n"
    "v_mfma_f32_16x16x32_bf16 a[248:251], a[196:199], a[188:191], a[248:251]\n"
    "v_mfma_f32_16x16x32_bf16 a[248:251], a[192:195], a[156:159], a[248:251]\n"
    "s_waitcnt lgkmcnt(15)\n"
    "v_mfma_f32_16x16x32_bf16 a[252:255], a[200:203], a[156:159], a[252:255]\n"
    "v_mfma_f32_16x16x32_bf16 a[252:255], a[204:207], a[188:191], a[252:255]\n"
    "v_mfma_f32_16x16x32_bf16 v[220:223], a[204:207], a[184:187], v[220:223]\n"
    "ds_read_b128 a[156:159], v241 offset:47104\n"
    "v_mfma_f32_16x16x32_bf16 v[220:223], a[200:203], a[152:155], v[220:223]\n"
    "ds_read_b128 a[188:191], v242 offset:47104\n"
    "v_mfma_f32_16x16x32_bf16 v[188:191], a[200:203], a[148:151], v[188:191]\n"
    "ds_read_b128 a[184:187], v242 offset:45056\n"
    "v_mfma_f32_16x16x32_bf16 v[188:191], a[204:207], a[180:183], v[188:191]\n"
    "ds_read_b128 a[152:155], v241 offset:45056\n"
    "v_mfma_f32_16x16x32_bf16 v[156:159], a[204:207], a[176:179], v[156:159]\n"
    "ds_read_b128 a[148:151], v241 offset:43008\n"
    "v_mfma_f32_16x16x32_bf16 v[156:159], a[200:203], a[144:147], v[156:159]\n"
    "ds_read_b128 a[180:183], v242 offset:43008\n"
    "v_mfma_f32_16x16x32_bf16 v[124:127], a[200:203], a[140:143], v[124:127]\n"
    "ds_read_b128 a[176:179], v242 offset:40960\n"
    "v_mfma_f32_16x16x32_bf16 v[124:127], a[204:207], a[172:175], v[124:127]\n"
    "ds_read_b128 a[144:147], v241 offset:40960\n"
    "v_mfma_f32_16x16x32_bf16 v[92:95], a[204:207], a[168:171], v[92:95]\n"
    "ds_read_b128 a[140:143], v241 offset:38912\n"
    "v_mfma_f32_16x16x32_bf16 v[92:95], a[200:203], a[136:139], v[92:95]\n"
    "s_add_u32 m0, s79, 0\n"
    "s_lshl_b32 s54, s64, 17\n"
    "s_xor_b32 s54, s54, s57\n"
    "v_mfma_f32_16x16x32_bf16 v[60:63], a[200:203], a[132:135], v[60:63]\n"
    "buffer_load_dwordx4 v254, s[72:75], s54 offen  lds\n"
    "v_mfma_f32_16x16x32_bf16 v[60:63], a[204:207], a[164:167], v[60:63]\n"
    "ds_read_b128 a[172:175], v242 offset:38912\n"
    "ds_read_b128 a[168:171], v242 offset:36864\n"
    "v_mfma_f32_16x16x32_bf16 v[28:31], a[204:207], a[160:163], v[28:31]\n"
    "ds_read_b128 a[136:139], v241 offset:36864\n"
    "ds_read_b128 a[132:135], v241 offset:34816\n"
    "v_mfma_f32_16x16x32_bf16 v[28:31], a[200:203], a[128:131], v[28:31]\n"
    "s_waitcnt lgkmcnt(11)\n"
    "s_addk_i32 m0, 1024\n"
    "s_addk_i32 s54, 16384\n"
    "v_mfma_f32_16x16x32_bf16 v[224:227], a[208:211], a[156:159], v[224:227]\n"
    "buffer_load_dwordx4 v254, s[72:75], s54 offen  lds\n"
    "v_mfma_f32_16x16x32_bf16 v[224:227], a[212:215], a[188:191], v[224:227]\n"
    "s_waitcnt lgkmcnt(9)\n"
    "ds_read_b128 a[164:167], v242 offset:34816\n"
    "ds_read_b128 a[160:163], v242 offset:32768\n"
    "v_mfma_f32_16x16x32_bf16 v[192:195], a[212:215], a[184:187], v[192:195]\n"
    "ds_read_b128 a[128:131], v241 offset:32768\n"
    "v_mfma_f32_16x16x32_bf16 v[192:195], a[208:211], a[152:155], v[192:195]\n"
    "s_waitcnt lgkmcnt(10)\n"
    "s_addk_i32 m0, 1024\n"
    "s_addk_i32 s54, 16384\n"
    "v_mfma_f32_16x16x32_bf16 v[160:163], a[208:211], a[148:151], v[160:163]\n"
    "buffer_load_dwordx4 v254, s[72:75], s54 offen  lds\n"
    "v_mfma_f32_16x16x32_bf16 v[160:163], a[212:215], a[180:183], v[160:163]\n"
    "s_waitcnt lgkmcnt(8)\n"
    "v_mfma_f32_16x16x32_bf16 v[128:131], a[212:215], a[176:179], v[128:131]\n"
    "v_mfma_f32_16x16x32_bf16 v[128:131], a[208:211], a[144:147], v[128:131]\n"
    "s_waitcnt lgkmcnt(6)\n"
    "s_addk_i32 m0, 1024\n"
    "s_addk_i32 s54, 16384\n"
    "v_mfma_f32_16x16x32_bf16 v[96:99], a[208:211], a[140:143], v[96:99]\n"
    "buffer_load_dwordx4 v254, s[72:75], s54 offen  lds\n"
    "v_mfma_f32_16x16x32_bf16 v[96:99], a[212:215], a[172:175], v[96:99]\n"
    "s_waitcnt lgkmcnt(4)\n"
    "v_mfma_f32_16x16x32_bf16 v[64:67], a[212:215], a[168:171], v[64:67]\n"
    "v_mfma_f32_16x16x32_bf16 v[64:67], a[208:211], a[136:139], v[64:67]\n"
    "s_waitcnt lgkmcnt(2)\n"
    "s_addk_i32 m0, 1024\n"
    "s_addk_i32 s54, 16384\n"
    "v_mfma_f32_16x16x32_bf16 v[32:35], a[208:211], a[132:135], v[32:35]\n"
    "buffer_load_dwordx4 v254, s[72:75], s54 offen  lds\n"
    "v_mfma_f32_16x16x32_bf16 v[32:35], a[212:215], a[164:167], v[32:35]\n"
    "s_waitcnt lgkmcnt(0)\n"
    "v_mfma_f32_16x16x32_bf16 v[0:3], a[212:215], a[160:163], v[0:3]\n"
    "v_mfma_f32_16x16x32_bf16 v[0:3], a[208:211], a[128:131], v[0:3]\n"
    "s_addk_i32 m0, 1024\n"
    "s_addk_i32 s54, 16384\n"
    "v_mfma_f32_16x16x32_bf16 v[4:7], a[216:219], a[128:131], v[4:7]\n"
    "buffer_load_dwordx4 v254, s[72:75], s54 offen  lds\n"
    "v_mfma_f32_16x16x32_bf16 v[4:7], a[220:223], a[160:163], v[4:7]\n"
    "v_mfma_f32_16x16x32_bf16 v[36:39], a[220:223], a[164:167], v[36:39]\n"
    "v_mfma_f32_16x16x32_bf16 v[36:39], a[216:219], a[132:135], v[36:39]\n"
    "s_addk_i32 m0, 1024\n"
    "s_addk_i32 s54, 16384\n"
    "v_mfma_f32_16x16x32_bf16 v[68:71], a[216:219], a[136:139], v[68:71]\n"
    "buffer_load_dwordx4 v254, s[72:75], s54 offen  lds\n"
    "v_mfma_f32_16x16x32_bf16 v[68:71], a[220:223], a[168:171], v[68:71]\n"
    "v_mfma_f32_16x16x32_bf16 v[100:103], a[220:223], a[172:175], v[100:103]\n"
    "v_mfma_f32_16x16x32_bf16 v[100:103], a[216:219], a[140:143], v[100:103]\n"
    "s_addk_i32 m0, 1024\n"
    "s_addk_i32 s54, 16384\n"
    "v_mfma_f32_16x16x32_bf16 v[132:135], a[216:219], a[144:147], v[132:135]\n"
    "buffer_load_dwordx4 v254, s[72:75], s54 offen  lds\n"
    "s_bitcmp1_b32 s85, 5\n"
    "s_cbranch_scc1 CP5_%=\n"
    "CR5_%=:\n"
    "v_mfma_f32_16x16x32_bf16 v[132:135], a[220:223], a[176:179], v[132:135]\n"
    "v_mfma_f32_16x16x32_bf16 v[164:167], a[220:223], a[180:183], v[164:167]\n"
    "v_mfma_f32_16x16x32_bf16 v[164:167], a[216:219], a[148:151], v[164:167]\n"
    "s_xor_b32 s59, s66, s67\n"
    "s_addk_i32 s67, 128\n"
    "s_cmpk_eq_u32 s67, 1536\n"
    "v_mfma_f32_16x16x32_bf16 v[196:199], a[216:219], a[152:155], v[196:199]\n"
    "s_cselect_b32 s67, 0, s67\n"
    "s_add_u32 m0, s68, 81920\n"
    "s_nop 0\n"
    "v_mfma_f32_16x16x32_bf16 v[196:199], a[220:223], a[184:187], v[196:199]\n"
    "buffer_load_dwordx4 v255, s[24:27], s59 offen  lds\n"
    "v_mfma_f32_16x16x32_bf16 v[228:231], a[220:223], a[188:191], v[228:231]\n"
    "v_mfma_f32_16x16x32_bf16 v[228:231], a[216:219], a[156:159], v[228:231]\n"
    "v_add_u32 v252, 0x20000, v252\n"
    "v_add_u32 v253, 0x20000, v253\n"
    "v_mfma_f32_16x16x32_bf16 v[232:235], a[224:227], a[156:159], v[232:235]\n"
    "s_waitcnt vmcnt(19)\n"
    "ds_read_b64_tr_b16 a[192:193], v252 offset:0\n"
    "ds_read_b64_tr_b16 a[194:195], v252 offset:512\n"
    "v_mfma_f32_16x16x32_bf16 v[232:235], a[228:231], a[188:191], v[232:235]\n"
    "s_add_u32 m0, s68, 82944\n"
    "s_addk_i32 s59, 16384\n"
    "v_mfma_f32_16x16x32_bf16 v[200:203], a[228:231], a[184:187], v[200:203]\n"
    "buffer_load_dwordx4 v243, s[24:27], s59 offen  lds\n"
    "v_mfma_f32_16x16x32_bf16 v[200:203], a[224:227], a[152:155], v[200:203]\n"
    "ds_read_b64_tr_b16 a[196:197], v252 offset:4096\n"
    "ds_read_b64_tr_b16 a[198:199], v252 offset:4608\n"
    "v_mfma_f32_16x16x32_bf16 v[168:171], a[224:227], a[148:151], v[168:171]\n"
    "ds_read_b64_tr_b16 a[200:201], v252 offset:8\n"
    "ds_read_b64_tr_b16 a[202:203], v252 offset:520\n"
    "v_mfma_f32_16x16x32_bf16 v[168:171], a[228:231], a[180:183], v[168:171]\n"
    "s_add_u32 m0, s68, 83968\n"
    "s_addk_i32 s59, 16384\n"
    "v_mfma_f32_16x16x32_bf16 v[136:139], a[228:231], a[176:179], v[136:139]\n"
    "buffer_load_dwordx4 v255, s[24:27], s59 offen  lds\n"
    "v_mfma_f32_16x16x32_bf16 v[136:139], a[224:227], a[144:147], v[136:139]\n"
    "ds_read_b64_tr_b16 a[204:205], v252 offset:4104\n"
    "ds_read_b64_tr_b16 a[206:207], v252 offset:4616\n"
    "v_mfma_f32_16x16x32_bf16 v[104:107], a[224:227], a[140:143], v[104:107]\n"
    "ds_read_b64_tr_b16 a[208:209], v253 offset:0\n"
    "ds_read_b64_tr_b16 a[210:211], v253 offset:512\n"
    "v_mfma_f32_16x16x32_bf16 v[104:107], a[228:231], a[172:175], v[104:107]\n"
    "s_add_u32 m0, s68, 84992\n"
    "s_addk_i32 s59, 16384\n"
    "v_mfma_f32_16x16x32_bf16 v[72:75], a[228:231], a[168:171], v[72:75]\n"
    "buffer_load_dwordx4 v243, s[24:27], s59 offen  lds\n"
    "v_mfma_f32_16x16x32_bf16 v[72:75], a[224:227], a[136:139], v[72:75]\n"
    "ds_read_b64_tr_b16 a[212:213], v253 offset:4096\n"
    "ds_read_b64_tr_b16 a[214:215], v253 offset:4608\n"
    "v_mfma_f32_16x16x32_bf16 v[40:43], a[224:227], a[132:135], v[40:43]\n"
    "ds_read_b64_tr_b16 a[216:217], v253 offset:8\n"
    "ds_read_b64_tr_b16 a[218:219], v253 offset:520\n"
    "v_mfma_f32_16x16x32_bf16 v[40:43], a[228:231], a[164:167], v[40:43]\n"
    "s_add_u32 m0, s79, 32768\n"
    "s_lshl_b32 s54, s64, 17\n"
    "s_xor_b32 s54, s54, s57\n"
    "v_mfma_f32_16x16x32_bf16 v[8:11], a[228:231], a[160:163], v[8:11]\n"
    "s_addk_i32 s54, 512\n"
    "v_mfma_f32_16x16x32_bf16 v[8:11], a[224:227], a[128:131], v[8:11]\n"
    "buffer_load_dwordx4 v254, s[72:75], s54 offen  lds\n"
    "v_mfma_f32_16x16x32_bf16 v[12:15], a[232:235], a[128:131], v[12:15]\n"
    "ds_read_b64_tr_b16 a[220:221], v253 offset:4104\n"
    "ds_read_b64_tr_b16 a[222:223], v253 offset:4616\n"
    "v_mfma_f32_16x16x32_bf16 v[12:15], a[236:239], a[160:163], v[12:15]\n"
    "s_addk_i32 m0, 1024\n"
    "s_addk_i32 s54, 16384\n"
    "v_mfma_f32_16x16x32_bf16 v[44:47], a[236:239], a[164:167], v[44:47]\n"
    "buffer_load_dwordx4 v254, s[72:75], s54 offen  lds\n"
    "v_mfma_f32_16x16x32_bf16 v[44:47], a[232:235], a[132:135], v[44:47]\n"
    "s_addk_i32 m0, 1024\n"
    "s_addk_i32 s54, 16384\n"
    "v_mfma_f32_16x16x32_bf16 v[76:79], a[232:235], a[136:139], v[76:79]\n"
    "buffer_load_dwordx4 v254, s[72:75], s54 offen  lds\n"
    "v_mfma_f32_16x16x32_bf16 v[76:79], a[236:239], a[168:171], v[76:79]\n"
    "v_mfma_f32_16x16x32_bf16 v[108:111], a[236:239], a[172:175], v[108:111]\n"
    "v_mfma_f32_16x16x32_bf16 v[108:111], a[232:235], a[140:143], v[108:111]\n"
    "s_addk_i32 m0, 1024\n"
    "s_addk_i32 s54, 16384\n"
    "v_mfma_f32_16x16x32_bf16 v[140:143], a[232:235], a[144:147], v[140:143]\n"
    "buffer_load_dwordx4 v254, s[72:75], s54 offen  lds\n"
    "v_mfma_f32_16x16x32_bf16 v[140:143], a[236:239], a[176:179], v[140:143]\n"
    "v_mfma_f32_16x16x32_bf16 v[172:175], a[236:239], a[180:183], v[172:175]\n"
    "v_mfma_f32_16x16x32_bf16 v[172:175], a[232:235], a[148:151], v[172:175]\n"
    "s_addk_i32 m0, 1024\n"
    "s_addk_i32 s54, 16384\n"
    "v_mfma_f32_16x16x32_bf16 v[204:207], a[232:235], a[152:155], v[204:207]\n"
    "buffer_load_dwordx4 v254, s[72:75], s54 offen  lds\n"
    "v_mfma_f32_16x16x32_bf16 v[204:207], a[236:239], a[184:187], v[204:207]\n"
    "v_mfma_f32_16x16x32_bf16 v[236:239], a[236:239], a[188:191], v[236:239]\n"
    "v_mfma_f32_16x16x32_bf16 v[236:239], a[232:235], a[156:159], v[236:239]\n"
    "s_waitcnt lgkmcnt(12)\n"
    "s_addk_i32 m0, 1024\n"
    "s_addk_i32 s54, 16384\n"
    "v_mfma_f32_16x16x32_bf16 a[240:243], a[192:195], a[156:159], a[240:243]\n"
    "buffer_load_dwordx4 v254, s[72:75], s54 offen  lds\n"
    "v_mfma_f32_16x16x32_bf16 a[240:243], a[196:199], a[188:191], a[240:243]\n"
    "v_mfma_f32_16x16x32_bf16 v[208:211], a[196:199], a[184:187], v[208:211]\n"
    "v_mfma_f32_16x16x32_bf16 v[208:211], a[192:195], a[152:155], v[208:211]\n"
    "s_addk_i32 m0, 1024\n"
    "s_addk_i32 s54, 16384\n"
    "v_mfma_f32_16x16x32_bf16 v[176:179], a[192:195], a[148:151], v[176:179]\n"
    "buffer_load_dwordx4 v254, s[72:75], s54 offen  lds\n"
    "v_mfma_f32_16x16x32_bf16 v[176:179], a[196:199], a[180:183], v[176:179]\n"
    "v_mfma_f32_16x16x32_bf16 v[144:147], a[196:199], a[176:179], v[144:147]\n"
    "v_mfma_f32_16x16x32_bf16 v[144:147], a[192:195], a[144:147], v[144:147]\n"
    "s_addk_i32 m0, 1024\n"
    "s_addk_i32 s54, 16384\n"
    "v_mfma_f32_16x16x32_bf16 v[112:115], a[192:195], a[140:143], v[112:115]\n"
    "buffer_load_dwordx4 v254, s[72:75], s54 offen  lds\n"
    "v_mfma_f32_16x16x32_bf16 v[112:115], a[196:199], a[172:175], v[112:115]\n"
    "s_add_u32 s57, s57, 131072\n"
    "s_cmp_ge_u32 s57, 1572864\n"
    "s_cselect_b32 s57, s70, s57\n"
    "v_mfma_f32_16x16x32_bf16 v[80:83], a[196:199], a[168:171], v[80:83]\n"
    "v_mfma_f32_16x16x32_bf16 v[80:83], a[192:195], a[136:139], v[80:83]\n"
    "s_waitcnt lgkmcnt(0)\n"
    "s_add_u32 m0, s79, 131072\n"
    "s_lshl_b32 s54, s64, 17\n"
    "v_mfma_f32_16x16x32_bf16 v[48:51], a[192:195], a[132:135], v[48:51]\n"
    "s_xor_b32 s54, s54, s57\n"
    "v_mfma_f32_16x16x32_bf16 v[48:51], a[196:199], a[164:167], v[48:51]\n"
    "buffer_load_dwordx4 v254, s[72:75], s54 offen  lds\n"
    "v_mfma_f32_16x16x32_bf16 v[16:19], a[196:199], a[160:163], v[16:19]\n"
    "v_mfma_f32_16x16x32_bf16 v[16:19], a[192:195], a[128:131], v[16:19]\n"
    "s_addk_i32 m0, 1024\n"
    "s_addk_i32 s54, 16384\n"
    "v_mfma_f32_16x16x32_bf16 v[20:23], a[200:203], a[128:131], v[20:23]\n"
    "buffer_load_dwordx4 v254, s[72:75], s54 offen  lds\n"
    "v_mfma_f32_16x16x32_bf16 v[20:23], a[204:207], a[160:163], v[20:23]\n"
    "v_mfma_f32_16x16x32_bf16 v[52:55], a[204:207], a[164:167], v[52:55]\n"
    "v_mfma_f32_16x16x32_bf16 v[52:55], a[200:203], a[132:135], v[52:55]\n"
    "s_addk_i32 m0, 1024\n"
    "s_addk_i32 s54, 16384\n"
    "v_mfma_f32_16x16x32_bf16 v[84:87], a[200:203], a[136:139], v[84:87]\n"
    "buffer_load_dwordx4 v254, s[72:75], s54 offen  lds\n"
    "v_mfma_f32_16x16x32_bf16 v[84:87], a[204:207], a[168:171], v[84:87]\n"
    "v_mfma_f32_16x16x32_bf16 v[116:119], a[204:207], a[172:175], v[116:119]\n"
    "v_mfma_f32_16x16x32_bf16 v[116:119], a[200:203], a[140:143], v[116:119]\n"
    "s_addk_i32 m0, 1024\n"
    "s_addk_i32 s54, 16384\n"
    "v_mfma_f32_16x16x32_bf16 v[148:151], a[200:203], a[144:147], v[148:151]\n"
    "buffer_load_dwordx4 v254, s[72:75], s54 offen  lds\n"
    "v_mfma_f32_16x16x32_bf16 v[148:151], a[204:207], a[176:179], v[148:151]\n"
    "v_mfma_f32_16x16x32_bf16 v[180:183], a[204:207], a[180:183], v[180:183]\n"
    "v_mfma_f32_16x16x32_bf16 v[180:183], a[200:203], a[148:151], v[180:183]\n"
    "s_addk_i32 m0, 1024\n"
    "s_addk_i32 s54, 16384\n"
    "v_mfma_f32_16x16x32_bf16 v[212:215], a[200:203], a[152:155], v[212:215]\n"
    "buffer_load_dwordx4 v254, s[72:75], s54 offen  lds\n"
    "v_mfma_f32_16x16x32_bf16 v[212:215], a[204:207], a[184:187], v[212:215]\n"
    "v_mfma_f32_16x16x32_bf16 a[244:247], a[204:207], a[188:191], a[244:247]\n"
    "v_mfma_f32_16x16x32_bf16 a[244:247], a[200:203], a[156:159], a[244:247]\n"
    "v_add_u32 v252, 0xfffe0000, v252\n"
    "v_add_u32 v253, 0xfffe0000, v253\n"
    "v_mfma_f32_16x16x32_bf16 a[248:251], a[208:211], a[156:159], a[248:251]\n"
    "s_waitcnt vmcnt(17)\n"
    "ds_read_b64_tr_b16 a[224:225], v252 offset:0\n"
    "ds_read_b64_tr_b16 a[226:227], v252 offset:512\n"
    "v_mfma_f32_16x16x32_bf16 a[248:251], a[212:215], a[188:191], a[248:251]\n"
    "s_addk_i32 m0, 1024\n"
    "s_addk_i32 s54, 16384\n"
    "v_mfma_f32_16x16x32_bf16 v[216:219], a[212:215], a[184:187], v[216:219]\n"
    "buffer_load_dwordx4 v254, s[72:75], s54 offen  lds\n"
    "v_mfma_f32_16x16x32_bf16 v[216:219], a[208:211], a[152:155], v[216:219]\n"
    "ds_read_b64_tr_b16 a[228:229], v252 offset:4096\n"
    "ds_read_b64_tr_b16 a[230:231], v252 offset:4608\n"
    "v_mfma_f32_16x16x32_bf16 v[184:187], a[208:211], a[148:151], v[184:187]\n"
    "ds_read_b64_tr_b16 a[232:233], v252 offset:8\n"
    "ds_read_b64_tr_b16 a[234:235], v252 offset:520\n"
    "v_mfma_f32_16x16x32_bf16 v[184:187], a[212:215], a[180:183], v[184:187]\n"
    "s_addk_i32 m0, 1024\n"
    "s_addk_i32 s54, 16384\n"
    "v_mfma_f32_16x16x32_bf16 v[152:155], a[212:215], a[176:179], v[152:155]\n"
    "buffer_load_dwordx4 v254, s[72:75], s54 offen  lds\n"
    "v_mfma_f32_16x16x32_bf16 v[152:155], a[208:211], a[144:147], v[152:155]\n"
    "ds_read_b64_tr_b16 a[236:237], v252 offset:4104\n"
    "ds_read_b64_tr_b16 a[238:239], v252 offset:4616\n"
    "v_mfma_f32_16x16x32_bf16 v[120:123], a[208:211], a[140:143], v[120:123]\n"
    "ds_read_b64_tr_b16 a[192:193], v253 offset:0\n"
    "ds_read_b64_tr_b16 a[194:195], v253 offset:512\n"
    "v_mfma_f32_16x16x32_bf16 v[120:123], a[212:215], a[172:175], v[120:123]\n"
    "s_addk_i32 m0, 1024\n"
    "s_addk_i32 s54, 16384\n"
    "v_mfma_f32_16x16x32_bf16 v[88:91], a[212:215], a[168:171], v[88:91]\n"
    "buffer_load_dwordx4 v254, s[72:75], s54 offen  lds\n"
    "v_mfma_f32_16x16x32_bf16 v[88:91], a[208:211], a[136:139], v[88:91]\n"
    "ds_read_b64_tr_b16 a[196:197], v253 offset:4096\n"
    "ds_read_b64_tr_b16 a[198:199], v253 offset:4608\n"
    "v_mfma_f32_16x16x32_bf16 v[56:59], a[208:211], a[132:135], v[56:59]\n"
    "ds_read_b64_tr_b16 a[200:201], v253 offset:8\n"
    "ds_read_b64_tr_b16 a[202:203], v253 offset:520\n"
    "v_mfma_f32_16x16x32_bf16 v[56:59], a[212:215], a[164:167], v[56:59]\n"
    "s_barrier\n"
    "ds_read_b64_tr_b16 a[204:205], v253 offset:4104\n"
    "ds_read_b64_tr_b16 a[206:207], v253 offset:4616\n"
    "v_mfma_f32_16x16x32_bf16 v[24:27], a[212:215], a[160:163], v[24:27]\n"
    "v_mfma_f32_16x16x32_bf16 v[24:27], a[208:211], a[128:131], v[24:27]\n"
    "v_mfma_f32_16x16x32_bf16 v[28:31], a[216:219], a[128:131], v[28:31]\n"
    "v_mfma_f32_16x16x32_bf16 v[28:31], a[220:223], a[160:163], v[28:31]\n"
    "v_mfma_f32_16x16x32_bf16 v[60:63], a[220:223], a[164:167], v[60:63]\n"
    "ds_read_b128 a[128:131], v241 offset:0\n"
    "v_mfma_f32_16x16x32_bf16 v[60:63], a[216:219], a[132:135], v[60:63]\n"
    "s_waitcnt lgkmcnt(1)\n"
    "s_add_u32 m0, s79, 0\n"
    "s_lshl_b32 s54, s64, 17\n"
    "v_mfma_f32_16x16x32_bf16 v[92:95], a[216:219], a[136:139], v[92:95]\n"
    "s_xor_b32 s54, s54, s57\n"
    "s_addk_i32 s54, 512\n"
    "v_mfma_f32_16x16x32_bf16 v[92:95], a[220:223], a[168:171], v[92:95]\n"
    "buffer_load_dwordx4 v254, s[72:75], s54 offen  lds\n"
    "v_mfma_f32_16x16x32_bf16 v[124:127], a[220:223], a[172:175], v[124:127]\n"
    "ds_read_b128 a[160:163], v242 offset:0\n"
    "ds_read_b128 a[164:167], v242 offset:2048\n"
    "v_mfma_f32_16x16x32_bf16 v[124:127], a[216:219], a[140:143], v[124:127]\n"
    "ds_read_b128 a[132:135], v241 offset:2048\n"
    "ds_read_b128 a[136:139], v241 offset:4096\n"
    "v_mfma_f32_16x16x32_bf16 v[156:159], a[216:219], a[144:147], v[156:159]\n"
    "s_addk_i32 m0, 1024\n"
    "s_addk_i32 s54, 16384\n"
    "v_mfma_f32_16x16x32_bf16 v[156:159], a[220:223], a[176:179], v[156:159]\n"
    "buffer_load_dwordx4 v254, s[72:75], s54 offen  lds\n"
    "v_mfma_f32_16x16x32_bf16 v[188:191], a[220:223], a[180:183], v[188:191]\n"
    "ds_read_b128 a[168:171], v242 offset:4096\n"
    "ds_read_b128 a[172:175], v242 offset:6144\n"
    "v_mfma_f32_16x16x32_bf16 v[188:191], a[216:219], a[148:151], v[188:191]\n"
    "ds_read_b128 a[140:143], v241 offset:6144\n"
    "ds_read_b128 a[144:147], v241 offset:8192\n"
    "v_mfma_f32_16x16x32_bf16 v[220:223], a[216:219], a[152:155], v[220:223]\n"
    "s_addk_i32 m0, 1024\n"
    "s_addk_i32 s54, 16384\n"
    "v_mfma_f32_16x16x32_bf16 v[220:223], a[220:223], a[184:187], v[220:223]\n"
    "buffer_load_dwordx4 v254, s[72:75], s54 offen  lds\n"
    "v_mfma_f32_16x16x32_bf16 a[252:255], a[220:223], a[188:191], a[252:255]\n"
    "ds_read_b128 a[176:179], v242 offset:8192\n"
    "ds_read_b128 a[180:183], v242 offset:10240\n"
    "v_mfma_f32_16x16x32_bf16 a[252:255], a[216:219], a[156:159], a[252:255]\n"
    "s_waitcnt lgkmcnt(9)\n"
    "ds_read_b128 a[148:151], v241 offset:10240\n"
    "ds_read_b128 a[152:155], v241 offset:12288\n"
    "v_mfma_f32_16x16x32_bf16 v[0:3], a[224:227], a[128:131], v[0:3]\n"
    "s_addk_i32 m0, 1024\n"
    "s_addk_i32 s54, 16384\n"
    "v_mfma_f32_16x16x32_bf16 v[0:3], a[228:231], a[160:163], v[0:3]\n"
    "s_waitcnt lgkmcnt(9)\n"
    "v_mfma_f32_16x16x32_bf16 v[32:35], a[228:231], a[164:167], v[32:35]\n"
    "buffer_load_dwordx4 v254, s[72:75], s54 offen  lds\n"
    "ds_read_b128 a[184:187], v242 offset:12288\n"
    "ds_read_b128 a[188:191], v242 offset:14336\n"
    "ds_read_b128 a[156:159], v241 offset:14336\n"
    "v_mfma_f32_16x16x32_bf16 v[32:35], a[224:227], a[132:135], v[32:35]\n"
    "s_waitcnt lgkmcnt(10)\n"
    "s_addk_i32 m0, 1024\n"
    "s_addk_i32 s54, 16384\n"
    "v_mfma_f32_16x16x32_bf16 v[64:67], a[224:227], a[136:139], v[64:67]\n"
    "buffer_load_dwordx4 v254, s[72:75], s54 offen  lds\n"
    "v_mfma_f32_16x16x32_bf16 v[64:67], a[228:231], a[168:171], v[64:67]\n"
    "s_waitcnt lgkmcnt(8)\n"
    "v_mfma_f32_16x16x32_bf16 v[96:99], a[228:231], a[172:175], v[96:99]\n"
    "v_mfma_f32_16x16x32_bf16 v[96:99], a[224:227], a[140:143], v[96:99]\n"
    "s_waitcnt lgkmcnt(6)\n"
    "s_addk_i32 m0, 1024\n"
    "s_addk_i32 s54, 16384\n"
    "v_mfma_f32_16x16x32_bf16 v[128:131], a[224:227], a[144:147], v[128:131]\n"
    "buffer_load_dwordx4 v254, s[72:75], s54 offen  lds\n"
    "v_mfma_f32_16x16x32_bf16 v[128:131], a[228:231], a[176:179], v[128:131]\n"
    "s_waitcnt lgkmcnt(4)\n"
    "v_mfma_f32_16x16x32_bf16 v[160:163], a[228:231], a[180:183], v[160:163]\n"
    "v_mfma_f32_16x16x32_bf16 v[160:163], a[224:227], a[148:151], v[160:163]\n"
    "s_waitcnt lgkmcnt(2)\n"
    "s_addk_i32 m0, 1024\n"
    "s_addk_i32 s54, 16384\n"
    "v_mfma_f32_16x16x32_bf16 v[192:195], a[224:227], a[152:155], v[192:195]\n"
    "buffer_load_dwordx4 v254, s[72:75], s54 offen  lds\n"
    "v_mfma_f32_16x16x32_bf16 v[192:195], a[228:231], a[184:187], v[192:195]\n"
    "s_waitcnt lgkmcnt(0)\n"
    "v_mfma_f32_16x16x32_bf16 v[224:227], a[228:231], a[188:191], v[224:227]\n"
    "v_mfma_f32_16x16x32_bf16 v[224:227], a[224:227], a[156:159], v[224:227]\n"
    "s_addk_i32 m0, 1024\n"
    "s_addk_i32 s54, 16384\n"
    "v_mfma_f32_16x16x32_bf16 v[228:231], a[232:235], a[156:159], v[228:231]\n"
    "buffer_load_dwordx4 v254, s[72:75], s54 offen  lds\n"
    "v_mfma_f32_16x16x32_bf16 v[228:231], a[236:239], a[188:191], v[228:231]\n"
    "s_add_u32 s57, s57, 131072\n"
    "s_cmp_ge_u32 s57, 1572864\n"
    "s_cselect_b32 s57, s70, s57\n"
    "v_mfma_f32_16x16x32_bf16 v[196:199], a[236:239], a[184:187], v[196:199]\n"
    "v_mfma_f32_16x16x32_bf16 v[196:199], a[232:235], a[152:155], v[196:199]\n"
    "v_mfma_f32_16x16x32_bf16 v[164:167], a[232:235], a[148:151], v[164:167]\n"
    "v_mfma_f32_16x16x32_bf16 v[164:167], a[236:239], a[180:183], v[164:167]\n"
    "s_xor_b32 s59, s66, s67\n"
    "s_addk_i32 s67, 128\n"
    "s_cmpk_eq_u32 s67, 1536\n"
    "v_mfma_f32_16x16x32_bf16 v[132:135], a[236:239], a[176:179], v[132:135]\n"
    "s_cselect_b32 s67, 0, s67\n"
    "s_add_u32 m0, s68, 98304\n"
    "s_nop 0\n"
    "v_mfma_f32_16x16x32_bf16 v[132:135], a[232:235], a[144:147], v[132:135]\n"
    "buffer_load_dwordx4 v255, s[24:27], s59 offen  lds\n"
    "v_mfma_f32_16x16x32_bf16 v[100:103], a[232:235], a[140:143], v[100:103]\n"
    "s_bitcmp1_b32 s85, 6\n"
    "s_cbranch_scc1 CP6_%=\n"
    "CR6_%=:\n"
    "s_add_u32 m0, s68, 99328\n"
    "v_mfma_f32_16x16x32_bf16 v[100:103], a[236:239], a[172:175], v[100:103]\n"
    "s_addk_i32 s59, 16384\n"
    "v_mfma_f32_16x16x32_bf16 v[68:71], a[236:239], a[168:171], v[68:71]\n"
    "buffer_load_dwordx4 v243, s[24:27], s59 offen  lds\n"
    "v_mfma_f32_16x16x32_bf16 v[68:71], a[232:235], a[136:139], v[68:71]\n"
    "s_add_u32 m0, s68, 100352\n"
    "s_addk_i32 s59, 16384\n"
    "v_mfma_f32_16x16x32_bf16 v[36:39], a[232:235], a[132:135], v[36:39]\n"
    "buffer_load_dwordx4 v255, s[24:27], s59 offen  lds\n"
    "v_mfma_f32_16x16x32_bf16 v[36:39], a[236:239], a[164:167], v[36:39]\n"
    "v_mfma_f32_16x16x32_bf16 v[4:7], a[236:239], a[160:163], v[4:7]\n"
    "s_add_u32 m0, s68, 101376\n"
    "s_addk_i32 s59, 16384\n"
    "v_mfma_f32_16x16x32_bf16 v[4:7], a[232:235], a[128:131], v[4:7]\n"
    "buffer_load_dwordx4 v243, s[24:27], s59 offen  lds\n"
    "v_mfma_f32_16x16x32_bf16 v[8:11], a[192:195], a[128:131], v[8:11]\n"
    "s_waitcnt vmcnt(20)\n"
    "ds_read_b64_tr_b16 a[208:209], v252 offset:32768\n"
    "ds_read_b64_tr_b16 a[210:211], v252 offset:33280\n"
    "v_mfma_f32_16x16x32_bf16 v[8:11], a[196:199], a[160:163], v[8:11]\n"
    "ds_read_b64_tr_b16 a[212:213], v252 offset:36864\n"
    "ds_read_b64_tr_b16 a[214:215], v252 offset:37376\n"
    "v_mfma_f32_16x16x32_bf16 v[40:43], a[196:199], a[164:167], v[40:43]\n"
    "v_add_u32 v246, s90, v252\n"
    "ds_read_b64_tr_b16 v[248:249], v246 offset:0\n"
    "v_mfma_f32_16x16x32_bf16 v[40:43], a[192:195], a[132:135], v[40:43]\n"
    "ds_read_b64_tr_b16 v[250:251], v246 offset:512\n"
    "ds_read_b64_tr_b16 a[216:217], v252 offset:32776\n"
    "v_mfma_f32_16x16x32_bf16 v[72:75], a[192:195], a[136:139], v[72:75]\n"
    "ds_read_b64_tr_b16 a[218:219], v252 offset:33288\n"
    "v_mfma_f32_16x16x32_bf16 v[72:75], a[196:199], a[168:171], v[72:75]\n"
    "ds_read_b64_tr_b16 a[220:221], v252 offset:36872\n"
    "ds_read_b64_tr_b16 a[222:223], v252 offset:37384\n"
    "v_mfma_f32_16x16x32_bf16 v[104:107], a[196:199], a[172:175], v[104:107]\n"
    "v_mfma_f32_16x16x32_bf16 v[104:107], a[192:195], a[140:143], v[104:107]\n"
    "ds_read_b64_tr_b16 a[224:225], v253 offset:32768\n"
    "ds_read_b64_tr_b16 a[226:227], v253 offset:33280\n"
    "v_mfma_f32_16x16x32_bf16 v[136:139], a[192:195], a[144:147], v[136:139]\n"
    "v_mfma_f32_16x16x32_bf16 v[136:139], a[196:199], a[176:179], v[136:139]\n"
    "ds_read_b64_tr_b16 a[228:229], v253 offset:36864\n"
    "ds_read_b64_tr_b16 a[230:231], v253 offset:37376\n"
    "v_mfma_f32_16x16x32_bf16 v[168:171], a[196:199], a[180:183], v[168:171]\n"
    "v_mfma_f32_16x16x32_bf16 v[168:171], a[192:195], a[148:151], v[168:171]\n"
    "ds_read_b64_tr_b16 a[232:233], v253 offset:32776\n"
    "ds_read_b64_tr_b16 a[234:235], v253 offset:33288\n"
    "v_mfma_f32_16x16x32_bf16 v[200:203], a[192:195], a[152:155], v[200:203]\n"
    "s_add_u32 s62, s91, 524288\n"
    "s_waitcnt lgkmcnt(10)\n"
    "v_mfma_f32_16x16x32_bf16 v[200:203], a[196:199], a[184:187], v[200:203]\n"
    "buffer_store_dwordx4 v[248:251], v240, s[28:31], s62 offen offset:0 sc1\n"
    "s_nop 0\n"
    "ds_read_b64_tr_b16 a[236:237], v253 offset:36872\n"
    "ds_read_b64_tr_b16 a[238:239], v253 offset:37384\n"
    "v_mfma_f32_16x16x32_bf16 v[232:235], a[196:199], a[188:191], v[232:235]\n"
    "v_mfma_f32_16x16x32_bf16 v[232:235], a[192:195], a[156:159], v[232:235]\n"
    "v_mfma_f32_16x16x32_bf16 v[236:239], a[200:203], a[156:159], v[236:239]\n"
    "v_mfma_f32_16x16x32_bf16 v[236:239], a[204:207], a[188:191], v[236:239]\n"
    "v_mfma_f32_16x16x32_bf16 v[204:207], a[204:207], a[184:187], v[204:207]\n"
    "v_add_u32 v246, s90, v252\n"
    "ds_read_b64_tr_b16 v[248:249], v246 offset:8\n"
    "v_mfma_f32_16x16x32_bf16 v[204:207], a[200:203], a[152:155], v[204:207]\n"
    "ds_read_b64_tr_b16 v[250:251], v246 offset:520\n"
    "s_waitcnt lgkmcnt(2)\n"
    "v_mfma_f32_16x16x32_bf16 v[172:175], a[200:203], a[148:151], v[172:175]\n"
    "s_add_u32 m0, s79, 32768\n"
    "s_lshl_b32 s54, s64, 17\n"
    "s_xor_b32 s54, s54, s57\n"
    "v_mfma_f32_16x16x32_bf16 v[172:175], a[204:207], a[180:183], v[172:175]\n"
    "buffer_load_dwordx4 v254, s[72:75], s54 offen  lds\n"
    "v_mfma_f32_16x16x32_bf16 v[140:143], a[204:207], a[176:179], v[140:143]\n"
    "v_mfma_f32_16x16x32_bf16 v[140:143], a[200:203], a[144:147], v[140:143]\n"
    "s_addk_i32 m0, 1024\n"
    "s_addk_i32 s54, 16384\n"
    "v_mfma_f32_16x16x32_bf16 v[108:111], a[200:203], a[140:143], v[108:111]\n"
    "buffer_load_dwordx4 v254, s[72:75], s54 offen  lds\n"
    "v_mfma_f32_16x16x32_bf16 v[108:111], a[204:207], a[172:175], v[108:111]\n"
    "v_mfma_f32_16x16x32_bf16 v[76:79], a[204:207], a[168:171], v[76:79]\n"
    "v_mfma_f32_16x16x32_bf16 v[76:79], a[200:203], a[136:139], v[76:79]\n"
    "v_mfma_f32_16x16x32_bf16 v[44:47], a[200:203], a[132:135], v[44:47]\n"
    "s_add_u32 s62, s91, 524288\n"
    "s_waitcnt lgkmcnt(0)\n"
    "v_mfma_f32_16x16x32_bf16 v[44:47], a[204:207], a[164:167], v[44:47]\n"
    "buffer_store_dwordx4 v[248:251], v240, s[28:31], s62 offen offset:2048 sc1\n"
    "s_nop 0\n"
    "v_mfma_f32_16x16x32_bf16 v[12:15], a[204:207], a[160:163], v[12:15]\n"
    "v_mfma_f32_16x16x32_bf16 v[12:15], a[200:203], a[128:131], v[12:15]\n"
    "v_mfma_f32_16x16x32_bf16 v[16:19], a[208:211], a[128:131], v[16:19]\n"
    "v_mfma_f32_16x16x32_bf16 v[16:19], a[212:215], a[160:163], v[16:19]\n"
    "v_mfma_f32_16x16x32_bf16 v[48:51], a[212:215], a[164:167], v[48:51]\n"
    "v_add_u32 v246, s90, v253\n"
    "ds_read_b64_tr_b16 v[248:249], v246 offset:0\n"
    "v_mfma_f32_16x16x32_bf16 v[48:51], a[208:211], a[132:135], v[48:51]\n"
    "ds_read_b64_tr_b16 v[250:251], v246 offset:512\n"
    "v_mfma_f32_16x16x32_bf16 v[80:83], a[208:211], a[136:139], v[80:83]\n"
    "v_mfma_f32_16x16x32_bf16 v[80:83], a[212:215], a[168:171], v[80:83]\n"
    "v_mfma_f32_16x16x32_bf16 v[112:115], a[212:215], a[172:175], v[112:115]\n"
    "v_mfma_f32_16x16x32_bf16 v[112:115], a[208:211], a[140:143], v[112:115]\n"
    "v_mfma_f32_16x16x32_bf16 v[144:147], a[208:211], a[144:147], v[144:147]\n"
    "v_mfma_f32_16x16x32_bf16 v[144:147], a[212:215], a[176:179], v[144:147]\n"
    "v_mfma_f32_16x16x32_bf16 v[176:179], a[212:215], a[180:183], v[176:179]\n"
    "v_mfma_f32_16x16x32_bf16 v[176:179], a[208:211], a[148:151], v[176:179]\n"
    "v_mfma_f32_16x16x32_bf16 v[208:211], a[208:211], a[152:155], v[208:211]\n"
    "s_add_u32 s62, s91, 528384\n"
    "s_waitcnt lgkmcnt(0)\n"
    "v_mfma_f32_16x16x32_bf16 v[208:211], a[212:215], a[184:187], v[208:211]\n"
    "buffer_store_dwordx4 v[248:251], v240, s[28:31], s62 offen offset:0 sc1\n"
    "s_nop 0\n"
    "v_mfma_f32_16x16x32_bf16 a[240:243], a[212:215], a[188:191], a[240:243]\n"
    "v_mfma_f32_16x16x32_bf16 a[240:243], a[208:211], a[156:159], a[240:243]\n"
    "v_mfma_f32_16x16x32_bf16 a[244:247], a[216:219], a[156:159], a[244:247]\n"
    "v_mfma_f32_16x16x32_bf16 a[244:247], a[220:223], a[188:191], a[244:247]\n"
    "v_mfma_f32_16x16x32_bf16 v[212:215], a[220:223], a[184:187], v[212:215]\n"
    "v_add_u32 v246, s90, v253\n"
    "ds_read_b64_tr_b16 v[248:249], v246 offset:8\n"
    "v_mfma_f32_16x16x32_bf16 v[212:215], a[216:219], a[152:155], v[212:215]\n"
    "ds_read_b64_tr_b16 v[250:251], v246 offset:520\n"
    "v_mfma_f32_16x16x32_bf16 v[180:183], a[216:219], a[148:151], v[180:183]\n"
    "v_mfma_f32_16x16x32_bf16 v[180:183], a[220:223], a[180:183], v[180:183]\n"
    "v_mfma_f32_16x16x32_bf16 v[148:151], a[220:223], a[176:179], v[148:151]\n"
    "v_mfma_f32_16x16x32_bf16 v[148:151], a[216:219], a[144:147], v[148:151]\n"
    "v_mfma_f32_16x16x32_bf16 v[116:119], a[216:219], a[140:143], v[116:119]\n"
    "v_mfma_f32_16x16x32_bf16 v[116:119], a[220:223], a[172:175], v[116:119]\n"
    "v_mfma_f32_16x16x32_bf16 v[84:87], a[220:223], a[168:171], v[84:87]\n"
    "v_mfma_f32_16x16x32_bf16 v[84:87], a[216:219], a[136:139], v[84:87]\n"
    "v_mfma_f32_16x16x32_bf16 v[52:55], a[216:219], a[132:135], v[52:55]\n"
    "s_add_u32 s62, s91, 528384\n"
    "s_waitcnt lgkmcnt(0)\n"
    "v_mfma_f32_16x16x32_bf16 v[52:55], a[220:223], a[164:167], v[52:55]\n"
    "buffer_store_dwordx4 v[248:251], v240, s[28:31], s62 offen offset:2048 sc1\n"
    "s_nop 0\n"
    "v_mfma_f32_16x16x32_bf16 v[20:23], a[220:223], a[160:163], v[20:23]\n"
    "v_mfma_f32_16x16x32_bf16 v[20:23], a[216:219], a[128:131], v[20:23]\n"
    "v_add_u32 v252, 0x20000, v252\n"
    "v_add_u32 v253, 0x20000, v253\n"
    "v_mfma_f32_16x16x32_bf16 v[24:27], a[224:227], a[128:131], v[24:27]\n"
    "s_waitcnt vmcnt(18)\n"
    "ds_read_b64_tr_b16 a[192:193], v252 offset:0\n"
    "ds_read_b64_tr_b16 a[194:195], v252 offset:512\n"
    "v_mfma_f32_16x16x32_bf16 v[24:27], a[228:231], a[160:163], v[24:27]\n"
    "ds_read_b64_tr_b16 a[196:197], v252 offset:4096\n"
    "ds_read_b64_tr_b16 a[198:199], v252 offset:4608\n"
    "v_mfma_f32_16x16x32_bf16 v[56:59], a[228:231], a[164:167], v[56:59]\n"
    "v_mfma_f32_16x16x32_bf16 v[56:59], a[224:227], a[132:135], v[56:59]\n"
    "ds_read_b64_tr_b16 a[200:201], v252 offset:8\n"
    "ds_read_b64_tr_b16 a[202:203], v252 offset:520\n"
    "v_mfma_f32_16x16x32_bf16 v[88:91], a[224:227], a[136:139], v[88:91]\n"
    "v_mfma_f32_16x16x32_bf16 v[88:91], a[228:231], a[168:171], v[88:91]\n"
    "ds_read_b64_tr_b16 a[204:205], v252 offset:4104\n"
    "ds_read_b64_tr_b16 a[206:207], v252 offset:4616\n"
    "v_mfma_f32_16x16x32_bf16 v[120:123], a[228:231], a[172:175], v[120:123]\n"
    "v_mfma_f32_16x16x32_bf16 v[120:123], a[224:227], a[140:143], v[120:123]\n"
    "ds_read_b64_tr_b16 a[208:209], v253 offset:0\n"
    "ds_read_b64_tr_b16 a[210:211], v253 offset:512\n"
    "v_mfma_f32_16x16x32_bf16 v[152:155], a[224:227], a[144:147], v[152:155]\n"
    "v_mfma_f32_16x16x32_bf16 v[152:155], a[228:231], a[176:179], v[152:155]\n"
    "ds_read_b64_tr_b16 a[212:213], v253 offset:4096\n"
    "ds_read_b64_tr_b16 a[214:215], v253 offset:4608\n"
    "v_mfma_f32_16x16x32_bf16 v[184:187], a[228:231], a[180:183], v[184:187]\n"
    "v_mfma_f32_16x16x32_bf16 v[184:187], a[224:227], a[148:151], v[184:187]\n"
    "ds_read_b64_tr_b16 a[216:217], v253 offset:8\n"
    "ds_read_b64_tr_b16 a[218:219], v253 offset:520\n"
    "v_mfma_f32_16x16x32_bf16 v[216:219], a[224:227], a[152:155], v[216:219]\n"
    "v_mfma_f32_16x16x32_bf16 v[216:219], a[228:231], a[184:187], v[216:219]\n"
    "s_barrier\n"
    "ds_read_b64_tr_b16 a[220:221], v253 offset:4104\n"
    "ds_read_b64_tr_b16 a[222:223], v253 offset:4616\n"
    "v_mfma_f32_16x16x32_bf16 a[248:251], a[228:231], a[188:191], a[248:251]\n"
    "v_mfma_f32_16x16x32_bf16 a[248:251], a[224:227], a[156:159], a[248:251]\n"
    "v_lshrrev_b32 v246, 1, v240\n"
    "s_lshl_b32 s61, s65, 9\n"
    "v_mfma_f32_16x16x32_bf16 a[252:255], a[232:235], a[156:159], a[252:255]\n"
    "s_mov_b32 s82, s87\n"
    "v_mfma_f32_16x16x32_bf16 a[252:255], a[236:239], a[188:191], a[252:255]\n"
    "buffer_load_dwordx2 v[246:247], v246, s[80:83], s61 offen sc1\n"
    "v_mfma_f32_16x16x32_bf16 v[220:223], a[236:239], a[184:187], v[220:223]\n"
    "v_lshrrev_b32 v248, 2, v240\n"
    "s_lshl_b32 s61, s65, 8\n"
    "v_mfma_f32_16x16x32_bf16 v[220:223], a[232:235], a[152:155], v[220:223]\n"
    "s_mov_b32 s82, s87\n"
    "v_mfma_f32_16x16x32_bf16 v[188:191], a[232:235], a[148:151], v[188:191]\n"
    "buffer_load_dword v248, v248, s[80:83], s61 offen offset:2048 sc1\n"
    "v_mfma_f32_16x16x32_bf16 v[188:191], a[236:239], a[180:183], v[188:191]\n"
    "ds_read_b128 a[156:159], v241 offset:30720\n"
    "ds_read_b128 a[188:191], v242 offset:30720\n"
    "v_mfma_f32_16x16x32_bf16 v[156:159], a[236:239], a[176:179], v[156:159]\n"
    "ds_read_b128 a[184:187], v242 offset:28672\n"
    "ds_read_b128 a[152:155], v241 offset:28672\n"
    "v_mfma_f32_16x16x32_bf16 v[156:159], a[232:235], a[144:147], v[156:159]\n"
    "ds_read_b128 a[148:151], v241 offset:26624\n"
    "ds_read_b128 a[180:183], v242 offset:26624\n"
    "v_mfma_f32_16x16x32_bf16 v[124:127], a[232:235], a[140:143], v[124:127]\n"
    "ds_read_b128 a[176:179], v242 offset:24576\n"
    "v_mfma_f32_16x16x32_bf16 v[124:127], a[236:239], a[172:175], v[124:127]\n"
    "ds_read_b128 a[144:147], v241 offset:24576\n"
    "v_mfma_f32_16x16x32_bf16 v[92:95], a[236:239], a[168:171], v[92:95]\n"
    "ds_read_b128 a[140:143], v241 offset:22528\n"
    "v_mfma_f32_16x16x32_bf16 v[92:95], a[232:235], a[136:139], v[92:95]\n"
    "s_addk_i32 m0, 1024\n"
    "s_addk_i32 s54, 16384\n"
    "v_mfma_f32_16x16x32_bf16 v[60:63], a[232:235], a[132:135], v[60:63]\n"
    "buffer_load_dwordx4 v254, s[72:75], s54 offen  lds\n"
    "v_mfma_f32_16x16x32_bf16 v[60:63], a[236:239], a[164:167], v[60:63]\n"
    "ds_read_b128 a[172:175], v242 offset:22528\n"
    "ds_read_b128 a[168:171], v242 offset:20480\n"
    "v_mfma_f32_16x16x32_bf16 v[28:31], a[236:239], a[160:163], v[28:31]\n"
    "ds_read_b128 a[136:139], v241 offset:20480\n"
    "ds_read_b128 a[132:135], v241 offset:18432\n"
    "v_mfma_f32_16x16x32_bf16 v[28:31], a[232:235], a[128:131], v[28:31]\n"
    "s_waitcnt lgkmcnt(11)\n"
    "s_addk_i32 m0, 1024\n"
    "s_addk_i32 s54, 16384\n"
    "v_mfma_f32_16x16x32_bf16 v[224:227], a[192:195], a[156:159], v[224:227]\n"
    "buffer_load_dwordx4 v254, s[72:75], s54 offen  lds\n"
    "v_mfma_f32_16x16x32_bf16 v[224:227], a[196:199], a[188:191], v[224:227]\n"
    "s_waitcnt lgkmcnt(9)\n"
    "ds_read_b128 a[164:167], v242 offset:18432\n"
    "ds_read_b128 a[160:163], v242 offset:16384\n"
    "v_mfma_f32_16x16x32_bf16 v[192:195], a[196:199], a[184:187], v[192:195]\n"
    "ds_read_b128 a[128:131], v241 offset:16384\n"
    "v_mfma_f32_16x16x32_bf16 v[192:195], a[192:195], a[152:155], v[192:195]\n"
    "s_waitcnt lgkmcnt(10)\n"
    "s_addk_i32 m0, 1024\n"
    "s_addk_i32 s54, 16384\n"
    "v_mfma_f32_16x16x32_bf16 v[160:163], a[192:195], a[148:151], v[160:163]\n"
    "buffer_load_dwordx4 v254, s[72:75], s54 offen  lds\n"
    "v_mfma_f32_16x16x32_bf16 v[160:163], a[196:199], a[180:183], v[160:163]\n"
    "s_waitcnt lgkmcnt(8)\n"
    "v_mfma_f32_16x16x32_bf16 v[128:131], a[196:199], a[176:179], v[128:131]\n"
    "v_mfma_f32_16x16x32_bf16 v[128:131], a[192:195], a[144:147], v[128:131]\n"
    "s_waitcnt lgkmcnt(6)\n"
    "s_addk_i32 m0, 1024\n"
    "s_addk_i32 s54, 16384\n"
    "v_mfma_f32_16x16x32_bf16 v[96:99], a[192:195], a[140:143], v[96:99]\n"
    "buffer_load_dwordx4 v254, s[72:75], s54 offen  lds\n"
    "v_mfma_f32_16x16x32_bf16 v[96:99], a[196:199], a[172:175], v[96:99]\n"
    "s_waitcnt lgkmcnt(4)\n"
    "v_mfma_f32_16x16x32_bf16 v[64:67], a[196:199], a[168:171], v[64:67]\n"
    "v_mfma_f32_16x16x32_bf16 v[64:67], a[192:195], a[136:139], v[64:67]\n"
    "s_waitcnt lgkmcnt(2)\n"
    "s_addk_i32 m0, 1024\n"
    "s_addk_i32 s54, 16384\n"
    "v_mfma_f32_16x16x32_bf16 v[32:35], a[192:195], a[132:135], v[32:35]\n"
    "buffer_load_dwordx4 v254, s[72:75], s54 offen  lds\n"
    "v_mfma_f32_16x16x32_bf16 v[32:35], a[196:199], a[164:167], v[32:35]\n"
    "s_waitcnt lgkmcnt(0)\n"
    "v_mfma_f32_16x16x32_bf16 v[0:3], a[196:199], a[160:163], v[0:3]\n"
    "v_mfma_f32_16x16x32_bf16 v[0:3], a[192:195], a[128:131], v[0:3]\n"
    "s_addk_i32 m0, 1024\n"
    "s_addk_i32 s54, 16384\n"
    "v_mfma_f32_16x16x32_bf16 v[4:7], a[200:203], a[128:131], v[4:7]\n"
    "buffer_load_dwordx4 v254, s[72:75], s54 offen  lds\n"
    "v_mfma_f32_16x16x32_bf16 v[4:7], a[204:207], a[160:163], v[4:7]\n"
    "v_mfma_f32_16x16x32_bf16 v[36:39], a[204:207], a[164:167], v[36:39]\n"
    "v_mfma_f32_16x16x32_bf16 v[36:39], a[200:203], a[132:135], v[36:39]\n"
    "s_xor_b32 s59, s66, s67\n"
    "s_addk_i32 s67, 128\n"
    "s_cmpk_eq_u32 s67, 1536\n"
    "v_mfma_f32_16x16x32_bf16 v[68:71], a[200:203], a[136:139], v[68:71]\n"
    "s_cselect_b32 s67, 0, s67\n"
    "s_add_u32 m0, s68, 65536\n"
    "s_nop 0\n"
    "v_mfma_f32_16x16x32_bf16 v[68:71], a[204:207], a[168:171], v[68:71]\n"
    "buffer_load_dwordx4 v255, s[24:27], s59 offen  lds\n"
    "v_mfma_f32_16x16x32_bf16 v[100:103], a[204:207], a[172:175], v[100:103]\n"
    "v_mfma_f32_16x16x32_bf16 v[100:103], a[200:203], a[140:143], v[100:103]\n"
    "s_add_u32 m0, s68, 66560\n"
    "s_addk_i32 s59, 16384\n"
    "v_mfma_f32_16x16x32_bf16 v[132:135], a[200:203], a[144:147], v[132:135]\n"
    "buffer_load_dwordx4 v243, s[24:27], s59 offen  lds\n"
    "s_bitcmp1_b32 s85, 7\n"
    "s_cbranch_scc1 CP7_%=\n"
    "CR7_%=:\n"
    "v_mfma_f32_16x16x32_bf16 v[132:135], a[204:207], a[176:179], v[132:135]\n"
    "v_mfma_f32_16x16x32_bf16 v[164:167], a[204:207], a[180:183], v[164:167]\n"
    "v_mfma_f32_16x16x32_bf16 v[164:167], a[200:203], a[148:151], v[164:167]\n"
    "s_add_u32 m0, s68, 67584\n"
    "s_addk_i32 s59, 16384\n"
    "v_mfma_f32_16x16x32_bf16 v[196:199], a[200:203], a[152:155], v[196:199]\n"
    "buffer_load_dwordx4 v255, s[24:27], s59 offen  lds\n"
    "v_mfma_f32_16x16x32_bf16 v[196:199], a[204:207], a[184:187], v[196:199]\n"
    "v_mfma_f32_16x16x32_bf16 v[228:231], a[204:207], a[188:191], v[228:231]\n"
    "v_mfma_f32_16x16x32_bf16 v[228:231], a[200:203], a[156:159], v[228:231]\n"
    "v_add_u32 v252, 0xfffe0000, v252\n"
    "v_add_u32 v253, 0xfffe0000, v253\n"
    "v_mfma_f32_16x16x32_bf16 v[232:235], a[208:211], a[156:159], v[232:235]\n"
    "s_waitcnt vmcnt(21)\n"
    "ds_read_b64_tr_b16 a[224:225], v252 offset:0\n"
    "ds_read_b64_tr_b16 a[226:227], v252 offset:512\n"
    "v_mfma_f32_16x16x32_bf16 v[232:235], a[212:215], a[188:191], v[232:235]\n"
    "s_add_u32 m0, s68, 68608\n"
    "s_addk_i32 s59, 16384\n"
    "v_mfma_f32_16x16x32_bf16 v[200:203], a[212:215], a[184:187], v[200:203]\n"
    "buffer_load_dwordx4 v243, s[24:27], s59 offen  lds\n"
    "v_mfma_f32_16x16x32_bf16 v[200:203], a[208:211], a[152:155], v[200:203]\n"
    "ds_read_b64_tr_b16 a[228:229], v252 offset:4096\n"
    "ds_read_b64_tr_b16 a[230:231], v252 offset:4608\n"
    "v_mfma_f32_16x16x32_bf16 v[168:171], a[208:211], a[148:151], v[168:171]\n"
    "ds_read_b64_tr_b16 a[232:233], v252 offset:8\n"
    "ds_read_b64_tr_b16 a[234:235], v252 offset:520\n"
    "v_mfma_f32_16x16x32_bf16 v[168:171], a[212:215], a[180:183], v[168:171]\n"
    "s_add_u32 m0, s79, 131072\n"
    "s_lshl_b32 s54, s64, 17\n"
    "s_xor_b32 s54, s54, s57\n"
    "v_mfma_f32_16x16x32_bf16 v[136:139], a[212:215], a[176:179], v[136:139]\n"
    "s_addk_i32 s54, 512\n"
    "v_mfma_f32_16x16x32_bf16 v[136:139], a[208:211], a[144:147], v[136:139]\n"
    "buffer_load_dwordx4 v254, s[72:75], s54 offen  lds\n"
    "v_mfma_f32_16x16x32_bf16 v[104:107], a[208:211], a[140:143], v[104:107]\n"
    "ds_read_b64_tr_b16 a[236:237], v252 offset:4104\n"
    "ds_read_b64_tr_b16 a[238:239], v252 offset:4616\n"
    "v_mfma_f32_16x16x32_bf16 v[104:107], a[212:215], a[172:175], v[104:107]\n"
    "ds_read_b64_tr_b16 a[192:193], v253 offset:0\n"
    "ds_read_b64_tr_b16 a[194:195], v253 offset:512\n"
    "v_mfma_f32_16x16x32_bf16 v[72:75], a[212:215], a[168:171], v[72:75]\n"
    "s_addk_i32 m0, 1024\n"
    "s_addk_i32 s54, 16384\n"
    "v_mfma_f32_16x16x32_bf16 v[72:75], a[208:211], a[136:139], v[72:75]\n"
    "buffer_load_dwordx4 v254, s[72:75], s54 offen  lds\n"
    "v_mfma_f32_16x16x32_bf16 v[40:43], a[208:211], a[132:135], v[40:43]\n"
    "ds_read_b64_tr_b16 a[196:197], v253 offset:4096\n"
    "ds_read_b64_tr_b16 a[198:199], v253 offset:4608\n"
    "v_mfma_f32_16x16x32_bf16 v[40:43], a[212:215], a[164:167], v[40:43]\n"
    "ds_read_b64_tr_b16 a[200:201], v253 offset:8\n"
    "ds_read_b64_tr_b16 a[202:203], v253 offset:520\n"
    "v_mfma_f32_16x16x32_bf16 v[8:11], a[212:215], a[160:163], v[8:11]\n"
    "s_addk_i32 m0, 1024\n"
    "s_addk_i32 s54, 16384\n"
    "v_mfma_f32_16x16x32_bf16 v[8:11], a[208:211], a[128:131], v[8:11]\n"
    "buffer_load_dwordx4 v254, s[72:75], s54 offen  lds\n"
    "v_mfma_f32_16x16x32_bf16 v[12:15], a[216:219], a[128:131], v[12:15]\n"
    "ds_read_b64_tr_b16 a[204:205], v253 offset:4104\n"
    "ds_read_b64_tr_b16 a[206:207], v253 offset:4616\n"
    "v_mfma_f32_16x16x32_bf16 v[12:15], a[220:223], a[160:163], v[12:15]\n"
    "s_addk_i32 m0, 1024\n"
    "s_addk_i32 s54, 16384\n"
    "v_mfma_f32_16x16x32_bf16 v[44:47], a[220:223], a[164:167], v[44:47]\n"
    "buffer_load_dwordx4 v254, s[72:75], s54 offen  lds\n"
    "v_mfma_f32_16x16x32_bf16 v[44:47], a[216:219], a[132:135], v[44:47]\n"
    "s_addk_i32 m0, 1024\n"
    "s_addk_i32 s54, 16384\n"
    "v_mfma_f32_16x16x32_bf16 v[76:79], a[216:219], a[136:139], v[76:79]\n"
    "buffer_load_dwordx4 v254, s[72:75], s54 offen  lds\n"
    "v_mfma_f32_16x16x32_bf16 v[76:79], a[220:223], a[168:171], v[76:79]\n"
    "v_mfma_f32_16x16x32_bf16 v[108:111], a[220:223], a[172:175], v[108:111]\n"
    "v_mfma_f32_16x16x32_bf16 v[108:111], a[216:219], a[140:143], v[108:111]\n"
    "s_addk_i32 m0, 1024\n"
    "s_addk_i32 s54, 16384\n"
    "v_mfma_f32_16x16x32_bf16 v[140:143], a[216:219], a[144:147], v[140:143]\n"
    "buffer_load_dwordx4 v254, s[72:75], s54 offen  lds\n"
    "v_mfma_f32_16x16x32_bf16 v[140:143], a[220:223], a[176:179], v[140:143]\n"
    "v_mfma_f32_16x16x32_bf16 v[172:175], a[220:223], a[180:183], v[172:175]\n"
    "v_mfma_f32_16x16x32_bf16 v[172:175], a[216:219], a[148:151], v[172:175]\n"
    "s_addk_i32 m0, 1024\n"
    "s_addk_i32 s54, 16384\n"
    "v_mfma_f32_16x16x32_bf16 v[204:207], a[216:219], a[152:155], v[204:207]\n"
    "buffer_load_dwordx4 v254, s[72:75], s54 offen  lds\n"
    "v_mfma_f32_16x16x32_bf16 v[204:207], a[220:223], a[184:187], v[204:207]\n"
    "v_mfma_f32_16x16x32_bf16 v[236:239], a[220:223], a[188:191], v[236:239]\n"
    "v_mfma_f32_16x16x32_bf16 v[236:239], a[216:219], a[156:159], v[236:239]\n"
    "s_waitcnt lgkmcnt(12)\n"
    "s_addk_i32 m0, 1024\n"
    "s_addk_i32 s54, 16384\n"
    "v_mfma_f32_16x16x32_bf16 a[240:243], a[224:227], a[156:159], a[240:243]\n"
    "buffer_load_dwordx4 v254, s[72:75], s54 offen  lds\n"
    "v_mfma_f32_16x16x32_bf16 a[240:243], a[228:231], a[188:191], a[240:243]\n"
    "s_add_u32 s57, s57, 131072\n"
    "s_cmp_ge_u32 s57, 1572864\n"
    "s_cselect_b32 s57, s70, s57\n"
    "v_mfma_f32_16x16x32_bf16 v[208:211], a[228:231], a[184:187], v[208:211]\n"
    "v_mfma_f32_16x16x32_bf16 v[208:211], a[224:227], a[152:155], v[208:211]\n"
    "s_waitcnt lgkmcnt(0)\n"
    "s_add_u32 m0, s79, 0\n"
    "s_lshl_b32 s54, s64, 17\n"
    "v_mfma_f32_16x16x32_bf16 v[176:179], a[224:227], a[148:151], v[176:179]\n"
    "s_xor_b32 s54, s54, s57\n"
    "v_mfma_f32_16x16x32_bf16 v[176:179], a[228:231], a[180:183], v[176:179]\n"
    "buffer_load_dwordx4 v254, s[72:75], s54 offen  lds\n"
    "v_mfma_f32_16x16x32_bf16 v[144:147], a[228:231], a[176:179], v[144:147]\n"
    "v_mfma_f32_16x16x32_bf16 v[144:147], a[224:227], a[144:147], v[144:147]\n"
    "s_addk_i32 m0, 1024\n"
    "s_addk_i32 s54, 16384\n"
    "v_mfma_f32_16x16x32_bf16 v[112:115], a[224:227], a[140:143], v[112:115]\n"
    "buffer_load_dwordx4 v254, s[72:75], s54 offen  lds\n"
    "v_mfma_f32_16x16x32_bf16 v[112:115], a[228:231], a[172:175], v[112:115]\n"
    "v_mfma_f32_16x16x32_bf16 v[80:83], a[228:231], a[168:171], v[80:83]\n"
    "v_mfma_f32_16x16x32_bf16 v[80:83], a[224:227], a[136:139], v[80:83]\n"
    "s_addk_i32 m0, 1024\n"
    "s_addk_i32 s54, 16384\n"
    "v_mfma_f32_16x16x32_bf16 v[48:51], a[224:227], a[132:135], v[48:51]\n"
    "buffer_load_dwordx4 v254, s[72:75], s54 offen  lds\n"
    "v_mfma_f32_16x16x32_bf16 v[48:51], a[228:231], a[164:167], v[48:51]\n"
    "v_mfma_f32_16x16x32_bf16 v[16:19], a[228:231], a[160:163], v[16:19]\n"
    "v_mfma_f32_16x16x32_bf16 v[16:19], a[224:227], a[128:131], v[16:19]\n"
    "s_addk_i32 m0, 1024\n"
    "s_addk_i32 s54, 16384\n"
    "v_mfma_f32_16x16x32_bf16 v[20:23], a[232:235], a[128:131], v[20:23]\n"
    "buffer_load_dwordx4 v254, s[72:75], s54 offen  lds\n"
    "v_mfma_f32_16x16x32_bf16 v[20:23], a[236:239], a[160:163], v[20:23]\n"
    "v_mfma_f32_16x16x32_bf16 v[52:55], a[236:239], a[164:167], v[52:55]\n"
    "v_mfma_f32_16x16x32_bf16 v[52:55], a[232:235], a[132:135], v[52:55]\n"
    "s_addk_i32 m0, 1024\n"
    "s_addk_i32 s54, 16384\n"
    "v_mfma_f32_16x16x32_bf16 v[84:87], a[232:235], a[136:139], v[84:87]\n"
    "buffer_load_dwordx4 v254, s[72:75], s54 offen  lds\n"
    "v_mfma_f32_16x16x32_bf16 v[84:87], a[236:239], a[168:171], v[84:87]\n"
    "v_mfma_f32_16x16x32_bf16 v[116:119], a[236:239], a[172:175], v[116:119]\n"
    "v_mfma_f32_16x16x32_bf16 v[116:119], a[232:235], a[140:143], v[116:119]\n"
    "s_addk_i32 m0, 1024\n"
    "s_addk_i32 s54, 16384\n"
    "v_mfma_f32_16x16x32_bf16 v[148:151], a[232:235], a[144:147], v[148:151]\n"
    "buffer_load_dwordx4 v254, s[72:75], s54 offen  lds\n"
    "v_mfma_f32_16x16x32_bf16 v[148:151], a[236:239], a[176:179], v[148:151]\n"
    "v_mfma_f32_16x16x32_bf16 v[180:183], a[236:239], a[180:183], v[180:183]\n"
    "v_mfma_f32_16x16x32_bf16 v[180:183], a[232:235], a[148:151], v[180:183]\n"
    "s_addk_i32 m0, 1024\n"
    "s_addk_i32 s54, 16384\n"
    "v_mfma_f32_16x16x32_bf16 v[212:215], a[232:235], a[152:155], v[212:215]\n"
    "buffer_load_dwordx4 v254, s[72:75], s54 offen  lds\n"
    "v_mfma_f32_16x16x32_bf16 v[212:215], a[236:239], a[184:187], v[212:215]\n"
    "v_mfma_f32_16x16x32_bf16 a[244:247], a[236:239], a[188:191], a[244:247]\n"
    "v_mfma_f32_16x16x32_bf16 a[244:247], a[232:235], a[156:159], a[244:247]\n"
    "s_waitcnt vmcnt(19)\n"
    "ds_read_b64_tr_b16 a[208:209], v252 offset:32768\n"
    "ds_read_b64_tr_b16 a[210:211], v252 offset:33280\n"
    "v_mfma_f32_16x16x32_bf16 a[248:251], a[192:195], a[156:159], a[248:251]\n"
    "s_addk_i32 m0, 1024\n"
    "s_addk_i32 s54, 16384\n"
    "v_mfma_f32_16x16x32_bf16 a[248:251], a[196:199], a[188:191], a[248:251]\n"
    "buffer_load_dwordx4 v254, s[72:75], s54 offen  lds\n"
    "v_mfma_f32_16x16x32_bf16 v[216:219], a[196:199], a[184:187], v[216:219]\n"
    "ds_read_b64_tr_b16 a[212:213], v252 offset:36864\n"
    "ds_read_b64_tr_b16 a[214:215], v252 offset:37376\n"
    "v_mfma_f32_16x16x32_bf16 v[216:219], a[192:195], a[152:155], v[216:219]\n"
    "ds_read_b64_tr_b16 a[216:217], v252 offset:32776\n"
    "ds_read_b64_tr_b16 a[218:219], v252 offset:33288\n"
    "v_mfma_f32_16x16x32_bf16 v[184:187], a[192:195], a[148:151], v[184:187]\n"
    "v_mfma_f32_16x16x32_bf16 v[184:187], a[196:199], a[180:183], v[184:187]\n"
    "ds_read_b64_tr_b16 a[220:221], v252 offset:36872\n"
    "ds_read_b64_tr_b16 a[222:223], v252 offset:37384\n"
    "v_mfma_f32_16x16x32_bf16 v[152:155], a[196:199], a[176:179], v[152:155]\n"
    "v_mfma_f32_16x16x32_bf16 v[152:155], a[192:195], a[144:147], v[152:155]\n"
    "ds_read_b64_tr_b16 a[224:225], v253 offset:32768\n"
    "ds_read_b64_tr_b16 a[226:227], v253 offset:33280\n"
    "v_mfma_f32_16x16x32_bf16 v[120:123], a[192:195], a[140:143], v[120:123]\n"
    "v_mfma_f32_16x16x32_bf16 v[120:123], a[196:199], a[172:175], v[120:123]\n"
    "ds_read_b64_tr_b16 a[228:229], v253 offset:36864\n"
    "ds_read_b64_tr_b16 a[230:231], v253 offset:37376\n"
    "v_mfma_f32_16x16x32_bf16 v[88:91], a[196:199], a[168:171], v[88:91]\n"
    "v_mfma_f32_16x16x32_bf16 v[88:91], a[192:195], a[136:139], v[88:91]\n"
    "ds_read_b64_tr_b16 a[232:233], v253 offset:32776\n"
    "ds_read_b64_tr_b16 a[234:235], v253 offset:33288\n"
    "v_mfma_f32_16x16x32_bf16 v[56:59], a[192:195], a[132:135], v[56:59]\n"
    "v_mfma_f32_16x16x32_bf16 v[56:59], a[196:199], a[164:167], v[56:59]\n"
    "v_cmp_ne_u32 vcc, v246, v248\n"
    "s_and_b32 s61, s86, 1\n"
    "s_nop 0\n"
    "s_andn2_b32 vcc_lo, vcc_lo, s61\n"
    "s_cmp_eq_u64 vcc, 0\n"
    "s_cbranch_scc1 LA1_%=\n"
    "s_mov_b32 s69, 0x400000\n"
    "LA2_%=:\n"
    "s_sleep 2\n"
    "v_lshrrev_b32 v246, 1, v240\n"
    "s_lshl_b32 s61, s65, 9\n"
    "s_mov_b32 s82, s87\n"
    "buffer_load_dwordx2 v[246:247], v246, s[80:83], s61 offen sc1\n"
    "s_waitcnt vmcnt(0)\n"
    "s_sub_u32 s69, s69, 1\n"
    "s_cbranch_scc1 LA1_%=\n"
    "v_cmp_ne_u32 vcc, v246, v248\n"
    "s_and_b32 s61, s86, 1\n"
    "s_nop 0\n"
    "s_andn2_b32 vcc_lo, vcc_lo, s61\n"
    "s_cmp_eq_u64 vcc, 0\n"
    "s_cbranch_scc0 LA2_%=\n"
    "LA1_%=:\n"
    "s_barrier\n"
    "v_mfma_f32_16x16x32_bf16 v[24:27], a[196:199], a[160:163], v[24:27]\n"
    "ds_read_b64_tr_b16 a[236:237], v253 offset:36872\n"
    "ds_read_b64_tr_b16 a[238:239], v253 offset:37384\n"
    "v_mfma_f32_16x16x32_bf16 v[24:27], a[192:195], a[128:131], v[24:27]\n"
    "v_mfma_f32_16x16x32_bf16 v[28:31], a[200:203], a[128:131], v[28:31]\n"
    "v_mfma_f32_16x16x32_bf16 v[28:31], a[204:207], a[160:163], v[28:31]\n"
    "v_mfma_f32_16x16x32_bf16 v[60:63], a[204:207], a[164:167], v[60:63]\n"
    "ds_read_b128 a[128:131], v241 offset:32768\n"
    "v_mfma_f32_16x16x32_bf16 v[60:63], a[200:203], a[132:135], v[60:63]\n"
    "s_waitcnt lgkmcnt(1)\n"
    "s_add_u32 m0, s79, 32768\n"
    "s_lshl_b32 s54, s64, 17\n"
    "v_mfma_f32_16x16x32_bf16 v[92:95], a[200:203], a[136:139], v[92:95]\n"
    "s_xor_b32 s54, s54, s57\n"
    "s_addk_i32 s54, 512\n"
    "v_mfma_f32_16x16x32_bf16 v[92:95], a[204:207], a[168:171], v[92:95]\n"
    "buffer_load_dwordx4 v254, s[72:75], s54 offen  lds\n"
    "v_mfma_f32_16x16x32_bf16 v[124:127], a[204:207], a[172:175], v[124:127]\n"
    "ds_read_b128 a[160:163], v242 offset:32768\n"
    "ds_read_b128 a[164:167], v242 offset:34816\n"
    "v_mfma_f32_16x16x32_bf16 v[124:127], a[200:203], a[140:143], v[124:127]\n"
    "ds_read_b128 a[132:135], v241 offset:34816\n"
    "ds_read_b128 a[136:139], v241 offset:36864\n"
    "v_mfma_f32_16x16x32_bf16 v[156:159], a[200:203], a[144:147], v[156:159]\n"
    "s_addk_i32 m0, 1024\n"
    "s_addk_i32 s54, 16384\n"
    "v_mfma_f32_16x16x32_bf16 v[156:159], a[204:207], a[176:179], v[156:159]\n"
    "buffer_load_dwordx4 v254, s[72:75], s54 offen  lds\n"
    "v_mfma_f32_16x16x32_bf16 v[188:191], a[204:207], a[180:183], v[188:191]\n"
    "ds_read_b128 a[168:171], v242 offset:36864\n"
    "ds_read_b128 a[172:175], v242 offset:38912\n"
    "v_mfma_f32_16x16x32_bf16 v[188:191], a[200:203], a[148:151], v[188:191]\n"
    "ds_read_b128 a[140:143], v241 offset:38912\n"
    "ds_read_b128 a[144:147], v241 offset:40960\n"
    "v_mfma_f32_16x16x32_bf16 v[220:223], a[200:203], a[152:155], v[220:223]\n"
    "s_addk_i32 m0, 1024\n"
    "s_addk_i32 s54, 16384\n"
    "v_mfma_f32_16x16x32_bf16 v[220:223], a[204:207], a[184:187], v[220:223]\n"
    "buffer_load_dwordx4 v254, s[72:75], s54 offen  lds\n"
    "v_mfma_f32_16x16x32_bf16 a[252:255], a[204:207], a[188:191], a[252:255]\n"
    "ds_read_b128 a[176:179], v242 offset:40960\n"
    "ds_read_b128 a[180:183], v242 offset:43008\n"
    "v_mfma_f32_16x16x32_bf16 a[252:255], a[200:203], a[156:159], a[252:255]\n"
    "s_waitcnt lgkmcnt(9)\n"
    "ds_read_b128 a[148:151], v241 offset:43008\n"
    "ds_read_b128 a[152:155], v241 offset:45056\n"
    "v_mfma_f32_16x16x32_bf16 v[0:3], a[208:211], a[128:131], v[0:3]\n"
    "s_addk_i32 m0, 1024\n"
    "s_addk_i32 s54, 16384\n"
    "v_mfma_f32_16x16x32_bf16 v[0:3], a[212:215], a[160:163], v[0:3]\n"
    "s_waitcnt lgkmcnt(9)\n"
    "v_mfma_f32_16x16x32_bf16 v[32:35], a[212:215], a[164:167], v[32:35]\n"
    "buffer_load_dwordx4 v254, s[72:75], s54 offen  lds\n"
    "ds_read_b128 a[184:187], v242 offset:45056\n"
    "ds_read_b128 a[188:191], v242 offset:47104\n"
    "ds_read_b128 a[156:159], v241 offset:47104\n"
    "v_lshrrev_b32 v246, 2, v240\n"
    "v_mfma_f32_16x16x32_bf16 v[32:35], a[208:211], a[132:135], v[32:35]\n"
    "s_waitcnt lgkmcnt(10)\n"
    "s_lshl_b32 s61, s65, 8\n"
    "s_mov_b32 s82, s87\n"
    "v_mfma_f32_16x16x32_bf16 v[64:67], a[208:211], a[136:139], v[64:67]\n"
    "buffer_store_dword v247, v246, s[80:83], s61 offen offset:3072 sc1\n"
    "v_mfma_f32_16x16x32_bf16 v[64:67], a[212:215], a[168:171], v[64:67]\n"
    "s_waitcnt lgkmcnt(8)\n"
    "s_addk_i32 m0, 1024\n"
    "s_addk_i32 s54, 16384\n"
    "v_mfma_f32_16x16x32_bf16 v[96:99], a[212:215], a[172:175], v[96:99]\n"
    "buffer_load_dwordx4 v254, s[72:75], s54 offen  lds\n"
    "v_mfma_f32_16x16x32_bf16 v[96:99], a[208:211], a[140:143], v[96:99]\n"
    "s_waitcnt lgkmcnt(6)\n"
    "s_addk_i32 m0, 1024\n"
    "s_addk_i32 s54, 16384\n"
    "v_mfma_f32_16x16x32_bf16 v[128:131], a[208:211], a[144:147], v[128:131]\n"
    "buffer_load_dwordx4 v254, s[72:75], s54 offen  lds\n"
    "v_mfma_f32_16x16x32_bf16 v[128:131], a[212:215], a[176:179], v[128:131]\n"
    "s_waitcnt lgkmcnt(4)\n"
    "v_mfma_f32_16x16x32_bf16 v[160:163], a[212:215], a[180:183], v[160:163]\n"
    "v_mfma_f32_16x16x32_bf16 v[160:163], a[208:211], a[148:151], v[160:163]\n"
    "s_waitcnt lgkmcnt(2)\n"
    "s_addk_i32 m0, 1024\n"
    "s_addk_i32 s54, 16384\n"
    "v_mfma_f32_16x16x32_bf16 v[192:195], a[208:211], a[152:155], v[192:195]\n"
    "buffer_load_dwordx4 v254, s[72:75], s54 offen  lds\n"
    "v_mfma_f32_16x16x32_bf16 v[192:195], a[212:215], a[184:187], v[192:195]\n"
    "s_waitcnt lgkmcnt(0)\n"
    "v_mfma_f32_16x16x32_bf16 v[224:227], a[212:215], a[188:191], v[224:227]\n"
    "v_mfma_f32_16x16x32_bf16 v[224:227], a[208:211], a[156:159], v[224:227]\n"
    "s_addk_i32 m0, 1024\n"
    "s_addk_i32 s54, 16384\n"
    "v_mfma_f32_16x16x32_bf16 v[228:231], a[216:219], a[156:159], v[228:231]\n"
    "buffer_load_dwordx4 v254, s[72:75], s54 offen  lds\n"
    "v_mfma_f32_16x16x32_bf16 v[228:231], a[220:223], a[188:191], v[228:231]\n"
    "s_add_u32 s57, s57, 131072\n"
    "s_cmp_ge_u32 s57, 1572864\n"
    "s_cselect_b32 s57, s70, s57\n"
    "v_mfma_f32_16x16x32_bf16 v[196:199], a[220:223], a[184:187], v[196:199]\n"
    "v_mfma_f32_16x16x32_bf16 v[196:199], a[216:219], a[152:155], v[196:199]\n"
    "v_mfma_f32_16x16x32_bf16 v[164:167], a[216:219], a[148:151], v[164:167]\n"
    "s_xor_b32 s59, s66, s67\n"
    "s_addk_i32 s67, 128\n"
    "s_cmpk_eq_u32 s67, 1536\n"
    "v_mfma_f32_16x16x32_bf16 v[164:167], a[220:223], a[180:183], v[164:167]\n"
    "s_cselect_b32 s67, 0, s67\n"
    "s_add_u32 m0, s68, 81920\n"
    "s_nop 0\n"
    "v_mfma_f32_16x16x32_bf16 v[132:135], a[220:223], a[176:179], v[132:135]\n"
    "buffer_load_dwordx4 v255, s[24:27], s59 offen  lds\n"
    "v_mfma_f32_16x16x32_bf16 v[132:135], a[216:219], a[144:147], v[132:135]\n"
    "s_add_u32 m0, s68, 82944\n"
    "s_addk_i32 s59, 16384\n"
    "v_mfma_f32_16x16x32_bf16 v[100:103], a[216:219], a[140:143], v[100:103]\n"
    "buffer_load_dwordx4 v243, s[24:27], s59 offen  lds\n"
    "s_bitcmp1_b32 s85, 8\n"
    "s_cbranch_scc1 CP8_%=\n"
    "CR8_%=:\n"
    "v_mfma_f32_16x16x32_bf16 v[100:103], a[220:223], a[172:175], v[100:103]\n"
    "v_mfma_f32_16x16x32_bf16 v[68:71], a[220:223], a[168:171], v[68:71]\n"
    "s_add_u32 m0, s68, 83968\n"
    "s_addk_i32 s59, 16384\n"
    "v_mfma_f32_16x16x32_bf16 v[68:71], a[216:219], a[136:139], v[68:71]\n"
    "buffer_load_dwordx4 v255, s[24:27], s59 offen  lds\n"
    "v_mfma_f32_16x16x32_bf16 v[36:39], a[216:219], a[132:135], v[36:39]\n"
    "v_mfma_f32_16x16x32_bf16 v[36:39], a[220:223], a[164:167], v[36:39]\n"
    "s_add_u32 m0, s68, 84992\n"
    "s_addk_i32 s59, 16384\n"
    "v_mfma_f32_16x16x32_bf16 v[4:7], a[220:223], a[160:163], v[4:7]\n"
    "buffer_load_dwordx4 v243, s[24:27], s59 offen  lds\n"
    "v_mfma_f32_16x16x32_bf16 v[4:7], a[216:219], a[128:131], v[4:7]\n"
    "v_add_u32 v252, 0x20000, v252\n"
    "v_add_u32 v253, 0x20000, v253\n"
    "v_mfma_f32_16x16x32_bf16 v[8:11], a[224:227], a[128:131], v[8:11]\n"
    "s_waitcnt vmcnt(21)\n"
    "ds_read_b64_tr_b16 a[192:193], v252 offset:0\n"
    "ds_read_b64_tr_b16 a[194:195], v252 offset:512\n"
    "v_mfma_f32_16x16x32_bf16 v[8:11], a[228:231], a[160:163], v[8:11]\n"
    "s_add_u32 m0, s68, 114688\n"
    "s_add_u32 s60, s78, 8388608\n"
    "v_mfma_f32_16x16x32_bf16 v[40:43], a[228:231], a[164:167], v[40:43]\n"
    "buffer_load_dwordx4 v254, s[72:75], s60 offen nt lds\n"
    "v_mfma_f32_16x16x32_bf16 v[40:43], a[224:227], a[132:135], v[40:43]\n"
    "ds_read_b64_tr_b16 a[196:197], v252 offset:4096\n"
    "ds_read_b64_tr_b16 a[198:199], v252 offset:4608\n"
    "v_mfma_f32_16x16x32_bf16 v[72:75], a[224:227], a[136:139], v[72:75]\n"
    "ds_read_b64_tr_b16 a[200:201], v252 offset:8\n"
    "ds_read_b64_tr_b16 a[202:203], v252 offset:520\n"
    "v_mfma_f32_16x16x32_bf16 v[72:75], a[228:231], a[168:171], v[72:75]\n"
    "s_add_u32 m0, s68, 115712\n"
    "s_add_u32 s60, s78, 8404992\n"
    "v_mfma_f32_16x16x32_bf16 v[104:107], a[228:231], a[172:175], v[104:107]\n"
    "buffer_load_dwordx4 v254, s[72:75], s60 offen nt lds\n"
    "v_mfma_f32_16x16x32_bf16 v[104:107], a[224:227], a[140:143], v[104:107]\n"
    "ds_read_b64_tr_b16 a[204:205], v252 offset:4104\n"
    "ds_read_b64_tr_b16 a[206:207], v252 offset:4616\n"
    "v_mfma_f32_16x16x32_bf16 v[136:139], a[224:227], a[144:147], v[136:139]\n"
    "s_add_u32 m0, s68, 116736\n"
    "s_add_u32 s60, s78, 8421376\n"
    "v_mfma_f32_16x16x32_bf16 v[136:139], a[228:231], a[176:179], v[136:139]\n"
    "buffer_load_dwordx4 v254, s[72:75], s60 offen nt lds\n"
    "v_mfma_f32_16x16x32_bf16 v[168:171], a[228:231], a[180:183], v[168:171]\n"
    "ds_read_b64_tr_b16 a[208:209], v253 offset:0\n"
    "ds_read_b64_tr_b16 a[210:211], v253 offset:512\n"
    "v_mfma_f32_16x16x32_bf16 v[168:171], a[224:227], a[148:151], v[168:171]\n"
    "ds_read_b64_tr_b16 a[212:213], v253 offset:4096\n"
    "ds_read_b64_tr_b16 a[214:215], v253 offset:4608\n"
    "v_mfma_f32_16x16x32_bf16 v[200:203], a[224:227], a[152:155], v[200:203]\n"
    "s_add_u32 m0, s68, 117760\n"
    "s_add_u32 s60, s78, 8437760\n"
    "v_mfma_f32_16x16x32_bf16 v[200:203], a[228:231], a[184:187], v[200:203]\n"
    "buffer_load_dwordx4 v254, s[72:75], s60 offen nt lds\n"
    "v_mfma_f32_16x16x32_bf16 v[232:235], a[228:231], a[188:191], v[232:235]\n"
    "ds_read_b64_tr_b16 a[216:217], v253 offset:8\n"
    "ds_read_b64_tr_b16 a[218:219], v253 offset:520\n"
    "v_mfma_f32_16x16x32_bf16 v[232:235], a[224:227], a[156:159], v[232:235]\n"
    "ds_read_b64_tr_b16 a[220:221], v253 offset:4104\n"
    "ds_read_b64_tr_b16 a[222:223], v253 offset:4616\n"
    "v_mfma_f32_16x16x32_bf16 v[236:239], a[232:235], a[156:159], v[236:239]\n"
    "v_mfma_f32_16x16x32_bf16 v[236:239], a[236:239], a[188:191], v[236:239]\n"
    "v_mfma_f32_16x16x32_bf16 v[204:207], a[236:239], a[184:187], v[204:207]\n"
    "v_mfma_f32_16x16x32_bf16 v[204:207], a[232:235], a[152:155], v[204:207]\n"
    "v_mfma_f32_16x16x32_bf16 v[172:175], a[232:235], a[148:151], v[172:175]\n"
    "v_mfma_f32_16x16x32_bf16 v[172:175], a[236:239], a[180:183], v[172:175]\n"
    "v_mfma_f32_16x16x32_bf16 v[140:143], a[236:239], a[176:179], v[140:143]\n"
    "v_mfma_f32_16x16x32_bf16 v[140:143], a[232:235], a[144:147], v[140:143]\n"
    "v_mfma_f32_16x16x32_bf16 v[108:111], a[232:235], a[140:143], v[108:111]\n"
    "v_mfma_f32_16x16x32_bf16 v[108:111], a[236:239], a[172:175], v[108:111]\n"
    "v_mfma_f32_16x16x32_bf16 v[76:79], a[236:239], a[168:171], v[76:79]\n"
    "v_mfma_f32_16x16x32_bf16 v[76:79], a[232:235], a[136:139], v[76:79]\n"
    "v_mfma_f32_16x16x32_bf16 v[44:47], a[232:235], a[132:135], v[44:47]\n"
    "v_mfma_f32_16x16x32_bf16 v[44:47], a[236:239], a[164:167], v[44:47]\n"
    "v_mfma_f32_16x16x32_bf16 v[12:15], a[236:239], a[160:163], v[12:15]\n"
    "v_mfma_f32_16x16x32_bf16 v[12:15], a[232:235], a[128:131], v[12:15]\n"
    "s_waitcnt lgkmcnt(12)\n"
    "v_mfma_f32_16x16x32_bf16 v[16:19], a[192:195], a[128:131], v[16:19]\n"
    "v_mfma_f32_16x16x32_bf16 v[16:19], a[196:199], a[160:163], v[16:19]\n"
    "v_mfma_f32_16x16x32_bf16 v[48:51], a[196:199], a[164:167], v[48:51]\n"
    "v_mfma_f32_16x16x32_bf16 v[48:51], a[192:195], a[132:135], v[48:51]\n"
    "v_mfma_f32_16x16x32_bf16 v[80:83], a[192:195], a[136:139], v[80:83]\n"
    "v_mfma_f32_16x16x32_bf16 v[80:83], a[196:199], a[168:171], v[80:83]\n"
    "v_mfma_f32_16x16x32_bf16 v[112:115], a[196:199], a[172:175], v[112:115]\n"
    "v_mfma_f32_16x16x32_bf16 v[112:115], a[192:195], a[140:143], v[112:115]\n"
    "v_mfma_f32_16x16x32_bf16 v[144:147], a[192:195], a[144:147], v[144:147]\n"
    "v_mfma_f32_16x16x32_bf16 v[144:147], a[196:199], a[176:179], v[144:147]\n"
    "v_mfma_f32_16x16x32_bf16 v[176:179], a[196:199], a[180:183], v[176:179]\n"
    "v_mfma_f32_16x16x32_bf16 v[176:179], a[192:195], a[148:151], v[176:179]\n"
    "v_mfma_f32_16x16x32_bf16 v[208:211], a[192:195], a[152:155], v[208:211]\n"
    "v_mfma_f32_16x16x32_bf16 v[208:211], a[196:199], a[184:187], v[208:211]\n"
    "v_mfma_f32_16x16x32_bf16 a[240:243], a[196:199], a[188:191], a[240:243]\n"
    "v_mfma_f32_16x16x32_bf16 a[240:243], a[192:195], a[156:159], a[240:243]\n"
    "s_waitcnt lgkmcnt(8)\n"
    "v_mfma_f32_16x16x32_bf16 a[244:247], a[200:203], a[156:159], a[244:247]\n"
    "v_mfma_f32_16x16x32_bf16 a[244:247], a[204:207], a[188:191], a[244:247]\n"
    "v_mfma_f32_16x16x32_bf16 v[212:215], a[204:207], a[184:187], v[212:215]\n"
    "v_mfma_f32_16x16x32_bf16 v[212:215], a[200:203], a[152:155], v[212:215]\n"
    "v_mfma_f32_16x16x32_bf16 v[180:183], a[200:203], a[148:151], v[180:183]\n"
    "v_mfma_f32_16x16x32_bf16 v[180:183], a[204:207], a[180:183], v[180:183]\n"
    "v_mfma_f32_16x16x32_bf16 v[148:151], a[204:207], a[176:179], v[148:151]\n"
    "v_mfma_f32_16x16x32_bf16 v[148:151], a[200:203], a[144:147], v[148:151]\n"
    "v_mfma_f32_16x16x32_bf16 v[116:119], a[200:203], a[140:143], v[116:119]\n"
    "v_mfma_f32_16x16x32_bf16 v[116:119], a[204:207], a[172:175], v[116:119]\n"
    "v_mfma_f32_16x16x32_bf16 v[84:87], a[204:207], a[168:171], v[84:87]\n"
    "v_mfma_f32_16x16x32_bf16 v[84:87], a[200:203], a[136:139], v[84:87]\n"
    "v_mfma_f32_16x16x32_bf16 v[52:55], a[200:203], a[132:135], v[52:55]\n"
    "v_mfma_f32_16x16x32_bf16 v[52:55], a[204:207], a[164:167], v[52:55]\n"
    "v_mfma_f32_16x16x32_bf16 v[20:23], a[204:207], a[160:163], v[20:23]\n"
    "v_mfma_f32_16x16x32_bf16 v[20:23], a[200:203], a[128:131], v[20:23]\n"
    "s_waitcnt lgkmcnt(4)\n"
    "v_add_u32 v252, 0xfffe0000, v252\n"
    "v_add_u32 v253, 0xfffe0000, v253\n"
    "v_mfma_f32_16x16x32_bf16 v[24:27], a[208:211], a[128:131], v[24:27]\n"
    "s_waitcnt vmcnt(17)\n"
    "ds_read_b64_tr_b16 a[224:225], v252 offset:0\n"
    "ds_read_b64_tr_b16 a[226:227], v252 offset:512\n"
    "v_mfma_f32_16x16x32_bf16 v[24:27], a[212:215], a[160:163], v[24:27]\n"
    "ds_read_b64_tr_b16 a[228:229], v252 offset:4096\n"
    "ds_read_b64_tr_b16 a[230:231], v252 offset:4608\n"
    "v_mfma_f32_16x16x32_bf16 v[56:59], a[212:215], a[164:167], v[56:59]\n"
    "v_mfma_f32_16x16x32_bf16 v[56:59], a[208:211], a[132:135], v[56:59]\n"
    "ds_read_b64_tr_b16 a[232:233], v252 offset:8\n"
    "ds_read_b64_tr_b16 a[234:235], v252 offset:520\n"
    "v_mfma_f32_16x16x32_bf16 v[88:91], a[208:211], a[136:139], v[88:91]\n"
    "v_mfma_f32_16x16x32_bf16 v[88:91], a[212:215], a[168:171], v[88:91]\n"
    "ds_read_b64_tr_b16 a[236:237], v252 offset:4104\n"
    "ds_read_b64_tr_b16 a[238:239], v252 offset:4616\n"
    "v_mfma_f32_16x16x32_bf16 v[120:123], a[212:215], a[172:175], v[120:123]\n"
    "v_mfma_f32_16x16x32_bf16 v[120:123], a[208:211], a[140:143], v[120:123]\n"
    "ds_read_b64_tr_b16 a[192:193], v253 offset:0\n"
    "ds_read_b64_tr_b16 a[194:195], v253 offset:512\n"
    "v_mfma_f32_16x16x32_bf16 v[152:155], a[208:211], a[144:147], v[152:155]\n"
    "v_mfma_f32_16x16x32_bf16 v[152:155], a[212:215], a[176:179], v[152:155]\n"
    "ds_read_b64_tr_b16 a[196:197], v253 offset:4096\n"
    "ds_read_b64_tr_b16 a[198:199], v253 offset:4608\n"
    "v_mfma_f32_16x16x32_bf16 v[184:187], a[212:215], a[180:183], v[184:187]\n"
    "v_mfma_f32_16x16x32_bf16 v[184:187], a[208:211], a[148:151], v[184:187]\n"
    "ds_read_b64_tr_b16 a[200:201], v253 offset:8\n"
    "ds_read_b64_tr_b16 a[202:203], v253 offset:520\n"
    "v_mfma_f32_16x16x32_bf16 v[216:219], a[208:211], a[152:155], v[216:219]\n"
    "v_mfma_f32_16x16x32_bf16 v[216:219], a[212:215], a[184:187], v[216:219]\n"
    "s_barrier\n"
    "ds_read_b64_tr_b16 a[204:205], v253 offset:4104\n"
    "ds_read_b64_tr_b16 a[206:207], v253 offset:4616\n"
    "v_mfma_f32_16x16x32_bf16 a[248:251], a[212:215], a[188:191], a[248:251]\n"
    "v_mfma_f32_16x16x32_bf16 a[248:251], a[208:211], a[156:159], a[248:251]\n"
    "s_waitcnt lgkmcnt(15)\n"
    "v_mfma_f32_16x16x32_bf16 a[252:255], a[216:219], a[156:159], a[252:255]\n"
    "v_mfma_f32_16x16x32_bf16 a[252:255], a[220:223], a[188:191], a[252:255]\n"
    "v_mfma_f32_16x16x32_bf16 v[220:223], a[220:223], a[184:187], v[220:223]\n"
    "ds_read_b128 a[156:159], v241 offset:14336\n"
    "v_mfma_f32_16x16x32_bf16 v[220:223], a[216:219], a[152:155], v[220:223]\n"
    "ds_read_b128 a[188:191], v242 offset:14336\n"
    "v_mfma_f32_16x16x32_bf16 v[188:191], a[216:219], a[148:151], v[188:191]\n"
    "ds_read_b128 a[184:187], v242 offset:12288\n"
    "v_mfma_f32_16x16x32_bf16 v[188:191], a[220:223], a[180:183], v[188:191]\n"
    "ds_read_b128 a[152:155], v241 offset:12288\n"
    "v_mfma_f32_16x16x32_bf16 v[156:159], a[220:223], a[176:179], v[156:159]\n"
    "ds_read_b128 a[148:151], v241 offset:10240\n"
    "v_mfma_f32_16x16x32_bf16 v[156:159], a[216:219], a[144:147], v[156:159]\n"
    "ds_read_b128 a[180:183], v242 offset:10240\n"
    "v_mfma_f32_16x16x32_bf16 v[124:127], a[216:219], a[140:143], v[124:127]\n"
    "ds_read_b128 a[176:179], v242 offset:8192\n"
    "v_mfma_f32_16x16x32_bf16 v[124:127], a[220:223], a[172:175], v[124:127]\n"
    "ds_read_b128 a[144:147], v241 offset:8192\n"
    "v_mfma_f32_16x16x32_bf16 v[92:95], a[220:223], a[168:171], v[92:95]\n"
    "ds_read_b128 a[140:143], v241 offset:6144\n"
    "v_mfma_f32_16x16x32_bf16 v[92:95], a[216:219], a[136:139], v[92:95]\n"
    "s_add_u32 m0, s79, 131072\n"
    "s_lshl_b32 s54, s64, 17\n"
    "s_xor_b32 s54, s54, s57\n"
    "v_mfma_f32_16x16x32_bf16 v[60:63], a[216:219], a[132:135], v[60:63]\n"
    "buffer_load_dwordx4 v254, s[72:75], s54 offen  lds\n"
    "v_mfma_f32_16x16x32_bf16 v[60:63], a[220:223], a[164:167], v[60:63]\n"
    "ds_read_b128 a[172:175], v242 offset:6144\n"
    "ds_read_b128 a[168:171], v242 offset:4096\n"
    "v_mfma_f32_16x16x32_bf16 v[28:31], a[220:223], a[160:163], v[28:31]\n"
    "ds_read_b128 a[136:139], v241 offset:4096\n"
    "ds_read_b128 a[132:135], v241 offset:2048\n"
    "v_mfma_f32_16x16x32_bf16 v[28:31], a[216:219], a[128:131], v[28:31]\n"
    "s_waitcnt lgkmcnt(11)\n"
    "s_addk_i32 m0, 1024\n"
    "s_addk_i32 s54, 16384\n"
    "v_mfma_f32_16x16x32_bf16 v[224:227], a[224:227], a[156:159], v[224:227]\n"
    "buffer_load_dwordx4 v254, s[72:75], s54 offen  lds\n"
    "v_mfma_f32_16x16x32_bf16 v[224:227], a[228:231], a[188:191], v[224:227]\n"
    "s_waitcnt lgkmcnt(9)\n"
    "ds_read_b128 a[164:167], v242 offset:2048\n"
    "ds_read_b128 a[160:163], v242 offset:0\n"
    "v_mfma_f32_16x16x32_bf16 v[192:195], a[228:231], a[184:187], v[192:195]\n"
    "ds_read_b128 a[128:131], v241 offset:0\n"
    "v_mfma_f32_16x16x32_bf16 v[192:195], a[224:227], a[152:155], v[192:195]\n"
    "s_waitcnt lgkmcnt(10)\n"
    "s_addk_i32 m0, 1024\n"
    "s_addk_i32 s54, 16384\n"
    "v_mfma_f32_16x16x32_bf16 v[160:163], a[224:227], a[148:151], v[160:163]\n"
    "buffer_load_dwordx4 v254, s[72:75], s54 offen  lds\n"
    "v_mfma_f32_16x16x32_bf16 v[160:163], a[228:231], a[180:183], v[160:163]\n"
    "s_waitcnt lgkmcnt(8)\n"
    "v_mfma_f32_16x16x32_bf16 v[128:131], a[228:231], a[176:179], v[128:131]\n"
    "v_mfma_f32_16x16x32_bf16 v[128:131], a[224:227], a[144:147], v[128:131]\n"
    "s_waitcnt lgkmcnt(6)\n"
    "s_addk_i32 m0, 1024\n"
    "s_addk_i32 s54, 16384\n"
    "v_mfma_f32_16x16x32_bf16 v[96:99], a[224:227], a[140:143], v[96:99]\n"
    "buffer_load_dwordx4 v254, s[72:75], s54 offen  lds\n"
    "v_mfma_f32_16x16x32_bf16 v[96:99], a[228:231], a[172:175], v[96:99]\n"
    "s_waitcnt lgkmcnt(4)\n"
    "v_mfma_f32_16x16x32_bf16 v[64:67], a[228:231], a[168:171], v[64:67]\n"
    "v_mfma_f32_16x16x32_bf16 v[64:67], a[224:227], a[136:139], v[64:67]\n"
    "s_waitcnt lgkmcnt(2)\n"
    "s_addk_i32 m0, 1024\n"
    "s_addk_i32 s54, 16384\n"
    "v_mfma_f32_16x16x32_bf16 v[32:35], a[224:227], a[132:135], v[32:35]\n"
    "buffer_load_dwordx4 v254, s[72:75], s54 offen  lds\n"
    "v_mfma_f32_16x16x32_bf16 v[32:35], a[228:231], a[164:167], v[32:35]\n"
    "s_waitcnt lgkmcnt(0)\n"
    "v_mfma_f32_16x16x32_bf16 v[0:3], a[228:231], a[160:163], v[0:3]\n"
    "v_mfma_f32_16x16x32_bf16 v[0:3], a[224:227], a[128:131], v[0:3]\n"
    "s_addk_i32 m0, 1024\n"
    "s_addk_i32 s54, 16384\n"
    "v_mfma_f32_16x16x32_bf16 v[4:7], a[232:235], a[128:131], v[4:7]\n"
    "buffer_load_dwordx4 v254, s[72:75], s54 offen  lds\n"
    "v_mfma_f32_16x16x32_bf16 v[4:7], a[236:239], a[160:163], v[4:7]\n"
    "v_mfma_f32_16x16x32_bf16 v[36:39], a[236:239], a[164:167], v[36:39]\n"
    "v_mfma_f32_16x16x32_bf16 v[36:39], a[232:235], a[132:135], v[36:39]\n"
    "s_addk_i32 m0, 1024\n"
    "s_addk_i32 s54, 16384\n"
    "v_mfma_f32_16x16x32_bf16 v[68:71], a[232:235], a[136:139], v[68:71]\n"
    "buffer_load_dwordx4 v254, s[72:75], s54 offen  lds\n"
    "v_mfma_f32_16x16x32_bf16 v[68:71], a[236:239], a[168:171], v[68:71]\n"
    "v_mfma_f32_16x16x32_bf16 v[100:103], a[236:239], a[172:175], v[100:103]\n"
    "v_mfma_f32_16x16x32_bf16 v[100:103], a[232:235], a[140:143], v[100:103]\n"
    "s_addk_i32 m0, 1024\n"
    "s_addk_i32 s54, 16384\n"
    "v_mfma_f32_16x16x32_bf16 v[132:135], a[232:235], a[144:147], v[132:135]\n"
    "buffer_load_dwordx4 v254, s[72:75], s54 offen  lds\n"
    "s_bitcmp1_b32 s85, 9\n"
    "s_cbranch_scc1 CP9_%=\n"
    "CR9_%=:\n"
    "v_mfma_f32_16x16x32_bf16 v[132:135], a[236:239], a[176:179], v[132:135]\n"
    "v_mfma_f32_16x16x32_bf16 v[164:167], a[236:239], a[180:183], v[164:167]\n"
    "v_mfma_f32_16x16x32_bf16 v[164:167], a[232:235], a[148:151], v[164:167]\n"
    "s_xor_b32 s59, s66, s67\n"
    "s_addk_i32 s67, 128\n"
    "s_cmpk_eq_u32 s67, 1536\n"
    "v_mfma_f32_16x16x32_bf16 v[196:199], a[232:235], a[152:155], v[196:199]\n"
    "s_cselect_b32 s67, 0, s67\n"
    "s_add_u32 m0, s68, 98304\n"
    "s_nop 0\n"
    "v_mfma_f32_16x16x32_bf16 v[196:199], a[236:239], a[184:187], v[196:199]\n"
    "buffer_load_dwordx4 v255, s[24:27], s59 offen  lds\n"
    "v_mfma_f32_16x16x32_bf16 v[228:231], a[236:239], a[188:191], v[228:231]\n"
    "v_mfma_f32_16x16x32_bf16 v[228:231], a[232:235], a[156:159], v[228:231]\n"
    "s_waitcnt vmcnt(17)\n"
    "ds_read_b64_tr_b16 a[208:209], v252 offset:32768\n"
    "ds_read_b64_tr_b16 a[210:211], v252 offset:33280\n"
    "v_mfma_f32_16x16x32_bf16 v[232:235], a[192:195], a[156:159], v[232:235]\n"
    "s_add_u32 m0, s68, 99328\n"
    "s_addk_i32 s59, 16384\n"
    "v_mfma_f32_16x16x32_bf16 v[232:235], a[196:199], a[188:191], v[232:235]\n"
    "buffer_load_dwordx4 v243, s[24:27], s59 offen  lds\n"
    "v_mfma_f32_16x16x32_bf16 v[200:203], a[196:199], a[184:187], v[200:203]\n"
    "ds_read_b64_tr_b16 a[212:213], v252 offset:36864\n"
    "ds_read_b64_tr_b16 a[214:215], v252 offset:37376\n"
    "v_mfma_f32_16x16x32_bf16 v[200:203], a[192:195], a[152:155], v[200:203]\n"
    "ds_read_b64_tr_b16 a[216:217], v252 offset:32776\n"
    "ds_read_b64_tr_b16 a[218:219], v252 offset:33288\n"
    "v_mfma_f32_16x16x32_bf16 v[168:171], a[192:195], a[148:151], v[168:171]\n"
    "s_add_u32 m0, s68, 100352\n"
    "s_addk_i32 s59, 16384\n"
    "v_mfma_f32_16x16x32_bf16 v[168:171], a[196:199], a[180:183], v[168:171]\n"
    "buffer_load_dwordx4 v255, s[24:27], s59 offen  lds\n"
    "v_mfma_f32_16x16x32_bf16 v[136:139], a[196:199], a[176:179], v[136:139]\n"
    "ds_read_b64_tr_b16 a[220:221], v252 offset:36872\n"
    "ds_read_b64_tr_b16 a[222:223], v252 offset:37384\n"
    "v_mfma_f32_16x16x32_bf16 v[136:139], a[192:195], a[144:147], v[136:139]\n"
    "ds_read_b64_tr_b16 a[224:225], v253 offset:32768\n"
    "ds_read_b64_tr_b16 a[226:227], v253 offset:33280\n"
    "v_mfma_f32_16x16x32_bf16 v[104:107], a[192:195], a[140:143], v[104:107]\n"
    "s_add_u32 m0, s68, 101376\n"
    "s_addk_i32 s59, 16384\n"
    "v_mfma_f32_16x16x32_bf16 v[104:107], a[196:199], a[172:175], v[104:107]\n"
    "buffer_load_dwordx4 v243, s[24:27], s59 offen  lds\n"
    "v_mfma_f32_16x16x32_bf16 v[72:75], a[196:199], a[168:171], v[72:75]\n"
    "ds_read_b64_tr_b16 a[228:229], v253 offset:36864\n"
    "ds_read_b64_tr_b16 a[230:231], v253 offset:37376\n"
    "v_mfma_f32_16x16x32_bf16 v[72:75], a[192:195], a[136:139], v[72:75]\n"
    "ds_read_b64_tr_b16 a[232:233], v253 offset:32776\n"
    "ds_read_b64_tr_b16 a[234:235], v253 offset:33288\n"
    "v_mfma_f32_16x16x32_bf16 v[40:43], a[192:195], a[132:135], v[40:43]\n"
    "s_add_u32 m0, s79, 0\n"
    "s_lshl_b32 s54, s64, 17\n"
    "s_xor_b32 s54, s54, s57\n"
    "v_mfma_f32_16x16x32_bf16 v[40:43], a[196:199], a[164:167], v[40:43]\n"
    "s_addk_i32 s54, 512\n"
    "v_mfma_f32_16x16x32_bf16 v[8:11], a[196:199], a[160:163], v[8:11]\n"
    "buffer_load_dwordx4 v254, s[72:75], s54 offen  lds\n"
    "v_mfma_f32_16x16x32_bf16 v[8:11], a[192:195], a[128:131], v[8:11]\n"
    "ds_read_b64_tr_b16 a[236:237], v253 offset:36872\n"
    "ds_read_b64_tr_b16 a[238:239], v253 offset:37384\n"
    "v_mfma_f32_16x16x32_bf16 v[12:15], a[200:203], a[128:131], v[12:15]\n"
    "s_addk_i32 m0, 1024\n"
    "s_addk_i32 s54, 16384\n"
    "v_mfma_f32_16x16x32_bf16 v[12:15], a[204:207], a[160:163], v[12:15]\n"
    "buffer_load_dwordx4 v254, s[72:75], s54 offen  lds\n"
    "v_mfma_f32_16x16x32_bf16 v[44:47], a[204:207], a[164:167], v[44:47]\n"
    "v_mfma_f32_16x16x32_bf16 v[44:47], a[200:203], a[132:135], v[44:47]\n"
    "s_addk_i32 m0, 1024\n"
    "s_addk_i32 s54, 16384\n"
    "v_mfma_f32_16x16x32_bf16 v[76:79], a[200:203], a[136:139], v[76:79]\n"
    "buffer_load_dwordx4 v254, s[72:75], s54 offen  lds\n"
    "v_mfma_f32_16x16x32_bf16 v[76:79], a[204:207], a[168:171], v[76:79]\n"
    "v_mfma_f32_16x16x32_bf16 v[108:111], a[204:207], a[172:175], v[108:111]\n"
    "v_mfma_f32_16x16x32_bf16 v[108:111], a[200:203], a[140:143], v[108:111]\n"
    "s_addk_i32 m0, 1024\n"
    "s_addk_i32 s54, 16384\n"
    "v_mfma_f32_16x16x32_bf16 v[140:143], a[200:203], a[144:147], v[140:143]\n"
    "buffer_load_dwordx4 v254, s[72:75], s54 offen  lds\n"
    "v_mfma_f32_16x16x32_bf16 v[140:143], a[204:207], a[176:179], v[140:143]\n"
    "v_mfma_f32_16x16x32_bf16 v[172:175], a[204:207], a[180:183], v[172:175]\n"
    "v_mfma_f32_16x16x32_bf16 v[172:175], a[200:203], a[148:151], v[172:175]\n"
    "s_addk_i32 m0, 1024\n"
    "s_addk_i32 s54, 16384\n"
    "v_mfma_f32_16x16x32_bf16 v[204:207], a[200:203], a[152:155], v[204:207]\n"
    "buffer_load_dwordx4 v254, s[72:75], s54 offen  lds\n"
    "v_mfma_f32_16x16x32_bf16 v[204:207], a[204:207], a[184:187], v[204:207]\n"
    "v_mfma_f32_16x16x32_bf16 v[236:239], a[204:207], a[188:191], v[236:239]\n"
    "v_mfma_f32_16x16x32_bf16 v[236:239], a[200:203], a[156:159], v[236:239]\n"
    "s_waitcnt lgkmcnt(12)\n"
    "s_addk_i32 m0, 1024\n"
    "s_addk_i32 s54, 16384\n"
    "v_mfma_f32_16x16x32_bf16 a[240:243], a[208:211], a[156:159], a[240:243]\n"
    "buffer_load_dwordx4 v254, s[72:75], s54 offen  lds\n"
    "v_mfma_f32_16x16x32_bf16 a[240:243], a[212:215], a[188:191], a[240:243]\n"
    "v_mfma_f32_16x16x32_bf16 v[208:211], a[212:215], a[184:187], v[208:211]\n"
    "v_add_u32 v246, s90, v252\n"
    "s_waitcnt vmcnt(18)\n"
    "v_mfma_f32_16x16x32_bf16 v[208:211], a[208:211], a[152:155], v[208:211]\n"
    "ds_read_b64_tr_b16 v[248:249], v246 offset:0\n"
    "ds_read_b64_tr_b16 v[250:251], v246 offset:512\n"
    "v_mfma_f32_16x16x32_bf16 v[176:179], a[208:211], a[148:151], v[176:179]\n"
    "s_addk_i32 m0, 1024\n"
    "s_addk_i32 s54, 16384\n"
    "v_mfma_f32_16x16x32_bf16 v[176:179], a[212:215], a[180:183], v[176:179]\n"
    "buffer_load_dwordx4 v254, s[72:75], s54 offen  lds\n"
    "v_mfma_f32_16x16x32_bf16 v[144:147], a[212:215], a[176:179], v[144:147]\n"
    "v_mfma_f32_16x16x32_bf16 v[144:147], a[208:211], a[144:147], v[144:147]\n"
    "s_addk_i32 m0, 1024\n"
    "s_addk_i32 s54, 16384\n"
    "v_mfma_f32_16x16x32_bf16 v[112:115], a[208:211], a[140:143], v[112:115]\n"
    "buffer_load_dwordx4 v254, s[72:75], s54 offen  lds\n"
    "v_mfma_f32_16x16x32_bf16 v[112:115], a[212:215], a[172:175], v[112:115]\n"
    "s_add_u32 s57, s57, 131072\n"
    "s_cmp_ge_u32 s57, 1572864\n"
    "s_cselect_b32 s57, s70, s57\n"
    "v_mfma_f32_16x16x32_bf16 v[80:83], a[212:215], a[168:171], v[80:83]\n"
    "v_mfma_f32_16x16x32_bf16 v[80:83], a[208:211], a[136:139], v[80:83]\n"
    "s_waitcnt lgkmcnt(2)\n"
    "s_add_u32 m0, s79, 32768\n"
    "s_lshl_b32 s54, s64, 17\n"
    "v_mfma_f32_16x16x32_bf16 v[48:51], a[208:211], a[132:135], v[48:51]\n"
    "s_xor_b32 s54, s54, s57\n"
    "v_mfma_f32_16x16x32_bf16 v[48:51], a[212:215], a[164:167], v[48:51]\n"
    "buffer_load_dwordx4 v254, s[72:75], s54 offen  lds\n"
    "v_mfma_f32_16x16x32_bf16 v[16:19], a[212:215], a[160:163], v[16:19]\n"
    "s_add_u32 s62, s91, 1048576\n"
    "s_waitcnt lgkmcnt(0)\n"
    "v_mfma_f32_16x16x32_bf16 v[16:19], a[208:211], a[128:131], v[16:19]\n"
    "buffer_store_dwordx4 v[248:251], v240, s[28:31], s62 offen offset:0 sc1\n"
    "s_nop 0\n"
    "s_addk_i32 m0, 1024\n"
    "s_addk_i32 s54, 16384\n"
    "v_mfma_f32_16x16x32_bf16 v[20:23], a[216:219], a[128:131], v[20:23]\n"
    "buffer_load_dwordx4 v254, s[72:75], s54 offen  lds\n"
    "v_mfma_f32_16x16x32_bf16 v[20:23], a[220:223], a[160:163], v[20:23]\n"
    "v_mfma_f32_16x16x32_bf16 v[52:55], a[220:223], a[164:167], v[52:55]\n"
    "v_add_u32 v246, s90, v252\n"
    "ds_read_b64_tr_b16 v[248:249], v246 offset:8\n"
    "v_mfma_f32_16x16x32_bf16 v[52:55], a[216:219], a[132:135], v[52:55]\n"
    "ds_read_b64_tr_b16 v[250:251], v246 offset:520\n"
    "s_addk_i32 m0, 1024\n"
    "v_mfma_f32_16x16x32_bf16 v[84:87], a[216:219], a[136:139], v[84:87]\n"
    "s_addk_i32 s54, 16384\n"
    "v_mfma_f32_16x16x32_bf16 v[84:87], a[220:223], a[168:171], v[84:87]\n"
    "buffer_load_dwordx4 v254, s[72:75], s54 offen  lds\n"
    "v_mfma_f32_16x16x32_bf16 v[116:119], a[220:223], a[172:175], v[116:119]\n"
    "v_mfma_f32_16x16x32_bf16 v[116:119], a[216:219], a[140:143], v[116:119]\n"
    "s_addk_i32 m0, 1024\n"
    "s_addk_i32 s54, 16384\n"
    "v_mfma_f32_16x16x32_bf16 v[148:151], a[216:219], a[144:147], v[148:151]\n"
    "buffer_load_dwordx4 v254, s[72:75], s54 offen  lds\n"
    "v_mfma_f32_16x16x32_bf16 v[148:151], a[220:223], a[176:179], v[148:151]\n"
    "v_mfma_f32_16x16x32_bf16 v[180:183], a[220:223], a[180:183], v[180:183]\n"
    "v_mfma_f32_16x16x32_bf16 v[180:183], a[216:219], a[148:151], v[180:183]\n"
    "s_addk_i32 m0, 1024\n"
    "s_addk_i32 s54, 16384\n"
    "v_mfma_f32_16x16x32_bf16 v[212:215], a[216:219], a[152:155], v[212:215]\n"
    "buffer_load_dwordx4 v254, s[72:75], s54 offen  lds\n"
    "v_mfma_f32_16x16x32_bf16 v[212:215], a[220:223], a[184:187], v[212:215]\n"
    "s_add_u32 s62, s91, 1048576\n"
    "s_waitcnt lgkmcnt(0)\n"
    "v_mfma_f32_16x16x32_bf16 a[244:247], a[220:223], a[188:191], a[244:247]\n"
    "buffer_store_dwordx4 v[248:251], v240, s[28:31], s62 offen offset:2048 sc1\n"
    "s_nop 0\n"
    "v_mfma_f32_16x16x32_bf16 a[244:247], a[216:219], a[156:159], a[244:247]\n"
    "v_add_u32 v252, 0x20000, v252\n"
    "v_add_u32 v253, 0x20000, v253\n"
    "v_mfma_f32_16x16x32_bf16 a[248:251], a[224:227], a[156:159], a[248:251]\n"
    "s_waitcnt vmcnt(19)\n"
    "ds_read_b64_tr_b16 a[192:193], v252 offset:0\n"
    "ds_read_b64_tr_b16 a[194:195], v252 offset:512\n"
    "v_mfma_f32_16x16x32_bf16 a[248:251], a[228:231], a[188:191], a[248:251]\n"
    "s_addk_i32 m0, 1024\n"
    "s_addk_i32 s54, 16384\n"
    "v_mfma_f32_16x16x32_bf16 v[216:219], a[228:231], a[184:187], v[216:219]\n"
    "buffer_load_dwordx4 v254, s[72:75], s54 offen  lds\n"
    "v_mfma_f32_16x16x32_bf16 v[216:219], a[224:227], a[152:155], v[216:219]\n"
    "ds_read_b64_tr_b16 a[196:197], v252 offset:4096\n"
    "ds_read_b64_tr_b16 a[198:199], v252 offset:4608\n"
    "v_mfma_f32_16x16x32_bf16 v[184:187], a[224:227], a[148:151], v[184:187]\n"
    "v_add_u32 v246, s90, v253\n"
    "v_add_u32 v246, 0xfffe0000, v246\n"
    "v_mfma_f32_16x16x32_bf16 v[184:187], a[228:231], a[180:183], v[184:187]\n"
    "ds_read_b64_tr_b16 v[248:249], v246 offset:0\n"
    "ds_read_b64_tr_b16 v[250:251], v246 offset:512\n"
    "v_mfma_f32_16x16x32_bf16 v[152:155], a[228:231], a[176:179], v[152:155]\n"
    "ds_read_b64_tr_b16 a[200:201], v252 offset:8\n"
    "ds_read_b64_tr_b16 a[202:203], v252 offset:520\n"
    "v_mfma_f32_16x16x32_bf16 v[152:155], a[224:227], a[144:147], v[152:155]\n"
    "s_addk_i32 m0, 1024\n"
    "s_addk_i32 s54, 16384\n"
    "v_mfma_f32_16x16x32_bf16 v[120:123], a[224:227], a[140:143], v[120:123]\n"
    "buffer_load_dwordx4 v254, s[72:75], s54 offen  lds\n"
    "ds_read_b64_tr_b16 a[204:205], v252 offset:4104\n"
    "ds_read_b64_tr_b16 a[206:207], v252 offset:4616\n"
    "v_mfma_f32_16x16x32_bf16 v[120:123], a[228:231], a[172:175], v[120:123]\n"
    "ds_read_b64_tr_b16 a[208:209], v253 offset:0\n"
    "ds_read_b64_tr_b16 a[210:211], v253 offset:512\n"
    "v_mfma_f32_16x16x32_bf16 v[88:91], a[228:231], a[168:171], v[88:91]\n"
    "s_addk_i32 m0, 1024\n"
    "s_addk_i32 s54, 16384\n"
    "v_mfma_f32_16x16x32_bf16 v[88:91], a[224:227], a[136:139], v[88:91]\n"
    "buffer_load_dwordx4 v254, s[72:75], s54 offen  lds\n"
    "v_mfma_f32_16x16x32_bf16 v[56:59], a[224:227], a[132:135], v[56:59]\n"
    "ds_read_b64_tr_b16 a[212:213], v253 offset:4096\n"
    "ds_read_b64_tr_b16 a[214:215], v253 offset:4608\n"
    "v_mfma_f32_16x16x32_bf16 v[56:59], a[228:231], a[164:167], v[56:59]\n"
    "ds_read_b64_tr_b16 a[216:217], v253 offset:8\n"
    "ds_read_b64_tr_b16 a[218:219], v253 offset:520\n"
    "s_add_u32 s62, s91, 1052672\n"
    "s_waitcnt lgkmcnt(10)\n"
    "buffer_store_dwordx4 v[248:251], v240, s[28:31], s62 offen offset:0 sc1\n"
    "s_nop 0\n"
    "s_barrier\n"
    "ds_read_b64_tr_b16 a[220:221], v253 offset:4104\n"
    "v_mfma_f32_16x16x32_bf16 v[24:27], a[228:231], a[160:163], v[24:27]\n"
    "ds_read_b64_tr_b16 a[222:223], v253 offset:4616\n"
    "v_mfma_f32_16x16x32_bf16 v[24:27], a[224:227], a[128:131], v[24:27]\n"
    "s_add_u32 s82, s77, 3076\n"
    "s_and_b32 s82, s82, s86\n"
    "v_mfma_f32_16x16x32_bf16 v[28:31], a[232:235], a[128:131], v[28:31]\n"
    "buffer_load_dword v244, v240, s[80:83], s77 offen offset:3072 sc1\n"
    "v_mfma_f32_16x16x32_bf16 v[28:31], a[236:239], a[160:163], v[28:31]\n"
    "v_mfma_f32_16x16x32_bf16 v[60:63], a[236:239], a[164:167], v[60:63]\n"
    "v_add_u32 v246, s90, v253\n"
    "v_add_u32 v246, 0xfffe0000, v246\n"
    "v_mfma_f32_16x16x32_bf16 v[60:63], a[232:235], a[132:135], v[60:63]\n"
    "ds_read_b64_tr_b16 v[248:249], v246 offset:8\n"
    "ds_read_b64_tr_b16 v[250:251], v246 offset:520\n"
    "v_mfma_f32_16x16x32_bf16 v[92:95], a[232:235], a[136:139], v[92:95]\n"
    "ds_read_b128 a[128:131], v241 offset:16384\n"
    "s_waitcnt lgkmcnt(3)\n"
    "v_mfma_f32_16x16x32_bf16 v[92:95], a[236:239], a[168:171], v[92:95]\n"
    "s_add_u32 m0, s79, 131072\n"
    "s_lshl_b32 s54, s64, 17\n"
    "s_xor_b32 s54, s54, s57\n"
    "v_mfma_f32_16x16x32_bf16 v[124:127], a[236:239], a[172:175], v[124:127]\n"
    "s_addk_i32 s54, 512\n"
    "v_mfma_f32_16x16x32_bf16 v[124:127], a[232:235], a[140:143], v[124:127]\n"
    "buffer_load_dwordx4 v254, s[72:75], s54 offen  lds\n"
    "ds_read_b128 a[160:163], v242 offset:16384\n"
    "ds_read_b128 a[164:167], v242 offset:18432\n"
    "v_mfma_f32_16x16x32_bf16 v[156:159], a[232:235], a[144:147], v[156:159]\n"
    "ds_read_b128 a[132:135], v241 offset:18432\n"
    "ds_read_b128 a[136:139], v241 offset:20480\n"
    "v_mfma_f32_16x16x32_bf16 v[156:159], a[236:239], a[176:179], v[156:159]\n"
    "s_addk_i32 m0, 1024\n"
    "s_addk_i32 s54, 16384\n"
    "v_mfma_f32_16x16x32_bf16 v[188:191], a[236:239], a[180:183], v[188:191]\n"
    "buffer_load_dwordx4 v254, s[72:75], s54 offen  lds\n"
    "ds_read_b128 a[168:171], v242 offset:20480\n"
    "v_mfma_f32_16x16x32_bf16 v[188:191], a[232:235], a[148:151], v[188:191]\n"
    "ds_read_b128 a[172:175], v242 offset:22528\n"
    "ds_read_b128 a[140:143], v241 offset:22528\n"
    "v_mfma_f32_16x16x32_bf16 v[220:223], a[232:235], a[152:155], v[220:223]\n"
    "ds_read_b128 a[144:147], v241 offset:24576\n"
    "s_addk_i32 m0, 1024\n"
    "v_mfma_f32_16x16x32_bf16 v[220:223], a[236:239], a[184:187], v[220:223]\n"
    "s_addk_i32 s54, 16384\n"
    "v_mfma_f32_16x16x32_bf16 a[252:255], a[236:239], a[188:191], a[252:255]\n"
    "buffer_load_dwordx4 v254, s[72:75], s54 offen  lds\n"
    "ds_read_b128 a[176:179], v242 offset:24576\n"
    "v_mfma_f32_16x16x32_bf16 a[252:255], a[232:235], a[156:159], a[252:255]\n"
    "s_waitcnt lgkmcnt(8)\n"
    "s_add_u32 s62, s91, 1052672\n"
    "s_waitcnt lgkmcnt(10)\n"
    "v_mfma_f32_16x16x32_bf16 v[0:3], a[192:195], a[128:131], v[0:3]\n"
    "buffer_store_dwordx4 v[248:251], v240, s[28:31], s62 offen offset:2048 sc1\n"
    "s_nop 0\n"
    "ds_read_b128 a[180:183], v242 offset:26624\n"
    "ds_read_b128 a[148:151], v241 offset:26624\n"
    "v_mfma_f32_16x16x32_bf16 v[0:3], a[196:199], a[160:163], v[0:3]\n"
    "s_waitcnt lgkmcnt(8)\n"
    "ds_read_b128 a[152:155], v241 offset:28672\n"
    "s_addk_i32 m0, 1024\n"
    "v_mfma_f32_16x16x32_bf16 v[32:35], a[196:199], a[164:167], v[32:35]\n"
    "s_addk_i32 s54, 16384\n"
    "v_mfma_f32_16x16x32_bf16 v[32:35], a[192:195], a[132:135], v[32:35]\n"
    "s_waitcnt lgkmcnt(7)\n"
    "v_mfma_f32_16x16x32_bf16 v[64:67], a[192:195], a[136:139], v[64:67]\n"
    "buffer_load_dwordx4 v254, s[72:75], s54 offen  lds\n"
    "ds_read_b128 a[184:187], v242 offset:28672\n"
    "ds_read_b128 a[188:191], v242 offset:30720\n"
    "ds_read_b128 a[156:159], v241 offset:30720\n"
    "v_mfma_f32_16x16x32_bf16 v[64:67], a[196:199], a[168:171], v[64:67]\n"
    "s_waitcnt lgkmcnt(8)\n"
    "s_addk_i32 m0, 1024\n"
    "s_addk_i32 s54, 16384\n"
    "v_mfma_f32_16x16x32_bf16 v[96:99], a[196:199], a[172:175], v[96:99]\n"
    "buffer_load_dwordx4 v254, s[72:75], s54 offen  lds\n"
    "v_mfma_f32_16x16x32_bf16 v[96:99], a[192:195], a[140:143], v[96:99]\n"
    "s_waitcnt lgkmcnt(6)\n"
    "s_addk_i32 m0, 1024\n"
    "s_addk_i32 s54, 16384\n"
    "v_mfma_f32_16x16x32_bf16 v[128:131], a[192:195], a[144:147], v[128:131]\n"
    "buffer_load_dwordx4 v254, s[72:75], s54 offen  lds\n"
    "v_mfma_f32_16x16x32_bf16 v[128:131], a[196:199], a[176:179], v[128:131]\n"
    "s_waitcnt lgkmcnt(4)\n"
    "v_mfma_f32_16x16x32_bf16 v[160:163], a[196:199], a[180:183], v[160:163]\n"
    "v_mfma_f32_16x16x32_bf16 v[160:163], a[192:195], a[148:151], v[160:163]\n"
    "s_waitcnt lgkmcnt(2)\n"
    "s_addk_i32 m0, 1024\n"
    "s_addk_i32 s54, 16384\n"
    "v_mfma_f32_16x16x32_bf16 v[192:195], a[192:195], a[152:155], v[192:195]\n"
    "buffer_load_dwordx4 v254, s[72:75], s54 offen  lds\n"
    "v_mfma_f32_16x16x32_bf16 v[192:195], a[196:199], a[184:187], v[192:195]\n"
    "s_waitcnt lgkmcnt(0)\n"
    "v_mfma_f32_16x16x32_bf16 v[224:227], a[196:199], a[188:191], v[224:227]\n"
    "v_mfma_f32_16x16x32_bf16 v[224:227], a[192:195], a[156:159], v[224:227]\n"
    "s_addk_i32 m0, 1024\n"
    "s_addk_i32 s54, 16384\n"
    "v_mfma_f32_16x16x32_bf16 v[228:231], a[200:203], a[156:159], v[228:231]\n"
    "buffer_load_dwordx4 v254, s[72:75], s54 offen  lds\n"
    "v_mfma_f32_16x16x32_bf16 v[228:231], a[204:207], a[188:191], v[228:231]\n"
    "s_add_u32 s57, s57, 131072\n"
    "s_cmp_ge_u32 s57, 1572864\n"
    "s_cselect_b32 s57, s70, s57\n"
    "v_mfma_f32_16x16x32_bf16 v[196:199], a[204:207], a[184:187], v[196:199]\n"
    "v_mfma_f32_16x16x32_bf16 v[196:199], a[200:203], a[152:155], v[196:199]\n"
    "v_mfma_f32_16x16x32_bf16 v[164:167], a[200:203], a[148:151], v[164:167]\n"
    "v_mfma_f32_16x16x32_bf16 v[164:167], a[204:207], a[180:183], v[164:167]\n"
    "v_mfma_f32_16x16x32_bf16 v[132:135], a[204:207], a[176:179], v[132:135]\n"
    "s_xor_b32 s59, s66, 1536\n"
    "s_add_u32 m0, s68, 65536\n"
    "s_nop 0\n"
    "v_mfma_f32_16x16x32_bf16 v[132:135], a[200:203], a[144:147], v[132:135]\n"
    "buffer_load_dwordx4 v255, s[24:27], s59 offen  lds\n"
    "v_mfma_f32_16x16x32_bf16 v[100:103], a[200:203], a[140:143], v[100:103]\n"
    "s_bitcmp1_b32 s85, 10\n"
    "s_cbranch_scc1 CP10_%=\n"
    "CR10_%=:\n"
    "v_mfma_f32_16x16x32_bf16 v[100:103], a[204:207], a[172:175], v[100:103]\n"
    "s_add_u32 m0, s68, 66560\n"
    "s_addk_i32 s59, 16384\n"
    "v_mfma_f32_16x16x32_bf16 v[68:71], a[204:207], a[168:171], v[68:71]\n"
    "buffer_load_dwordx4 v243, s[24:27], s59 offen  lds\n"
    "v_mfma_f32_16x16x32_bf16 v[68:71], a[200:203], a[136:139], v[68:71]\n"
    "v_mfma_f32_16x16x32_bf16 v[36:39], a[200:203], a[132:135], v[36:39]\n"
    "s_add_u32 m0, s68, 67584\n"
    "s_addk_i32 s59, 16384\n"
    "v_mfma_f32_16x16x32_bf16 v[36:39], a[204:207], a[164:167], v[36:39]\n"
    "buffer_load_dwordx4 v255, s[24:27], s59 offen  lds\n"
    "v_mfma_f32_16x16x32_bf16 v[4:7], a[204:207], a[160:163], v[4:7]\n"
    "v_mfma_f32_16x16x32_bf16 v[4:7], a[200:203], a[128:131], v[4:7]\n"
    "v_add_u32 v252, 0xfffe0000, v252\n"
    "v_add_u32 v253, 0xfffe0000, v253\n"
    "v_mfma_f32_16x16x32_bf16 v[8:11], a[208:211], a[128:131], v[8:11]\n"
    "s_waitcnt vmcnt(24)\n"
    "ds_read_b64_tr_b16 a[224:225], v252 offset:0\n"
    "ds_read_b64_tr_b16 a[226:227], v252 offset:512\n"
    "v_mfma_f32_16x16x32_bf16 v[8:11], a[212:215], a[160:163], v[8:11]\n"
    "s_add_u32 m0, s68, 68608\n"
    "s_addk_i32 s59, 16384\n"
    "v_mfma_f32_16x16x32_bf16 v[40:43], a[212:215], a[164:167], v[40:43]\n"
    "buffer_load_dwordx4 v243, s[24:27], s59 offen  lds\n"
    "v_mfma_f32_16x16x32_bf16 v[40:43], a[208:211], a[132:135], v[40:43]\n"
    "ds_read_b64_tr_b16 a[228:229], v252 offset:4096\n"
    "ds_read_b64_tr_b16 a[230:231], v252 offset:4608\n"
    "v_mfma_f32_16x16x32_bf16 v[72:75], a[208:211], a[136:139], v[72:75]\n"
    "ds_read_b64_tr_b16 a[232:233], v252 offset:8\n"
    "ds_read_b64_tr_b16 a[234:235], v252 offset:520\n"
    "v_mfma_f32_16x16x32_bf16 v[72:75], a[212:215], a[168:171], v[72:75]\n"
    "ds_read_b64_tr_b16 a[236:237], v252 offset:4104\n"
    "ds_read_b64_tr_b16 a[238:239], v252 offset:4616\n"
    "v_mfma_f32_16x16x32_bf16 v[104:107], a[212:215], a[172:175], v[104:107]\n"
    "v_mfma_f32_16x16x32_bf16 v[104:107], a[208:211], a[140:143], v[104:107]\n"
    "ds_read_b64_tr_b16 a[192:193], v253 offset:0\n"
    "ds_read_b64_tr_b16 a[194:195], v253 offset:512\n"
    "v_mfma_f32_16x16x32_bf16 v[136:139], a[208:211], a[144:147], v[136:139]\n"
    "v_mfma_f32_16x16x32_bf16 v[136:139], a[212:215], a[176:179], v[136:139]\n"
    "ds_read_b64_tr_b16 a[196:197], v253 offset:4096\n"
    "ds_read_b64_tr_b16 a[198:199], v253 offset:4608\n"
    "v_mfma_f32_16x16x32_bf16 v[168:171], a[212:215], a[180:183], v[168:171]\n"
    "v_mfma_f32_16x16x32_bf16 v[168:171], a[208:211], a[148:151], v[168:171]\n"
    "ds_read_b64_tr_b16 a[200:201], v253 offset:8\n"
    "ds_read_b64_tr_b16 a[202:203], v253 offset:520\n"
    "v_mfma_f32_16x16x32_bf16 v[200:203], a[208:211], a[152:155], v[200:203]\n"
    "v_mfma_f32_16x16x32_bf16 v[200:203], a[212:215], a[184:187], v[200:203]\n"
    "ds_read_b64_tr_b16 a[204:205], v253 offset:4104\n"
    "ds_read_b64_tr_b16 a[206:207], v253 offset:4616\n"
    "v_mfma_f32_16x16x32_bf16 v[232:235], a[212:215], a[188:191], v[232:235]\n"
    "v_mfma_f32_16x16x32_bf16 v[232:235], a[208:211], a[156:159], v[232:235]\n"
    "v_mfma_f32_16x16x32_bf16 v[236:239], a[216:219], a[156:159], v[236:239]\n"
    "v_mfma_f32_16x16x32_bf16 v[236:239], a[220:223], a[188:191], v[236:239]\n"
    "v_mfma_f32_16x16x32_bf16 v[204:207], a[220:223], a[184:187], v[204:207]\n"
    "v_mfma_f32_16x16x32_bf16 v[204:207], a[216:219], a[152:155], v[204:207]\n"
    "v_mfma_f32_16x16x32_bf16 v[172:175], a[216:219], a[148:151], v[172:175]\n"
    "v_mfma_f32_16x16x32_bf16 v[172:175], a[220:223], a[180:183], v[172:175]\n"
    "v_mfma_f32_16x16x32_bf16 v[140:143], a[220:223], a[176:179], v[140:143]\n"
    "v_mfma_f32_16x16x32_bf16 v[140:143], a[216:219], a[144:147], v[140:143]\n"
    "v_mfma_f32_16x16x32_bf16 v[108:111], a[216:219], a[140:143], v[108:111]\n"
    "v_mfma_f32_16x16x32_bf16 v[108:111], a[220:223], a[172:175], v[108:111]\n"
    "v_mfma_f32_16x16x32_bf16 v[76:79], a[220:223], a[168:171], v[76:79]\n"
    "v_mfma_f32_16x16x32_bf16 v[76:79], a[216:219], a[136:139], v[76:79]\n"
    "v_mfma_f32_16x16x32_bf16 v[44:47], a[216:219], a[132:135], v[44:47]\n"
    "v_mfma_f32_16x16x32_bf16 v[44:47], a[220:223], a[164:167], v[44:47]\n"
    "v_mfma_f32_16x16x32_bf16 v[12:15], a[220:223], a[160:163], v[12:15]\n"
    "v_mfma_f32_16x16x32_bf16 v[12:15], a[216:219], a[128:131], v[12:15]\n"
    "s_waitcnt lgkmcnt(12)\n"
    "v_mfma_f32_16x16x32_bf16 v[16:19], a[224:227], a[128:131], v[16:19]\n"
    "v_mfma_f32_16x16x32_bf16 v[16:19], a[228:231], a[160:163], v[16:19]\n"
    "v_mfma_f32_16x16x32_bf16 v[48:51], a[228:231], a[164:167], v[48:51]\n"
    "v_mfma_f32_16x16x32_bf16 v[48:51], a[224:227], a[132:135], v[48:51]\n"
    "v_mfma_f32_16x16x32_bf16 v[80:83], a[224:227], a[136:139], v[80:83]\n"
    "v_mfma_f32_16x16x32_bf16 v[80:83], a[228:231], a[168:171], v[80:83]\n"
    "v_mfma_f32_16x16x32_bf16 v[112:115], a[228:231], a[172:175], v[112:115]\n"
    "v_mfma_f32_16x16x32_bf16 v[112:115], a[224:227], a[140:143], v[112:115]\n"
    "v_mfma_f32_16x16x32_bf16 v[144:147], a[224:227], a[144:147], v[144:147]\n"
    "v_mfma_f32_16x16x32_bf16 v[144:147], a[228:231], a[176:179], v[144:147]\n"
    "v_mfma_f32_16x16x32_bf16 v[176:179], a[228:231], a[180:183], v[176:179]\n"
    "v_mfma_f32_16x16x32_bf16 v[176:179], a[224:227], a[148:151], v[176:179]\n"
    "v_mfma_f32_16x16x32_bf16 v[208:211], a[224:227], a[152:155], v[208:211]\n"
    "v_mfma_f32_16x16x32_bf16 v[208:211], a[228:231], a[184:187], v[208:211]\n"
    "v_mfma_f32_16x16x32_bf16 a[240:243], a[228:231], a[188:191], a[240:243]\n"
    "v_mfma_f32_16x16x32_bf16 a[240:243], a[224:227], a[156:159], a[240:243]\n"
    "s_waitcnt lgkmcnt(8)\n"
    "v_mfma_f32_16x16x32_bf16 a[244:247], a[232:235], a[156:159], a[244:247]\n"
    "v_mfma_f32_16x16x32_bf16 a[244:247], a[236:239], a[188:191], a[244:247]\n"
    "v_mfma_f32_16x16x32_bf16 v[212:215], a[236:239], a[184:187], v[212:215]\n"
    "v_mfma_f32_16x16x32_bf16 v[212:215], a[232:235], a[152:155], v[212:215]\n"
    "v_mfma_f32_16x16x32_bf16 v[180:183], a[232:235], a[148:151], v[180:183]\n"
    "v_mfma_f32_16x16x32_bf16 v[180:183], a[236:239], a[180:183], v[180:183]\n"
    "v_mfma_f32_16x16x32_bf16 v[148:151], a[236:239], a[176:179], v[148:151]\n"
    "v_mfma_f32_16x16x32_bf16 v[148:151], a[232:235], a[144:147], v[148:151]\n"
    "v_mfma_f32_16x16x32_bf16 v[116:119], a[232:235], a[140:143], v[116:119]\n"
    "v_mfma_f32_16x16x32_bf16 v[116:119], a[236:239], a[172:175], v[116:119]\n"
    "v_mfma_f32_16x16x32_bf16 v[84:87], a[236:239], a[168:171], v[84:87]\n"
    "v_mfma_f32_16x16x32_bf16 v[84:87], a[232:235], a[136:139], v[84:87]\n"
    "v_mfma_f32_16x16x32_bf16 v[52:55], a[232:235], a[132:135], v[52:55]\n"
    "v_mfma_f32_16x16x32_bf16 v[52:55], a[236:239], a[164:167], v[52:55]\n"
    "v_mfma_f32_16x16x32_bf16 v[20:23], a[236:239], a[160:163], v[20:23]\n"
    "v_mfma_f32_16x16x32_bf16 v[20:23], a[232:235], a[128:131], v[20:23]\n"
    "s_waitcnt lgkmcnt(4)\n"
    "s_waitcnt vmcnt(15)\n"
    "ds_read_b64_tr_b16 a[208:209], v252 offset:32768\n"
    "v_mfma_f32_16x16x32_bf16 v[24:27], a[192:195], a[128:131], v[24:27]\n"
    "ds_read_b64_tr_b16 a[210:211], v252 offset:33280\n"
    "v_mfma_f32_16x16x32_bf16 v[24:27], a[196:199], a[160:163], v[24:27]\n"
    "ds_read_b64_tr_b16 a[212:213], v252 offset:36864\n"
    "ds_read_b64_tr_b16 a[214:215], v252 offset:37376\n"
    "v_mfma_f32_16x16x32_bf16 v[56:59], a[196:199], a[164:167], v[56:59]\n"
    "v_mfma_f32_16x16x32_bf16 v[56:59], a[192:195], a[132:135], v[56:59]\n"
    "ds_read_b64_tr_b16 a[216:217], v252 offset:32776\n"
    "ds_read_b64_tr_b16 a[218:219], v252 offset:33288\n"
    "v_mfma_f32_16x16x32_bf16 v[88:91], a[192:195], a[136:139], v[88:91]\n"
    "v_mfma_f32_16x16x32_bf16 v[88:91], a[196:199], a[168:171], v[88:91]\n"
    "ds_read_b64_tr_b16 a[220:221], v252 offset:36872\n"
    "ds_read_b64_tr_b16 a[222:223], v252 offset:37384\n"
    "v_mfma_f32_16x16x32_bf16 v[120:123], a[196:199], a[172:175], v[120:123]\n"
    "v_mfma_f32_16x16x32_bf16 v[120:123], a[192:195], a[140:143], v[120:123]\n"
    "ds_read_b64_tr_b16 a[224:225], v253 offset:32768\n"
    "ds_read_b64_tr_b16 a[226:227], v253 offset:33280\n"
    "v_mfma_f32_16x16x32_bf16 v[152:155], a[192:195], a[144:147], v[152:155]\n"
    "v_mfma_f32_16x16x32_bf16 v[152:155], a[196:199], a[176:179], v[152:155]\n"
    "ds_read_b64_tr_b16 a[228:229], v253 offset:36864\n"
    "ds_read_b64_tr_b16 a[230:231], v253 offset:37376\n"
    "v_mfma_f32_16x16x32_bf16 v[184:187], a[196:199], a[180:183], v[184:187]\n"
    "v_mfma_f32_16x16x32_bf16 v[184:187], a[192:195], a[148:151], v[184:187]\n"
    "ds_read_b64_tr_b16 a[232:233], v253 offset:32776\n"
    "ds_read_b64_tr_b16 a[234:235], v253 offset:33288\n"
    "v_mfma_f32_16x16x32_bf16 v[216:219], a[192:195], a[152:155], v[216:219]\n"
    "v_mfma_f32_16x16x32_bf16 v[216:219], a[196:199], a[184:187], v[216:219]\n"
    "s_waitcnt vmcnt(13)\n"
    "v_readfirstlane_b32 s62, v244\n"
    "s_cmp_eq_u32 s86, 0\n"
    "s_cbranch_scc1 LA3_%=\n"
    "s_cmp_eq_u32 %0, 0\n"
    "s_cbranch_scc1 LA3_%=\n"
    "s_cmp_eq_u32 s62, s88\n"
    "s_cbranch_scc1 LA3_%=\n"
    "s_mov_b32 s69, 0x400000\n"
    "LA4_%=:\n"
    "s_sleep 2\n"
    "s_add_u32 s82, s77, 2052\n"
    "s_and_b32 s82, s82, s86\n"
    "buffer_load_dword v244, v240, s[80:83], s77 offen offset:2048 sc1\n"
    "s_waitcnt vmcnt(0)\n"
    "s_add_u32 s82, s76, 8\n"
    "s_and_b32 s82, s82, s86\n"
    "buffer_store_dwordx2 v[244:245], v240, s[80:83], s76 offen sc1\n"
    "s_waitcnt vmcnt(0)\n"
    "s_add_u32 s82, s77, 3076\n"
    "s_and_b32 s82, s82, s86\n"
    "buffer_load_dword v244, v240, s[80:83], s77 offen offset:3072 sc1\n"
    "s_waitcnt vmcnt(0)\n"
    "v_readfirstlane_b32 s62, v244\n"
    "s_sub_u32 s69, s69, 1\n"
    "s_cbranch_scc1 LA3_%=\n"
    "s_cmp_eq_u32 s62, s88\n"
    "s_cbranch_scc0 LA4_%=\n"
    "LA3_%=:\n"
    "s_waitcnt vmcnt(9)\n"
    "s_barrier\n"
    "v_mfma_f32_16x16x32_bf16 a[248:251], a[196:199], a[188:191], a[248:251]\n"
    "ds_read_b64_tr_b16 a[236:237], v253 offset:36872\n"
    "ds_read_b64_tr_b16 a[238:239], v253 offset:37384\n"
    "v_mfma_f32_16x16x32_bf16 a[248:251], a[192:195], a[156:159], a[248:251]\n"
    "s_waitcnt lgkmcnt(15)\n"
    "v_mfma_f32_16x16x32_bf16 a[252:255], a[200:203], a[156:159], a[252:255]\n"
    "v_mfma_f32_16x16x32_bf16 a[252:255], a[204:207], a[188:191], a[252:255]\n"
    "v_mfma_f32_16x16x32_bf16 v[220:223], a[204:207], a[184:187], v[220:223]\n"
    "ds_read_b128 a[156:159], v241 offset:47104\n"
    "v_mfma_f32_16x16x32_bf16 v[220:223], a[200:203], a[152:155], v[220:223]\n"
    "s_xor_b32 s59, s66, 1664\n"
    "s_add_u32 m0, s68, 81920\n"
    "s_nop 0\n"
    "v_mfma_f32_16x16x32_bf16 v[188:191], a[200:203], a[148:151], v[188:191]\n"
    "buffer_load_dwordx4 v255, s[24:27], s59 offen  lds\n"
    "v_mfma_f32_16x16x32_bf16 v[188:191], a[204:207], a[180:183], v[188:191]\n"
    "ds_read_b128 a[188:191], v242 offset:47104\n"
    "ds_read_b128 a[184:187], v242 offset:45056\n"
    "v_mfma_f32_16x16x32_bf16 v[156:159], a[204:207], a[176:179], v[156:159]\n"
    "ds_read_b128 a[152:155], v241 offset:45056\n"
    "ds_read_b128 a[148:151], v241 offset:43008\n"
    "v_mfma_f32_16x16x32_bf16 v[156:159], a[200:203], a[144:147], v[156:159]\n"
    "s_add_u32 m0, s68, 82944\n"
    "s_addk_i32 s59, 16384\n"
    "v_mfma_f32_16x16x32_bf16 v[124:127], a[200:203], a[140:143], v[124:127]\n"
    "buffer_load_dwordx4 v243, s[24:27], s59 offen  lds\n"
    "v_mfma_f32_16x16x32_bf16 v[124:127], a[204:207], a[172:175], v[124:127]\n"
    "ds_read_b128 a[180:183], v242 offset:43008\n"
    "ds_read_b128 a[176:179], v242 offset:40960\n"
    "v_mfma_f32_16x16x32_bf16 v[92:95], a[204:207], a[168:171], v[92:95]\n"
    "ds_read_b128 a[144:147], v241 offset:40960\n"
    "ds_read_b128 a[140:143], v241 offset:38912\n"
    "v_mfma_f32_16x16x32_bf16 v[92:95], a[200:203], a[136:139], v[92:95]\n"
    "s_add_u32 m0, s68, 83968\n"
    "s_addk_i32 s59, 16384\n"
    "v_mfma_f32_16x16x32_bf16 v[60:63], a[200:203], a[132:135], v[60:63]\n"
    "buffer_load_dwordx4 v255, s[24:27], s59 offen  lds\n"
    "v_mfma_f32_16x16x32_bf16 v[60:63], a[204:207], a[164:167], v[60:63]\n"
    "ds_read_b128 a[172:175], v242 offset:38912\n"
    "ds_read_b128 a[168:171], v242 offset:36864\n"
    "v_mfma_f32_16x16x32_bf16 v[28:31], a[204:207], a[160:163], v[28:31]\n"
    "ds_read_b128 a[136:139], v241 offset:36864\n"
    "ds_read_b128 a[132:135], v241 offset:34816\n"
    "v_mfma_f32_16x16x32_bf16 v[28:31], a[200:203], a[128:131], v[28:31]\n"
    "s_waitcnt lgkmcnt(11)\n"
    "s_add_u32 m0, s68, 84992\n"
    "s_addk_i32 s59, 16384\n"
    "v_mfma_f32_16x16x32_bf16 v[224:227], a[208:211], a[156:159], v[224:227]\n"
    "buffer_load_dwordx4 v243, s[24:27], s59 offen  lds\n"
    "v_mfma_f32_16x16x32_bf16 v[224:227], a[212:215], a[188:191], v[224:227]\n"
    "s_waitcnt lgkmcnt(9)\n"
    "ds_read_b128 a[164:167], v242 offset:34816\n"
    "ds_read_b128 a[160:163], v242 offset:32768\n"
    "v_mfma_f32_16x16x32_bf16 v[192:195], a[212:215], a[184:187], v[192:195]\n"
    "ds_read_b128 a[128:131], v241 offset:32768\n"
    "s_lshr_b32 s81, %3, 2\n"
    "v_mfma_f32_16x16x32_bf16 v[192:195], a[208:211], a[152:155], v[192:195]\n"
    "s_waitcnt lgkmcnt(10)\n"
    "s_lshl_b32 s81, s81, 21\n"
    "s_and_b32 s61, %3, 3\n"
    "s_lshl_b32 s61, s61, 15\n"
    "s_add_u32 s81, s81, s61\n"
    "s_mul_i32 s61, s65, 0x2400\n"
    "s_and_b32 s61, s61, 0x6400\n"
    "s_add_u32 s81, s81, s61\n"
    "s_mov_b32 s82, 0\n"
    "s_mov_b32 s83, 0\n"
    "v_mfma_f32_16x16x32_bf16 v[160:163], a[208:211], a[148:151], v[160:163]\n"
    "s_xor_b32 s59, s66, 1792\n"
    "s_add_u32 m0, s68, 114688\n"
    "s_nop 0\n"
    "v_mfma_f32_16x16x32_bf16 v[160:163], a[212:215], a[180:183], v[160:163]\n"
    "s_waitcnt lgkmcnt(8)\n"
    "v_mfma_f32_16x16x32_bf16 v[128:131], a[212:215], a[176:179], v[128:131]\n"
    "buffer_load_dwordx4 v255, s[24:27], s59 offen  lds\n"
    "v_mfma_f32_16x16x32_bf16 v[128:131], a[208:211], a[144:147], v[128:131]\n"
    "s_waitcnt lgkmcnt(6)\n"
    "s_add_u32 m0, s68, 115712\n"
    "s_addk_i32 s59, 16384\n"
    "v_mfma_f32_16x16x32_bf16 v[96:99], a[208:211], a[140:143], v[96:99]\n"
    "buffer_load_dwordx4 v243, s[24:27], s59 offen  lds\n"
    "v_mfma_f32_16x16x32_bf16 v[96:99], a[212:215], a[172:175], v[96:99]\n"
    "s_waitcnt lgkmcnt(4)\n"
    "v_mfma_f32_16x16x32_bf16 v[64:67], a[212:215], a[168:171], v[64:67]\n"
    "v_mfma_f32_16x16x32_bf16 v[64:67], a[208:211], a[136:139], v[64:67]\n"
    "s_waitcnt lgkmcnt(2)\n"
    "s_add_u32 m0, s68, 116736\n"
    "s_addk_i32 s59, 16384\n"
    "v_mfma_f32_16x16x32_bf16 v[32:35], a[208:211], a[132:135], v[32:35]\n"
    "buffer_load_dwordx4 v255, s[24:27], s59 offen  lds\n"
    "v_mfma_f32_16x16x32_bf16 v[32:35], a[212:215], a[164:167], v[32:35]\n"
    "s_waitcnt lgkmcnt(0)\n"
    "v_mfma_f32_16x16x32_bf16 v[0:3], a[212:215], a[160:163], v[0:3]\n"
    "v_mfma_f32_16x16x32_bf16 v[0:3], a[208:211], a[128:131], v[0:3]\n"
    "s_add_u32 m0, s68, 117760\n"
    "s_addk_i32 s59, 16384\n"
    "v_mfma_f32_16x16x32_bf16 v[4:7], a[216:219], a[128:131], v[4:7]\n"
    "buffer_load_dwordx4 v243, s[24:27], s59 offen  lds\n"
    "v_mfma_f32_16x16x32_bf16 v[4:7], a[220:223], a[160:163], v[4:7]\n"
    "v_mfma_f32_16x16x32_bf16 v[36:39], a[220:223], a[164:167], v[36:39]\n"
    "v_mfma_f32_16x16x32_bf16 v[36:39], a[216:219], a[132:135], v[36:39]\n"
    "v_mfma_f32_16x16x32_bf16 v[68:71], a[216:219], a[136:139], v[68:71]\n"
    "s_load_dword s80, s[32:33], s81\n"
    "s_xor_b32 s81, s81, 8192\n"
    "s_load_dword s80, s[32:33], s81\n"
    "v_mfma_f32_16x16x32_bf16 v[68:71], a[220:223], a[168:171], v[68:71]\n"
    "s_xor_b32 s81, s81, 8192\n"
    "v_mfma_f32_16x16x32_bf16 v[100:103], a[220:223], a[172:175], v[100:103]\n"
    "v_mfma_f32_16x16x32_bf16 v[100:103], a[216:219], a[140:143], v[100:103]\n"
    "v_mfma_f32_16x16x32_bf16 v[132:135], a[216:219], a[144:147], v[132:135]\n"
    "s_bitcmp1_b32 s85, 11\n"
    "s_cbranch_scc1 CP11_%=\n"
    "CR11_%=:\n"
    "v_mfma_f32_16x16x32_bf16 v[132:135], a[220:223], a[176:179], v[132:135]\n"
    "v_mfma_f32_16x16x32_bf16 v[164:167], a[220:223], a[180:183], v[164:167]\n"
    "v_mfma_f32_16x16x32_bf16 v[164:167], a[216:219], a[148:151], v[164:167]\n"
    "v_mfma_f32_16x16x32_bf16 v[196:199], a[216:219], a[152:155], v[196:199]\n"
    "v_mfma_f32_16x16x32_bf16 v[196:199], a[220:223], a[184:187], v[196:199]\n"
    "v_mfma_f32_16x16x32_bf16 v[228:231], a[220:223], a[188:191], v[228:231]\n"
    "v_mfma_f32_16x16x32_bf16 v[228:231], a[216:219], a[156:159], v[228:231]\n"
    "v_add_u32 v252, 0x20000, v252\n"
    "v_add_u32 v253, 0x20000, v253\n"
    "v_mfma_f32_16x16x32_bf16 v[232:235], a[224:227], a[156:159], v[232:235]\n"
    "s_waitcnt vmcnt(12)\n"
    "ds_read_b64_tr_b16 a[192:193], v252 offset:0\n"
    "ds_read_b64_tr_b16 a[194:195], v252 offset:512\n"
    "v_mfma_f32_16x16x32_bf16 v[232:235], a[228:231], a[188:191], v[232:235]\n"
    "ds_read_b64_tr_b16 a[196:197], v252 offset:4096\n"
    "ds_read_b64_tr_b16 a[198:199], v252 offset:4608\n"
    "v_mfma_f32_16x16x32_bf16 v[200:203], a[228:231], a[184:187], v[200:203]\n"
    "v_mfma_f32_16x16x32_bf16 v[200:203], a[224:227], a[152:155], v[200:203]\n"
    "ds_read_b64_tr_b16 a[200:201], v252 offset:8\n"
    "ds_read_b64_tr_b16 a[202:203], v252 offset:520\n"
    "v_mfma_f32_16x16x32_bf16 v[168:171], a[224:227], a[148:151], v[168:171]\n"
    "v_mfma_f32_16x16x32_bf16 v[168:171], a[228:231], a[180:183], v[168:171]\n"
    "ds_read_b64_tr_b16 a[204:205], v252 offset:4104\n"
    "ds_read_b64_tr_b16 a[206:207], v252 offset:4616\n"
    "v_mfma_f32_16x16x32_bf16 v[136:139], a[228:231], a[176:179], v[136:139]\n"
    "v_mfma_f32_16x16x32_bf16 v[136:139], a[224:227], a[144:147], v[136:139]\n"
    "ds_read_b64_tr_b16 a[208:209], v253 offset:0\n"
    "ds_read_b64_tr_b16 a[210:211], v253 offset:512\n"
    "v_mfma_f32_16x16x32_bf16 v[104:107], a[224:227], a[140:143], v[104:107]\n"
    "v_mfma_f32_16x16x32_bf16 v[104:107], a[228:231], a[172:175], v[104:107]\n"
    "ds_read_b64_tr_b16 a[212:213], v253 offset:4096\n"
    "ds_read_b64_tr_b16 a[214:215], v253 offset:4608\n"
    "v_mfma_f32_16x16x32_bf16 v[72:75], a[228:231], a[168:171], v[72:75]\n"
    "v_mfma_f32_16x16x32_bf16 v[72:75], a[224:227], a[136:139], v[72:75]\n"
    "ds_read_b64_tr_b16 a[216:217], v253 offset:8\n"
    "ds_read_b64_tr_b16 a[218:219], v253 offset:520\n"
    "v_mfma_f32_16x16x32_bf16 v[40:43], a[224:227], a[132:135], v[40:43]\n"
    "v_mfma_f32_16x16x32_bf16 v[40:43], a[228:231], a[164:167], v[40:43]\n"
    "ds_read_b64_tr_b16 a[220:221], v253 offset:4104\n"
    "ds_read_b64_tr_b16 a[222:223], v253 offset:4616\n"
    "v_mfma_f32_16x16x32_bf16 v[8:11], a[228:231], a[160:163], v[8:11]\n"
    "v_mfma_f32_16x16x32_bf16 v[8:11], a[224:227], a[128:131], v[8:11]\n"
    "buffer_load_dwordx4 a[224:227], v240, s[20:23], s53 offen offset:0 sc1\n"
    "buffer_load_dwordx4 a[228:231], v240, s[20:23], s53 offen offset:1024 sc1\n"
    "s_lshr_b32 s60, %3, 3\n"
    "s_lshl_b32 s60, s60, 2\n"
    "s_add_u32 s60, s60, s65\n"
    "s_lshl_b32 s60, s60, 10\n"
    "s_min_u32 s60, s60, LAST_%= - LOOP_%= - 1024\n"
    "s_getpc_b64 s[62:63]\n"
    "LA5_%=:\n"
    "v_mfma_f32_16x16x32_bf16 v[12:15], a[232:235], a[128:131], v[12:15]\n"
    "s_add_u32 s62, s62, LOOP_%= - LA5_%=\n"
    "s_addc_u32 s63, s63, 0\n"
    "s_add_u32 s62, s62, s60\n"
    "v_mfma_f32_16x16x32_bf16 v[12:15], a[236:239], a[160:163], v[12:15]\n"
    "s_addc_u32 s63, s63, 0\n"
    "v_mfma_f32_16x16x32_bf16 v[44:47], a[236:239], a[164:167], v[44:47]\n"
    "global_load_dwordx4 v[248:251], v240, s[62:63]\n"
    "v_mfma_f32_16x16x32_bf16 v[44:47], a[232:235], a[132:135], v[44:47]\n"
    "v_mfma_f32_16x16x32_bf16 v[76:79], a[232:235], a[136:139], v[76:79]\n"
    "v_mfma_f32_16x16x32_bf16 v[76:79], a[236:239], a[168:171], v[76:79]\n"
    "v_mfma_f32_16x16x32_bf16 v[108:111], a[236:239], a[172:175], v[108:111]\n"
    "v_mfma_f32_16x16x32_bf16 v[108:111], a[232:235], a[140:143], v[108:111]\n"
    "v_mfma_f32_16x16x32_bf16 v[140:143], a[232:235], a[144:147], v[140:143]\n"
    "v_mfma_f32_16x16x32_bf16 v[140:143], a[236:239], a[176:179], v[140:143]\n"
    "v_mfma_f32_16x16x32_bf16 v[172:175], a[236:239], a[180:183], v[172:175]\n"
    "v_mfma_f32_16x16x32_bf16 v[172:175], a[232:235], a[148:151], v[172:175]\n"
    "v_mfma_f32_16x16x32_bf16 v[204:207], a[232:235], a[152:155], v[204:207]\n"
    "v_mfma_f32_16x16x32_bf16 v[204:207], a[236:239], a[184:187], v[204:207]\n"
    "v_mfma_f32_16x16x32_bf16 v[236:239], a[236:239], a[188:191], v[236:239]\n"
    "v_mfma_f32_16x16x32_bf16 v[236:239], a[232:235], a[156:159], v[236:239]\n"
    "s_waitcnt lgkmcnt(0)\n"
    "s_xor_b32 s81, s81, 8192\n"
    "s_load_dword s80, s[32:33], s81 offset:128\n"
    "s_load_dword s80, s[32:33], s81 offset:256\n"
    "v_mfma_f32_16x16x32_bf16 a[240:243], a[192:195], a[156:159], a[240:243]\n"
    "buffer_load_dwordx4 a[232:235], v240, s[20:23], s53 offen offset:2048 sc1\n"
    "v_mfma_f32_16x16x32_bf16 a[240:243], a[196:199], a[188:191], a[240:243]\n"
    "buffer_load_dwordx4 a[236:239], v240, s[20:23], s53 offen offset:3072 sc1\n"
    "v_mfma_f32_16x16x32_bf16 v[208:211], a[196:199], a[184:187], v[208:211]\n"
    "s_addk_i32 s53, 4096\n"
    "v_mfma_f32_16x16x32_bf16 v[208:211], a[192:195], a[152:155], v[208:211]\n"
    "v_mfma_f32_16x16x32_bf16 v[176:179], a[192:195], a[148:151], v[176:179]\n"
    "v_mfma_f32_16x16x32_bf16 v[176:179], a[196:199], a[180:183], v[176:179]\n"
    "v_mfma_f32_16x16x32_bf16 v[144:147], a[196:199], a[176:179], v[144:147]\n"
    "v_mfma_f32_16x16x32_bf16 v[144:147], a[192:195], a[144:147], v[144:147]\n"
    "v_mfma_f32_16x16x32_bf16 v[112:115], a[192:195], a[140:143], v[112:115]\n"
    "v_mfma_f32_16x16x32_bf16 v[112:115], a[196:199], a[172:175], v[112:115]\n"
    "v_mfma_f32_16x16x32_bf16 v[80:83], a[196:199], a[168:171], v[80:83]\n"
    "v_mfma_f32_16x16x32_bf16 v[80:83], a[192:195], a[136:139], v[80:83]\n"
    "v_mfma_f32_16x16x32_bf16 v[48:51], a[192:195], a[132:135], v[48:51]\n"
    "v_mfma_f32_16x16x32_bf16 v[48:51], a[196:199], a[164:167], v[48:51]\n"
    "v_mfma_f32_16x16x32_bf16 v[16:19], a[196:199], a[160:163], v[16:19]\n"
    "v_mfma_f32_16x16x32_bf16 v[16:19], a[192:195], a[128:131], v[16:19]\n"
    "s_getreg_b32 s73, hwreg(HW_REG_IB_STS, 8, 4)\n"
    "s_add_u32 s82, s82, s73\n"
    "v_mfma_f32_16x16x32_bf16 v[20:23], a[200:203], a[128:131], v[20:23]\n"
    "buffer_load_dwordx4 a[192:195], v240, s[20:23], s53 offen offset:0 sc1\n"
    "v_mfma_f32_16x16x32_bf16 v[20:23], a[204:207], a[160:163], v[20:23]\n"
    "buffer_load_dwordx4 a[196:199], v240, s[20:23], s53 offen offset:1024 sc1\n"
    "v_mfma_f32_16x16x32_bf16 v[52:55], a[204:207], a[164:167], v[52:55]\n"
    "s_getreg_b32 s73, hwreg(HW_REG_IB_STS, 8, 4)\n"
    "s_add_u32 s82, s82, s73\n"
    "v_mfma_f32_16x16x32_bf16 v[52:55], a[200:203], a[132:135], v[52:55]\n"
    "s_getreg_b32 s73, hwreg(HW_REG_IB_STS, 8, 4)\n"
    "s_add_u32 s82, s82, s73\n"
    "v_mfma_f32_16x16x32_bf16 v[84:87], a[200:203], a[136:139], v[84:87]\n"
    "v_mfma_f32_16x16x32_bf16 v[84:87], a[204:207], a[168:171], v[84:87]\n"
    "s_getreg_b32 s73, hwreg(HW_REG_IB_STS, 8, 4)\n"
    "s_add_u32 s82, s82, s73\n"
    "v_mfma_f32_16x16x32_bf16 v[116:119], a[204:207], a[172:175], v[116:119]\n"
    "v_mfma_f32_16x16x32_bf16 v[116:119], a[200:203], a[140:143], v[116:119]\n"
    "s_getreg_b32 s73, hwreg(HW_REG_IB_STS, 8, 4)\n"
    "s_add_u32 s82, s82, s73\n"
    "v_mfma_f32_16x16x32_bf16 v[148:151], a[200:203], a[144:147], v[148:151]\n"
    "v_mfma_f32_16x16x32_bf16 v[148:151], a[204:207], a[176:179], v[148:151]\n"
    "s_getreg_b32 s73, hwreg(HW_REG_IB_STS, 8, 4)\n"
    "s_add_u32 s82, s82, s73\n"
    "v_mfma_f32_16x16x32_bf16 v[180:183], a[204:207], a[180:183], v[180:183]\n"
    "v_mfma_f32_16x16x32_bf16 v[180:183], a[200:203], a[148:151], v[180:183]\n"
    "s_getreg_b32 s73, hwreg(HW_REG_IB_STS, 8, 4)\n"
    "s_add_u32 s82, s82, s73\n"
    "v_mfma_f32_16x16x32_bf16 v[212:215], a[200:203], a[152:155], v[212:215]\n"
    "v_mfma_f32_16x16x32_bf16 v[212:215], a[204:207], a[184:187], v[212:215]\n"
    "s_getreg_b32 s73, hwreg(HW_REG_IB_STS, 8, 4)\n"
    "s_add_u32 s82, s82, s73\n"
    "v_mfma_f32_16x16x32_bf16 a[244:247], a[204:207], a[188:191], a[244:247]\n"
    "v_mfma_f32_16x16x32_bf16 a[244:247], a[200:203], a[156:159], a[244:247]\n"
    "s_getreg_b32 s73, hwreg(HW_REG_IB_STS, 8, 4)\n"
    "s_add_u32 s82, s82, s73\n"
    "v_mfma_f32_16x16x32_bf16 a[248:251], a[208:211], a[156:159], a[248:251]\n"
    "buffer_load_dwordx4 a[200:203], v240, s[20:23], s53 offen offset:2048 sc1\n"
    "v_mfma_f32_16x16x32_bf16 a[248:251], a[212:215], a[188:191], a[248:251]\n"
    "buffer_load_dwordx4 a[204:207], v240, s[20:23], s53 offen offset:3072 sc1\n"
    "v_mfma_f32_16x16x32_bf16 v[216:219], a[212:215], a[184:187], v[216:219]\n"
    "s_addk_i32 s53, 4096\n"
    "s_getreg_b32 s73, hwreg(HW_REG_IB_STS, 8, 4)\n"
    "s_add_u32 s82, s82, s73\n"
    "v_mfma_f32_16x16x32_bf16 v[216:219], a[208:211], a[152:155], v[216:219]\n"
    "s_getreg_b32 s73, hwreg(HW_REG_IB_STS, 8, 4)\n"
    "s_add_u32 s82, s82, s73\n"
    "v_mfma_f32_16x16x32_bf16 v[184:187], a[208:211], a[148:151], v[184:187]\n"
    "v_mfma_f32_16x16x32_bf16 v[184:187], a[212:215], a[180:183], v[184:187]\n"
    "s_getreg_b32 s73, hwreg(HW_REG_IB_STS, 8, 4)\n"
    "s_add_u32 s82, s82, s73\n"
    "v_mfma_f32_16x16x32_bf16 v[152:155], a[212:215], a[176:179], v[152:155]\n"
    "v_mfma_f32_16x16x32_bf16 v[152:155], a[208:211], a[144:147], v[152:155]\n"
    "s_getreg_b32 s73, hwreg(HW_REG_IB_STS, 8, 4)\n"
    "s_add_u32 s82, s82, s73\n"
    "v_mfma_f32_16x16x32_bf16 v[120:123], a[208:211], a[140:143], v[120:123]\n"
    "v_mfma_f32_16x16x32_bf16 v[120:123], a[212:215], a[172:175], v[120:123]\n"
    "s_getreg_b32 s73, hwreg(HW_REG_IB_STS, 8, 4)\n"
    "s_add_u32 s82, s82, s73\n"
    "v_mfma_f32_16x16x32_bf16 v[88:91], a[212:215], a[168:171], v[88:91]\n"
    "v_mfma_f32_16x16x32_bf16 v[88:91], a[208:211], a[136:139], v[88:91]\n"
    "s_getreg_b32 s73, hwreg(HW_REG_IB_STS, 8, 4)\n"
    "s_add_u32 s82, s82, s73\n"
    "v_mfma_f32_16x16x32_bf16 v[56:59], a[208:211], a[132:135], v[56:59]\n"
    "v_mfma_f32_16x16x32_bf16 v[56:59], a[212:215], a[164:167], v[56:59]\n"
    "s_waitcnt vmcnt(13)\n"
    "s_barrier\n"
    "s_getreg_b32 s73, hwreg(HW_REG_IB_STS, 8, 4)\n"
    "v_mfma_f32_16x16x32_bf16 v[24:27], a[212:215], a[160:163], v[24:27]\n"
    "s_add_u32 s82, s82, s73\n"
    "v_mfma_f32_16x16x32_bf16 v[24:27], a[208:211], a[128:131], v[24:27]\n"
    "buffer_load_dwordx4 a[208:211], v240, s[20:23], s53 offen offset:0 sc1\n"
    "v_mfma_f32_16x16x32_bf16 v[28:31], a[216:219], a[128:131], v[28:31]\n"
    "buffer_load_dwordx4 a[212:215], v240, s[20:23], s53 offen offset:1024 sc1\n"
    "v_mfma_f32_16x16x32_bf16 v[28:31], a[220:223], a[160:163], v[28:31]\n"
    "v_mfma_f32_16x16x32_bf16 v[60:63], a[220:223], a[164:167], v[60:63]\n"
    "ds_read_b128 a[128:131], v241 offset:0\n"
    "v_mfma_f32_16x16x32_bf16 v[60:63], a[216:219], a[132:135], v[60:63]\n"
    "s_xor_b32 s59, s66, 1920\n"
    "s_add_u32 m0, s68, 98304\n"
    "s_nop 0\n"
    "v_mfma_f32_16x16x32_bf16 v[92:95], a[216:219], a[136:139], v[92:95]\n"
    "buffer_load_dwordx4 v255, s[24:27], s59 offen  lds\n"
    "v_mfma_f32_16x16x32_bf16 v[92:95], a[220:223], a[168:171], v[92:95]\n"
    "ds_read_b128 a[160:163], v242 offset:0\n"
    "ds_read_b128 a[164:167], v242 offset:2048\n"
    "v_mfma_f32_16x16x32_bf16 v[124:127], a[220:223], a[172:175], v[124:127]\n"
    "ds_read_b128 a[132:135], v241 offset:2048\n"
    "ds_read_b128 a[136:139], v241 offset:4096\n"
    "v_mfma_f32_16x16x32_bf16 v[124:127], a[216:219], a[140:143], v[124:127]\n"
    "s_add_u32 m0, s68, 99328\n"
    "s_addk_i32 s59, 16384\n"
    "v_mfma_f32_16x16x32_bf16 v[156:159], a[216:219], a[144:147], v[156:159]\n"
    "buffer_load_dwordx4 v243, s[24:27], s59 offen  lds\n"
    "v_mfma_f32_16x16x32_bf16 v[156:159], a[220:223], a[176:179], v[156:159]\n"
    "ds_read_b128 a[168:171], v242 offset:4096\n"
    "ds_read_b128 a[172:175], v242 offset:6144\n"
    "v_mfma_f32_16x16x32_bf16 v[188:191], a[220:223], a[180:183], v[188:191]\n"
    "ds_read_b128 a[140:143], v241 offset:6144\n"
    "ds_read_b128 a[144:147], v241 offset:8192\n"
    "v_mfma_f32_16x16x32_bf16 v[188:191], a[216:219], a[148:151], v[188:191]\n"
    "s_add_u32 m0, s68, 100352\n"
    "s_addk_i32 s59, 16384\n"
    "v_mfma_f32_16x16x32_bf16 v[220:223], a[216:219], a[152:155], v[220:223]\n"
    "buffer_load_dwordx4 v255, s[24:27], s59 offen  lds\n"
    "v_mfma_f32_16x16x32_bf16 v[220:223], a[220:223], a[184:187], v[220:223]\n"
    "ds_read_b128 a[176:179], v242 offset:8192\n"
    "ds_read_b128 a[180:183], v242 offset:10240\n"
    "v_mfma_f32_16x16x32_bf16 a[252:255], a[220:223], a[188:191], a[252:255]\n"
    "ds_read_b128 a[148:151], v241 offset:10240\n"
    "ds_read_b128 a[152:155], v241 offset:12288\n"
    "v_mfma_f32_16x16x32_bf16 a[252:255], a[216:219], a[156:159], a[252:255]\n"
    "s_waitcnt vmcnt(12) lgkmcnt(11)\n"
    "s_add_u32 m0, s68, 101376\n"
    "s_addk_i32 s59, 16384\n"
    "v_mfma_f32_16x16x32_bf16 v[0:3], a[224:227], a[128:131], v[0:3]\n"
    "buffer_load_dwordx4 v243, s[24:27], s59 offen  lds\n"
    "v_mfma_f32_16x16x32_bf16 v[0:3], a[228:231], a[160:163], v[0:3]\n"
    "s_waitcnt lgkmcnt(9)\n"
    "ds_read_b128 a[184:187], v242 offset:12288\n"
    "ds_read_b128 a[188:191], v242 offset:14336\n"
    "v_mfma_f32_16x16x32_bf16 v[32:35], a[228:231], a[164:167], v[32:35]\n"
    "ds_read_b128 a[156:159], v241 offset:14336\n"
    "v_mfma_f32_16x16x32_bf16 v[32:35], a[224:227], a[132:135], v[32:35]\n"
    "s_waitcnt lgkmcnt(10)\n"
    "v_mfma_f32_16x16x32_bf16 v[64:67], a[224:227], a[136:139], v[64:67]\n"
    "buffer_load_dwordx4 a[216:219], v240, s[20:23], s53 offen offset:2048 sc1\n"
    "v_mfma_f32_16x16x32_bf16 v[64:67], a[228:231], a[168:171], v[64:67]\n"
    "s_waitcnt lgkmcnt(8)\n"
    "v_mfma_f32_16x16x32_bf16 v[96:99], a[228:231], a[172:175], v[96:99]\n"
    "buffer_load_dwordx4 a[220:223], v240, s[20:23], s53 offen offset:3072 sc1\n"
    "s_addk_i32 s53, 4096\n"
    "v_mfma_f32_16x16x32_bf16 v[96:99], a[224:227], a[140:143], v[96:99]\n"
    "s_waitcnt lgkmcnt(6)\n"
    "v_mfma_f32_16x16x32_bf16 v[128:131], a[224:227], a[144:147], v[128:131]\n"
    "v_mfma_f32_16x16x32_bf16 v[128:131], a[228:231], a[176:179], v[128:131]\n"
    "s_waitcnt lgkmcnt(4)\n"
    "v_mfma_f32_16x16x32_bf16 v[160:163], a[228:231], a[180:183], v[160:163]\n"
    "v_mfma_f32_16x16x32_bf16 v[160:163], a[224:227], a[148:151], v[160:163]\n"
    "s_waitcnt lgkmcnt(2)\n"
    "v_mfma_f32_16x16x32_bf16 v[192:195], a[224:227], a[152:155], v[192:195]\n"
    "v_mfma_f32_16x16x32_bf16 v[192:195], a[228:231], a[184:187], v[192:195]\n"
    "s_bitcmp1_b32 s85, 12\n"
    "s_cbranch_scc1 CP12_%=\n"
    "CR12_%=:\n"
    "s_waitcnt lgkmcnt(0)\n"
    "v_mfma_f32_16x16x32_bf16 v[224:227], a[228:231], a[188:191], v[224:227]\n"
    "v_mfma_f32_16x16x32_bf16 v[224:227], a[224:227], a[156:159], v[224:227]\n"
    "s_waitcnt lgkmcnt(0)\n"
    "s_xor_b32 s81, s81, 8192\n"
    "s_load_dword s80, s[32:33], s81 offset:128\n"
    "s_load_dword s80, s[32:33], s81 offset:256\n"
    "s_waitcnt vmcnt(12)\n"
    "v_mfma_f32_16x16x32_bf16 v[228:231], a[232:235], a[156:159], v[228:231]\n"
    "buffer_load_dwordx4 a[224:227], v240, s[20:23], s53 offen offset:0 sc1\n"
    "v_mfma_f32_16x16x32_bf16 v[228:231], a[236:239], a[188:191], v[228:231]\n"
    "buffer_load_dwordx4 a[228:231], v240, s[20:23], s53 offen offset:1024 sc1\n"
    "v_mfma_f32_16x16x32_bf16 v[196:199], a[236:239], a[184:187], v[196:199]\n"
    "v_mfma_f32_16x16x32_bf16 v[196:199], a[232:235], a[152:155], v[196:199]\n"
    "v_mfma_f32_16x16x32_bf16 v[164:167], a[232:235], a[148:151], v[164:167]\n"
    "v_mfma_f32_16x16x32_bf16 v[164:167], a[236:239], a[180:183], v[164:167]\n"
    "v_mfma_f32_16x16x32_bf16 v[132:135], a[236:239], a[176:179], v[132:135]\n"
    "v_mfma_f32_16x16x32_bf16 v[132:135], a[232:235], a[144:147], v[132:135]\n"
    "v_mfma_f32_16x16x32_bf16 v[100:103], a[232:235], a[140:143], v[100:103]\n"
    "v_mfma_f32_16x16x32_bf16 v[100:103], a[236:239], a[172:175], v[100:103]\n"
    "v_mfma_f32_16x16x32_bf16 v[68:71], a[236:239], a[168:171], v[68:71]\n"
    "v_mfma_f32_16x16x32_bf16 v[68:71], a[232:235], a[136:139], v[68:71]\n"
    "v_mfma_f32_16x16x32_bf16 v[36:39], a[232:235], a[132:135], v[36:39]\n"
    "v_mfma_f32_16x16x32_bf16 v[36:39], a[236:239], a[164:167], v[36:39]\n"
    "v_mfma_f32_16x16x32_bf16 v[4:7], a[236:239], a[160:163], v[4:7]\n"
    "v_mfma_f32_16x16x32_bf16 v[4:7], a[232:235], a[128:131], v[4:7]\n"
    "s_waitcnt vmcnt(12)\n"
    "s_getreg_b32 s73, hwreg(HW_REG_IB_STS, 8, 4)\n"
    "s_add_u32 s83, s83, s73\n"
    "v_mfma_f32_16x16x32_bf16 v[8:11], a[192:195], a[128:131], v[8:11]\n"
    "buffer_load_dwordx4 a[232:235], v240, s[20:23], s53 offen offset:2048 sc1\n"
    "v_mfma_f32_16x16x32_bf16 v[8:11], a[196:199], a[160:163], v[8:11]\n"
    "buffer_load_dwordx4 a[236:239], v240, s[20:23], s53 offen offset:3072 sc1\n"
    "v_mfma_f32_16x16x32_bf16 v[40:43], a[196:199], a[164:167], v[40:43]\n"
    "s_addk_i32 s53, 4096\n"
    "v_mfma_f32_16x16x32_bf16 v[40:43], a[192:195], a[132:135], v[40:43]\n"
    "buffer_load_dword v253, v240, s[36:39], s55 offen offset:0 sc1\n"
    "v_mfma_f32_16x16x32_bf16 v[72:75], a[192:195], a[136:139], v[72:75]\n"
    "buffer_load_dword v252, v240, s[36:39], s55 offen offset:1024 sc1\n"
    "v_mfma_f32_16x16x32_bf16 v[72:75], a[196:199], a[168:171], v[72:75]\n"
    "s_getreg_b32 s73, hwreg(HW_REG_IB_STS, 8, 4)\n"
    "s_add_u32 s83, s83, s73\n"
    "s_getreg_b32 s73, hwreg(HW_REG_IB_STS, 8, 4)\n"
    "v_mfma_f32_16x16x32_bf16 v[104:107], a[196:199], a[172:175], v[104:107]\n"
    "s_add_u32 s83, s83, s73\n"
    "s_getreg_b32 s73, hwreg(HW_REG_IB_STS, 8, 4)\n"
    "s_add_u32 s83, s83, s73\n"
    "v_mfma_f32_16x16x32_bf16 v[104:107], a[192:195], a[140:143], v[104:107]\n"
    "s_getreg_b32 s73, hwreg(HW_REG_IB_STS, 8, 4)\n"
    "s_add_u32 s83, s83, s73\n"
    "v_mfma_f32_16x16x32_bf16 v[136:139], a[192:195], a[144:147], v[136:139]\n"
    "v_mfma_f32_16x16x32_bf16 v[136:139], a[196:199], a[176:179], v[136:139]\n"
    "s_getreg_b32 s73, hwreg(HW_REG_IB_STS, 8, 4)\n"
    "s_add_u32 s83, s83, s73\n"
    "v_mfma_f32_16x16x32_bf16 v[168:171], a[196:199], a[180:183], v[168:171]\n"
    "v_mfma_f32_16x16x32_bf16 v[168:171], a[192:195], a[148:151], v[168:171]\n"
    "s_getreg_b32 s73, hwreg(HW_REG_IB_STS, 8, 4)\n"
    "s_add_u32 s83, s83, s73\n"
    "v_mfma_f32_16x16x32_bf16 v[200:203], a[192:195], a[152:155], v[200:203]\n"
    "v_mfma_f32_16x16x32_bf16 v[200:203], a[196:199], a[184:187], v[200:203]\n"
    "s_getreg_b32 s73, hwreg(HW_REG_IB_STS, 8, 4)\n"
    "s_add_u32 s83, s83, s73\n"
    "v_mfma_f32_16x16x32_bf16 v[232:235], a[196:199], a[188:191], v[232:235]\n"
    "v_mfma_f32_16x16x32_bf16 v[232:235], a[192:195], a[156:159], v[232:235]\n"
    "s_waitcnt vmcnt(14)\n"
    "s_getreg_b32 s73, hwreg(HW_REG_IB_STS, 8, 4)\n"
    "s_add_u32 s83, s83, s73\n"
    "v_mfma_f32_16x16x32_bf16 v[236:239], a[200:203], a[156:159], v[236:239]\n"
    "buffer_load_dwordx4 a[192:195], v240, s[20:23], s53 offen offset:0 sc1\n"
    "v_mfma_f32_16x16x32_bf16 v[236:239], a[204:207], a[188:191], v[236:239]\n"
    "buffer_load_dwordx4 a[196:199], v240, s[20:23], s53 offen offset:1024 sc1\n"
    "v_mfma_f32_16x16x32_bf16 v[204:207], a[204:207], a[184:187], v[204:207]\n"
    "s_getreg_b32 s73, hwreg(HW_REG_IB_STS, 8, 4)\n"
    "s_add_u32 s83, s83, s73\n"
    "ds_read_b128 a[156:159], v241 offset:30720\n"
    "v_mfma_f32_16x16x32_bf16 v[204:207], a[200:203], a[152:155], v[204:207]\n"
    "s_getreg_b32 s73, hwreg(HW_REG_IB_STS, 8, 4)\n"
    "s_add_u32 s83, s83, s73\n"
    "ds_read_b128 a[188:191], v242 offset:30720\n"
    "v_mfma_f32_16x16x32_bf16 v[172:175], a[200:203], a[148:151], v[172:175]\n"
    "ds_read_b128 a[184:187], v242 offset:28672\n"
    "v_mfma_f32_16x16x32_bf16 v[172:175], a[204:207], a[180:183], v[172:175]\n"
    "s_getreg_b32 s73, hwreg(HW_REG_IB_STS, 8, 4)\n"
    "s_add_u32 s83, s83, s73\n"
    "ds_read_b128 a[152:155], v241 offset:28672\n"
    "v_mfma_f32_16x16x32_bf16 v[140:143], a[204:207], a[176:179], v[140:143]\n"
    "ds_read_b128 a[148:151], v241 offset:26624\n"
    "v_mfma_f32_16x16x32_bf16 v[140:143], a[200:203], a[144:147], v[140:143]\n"
    "s_getreg_b32 s73, hwreg(HW_REG_IB_STS, 8, 4)\n"
    "s_add_u32 s83, s83, s73\n"
    "ds_read_b128 a[180:183], v242 offset:26624\n"
    "v_mfma_f32_16x16x32_bf16 v[108:111], a[200:203], a[140:143], v[108:111]\n"
    "ds_read_b128 a[176:179], v242 offset:24576\n"
    "v_mfma_f32_16x16x32_bf16 v[108:111], a[204:207], a[172:175], v[108:111]\n"
    "s_getreg_b32 s73, hwreg(HW_REG_IB_STS, 8, 4)\n"
    "s_add_u32 s83, s83, s73\n"
    "ds_read_b128 a[144:147], v241 offset:24576\n"
    "v_mfma_f32_16x16x32_bf16 v[76:79], a[204:207], a[168:171], v[76:79]\n"
    "ds_read_b128 a[140:143], v241 offset:22528\n"
    "v_mfma_f32_16x16x32_bf16 v[76:79], a[200:203], a[136:139], v[76:79]\n"
    "s_getreg_b32 s73, hwreg(HW_REG_IB_STS, 8, 4)\n"
    "s_add_u32 s83, s83, s73\n"
    "ds_read_b128 a[172:175], v242 offset:22528\n"
    "v_mfma_f32_16x16x32_bf16 v[44:47], a[200:203], a[132:135], v[44:47]\n"
    "ds_read_b128 a[168:171], v242 offset:20480\n"
    "v_mfma_f32_16x16x32_bf16 v[44:47], a[204:207], a[164:167], v[44:47]\n"
    "s_getreg_b32 s73, hwreg(HW_REG_IB_STS, 8, 4)\n"
    "s_add_u32 s83, s83, s73\n"
    "ds_read_b128 a[136:139], v241 offset:20480\n"
    "v_mfma_f32_16x16x32_bf16 v[12:15], a[204:207], a[160:163], v[12:15]\n"
    "ds_read_b128 a[132:135], v241 offset:18432\n"
    "v_mfma_f32_16x16x32_bf16 v[12:15], a[200:203], a[128:131], v[12:15]\n"
    "s_waitcnt vmcnt(14) lgkmcnt(11)\n"
    "ds_read_b128 a[164:167], v242 offset:18432\n"
    "ds_read_b128 a[160:163], v242 offset:16384\n"
    "v_mfma_f32_16x16x32_bf16 v[224:227], a[208:211], a[156:159], v[224:227]\n"
    "ds_read_b128 a[128:131], v241 offset:16384\n"
    "v_mfma_f32_16x16x32_bf16 v[224:227], a[212:215], a[188:191], v[224:227]\n"
    "s_waitcnt lgkmcnt(12)\n"
    "v_mfma_f32_16x16x32_bf16 v[192:195], a[212:215], a[184:187], v[192:195]\n"
    "buffer_load_dwordx4 a[200:203], v240, s[20:23], s53 offen offset:2048 sc1\n"
    "v_mfma_f32_16x16x32_bf16 v[192:195], a[208:211], a[152:155], v[192:195]\n"
    "s_waitcnt lgkmcnt(10)\n"
    "v_mfma_f32_16x16x32_bf16 v[160:163], a[208:211], a[148:151], v[160:163]\n"
    "buffer_load_dwordx4 a[204:207], v240, s[20:23], s53 offen offset:3072 sc1\n"
    "s_addk_i32 s53, 4096\n"
    "v_mfma_f32_16x16x32_bf16 v[160:163], a[212:215], a[180:183], v[160:163]\n"
    "s_waitcnt lgkmcnt(8)\n"
    "s_bitcmp1_b32 s65, 0\n"
    "s_cselect_b32 s60, s82, s83\n"
    "v_mfma_f32_16x16x32_bf16 v[128:131], a[212:215], a[176:179], v[128:131]\n"
    "s_cselect_b32 s61, s83, s82\n"
    "v_mov_b32 v246, s60\n"
    "v_mov_b32 v247, s61\n"
    "v_mfma_f32_16x16x32_bf16 v[128:131], a[208:211], a[144:147], v[128:131]\n"
    "s_waitcnt lgkmcnt(6)\n"
    "s_lshl_b32 s60, s65, 3\n"
    "s_add_u32 s60, s60, %2\n"
    "v_mfma_f32_16x16x32_bf16 v[96:99], a[208:211], a[140:143], v[96:99]\n"
    "s_add_u32 s60, s60, 131072\n"
    "v_mov_b32 v248, s60\n"
    "v_mfma_f32_16x16x32_bf16 v[96:99], a[212:215], a[172:175], v[96:99]\n"
    "s_waitcnt lgkmcnt(4)\n"
    "v_mfma_f32_16x16x32_bf16 v[64:67], a[212:215], a[168:171], v[64:67]\n"
    "ds_write_b64 v248, v[246:247]\n"
    "v_mfma_f32_16x16x32_bf16 v[64:67], a[208:211], a[136:139], v[64:67]\n"
    "s_waitcnt lgkmcnt(3)\n"
    "v_mfma_f32_16x16x32_bf16 v[32:35], a[208:211], a[132:135], v[32:35]\n"
    "v_mfma_f32_16x16x32_bf16 v[32:35], a[212:215], a[164:167], v[32:35]\n"
    "s_bitcmp1_b32 s85, 13\n"
    "s_cbranch_scc1 CP13_%=\n"
    "CR13_%=:\n"
    "s_waitcnt lgkmcnt(1)\n"
    "v_mfma_f32_16x16x32_bf16 v[0:3], a[212:215], a[160:163], v[0:3]\n"
    "v_mfma_f32_16x16x32_bf16 v[0:3], a[208:211], a[128:131], v[0:3]\n"
    "s_waitcnt vmcnt(10)\n"
    "v_mfma_f32_16x16x32_bf16 v[4:7], a[216:219], a[128:131], v[4:7]\n"
    "buffer_load_dwordx4 a[208:211], v240, s[20:23], s53 offen offset:0 sc1\n"
    "v_mfma_f32_16x16x32_bf16 v[4:7], a[220:223], a[160:163], v[4:7]\n"
    "buffer_load_dwordx4 a[212:215], v240, s[20:23], s53 offen offset:1024 sc1\n"
    "v_mfma_f32_16x16x32_bf16 v[36:39], a[220:223], a[164:167], v[36:39]\n"
    "s_lshr_b32 s60, s65, 1\n"
    "s_lshl_b32 s60, s60, 14\n"
    "s_and_b32 s61, s65, 1\n"
    "v_mfma_f32_16x16x32_bf16 v[36:39], a[216:219], a[132:135], v[36:39]\n"
    "s_lshl_b32 s61, s61, 10\n"
    "s_add_u32 s62, s60, s61\n"
    "s_add_u32 s62, s62, %2\n"
    "v_add_u32 v243, s62, v240\n"
    "s_mov_b32 s71, s62\n"
    "s_add_u32 s56, s62, 131072\n"
    "s_lshl_b32 s60, s60, 1\n"
    "s_add_u32 s60, s60, s61\n"
    "s_add_u32 s32, s32, s60\n"
    "s_lshr_b32 s60, %3, 4\n"
    "s_lshl_b32 s60, s60, 22\n"
    "s_and_b32 s61, %3, 7\n"
    "s_lshl_b32 s61, s61, 19\n"
    "s_add_u32 s54, s60, s61\n"
    "s_add_u32 s54, s54, 134217728\n"
    "s_bitcmp1_b32 %3, 3\n"
    "s_cselect_b32 s60, 0x40000, 0\n"
    "s_xor_b32 s54, s54, s60\n"
    "s_xor_b32 s59, s54, 0x40000\n"
    "s_mov_b64 s[24:25], s[12:13]\n"
    "s_add_u32 s24, s24, 266240\n"
    "s_addc_u32 s25, s25, 0\n"
    "s_and_b32 s25, s25, 0xffff\n"
    "s_mov_b32 s26, 1024\n"
    "s_mov_b32 s66, %2\n"
    "s_add_u32 s57, %2, 65536\n"
    "s_add_u32 s67, %2, 131072\n"
    "v_mfma_f32_16x16x32_bf16 v[68:71], a[216:219], a[136:139], v[68:71]\n"
    "v_mfma_f32_16x16x32_bf16 v[68:71], a[220:223], a[168:171], v[68:71]\n"
    "v_mfma_f32_16x16x32_bf16 v[100:103], a[220:223], a[172:175], v[100:103]\n"
    "v_mfma_f32_16x16x32_bf16 v[100:103], a[216:219], a[140:143], v[100:103]\n"
    "v_mfma_f32_16x16x32_bf16 v[132:135], a[216:219], a[144:147], v[132:135]\n"
    "v_mfma_f32_16x16x32_bf16 v[132:135], a[220:223], a[176:179], v[132:135]\n"
    "v_mfma_f32_16x16x32_bf16 v[164:167], a[220:223], a[180:183], v[164:167]\n"
    "v_mfma_f32_16x16x32_bf16 v[164:167], a[216:219], a[148:151], v[164:167]\n"
    "v_mfma_f32_16x16x32_bf16 v[196:199], a[216:219], a[152:155], v[196:199]\n"
    "v_mfma_f32_16x16x32_bf16 v[196:199], a[220:223], a[184:187], v[196:199]\n"
    "v_mfma_f32_16x16x32_bf16 v[228:231], a[220:223], a[188:191], v[228:231]\n"
    "v_mfma_f32_16x16x32_bf16 v[228:231], a[216:219], a[156:159], v[228:231]\n"
    "s_waitcnt vmcnt(10)\n"
    "v_mfma_f32_16x16x32_bf16 v[232:235], a[224:227], a[156:159], v[232:235]\n"
    "buffer_load_dwordx4 a[216:219], v240, s[20:23], s53 offen offset:2048 sc1\n"
    "v_mfma_f32_16x16x32_bf16 v[232:235], a[228:231], a[188:191], v[232:235]\n"
    "buffer_load_dwordx4 a[220:223], v240, s[20:23], s53 offen offset:3072 sc1\n"
    "v_mfma_f32_16x16x32_bf16 v[200:203], a[228:231], a[184:187], v[200:203]\n"
    "s_addk_i32 s53, 4096\n"
    "v_mfma_f32_16x16x32_bf16 v[200:203], a[224:227], a[152:155], v[200:203]\n"
    "v_mfma_f32_16x16x32_bf16 v[168:171], a[224:227], a[148:151], v[168:171]\n"
    "v_mfma_f32_16x16x32_bf16 v[168:171], a[228:231], a[180:183], v[168:171]\n"
    "v_mfma_f32_16x16x32_bf16 v[136:139], a[228:231], a[176:179], v[136:139]\n"
    "v_mfma_f32_16x16x32_bf16 v[136:139], a[224:227], a[144:147], v[136:139]\n"
    "v_mfma_f32_16x16x32_bf16 v[104:107], a[224:227], a[140:143], v[104:107]\n"
    "v_mfma_f32_16x16x32_bf16 v[104:107], a[228:231], a[172:175], v[104:107]\n"
    "v_mfma_f32_16x16x32_bf16 v[72:75], a[228:231], a[168:171], v[72:75]\n"
    "v_mfma_f32_16x16x32_bf16 v[72:75], a[224:227], a[136:139], v[72:75]\n"
    "v_mfma_f32_16x16x32_bf16 v[40:43], a[224:227], a[132:135], v[40:43]\n"
    "v_mfma_f32_16x16x32_bf16 v[40:43], a[228:231], a[164:167], v[40:43]\n"
    "s_waitcnt lgkmcnt(0)\n"
    "s_barrier\n"
    "v_mfma_f32_16x16x32_bf16 v[8:11], a[228:231], a[160:163], v[8:11]\n"
    "v_mfma_f32_16x16x32_bf16 v[8:11], a[224:227], a[128:131], v[8:11]\n"
    "s_waitcnt vmcnt(10)\n"
    "v_mfma_f32_16x16x32_bf16 v[12:15], a[232:235], a[128:131], v[12:15]\n"
    "buffer_load_dwordx4 a[224:227], v240, s[20:23], s53 offen offset:0 sc1\n"
    "v_mfma_f32_16x16x32_bf16 v[12:15], a[236:239], a[160:163], v[12:15]\n"
    "buffer_load_dwordx4 a[228:231], v240, s[20:23], s53 offen offset:1024 sc1\n"
    "v_mfma_f32_16x16x32_bf16 v[44:47], a[236:239], a[164:167], v[44:47]\n"
    "ds_read_b128 a[128:131], v241 offset:49152\n"
    "v_mfma_f32_16x16x32_bf16 v[44:47], a[232:235], a[132:135], v[44:47]\n"
    "ds_read_b128 a[160:163], v242 offset:49152\n"
    "v_mfma_f32_16x16x32_bf16 v[76:79], a[232:235], a[136:139], v[76:79]\n"
    "ds_read_b128 a[164:167], v242 offset:51200\n"
    "v_mfma_f32_16x16x32_bf16 v[76:79], a[236:239], a[168:171], v[76:79]\n"
    "ds_read_b128 a[132:135], v241 offset:51200\n"
    "v_mfma_f32_16x16x32_bf16 v[108:111], a[236:239], a[172:175], v[108:111]\n"
    "ds_read_b128 a[136:139], v241 offset:53248\n"
    "v_mfma_f32_16x16x32_bf16 v[108:111], a[232:235], a[140:143], v[108:111]\n"
    "ds_read_b128 a[168:171], v242 offset:53248\n"
    "v_mfma_f32_16x16x32_bf16 v[140:143], a[232:235], a[144:147], v[140:143]\n"
    "ds_read_b128 a[172:175], v242 offset:55296\n"
    "v_mfma_f32_16x16x32_bf16 v[140:143], a[236:239], a[176:179], v[140:143]\n"
    "ds_read_b128 a[140:143], v241 offset:55296\n"
    "v_mfma_f32_16x16x32_bf16 v[172:175], a[236:239], a[180:183], v[172:175]\n"
    "ds_read_b128 a[144:147], v241 offset:57344\n"
    "v_mfma_f32_16x16x32_bf16 v[172:175], a[232:235], a[148:151], v[172:175]\n"
    "ds_read_b128 a[176:179], v242 offset:57344\n"
    "v_mfma_f32_16x16x32_bf16 v[204:207], a[232:235], a[152:155], v[204:207]\n"
    "ds_read_b128 a[180:183], v242 offset:59392\n"
    "v_mfma_f32_16x16x32_bf16 v[204:207], a[236:239], a[184:187], v[204:207]\n"
    "ds_read_b128 a[148:151], v241 offset:59392\n"
    "v_mfma_f32_16x16x32_bf16 v[236:239], a[236:239], a[188:191], v[236:239]\n"
    "ds_read_b128 a[152:155], v241 offset:61440\n"
    "v_mfma_f32_16x16x32_bf16 v[236:239], a[232:235], a[156:159], v[236:239]\n"
    "s_waitcnt vmcnt(8) lgkmcnt(11)\n"
    "ds_read_b128 a[184:187], v242 offset:61440\n"
    "ds_read_b128 a[188:191], v242 offset:63488\n"
    "v_mfma_f32_16x16x32_bf16 v[0:3], a[192:195], a[128:131], v[0:3]\n"
    "ds_read_b128 a[156:159], v241 offset:63488\n"
    "v_mfma_f32_16x16x32_bf16 v[0:3], a[196:199], a[160:163], v[0:3]\n"
    "s_waitcnt lgkmcnt(12)\n"
    "v_mfma_f32_16x16x32_bf16 v[32:35], a[196:199], a[164:167], v[32:35]\n"
    "buffer_load_dwordx4 a[232:235], v240, s[20:23], s53 offen offset:2048 sc1\n"
    "v_mfma_f32_16x16x32_bf16 v[32:35], a[192:195], a[132:135], v[32:35]\n"
    "s_waitcnt lgkmcnt(10)\n"
    "v_mfma_f32_16x16x32_bf16 v[64:67], a[192:195], a[136:139], v[64:67]\n"
    "buffer_load_dwordx4 a[236:239], v240, s[20:23], s53 offen offset:3072 sc1\n"
    "s_addk_i32 s53, 4096\n"
    "v_mfma_f32_16x16x32_bf16 v[64:67], a[196:199], a[168:171], v[64:67]\n"
    "s_waitcnt lgkmcnt(8)\n"
    "v_mfma_f32_16x16x32_bf16 v[96:99], a[196:199], a[172:175], v[96:99]\n"
    "v_mfma_f32_16x16x32_bf16 v[96:99], a[192:195], a[140:143], v[96:99]\n"
    "s_waitcnt lgkmcnt(6)\n"
    "v_mfma_f32_16x16x32_bf16 v[128:131], a[192:195], a[144:147], v[128:131]\n"
    "v_mfma_f32_16x16x32_bf16 v[128:131], a[196:199], a[176:179], v[128:131]\n"
    "s_waitcnt lgkmcnt(4)\n"
    "v_mfma_f32_16x16x32_bf16 v[160:163], a[196:199], a[180:183], v[160:163]\n"
    "v_mfma_f32_16x16x32_bf16 v[160:163], a[192:195], a[148:151], v[160:163]\n"
    "s_waitcnt lgkmcnt(2)\n"
    "v_mfma_f32_16x16x32_bf16 v[192:195], a[192:195], a[152:155], v[192:195]\n"
    "v_mfma_f32_16x16x32_bf16 v[192:195], a[196:199], a[184:187], v[192:195]\n"
    "s_bitcmp1_b32 s85, 14\n"
    "s_cbranch_scc1 CP14_%=\n"
    "CR14_%=:\n"
    "s_waitcnt lgkmcnt(0)\n"
    "v_mfma_f32_16x16x32_bf16 v[224:227], a[196:199], a[188:191], v[224:227]\n"
    "v_mfma_f32_16x16x32_bf16 v[224:227], a[192:195], a[156:159], v[224:227]\n"
    "s_waitcnt vmcnt(8)\n"
    "v_mfma_f32_16x16x32_bf16 v[228:231], a[200:203], a[156:159], v[228:231]\n"
    "buffer_load_dwordx4 a[192:195], v240, s[20:23], s53 offen offset:0 sc1\n"
    "v_mfma_f32_16x16x32_bf16 v[228:231], a[204:207], a[188:191], v[228:231]\n"
    "buffer_load_dwordx4 a[196:199], v240, s[20:23], s53 offen offset:1024 sc1\n"
    "v_mfma_f32_16x16x32_bf16 v[196:199], a[204:207], a[184:187], v[196:199]\n"
    "s_add_u32 s60, %2, 131072\n"
    "v_mov_b32 v244, s60\n"
    "ds_read_b128 v[248:251], v244\n"
    "ds_read_b128 v[244:247], v244 offset:16\n"
    "v_mfma_f32_16x16x32_bf16 v[196:199], a[200:203], a[152:155], v[196:199]\n"
    "v_mfma_f32_16x16x32_bf16 v[164:167], a[200:203], a[148:151], v[164:167]\n"
    "v_mfma_f32_16x16x32_bf16 v[164:167], a[204:207], a[180:183], v[164:167]\n"
    "v_mfma_f32_16x16x32_bf16 v[132:135], a[204:207], a[176:179], v[132:135]\n"
    "v_mfma_f32_16x16x32_bf16 v[132:135], a[200:203], a[144:147], v[132:135]\n"
    "v_mfma_f32_16x16x32_bf16 v[100:103], a[200:203], a[140:143], v[100:103]\n"
    "v_mfma_f32_16x16x32_bf16 v[100:103], a[204:207], a[172:175], v[100:103]\n"
    "v_mfma_f32_16x16x32_bf16 v[68:71], a[204:207], a[168:171], v[68:71]\n"
    "v_mfma_f32_16x16x32_bf16 v[68:71], a[200:203], a[136:139], v[68:71]\n"
    "v_mfma_f32_16x16x32_bf16 v[36:39], a[200:203], a[132:135], v[36:39]\n"
    "v_mfma_f32_16x16x32_bf16 v[36:39], a[204:207], a[164:167], v[36:39]\n"
    "v_mfma_f32_16x16x32_bf16 v[4:7], a[204:207], a[160:163], v[4:7]\n"
    "v_mfma_f32_16x16x32_bf16 v[4:7], a[200:203], a[128:131], v[4:7]\n"
    "s_waitcnt vmcnt(8)\n"
    "v_mfma_f32_16x16x32_bf16 v[8:11], a[208:211], a[128:131], v[8:11]\n"
    "buffer_load_dwordx4 a[200:203], v240, s[20:23], s53 offen offset:2048 sc1\n"
    "v_mfma_f32_16x16x32_bf16 v[8:11], a[212:215], a[160:163], v[8:11]\n"
    "buffer_load_dwordx4 a[204:207], v240, s[20:23], s53 offen offset:3072 sc1\n"
    "v_mfma_f32_16x16x32_bf16 v[40:43], a[212:215], a[164:167], v[40:43]\n"
    "s_addk_i32 s53, 4096\n"
    "s_waitcnt lgkmcnt(0)\n"
    "v_add_u32 v248, v248, v250\n"
    "v_add_u32 v249, v249, v251\n"
    "v_add_u32 v244, v244, v246\n"
    "v_add_u32 v245, v245, v247\n"
    "v_readfirstlane_b32 s72, v248\n"
    "v_readfirstlane_b32 s73, v249\n"
    "v_readfirstlane_b32 s74, v244\n"
    "v_readfirstlane_b32 s75, v245\n"
    "v_mfma_f32_16x16x32_bf16 v[40:43], a[208:211], a[132:135], v[40:43]\n"
    "s_add_u32 s60, s72, s74\n"
    "s_add_u32 s61, s73, s75\n"
    "s_cmp_lt_u32 s61, s60\n"
    "s_cselect_b32 s64, 1, 0\n"
    "s_cselect_b32 s72, s73, s72\n"
    "s_cselect_b32 s74, s75, s74\n"
    "s_lshl_b32 s62, s64, 13\n"
    "s_xor_b32 s54, s54, s62\n"
    "s_sub_i32 s73, s72, s74\n"
    "s_sub_i32 s75, s74, s72\n"
    "s_bitcmp1_b32 %3, 0\n"
    "s_cselect_b32 s73, s75, s73\n"
    "s_lshl_b32 s75, s73, 1\n"
    "s_addk_i32 s75, 4097\n"
    "s_sub_i32 s72, s60, s61\n"
    "s_min_i32 s72, s72, 255\n"
    "s_max_i32 s72, s72, -255\n"
    "s_addk_i32 s72, 256\n"
    "s_lshl_b32 s72, s72, 20\n"
    "s_or_b32 s75, s75, s72\n"
    "v_mov_b32 v248, s75\n"
    "s_add_u32 s26, s77, 4\n"
    "s_and_b32 s26, s26, s86\n"
    "v_mfma_f32_16x16x32_bf16 v[72:75], a[208:211], a[136:139], v[72:75]\n"
    "buffer_store_dword v248, v240, s[24:27], s77 offen sc1\n"
    "s_movk_i32 s26, 1024\n"
    "v_mfma_f32_16x16x32_bf16 v[72:75], a[212:215], a[168:171], v[72:75]\n"
    "v_mfma_f32_16x16x32_bf16 v[104:107], a[212:215], a[172:175], v[104:107]\n"
    "v_mfma_f32_16x16x32_bf16 v[104:107], a[208:211], a[140:143], v[104:107]\n"
    "v_mfma_f32_16x16x32_bf16 v[136:139], a[208:211], a[144:147], v[136:139]\n"
    "v_mfma_f32_16x16x32_bf16 v[136:139], a[212:215], a[176:179], v[136:139]\n"
    "v_mfma_f32_16x16x32_bf16 v[168:171], a[212:215], a[180:183], v[168:171]\n"
    "v_mfma_f32_16x16x32_bf16 v[168:171], a[208:211], a[148:151], v[168:171]\n"
    "v_mfma_f32_16x16x32_bf16 v[200:203], a[208:211], a[152:155], v[200:203]\n"
    "v_mfma_f32_16x16x32_bf16 v[200:203], a[212:215], a[184:187], v[200:203]\n"
    "s_barrier\n"
    "v_mfma_f32_16x16x32_bf16 v[232:235], a[212:215], a[188:191], v[232:235]\n"
    "v_mfma_f32_16x16x32_bf16 v[232:235], a[208:211], a[156:159], v[232:235]\n"
    "s_waitcnt vmcnt(9)\n"
    "v_mfma_f32_16x16x32_bf16 v[236:239], a[216:219], a[156:159], v[236:239]\n"
    "buffer_load_dwordx4 a[208:211], v240, s[20:23], s53 offen offset:0 sc1\n"
    "v_mfma_f32_16x16x32_bf16 v[236:239], a[220:223], a[188:191], v[236:239]\n"
    "buffer_load_dwordx4 a[212:215], v240, s[20:23], s53 offen offset:1024 sc1\n"
    "v_mfma_f32_16x16x32_bf16 v[204:207], a[220:223], a[184:187], v[204:207]\n"
    "ds_read_b128 a[156:159], v241 offset:47104\n"
    "v_mfma_f32_16x16x32_bf16 v[204:207], a[216:219], a[152:155], v[204:207]\n"
    "ds_read_b128 a[188:191], v242 offset:47104\n"
    "v_mfma_f32_16x16x32_bf16 v[172:175], a[216:219], a[148:151], v[172:175]\n"
    "ds_read_b128 a[184:187], v242 offset:45056\n"
    "v_mfma_f32_16x16x32_bf16 v[172:175], a[220:223], a[180:183], v[172:175]\n"
    "ds_read_b128 a[152:155], v241 offset:45056\n"
    "v_mfma_f32_16x16x32_bf16 v[140:143], a[220:223], a[176:179], v[140:143]\n"
    "ds_read_b128 a[148:151], v241 offset:43008\n"
    "v_mfma_f32_16x16x32_bf16 v[140:143], a[216:219], a[144:147], v[140:143]\n"
    "ds_read_b128 a[180:183], v242 offset:43008\n"
    "v_mfma_f32_16x16x32_bf16 v[108:111], a[216:219], a[140:143], v[108:111]\n"
    "ds_read_b128 a[176:179], v242 offset:40960\n"
    "v_mfma_f32_16x16x32_bf16 v[108:111], a[220:223], a[172:175], v[108:111]\n"
    "ds_read_b128 a[144:147], v241 offset:40960\n"
    "v_mfma_f32_16x16x32_bf16 v[76:79], a[220:223], a[168:171], v[76:79]\n"
    "ds_read_b128 a[140:143], v241 offset:38912\n"
    "v_mfma_f32_16x16x32_bf16 v[76:79], a[216:219], a[136:139], v[76:79]\n"
    "ds_read_b128 a[172:175], v242 offset:38912\n"
    "v_mfma_f32_16x16x32_bf16 v[44:47], a[216:219], a[132:135], v[44:47]\n"
    "ds_read_b128 a[168:171], v242 offset:36864\n"
    "v_mfma_f32_16x16x32_bf16 v[44:47], a[220:223], a[164:167], v[44:47]\n"
    "ds_read_b128 a[136:139], v241 offset:36864\n"
    "v_mfma_f32_16x16x32_bf16 v[12:15], a[220:223], a[160:163], v[12:15]\n"
    "ds_read_b128 a[132:135], v241 offset:34816\n"
    "v_mfma_f32_16x16x32_bf16 v[12:15], a[216:219], a[128:131], v[12:15]\n"
    "s_waitcnt vmcnt(9) lgkmcnt(11)\n"
    "ds_read_b128 a[164:167], v242 offset:34816\n"
    "ds_read_b128 a[160:163], v242 offset:32768\n"
    "v_mfma_f32_16x16x32_bf16 v[224:227], a[224:227], a[156:159], v[224:227]\n"
    "ds_read_b128 a[128:131], v241 offset:32768\n"
    "v_mfma_f32_16x16x32_bf16 v[224:227], a[228:231], a[188:191], v[224:227]\n"
    "s_waitcnt lgkmcnt(12)\n"
    "v_mfma_f32_16x16x32_bf16 v[192:195], a[228:231], a[184:187], v[192:195]\n"
    "buffer_load_dwordx4 a[216:219], v240, s[20:23], s53 offen offset:2048 sc1\n"
    "v_mfma_f32_16x16x32_bf16 v[192:195], a[224:227], a[152:155], v[192:195]\n"
    "s_waitcnt lgkmcnt(10)\n"
    "v_mfma_f32_16x16x32_bf16 v[160:163], a[224:227], a[148:151], v[160:163]\n"
    "buffer_load_dwordx4 a[220:223], v240, s[20:23], s53 offen offset:3072 sc1\n"
    "s_addk_i32 s53, 4096\n"
    "v_mfma_f32_16x16x32_bf16 v[160:163], a[228:231], a[180:183], v[160:163]\n"
    "s_waitcnt lgkmcnt(8)\n"
    "v_mfma_f32_16x16x32_bf16 v[128:131], a[228:231], a[176:179], v[128:131]\n"
    "v_mfma_f32_16x16x32_bf16 v[128:131], a[224:227], a[144:147], v[128:131]\n"
    "s_waitcnt lgkmcnt(6)\n"
    "v_mfma_f32_16x16x32_bf16 v[96:99], a[224:227], a[140:143], v[96:99]\n"
    "v_mfma_f32_16x16x32_bf16 v[96:99], a[228:231], a[172:175], v[96:99]\n"
    "s_waitcnt lgkmcnt(4)\n"
    "v_mfma_f32_16x16x32_bf16 v[64:67], a[228:231], a[168:171], v[64:67]\n"
    "v_mfma_f32_16x16x32_bf16 v[64:67], a[224:227], a[136:139], v[64:67]\n"
    "s_waitcnt lgkmcnt(2)\n"
    "v_mfma_f32_16x16x32_bf16 v[32:35], a[224:227], a[132:135], v[32:35]\n"
    "v_mfma_f32_16x16x32_bf16 v[32:35], a[228:231], a[164:167], v[32:35]\n"
    "s_bitcmp1_b32 s85, 15\n"
    "s_cbranch_scc1 CP15_%=\n"
    "CR15_%=:\n"
    "s_waitcnt lgkmcnt(0)\n"
    "v_mfma_f32_16x16x32_bf16 v[0:3], a[228:231], a[160:163], v[0:3]\n"
    "v_mfma_f32_16x16x32_bf16 v[0:3], a[224:227], a[128:131], v[0:3]\n"
    "s_waitcnt vmcnt(9)\n"
    "v_mfma_f32_16x16x32_bf16 v[4:7], a[232:235], a[128:131], v[4:7]\n"
    "buffer_load_dwordx4 a[224:227], v240, s[20:23], s53 offen offset:0 sc1\n"
    "v_mfma_f32_16x16x32_bf16 v[4:7], a[236:239], a[160:163], v[4:7]\n"
    "buffer_load_dwordx4 a[228:231], v240, s[20:23], s53 offen offset:1024 sc1\n"
    "v_mfma_f32_16x16x32_bf16 v[36:39], a[236:239], a[164:167], v[36:39]\n"
    "v_mfma_f32_16x16x32_bf16 v[36:39], a[232:235], a[132:135], v[36:39]\n"
    "v_mfma_f32_16x16x32_bf16 v[68:71], a[232:235], a[136:139], v[68:71]\n"
    "v_mfma_f32_16x16x32_bf16 v[68:71], a[236:239], a[168:171], v[68:71]\n"
    "v_mfma_f32_16x16x32_bf16 v[100:103], a[236:239], a[172:175], v[100:103]\n"
    "v_mfma_f32_16x16x32_bf16 v[100:103], a[232:235], a[140:143], v[100:103]\n"
    "v_mfma_f32_16x16x32_bf16 v[132:135], a[232:235], a[144:147], v[132:135]\n"
    "v_mfma_f32_16x16x32_bf16 v[132:135], a[236:239], a[176:179], v[132:135]\n"
    "v_mfma_f32_16x16x32_bf16 v[164:167], a[236:239], a[180:183], v[164:167]\n"
    "v_accvgpr_read_b32 v245, a0\n"
    "v_accvgpr_read_b32 v247, a1\n"
    "v_mfma_f32_16x16x32_bf16 v[164:167], a[232:235], a[148:151], v[164:167]\n"
    "v_lshlrev_b32 v244, 16, v245\n"
    "v_lshlrev_b32 v246, 16, v247\n"
    "v_mfma_f32_16x16x32_bf16 v[196:199], a[232:235], a[152:155], v[196:199]\n"
    "v_and_b32 v245, s58, v245\n"
    "v_and_b32 v247, s58, v247\n"
    "v_mfma_f32_16x16x32_bf16 v[196:199], a[236:239], a[184:187], v[196:199]\n"
    "v_add_f32_dpp v254, v253, v0 row_newbcast:0 row_mask:0xf bank_mask:0xf\n"
    "v_add_f32_dpp v255, v253, v2 row_newbcast:2 row_mask:0xf bank_mask:0xf\n"
    "v_mfma_f32_16x16x32_bf16 v[228:231], a[236:239], a[188:191], v[228:231]\n"
    "v_mul_f32 v244, v244, v254\n"
    "v_mul_f32 v246, v246, v255\n"
    "v_mfma_f32_16x16x32_bf16 v[228:231], a[232:235], a[156:159], v[228:231]\n"
    "s_waitcnt vmcnt(9)\n"
    "v_add_f32_dpp v254, v253, v1 row_newbcast:1 row_mask:0xf bank_mask:0xf\n"
    "v_add_f32_dpp v255, v253, v3 row_newbcast:3 row_mask:0xf bank_mask:0xf\n"
    "v_mfma_f32_16x16x32_bf16 v[232:235], a[192:195], a[156:159], v[232:235]\n"
    "buffer_load_dwordx4 a[232:235], v240, s[20:23], s53 offen offset:2048 sc1\n"
    "v_mfma_f32_16x16x32_bf16 v[232:235], a[196:199], a[188:191], v[232:235]\n"
    "buffer_load_dwordx4 a[236:239], v240, s[20:23], s53 offen offset:3072 sc1\n"
    "v_mfma_f32_16x16x32_bf16 v[200:203], a[196:199], a[184:187], v[200:203]\n"
    "s_addk_i32 s53, 4096\n"
    "v_mul_f32 v245, v245, v254\n"
    "v_mul_f32 v247, v247, v255\n"
    "v_mfma_f32_16x16x32_bf16 v[200:203], a[192:195], a[152:155], v[200:203]\n"
    "v_cvt_pk_bf16_f32 v248, v244, v245\n"
    "v_cvt_pk_bf16_f32 v249, v246, v247\n"
    "v_mfma_f32_16x16x32_bf16 v[168:171], a[192:195], a[148:151], v[168:171]\n"
    "v_accvgpr_read_b32 v245, a2\n"
    "v_accvgpr_read_b32 v247, a3\n"
    "v_mfma_f32_16x16x32_bf16 v[168:171], a[196:199], a[180:183], v[168:171]\n"
    "v_lshlrev_b32 v244, 16, v245\n"
    "v_lshlrev_b32 v246, 16, v247\n"
    "v_mfma_f32_16x16x32_bf16 v[136:139], a[196:199], a[176:179], v[136:139]\n"
    "v_and_b32 v245, s58, v245\n"
    "v_and_b32 v247, s58, v247\n"
    "v_mfma_f32_16x16x32_bf16 v[136:139], a[192:195], a[144:147], v[136:139]\n"
    "s_xor_b32 s91, s54, 0x40000\n"
    "v_add_f32_dpp v254, v253, v4 row_newbcast:4 row_mask:0xf bank_mask:0xf\n"
    "v_add_f32_dpp v255, v253, v6 row_newbcast:6 row_mask:0xf bank_mask:0xf\n"
    "v_mfma_f32_16x16x32_bf16 v[104:107], a[192:195], a[140:143], v[104:107]\n"
    "v_mul_f32 v244, v244, v254\n"
    "v_mul_f32 v246, v246, v255\n"
    "v_mfma_f32_16x16x32_bf16 v[104:107], a[196:199], a[172:175], v[104:107]\n"
    "v_add_f32_dpp v254, v253, v5 row_newbcast:5 row_mask:0xf bank_mask:0xf\n"
    "v_add_f32_dpp v255, v253, v7 row_newbcast:7 row_mask:0xf bank_mask:0xf\n"
    "v_mfma_f32_16x16x32_bf16 v[72:75], a[196:199], a[168:171], v[72:75]\n"
    "v_mul_f32 v245, v245, v254\n"
    "v_mul_f32 v247, v247, v255\n"
    "v_mfma_f32_16x16x32_bf16 v[72:75], a[192:195], a[136:139], v[72:75]\n"
    "v_cvt_pk_bf16_f32 v250, v244, v245\n"
    "v_cvt_pk_bf16_f32 v251, v246, v247\n"
    "v_mfma_f32_16x16x32_bf16 v[40:43], a[192:195], a[132:135], v[40:43]\n"
    "v_accvgpr_read_b32 v245, a4\n"
    "v_accvgpr_read_b32 v247, a5\n"
    "v_mfma_f32_16x16x32_bf16 v[40:43], a[196:199], a[164:167], v[40:43]\n"
    "v_lshlrev_b32 v244, 16, v245\n"
    "v_lshlrev_b32 v246, 16, v247\n"
    "v_mfma_f32_16x16x32_bf16 v[8:11], a[196:199], a[160:163], v[8:11]\n"
    "v_and_b32 v245, s58, v245\n"
    "v_and_b32 v247, s58, v247\n"
    "v_mfma_f32_16x16x32_bf16 v[8:11], a[192:195], a[128:131], v[8:11]\n"
    "s_waitcnt vmcnt(9)\n"
    "v_add_f32_dpp v254, v253, v32 row_newbcast:0 row_mask:0xf bank_mask:0xf\n"
    "v_add_f32_dpp v255, v253, v34 row_newbcast:2 row_mask:0xf bank_mask:0xf\n"
    "v_mfma_f32_16x16x32_bf16 v[12:15], a[200:203], a[128:131], v[12:15]\n"
    "buffer_load_dwordx4 a[192:195], v240, s[20:23], s53 offen offset:0 sc1\n"
    "v_mfma_f32_16x16x32_bf16 v[12:15], a[204:207], a[160:163], v[12:15]\n"
    "buffer_load_dwordx4 a[196:199], v240, s[20:23], s53 offen offset:1024 sc1\n"
    "v_mfma_f32_16x16x32_bf16 v[44:47], a[204:207], a[164:167], v[44:47]\n"
    "v_mul_f32 v244, v244, v254\n"
    "v_mul_f32 v246, v246, v255\n"
    "v_mfma_f32_16x16x32_bf16 v[44:47], a[200:203], a[132:135], v[44:47]\n"
    "v_add_f32_dpp v254, v253, v33 row_newbcast:1 row_mask:0xf bank_mask:0xf\n"
    "v_add_f32_dpp v255, v253, v35 row_newbcast:3 row_mask:0xf bank_mask:0xf\n"
    "v_mfma_f32_16x16x32_bf16 v[76:79], a[200:203], a[136:139], v[76:79]\n"
    "v_mul_f32 v245, v245, v254\n"
    "v_mul_f32 v247, v247, v255\n"
    "v_mfma_f32_16x16x32_bf16 v[76:79], a[204:207], a[168:171], v[76:79]\n"
    "buffer_store_dwordx4 v[248:251], v240, s[32:35], s91 offen sc1\n"
    "ds_write_b128 v243, v[248:251] offset:0\n"
    "v_mfma_f32_16x16x32_bf16 v[108:111], a[204:207], a[172:175], v[108:111]\n"
    "v_cvt_pk_bf16_f32 v248, v244, v245\n"
    "v_cvt_pk_bf16_f32 v249, v246, v247\n"
    "v_mfma_f32_16x16x32_bf16 v[108:111], a[200:203], a[140:143], v[108:111]\n"
    "v_accvgpr_read_b32 v245, a6\n"
    "v_accvgpr_read_b32 v247, a7\n"
    "v_mfma_f32_16x16x32_bf16 v[140:143], a[200:203], a[144:147], v[140:143]\n"
    "v_lshlrev_b32 v244, 16, v245\n"
    "v_lshlrev_b32 v246, 16, v247\n"
    "v_mfma_f32_16x16x32_bf16 v[140:143], a[204:207], a[176:179], v[140:143]\n"
    "v_and_b32 v245, s58, v245\n"
    "v_and_b32 v247, s58, v247\n"
    "v_mfma_f32_16x16x32_bf16 v[172:175], a[204:207], a[180:183], v[172:175]\n"
    "s_xor_b32 s91, s91, 0x800\n"
    "v_add_f32_dpp v254, v253, v36 row_newbcast:4 row_mask:0xf bank_mask:0xf\n"
    "v_add_f32_dpp v255, v253, v38 row_newbcast:6 row_mask:0xf bank_mask:0xf\n"
    "v_mfma_f32_16x16x32_bf16 v[172:175], a[200:203], a[148:151], v[172:175]\n"
    "v_mul_f32 v244, v244, v254\n"
    "v_mul_f32 v246, v246, v255\n"
    "v_mfma_f32_16x16x32_bf16 v[204:207], a[200:203], a[152:155], v[204:207]\n"
    "v_add_f32_dpp v254, v253, v37 row_newbcast:5 row_mask:0xf bank_mask:0xf\n"
    "v_add_f32_dpp v255, v253, v39 row_newbcast:7 row_mask:0xf bank_mask:0xf\n"
    "v_mfma_f32_16x16x32_bf16 v[204:207], a[204:207], a[184:187], v[204:207]\n"
    "v_mul_f32 v245, v245, v254\n"
    "v_mul_f32 v247, v247, v255\n"
    "v_mfma_f32_16x16x32_bf16 v[236:239], a[204:207], a[188:191], v[236:239]\n"
    "v_cvt_pk_bf16_f32 v250, v244, v245\n"
    "v_cvt_pk_bf16_f32 v251, v246, v247\n"
    "v_mfma_f32_16x16x32_bf16 v[236:239], a[200:203], a[156:159], v[236:239]\n"
    "s_waitcnt vmcnt(9)\n"
    "v_accvgpr_read_b32 v245, a8\n"
    "v_accvgpr_read_b32 v247, a9\n"
    "v_mfma_f32_16x16x32_bf16 a[240:243], a[208:211], a[156:159], a[240:243]\n"
    "buffer_load_dwordx4 a[200:203], v240, s[20:23], s53 offen offset:2048 sc1\n"
    "v_mfma_f32_16x16x32_bf16 a[240:243], a[212:215], a[188:191], a[240:243]\n"
    "buffer_load_dwordx4 a[204:207], v240, s[20:23], s53 offen offset:3072 sc1\n"
    "v_mfma_f32_16x16x32_bf16 v[208:211], a[212:215], a[184:187], v[208:211]\n"
    "s_addk_i32 s53, 4096\n"
    "v_lshlrev_b32 v244, 16, v245\n"
    "v_lshlrev_b32 v246, 16, v247\n"
    "v_mfma_f32_16x16x32_bf16 v[208:211], a[208:211], a[152:155], v[208:211]\n"
    "v_and_b32 v245, s58, v245\n"
    "v_and_b32 v247, s58, v247\n"
    "v_mfma_f32_16x16x32_bf16 v[176:179], a[208:211], a[148:151], v[176:179]\n"
    "v_add_f32_dpp v254, v253, v64 row_newbcast:0 row_mask:0xf bank_mask:0xf\n"
    "v_add_f32_dpp v255, v253, v66 row_newbcast:2 row_mask:0xf bank_mask:0xf\n"
    "v_mfma_f32_16x16x32_bf16 v[176:179], a[212:215], a[180:183], v[176:179]\n"
    "v_mul_f32 v244, v244, v254\n"
    "v_mul_f32 v246, v246, v255\n"
    "v_mfma_f32_16x16x32_bf16 v[144:147], a[212:215], a[176:179], v[144:147]\n"
    "v_add_f32_dpp v254, v253, v65 row_newbcast:1 row_mask:0xf bank_mask:0xf\n"
    "v_add_f32_dpp v255, v253, v67 row_newbcast:3 row_mask:0xf bank_mask:0xf\n"
    "v_mfma_f32_16x16x32_bf16 v[144:147], a[208:211], a[144:147], v[144:147]\n"
    "v_mul_f32 v245, v245, v254\n"
    "v_mul_f32 v247, v247, v255\n"
    "v_mfma_f32_16x16x32_bf16 v[112:115], a[208:211], a[140:143], v[112:115]\n"
    "v_mfma_f32_16x16x32_bf16 v[112:115], a[212:215], a[172:175], v[112:115]\n"
    "buffer_store_dwordx4 v[248:251], v240, s[32:35], s91 offen sc1\n"
    "ds_write_b128 v243, v[248:251] offset:2048\n"
    "v_mfma_f32_16x16x32_bf16 v[80:83], a[212:215], a[168:171], v[80:83]\n"
    "v_cvt_pk_bf16_f32 v248, v244, v245\n"
    "v_cvt_pk_bf16_f32 v249, v246, v247\n"
    "v_mfma_f32_16x16x32_bf16 v[80:83], a[208:211], a[136:139], v[80:83]\n"
    "v_accvgpr_read_b32 v245, a10\n"
    "v_accvgpr_read_b32 v247, a11\n"
    "v_mfma_f32_16x16x32_bf16 v[48:51], a[208:211], a[132:135], v[48:51]\n"
    "v_lshlrev_b32 v244, 16, v245\n"
    "v_lshlrev_b32 v246, 16, v247\n"
    "v_mfma_f32_16x16x32_bf16 v[48:51], a[212:215], a[164:167], v[48:51]\n"
    "v_and_b32 v245, s58, v245\n"
    "v_and_b32 v247, s58, v247\n"
    "v_mfma_f32_16x16x32_bf16 v[16:19], a[212:215], a[160:163], v[16:19]\n"
    "s_xor_b32 s91, s91, 0x101800\n"
    "v_add_f32_dpp v254, v253, v68 row_newbcast:4 row_mask:0xf bank_mask:0xf\n"
    "v_add_f32_dpp v255, v253, v70 row_newbcast:6 row_mask:0xf bank_mask:0xf\n"
    "v_mfma_f32_16x16x32_bf16 v[16:19], a[208:211], a[128:131], v[16:19]\n"
    "s_waitcnt vmcnt(10)\n"
    "v_mul_f32 v244, v244, v254\n"
    "v_mul_f32 v246, v246, v255\n"
    "v_mfma_f32_16x16x32_bf16 v[20:23], a[216:219], a[128:131], v[20:23]\n"
    "buffer_load_dwordx4 a[208:211], v240, s[20:23], s53 offen offset:0 sc1\n"
    "v_mfma_f32_16x16x32_bf16 v[20:23], a[220:223], a[160:163], v[20:23]\n"
    "buffer_load_dwordx4 a[212:215], v240, s[20:23], s53 offen offset:1024 sc1\n"
    "v_mfma_f32_16x16x32_bf16 v[52:55], a[220:223], a[164:167], v[52:55]\n"
    "v_add_f32_dpp v254, v253, v69 row_newbcast:5 row_mask:0xf bank_mask:0xf\n"
    "v_add_f32_dpp v255, v253, v71 row_newbcast:7 row_mask:0xf bank_mask:0xf\n"
    "v_mfma_f32_16x16x32_bf16 v[52:55], a[216:219], a[132:135], v[52:55]\n"
    "v_mul_f32 v245, v245, v254\n"
    "v_mul_f32 v247, v247, v255\n"
    "v_mfma_f32_16x16x32_bf16 v[84:87], a[216:219], a[136:139], v[84:87]\n"
    "v_cvt_pk_bf16_f32 v250, v244, v245\n"
    "v_cvt_pk_bf16_f32 v251, v246, v247\n"
    "v_mfma_f32_16x16x32_bf16 v[84:87], a[220:223], a[168:171], v[84:87]\n"
    "v_accvgpr_read_b32 v245, a12\n"
    "v_accvgpr_read_b32 v247, a13\n"
    "v_mfma_f32_16x16x32_bf16 v[116:119], a[220:223], a[172:175], v[116:119]\n"
    "v_lshlrev_b32 v244, 16, v245\n"
    "v_lshlrev_b32 v246, 16, v247\n"
    "v_mfma_f32_16x16x32_bf16 v[116:119], a[216:219], a[140:143], v[116:119]\n"
    "v_and_b32 v245, s58, v245\n"
    "v_and_b32 v247, s58, v247\n"
    "v_mfma_f32_16x16x32_bf16 v[148:151], a[216:219], a[144:147], v[148:151]\n"
    "v_add_f32_dpp v254, v253, v96 row_newbcast:0 row_mask:0xf bank_mask:0xf\n"
    "v_add_f32_dpp v255, v253, v98 row_newbcast:2 row_mask:0xf bank_mask:0xf\n"
    "v_mfma_f32_16x16x32_bf16 v[148:151], a[220:223], a[176:179], v[148:151]\n"
    "v_mul_f32 v244, v244, v254\n"
    "v_mul_f32 v246, v246, v255\n"
    "v_mfma_f32_16x16x32_bf16 v[180:183], a[220:223], a[180:183], v[180:183]\n"
    "v_add_f32_dpp v254, v253, v97 row_newbcast:1 row_mask:0xf bank_mask:0xf\n"
    "v_add_f32_dpp v255, v253, v99 row_newbcast:3 row_mask:0xf bank_mask:0xf\n"
    "v_mfma_f32_16x16x32_bf16 v[180:183], a[216:219], a[148:151], v[180:183]\n"
    "v_mul_f32 v245, v245, v254\n"
    "v_mul_f32 v247, v247, v255\n"
    "v_mfma_f32_16x16x32_bf16 v[212:215], a[216:219], a[152:155], v[212:215]\n"
    "v_mfma_f32_16x16x32_bf16 v[212:215], a[220:223], a[184:187], v[212:215]\n"
    "buffer_store_dwordx4 v[248:251], v240, s[32:35], s91 offen sc1\n"
    "ds_write_b128 v243, v[248:251] offset:4096\n"
    "v_mfma_f32_16x16x32_bf16 a[244:247], a[220:223], a[188:191], a[244:247]\n"
    "v_cvt_pk_bf16_f32 v248, v244, v245\n"
    "v_cvt_pk_bf16_f32 v249, v246, v247\n"
    "v_mfma_f32_16x16x32_bf16 a[244:247], a[216:219], a[156:159], a[244:247]\n"
    "s_waitcnt vmcnt(11)\n"
    "v_accvgpr_read_b32 v245, a14\n"
    "v_accvgpr_read_b32 v247, a15\n"
    "v_mfma_f32_16x16x32_bf16 a[248:251], a[224:227], a[156:159], a[248:251]\n"
    "buffer_load_dwordx4 a[216:219], v240, s[20:23], s53 offen offset:2048 sc1\n"
    "v_mfma_f32_16x16x32_bf16 a[248:251], a[228:231], a[188:191], a[248:251]\n"
    "buffer_load_dwordx4 a[220:223], v240, s[20:23], s53 offen offset:3072 sc1\n"
    "v_mfma_f32_16x16x32_bf16 v[216:219], a[228:231], a[184:187], v[216:219]\n"
    "s_addk_i32 s53, 4096\n"
    "v_lshlrev_b32 v244, 16, v245\n"
    "v_lshlrev_b32 v246, 16, v247\n"
    "v_mfma_f32_16x16x32_bf16 v[216:219], a[224:227], a[152:155], v[216:219]\n"
    "v_and_b32 v245, s58, v245\n"
    "v_and_b32 v247, s58, v247\n"
    "v_mfma_f32_16x16x32_bf16 v[184:187], a[224:227], a[148:151], v[184:187]\n"
    "s_xor_b32 s91, s91, 0x800\n"
    "v_add_f32_dpp v254, v253, v100 row_newbcast:4 row_mask:0xf bank_mask:0xf\n"
    "v_add_f32_dpp v255, v253, v102 row_newbcast:6 row_mask:0xf bank_mask:0xf\n"
    "v_mfma_f32_16x16x32_bf16 v[184:187], a[228:231], a[180:183], v[184:187]\n"
    "v_mul_f32 v244, v244, v254\n"
    "v_mul_f32 v246, v246, v255\n"
    "v_mfma_f32_16x16x32_bf16 v[152:155], a[228:231], a[176:179], v[152:155]\n"
    "v_add_f32_dpp v254, v253, v101 row_newbcast:5 row_mask:0xf bank_mask:0xf\n"
    "v_add_f32_dpp v255, v253, v103 row_newbcast:7 row_mask:0xf bank_mask:0xf\n"
    "v_mfma_f32_16x16x32_bf16 v[152:155], a[224:227], a[144:147], v[152:155]\n"
    "v_mul_f32 v245, v245, v254\n"
    "v_mul_f32 v247, v247, v255\n"
    "v_mfma_f32_16x16x32_bf16 v[120:123], a[224:227], a[140:143], v[120:123]\n"
    "v_cvt_pk_bf16_f32 v250, v244, v245\n"
    "v_cvt_pk_bf16_f32 v251, v246, v247\n"
    "v_mfma_f32_16x16x32_bf16 v[120:123], a[228:231], a[172:175], v[120:123]\n"
    "v_accvgpr_read_b32 v245, a16\n"
    "v_accvgpr_read_b32 v247, a17\n"
    "v_mfma_f32_16x16x32_bf16 v[88:91], a[228:231], a[168:171], v[88:91]\n"
    "v_lshlrev_b32 v244, 16, v245\n"
    "v_lshlrev_b32 v246, 16, v247\n"
    "v_mfma_f32_16x16x32_bf16 v[88:91], a[224:227], a[136:139], v[88:91]\n"
    "v_and_b32 v245, s58, v245\n"
    "v_and_b32 v247, s58, v247\n"
    "v_mfma_f32_16x16x32_bf16 v[56:59], a[224:227], a[132:135], v[56:59]\n"
    "v_add_f32_dpp v254, v253, v128 row_newbcast:0 row_mask:0xf bank_mask:0xf\n"
    "v_add_f32_dpp v255, v253, v130 row_newbcast:2 row_mask:0xf bank_mask:0xf\n"
    "v_mfma_f32_16x16x32_bf16 v[56:59], a[228:231], a[164:167], v[56:59]\n"
    "v_mul_f32 v244, v244, v254\n"
    "v_mul_f32 v246, v246, v255\n"
    "v_mfma_f32_16x16x32_bf16 v[24:27], a[228:231], a[160:163], v[24:27]\n"
    "v_add_f32_dpp v254, v253, v129 row_newbcast:1 row_mask:0xf bank_mask:0xf\n"
    "v_add_f32_dpp v255, v253, v131 row_newbcast:3 row_mask:0xf bank_mask:0xf\n"
    "v_mfma_f32_16x16x32_bf16 v[24:27], a[224:227], a[128:131], v[24:27]\n"
    "s_waitcnt vmcnt(11)\n"
    "v_mul_f32 v245, v245, v254\n"
    "v_mul_f32 v247, v247, v255\n"
    "v_mfma_f32_16x16x32_bf16 v[28:31], a[232:235], a[128:131], v[28:31]\n"
    "buffer_load_dwordx4 a[224:227], v240, s[20:23], s53 offen offset:0 sc1\n"
    "v_mfma_f32_16x16x32_bf16 v[28:31], a[236:239], a[160:163], v[28:31]\n"
    "buffer_load_dwordx4 a[228:231], v240, s[20:23], s53 offen offset:1024 sc1\n"
    "v_mfma_f32_16x16x32_bf16 v[60:63], a[236:239], a[164:167], v[60:63]\n"
    "ds_read_b128 a[128:131], v241 offset:49152\n"
    "v_mfma_f32_16x16x32_bf16 v[60:63], a[232:235], a[132:135], v[60:63]\n"
    "buffer_store_dwordx4 v[248:251], v240, s[32:35], s91 offen sc1\n"
    "ds_write_b128 v243, v[248:251] offset:6144\n"
    "v_mfma_f32_16x16x32_bf16 v[92:95], a[232:235], a[136:139], v[92:95]\n"
    "ds_read_b128 a[160:163], v242 offset:49152\n"
    "ds_read_b128 a[164:167], v242 offset:51200\n"
    "v_mfma_f32_16x16x32_bf16 v[92:95], a[236:239], a[168:171], v[92:95]\n"
    "ds_read_b128 a[132:135], v241 offset:51200\n"
    "v_cvt_pk_bf16_f32 v248, v244, v245\n"
    "v_mfma_f32_16x16x32_bf16 v[124:127], a[236:239], a[172:175], v[124:127]\n"
    "ds_read_b128 a[136:139], v241 offset:53248\n"
    "v_cvt_pk_bf16_f32 v249, v246, v247\n"
    "v_mfma_f32_16x16x32_bf16 v[124:127], a[232:235], a[140:143], v[124:127]\n"
    "ds_read_b128 a[168:171], v242 offset:53248\n"
    "v_accvgpr_read_b32 v245, a18\n"
    "v_mfma_f32_16x16x32_bf16 v[156:159], a[232:235], a[144:147], v[156:159]\n"
    "ds_read_b128 a[172:175], v242 offset:55296\n"
    "v_accvgpr_read_b32 v247, a19\n"
    "v_mfma_f32_16x16x32_bf16 v[156:159], a[236:239], a[176:179], v[156:159]\n"
    "ds_read_b128 a[140:143], v241 offset:55296\n"
    "v_lshlrev_b32 v244, 16, v245\n"
    "v_mfma_f32_16x16x32_bf16 v[188:191], a[236:239], a[180:183], v[188:191]\n"
    "ds_read_b128 a[144:147], v241 offset:57344\n"
    "v_lshlrev_b32 v246, 16, v247\n"
    "v_mfma_f32_16x16x32_bf16 v[188:191], a[232:235], a[148:151], v[188:191]\n"
    "ds_read_b128 a[176:179], v242 offset:57344\n"
    "v_and_b32 v245, s58, v245\n"
    "v_mfma_f32_16x16x32_bf16 v[220:223], a[232:235], a[152:155], v[220:223]\n"
    "ds_read_b128 a[180:183], v242 offset:59392\n"
    "v_and_b32 v247, s58, v247\n"
    "v_mfma_f32_16x16x32_bf16 v[220:223], a[236:239], a[184:187], v[220:223]\n"
    "ds_read_b128 a[148:151], v241 offset:59392\n"
    "v_add_f32_dpp v254, v253, v132 row_newbcast:4 row_mask:0xf bank_mask:0xf\n"
    "v_mfma_f32_16x16x32_bf16 a[252:255], a[236:239], a[188:191], a[252:255]\n"
    "ds_read_b128 a[152:155], v241 offset:61440\n"
    "s_xor_b32 s91, s91, 0x185800\n"
    "v_mfma_f32_16x16x32_bf16 a[252:255], a[232:235], a[156:159], a[252:255]\n"
    "s_waitcnt vmcnt(12) lgkmcnt(11)\n"
    "ds_read_b128 a[184:187], v242 offset:61440\n"
    "ds_read_b128 a[188:191], v242 offset:63488\n"
    "v_mfma_f32_16x16x32_bf16 v[16:19], a[192:195], a[128:131], v[16:19]\n"
    "ds_read_b128 a[156:159], v241 offset:63488\n"
    "v_add_f32_dpp v255, v253, v134 row_newbcast:6 row_mask:0xf bank_mask:0xf\n"
    "v_mfma_f32_16x16x32_bf16 v[16:19], a[196:199], a[160:163], v[16:19]\n"
    "s_waitcnt lgkmcnt(12)\n"
    "v_mul_f32 v244, v244, v254\n"
    "v_mul_f32 v246, v246, v255\n"
    "v_mfma_f32_16x16x32_bf16 v[48:51], a[196:199], a[164:167], v[48:51]\n"
    "buffer_load_dwordx4 a[232:235], v240, s[20:23], s53 offen offset:2048 sc1\n"
    "v_mfma_f32_16x16x32_bf16 v[48:51], a[192:195], a[132:135], v[48:51]\n"
    "s_waitcnt lgkmcnt(10)\n"
    "v_add_f32_dpp v254, v253, v133 row_newbcast:5 row_mask:0xf bank_mask:0xf\n"
    "v_add_f32_dpp v255, v253, v135 row_newbcast:7 row_mask:0xf bank_mask:0xf\n"
    "v_mfma_f32_16x16x32_bf16 v[80:83], a[192:195], a[136:139], v[80:83]\n"
    "buffer_load_dwordx4 a[236:239], v240, s[20:23], s53 offen offset:3072 sc1\n"
    "s_addk_i32 s53, 4096\n"
    "v_mfma_f32_16x16x32_bf16 v[80:83], a[196:199], a[168:171], v[80:83]\n"
    "s_waitcnt lgkmcnt(8)\n"
    "v_mul_f32 v245, v245, v254\n"
    "v_mul_f32 v247, v247, v255\n"
    "v_mfma_f32_16x16x32_bf16 v[112:115], a[196:199], a[172:175], v[112:115]\n"
    "v_cvt_pk_bf16_f32 v250, v244, v245\n"
    "v_cvt_pk_bf16_f32 v251, v246, v247\n"
    "v_mfma_f32_16x16x32_bf16 v[112:115], a[192:195], a[140:143], v[112:115]\n"
    "s_waitcnt lgkmcnt(6)\n"
    "v_accvgpr_read_b32 v245, a20\n"
    "v_accvgpr_read_b32 v247, a21\n"
    "v_mfma_f32_16x16x32_bf16 v[144:147], a[192:195], a[144:147], v[144:147]\n"
    "v_lshlrev_b32 v244, 16, v245\n"
    "v_lshlrev_b32 v246, 16, v247\n"
    "v_mfma_f32_16x16x32_bf16 v[144:147], a[196:199], a[176:179], v[144:147]\n"
    "s_waitcnt lgkmcnt(4)\n"
    "v_and_b32 v245, s58, v245\n"
    "v_and_b32 v247, s58, v247\n"
    "v_mfma_f32_16x16x32_bf16 v[176:179], a[196:199], a[180:183], v[176:179]\n"
    "v_add_f32_dpp v254, v253, v160 row_newbcast:0 row_mask:0xf bank_mask:0xf\n"
    "v_add_f32_dpp v255, v253, v162 row_newbcast:2 row_mask:0xf bank_mask:0xf\n"
    "v_mfma_f32_16x16x32_bf16 v[176:179], a[192:195], a[148:151], v[176:179]\n"
    "s_waitcnt lgkmcnt(2)\n"
    "v_mul_f32 v244, v244, v254\n"
    "v_mul_f32 v246, v246, v255\n"
    "v_mfma_f32_16x16x32_bf16 v[208:211], a[192:195], a[152:155], v[208:211]\n"
    "v_add_f32_dpp v254, v253, v161 row_newbcast:1 row_mask:0xf bank_mask:0xf\n"
    "v_add_f32_dpp v255, v253, v163 row_newbcast:3 row_mask:0xf bank_mask:0xf\n"
    "v_mfma_f32_16x16x32_bf16 v[208:211], a[196:199], a[184:187], v[208:211]\n"
    "s_waitcnt lgkmcnt(0)\n"
    "v_mul_f32 v245, v245, v254\n"
    "v_mul_f32 v247, v247, v255\n"
    "v_mfma_f32_16x16x32_bf16 a[240:243], a[196:199], a[188:191], a[240:243]\n"
    "v_mfma_f32_16x16x32_bf16 a[240:243], a[192:195], a[156:159], a[240:243]\n"
    "s_waitcnt vmcnt(11)\n"
    "v_mfma_f32_16x16x32_bf16 a[244:247], a[200:203], a[156:159], a[244:247]\n"
    "buffer_store_dwordx4 v[248:251], v240, s[32:35], s91 offen sc1\n"
    "ds_write_b128 v243, v[248:251] offset:8192\n"
    "v_mfma_f32_16x16x32_bf16 a[244:247], a[204:207], a[188:191], a[244:247]\n"
    "buffer_load_dwordx4 a[192:195], v240, s[20:23], s53 offen offset:0 sc1\n"
    "v_mfma_f32_16x16x32_bf16 v[212:215], a[204:207], a[184:187], v[212:215]\n"
    "buffer_load_dwordx4 a[196:199], v240, s[20:23], s53 offen offset:1024 sc1\n"
    "v_mfma_f32_16x16x32_bf16 v[212:215], a[200:203], a[152:155], v[212:215]\n"
    "v_cvt_pk_bf16_f32 v248, v244, v245\n"
    "v_cvt_pk_bf16_f32 v249, v246, v247\n"
    "v_mfma_f32_16x16x32_bf16 v[180:183], a[200:203], a[148:151], v[180:183]\n"
    "v_accvgpr_read_b32 v245, a22\n"
    "v_accvgpr_read_b32 v247, a23\n"
    "v_mfma_f32_16x16x32_bf16 v[180:183], a[204:207], a[180:183], v[180:183]\n"
    "v_lshlrev_b32 v244, 16, v245\n"
    "v_lshlrev_b32 v246, 16, v247\n"
    "v_mfma_f32_16x16x32_bf16 v[148:151], a[204:207], a[176:179], v[148:151]\n"
    "v_and_b32 v245, s58, v245\n"
    "v_and_b32 v247, s58, v247\n"
    "v_mfma_f32_16x16x32_bf16 v[148:151], a[200:203], a[144:147], v[148:151]\n"
    "s_xor_b32 s91, s91, 0x800\n"
    "v_add_f32_dpp v254, v253, v164 row_newbcast:4 row_mask:0xf bank_mask:0xf\n"
    "v_add_f32_dpp v255, v253, v166 row_newbcast:6 row_mask:0xf bank_mask:0xf\n"
    "v_mfma_f32_16x16x32_bf16 v[116:119], a[200:203], a[140:143], v[116:119]\n"
    "v_mul_f32 v244, v244, v254\n"
    "v_mul_f32 v246, v246, v255\n"
    "v_mfma_f32_16x16x32_bf16 v[116:119], a[204:207], a[172:175], v[116:119]\n"
    "v_add_f32_dpp v254, v253, v165 row_newbcast:5 row_mask:0xf bank_mask:0xf\n"
    "v_add_f32_dpp v255, v253, v167 row_newbcast:7 row_mask:0xf bank_mask:0xf\n"
    "v_mfma_f32_16x16x32_bf16 v[84:87], a[204:207], a[168:171], v[84:87]\n"
    "v_mul_f32 v245, v245, v254\n"
    "v_mul_f32 v247, v247, v255\n"
    "v_mfma_f32_16x16x32_bf16 v[84:87], a[200:203], a[136:139], v[84:87]\n"
    "v_cvt_pk_bf16_f32 v250, v244, v245\n"
    "v_cvt_pk_bf16_f32 v251, v246, v247\n"
    "v_mfma_f32_16x16x32_bf16 v[52:55], a[200:203], a[132:135], v[52:55]\n"
    "v_accvgpr_read_b32 v245, a24\n"
    "v_accvgpr_read_b32 v247, a25\n"
    "v_mfma_f32_16x16x32_bf16 v[52:55], a[204:207], a[164:167], v[52:55]\n"
    "v_lshlrev_b32 v244, 16, v245\n"
    "v_lshlrev_b32 v246, 16, v247\n"
    "v_mfma_f32_16x16x32_bf16 v[20:23], a[204:207], a[160:163], v[20:23]\n"
    "v_and_b32 v245, s58, v245\n"
    "v_and_b32 v247, s58, v247\n"
    "v_mfma_f32_16x16x32_bf16 v[20:23], a[200:203], a[128:131], v[20:23]\n"
    "s_waitcnt vmcnt(11)\n"
    "v_add_f32_dpp v254, v253, v192 row_newbcast:0 row_mask:0xf bank_mask:0xf\n"
    "v_add_f32_dpp v255, v253, v194 row_newbcast:2 row_mask:0xf bank_mask:0xf\n"
    "v_mfma_f32_16x16x32_bf16 v[24:27], a[208:211], a[128:131], v[24:27]\n"
    "buffer_load_dwordx4 a[200:203], v240, s[20:23], s53 offen offset:2048 sc1\n"
    "v_mfma_f32_16x16x32_bf16 v[24:27], a[212:215], a[160:163], v[24:27]\n"
    "buffer_load_dwordx4 a[204:207], v240, s[20:23], s53 offen offset:3072 sc1\n"
    "v_mfma_f32_16x16x32_bf16 v[56:59], a[212:215], a[164:167], v[56:59]\n"
    "s_addk_i32 s53, 4096\n"
    "v_mul_f32 v244, v244, v254\n"
    "v_mul_f32 v246, v246, v255\n"
    "v_mfma_f32_16x16x32_bf16 v[56:59], a[208:211], a[132:135], v[56:59]\n"
    "v_add_f32_dpp v254, v253, v193 row_newbcast:1 row_mask:0xf bank_mask:0xf\n"
    "v_add_f32_dpp v255, v253, v195 row_newbcast:3 row_mask:0xf bank_mask:0xf\n"
    "v_mfma_f32_16x16x32_bf16 v[88:91], a[208:211], a[136:139], v[88:91]\n"
    "buffer_store_dwordx4 v[248:251], v240, s[32:35], s91 offen sc1\n"
    "ds_write_b128 v243, v[248:251] offset:10240\n"
    "v_mfma_f32_16x16x32_bf16 v[88:91], a[212:215], a[168:171], v[88:91]\n"
    "v_mul_f32 v245, v245, v254\n"
    "v_mul_f32 v247, v247, v255\n"
    "v_mfma_f32_16x16x32_bf16 v[120:123], a[212:215], a[172:175], v[120:123]\n"
    "v_cvt_pk_bf16_f32 v248, v244, v245\n"
    "v_cvt_pk_bf16_f32 v249, v246, v247\n"
    "v_mfma_f32_16x16x32_bf16 v[120:123], a[208:211], a[140:143], v[120:123]\n"
    "s_xor_b32 s91, s91, 0x101800\n"
    "v_accvgpr_read_b32 v245, a26\n"
    "v_accvgpr_read_b32 v247, a27\n"
    "v_mfma_f32_16x16x32_bf16 v[152:155], a[208:211], a[144:147], v[152:155]\n"
    "v_lshlrev_b32 v244, 16, v245\n"
    "v_lshlrev_b32 v246, 16, v247\n"
    "v_mfma_f32_16x16x32_bf16 v[152:155], a[212:215], a[176:179], v[152:155]\n"
    "v_and_b32 v245, s58, v245\n"
    "v_and_b32 v247, s58, v247\n"
    "v_mfma_f32_16x16x32_bf16 v[184:187], a[212:215], a[180:183], v[184:187]\n"
    "v_add_f32_dpp v254, v253, v196 row_newbcast:4 row_mask:0xf bank_mask:0xf\n"
    "v_add_f32_dpp v255, v253, v198 row_newbcast:6 row_mask:0xf bank_mask:0xf\n"
    "v_mfma_f32_16x16x32_bf16 v[184:187], a[208:211], a[148:151], v[184:187]\n"
    "v_mul_f32 v244, v244, v254\n"
    "v_mul_f32 v246, v246, v255\n"
    "v_mfma_f32_16x16x32_bf16 v[216:219], a[208:211], a[152:155], v[216:219]\n"
    "v_add_f32_dpp v254, v253, v197 row_newbcast:5 row_mask:0xf bank_mask:0xf\n"
    "v_add_f32_dpp v255, v253, v199 row_newbcast:7 row_mask:0xf bank_mask:0xf\n"
    "v_mfma_f32_16x16x32_bf16 v[216:219], a[212:215], a[184:187], v[216:219]\n"
    "v_mul_f32 v245, v245, v254\n"
    "v_mul_f32 v247, v247, v255\n"
    "v_mfma_f32_16x16x32_bf16 a[248:251], a[212:215], a[188:191], a[248:251]\n"
    "v_cvt_pk_bf16_f32 v250, v244, v245\n"
    "v_cvt_pk_bf16_f32 v251, v246, v247\n"
    "v_mfma_f32_16x16x32_bf16 a[248:251], a[208:211], a[156:159], a[248:251]\n"
    "s_waitcnt vmcnt(11)\n"
    "v_accvgpr_read_b32 v245, a28\n"
    "v_accvgpr_read_b32 v247, a29\n"
    "v_mfma_f32_16x16x32_bf16 a[252:255], a[216:219], a[156:159], a[252:255]\n"
    "buffer_store_dwordx4 v[248:251], v240, s[32:35], s91 offen sc1\n"
    "ds_write_b128 v243, v[248:251] offset:12288\n"
    "v_mfma_f32_16x16x32_bf16 a[252:255], a[220:223], a[188:191], a[252:255]\n"
    "buffer_load_dwordx4 a[208:211], v240, s[20:23], s53 offen offset:0 sc1\n"
    "v_mfma_f32_16x16x32_bf16 v[220:223], a[220:223], a[184:187], v[220:223]\n"
    "buffer_load_dwordx4 a[212:215], v240, s[20:23], s53 offen offset:1024 sc1\n"
    "v_lshlrev_b32 v244, 16, v245\n"
    "v_lshlrev_b32 v246, 16, v247\n"
    "v_mfma_f32_16x16x32_bf16 v[220:223], a[216:219], a[152:155], v[220:223]\n"
    "ds_read_b128 a[156:159], v241 offset:30720\n"
    "ds_read_b128 a[188:191], v242 offset:30720\n"
    "v_and_b32 v245, s58, v245\n"
    "v_and_b32 v247, s58, v247\n"
    "v_add_f32_dpp v254, v253, v224 row_newbcast:0 row_mask:0xf bank_mask:0xf\n"
    "v_add_f32_dpp v255, v253, v226 row_newbcast:2 row_mask:0xf bank_mask:0xf\n"
    "v_mfma_f32_16x16x32_bf16 v[188:191], a[216:219], a[148:151], v[188:191]\n"
    "ds_read_b128 a[184:187], v242 offset:28672\n"
    "v_mul_f32 v244, v244, v254\n"
    "v_mul_f32 v246, v246, v255\n"
    "v_add_f32_dpp v254, v253, v225 row_newbcast:1 row_mask:0xf bank_mask:0xf\n"
    "v_add_f32_dpp v255, v253, v227 row_newbcast:3 row_mask:0xf bank_mask:0xf\n"
    "v_mfma_f32_16x16x32_bf16 v[188:191], a[220:223], a[180:183], v[188:191]\n"
    "ds_read_b128 a[152:155], v241 offset:28672\n"
    "v_mul_f32 v245, v245, v254\n"
    "v_mul_f32 v247, v247, v255\n"
    "v_cvt_pk_bf16_f32 v248, v244, v245\n"
    "v_mfma_f32_16x16x32_bf16 v[156:159], a[220:223], a[176:179], v[156:159]\n"
    "ds_read_b128 a[148:151], v241 offset:26624\n"
    "v_cvt_pk_bf16_f32 v249, v246, v247\n"
    "v_accvgpr_read_b32 v245, a30\n"
    "v_accvgpr_read_b32 v247, a31\n"
    "v_lshlrev_b32 v244, 16, v245\n"
    "v_mfma_f32_16x16x32_bf16 v[156:159], a[216:219], a[144:147], v[156:159]\n"
    "ds_read_b128 a[180:183], v242 offset:26624\n"
    "v_lshlrev_b32 v246, 16, v247\n"
    "v_and_b32 v245, s58, v245\n"
    "v_and_b32 v247, s58, v247\n"
    "v_mfma_f32_16x16x32_bf16 v[124:127], a[216:219], a[140:143], v[124:127]\n"
    "ds_read_b128 a[176:179], v242 offset:24576\n"
    "v_add_f32_dpp v254, v253, v228 row_newbcast:4 row_mask:0xf bank_mask:0xf\n"
    "v_mfma_f32_16x16x32_bf16 v[124:127], a[220:223], a[172:175], v[124:127]\n"
    "ds_read_b128 a[144:147], v241 offset:24576\n"
    "s_xor_b32 s91, s91, 0x800\n"
    "v_mfma_f32_16x16x32_bf16 v[92:95], a[220:223], a[168:171], v[92:95]\n"
    "ds_read_b128 a[140:143], v241 offset:22528\n"
    "v_add_f32_dpp v255, v253, v230 row_newbcast:6 row_mask:0xf bank_mask:0xf\n"
    "v_mfma_f32_16x16x32_bf16 v[92:95], a[216:219], a[136:139], v[92:95]\n"
    "ds_read_b128 a[172:175], v242 offset:22528\n"
    "v_mul_f32 v244, v244, v254\n"
    "v_mfma_f32_16x16x32_bf16 v[60:63], a[216:219], a[132:135], v[60:63]\n"
    "ds_read_b128 a[168:171], v242 offset:20480\n"
    "v_mul_f32 v246, v246, v255\n"
    "v_mfma_f32_16x16x32_bf16 v[60:63], a[220:223], a[164:167], v[60:63]\n"
    "ds_read_b128 a[136:139], v241 offset:20480\n"
    "v_add_f32_dpp v254, v253, v229 row_newbcast:5 row_mask:0xf bank_mask:0xf\n"
    "v_mfma_f32_16x16x32_bf16 v[28:31], a[220:223], a[160:163], v[28:31]\n"
    "ds_read_b128 a[132:135], v241 offset:18432\n"
    "v_add_f32_dpp v255, v253, v231 row_newbcast:7 row_mask:0xf bank_mask:0xf\n"
    "v_mfma_f32_16x16x32_bf16 v[28:31], a[216:219], a[128:131], v[28:31]\n"
    "s_waitcnt vmcnt(12) lgkmcnt(11)\n"
    "ds_read_b128 a[164:167], v242 offset:18432\n"
    "ds_read_b128 a[160:163], v242 offset:16384\n"
    "v_mfma_f32_16x16x32_bf16 a[240:243], a[224:227], a[156:159], a[240:243]\n"
    "ds_read_b128 a[128:131], v241 offset:16384\n"
    "v_mul_f32 v245, v245, v254\n"
    "v_mfma_f32_16x16x32_bf16 a[240:243], a[228:231], a[188:191], a[240:243]\n"
    "s_waitcnt lgkmcnt(12)\n"
    "v_mul_f32 v247, v247, v255\n"
    "v_cvt_pk_bf16_f32 v250, v244, v245\n"
    "v_mfma_f32_16x16x32_bf16 v[208:211], a[228:231], a[184:187], v[208:211]\n"
    "buffer_load_dwordx4 a[216:219], v240, s[20:23], s53 offen offset:2048 sc1\n"
    "v_mfma_f32_16x16x32_bf16 v[208:211], a[224:227], a[152:155], v[208:211]\n"
    "s_waitcnt lgkmcnt(10)\n"
    "v_cvt_pk_bf16_f32 v251, v246, v247\n"
    "v_mfma_f32_16x16x32_bf16 v[176:179], a[224:227], a[148:151], v[176:179]\n"
    "buffer_load_dwordx4 a[220:223], v240, s[20:23], s53 offen offset:3072 sc1\n"
    "s_addk_i32 s53, 4096\n"
    "v_mfma_f32_16x16x32_bf16 v[176:179], a[228:231], a[180:183], v[176:179]\n"
    "s_waitcnt lgkmcnt(8)\n"
    "v_accvgpr_read_b32 v245, a32\n"
    "v_accvgpr_read_b32 v247, a33\n"
    "v_mfma_f32_16x16x32_bf16 v[144:147], a[228:231], a[176:179], v[144:147]\n"
    "buffer_store_dwordx4 v[248:251], v240, s[32:35], s91 offen sc1\n"
    "ds_write_b128 v243, v[248:251] offset:14336\n"
    "v_mfma_f32_16x16x32_bf16 v[144:147], a[224:227], a[144:147], v[144:147]\n"
    "s_waitcnt lgkmcnt(7)\n"
    "v_lshlrev_b32 v244, 16, v245\n"
    "v_lshlrev_b32 v246, 16, v247\n"
    "v_mfma_f32_16x16x32_bf16 v[112:115], a[224:227], a[140:143], v[112:115]\n"
    "v_and_b32 v245, s58, v245\n"
    "v_and_b32 v247, s58, v247\n"
    "v_mfma_f32_16x16x32_bf16 v[112:115], a[228:231], a[172:175], v[112:115]\n"
    "s_waitcnt lgkmcnt(5)\n"
    "v_add_f32_dpp v254, v253, v8 row_newbcast:8 row_mask:0xf bank_mask:0xf\n"
    "v_add_f32_dpp v255, v253, v10 row_newbcast:10 row_mask:0xf bank_mask:0xf\n"
    "v_mfma_f32_16x16x32_bf16 v[80:83], a[228:231], a[168:171], v[80:83]\n"
    "v_mul_f32 v244, v244, v254\n"
    "v_mul_f32 v246, v246, v255\n"
    "v_mfma_f32_16x16x32_bf16 v[80:83], a[224:227], a[136:139], v[80:83]\n"
    "s_waitcnt lgkmcnt(3)\n"
    "v_add_f32_dpp v254, v253, v9 row_newbcast:9 row_mask:0xf bank_mask:0xf\n"
    "v_add_f32_dpp v255, v253, v11 row_newbcast:11 row_mask:0xf bank_mask:0xf\n"
    "v_mfma_f32_16x16x32_bf16 v[48:51], a[224:227], a[132:135], v[48:51]\n"
    "v_mul_f32 v245, v245, v254\n"
    "v_mul_f32 v247, v247, v255\n"
    "v_mfma_f32_16x16x32_bf16 v[48:51], a[228:231], a[164:167], v[48:51]\n"
    "s_waitcnt lgkmcnt(1)\n"
    "s_xor_b32 s91, s91, 0x195800\n"
    "v_cvt_pk_bf16_f32 v248, v244, v245\n"
    "v_mfma_f32_16x16x32_bf16 v[16:19], a[228:231], a[160:163], v[16:19]\n"
    "v_cvt_pk_bf16_f32 v249, v246, v247\n"
    "v_accvgpr_read_b32 v245, a34\n"
    "v_mfma_f32_16x16x32_bf16 v[16:19], a[224:227], a[128:131], v[16:19]\n"
    "s_waitcnt vmcnt(12)\n"
    "v_accvgpr_read_b32 v247, a35\n"
    "v_lshlrev_b32 v244, 16, v245\n"
    "v_mfma_f32_16x16x32_bf16 v[20:23], a[232:235], a[128:131], v[20:23]\n"
    "buffer_load_dwordx4 a[224:227], v240, s[20:23], s53 offen offset:0 sc1\n"
    "v_mfma_f32_16x16x32_bf16 v[20:23], a[236:239], a[160:163], v[20:23]\n"
    "buffer_load_dwordx4 a[228:231], v240, s[20:23], s53 offen offset:1024 sc1\n"
    "v_mfma_f32_16x16x32_bf16 v[52:55], a[236:239], a[164:167], v[52:55]\n"
    "v_lshlrev_b32 v246, 16, v247\n"
    "v_and_b32 v245, s58, v245\n"
    "v_mfma_f32_16x16x32_bf16 v[52:55], a[232:235], a[132:135], v[52:55]\n"
    "v_and_b32 v247, s58, v247\n"
    "v_add_f32_dpp v254, v253, v12 row_newbcast:12 row_mask:0xf bank_mask:0xf\n"
    "v_mfma_f32_16x16x32_bf16 v[84:87], a[232:235], a[136:139], v[84:87]\n"
    "v_add_f32_dpp v255, v253, v14 row_newbcast:14 row_mask:0xf bank_mask:0xf\n"
    "v_mul_f32 v244, v244, v254\n"
    "v_mfma_f32_16x16x32_bf16 v[84:87], a[236:239], a[168:171], v[84:87]\n"
    "v_mul_f32 v246, v246, v255\n"
    "v_add_f32_dpp v254, v253, v13 row_newbcast:13 row_mask:0xf bank_mask:0xf\n"
    "v_mfma_f32_16x16x32_bf16 v[116:119], a[236:239], a[172:175], v[116:119]\n"
    "v_add_f32_dpp v255, v253, v15 row_newbcast:15 row_mask:0xf bank_mask:0xf\n"
    "v_mul_f32 v245, v245, v254\n"
    "v_mfma_f32_16x16x32_bf16 v[116:119], a[232:235], a[140:143], v[116:119]\n"
    "v_mul_f32 v247, v247, v255\n"
    "v_cvt_pk_bf16_f32 v250, v244, v245\n"
    "v_mfma_f32_16x16x32_bf16 v[148:151], a[232:235], a[144:147], v[148:151]\n"
    "v_cvt_pk_bf16_f32 v251, v246, v247\n"
    "v_accvgpr_read_b32 v245, a36\n"
    "v_mfma_f32_16x16x32_bf16 v[148:151], a[236:239], a[176:179], v[148:151]\n"
    "buffer_store_dwordx4 v[248:251], v240, s[32:35], s91 offen sc1\n"
    "ds_write_b128 v243, v[248:251] offset:32768\n"
    "v_accvgpr_read_b32 v247, a37\n"
    "v_lshlrev_b32 v244, 16, v245\n"
    "v_lshlrev_b32 v246, 16, v247\n"
    "v_and_b32 v245, s58, v245\n"
    "v_mfma_f32_16x16x32_bf16 v[180:183], a[236:239], a[180:183], v[180:183]\n"
    "s_xor_b32 s91, s91, 0x800\n"
    "v_and_b32 v247, s58, v247\n"
    "v_add_f32_dpp v254, v253, v40 row_newbcast:8 row_mask:0xf bank_mask:0xf\n"
    "v_add_f32_dpp v255, v253, v42 row_newbcast:10 row_mask:0xf bank_mask:0xf\n"
    "v_mul_f32 v244, v244, v254\n"
    "v_mfma_f32_16x16x32_bf16 v[180:183], a[232:235], a[148:151], v[180:183]\n"
    "v_mul_f32 v246, v246, v255\n"
    "v_add_f32_dpp v254, v253, v41 row_newbcast:9 row_mask:0xf bank_mask:0xf\n"
    "v_add_f32_dpp v255, v253, v43 row_newbcast:11 row_mask:0xf bank_mask:0xf\n"
    "v_mul_f32 v245, v245, v254\n"
    "v_mfma_f32_16x16x32_bf16 v[212:215], a[232:235], a[152:155], v[212:215]\n"
    "v_mul_f32 v247, v247, v255\n"
    "v_cvt_pk_bf16_f32 v248, v244, v245\n"
    "v_mfma_f32_16x16x32_bf16 v[212:215], a[236:239], a[184:187], v[212:215]\n"
    "v_cvt_pk_bf16_f32 v249, v246, v247\n"
    "v_accvgpr_read_b32 v245, a38\n"
    "v_accvgpr_read_b32 v247, a39\n"
    "v_lshlrev_b32 v244, 16, v245\n"
    "v_mfma_f32_16x16x32_bf16 a[244:247], a[236:239], a[188:191], a[244:247]\n"
    "v_lshlrev_b32 v246, 16, v247\n"
    "v_and_b32 v245, s58, v245\n"
    "v_and_b32 v247, s58, v247\n"
    "v_add_f32_dpp v254, v253, v44 row_newbcast:12 row_mask:0xf bank_mask:0xf\n"
    "v_mfma_f32_16x16x32_bf16 a[244:247], a[232:235], a[156:159], a[244:247]\n"
    "s_waitcnt vmcnt(12)\n"
    "v_add_f32_dpp v255, v253, v46 row_newbcast:14 row_mask:0xf bank_mask:0xf\n"
    "v_mul_f32 v244, v244, v254\n"
    "v_mul_f32 v246, v246, v255\n"
    "v_mfma_f32_16x16x32_bf16 a[248:251], a[192:195], a[156:159], a[248:251]\n"
    "v_add_f32_dpp v254, v253, v45 row_newbcast:13 row_mask:0xf bank_mask:0xf\n"
    "v_add_f32_dpp v255, v253, v47 row_newbcast:15 row_mask:0xf bank_mask:0xf\n"
    "v_mul_f32 v245, v245, v254\n"
    "v_mfma_f32_16x16x32_bf16 a[248:251], a[196:199], a[188:191], a[248:251]\n"
    "v_mul_f32 v247, v247, v255\n"
    "v_cvt_pk_bf16_f32 v250, v244, v245\n"
    "v_cvt_pk_bf16_f32 v251, v246, v247\n"
    "v_accvgpr_read_b32 v245, a40\n"
    "v_mfma_f32_16x16x32_bf16 v[216:219], a[196:199], a[184:187], v[216:219]\n"
    "buffer_store_dwordx4 v[248:251], v240, s[32:35], s91 offen sc1\n"
    "ds_write_b128 v243, v[248:251] offset:34816\n"
    "v_accvgpr_read_b32 v247, a41\n"
    "v_lshlrev_b32 v244, 16, v245\n"
    "v_lshlrev_b32 v246, 16, v247\n"
    "v_and_b32 v245, s58, v245\n"
    "v_mfma_f32_16x16x32_bf16 v[216:219], a[192:195], a[152:155], v[216:219]\n"
    "buffer_load_dwordx4 a[232:235], v240, s[20:23], s53 offen offset:2048 sc1\n"
    "v_and_b32 v247, s58, v247\n"
    "v_add_f32_dpp v254, v253, v72 row_newbcast:8 row_mask:0xf bank_mask:0xf\n"
    "v_add_f32_dpp v255, v253, v74 row_newbcast:10 row_mask:0xf bank_mask:0xf\n"
    "v_mul_f32 v244, v244, v254\n"
    "v_mfma_f32_16x16x32_bf16 v[184:187], a[192:195], a[148:151], v[184:187]\n"
    "buffer_load_dwordx4 a[236:239], v240, s[20:23], s53 offen offset:3072 sc1\n"
    "s_addk_i32 s53, 4096\n"
    "s_xor_b32 s91, s91, 0x101800\n"
    "v_mul_f32 v246, v246, v255\n"
    "v_add_f32_dpp v254, v253, v73 row_newbcast:9 row_mask:0xf bank_mask:0xf\n"
    "v_add_f32_dpp v255, v253, v75 row_newbcast:11 row_mask:0xf bank_mask:0xf\n"
    "v_mul_f32 v245, v245, v254\n"
    "v_mfma_f32_16x16x32_bf16 v[184:187], a[196:199], a[180:183], v[184:187]\n"
    "v_mul_f32 v247, v247, v255\n"
    "v_cvt_pk_bf16_f32 v248, v244, v245\n"
    "v_mfma_f32_16x16x32_bf16 v[152:155], a[196:199], a[176:179], v[152:155]\n"
    "v_cvt_pk_bf16_f32 v249, v246, v247\n"
    "v_accvgpr_read_b32 v245, a42\n"
    "v_accvgpr_read_b32 v247, a43\n"
    "v_lshlrev_b32 v244, 16, v245\n"
    "v_mfma_f32_16x16x32_bf16 v[152:155], a[192:195], a[144:147], v[152:155]\n"
    "v_lshlrev_b32 v246, 16, v247\n"
    "v_and_b32 v245, s58, v245\n"
    "v_and_b32 v247, s58, v247\n"
    "v_add_f32_dpp v254, v253, v76 row_newbcast:12 row_mask:0xf bank_mask:0xf\n"
    "v_mfma_f32_16x16x32_bf16 v[120:123], a[192:195], a[140:143], v[120:123]\n"
    "v_add_f32_dpp v255, v253, v78 row_newbcast:14 row_mask:0xf bank_mask:0xf\n"
    "v_mul_f32 v244, v244, v254\n"
    "v_mul_f32 v246, v246, v255\n"
    "v_mfma_f32_16x16x32_bf16 v[120:123], a[196:199], a[172:175], v[120:123]\n"
    "v_add_f32_dpp v254, v253, v77 row_newbcast:13 row_mask:0xf bank_mask:0xf\n"
    "v_add_f32_dpp v255, v253, v79 row_newbcast:15 row_mask:0xf bank_mask:0xf\n"
    "v_mul_f32 v245, v245, v254\n"
    "v_mfma_f32_16x16x32_bf16 v[88:91], a[196:199], a[168:171], v[88:91]\n"
    "v_mul_f32 v247, v247, v255\n"
    "v_cvt_pk_bf16_f32 v250, v244, v245\n"
    "v_cvt_pk_bf16_f32 v251, v246, v247\n"
    "v_accvgpr_read_b32 v245, a44\n"
    "v_mfma_f32_16x16x32_bf16 v[88:91], a[192:195], a[136:139], v[88:91]\n"
    "buffer_store_dwordx4 v[248:251], v240, s[32:35], s91 offen sc1\n"
    "ds_write_b128 v243, v[248:251] offset:36864\n"
    "v_accvgpr_read_b32 v247, a45\n"
    "v_lshlrev_b32 v244, 16, v245\n"
    "v_lshlrev_b32 v246, 16, v247\n"
    "v_and_b32 v245, s58, v245\n"
    "v_mfma_f32_16x16x32_bf16 v[56:59], a[192:195], a[132:135], v[56:59]\n"
    "s_xor_b32 s91, s91, 0x800\n"
    "v_and_b32 v247, s58, v247\n"
    "v_add_f32_dpp v254, v253, v104 row_newbcast:8 row_mask:0xf bank_mask:0xf\n"
    "v_add_f32_dpp v255, v253, v106 row_newbcast:10 row_mask:0xf bank_mask:0xf\n"
    "v_mul_f32 v244, v244, v254\n"
    "v_mfma_f32_16x16x32_bf16 v[56:59], a[196:199], a[164:167], v[56:59]\n"
    "v_mul_f32 v246, v246, v255\n"
    "v_add_f32_dpp v254, v253, v105 row_newbcast:9 row_mask:0xf bank_mask:0xf\n"
    "v_add_f32_dpp v255, v253, v107 row_newbcast:11 row_mask:0xf bank_mask:0xf\n"
    "v_mfma_f32_16x16x32_bf16 v[24:27], a[196:199], a[160:163], v[24:27]\n"
    "v_mul_f32 v245, v245, v254\n"
    "v_mul_f32 v247, v247, v255\n"
    "v_cvt_pk_bf16_f32 v248, v244, v245\n"
    "v_mfma_f32_16x16x32_bf16 v[24:27], a[192:195], a[128:131], v[24:27]\n"
    "s_waitcnt vmcnt(14)\n"
    "v_cvt_pk_bf16_f32 v249, v246, v247\n"
    "v_accvgpr_read_b32 v245, a46\n"
    "v_accvgpr_read_b32 v247, a47\n"
    "v_lshlrev_b32 v244, 16, v245\n"
    "v_mfma_f32_16x16x32_bf16 v[28:31], a[200:203], a[128:131], v[28:31]\n"
    "v_lshlrev_b32 v246, 16, v247\n"
    "v_and_b32 v245, s58, v245\n"
    "v_and_b32 v247, s58, v247\n"
    "v_add_f32_dpp v254, v253, v108 row_newbcast:12 row_mask:0xf bank_mask:0xf\n"
    "v_mfma_f32_16x16x32_bf16 v[28:31], a[204:207], a[160:163], v[28:31]\n"
    "v_add_f32_dpp v255, v253, v110 row_newbcast:14 row_mask:0xf bank_mask:0xf\n"
    "v_mul_f32 v244, v244, v254\n"
    "v_mul_f32 v246, v246, v255\n"
    "v_mfma_f32_16x16x32_bf16 v[60:63], a[204:207], a[164:167], v[60:63]\n"
    "v_add_f32_dpp v254, v253, v109 row_newbcast:13 row_mask:0xf bank_mask:0xf\n"
    "v_add_f32_dpp v255, v253, v111 row_newbcast:15 row_mask:0xf bank_mask:0xf\n"
    "v_mul_f32 v245, v245, v254\n"
    "v_mfma_f32_16x16x32_bf16 v[60:63], a[200:203], a[132:135], v[60:63]\n"
    "v_mul_f32 v247, v247, v255\n"
    "v_cvt_pk_bf16_f32 v250, v244, v245\n"
    "v_cvt_pk_bf16_f32 v251, v246, v247\n"
    "v_accvgpr_read_b32 v245, a48\n"
    "v_mfma_f32_16x16x32_bf16 v[92:95], a[200:203], a[136:139], v[92:95]\n"
    "buffer_store_dwordx4 v[248:251], v240, s[32:35], s91 offen sc1\n"
    "ds_write_b128 v243, v[248:251] offset:38912\n"
    "v_accvgpr_read_b32 v247, a49\n"
    "v_lshlrev_b32 v244, 16, v245\n"
    "v_lshlrev_b32 v246, 16, v247\n"
    "v_and_b32 v245, s58, v245\n"
    "v_mfma_f32_16x16x32_bf16 v[92:95], a[204:207], a[168:171], v[92:95]\n"
    "buffer_load_dwordx4 a[192:195], v240, s[20:23], s53 offen offset:0 sc1\n"
    "v_and_b32 v247, s58, v247\n"
    "v_add_f32_dpp v254, v253, v136 row_newbcast:8 row_mask:0xf bank_mask:0xf\n"
    "v_add_f32_dpp v255, v253, v138 row_newbcast:10 row_mask:0xf bank_mask:0xf\n"
    "v_mul_f32 v244, v244, v254\n"
    "v_mfma_f32_16x16x32_bf16 v[124:127], a[204:207], a[172:175], v[124:127]\n"
    "buffer_load_dwordx4 a[196:199], v240, s[20:23], s53 offen offset:1024 sc1\n"
    "ds_read_b128 a[128:131], v241 offset:0\n"
    "ds_read_b128 a[160:163], v242 offset:0\n"
    "v_mul_f32 v246, v246, v255\n"
    "v_add_f32_dpp v254, v253, v137 row_newbcast:9 row_mask:0xf bank_mask:0xf\n"
    "v_add_f32_dpp v255, v253, v139 row_newbcast:11 row_mask:0xf bank_mask:0xf\n"
    "v_mfma_f32_16x16x32_bf16 v[124:127], a[200:203], a[140:143], v[124:127]\n"
    "ds_read_b128 a[164:167], v242 offset:2048\n"
    "ds_read_b128 a[132:135], v241 offset:2048\n"
    "v_mul_f32 v245, v245, v254\n"
    "v_mul_f32 v247, v247, v255\n"
    "v_cvt_pk_bf16_f32 v248, v244, v245\n"
    "v_mfma_f32_16x16x32_bf16 v[156:159], a[200:203], a[144:147], v[156:159]\n"
    "ds_read_b128 a[136:139], v241 offset:4096\n"
    "ds_read_b128 a[168:171], v242 offset:4096\n"
    "v_mfma_f32_16x16x32_bf16 v[156:159], a[204:207], a[176:179], v[156:159]\n"
    "ds_read_b128 a[172:175], v242 offset:6144\n"
    "ds_read_b128 a[140:143], v241 offset:6144\n"
    "v_mfma_f32_16x16x32_bf16 v[188:191], a[204:207], a[180:183], v[188:191]\n"
    "ds_read_b128 a[144:147], v241 offset:8192\n"
    "v_cvt_pk_bf16_f32 v249, v246, v247\n"
    "v_mfma_f32_16x16x32_bf16 v[188:191], a[200:203], a[148:151], v[188:191]\n"
    "ds_read_b128 a[176:179], v242 offset:8192\n"
    "v_accvgpr_read_b32 v245, a50\n"
    "v_mfma_f32_16x16x32_bf16 v[220:223], a[200:203], a[152:155], v[220:223]\n"
    "ds_read_b128 a[180:183], v242 offset:10240\n"
    "v_accvgpr_read_b32 v247, a51\n"
    "v_mfma_f32_16x16x32_bf16 v[220:223], a[204:207], a[184:187], v[220:223]\n"
    "ds_read_b128 a[148:151], v241 offset:10240\n"
    "v_lshlrev_b32 v244, 16, v245\n"
    "v_mfma_f32_16x16x32_bf16 a[252:255], a[204:207], a[188:191], a[252:255]\n"
    "ds_read_b128 a[152:155], v241 offset:12288\n"
    "v_lshlrev_b32 v246, 16, v247\n"
    "v_mfma_f32_16x16x32_bf16 a[252:255], a[200:203], a[156:159], a[252:255]\n"
    "s_waitcnt vmcnt(13) lgkmcnt(11)\n"
    "ds_read_b128 a[184:187], v242 offset:12288\n"
    "ds_read_b128 a[188:191], v242 offset:14336\n"
    "v_mfma_f32_16x16x32_bf16 v[16:19], a[208:211], a[128:131], v[16:19]\n"
    "ds_read_b128 a[156:159], v241 offset:14336\n"
    "v_and_b32 v245, s58, v245\n"
    "v_mfma_f32_16x16x32_bf16 v[16:19], a[212:215], a[160:163], v[16:19]\n"
    "s_waitcnt lgkmcnt(12)\n"
    "v_and_b32 v247, s58, v247\n"
    "v_add_f32_dpp v254, v253, v140 row_newbcast:12 row_mask:0xf bank_mask:0xf\n"
    "v_mfma_f32_16x16x32_bf16 v[48:51], a[212:215], a[164:167], v[48:51]\n"
    "buffer_load_dwordx4 a[200:203], v240, s[20:23], s53 offen offset:2048 sc1\n"
    "v_mfma_f32_16x16x32_bf16 v[48:51], a[208:211], a[132:135], v[48:51]\n"
    "s_waitcnt lgkmcnt(10)\n"
    "v_add_f32_dpp v255, v253, v142 row_newbcast:14 row_mask:0xf bank_mask:0xf\n"
    "v_mul_f32 v244, v244, v254\n"
    "v_mfma_f32_16x16x32_bf16 v[80:83], a[208:211], a[136:139], v[80:83]\n"
    "buffer_load_dwordx4 a[204:207], v240, s[20:23], s53 offen offset:3072 sc1\n"
    "s_addk_i32 s53, 4096\n"
    "v_mfma_f32_16x16x32_bf16 v[80:83], a[212:215], a[168:171], v[80:83]\n"
    "s_waitcnt lgkmcnt(8)\n"
    "s_xor_b32 s91, s91, 0x185800\n"
    "v_mul_f32 v246, v246, v255\n"
    "v_mfma_f32_16x16x32_bf16 v[112:115], a[212:215], a[172:175], v[112:115]\n"
    "v_add_f32_dpp v254, v253, v141 row_newbcast:13 row_mask:0xf bank_mask:0xf\n"
    "v_add_f32_dpp v255, v253, v143 row_newbcast:15 row_mask:0xf bank_mask:0xf\n"
    "v_mfma_f32_16x16x32_bf16 v[112:115], a[208:211], a[140:143], v[112:115]\n"
    "s_waitcnt lgkmcnt(6)\n"
    "v_mul_f32 v245, v245, v254\n"
    "v_mul_f32 v247, v247, v255\n"
    "v_mfma_f32_16x16x32_bf16 v[144:147], a[208:211], a[144:147], v[144:147]\n"
    "v_cvt_pk_bf16_f32 v250, v244, v245\n"
    "v_cvt_pk_bf16_f32 v251, v246, v247\n"
    "v_mfma_f32_16x16x32_bf16 v[144:147], a[212:215], a[176:179], v[144:147]\n"
    "s_waitcnt lgkmcnt(4)\n"
    "v_accvgpr_read_b32 v245, a52\n"
    "v_accvgpr_read_b32 v247, a53\n"
    "v_mfma_f32_16x16x32_bf16 v[176:179], a[212:215], a[180:183], v[176:179]\n"
    "v_lshlrev_b32 v244, 16, v245\n"
    "v_lshlrev_b32 v246, 16, v247\n"
    "v_mfma_f32_16x16x32_bf16 v[176:179], a[208:211], a[148:151], v[176:179]\n"
    "s_waitcnt lgkmcnt(2)\n"
    "v_and_b32 v245, s58, v245\n"
    "v_and_b32 v247, s58, v247\n"
    "v_mfma_f32_16x16x32_bf16 v[208:211], a[208:211], a[152:155], v[208:211]\n"
    "buffer_store_dwordx4 v[248:251], v240, s[32:35], s91 offen sc1\n"
    "ds_write_b128 v243, v[248:251] offset:40960\n"
    "v_mfma_f32_16x16x32_bf16 v[208:211], a[212:215], a[184:187], v[208:211]\n"
    "s_waitcnt lgkmcnt(1)\n"
    "v_add_f32_dpp v254, v253, v168 row_newbcast:8 row_mask:0xf bank_mask:0xf\n"
    "v_add_f32_dpp v255, v253, v170 row_newbcast:10 row_mask:0xf bank_mask:0xf\n"
    "v_mfma_f32_16x16x32_bf16 a[240:243], a[212:215], a[188:191], a[240:243]\n"
    "v_mul_f32 v244, v244, v254\n"
    "v_mul_f32 v246, v246, v255\n"
    "v_mfma_f32_16x16x32_bf16 a[240:243], a[208:211], a[156:159], a[240:243]\n"
    "s_waitcnt vmcnt(14)\n"
    "v_add_f32_dpp v254, v253, v169 row_newbcast:9 row_mask:0xf bank_mask:0xf\n"
    "v_add_f32_dpp v255, v253, v171 row_newbcast:11 row_mask:0xf bank_mask:0xf\n"
    "v_mfma_f32_16x16x32_bf16 a[244:247], a[216:219], a[156:159], a[244:247]\n"
    "buffer_load_dwordx4 a[208:211], v240, s[20:23], s53 offen offset:0 sc1\n"
    "v_mfma_f32_16x16x32_bf16 a[244:247], a[220:223], a[188:191], a[244:247]\n"
    "buffer_load_dwordx4 a[212:215], v240, s[20:23], s53 offen offset:1024 sc1\n"
    "v_mfma_f32_16x16x32_bf16 v[212:215], a[220:223], a[184:187], v[212:215]\n"
    "s_xor_b32 s91, s91, 0x800\n"
    "v_mul_f32 v245, v245, v254\n"
    "v_mul_f32 v247, v247, v255\n"
    "v_mfma_f32_16x16x32_bf16 v[212:215], a[216:219], a[152:155], v[212:215]\n"
    "v_cvt_pk_bf16_f32 v248, v244, v245\n"
    "v_cvt_pk_bf16_f32 v249, v246, v247\n"
    "v_mfma_f32_16x16x32_bf16 v[180:183], a[216:219], a[148:151], v[180:183]\n"
    "v_accvgpr_read_b32 v245, a54\n"
    "v_accvgpr_read_b32 v247, a55\n"
    "v_mfma_f32_16x16x32_bf16 v[180:183], a[220:223], a[180:183], v[180:183]\n"
    "v_lshlrev_b32 v244, 16, v245\n"
    "v_lshlrev_b32 v246, 16, v247\n"
    "v_mfma_f32_16x16x32_bf16 v[148:151], a[220:223], a[176:179], v[148:151]\n"
    "v_and_b32 v245, s58, v245\n"
    "v_and_b32 v247, s58, v247\n"
    "v_mfma_f32_16x16x32_bf16 v[148:151], a[216:219], a[144:147], v[148:151]\n"
    "v_add_f32_dpp v254, v253, v172 row_newbcast:12 row_mask:0xf bank_mask:0xf\n"
    "v_add_f32_dpp v255, v253, v174 row_newbcast:14 row_mask:0xf bank_mask:0xf\n"
    "v_mfma_f32_16x16x32_bf16 v[116:119], a[216:219], a[140:143], v[116:119]\n"
    "v_mul_f32 v244, v244, v254\n"
    "v_mul_f32 v246, v246, v255\n"
    "v_mfma_f32_16x16x32_bf16 v[116:119], a[220:223], a[172:175], v[116:119]\n"
    "v_add_f32_dpp v254, v253, v173 row_newbcast:13 row_mask:0xf bank_mask:0xf\n"
    "v_add_f32_dpp v255, v253, v175 row_newbcast:15 row_mask:0xf bank_mask:0xf\n"
    "v_mul_f32 v245, v245, v254\n"
    "v_mul_f32 v247, v247, v255\n"
    "v_mfma_f32_16x16x32_bf16 v[84:87], a[220:223], a[168:171], v[84:87]\n"
    "v_cvt_pk_bf16_f32 v250, v244, v245\n"
    "v_cvt_pk_bf16_f32 v251, v246, v247\n"
    "v_accvgpr_read_b32 v245, a56\n"
    "v_accvgpr_read_b32 v247, a57\n"
    "v_mfma_f32_16x16x32_bf16 v[84:87], a[216:219], a[136:139], v[84:87]\n"
    "buffer_store_dwordx4 v[248:251], v240, s[32:35], s91 offen sc1\n"
    "ds_write_b128 v243, v[248:251] offset:43008\n"
    "v_lshlrev_b32 v244, 16, v245\n"
    "v_lshlrev_b32 v246, 16, v247\n"
    "v_and_b32 v245, s58, v245\n"
    "v_mfma_f32_16x16x32_bf16 v[52:55], a[216:219], a[132:135], v[52:55]\n"
    "s_xor_b32 s91, s91, 0x101800\n"
    "v_and_b32 v247, s58, v247\n"
    "v_add_f32_dpp v254, v253, v200 row_newbcast:8 row_mask:0xf bank_mask:0xf\n"
    "v_add_f32_dpp v255, v253, v202 row_newbcast:10 row_mask:0xf bank_mask:0xf\n"
    "v_mul_f32 v244, v244, v254\n"
    "v_mfma_f32_16x16x32_bf16 v[52:55], a[220:223], a[164:167], v[52:55]\n"
    "v_mul_f32 v246, v246, v255\n"
    "v_add_f32_dpp v254, v253, v201 row_newbcast:9 row_mask:0xf bank_mask:0xf\n"
    "v_add_f32_dpp v255, v253, v203 row_newbcast:11 row_mask:0xf bank_mask:0xf\n"
    "v_mul_f32 v245, v245, v254\n"
    "v_mfma_f32_16x16x32_bf16 v[20:23], a[220:223], a[160:163], v[20:23]\n"
    "v_mul_f32 v247, v247, v255\n"
    "v_cvt_pk_bf16_f32 v248, v244, v245\n"
    "v_cvt_pk_bf16_f32 v249, v246, v247\n"
    "v_mfma_f32_16x16x32_bf16 v[20:23], a[216:219], a[128:131], v[20:23]\n"
    "s_waitcnt vmcnt(14)\n"
    "v_accvgpr_read_b32 v245, a58\n"
    "v_accvgpr_read_b32 v247, a59\n"
    "v_lshlrev_b32 v244, 16, v245\n"
    "v_lshlrev_b32 v246, 16, v247\n"
    "v_mfma_f32_16x16x32_bf16 v[24:27], a[224:227], a[128:131], v[24:27]\n"
    "v_and_b32 v245, s58, v245\n"
    "v_and_b32 v247, s58, v247\n"
    "v_add_f32_dpp v254, v253, v204 row_newbcast:12 row_mask:0xf bank_mask:0xf\n"
    "v_mfma_f32_16x16x32_bf16 v[24:27], a[228:231], a[160:163], v[24:27]\n"
    "v_add_f32_dpp v255, v253, v206 row_newbcast:14 row_mask:0xf bank_mask:0xf\n"
    "v_mul_f32 v244, v244, v254\n"
    "v_mul_f32 v246, v246, v255\n"
    "v_mfma_f32_16x16x32_bf16 v[56:59], a[228:231], a[164:167], v[56:59]\n"
    "v_add_f32_dpp v254, v253, v205 row_newbcast:13 row_mask:0xf bank_mask:0xf\n"
    "v_add_f32_dpp v255, v253, v207 row_newbcast:15 row_mask:0xf bank_mask:0xf\n"
    "v_mul_f32 v245, v245, v254\n"
    "v_mul_f32 v247, v247, v255\n"
    "v_mfma_f32_16x16x32_bf16 v[56:59], a[224:227], a[132:135], v[56:59]\n"
    "v_cvt_pk_bf16_f32 v250, v244, v245\n"
    "v_cvt_pk_bf16_f32 v251, v246, v247\n"
    "v_accvgpr_read_b32 v245, a60\n"
    "v_accvgpr_read_b32 v247, a61\n"
    "v_mfma_f32_16x16x32_bf16 v[88:91], a[224:227], a[136:139], v[88:91]\n"
    "buffer_store_dwordx4 v[248:251], v240, s[32:35], s91 offen sc1\n"
    "ds_write_b128 v243, v[248:251] offset:45056\n"
    "v_lshlrev_b32 v244, 16, v245\n"
    "v_lshlrev_b32 v246, 16, v247\n"
    "v_and_b32 v245, s58, v245\n"
    "v_mfma_f32_16x16x32_bf16 v[88:91], a[228:231], a[168:171], v[88:91]\n"
    "buffer_load_dwordx4 a[216:219], v240, s[20:23], s53 offen offset:2048 sc1\n"
    "v_and_b32 v247, s58, v247\n"
    "v_add_f32_dpp v254, v253, v232 row_newbcast:8 row_mask:0xf bank_mask:0xf\n"
    "v_add_f32_dpp v255, v253, v234 row_newbcast:10 row_mask:0xf bank_mask:0xf\n"
    "v_mul_f32 v244, v244, v254\n"
    "v_mfma_f32_16x16x32_bf16 v[120:123], a[228:231], a[172:175], v[120:123]\n"
    "buffer_load_dwordx4 a[220:223], v240, s[20:23], s53 offen offset:3072 sc1\n"
    "s_addk_i32 s53, 4096\n"
    "s_xor_b32 s91, s91, 0x800\n"
    "v_mul_f32 v246, v246, v255\n"
    "v_add_f32_dpp v254, v253, v233 row_newbcast:9 row_mask:0xf bank_mask:0xf\n"
    "v_add_f32_dpp v255, v253, v235 row_newbcast:11 row_mask:0xf bank_mask:0xf\n"
    "v_mul_f32 v245, v245, v254\n"
    "v_mfma_f32_16x16x32_bf16 v[120:123], a[224:227], a[140:143], v[120:123]\n"
    "v_mul_f32 v247, v247, v255\n"
    "v_cvt_pk_bf16_f32 v248, v244, v245\n"
    "v_cvt_pk_bf16_f32 v249, v246, v247\n"
    "v_mfma_f32_16x16x32_bf16 v[152:155], a[224:227], a[144:147], v[152:155]\n"
    "v_accvgpr_read_b32 v245, a62\n"
    "v_accvgpr_read_b32 v247, a63\n"
    "v_lshlrev_b32 v244, 16, v245\n"
    "v_mfma_f32_16x16x32_bf16 v[152:155], a[228:231], a[176:179], v[152:155]\n"
    "v_lshlrev_b32 v246, 16, v247\n"
    "v_and_b32 v245, s58, v245\n"
    "v_and_b32 v247, s58, v247\n"
    "v_add_f32_dpp v254, v253, v236 row_newbcast:12 row_mask:0xf bank_mask:0xf\n"
    "v_mfma_f32_16x16x32_bf16 v[184:187], a[228:231], a[180:183], v[184:187]\n"
    "v_add_f32_dpp v255, v253, v238 row_newbcast:14 row_mask:0xf bank_mask:0xf\n"
    "v_mul_f32 v244, v244, v254\n"
    "v_mul_f32 v246, v246, v255\n"
    "v_mfma_f32_16x16x32_bf16 v[184:187], a[224:227], a[148:151], v[184:187]\n"
    "v_add_f32_dpp v254, v253, v237 row_newbcast:13 row_mask:0xf bank_mask:0xf\n"
    "v_add_f32_dpp v255, v253, v239 row_newbcast:15 row_mask:0xf bank_mask:0xf\n"
    "v_mul_f32 v245, v245, v254\n"
    "v_mul_f32 v247, v247, v255\n"
    "v_mfma_f32_16x16x32_bf16 v[216:219], a[224:227], a[152:155], v[216:219]\n"
    "v_cvt_pk_bf16_f32 v250, v244, v245\n"
    "v_cvt_pk_bf16_f32 v251, v246, v247\n"
    "v_mfma_f32_16x16x32_bf16 v[216:219], a[228:231], a[184:187], v[216:219]\n"
    "buffer_store_dwordx4 v[248:251], v240, s[32:35], s91 offen sc1\n"
    "ds_write_b128 v243, v[248:251] offset:47104\n"
    "s_waitcnt lgkmcnt(0)\n"
    "s_barrier\n"
    "v_mfma_f32_16x16x32_bf16 a[248:251], a[228:231], a[188:191], a[248:251]\n"
    "v_mfma_f32_16x16x32_bf16 a[248:251], a[224:227], a[156:159], a[248:251]\n"
    "s_waitcnt vmcnt(14)\n"
    "v_mfma_f32_16x16x32_bf16 a[252:255], a[232:235], a[156:159], a[252:255]\n"
    "buffer_load_dwordx4 a[224:227], v240, s[20:23], s53 offen offset:0 sc1\n"
    "v_mfma_f32_16x16x32_bf16 a[252:255], a[236:239], a[188:191], a[252:255]\n"
    "buffer_load_dwordx4 a[228:231], v240, s[20:23], s53 offen offset:1024 sc1\n"
    "v_mfma_f32_16x16x32_bf16 v[220:223], a[236:239], a[184:187], v[220:223]\n"
    "v_add_u32 v241, s66, v240\n"
    "ds_read_b128 a[156:159], v241 offset:14336\n"
    "v_mfma_f32_16x16x32_bf16 v[220:223], a[232:235], a[152:155], v[220:223]\n"
    "ds_read_b128 a[188:191], v241 offset:15360\n"
    "v_mfma_f32_16x16x32_bf16 v[188:191], a[232:235], a[148:151], v[188:191]\n"
    "ds_read_b128 a[184:187], v241 offset:13312\n"
    "v_mfma_f32_16x16x32_bf16 v[188:191], a[236:239], a[180:183], v[188:191]\n"
    "ds_read_b128 a[152:155], v241 offset:12288\n"
    "v_mfma_f32_16x16x32_bf16 v[156:159], a[236:239], a[176:179], v[156:159]\n"
    "ds_read_b128 a[148:151], v241 offset:10240\n"
    "v_mfma_f32_16x16x32_bf16 v[156:159], a[232:235], a[144:147], v[156:159]\n"
    "ds_read_b128 a[180:183], v241 offset:11264\n"
    "v_mfma_f32_16x16x32_bf16 v[124:127], a[232:235], a[140:143], v[124:127]\n"
    "ds_read_b128 a[176:179], v241 offset:9216\n"
    "v_mfma_f32_16x16x32_bf16 v[124:127], a[236:239], a[172:175], v[124:127]\n"
    "ds_read_b128 a[144:147], v241 offset:8192\n"
    "v_mfma_f32_16x16x32_bf16 v[92:95], a[236:239], a[168:171], v[92:95]\n"
    "ds_read_b128 a[140:143], v241 offset:6144\n"
    "v_mfma_f32_16x16x32_bf16 v[92:95], a[232:235], a[136:139], v[92:95]\n"
    "ds_read_b128 a[172:175], v241 offset:7168\n"
    "v_mfma_f32_16x16x32_bf16 v[60:63], a[232:235], a[132:135], v[60:63]\n"
    "ds_read_b128 a[168:171], v241 offset:5120\n"
    "v_mfma_f32_16x16x32_bf16 v[60:63], a[236:239], a[164:167], v[60:63]\n"
    "ds_read_b128 a[136:139], v241 offset:4096\n"
    "v_mfma_f32_16x16x32_bf16 v[28:31], a[236:239], a[160:163], v[28:31]\n"
    "ds_read_b128 a[132:135], v241 offset:2048\n"
    "v_mfma_f32_16x16x32_bf16 v[28:31], a[232:235], a[128:131], v[28:31]\n"
    "ds_read_b128 a[164:167], v241 offset:3072\n"
    "ds_read_b128 a[160:163], v241 offset:1024\n"
    "ds_read_b128 a[128:131], v241 offset:0\n"
    "buffer_load_dwordx4 a[232:235], v240, s[20:23], s53 offen offset:2048 sc1\n"
    "buffer_load_dwordx4 a[236:239], v240, s[20:23], s53 offen offset:3072 sc1\n"
    "s_addk_i32 s53, 4096\n"
    "LOOP_%=:\n"
    "s_waitcnt vmcnt(10) lgkmcnt(14)\n"
    "v_mfma_f32_16x16x32_bf16 v[224:227], a[192:195], a[156:159], v[224:227]\n"
    "s_add_u32 s52, s52, 2\n"
    "s_xor_b32 s54, s54, 0x40000\n"
    "s_xor_b32 s59, s59, 0x40000\n"
    "v_mfma_f32_16x16x32_bf16 v[224:227], a[196:199], a[188:191], v[224:227]\n"
    "s_waitcnt lgkmcnt(12)\n"
    "s_addk_i32 s55, 2048\n"
    "s_xor_b32 s57, s57, 65536\n"
    "v_mfma_f32_16x16x32_bf16 v[192:195], a[196:199], a[184:187], v[192:195]\n"
    "s_xor_b32 s66, s66, 65536\n"
    "s_xor_b32 s71, s71, 65536\n"
    "v_xor_b32 v243, 0x10000, v243\n"
    "v_mfma_f32_16x16x32_bf16 v[192:195], a[192:195], a[152:155], v[192:195]\n"
    "s_waitcnt lgkmcnt(10)\n"
    "v_accvgpr_read_b32 v245, a64\n"
    "v_accvgpr_read_b32 v247, a65\n"
    "v_mfma_f32_16x16x32_bf16 v[160:163], a[192:195], a[148:151], v[160:163]\n"
    "v_lshlrev_b32 v244, 16, v245\n"
    "v_lshlrev_b32 v246, 16, v247\n"
    "v_mfma_f32_16x16x32_bf16 v[160:163], a[196:199], a[180:183], v[160:163]\n"
    "s_waitcnt lgkmcnt(8)\n"
    "v_and_b32 v245, s58, v245\n"
    "v_and_b32 v247, s58, v247\n"
    "v_mfma_f32_16x16x32_bf16 v[128:131], a[196:199], a[176:179], v[128:131]\n"
    "v_add_f32_dpp v254, v252, v16 row_newbcast:0 row_mask:0xf bank_mask:0xf\n"
    "v_add_f32_dpp v255, v252, v18 row_newbcast:2 row_mask:0xf bank_mask:0xf\n"
    "v_mfma_f32_16x16x32_bf16 v[128:131], a[192:195], a[144:147], v[128:131]\n"
    "s_waitcnt lgkmcnt(6)\n"
    "v_mul_f32 v244, v244, v254\n"
    "v_mul_f32 v246, v246, v255\n"
    "v_mfma_f32_16x16x32_bf16 v[96:99], a[192:195], a[140:143], v[96:99]\n"
    "v_add_f32_dpp v254, v252, v17 row_newbcast:1 row_mask:0xf bank_mask:0xf\n"
    "v_add_f32_dpp v255, v252, v19 row_newbcast:3 row_mask:0xf bank_mask:0xf\n"
    "v_mfma_f32_16x16x32_bf16 v[96:99], a[196:199], a[172:175], v[96:99]\n"
    "s_waitcnt lgkmcnt(4)\n"
    "v_mul_f32 v245, v245, v254\n"
    "v_mul_f32 v247, v247, v255\n"
    "v_mfma_f32_16x16x32_bf16 v[64:67], a[196:199], a[168:171], v[64:67]\n"
    "v_cvt_pk_bf16_f32 v248, v244, v245\n"
    "v_cvt_pk_bf16_f32 v249, v246, v247\n"
    "v_mfma_f32_16x16x32_bf16 v[64:67], a[192:195], a[136:139], v[64:67]\n"
    "s_waitcnt lgkmcnt(2)\n"
    "v_accvgpr_read_b32 v245, a66\n"
    "v_accvgpr_read_b32 v247, a67\n"
    "v_mfma_f32_16x16x32_bf16 v[32:35], a[192:195], a[132:135], v[32:35]\n"
    "v_lshlrev_b32 v244, 16, v245\n"
    "v_lshlrev_b32 v246, 16, v247\n"
    "v_mfma_f32_16x16x32_bf16 v[32:35], a[196:199], a[164:167], v[32:35]\n"
    "s_waitcnt lgkmcnt(0)\n"
    "v_and_b32 v245, s58, v245\n"
    "v_and_b32 v247, s58, v247\n"
    "v_mfma_f32_16x16x32_bf16 v[0:3], a[196:199], a[160:163], v[0:3]\n"
    "s_xor_b32 s91, s54, 0x20000\n"
    "v_add_f32_dpp v254, v252, v20 row_newbcast:4 row_mask:0xf bank_mask:0xf\n"
    "v_add_f32_dpp v255, v252, v22 row_newbcast:6 row_mask:0xf bank_mask:0xf\n"
    "v_mfma_f32_16x16x32_bf16 v[0:3], a[192:195], a[128:131], v[0:3]\n"
    "s_waitcnt vmcnt(8)\n"
    "v_mul_f32 v244, v244, v254\n"
    "v_mul_f32 v246, v246, v255\n"
    "v_mfma_f32_16x16x32_bf16 v[4:7], a[200:203], a[128:131], v[4:7]\n"
    "buffer_load_dwordx4 a[192:195], v240, s[20:23], s53 offen offset:0 sc1\n"
    "v_mfma_f32_16x16x32_bf16 v[4:7], a[204:207], a[160:163], v[4:7]\n"
    "buffer_load_dwordx4 a[196:199], v240, s[20:23], s53 offen offset:1024 sc1\n"
    "v_mfma_f32_16x16x32_bf16 v[36:39], a[204:207], a[164:167], v[36:39]\n"
    "v_add_f32_dpp v254, v252, v21 row_newbcast:5 row_mask:0xf bank_mask:0xf\n"
    "v_add_f32_dpp v255, v252, v23 row_newbcast:7 row_mask:0xf bank_mask:0xf\n"
    "v_mfma_f32_16x16x32_bf16 v[36:39], a[200:203], a[132:135], v[36:39]\n"
    "v_mul_f32 v245, v245, v254\n"
    "v_mul_f32 v247, v247, v255\n"
    "v_mfma_f32_16x16x32_bf16 v[68:71], a[200:203], a[136:139], v[68:71]\n"
    "v_cvt_pk_bf16_f32 v250, v244, v245\n"
    "v_cvt_pk_bf16_f32 v251, v246, v247\n"
    "v_mfma_f32_16x16x32_bf16 v[68:71], a[204:207], a[168:171], v[68:71]\n"
    "v_accvgpr_read_b32 v245, a68\n"
    "v_accvgpr_read_b32 v247, a69\n"
    "v_mfma_f32_16x16x32_bf16 v[100:103], a[204:207], a[172:175], v[100:103]\n"
    "v_lshlrev_b32 v244, 16, v245\n"
    "v_lshlrev_b32 v246, 16, v247\n"
    "v_mfma_f32_16x16x32_bf16 v[100:103], a[200:203], a[140:143], v[100:103]\n"
    "v_and_b32 v245, s58, v245\n"
    "v_and_b32 v247, s58, v247\n"
    "v_mfma_f32_16x16x32_bf16 v[132:135], a[200:203], a[144:147], v[132:135]\n"
    "v_add_f32_dpp v254, v252, v48 row_newbcast:0 row_mask:0xf bank_mask:0xf\n"
    "v_add_f32_dpp v255, v252, v50 row_newbcast:2 row_mask:0xf bank_mask:0xf\n"
    "v_mfma_f32_16x16x32_bf16 v[132:135], a[204:207], a[176:179], v[132:135]\n"
    "v_mul_f32 v244, v244, v254\n"
    "v_mul_f32 v246, v246, v255\n"
    "v_mfma_f32_16x16x32_bf16 v[164:167], a[204:207], a[180:183], v[164:167]\n"
    "v_add_f32_dpp v254, v252, v49 row_newbcast:1 row_mask:0xf bank_mask:0xf\n"
    "v_add_f32_dpp v255, v252, v51 row_newbcast:3 row_mask:0xf bank_mask:0xf\n"
    "v_mfma_f32_16x16x32_bf16 v[164:167], a[200:203], a[148:151], v[164:167]\n"
    "v_mul_f32 v245, v245, v254\n"
    "v_mul_f32 v247, v247, v255\n"
    "v_mfma_f32_16x16x32_bf16 v[196:199], a[200:203], a[152:155], v[196:199]\n"
    "v_mfma_f32_16x16x32_bf16 v[196:199], a[204:207], a[184:187], v[196:199]\n"
    "buffer_store_dwordx4 v[248:251], v240, s[32:35], s91 offen sc1\n"
    "ds_write_b128 v243, v[248:251] offset:0\n"
    "v_mfma_f32_16x16x32_bf16 v[228:231], a[204:207], a[188:191], v[228:231]\n"
    "v_cvt_pk_bf16_f32 v248, v244, v245\n"
    "v_cvt_pk_bf16_f32 v249, v246, v247\n"
    "v_mfma_f32_16x16x32_bf16 v[228:231], a[200:203], a[156:159], v[228:231]\n"
    "s_waitcnt vmcnt(9)\n"
    "v_accvgpr_read_b32 v245, a70\n"
    "v_accvgpr_read_b32 v247, a71\n"
    "v_mfma_f32_16x16x32_bf16 v[232:235], a[208:211], a[156:159], v[232:235]\n"
    "buffer_load_dwordx4 a[200:203], v240, s[20:23], s53 offen offset:2048 sc1\n"
    "v_mfma_f32_16x16x32_bf16 v[232:235], a[212:215], a[188:191], v[232:235]\n"
    "buffer_load_dwordx4 a[204:207], v240, s[20:23], s53 offen offset:3072 sc1\n"
    "v_mfma_f32_16x16x32_bf16 v[200:203], a[212:215], a[184:187], v[200:203]\n"
    "s_addk_i32 s53, 4096\n"
    "v_lshlrev_b32 v244, 16, v245\n"
    "v_lshlrev_b32 v246, 16, v247\n"
    "v_mfma_f32_16x16x32_bf16 v[200:203], a[208:211], a[152:155], v[200:203]\n"
    "v_and_b32 v245, s58, v245\n"
    "v_and_b32 v247, s58, v247\n"
    "v_mfma_f32_16x16x32_bf16 v[168:171], a[208:211], a[148:151], v[168:171]\n"
    "s_xor_b32 s91, s91, 0x800\n"
    "v_add_f32_dpp v254, v252, v52 row_newbcast:4 row_mask:0xf bank_mask:0xf\n"
    "v_add_f32_dpp v255, v252, v54 row_newbcast:6 row_mask:0xf bank_mask:0xf\n"
    "v_mfma_f32_16x16x32_bf16 v[168:171], a[212:215], a[180:183], v[168:171]\n"
    "v_mul_f32 v244, v244, v254\n"
    "v_mul_f32 v246, v246, v255\n"
    "v_mfma_f32_16x16x32_bf16 v[136:139], a[212:215], a[176:179], v[136:139]\n"
    "v_add_f32_dpp v254, v252, v53 row_newbcast:5 row_mask:0xf bank_mask:0xf\n"
    "v_add_f32_dpp v255, v252, v55 row_newbcast:7 row_mask:0xf bank_mask:0xf\n"
    "v_mfma_f32_16x16x32_bf16 v[136:139], a[208:211], a[144:147], v[136:139]\n"
    "v_mul_f32 v245, v245, v254\n"
    "v_mul_f32 v247, v247, v255\n"
    "v_mfma_f32_16x16x32_bf16 v[104:107], a[208:211], a[140:143], v[104:107]\n"
    "v_cvt_pk_bf16_f32 v250, v244, v245\n"
    "v_cvt_pk_bf16_f32 v251, v246, v247\n"
    "v_mfma_f32_16x16x32_bf16 v[104:107], a[212:215], a[172:175], v[104:107]\n"
    "v_accvgpr_read_b32 v245, a72\n"
    "v_accvgpr_read_b32 v247, a73\n"
    "v_mfma_f32_16x16x32_bf16 v[72:75], a[212:215], a[168:171], v[72:75]\n"
    "v_lshlrev_b32 v244, 16, v245\n"
    "v_lshlrev_b32 v246, 16, v247\n"
    "v_mfma_f32_16x16x32_bf16 v[72:75], a[208:211], a[136:139], v[72:75]\n"
    "v_and_b32 v245, s58, v245\n"
    "v_and_b32 v247, s58, v247\n"
    "v_mfma_f32_16x16x32_bf16 v[40:43], a[208:211], a[132:135], v[40:43]\n"
    "v_add_f32_dpp v254, v252, v80 row_newbcast:0 row_mask:0xf bank_mask:0xf\n"
    "v_add_f32_dpp v255, v252, v82 row_newbcast:2 row_mask:0xf bank_mask:0xf\n"
    "v_mfma_f32_16x16x32_bf16 v[40:43], a[212:215], a[164:167], v[40:43]\n"
    "v_mul_f32 v244, v244, v254\n"
    "v_mul_f32 v246, v246, v255\n"
    "v_mfma_f32_16x16x32_bf16 v[8:11], a[212:215], a[160:163], v[8:11]\n"
    "v_add_f32_dpp v254, v252, v81 row_newbcast:1 row_mask:0xf bank_mask:0xf\n"
    "v_add_f32_dpp v255, v252, v83 row_newbcast:3 row_mask:0xf bank_mask:0xf\n"
    "v_mfma_f32_16x16x32_bf16 v[8:11], a[208:211], a[128:131], v[8:11]\n"
    "s_waitcnt vmcnt(9)\n"
    "v_mul_f32 v245, v245, v254\n"
    "v_mul_f32 v247, v247, v255\n"
    "v_mfma_f32_16x16x32_bf16 v[12:15], a[216:219], a[128:131], v[12:15]\n"
    "buffer_load_dwordx4 a[208:211], v240, s[20:23], s53 offen offset:0 sc1\n"
    "v_mfma_f32_16x16x32_bf16 v[12:15], a[220:223], a[160:163], v[12:15]\n"
    "buffer_load_dwordx4 a[212:215], v240, s[20:23], s53 offen offset:1024 sc1\n"
    "v_mfma_f32_16x16x32_bf16 v[44:47], a[220:223], a[164:167], v[44:47]\n"
    "ds_read_b128 a[128:131], v241 offset:16384\n"
    "v_mfma_f32_16x16x32_bf16 v[44:47], a[216:219], a[132:135], v[44:47]\n"
    "buffer_store_dwordx4 v[248:251], v240, s[32:35], s91 offen sc1\n"
    "ds_write_b128 v243, v[248:251] offset:2048\n"
    "v_mfma_f32_16x16x32_bf16 v[76:79], a[216:219], a[136:139], v[76:79]\n"
    "ds_read_b128 a[160:163], v241 offset:17408\n"
    "ds_read_b128 a[164:167], v241 offset:19456\n"
    "v_mfma_f32_16x16x32_bf16 v[76:79], a[220:223], a[168:171], v[76:79]\n"
    "ds_read_b128 a[132:135], v241 offset:18432\n"
    "v_cvt_pk_bf16_f32 v248, v244, v245\n"
    "v_mfma_f32_16x16x32_bf16 v[108:111], a[220:223], a[172:175], v[108:111]\n"
    "ds_read_b128 a[136:139], v241 offset:20480\n"
    "v_cvt_pk_bf16_f32 v249, v246, v247\n"
    "v_mfma_f32_16x16x32_bf16 v[108:111], a[216:219], a[140:143], v[108:111]\n"
    "ds_read_b128 a[168:171], v241 offset:21504\n"
    "v_accvgpr_read_b32 v245, a74\n"
    "v_mfma_f32_16x16x32_bf16 v[140:143], a[216:219], a[144:147], v[140:143]\n"
    "ds_read_b128 a[172:175], v241 offset:23552\n"
    "v_accvgpr_read_b32 v247, a75\n"
    "v_mfma_f32_16x16x32_bf16 v[140:143], a[220:223], a[176:179], v[140:143]\n"
    "ds_read_b128 a[140:143], v241 offset:22528\n"
    "v_lshlrev_b32 v244, 16, v245\n"
    "v_mfma_f32_16x16x32_bf16 v[172:175], a[220:223], a[180:183], v[172:175]\n"
    "ds_read_b128 a[144:147], v241 offset:24576\n"
    "v_lshlrev_b32 v246, 16, v247\n"
    "v_mfma_f32_16x16x32_bf16 v[172:175], a[216:219], a[148:151], v[172:175]\n"
    "ds_read_b128 a[176:179], v241 offset:25600\n"
    "v_and_b32 v245, s58, v245\n"
    "v_mfma_f32_16x16x32_bf16 v[204:207], a[216:219], a[152:155], v[204:207]\n"
    "ds_read_b128 a[180:183], v241 offset:27648\n"
    "v_and_b32 v247, s58, v247\n"
    "v_mfma_f32_16x16x32_bf16 v[204:207], a[220:223], a[184:187], v[204:207]\n"
    "ds_read_b128 a[148:151], v241 offset:26624\n"
    "v_add_f32_dpp v254, v252, v84 row_newbcast:4 row_mask:0xf bank_mask:0xf\n"
    "v_mfma_f32_16x16x32_bf16 v[236:239], a[220:223], a[188:191], v[236:239]\n"
    "ds_read_b128 a[152:155], v241 offset:28672\n"
    "s_xor_b32 s91, s91, 0x101800\n"
    "v_mfma_f32_16x16x32_bf16 v[236:239], a[216:219], a[156:159], v[236:239]\n"
    "s_waitcnt vmcnt(10) lgkmcnt(11)\n"
    "ds_read_b128 a[184:187], v241 offset:29696\n"
    "v_add_f32_dpp v255, v252, v86 row_newbcast:6 row_mask:0xf bank_mask:0xf\n"
    "v_mfma_f32_16x16x32_bf16 v[0:3], a[224:227], a[128:131], v[0:3]\n"
    "buffer_load_dwordx4 a[216:219], v240, s[20:23], s53 offen offset:2048 sc1\n"
    "v_mfma_f32_16x16x32_bf16 v[0:3], a[228:231], a[160:163], v[0:3]\n"
    "s_waitcnt lgkmcnt(10)\n"
    "v_mul_f32 v244, v244, v254\n"
    "v_mul_f32 v246, v246, v255\n"
    "v_mfma_f32_16x16x32_bf16 v[32:35], a[228:231], a[164:167], v[32:35]\n"
    "buffer_load_dwordx4 a[220:223], v240, s[20:23], s53 offen offset:3072 sc1\n"
    "v_mfma_f32_16x16x32_bf16 v[32:35], a[224:227], a[132:135], v[32:35]\n"
    "s_waitcnt lgkmcnt(8)\n"
    "s_addk_i32 s53, 4096\n"
    "ds_read_b128 a[188:191], v241 offset:31744\n"
    "v_mfma_f32_16x16x32_bf16 v[64:67], a[224:227], a[136:139], v[64:67]\n"
    "ds_read_b128 a[156:159], v241 offset:30720\n"
    "v_add_f32_dpp v254, v252, v85 row_newbcast:5 row_mask:0xf bank_mask:0xf\n"
    "v_mfma_f32_16x16x32_bf16 v[64:67], a[228:231], a[168:171], v[64:67]\n"
    "s_waitcnt lgkmcnt(8)\n"
    "v_add_f32_dpp v255, v252, v87 row_newbcast:7 row_mask:0xf bank_mask:0xf\n"
    "v_mul_f32 v245, v245, v254\n"
    "v_mfma_f32_16x16x32_bf16 v[96:99], a[228:231], a[172:175], v[96:99]\n"
    "v_mul_f32 v247, v247, v255\n"
    "v_cvt_pk_bf16_f32 v250, v244, v245\n"
    "v_mfma_f32_16x16x32_bf16 v[96:99], a[224:227], a[140:143], v[96:99]\n"
    "s_waitcnt lgkmcnt(6)\n"
    "v_cvt_pk_bf16_f32 v251, v246, v247\n"
    "v_accvgpr_read_b32 v245, a76\n"
    "v_mfma_f32_16x16x32_bf16 v[128:131], a[224:227], a[144:147], v[128:131]\n"
    "v_accvgpr_read_b32 v247, a77\n"
    "v_lshlrev_b32 v244, 16, v245\n"
    "v_mfma_f32_16x16x32_bf16 v[128:131], a[228:231], a[176:179], v[128:131]\n"
    "s_waitcnt lgkmcnt(4)\n"
    "v_lshlrev_b32 v246, 16, v247\n"
    "v_and_b32 v245, s58, v245\n"
    "v_mfma_f32_16x16x32_bf16 v[160:163], a[228:231], a[180:183], v[160:163]\n"
    "v_and_b32 v247, s58, v247\n"
    "v_add_f32_dpp v254, v252, v112 row_newbcast:0 row_mask:0xf bank_mask:0xf\n"
    "v_mfma_f32_16x16x32_bf16 v[160:163], a[224:227], a[148:151], v[160:163]\n"
    "s_waitcnt lgkmcnt(2)\n"
    "v_add_f32_dpp v255, v252, v114 row_newbcast:2 row_mask:0xf bank_mask:0xf\n"
    "v_mul_f32 v244, v244, v254\n"
    "v_mfma_f32_16x16x32_bf16 v[192:195], a[224:227], a[152:155], v[192:195]\n"
    "v_mul_f32 v246, v246, v255\n"
    "v_add_f32_dpp v254, v252, v113 row_newbcast:1 row_mask:0xf bank_mask:0xf\n"
    "v_mfma_f32_16x16x32_bf16 v[192:195], a[228:231], a[184:187], v[192:195]\n"
    "s_waitcnt lgkmcnt(0)\n"
    "v_add_f32_dpp v255, v252, v115 row_newbcast:3 row_mask:0xf bank_mask:0xf\n"
    "v_mul_f32 v245, v245, v254\n"
    "v_mfma_f32_16x16x32_bf16 v[224:227], a[228:231], a[188:191], v[224:227]\n"
    "v_mul_f32 v247, v247, v255\n"
    "v_mfma_f32_16x16x32_bf16 v[224:227], a[224:227], a[156:159], v[224:227]\n"
    "s_waitcnt vmcnt(10)\n"
    "v_mfma_f32_16x16x32_bf16 v[228:231], a[232:235], a[156:159], v[228:231]\n"
    "buffer_load_dwordx4 a[224:227], v240, s[20:23], s53 offen offset:0 sc1\n"
    "v_mfma_f32_16x16x32_bf16 v[228:231], a[236:239], a[188:191], v[228:231]\n"
    "buffer_load_dwordx4 a[228:231], v240, s[20:23], s53 offen offset:1024 sc1\n"
    "v_mfma_f32_16x16x32_bf16 v[196:199], a[236:239], a[184:187], v[196:199]\n"
    "v_mfma_f32_16x16x32_bf16 v[196:199], a[232:235], a[152:155], v[196:199]\n"
    "buffer_store_dwordx4 v[248:251], v240, s[32:35], s91 offen sc1\n"
    "ds_write_b128 v243, v[248:251] offset:4096\n"
    "v_mfma_f32_16x16x32_bf16 v[164:167], a[232:235], a[148:151], v[164:167]\n"
    "v_cvt_pk_bf16_f32 v248, v244, v245\n"
    "v_cvt_pk_bf16_f32 v249, v246, v247\n"
    "v_mfma_f32_16x16x32_bf16 v[164:167], a[236:239], a[180:183], v[164:167]\n"
    "v_accvgpr_read_b32 v245, a78\n"
    "v_accvgpr_read_b32 v247, a79\n"
    "v_mfma_f32_16x16x32_bf16 v[132:135], a[236:239], a[176:179], v[132:135]\n"
    "v_lshlrev_b32 v244, 16, v245\n"
    "v_lshlrev_b32 v246, 16, v247\n"
    "v_mfma_f32_16x16x32_bf16 v[132:135], a[232:235], a[144:147], v[132:135]\n"
    "v_and_b32 v245, s58, v245\n"
    "v_and_b32 v247, s58, v247\n"
    "v_mfma_f32_16x16x32_bf16 v[100:103], a[232:235], a[140:143], v[100:103]\n"
    "s_xor_b32 s91, s91, 0x800\n"
    "v_add_f32_dpp v254, v252, v116 row_newbcast:4 row_mask:0xf bank_mask:0xf\n"
    "v_add_f32_dpp v255, v252, v118 row_newbcast:6 row_mask:0xf bank_mask:0xf\n"
    "v_mfma_f32_16x16x32_bf16 v[100:103], a[236:239], a[172:175], v[100:103]\n"
    "v_mul_f32 v244, v244, v254\n"
    "v_mul_f32 v246, v246, v255\n"
    "v_mfma_f32_16x16x32_bf16 v[68:71], a[236:239], a[168:171], v[68:71]\n"
    "v_add_f32_dpp v254, v252, v117 row_newbcast:5 row_mask:0xf bank_mask:0xf\n"
    "v_add_f32_dpp v255, v252, v119 row_newbcast:7 row_mask:0xf bank_mask:0xf\n"
    "v_mfma_f32_16x16x32_bf16 v[68:71], a[232:235], a[136:139], v[68:71]\n"
    "v_mul_f32 v245, v245, v254\n"
    "v_mul_f32 v247, v247, v255\n"
    "v_mfma_f32_16x16x32_bf16 v[36:39], a[232:235], a[132:135], v[36:39]\n"
    "v_cvt_pk_bf16_f32 v250, v244, v245\n"
    "v_cvt_pk_bf16_f32 v251, v246, v247\n"
    "v_mfma_f32_16x16x32_bf16 v[36:39], a[236:239], a[164:167], v[36:39]\n"
    "v_accvgpr_read_b32 v245, a80\n"
    "v_accvgpr_read_b32 v247, a81\n"
    "v_mfma_f32_16x16x32_bf16 v[4:7], a[236:239], a[160:163], v[4:7]\n"
    "v_lshlrev_b32 v244, 16, v245\n"
    "v_lshlrev_b32 v246, 16, v247\n"
    "v_mfma_f32_16x16x32_bf16 v[4:7], a[232:235], a[128:131], v[4:7]\n"
    "s_waitcnt vmcnt(11)\n"
    "v_and_b32 v245, s58, v245\n"
    "v_and_b32 v247, s58, v247\n"
    "v_mfma_f32_16x16x32_bf16 v[8:11], a[192:195], a[128:131], v[8:11]\n"
    "buffer_load_dwordx4 a[232:235], v240, s[20:23], s53 offen offset:2048 sc1\n"
    "v_mfma_f32_16x16x32_bf16 v[8:11], a[196:199], a[160:163], v[8:11]\n"
    "buffer_load_dwordx4 a[236:239], v240, s[20:23], s53 offen offset:3072 sc1\n"
    "v_mfma_f32_16x16x32_bf16 v[40:43], a[196:199], a[164:167], v[40:43]\n"
    "s_addk_i32 s53, 4096\n"
    "v_add_f32_dpp v254, v252, v144 row_newbcast:0 row_mask:0xf bank_mask:0xf\n"
    "v_add_f32_dpp v255, v252, v146 row_newbcast:2 row_mask:0xf bank_mask:0xf\n"
    "v_mfma_f32_16x16x32_bf16 v[40:43], a[192:195], a[132:135], v[40:43]\n"
    "v_mul_f32 v244, v244, v254\n"
    "v_mul_f32 v246, v246, v255\n"
    "v_mfma_f32_16x16x32_bf16 v[72:75], a[192:195], a[136:139], v[72:75]\n"
    "v_add_f32_dpp v254, v252, v145 row_newbcast:1 row_mask:0xf bank_mask:0xf\n"
    "v_add_f32_dpp v255, v252, v147 row_newbcast:3 row_mask:0xf bank_mask:0xf\n"
    "v_mfma_f32_16x16x32_bf16 v[72:75], a[196:199], a[168:171], v[72:75]\n"
    "v_mul_f32 v245, v245, v254\n"
    "v_mul_f32 v247, v247, v255\n"
    "v_mfma_f32_16x16x32_bf16 v[104:107], a[196:199], a[172:175], v[104:107]\n"
    "v_mfma_f32_16x16x32_bf16 v[104:107], a[192:195], a[140:143], v[104:107]\n"
    "buffer_store_dwordx4 v[248:251], v240, s[32:35], s91 offen sc1\n"
    "ds_write_b128 v243, v[248:251] offset:6144\n"
    "v_mfma_f32_16x16x32_bf16 v[136:139], a[192:195], a[144:147], v[136:139]\n"
    "v_cvt_pk_bf16_f32 v248, v244, v245\n"
    "v_cvt_pk_bf16_f32 v249, v246, v247\n"
    "v_mfma_f32_16x16x32_bf16 v[136:139], a[196:199], a[176:179], v[136:139]\n"
    "v_accvgpr_read_b32 v245, a82\n"
    "v_accvgpr_read_b32 v247, a83\n"
    "v_mfma_f32_16x16x32_bf16 v[168:171], a[196:199], a[180:183], v[168:171]\n"
    "v_lshlrev_b32 v244, 16, v245\n"
    "v_lshlrev_b32 v246, 16, v247\n"
    "v_mfma_f32_16x16x32_bf16 v[168:171], a[192:195], a[148:151], v[168:171]\n"
    "v_and_b32 v245, s58, v245\n"
    "v_and_b32 v247, s58, v247\n"
    "v_mfma_f32_16x16x32_bf16 v[200:203], a[192:195], a[152:155], v[200:203]\n"
    "s_xor_b32 s91, s91, 0x185800\n"
    "v_add_f32_dpp v254, v252, v148 row_newbcast:4 row_mask:0xf bank_mask:0xf\n"
    "v_add_f32_dpp v255, v252, v150 row_newbcast:6 row_mask:0xf bank_mask:0xf\n"
    "v_mfma_f32_16x16x32_bf16 v[200:203], a[196:199], a[184:187], v[200:203]\n"
    "v_mul_f32 v244, v244, v254\n"
    "v_mul_f32 v246, v246, v255\n"
    "v_mfma_f32_16x16x32_bf16 v[232:235], a[196:199], a[188:191], v[232:235]\n"
    "v_add_f32_dpp v254, v252, v149 row_newbcast:5 row_mask:0xf bank_mask:0xf\n"
    "v_add_f32_dpp v255, v252, v151 row_newbcast:7 row_mask:0xf bank_mask:0xf\n"
    "v_mfma_f32_16x16x32_bf16 v[232:235], a[192:195], a[156:159], v[232:235]\n"
    "s_waitcnt vmcnt(11)\n"
    "v_mul_f32 v245, v245, v254\n"
    "v_mul_f32 v247, v247, v255\n"
    "v_mfma_f32_16x16x32_bf16 v[236:239], a[200:203], a[156:159], v[236:239]\n"
    "buffer_load_dwordx4 a[192:195], v240, s[20:23], s53 offen offset:0 sc1\n"
    "v_mfma_f32_16x16x32_bf16 v[236:239], a[204:207], a[188:191], v[236:239]\n"
    "buffer_load_dwordx4 a[196:199], v240, s[20:23], s53 offen offset:1024 sc1\n"
    "v_mfma_f32_16x16x32_bf16 v[204:207], a[204:207], a[184:187], v[204:207]\n"
    "ds_read_b128 a[156:159], v241 offset:47104\n"
    "v_cvt_pk_bf16_f32 v250, v244, v245\n"
    "v_mfma_f32_16x16x32_bf16 v[204:207], a[200:203], a[152:155], v[204:207]\n"
    "ds_read_b128 a[188:191], v241 offset:48128\n"
    "v_cvt_pk_bf16_f32 v251, v246, v247\n"
    "v_mfma_f32_16x16x32_bf16 v[172:175], a[200:203], a[148:151], v[172:175]\n"
    "ds_read_b128 a[184:187], v241 offset:46080\n"
    "v_accvgpr_read_b32 v245, a84\n"
    "v_mfma_f32_16x16x32_bf16 v[172:175], a[204:207], a[180:183], v[172:175]\n"
    "ds_read_b128 a[152:155], v241 offset:45056\n"
    "v_accvgpr_read_b32 v247, a85\n"
    "v_mfma_f32_16x16x32_bf16 v[140:143], a[204:207], a[176:179], v[140:143]\n"
    "ds_read_b128 a[148:151], v241 offset:43008\n"
    "v_lshlrev_b32 v244, 16, v245\n"
    "v_mfma_f32_16x16x32_bf16 v[140:143], a[200:203], a[144:147], v[140:143]\n"
    "ds_read_b128 a[180:183], v241 offset:44032\n"
    "v_lshlrev_b32 v246, 16, v247\n"
    "v_mfma_f32_16x16x32_bf16 v[108:111], a[200:203], a[140:143], v[108:111]\n"
    "s_add_u32 s62, s52, s64\n"
    "v_mov_b32 v242, s62\n"
    "s_mov_b32 s46, 4\n"
    "v_mfma_f32_16x16x32_bf16 v[108:111], a[204:207], a[172:175], v[108:111]\n"
    "buffer_store_dword v242, v240, s[44:47], 0 offen offset:0 sc1\n"
    "v_mfma_f32_16x16x32_bf16 v[76:79], a[204:207], a[168:171], v[76:79]\n"
    "ds_read_b128 a[176:179], v241 offset:41984\n"
    "ds_read_b128 a[144:147], v241 offset:40960\n"
    "v_mfma_f32_16x16x32_bf16 v[76:79], a[200:203], a[136:139], v[76:79]\n"
    "ds_read_b128 a[140:143], v241 offset:38912\n"
    "ds_read_b128 a[172:175], v241 offset:39936\n"
    "v_mfma_f32_16x16x32_bf16 v[44:47], a[200:203], a[132:135], v[44:47]\n"
    "ds_read_b128 a[168:171], v241 offset:37888\n"
    "v_and_b32 v245, s58, v245\n"
    "v_mfma_f32_16x16x32_bf16 v[44:47], a[204:207], a[164:167], v[44:47]\n"
    "ds_read_b128 a[136:139], v241 offset:36864\n"
    "v_and_b32 v247, s58, v247\n"
    "v_mfma_f32_16x16x32_bf16 v[12:15], a[204:207], a[160:163], v[12:15]\n"
    "ds_read_b128 a[132:135], v241 offset:34816\n"
    "v_add_f32_dpp v254, v252, v176 row_newbcast:0 row_mask:0xf bank_mask:0xf\n"
    "v_mfma_f32_16x16x32_bf16 v[12:15], a[200:203], a[128:131], v[12:15]\n"
    "s_waitcnt vmcnt(12) lgkmcnt(11)\n"
    "ds_read_b128 a[164:167], v241 offset:35840\n"
    "v_add_f32_dpp v255, v252, v178 row_newbcast:2 row_mask:0xf bank_mask:0xf\n"
    "v_mfma_f32_16x16x32_bf16 v[224:227], a[208:211], a[156:159], v[224:227]\n"
    "buffer_load_dwordx4 a[200:203], v240, s[20:23], s53 offen offset:2048 sc1\n"
    "v_mfma_f32_16x16x32_bf16 v[224:227], a[212:215], a[188:191], v[224:227]\n"
    "s_waitcnt lgkmcnt(10)\n"
    "v_mul_f32 v244, v244, v254\n"
    "v_mul_f32 v246, v246, v255\n"
    "v_mfma_f32_16x16x32_bf16 v[192:195], a[212:215], a[184:187], v[192:195]\n"
    "buffer_load_dwordx4 a[204:207], v240, s[20:23], s53 offen offset:3072 sc1\n"
    "v_mfma_f32_16x16x32_bf16 v[192:195], a[208:211], a[152:155], v[192:195]\n"
    "s_waitcnt lgkmcnt(8)\n"
    "s_addk_i32 s53, 4096\n"
    "ds_read_b128 a[160:163], v241 offset:33792\n"
    "v_mfma_f32_16x16x32_bf16 v[160:163], a[208:211], a[148:151], v[160:163]\n"
    "ds_read_b128 a[128:131], v241 offset:32768\n"
    "v_add_f32_dpp v254, v252, v177 row_newbcast:1 row_mask:0xf bank_mask:0xf\n"
    "v_mfma_f32_16x16x32_bf16 v[160:163], a[212:215], a[180:183], v[160:163]\n"
    "s_waitcnt lgkmcnt(8)\n"
    "v_add_f32_dpp v255, v252, v179 row_newbcast:3 row_mask:0xf bank_mask:0xf\n"
    "v_mul_f32 v245, v245, v254\n"
    "v_mfma_f32_16x16x32_bf16 v[128:131], a[212:215], a[176:179], v[128:131]\n"
    "v_mul_f32 v247, v247, v255\n"
    "v_mfma_f32_16x16x32_bf16 v[128:131], a[208:211], a[144:147], v[128:131]\n"
    "s_waitcnt lgkmcnt(6)\n"
    "v_mfma_f32_16x16x32_bf16 v[96:99], a[208:211], a[140:143], v[96:99]\n"
    "buffer_store_dwordx4 v[248:251], v240, s[32:35], s91 offen sc1\n"
    "ds_write_b128 v243, v[248:251] offset:8192\n"
    "v_mfma_f32_16x16x32_bf16 v[96:99], a[212:215], a[172:175], v[96:99]\n"
    "s_waitcnt lgkmcnt(5)\n"
    "v_cvt_pk_bf16_f32 v248, v244, v245\n"
    "v_cvt_pk_bf16_f32 v249, v246, v247\n"
    "v_mfma_f32_16x16x32_bf16 v[64:67], a[212:215], a[168:171], v[64:67]\n"
    "v_accvgpr_read_b32 v245, a86\n"
    "v_accvgpr_read_b32 v247, a87\n"
    "v_mfma_f32_16x16x32_bf16 v[64:67], a[208:211], a[136:139], v[64:67]\n"
    "s_waitcnt lgkmcnt(3)\n"
    "v_lshlrev_b32 v244, 16, v245\n"
    "v_lshlrev_b32 v246, 16, v247\n"
    "v_mfma_f32_16x16x32_bf16 v[32:35], a[208:211], a[132:135], v[32:35]\n"
    "v_and_b32 v245, s58, v245\n"
    "v_and_b32 v247, s58, v247\n"
    "v_mfma_f32_16x16x32_bf16 v[32:35], a[212:215], a[164:167], v[32:35]\n"
    "s_waitcnt lgkmcnt(1)\n"
    "s_xor_b32 s91, s91, 0x800\n"
    "v_add_f32_dpp v254, v252, v180 row_newbcast:4 row_mask:0xf bank_mask:0xf\n"
    "v_mfma_f32_16x16x32_bf16 v[0:3], a[212:215], a[160:163], v[0:3]\n"
    "v_add_f32_dpp v255, v252, v182 row_newbcast:6 row_mask:0xf bank_mask:0xf\n"
    "v_mul_f32 v244, v244, v254\n"
    "v_mfma_f32_16x16x32_bf16 v[0:3], a[208:211], a[128:131], v[0:3]\n"
    "s_waitcnt vmcnt(12)\n"
    "v_mul_f32 v246, v246, v255\n"
    "v_add_f32_dpp v254, v252, v181 row_newbcast:5 row_mask:0xf bank_mask:0xf\n"
    "v_mfma_f32_16x16x32_bf16 v[4:7], a[216:219], a[128:131], v[4:7]\n"
    "buffer_load_dwordx4 a[212:215], v240, s[20:23], s53 offen offset:0 sc1\n"
    "v_mfma_f32_16x16x32_bf16 v[4:7], a[220:223], a[160:163], v[4:7]\n"
    "v_add_f32_dpp v255, v252, v183 row_newbcast:7 row_mask:0xf bank_mask:0xf\n"
    "v_mul_f32 v245, v245, v254\n"
    "v_mfma_f32_16x16x32_bf16 v[36:39], a[220:223], a[164:167], v[36:39]\n"
    "v_mul_f32 v247, v247, v255\n"
    "v_cvt_pk_bf16_f32 v250, v244, v245\n"
    "v_mfma_f32_16x16x32_bf16 v[36:39], a[216:219], a[132:135], v[36:39]\n"
    "v_cvt_pk_bf16_f32 v251, v246, v247\n"
    "v_accvgpr_read_b32 v245, a88\n"
    "v_mfma_f32_16x16x32_bf16 v[68:71], a[216:219], a[136:139], v[68:71]\n"
    "v_accvgpr_read_b32 v247, a89\n"
    "v_lshlrev_b32 v244, 16, v245\n"
    "v_mfma_f32_16x16x32_bf16 v[68:71], a[220:223], a[168:171], v[68:71]\n"
    "v_lshlrev_b32 v246, 16, v247\n"
    "v_and_b32 v245, s58, v245\n"
    "v_mfma_f32_16x16x32_bf16 v[100:103], a[220:223], a[172:175], v[100:103]\n"
    "v_and_b32 v247, s58, v247\n"
    "v_add_f32_dpp v254, v252, v208 row_newbcast:0 row_mask:0xf bank_mask:0xf\n"
    "v_mfma_f32_16x16x32_bf16 v[100:103], a[216:219], a[140:143], v[100:103]\n"
    "v_add_f32_dpp v255, v252, v210 row_newbcast:2 row_mask:0xf bank_mask:0xf\n"
    "v_mul_f32 v244, v244, v254\n"
    "v_mfma_f32_16x16x32_bf16 v[132:135], a[216:219], a[144:147], v[132:135]\n"
    "v_mul_f32 v246, v246, v255\n"
    "v_add_f32_dpp v254, v252, v209 row_newbcast:1 row_mask:0xf bank_mask:0xf\n"
    "v_mfma_f32_16x16x32_bf16 v[132:135], a[220:223], a[176:179], v[132:135]\n"
    "v_add_f32_dpp v255, v252, v211 row_newbcast:3 row_mask:0xf bank_mask:0xf\n"
    "v_mul_f32 v245, v245, v254\n"
    "v_mfma_f32_16x16x32_bf16 v[164:167], a[220:223], a[180:183], v[164:167]\n"
    "v_mul_f32 v247, v247, v255\n"
    "v_mfma_f32_16x16x32_bf16 v[164:167], a[216:219], a[148:151], v[164:167]\n"
    "buffer_store_dwordx4 v[248:251], v240, s[32:35], s91 offen sc1\n"
    "ds_write_b128 v243, v[248:251] offset:10240\n"
    "v_mfma_f32_16x16x32_bf16 v[196:199], a[216:219], a[152:155], v[196:199]\n"
    "v_cvt_pk_bf16_f32 v248, v244, v245\n"
    "v_cvt_pk_bf16_f32 v249, v246, v247\n"
    "v_mfma_f32_16x16x32_bf16 v[196:199], a[220:223], a[184:187], v[196:199]\n"
    "v_accvgpr_read_b32 v245, a90\n"
    "v_accvgpr_read_b32 v247, a91\n"
    "v_mfma_f32_16x16x32_bf16 v[228:231], a[220:223], a[188:191], v[228:231]\n"
    "v_lshlrev_b32 v244, 16, v245\n"
    "v_lshlrev_b32 v246, 16, v247\n"
    "v_mfma_f32_16x16x32_bf16 v[228:231], a[216:219], a[156:159], v[228:231]\n"
    "s_waitcnt vmcnt(12)\n"
    "v_and_b32 v245, s58, v245\n"
    "v_and_b32 v247, s58, v247\n"
    "v_mfma_f32_16x16x32_bf16 v[232:235], a[224:227], a[156:159], v[232:235]\n"
    "buffer_load_dwordx4 a[216:219], v240, s[20:23], s53 offen offset:1024 sc1\n"
    "v_mfma_f32_16x16x32_bf16 v[232:235], a[228:231], a[188:191], v[232:235]\n"
    "buffer_load_dwordx4 a[220:223], v240, s[20:23], s53 offen offset:2048 sc1\n"
    "v_mfma_f32_16x16x32_bf16 v[200:203], a[228:231], a[184:187], v[200:203]\n"
    "s_xor_b32 s91, s91, 0x101800\n"
    "v_add_f32_dpp v254, v252, v212 row_newbcast:4 row_mask:0xf bank_mask:0xf\n"
    "v_add_f32_dpp v255, v252, v214 row_newbcast:6 row_mask:0xf bank_mask:0xf\n"
    "v_mfma_f32_16x16x32_bf16 v[200:203], a[224:227], a[152:155], v[200:203]\n"
    "v_mul_f32 v244, v244, v254\n"
    "v_mul_f32 v246, v246, v255\n"
    "v_mfma_f32_16x16x32_bf16 v[168:171], a[224:227], a[148:151], v[168:171]\n"
    "v_add_f32_dpp v254, v252, v213 row_newbcast:5 row_mask:0xf bank_mask:0xf\n"
    "v_add_f32_dpp v255, v252, v215 row_newbcast:7 row_mask:0xf bank_mask:0xf\n"
    "v_mfma_f32_16x16x32_bf16 v[168:171], a[228:231], a[180:183], v[168:171]\n"
    "v_mul_f32 v245, v245, v254\n"
    "v_mul_f32 v247, v247, v255\n"
    "v_mfma_f32_16x16x32_bf16 v[136:139], a[228:231], a[176:179], v[136:139]\n"
    "v_cvt_pk_bf16_f32 v250, v244, v245\n"
    "v_cvt_pk_bf16_f32 v251, v246, v247\n"
    "v_mfma_f32_16x16x32_bf16 v[136:139], a[224:227], a[144:147], v[136:139]\n"
    "v_accvgpr_read_b32 v245, a92\n"
    "v_accvgpr_read_b32 v247, a93\n"
    "v_mfma_f32_16x16x32_bf16 v[104:107], a[224:227], a[140:143], v[104:107]\n"
    "v_lshlrev_b32 v244, 16, v245\n"
    "v_lshlrev_b32 v246, 16, v247\n"
    "v_mfma_f32_16x16x32_bf16 v[104:107], a[228:231], a[172:175], v[104:107]\n"
    "v_and_b32 v245, s58, v245\n"
    "v_and_b32 v247, s58, v247\n"
    "v_mfma_f32_16x16x32_bf16 v[72:75], a[228:231], a[168:171], v[72:75]\n"
    "v_accvgpr_read_b32 v254, a240\n"
    "v_accvgpr_read_b32 v255, a242\n"
    "v_mfma_f32_16x16x32_bf16 v[72:75], a[224:227], a[136:139], v[72:75]\n"
    "v_add_f32_dpp v254, v252, v254 row_newbcast:0 row_mask:0xf bank_mask:0xf\n"
    "v_add_f32_dpp v255, v252, v255 row_newbcast:2 row_mask:0xf bank_mask:0xf\n"
    "v_mfma_f32_16x16x32_bf16 v[40:43], a[224:227], a[132:135], v[40:43]\n"
    "v_mul_f32 v244, v244, v254\n"
    "v_mul_f32 v246, v246, v255\n"
    "v_mfma_f32_16x16x32_bf16 v[40:43], a[228:231], a[164:167], v[40:43]\n"
    "v_accvgpr_read_b32 v254, a241\n"
    "v_accvgpr_read_b32 v255, a243\n"
    "v_mfma_f32_16x16x32_bf16 v[8:11], a[228:231], a[160:163], v[8:11]\n"
    "v_add_f32_dpp v254, v252, v254 row_newbcast:1 row_mask:0xf bank_mask:0xf\n"
    "v_add_f32_dpp v255, v252, v255 row_newbcast:3 row_mask:0xf bank_mask:0xf\n"
    "v_mfma_f32_16x16x32_bf16 v[8:11], a[224:227], a[128:131], v[8:11]\n"
    "s_waitcnt vmcnt(11)\n"
    "v_mul_f32 v245, v245, v254\n"
    "v_mul_f32 v247, v247, v255\n"
    "v_mfma_f32_16x16x32_bf16 v[12:15], a[232:235], a[128:131], v[12:15]\n"
    "buffer_load_dwordx4 a[224:227], v240, s[20:23], s53 offen offset:3072 sc1\n"
    "v_mfma_f32_16x16x32_bf16 v[12:15], a[236:239], a[160:163], v[12:15]\n"
    "s_addk_i32 s53, 4096\n"
    "v_mfma_f32_16x16x32_bf16 v[44:47], a[236:239], a[164:167], v[44:47]\n"
    "buffer_load_dwordx4 a[228:231], v240, s[20:23], s53 offen offset:0 sc1\n"
    "v_mfma_f32_16x16x32_bf16 v[44:47], a[232:235], a[132:135], v[44:47]\n"
    "ds_read_b128 a[128:131], v241 offset:49152\n"
    "ds_read_b128 a[160:163], v241 offset:50176\n"
    "v_mfma_f32_16x16x32_bf16 v[76:79], a[232:235], a[136:139], v[76:79]\n"
    "ds_read_b128 a[164:167], v241 offset:52224\n"
    "v_mfma_f32_16x16x32_bf16 v[76:79], a[236:239], a[168:171], v[76:79]\n"
    "buffer_store_dwordx4 v[248:251], v240, s[32:35], s91 offen sc1\n"
    "ds_write_b128 v243, v[248:251] offset:12288\n"
    "v_mfma_f32_16x16x32_bf16 v[108:111], a[236:239], a[172:175], v[108:111]\n"
    "ds_read_b128 a[132:135], v241 offset:51200\n"
    "ds_read_b128 a[136:139], v241 offset:53248\n"
    "v_mfma_f32_16x16x32_bf16 v[108:111], a[232:235], a[140:143], v[108:111]\n"
    "ds_read_b128 a[168:171], v241 offset:54272\n"
    "v_cvt_pk_bf16_f32 v248, v244, v245\n"
    "v_mfma_f32_16x16x32_bf16 v[140:143], a[232:235], a[144:147], v[140:143]\n"
    "ds_read_b128 a[172:175], v241 offset:56320\n"
    "v_cvt_pk_bf16_f32 v249, v246, v247\n"
    "v_mfma_f32_16x16x32_bf16 v[140:143], a[236:239], a[176:179], v[140:143]\n"
    "ds_read_b128 a[140:143], v241 offset:55296\n"
    "v_accvgpr_read_b32 v245, a94\n"
    "v_mfma_f32_16x16x32_bf16 v[172:175], a[236:239], a[180:183], v[172:175]\n"
    "ds_read_b128 a[144:147], v241 offset:57344\n"
    "v_accvgpr_read_b32 v247, a95\n"
    "v_mfma_f32_16x16x32_bf16 v[172:175], a[232:235], a[148:151], v[172:175]\n"
    "ds_read_b128 a[176:179], v241 offset:58368\n"
    "v_lshlrev_b32 v244, 16, v245\n"
    "v_mfma_f32_16x16x32_bf16 v[204:207], a[232:235], a[152:155], v[204:207]\n"
    "ds_read_b128 a[180:183], v241 offset:60416\n"
    "v_lshlrev_b32 v246, 16, v247\n"
    "v_mfma_f32_16x16x32_bf16 v[204:207], a[236:239], a[184:187], v[204:207]\n"
    "ds_read_b128 a[148:151], v241 offset:59392\n"
    "v_and_b32 v245, s58, v245\n"
    "v_mfma_f32_16x16x32_bf16 v[236:239], a[236:239], a[188:191], v[236:239]\n"
    "ds_read_b128 a[152:155], v241 offset:61440\n"
    "v_and_b32 v247, s58, v247\n"
    "v_mfma_f32_16x16x32_bf16 v[236:239], a[232:235], a[156:159], v[236:239]\n"
    "s_waitcnt vmcnt(11) lgkmcnt(12)\n"
    "ds_read_b128 a[184:187], v241 offset:62464\n"
    "v_accvgpr_read_b32 v254, a244\n"
    "v_mfma_f32_16x16x32_bf16 v[0:3], a[192:195], a[128:131], v[0:3]\n"
    "buffer_load_dwordx4 a[232:235], v240, s[20:23], s53 offen offset:1024 sc1\n"
    "v_mfma_f32_16x16x32_bf16 v[0:3], a[196:199], a[160:163], v[0:3]\n"
    "s_waitcnt lgkmcnt(10)\n"
    "ds_read_b128 a[188:191], v241 offset:64512\n"
    "ds_read_b128 a[156:159], v241 offset:63488\n"
    "v_mfma_f32_16x16x32_bf16 v[32:35], a[196:199], a[164:167], v[32:35]\n"
    "v_accvgpr_read_b32 v255, a246\n"
    "v_add_f32_dpp v254, v252, v254 row_newbcast:4 row_mask:0xf bank_mask:0xf\n"
    "v_mfma_f32_16x16x32_bf16 v[32:35], a[192:195], a[132:135], v[32:35]\n"
    "s_waitcnt lgkmcnt(10)\n"
    "v_add_f32_dpp v255, v252, v255 row_newbcast:6 row_mask:0xf bank_mask:0xf\n"
    "v_mul_f32 v244, v244, v254\n"
    "v_mfma_f32_16x16x32_bf16 v[64:67], a[192:195], a[136:139], v[64:67]\n"
    "s_xor_b32 s91, s91, 0x800\n"
    "v_mul_f32 v246, v246, v255\n"
    "v_accvgpr_read_b32 v254, a245\n"
    "v_mfma_f32_16x16x32_bf16 v[64:67], a[196:199], a[168:171], v[64:67]\n"
    "s_waitcnt lgkmcnt(8)\n"
    "v_accvgpr_read_b32 v255, a247\n"
    "v_add_f32_dpp v254, v252, v254 row_newbcast:5 row_mask:0xf bank_mask:0xf\n"
    "v_mfma_f32_16x16x32_bf16 v[96:99], a[196:199], a[172:175], v[96:99]\n"
    "v_add_f32_dpp v255, v252, v255 row_newbcast:7 row_mask:0xf bank_mask:0xf\n"
    "v_mul_f32 v245, v245, v254\n"
    "v_mfma_f32_16x16x32_bf16 v[96:99], a[192:195], a[140:143], v[96:99]\n"
    "s_waitcnt lgkmcnt(6)\n"
    "v_mul_f32 v247, v247, v255\n"
    "v_cvt_pk_bf16_f32 v250, v244, v245\n"
    "v_mfma_f32_16x16x32_bf16 v[128:131], a[192:195], a[144:147], v[128:131]\n"
    "v_cvt_pk_bf16_f32 v251, v246, v247\n"
    "v_mfma_f32_16x16x32_bf16 v[128:131], a[196:199], a[176:179], v[128:131]\n"
    "s_waitcnt lgkmcnt(4)\n"
    "v_mfma_f32_16x16x32_bf16 v[160:163], a[196:199], a[180:183], v[160:163]\n"
    "buffer_store_dwordx4 v[248:251], v240, s[32:35], s91 offen sc1\n"
    "ds_write_b128 v243, v[248:251] offset:14336\n"
    "v_mfma_f32_16x16x32_bf16 v[160:163], a[192:195], a[148:151], v[160:163]\n"
    "s_waitcnt lgkmcnt(3)\n"
    "v_accvgpr_read_b32 v245, a96\n"
    "v_accvgpr_read_b32 v247, a97\n"
    "v_mfma_f32_16x16x32_bf16 v[192:195], a[192:195], a[152:155], v[192:195]\n"
    "v_lshlrev_b32 v244, 16, v245\n"
    "v_lshlrev_b32 v246, 16, v247\n"
    "v_mfma_f32_16x16x32_bf16 v[192:195], a[196:199], a[184:187], v[192:195]\n"
    "s_waitcnt lgkmcnt(1)\n"
    "v_and_b32 v245, s58, v245\n"
    "v_and_b32 v247, s58, v247\n"
    "v_mfma_f32_16x16x32_bf16 v[224:227], a[196:199], a[188:191], v[224:227]\n"
    "v_add_f32_dpp v254, v252, v24 row_newbcast:8 row_mask:0xf bank_mask:0xf\n"
    "v_add_f32_dpp v255, v252, v26 row_newbcast:10 row_mask:0xf bank_mask:0xf\n"
    "v_mfma_f32_16x16x32_bf16 v[224:227], a[192:195], a[156:159], v[224:227]\n"
    "s_waitcnt vmcnt(10)\n"
    "v_mul_f32 v244, v244, v254\n"
    "v_mul_f32 v246, v246, v255\n"
    "v_mfma_f32_16x16x32_bf16 v[228:231], a[200:203], a[156:159], v[228:231]\n"
    "buffer_load_dwordx4 a[192:195], v240, s[20:23], s53 offen offset:2048 sc1\n"
    "v_mfma_f32_16x16x32_bf16 v[228:231], a[204:207], a[188:191], v[228:231]\n"
    "buffer_load_dwordx4 a[196:199], v240, s[20:23], s53 offen offset:3072 sc1\n"
    "v_mfma_f32_16x16x32_bf16 v[196:199], a[204:207], a[184:187], v[196:199]\n"
    "s_addk_i32 s53, 4096\n"
    "v_add_f32_dpp v254, v252, v25 row_newbcast:9 row_mask:0xf bank_mask:0xf\n"
    "v_add_f32_dpp v255, v252, v27 row_newbcast:11 row_mask:0xf bank_mask:0xf\n"
    "v_mfma_f32_16x16x32_bf16 v[196:199], a[200:203], a[152:155], v[196:199]\n"
    "v_mul_f32 v245, v245, v254\n"
    "v_mul_f32 v247, v247, v255\n"
    "v_mfma_f32_16x16x32_bf16 v[164:167], a[200:203], a[148:151], v[164:167]\n"
    "v_cvt_pk_bf16_f32 v248, v244, v245\n"
    "v_cvt_pk_bf16_f32 v249, v246, v247\n"
    "v_mfma_f32_16x16x32_bf16 v[164:167], a[204:207], a[180:183], v[164:167]\n"
    "v_accvgpr_read_b32 v245, a98\n"
    "v_accvgpr_read_b32 v247, a99\n"
    "v_mfma_f32_16x16x32_bf16 v[132:135], a[204:207], a[176:179], v[132:135]\n"
    "v_lshlrev_b32 v244, 16, v245\n"
    "v_lshlrev_b32 v246, 16, v247\n"
    "v_mfma_f32_16x16x32_bf16 v[132:135], a[200:203], a[144:147], v[132:135]\n"
    "v_and_b32 v245, s58, v245\n"
    "v_and_b32 v247, s58, v247\n"
    "v_mfma_f32_16x16x32_bf16 v[100:103], a[200:203], a[140:143], v[100:103]\n"
    "s_xor_b32 s91, s91, 0x195800\n"
    "v_add_f32_dpp v254, v252, v28 row_newbcast:12 row_mask:0xf bank_mask:0xf\n"
    "v_add_f32_dpp v255, v252, v30 row_newbcast:14 row_mask:0xf bank_mask:0xf\n"
    "v_mfma_f32_16x16x32_bf16 v[100:103], a[204:207], a[172:175], v[100:103]\n"
    "v_mul_f32 v244, v244, v254\n"
    "v_mul_f32 v246, v246, v255\n"
    "v_mfma_f32_16x16x32_bf16 v[68:71], a[204:207], a[168:171], v[68:71]\n"
    "v_add_f32_dpp v254, v252, v29 row_newbcast:13 row_mask:0xf bank_mask:0xf\n"
    "v_add_f32_dpp v255, v252, v31 row_newbcast:15 row_mask:0xf bank_mask:0xf\n"
    "v_mfma_f32_16x16x32_bf16 v[68:71], a[200:203], a[136:139], v[68:71]\n"
    "v_mul_f32 v245, v245, v254\n"
    "v_mul_f32 v247, v247, v255\n"
    "v_mfma_f32_16x16x32_bf16 v[36:39], a[200:203], a[132:135], v[36:39]\n"
    "v_cvt_pk_bf16_f32 v250, v244, v245\n"
    "v_cvt_pk_bf16_f32 v251, v246, v247\n"
    "v_mfma_f32_16x16x32_bf16 v[36:39], a[204:207], a[164:167], v[36:39]\n"
    "v_accvgpr_read_b32 v245, a100\n"
    "v_accvgpr_read_b32 v247, a101\n"
    "v_mfma_f32_16x16x32_bf16 v[4:7], a[204:207], a[160:163], v[4:7]\n"
    "v_lshlrev_b32 v244, 16, v245\n"
    "v_lshlrev_b32 v246, 16, v247\n"
    "v_mfma_f32_16x16x32_bf16 v[4:7], a[200:203], a[128:131], v[4:7]\n"
    "s_waitcnt vmcnt(8)\n"
    "v_and_b32 v245, s58, v245\n"
    "v_and_b32 v247, s58, v247\n"
    "v_mfma_f32_16x16x32_bf16 v[8:11], a[212:215], a[128:131], v[8:11]\n"
    "buffer_load_dwordx4 a[200:203], v240, s[20:23], s53 offen offset:0 sc1\n"
    "v_mfma_f32_16x16x32_bf16 v[8:11], a[216:219], a[160:163], v[8:11]\n"
    "buffer_load_dwordx4 a[204:207], v240, s[20:23], s53 offen offset:1024 sc1\n"
    "v_mfma_f32_16x16x32_bf16 v[40:43], a[216:219], a[164:167], v[40:43]\n"
    "buffer_load_dwordx4 a[208:211], v240, s[20:23], s53 offen offset:2048 sc1\n"
    "v_mfma_f32_16x16x32_bf16 v[40:43], a[212:215], a[132:135], v[40:43]\n"
    "v_add_f32_dpp v254, v252, v56 row_newbcast:8 row_mask:0xf bank_mask:0xf\n"
    "v_add_f32_dpp v255, v252, v58 row_newbcast:10 row_mask:0xf bank_mask:0xf\n"
    "v_mfma_f32_16x16x32_bf16 v[72:75], a[212:215], a[136:139], v[72:75]\n"
    "v_mul_f32 v244, v244, v254\n"
    "v_mul_f32 v246, v246, v255\n"
    "v_mfma_f32_16x16x32_bf16 v[72:75], a[216:219], a[168:171], v[72:75]\n"
    "v_add_f32_dpp v254, v252, v57 row_newbcast:9 row_mask:0xf bank_mask:0xf\n"
    "v_add_f32_dpp v255, v252, v59 row_newbcast:11 row_mask:0xf bank_mask:0xf\n"
    "v_mfma_f32_16x16x32_bf16 v[104:107], a[216:219], a[172:175], v[104:107]\n"
    "v_mul_f32 v245, v245, v254\n"
    "v_mul_f32 v247, v247, v255\n"
    "v_mfma_f32_16x16x32_bf16 v[104:107], a[212:215], a[140:143], v[104:107]\n"
    "buffer_store_dwordx4 v[248:251], v240, s[32:35], s91 offen sc1\n"
    "ds_write_b128 v243, v[248:251] offset:32768\n"
    "v_mfma_f32_16x16x32_bf16 v[136:139], a[212:215], a[144:147], v[136:139]\n"
    "v_cvt_pk_bf16_f32 v248, v244, v245\n"
    "v_cvt_pk_bf16_f32 v249, v246, v247\n"
    "v_mfma_f32_16x16x32_bf16 v[136:139], a[216:219], a[176:179], v[136:139]\n"
    "v_accvgpr_read_b32 v245, a102\n"
    "v_accvgpr_read_b32 v247, a103\n"
    "v_mfma_f32_16x16x32_bf16 v[168:171], a[216:219], a[180:183], v[168:171]\n"
    "v_lshlrev_b32 v244, 16, v245\n"
    "v_lshlrev_b32 v246, 16, v247\n"
    "v_mfma_f32_16x16x32_bf16 v[168:171], a[212:215], a[148:151], v[168:171]\n"
    "v_and_b32 v245, s58, v245\n"
    "v_and_b32 v247, s58, v247\n"
    "v_mfma_f32_16x16x32_bf16 v[200:203], a[212:215], a[152:155], v[200:203]\n"
    "s_xor_b32 s91, s91, 0x800\n"
    "v_add_f32_dpp v254, v252, v60 row_newbcast:12 row_mask:0xf bank_mask:0xf\n"
    "v_add_f32_dpp v255, v252, v62 row_newbcast:14 row_mask:0xf bank_mask:0xf\n"
    "v_mfma_f32_16x16x32_bf16 v[200:203], a[216:219], a[184:187], v[200:203]\n"
    "v_mul_f32 v244, v244, v254\n"
    "s_waitcnt lgkmcnt(0)\n"
    "s_barrier\n"
    "v_mfma_f32_16x16x32_bf16 v[232:235], a[216:219], a[188:191], v[232:235]\n"
    "v_mul_f32 v246, v246, v255\n"
    "v_add_f32_dpp v254, v252, v61 row_newbcast:13 row_mask:0xf bank_mask:0xf\n"
    "v_mfma_f32_16x16x32_bf16 v[232:235], a[212:215], a[156:159], v[232:235]\n"
    "s_waitcnt vmcnt(10)\n"
    "v_add_f32_dpp v255, v252, v63 row_newbcast:15 row_mask:0xf bank_mask:0xf\n"
    "v_mul_f32 v245, v245, v254\n"
    "v_mfma_f32_16x16x32_bf16 v[236:239], a[220:223], a[156:159], v[236:239]\n"
    "buffer_load_dwordx4 a[212:215], v240, s[20:23], s53 offen offset:3072 sc1\n"
    "v_mfma_f32_16x16x32_bf16 v[236:239], a[224:227], a[188:191], v[236:239]\n"
    "s_addk_i32 s53, 4096\n"
    "v_mul_f32 v247, v247, v255\n"
    "v_cvt_pk_bf16_f32 v250, v244, v245\n"
    "v_mfma_f32_16x16x32_bf16 v[204:207], a[224:227], a[184:187], v[204:207]\n"
    "v_add_u32 v241, s66, v240\n"
    "ds_read_b128 a[156:159], v241 offset:14336\n"
    "v_mfma_f32_16x16x32_bf16 v[204:207], a[220:223], a[152:155], v[204:207]\n"
    "ds_read_b128 a[188:191], v241 offset:15360\n"
    "v_cvt_pk_bf16_f32 v251, v246, v247\n"
    "v_mfma_f32_16x16x32_bf16 v[172:175], a[220:223], a[148:151], v[172:175]\n"
    "ds_read_b128 a[184:187], v241 offset:13312\n"
    "v_accvgpr_read_b32 v245, a104\n"
    "v_mfma_f32_16x16x32_bf16 v[172:175], a[224:227], a[180:183], v[172:175]\n"
    "ds_read_b128 a[152:155], v241 offset:12288\n"
    "v_accvgpr_read_b32 v247, a105\n"
    "v_mfma_f32_16x16x32_bf16 v[140:143], a[224:227], a[176:179], v[140:143]\n"
    "ds_read_b128 a[148:151], v241 offset:10240\n"
    "v_lshlrev_b32 v244, 16, v245\n"
    "v_mfma_f32_16x16x32_bf16 v[140:143], a[220:223], a[144:147], v[140:143]\n"
    "ds_read_b128 a[180:183], v241 offset:11264\n"
    "v_lshlrev_b32 v246, 16, v247\n"
    "v_mfma_f32_16x16x32_bf16 v[108:111], a[220:223], a[140:143], v[108:111]\n"
    "ds_read_b128 a[176:179], v241 offset:9216\n"
    "v_and_b32 v245, s58, v245\n"
    "v_mfma_f32_16x16x32_bf16 v[108:111], a[224:227], a[172:175], v[108:111]\n"
    "ds_read_b128 a[144:147], v241 offset:8192\n"
    "v_and_b32 v247, s58, v247\n"
    "v_mfma_f32_16x16x32_bf16 v[76:79], a[224:227], a[168:171], v[76:79]\n"
    "ds_read_b128 a[140:143], v241 offset:6144\n"
    "v_add_f32_dpp v254, v252, v88 row_newbcast:8 row_mask:0xf bank_mask:0xf\n"
    "v_mfma_f32_16x16x32_bf16 v[76:79], a[220:223], a[136:139], v[76:79]\n"
    "ds_read_b128 a[172:175], v241 offset:7168\n"
    "v_add_f32_dpp v255, v252, v90 row_newbcast:10 row_mask:0xf bank_mask:0xf\n"
    "v_mfma_f32_16x16x32_bf16 v[44:47], a[220:223], a[132:135], v[44:47]\n"
    "ds_read_b128 a[168:171], v241 offset:5120\n"
    "v_mul_f32 v244, v244, v254\n"
    "v_mfma_f32_16x16x32_bf16 v[44:47], a[224:227], a[164:167], v[44:47]\n"
    "ds_read_b128 a[136:139], v241 offset:4096\n"
    "v_mul_f32 v246, v246, v255\n"
    "v_mfma_f32_16x16x32_bf16 v[12:15], a[224:227], a[160:163], v[12:15]\n"
    "ds_read_b128 a[132:135], v241 offset:2048\n"
    "v_add_f32_dpp v254, v252, v89 row_newbcast:9 row_mask:0xf bank_mask:0xf\n"
    "v_mfma_f32_16x16x32_bf16 v[12:15], a[220:223], a[128:131], v[12:15]\n"
    "s_waitcnt vmcnt(8) lgkmcnt(11)\n"
    "ds_read_b128 a[164:167], v241 offset:3072\n"
    "v_add_f32_dpp v255, v252, v91 row_newbcast:11 row_mask:0xf bank_mask:0xf\n"
    "v_mfma_f32_16x16x32_bf16 v[224:227], a[228:231], a[156:159], v[224:227]\n"
    "buffer_load_dwordx4 a[220:223], v240, s[20:23], s53 offen offset:0 sc1\n"
    "v_mfma_f32_16x16x32_bf16 v[224:227], a[232:235], a[188:191], v[224:227]\n"
    "s_waitcnt lgkmcnt(10)\n"
    "v_mul_f32 v245, v245, v254\n"
    "v_mul_f32 v247, v247, v255\n"
    "v_mfma_f32_16x16x32_bf16 v[192:195], a[232:235], a[184:187], v[192:195]\n"
    "buffer_load_dwordx4 a[224:227], v240, s[20:23], s53 offen offset:1024 sc1\n"
    "v_mfma_f32_16x16x32_bf16 v[192:195], a[228:231], a[152:155], v[192:195]\n"
    "s_waitcnt lgkmcnt(8)\n"
    "ds_read_b128 a[160:163], v241 offset:1024\n"
    "ds_read_b128 a[128:131], v241 offset:0\n"
    "v_mfma_f32_16x16x32_bf16 v[160:163], a[228:231], a[148:151], v[160:163]\n"
    "v_mfma_f32_16x16x32_bf16 v[160:163], a[232:235], a[180:183], v[160:163]\n"
    "s_waitcnt lgkmcnt(8)\n"
    "v_mfma_f32_16x16x32_bf16 v[128:131], a[232:235], a[176:179], v[128:131]\n"
    "buffer_store_dwordx4 v[248:251], v240, s[32:35], s91 offen sc1\n"
    "ds_write_b128 v243, v[248:251] offset:34816\n"
    "v_mfma_f32_16x16x32_bf16 v[128:131], a[228:231], a[144:147], v[128:131]\n"
    "s_waitcnt lgkmcnt(7)\n"
    "v_cvt_pk_bf16_f32 v248, v244, v245\n"
    "v_cvt_pk_bf16_f32 v249, v246, v247\n"
    "v_mfma_f32_16x16x32_bf16 v[96:99], a[228:231], a[140:143], v[96:99]\n"
    "v_accvgpr_read_b32 v245, a106\n"
    "v_accvgpr_read_b32 v247, a107\n"
    "v_mfma_f32_16x16x32_bf16 v[96:99], a[232:235], a[172:175], v[96:99]\n"
    "s_waitcnt lgkmcnt(5)\n"
    "v_lshlrev_b32 v244, 16, v245\n"
    "v_lshlrev_b32 v246, 16, v247\n"
    "v_mfma_f32_16x16x32_bf16 v[64:67], a[232:235], a[168:171], v[64:67]\n"
    "v_and_b32 v245, s58, v245\n"
    "v_and_b32 v247, s58, v247\n"
    "v_mfma_f32_16x16x32_bf16 v[64:67], a[228:231], a[136:139], v[64:67]\n"
    "s_waitcnt lgkmcnt(3)\n"
    "s_xor_b32 s91, s91, 0x101800\n"
    "v_add_f32_dpp v254, v252, v92 row_newbcast:12 row_mask:0xf bank_mask:0xf\n"
    "v_mfma_f32_16x16x32_bf16 v[32:35], a[228:231], a[132:135], v[32:35]\n"
    "v_add_f32_dpp v255, v252, v94 row_newbcast:14 row_mask:0xf bank_mask:0xf\n"
    "v_mul_f32 v244, v244, v254\n"
    "v_mfma_f32_16x16x32_bf16 v[32:35], a[232:235], a[164:167], v[32:35]\n"
    "s_waitcnt lgkmcnt(1)\n"
    "v_mul_f32 v246, v246, v255\n"
    "v_add_f32_dpp v254, v252, v93 row_newbcast:13 row_mask:0xf bank_mask:0xf\n"
    "v_mfma_f32_16x16x32_bf16 v[0:3], a[232:235], a[160:163], v[0:3]\n"
    "v_add_f32_dpp v255, v252, v95 row_newbcast:15 row_mask:0xf bank_mask:0xf\n"
    "v_mul_f32 v245, v245, v254\n"
    "v_mfma_f32_16x16x32_bf16 v[0:3], a[228:231], a[128:131], v[0:3]\n"
    "s_waitcnt vmcnt(8)\n"
    "v_mul_f32 v247, v247, v255\n"
    "v_cvt_pk_bf16_f32 v250, v244, v245\n"
    "v_mfma_f32_16x16x32_bf16 v[4:7], a[192:195], a[128:131], v[4:7]\n"
    "buffer_load_dwordx4 a[228:231], v240, s[20:23], s53 offen offset:2048 sc1\n"
    "v_mfma_f32_16x16x32_bf16 v[4:7], a[196:199], a[160:163], v[4:7]\n"
    "buffer_load_dwordx4 a[232:235], v240, s[20:23], s53 offen offset:3072 sc1\n"
    "v_mfma_f32_16x16x32_bf16 v[36:39], a[196:199], a[164:167], v[36:39]\n"
    "s_addk_i32 s53, 4096\n"
    "v_cvt_pk_bf16_f32 v251, v246, v247\n"
    "v_accvgpr_read_b32 v245, a108\n"
    "v_mfma_f32_16x16x32_bf16 v[36:39], a[192:195], a[132:135], v[36:39]\n"
    "buffer_load_dwordx4 a[236:239], v240, s[20:23], s53 offen offset:0 sc1\n"
    "v_mfma_f32_16x16x32_bf16 v[68:71], a[192:195], a[136:139], v[68:71]\n"
    "v_accvgpr_read_b32 v247, a109\n"
    "v_lshlrev_b32 v244, 16, v245\n"
    "v_mfma_f32_16x16x32_bf16 v[68:71], a[196:199], a[168:171], v[68:71]\n"
    "v_lshlrev_b32 v246, 16, v247\n"
    "v_and_b32 v245, s58, v245\n"
    "v_mfma_f32_16x16x32_bf16 v[100:103], a[196:199], a[172:175], v[100:103]\n"
    "v_and_b32 v247, s58, v247\n"
    "v_add_f32_dpp v254, v252, v120 row_newbcast:8 row_mask:0xf bank_mask:0xf\n"
    "v_mfma_f32_16x16x32_bf16 v[100:103], a[192:195], a[140:143], v[100:103]\n"
    "v_add_f32_dpp v255, v252, v122 row_newbcast:10 row_mask:0xf bank_mask:0xf\n"
    "v_mul_f32 v244, v244, v254\n"
    "v_mfma_f32_16x16x32_bf16 v[132:135], a[192:195], a[144:147], v[132:135]\n"
    "v_mul_f32 v246, v246, v255\n"
    "v_add_f32_dpp v254, v252, v121 row_newbcast:9 row_mask:0xf bank_mask:0xf\n"
    "v_mfma_f32_16x16x32_bf16 v[132:135], a[196:199], a[176:179], v[132:135]\n"
    "v_add_f32_dpp v255, v252, v123 row_newbcast:11 row_mask:0xf bank_mask:0xf\n"
    "v_mul_f32 v245, v245, v254\n"
    "v_mfma_f32_16x16x32_bf16 v[164:167], a[196:199], a[180:183], v[164:167]\n"
    "v_mul_f32 v247, v247, v255\n"
    "v_mfma_f32_16x16x32_bf16 v[164:167], a[192:195], a[148:151], v[164:167]\n"
    "buffer_store_dwordx4 v[248:251], v240, s[32:35], s91 offen sc1\n"
    "ds_write_b128 v243, v[248:251] offset:36864\n"
    "v_mfma_f32_16x16x32_bf16 v[196:199], a[192:195], a[152:155], v[196:199]\n"
    "v_cvt_pk_bf16_f32 v248, v244, v245\n"
    "v_cvt_pk_bf16_f32 v249, v246, v247\n"
    "v_mfma_f32_16x16x32_bf16 v[196:199], a[196:199], a[184:187], v[196:199]\n"
    "v_accvgpr_read_b32 v245, a110\n"
    "v_accvgpr_read_b32 v247, a111\n"
    "v_mfma_f32_16x16x32_bf16 v[228:231], a[196:199], a[188:191], v[228:231]\n"
    "v_lshlrev_b32 v244, 16, v245\n"
    "v_lshlrev_b32 v246, 16, v247\n"
    "v_mfma_f32_16x16x32_bf16 v[228:231], a[192:195], a[156:159], v[228:231]\n"
    "s_waitcnt vmcnt(10)\n"
    "v_and_b32 v245, s58, v245\n"
    "v_and_b32 v247, s58, v247\n"
    "v_mfma_f32_16x16x32_bf16 v[232:235], a[200:203], a[156:159], v[232:235]\n"
    "buffer_load_dwordx4 a[192:195], v240, s[20:23], s53 offen offset:1024 sc1\n"
    "v_mfma_f32_16x16x32_bf16 v[232:235], a[204:207], a[188:191], v[232:235]\n"
    "s_xor_b32 s91, s91, 0x800\n"
    "v_add_f32_dpp v254, v252, v124 row_newbcast:12 row_mask:0xf bank_mask:0xf\n"
    "v_add_f32_dpp v255, v252, v126 row_newbcast:14 row_mask:0xf bank_mask:0xf\n"
    "v_mfma_f32_16x16x32_bf16 v[200:203], a[204:207], a[184:187], v[200:203]\n"
    "v_mul_f32 v244, v244, v254\n"
    "v_mul_f32 v246, v246, v255\n"
    "v_mfma_f32_16x16x32_bf16 v[200:203], a[200:203], a[152:155], v[200:203]\n"
    "v_add_f32_dpp v254, v252, v125 row_newbcast:13 row_mask:0xf bank_mask:0xf\n"
    "v_add_f32_dpp v255, v252, v127 row_newbcast:15 row_mask:0xf bank_mask:0xf\n"
    "v_mfma_f32_16x16x32_bf16 v[168:171], a[200:203], a[148:151], v[168:171]\n"
    "v_mul_f32 v245, v245, v254\n"
    "v_mul_f32 v247, v247, v255\n"
    "v_mfma_f32_16x16x32_bf16 v[168:171], a[204:207], a[180:183], v[168:171]\n"
    "v_cvt_pk_bf16_f32 v250, v244, v245\n"
    "v_cvt_pk_bf16_f32 v251, v246, v247\n"
    "v_mfma_f32_16x16x32_bf16 v[136:139], a[204:207], a[176:179], v[136:139]\n"
    "v_accvgpr_read_b32 v245, a112\n"
    "v_accvgpr_read_b32 v247, a113\n"
    "v_mfma_f32_16x16x32_bf16 v[136:139], a[200:203], a[144:147], v[136:139]\n"
    "v_lshlrev_b32 v244, 16, v245\n"
    "v_lshlrev_b32 v246, 16, v247\n"
    "v_mfma_f32_16x16x32_bf16 v[104:107], a[200:203], a[140:143], v[104:107]\n"
    "v_and_b32 v245, s58, v245\n"
    "v_and_b32 v247, s58, v247\n"
    "v_mfma_f32_16x16x32_bf16 v[104:107], a[204:207], a[172:175], v[104:107]\n"
    "v_add_f32_dpp v254, v252, v152 row_newbcast:8 row_mask:0xf bank_mask:0xf\n"
    "v_add_f32_dpp v255, v252, v154 row_newbcast:10 row_mask:0xf bank_mask:0xf\n"
    "v_mfma_f32_16x16x32_bf16 v[72:75], a[204:207], a[168:171], v[72:75]\n"
    "v_mul_f32 v244, v244, v254\n"
    "v_mul_f32 v246, v246, v255\n"
    "v_mfma_f32_16x16x32_bf16 v[72:75], a[200:203], a[136:139], v[72:75]\n"
    "v_add_f32_dpp v254, v252, v153 row_newbcast:9 row_mask:0xf bank_mask:0xf\n"
    "v_add_f32_dpp v255, v252, v155 row_newbcast:11 row_mask:0xf bank_mask:0xf\n"
    "v_mfma_f32_16x16x32_bf16 v[40:43], a[200:203], a[132:135], v[40:43]\n"
    "v_mul_f32 v245, v245, v254\n"
    "v_mul_f32 v247, v247, v255\n"
    "v_mfma_f32_16x16x32_bf16 v[40:43], a[204:207], a[164:167], v[40:43]\n"
    "buffer_store_dwordx4 v[248:251], v240, s[32:35], s91 offen sc1\n"
    "ds_write_b128 v243, v[248:251] offset:38912\n"
    "v_mfma_f32_16x16x32_bf16 v[8:11], a[204:207], a[160:163], v[8:11]\n"
    "v_cvt_pk_bf16_f32 v248, v244, v245\n"
    "v_cvt_pk_bf16_f32 v249, v246, v247\n"
    "v_mfma_f32_16x16x32_bf16 v[8:11], a[200:203], a[128:131], v[8:11]\n"
    "s_waitcnt vmcnt(9)\n"
    "v_accvgpr_read_b32 v245, a114\n"
    "v_accvgpr_read_b32 v247, a115\n"
    "v_mfma_f32_16x16x32_bf16 v[12:15], a[208:211], a[128:131], v[12:15]\n"
    "buffer_load_dwordx4 a[200:203], v240, s[20:23], s53 offen offset:2048 sc1\n"
    "v_mfma_f32_16x16x32_bf16 v[12:15], a[212:215], a[160:163], v[12:15]\n"
    "buffer_load_dwordx4 a[204:207], v240, s[20:23], s53 offen offset:3072 sc1\n"
    "v_mfma_f32_16x16x32_bf16 v[44:47], a[212:215], a[164:167], v[44:47]\n"
    "s_addk_i32 s53, 4096\n"
    "v_lshlrev_b32 v244, 16, v245\n"
    "v_lshlrev_b32 v246, 16, v247\n"
    "v_mfma_f32_16x16x32_bf16 v[44:47], a[208:211], a[132:135], v[44:47]\n"
    "v_and_b32 v245, s58, v245\n"
    "v_and_b32 v247, s58, v247\n"
    "v_mfma_f32_16x16x32_bf16 v[76:79], a[208:211], a[136:139], v[76:79]\n"
    "s_xor_b32 s91, s91, 0x185800\n"
    "v_add_f32_dpp v254, v252, v156 row_newbcast:12 row_mask:0xf bank_mask:0xf\n"
    "v_add_f32_dpp v255, v252, v158 row_newbcast:14 row_mask:0xf bank_mask:0xf\n"
    "v_mfma_f32_16x16x32_bf16 v[76:79], a[212:215], a[168:171], v[76:79]\n"
    "s_mov_b32 s50, 64\n"
    "v_mul_f32 v244, v244, v254\n"
    "v_mul_f32 v246, v246, v255\n"
    "v_mfma_f32_16x16x32_bf16 v[108:111], a[212:215], a[172:175], v[108:111]\n"
    "buffer_load_dword v242, v240, s[48:51], 0 offen offset:0 sc1\n"
    "v_mfma_f32_16x16x32_bf16 v[108:111], a[208:211], a[140:143], v[108:111]\n"
    "v_add_f32_dpp v254, v252, v157 row_newbcast:13 row_mask:0xf bank_mask:0xf\n"
    "v_add_f32_dpp v255, v252, v159 row_newbcast:15 row_mask:0xf bank_mask:0xf\n"
    "v_mfma_f32_16x16x32_bf16 v[140:143], a[208:211], a[144:147], v[140:143]\n"
    "v_mul_f32 v245, v245, v254\n"
    "v_mul_f32 v247, v247, v255\n"
    "v_mfma_f32_16x16x32_bf16 v[140:143], a[212:215], a[176:179], v[140:143]\n"
    "v_cvt_pk_bf16_f32 v250, v244, v245\n"
    "v_cvt_pk_bf16_f32 v251, v246, v247\n"
    "v_mfma_f32_16x16x32_bf16 v[172:175], a[212:215], a[180:183], v[172:175]\n"
    "v_accvgpr_read_b32 v245, a116\n"
    "v_accvgpr_read_b32 v247, a117\n"
    "v_mfma_f32_16x16x32_bf16 v[172:175], a[208:211], a[148:151], v[172:175]\n"
    "v_lshlrev_b32 v244, 16, v245\n"
    "v_lshlrev_b32 v246, 16, v247\n"
    "v_mfma_f32_16x16x32_bf16 v[204:207], a[208:211], a[152:155], v[204:207]\n"
    "v_and_b32 v245, s58, v245\n"
    "v_and_b32 v247, s58, v247\n"
    "v_mfma_f32_16x16x32_bf16 v[204:207], a[212:215], a[184:187], v[204:207]\n"
    "v_add_f32_dpp v254, v252, v184 row_newbcast:8 row_mask:0xf bank_mask:0xf\n"
    "v_add_f32_dpp v255, v252, v186 row_newbcast:10 row_mask:0xf bank_mask:0xf\n"
    "v_mfma_f32_16x16x32_bf16 v[236:239], a[212:215], a[188:191], v[236:239]\n"
    "v_mul_f32 v244, v244, v254\n"
    "v_mul_f32 v246, v246, v255\n"
    "v_mfma_f32_16x16x32_bf16 v[236:239], a[208:211], a[156:159], v[236:239]\n"
    "s_waitcnt vmcnt(10)\n"
    "v_add_f32_dpp v254, v252, v185 row_newbcast:9 row_mask:0xf bank_mask:0xf\n"
    "v_add_f32_dpp v255, v252, v187 row_newbcast:11 row_mask:0xf bank_mask:0xf\n"
    "v_mfma_f32_16x16x32_bf16 a[240:243], a[220:223], a[156:159], a[240:243]\n"
    "buffer_load_dwordx4 a[208:211], v240, s[20:23], s53 offen offset:0 sc1\n"
    "v_mfma_f32_16x16x32_bf16 a[240:243], a[224:227], a[188:191], a[240:243]\n"
    "buffer_load_dwordx4 a[212:215], v240, s[20:23], s53 offen offset:1024 sc1\n"
    "v_mfma_f32_16x16x32_bf16 v[208:211], a[224:227], a[184:187], v[208:211]\n"
    "buffer_load_dwordx4 a[216:219], v240, s[20:23], s53 offen offset:2048 sc1\n"
    "v_mfma_f32_16x16x32_bf16 v[208:211], a[220:223], a[152:155], v[208:211]\n"
    "buffer_store_dwordx4 v[248:251], v240, s[32:35], s91 offen sc1\n"
    "ds_write_b128 v243, v[248:251] offset:40960\n"
    "v_mfma_f32_16x16x32_bf16 v[176:179], a[220:223], a[148:151], v[176:179]\n"
    "v_mul_f32 v245, v245, v254\n"
    "v_mul_f32 v247, v247, v255\n"
    "v_mfma_f32_16x16x32_bf16 v[176:179], a[224:227], a[180:183], v[176:179]\n"
    "v_cvt_pk_bf16_f32 v248, v244, v245\n"
    "v_cvt_pk_bf16_f32 v249, v246, v247\n"
    "v_mfma_f32_16x16x32_bf16 v[144:147], a[224:227], a[176:179], v[144:147]\n"
    "s_xor_b32 s91, s91, 0x800\n"
    "v_accvgpr_read_b32 v245, a118\n"
    "v_accvgpr_read_b32 v247, a119\n"
    "v_mfma_f32_16x16x32_bf16 v[144:147], a[220:223], a[144:147], v[144:147]\n"
    "v_lshlrev_b32 v244, 16, v245\n"
    "v_lshlrev_b32 v246, 16, v247\n"
    "v_mfma_f32_16x16x32_bf16 v[112:115], a[220:223], a[140:143], v[112:115]\n"
    "v_and_b32 v245, s58, v245\n"
    "v_and_b32 v247, s58, v247\n"
    "v_mfma_f32_16x16x32_bf16 v[112:115], a[224:227], a[172:175], v[112:115]\n"
    "v_add_f32_dpp v254, v252, v188 row_newbcast:12 row_mask:0xf bank_mask:0xf\n"
    "v_add_f32_dpp v255, v252, v190 row_newbcast:14 row_mask:0xf bank_mask:0xf\n"
    "v_mfma_f32_16x16x32_bf16 v[80:83], a[224:227], a[168:171], v[80:83]\n"
    "v_mul_f32 v244, v244, v254\n"
    "v_mul_f32 v246, v246, v255\n"
    "v_mfma_f32_16x16x32_bf16 v[80:83], a[220:223], a[136:139], v[80:83]\n"
    "v_add_f32_dpp v254, v252, v189 row_newbcast:13 row_mask:0xf bank_mask:0xf\n"
    "v_add_f32_dpp v255, v252, v191 row_newbcast:15 row_mask:0xf bank_mask:0xf\n"
    "v_mfma_f32_16x16x32_bf16 v[48:51], a[220:223], a[132:135], v[48:51]\n"
    "v_mul_f32 v245, v245, v254\n"
    "v_mul_f32 v247, v247, v255\n"
    "v_mfma_f32_16x16x32_bf16 v[48:51], a[224:227], a[164:167], v[48:51]\n"
    "v_cvt_pk_bf16_f32 v250, v244, v245\n"
    "v_cvt_pk_bf16_f32 v251, v246, v247\n"
    "v_mfma_f32_16x16x32_bf16 v[16:19], a[224:227], a[160:163], v[16:19]\n"
    "v_accvgpr_read_b32 v245, a120\n"
    "v_accvgpr_read_b32 v247, a121\n"
    "v_mfma_f32_16x16x32_bf16 v[16:19], a[220:223], a[128:131], v[16:19]\n"
    "s_waitcnt vmcnt(11)\n"
    "v_lshlrev_b32 v244, 16, v245\n"
    "v_lshlrev_b32 v246, 16, v247\n"
    "v_mfma_f32_16x16x32_bf16 v[20:23], a[228:231], a[128:131], v[20:23]\n"
    "buffer_load_dwordx4 a[220:223], v240, s[20:23], s53 offen offset:3072 sc1\n"
    "v_mfma_f32_16x16x32_bf16 v[20:23], a[232:235], a[160:163], v[20:23]\n"
    "s_addk_i32 s53, 4096\n"
    "v_and_b32 v245, s58, v245\n"
    "v_and_b32 v247, s58, v247\n"
    "v_mfma_f32_16x16x32_bf16 v[52:55], a[232:235], a[164:167], v[52:55]\n"
    "buffer_store_dwordx4 v[248:251], v240, s[32:35], s91 offen sc1\n"
    "ds_write_b128 v243, v[248:251] offset:43008\n"
    "v_mfma_f32_16x16x32_bf16 v[52:55], a[228:231], a[132:135], v[52:55]\n"
    "v_add_f32_dpp v254, v252, v216 row_newbcast:8 row_mask:0xf bank_mask:0xf\n"
    "v_add_f32_dpp v255, v252, v218 row_newbcast:10 row_mask:0xf bank_mask:0xf\n"
    "v_mfma_f32_16x16x32_bf16 v[84:87], a[228:231], a[136:139], v[84:87]\n"
    "s_xor_b32 s91, s91, 0x101800\n"
    "v_mul_f32 v244, v244, v254\n"
    "v_mul_f32 v246, v246, v255\n"
    "v_mfma_f32_16x16x32_bf16 v[84:87], a[232:235], a[168:171], v[84:87]\n"
    "v_add_f32_dpp v254, v252, v217 row_newbcast:9 row_mask:0xf bank_mask:0xf\n"
    "v_add_f32_dpp v255, v252, v219 row_newbcast:11 row_mask:0xf bank_mask:0xf\n"
    "v_mfma_f32_16x16x32_bf16 v[116:119], a[232:235], a[172:175], v[116:119]\n"
    "v_mul_f32 v245, v245, v254\n"
    "v_mul_f32 v247, v247, v255\n"
    "v_mfma_f32_16x16x32_bf16 v[116:119], a[228:231], a[140:143], v[116:119]\n"
    "v_cvt_pk_bf16_f32 v248, v244, v245\n"
    "v_cvt_pk_bf16_f32 v249, v246, v247\n"
    "v_mfma_f32_16x16x32_bf16 v[148:151], a[228:231], a[144:147], v[148:151]\n"
    "v_accvgpr_read_b32 v245, a122\n"
    "v_accvgpr_read_b32 v247, a123\n"
    "v_mfma_f32_16x16x32_bf16 v[148:151], a[232:235], a[176:179], v[148:151]\n"
    "v_lshlrev_b32 v244, 16, v245\n"
    "v_lshlrev_b32 v246, 16, v247\n"
    "v_mfma_f32_16x16x32_bf16 v[180:183], a[232:235], a[180:183], v[180:183]\n"
    "v_and_b32 v245, s58, v245\n"
    "v_and_b32 v247, s58, v247\n"
    "v_mfma_f32_16x16x32_bf16 v[180:183], a[228:231], a[148:151], v[180:183]\n"
    "v_add_f32_dpp v254, v252, v220 row_newbcast:12 row_mask:0xf bank_mask:0xf\n"
    "v_add_f32_dpp v255, v252, v222 row_newbcast:14 row_mask:0xf bank_mask:0xf\n"
    "v_mfma_f32_16x16x32_bf16 v[212:215], a[228:231], a[152:155], v[212:215]\n"
    "v_mul_f32 v244, v244, v254\n"
    "v_mul_f32 v246, v246, v255\n"
    "v_mfma_f32_16x16x32_bf16 v[212:215], a[232:235], a[184:187], v[212:215]\n"
    "v_add_f32_dpp v254, v252, v221 row_newbcast:13 row_mask:0xf bank_mask:0xf\n"
    "v_add_f32_dpp v255, v252, v223 row_newbcast:15 row_mask:0xf bank_mask:0xf\n"
    "v_mfma_f32_16x16x32_bf16 a[244:247], a[232:235], a[188:191], a[244:247]\n"
    "v_mul_f32 v245, v245, v254\n"
    "v_mul_f32 v247, v247, v255\n"
    "v_mfma_f32_16x16x32_bf16 a[244:247], a[228:231], a[156:159], a[244:247]\n"
    "v_cvt_pk_bf16_f32 v250, v244, v245\n"
    "v_cvt_pk_bf16_f32 v251, v246, v247\n"
    "v_accvgpr_read_b32 v245, a124\n"
    "v_accvgpr_read_b32 v247, a125\n"
    "v_lshlrev_b32 v244, 16, v245\n"
    "v_lshlrev_b32 v246, 16, v247\n"
    "v_and_b32 v245, s58, v245\n"
    "v_and_b32 v247, s58, v247\n"
    "v_accvgpr_read_b32 v254, a248\n"
    "v_accvgpr_read_b32 v255, a250\n"
    "s_nop 0\n"
    "v_add_f32_dpp v254, v252, v254 row_newbcast:8 row_mask:0xf bank_mask:0xf\n"
    "v_add_f32_dpp v255, v252, v255 row_newbcast:10 row_mask:0xf bank_mask:0xf\n"
    "v_mul_f32 v244, v244, v254\n"
    "v_mul_f32 v246, v246, v255\n"
    "v_accvgpr_read_b32 v254, a249\n"
    "v_accvgpr_read_b32 v255, a251\n"
    "s_waitcnt vmcnt(10)\n"
    "v_mfma_f32_16x16x32_bf16 a[248:251], a[236:239], a[156:159], a[248:251]\n"
    "buffer_store_dwordx4 v[248:251], v240, s[32:35], s91 offen sc1\n"
    "ds_write_b128 v243, v[248:251] offset:45056\n"
    "v_mfma_f32_16x16x32_bf16 a[248:251], a[192:195], a[188:191], a[248:251]\n"
    "buffer_load_dwordx4 a[228:231], v240, s[20:23], s53 offen offset:0 sc1\n"
    "v_mfma_f32_16x16x32_bf16 v[216:219], a[192:195], a[184:187], v[216:219]\n"
    "buffer_load_dwordx4 a[232:235], v240, s[20:23], s53 offen offset:1024 sc1\n"
    "v_mfma_f32_16x16x32_bf16 v[216:219], a[236:239], a[152:155], v[216:219]\n"
    "v_add_f32_dpp v254, v252, v254 row_newbcast:9 row_mask:0xf bank_mask:0xf\n"
    "v_add_f32_dpp v255, v252, v255 row_newbcast:11 row_mask:0xf bank_mask:0xf\n"
    "v_mfma_f32_16x16x32_bf16 v[184:187], a[236:239], a[148:151], v[184:187]\n"
    "v_mul_f32 v245, v245, v254\n"
    "v_mul_f32 v247, v247, v255\n"
    "v_mfma_f32_16x16x32_bf16 v[184:187], a[192:195], a[180:183], v[184:187]\n"
    "s_xor_b32 s91, s91, 0x800\n"
    "v_cvt_pk_bf16_f32 v248, v244, v245\n"
    "v_cvt_pk_bf16_f32 v249, v246, v247\n"
    "v_mfma_f32_16x16x32_bf16 v[152:155], a[192:195], a[176:179], v[152:155]\n"
    "v_accvgpr_read_b32 v245, a126\n"
    "v_accvgpr_read_b32 v247, a127\n"
    "v_mfma_f32_16x16x32_bf16 v[152:155], a[236:239], a[144:147], v[152:155]\n"
    "v_lshlrev_b32 v244, 16, v245\n"
    "v_lshlrev_b32 v246, 16, v247\n"
    "v_mfma_f32_16x16x32_bf16 v[120:123], a[236:239], a[140:143], v[120:123]\n"
    "v_and_b32 v245, s58, v245\n"
    "v_and_b32 v247, s58, v247\n"
    "v_mfma_f32_16x16x32_bf16 v[120:123], a[192:195], a[172:175], v[120:123]\n"
    "v_accvgpr_read_b32 v254, a252\n"
    "v_accvgpr_read_b32 v255, a254\n"
    "v_mfma_f32_16x16x32_bf16 v[88:91], a[192:195], a[168:171], v[88:91]\n"
    "v_add_f32_dpp v254, v252, v254 row_newbcast:12 row_mask:0xf bank_mask:0xf\n"
    "v_add_f32_dpp v255, v252, v255 row_newbcast:14 row_mask:0xf bank_mask:0xf\n"
    "v_mfma_f32_16x16x32_bf16 v[88:91], a[236:239], a[136:139], v[88:91]\n"
    "v_mul_f32 v244, v244, v254\n"
    "v_mul_f32 v246, v246, v255\n"
    "v_mfma_f32_16x16x32_bf16 v[56:59], a[236:239], a[132:135], v[56:59]\n"
    "v_accvgpr_read_b32 v254, a253\n"
    "v_accvgpr_read_b32 v255, a255\n"
    "v_mfma_f32_16x16x32_bf16 v[56:59], a[192:195], a[164:167], v[56:59]\n"
    "v_add_f32_dpp v254, v252, v254 row_newbcast:13 row_mask:0xf bank_mask:0xf\n"
    "v_add_f32_dpp v255, v252, v255 row_newbcast:15 row_mask:0xf bank_mask:0xf\n"
    "v_mfma_f32_16x16x32_bf16 v[24:27], a[192:195], a[160:163], v[24:27]\n"
    "v_mul_f32 v245, v245, v254\n"
    "v_mul_f32 v247, v247, v255\n"
    "v_mfma_f32_16x16x32_bf16 v[24:27], a[236:239], a[128:131], v[24:27]\n"
    "s_waitcnt vmcnt(10)\n"
    "v_cvt_pk_bf16_f32 v250, v244, v245\n"
    "v_cvt_pk_bf16_f32 v251, v246, v247\n"
    "v_mfma_f32_16x16x32_bf16 v[28:31], a[200:203], a[128:131], v[28:31]\n"
    "buffer_store_dwordx4 v[248:251], v240, s[32:35], s91 offen sc1\n"
    "ds_write_b128 v243, v[248:251] offset:47104\n"
    "v_mfma_f32_16x16x32_bf16 v[28:31], a[204:207], a[160:163], v[28:31]\n"
    "buffer_load_dwordx4 a[236:239], v240, s[20:23], s53 offen offset:2048 sc1\n"
    "v_mfma_f32_16x16x32_bf16 v[60:63], a[204:207], a[164:167], v[60:63]\n"
    "buffer_load_dwordx4 a[192:195], v240, s[20:23], s53 offen offset:3072 sc1\n"
    "v_mfma_f32_16x16x32_bf16 v[60:63], a[200:203], a[132:135], v[60:63]\n"
    "s_addk_i32 s53, 4096\n"
    "v_mfma_f32_16x16x32_bf16 v[92:95], a[200:203], a[136:139], v[92:95]\n"
    "buffer_load_dwordx4 a[196:199], v240, s[20:23], s53 offen offset:0 sc1\n"
    "v_mfma_f32_16x16x32_bf16 v[92:95], a[204:207], a[168:171], v[92:95]\n"
    "ds_read_b128 a[128:131], v241 offset:16384\n"
    "ds_read_b128 a[160:163], v241 offset:17408\n"
    "v_mfma_f32_16x16x32_bf16 v[124:127], a[204:207], a[172:175], v[124:127]\n"
    "ds_read_b128 a[164:167], v241 offset:19456\n"
    "ds_read_b128 a[132:135], v241 offset:18432\n"
    "v_mfma_f32_16x16x32_bf16 v[124:127], a[200:203], a[140:143], v[124:127]\n"
    "ds_read_b128 a[136:139], v241 offset:20480\n"
    "ds_read_b128 a[168:171], v241 offset:21504\n"
    "v_mfma_f32_16x16x32_bf16 v[156:159], a[200:203], a[144:147], v[156:159]\n"
    "ds_read_b128 a[172:175], v241 offset:23552\n"
    "v_mfma_f32_16x16x32_bf16 v[156:159], a[204:207], a[176:179], v[156:159]\n"
    "ds_read_b128 a[140:143], v241 offset:22528\n"
    "v_mfma_f32_16x16x32_bf16 v[188:191], a[204:207], a[180:183], v[188:191]\n"
    "ds_read_b128 a[144:147], v241 offset:24576\n"
    "v_mfma_f32_16x16x32_bf16 v[188:191], a[200:203], a[148:151], v[188:191]\n"
    "ds_read_b128 a[176:179], v241 offset:25600\n"
    "v_mfma_f32_16x16x32_bf16 v[220:223], a[200:203], a[152:155], v[220:223]\n"
    "ds_read_b128 a[180:183], v241 offset:27648\n"
    "v_mfma_f32_16x16x32_bf16 v[220:223], a[204:207], a[184:187], v[220:223]\n"
    "ds_read_b128 a[148:151], v241 offset:26624\n"
    "v_mfma_f32_16x16x32_bf16 a[252:255], a[204:207], a[188:191], a[252:255]\n"
    "ds_read_b128 a[152:155], v241 offset:28672\n"
    "v_mfma_f32_16x16x32_bf16 a[252:255], a[200:203], a[156:159], a[252:255]\n"
    "s_waitcnt vmcnt(11) lgkmcnt(11)\n"
    "ds_read_b128 a[184:187], v241 offset:29696\n"
    "v_mfma_f32_16x16x32_bf16 v[0:3], a[208:211], a[128:131], v[0:3]\n"
    "buffer_load_dwordx4 a[200:203], v240, s[20:23], s53 offen offset:1024 sc1\n"
    "v_mfma_f32_16x16x32_bf16 v[0:3], a[212:215], a[160:163], v[0:3]\n"
    "s_waitcnt lgkmcnt(10)\n"
    "ds_read_b128 a[188:191], v241 offset:31744\n"
    "ds_read_b128 a[156:159], v241 offset:30720\n"
    "v_mfma_f32_16x16x32_bf16 v[32:35], a[212:215], a[164:167], v[32:35]\n"
    "v_mfma_f32_16x16x32_bf16 v[32:35], a[208:211], a[132:135], v[32:35]\n"
    "s_waitcnt lgkmcnt(10)\n"
    "v_mfma_f32_16x16x32_bf16 v[64:67], a[208:211], a[136:139], v[64:67]\n"
    "v_mfma_f32_16x16x32_bf16 v[64:67], a[212:215], a[168:171], v[64:67]\n"
    "s_waitcnt lgkmcnt(8)\n"
    "v_mfma_f32_16x16x32_bf16 v[96:99], a[212:215], a[172:175], v[96:99]\n"
    "v_mfma_f32_16x16x32_bf16 v[96:99], a[208:211], a[140:143], v[96:99]\n"
    "s_waitcnt lgkmcnt(6)\n"
    "v_mfma_f32_16x16x32_bf16 v[128:131], a[208:211], a[144:147], v[128:131]\n"
    "v_mfma_f32_16x16x32_bf16 v[128:131], a[212:215], a[176:179], v[128:131]\n"
    "s_waitcnt lgkmcnt(4)\n"
    "v_mfma_f32_16x16x32_bf16 v[160:163], a[212:215], a[180:183], v[160:163]\n"
    "v_mfma_f32_16x16x32_bf16 v[160:163], a[208:211], a[148:151], v[160:163]\n"
    "s_waitcnt lgkmcnt(2)\n"
    "v_mfma_f32_16x16x32_bf16 v[192:195], a[208:211], a[152:155], v[192:195]\n"
    "v_mfma_f32_16x16x32_bf16 v[192:195], a[212:215], a[184:187], v[192:195]\n"
    "s_waitcnt lgkmcnt(0)\n"
    "v_mfma_f32_16x16x32_bf16 v[224:227], a[212:215], a[188:191], v[224:227]\n"
    "v_mfma_f32_16x16x32_bf16 v[224:227], a[208:211], a[156:159], v[224:227]\n"
    "v_cmp_le_u32 vcc, s52, v242\n"
    "s_nop 1\n"
    "s_and_b32 s63, vcc_lo, 15\n"
    "s_cmp_eq_u32 s63, 15\n"
    "s_cbranch_scc1 LA6_%=\n"
    "s_mov_b32 s69, 0x400000\n"
    "LA7_%=:\n"
    "s_sleep 1\n"
    "buffer_load_dword v242, v240, s[48:51], 0 offen offset:0 sc1\n"
    "s_waitcnt vmcnt(0)\n"
    "v_cmp_le_u32 vcc, s52, v242\n"
    "s_sub_u32 s69, s69, 1\n"
    "s_cbranch_scc1 LA6_%=\n"
    "s_and_b32 s63, vcc_lo, 15\n"
    "s_cmp_eq_u32 s63, 15\n"
    "s_cbranch_scc0 LA7_%=\n"
    "LA6_%=:\n"
    "s_waitcnt vmcnt(9)\n"
    "v_mfma_f32_16x16x32_bf16 v[228:231], a[216:219], a[156:159], v[228:231]\n"
    "v_readfirstlane_b32 s90, v242\n"
    "s_and_b32 s90, s90, 1\n"
    "v_mfma_f32_16x16x32_bf16 v[228:231], a[220:223], a[188:191], v[228:231]\n"
    "s_lshl_b32 s90, s90, 13\n"
    "v_mfma_f32_16x16x32_bf16 v[196:199], a[220:223], a[184:187], v[196:199]\n"
    "buffer_load_dwordx4 a[208:211], v240, s[20:23], s53 offen offset:2048 sc1\n"
    "v_mfma_f32_16x16x32_bf16 v[196:199], a[216:219], a[152:155], v[196:199]\n"
    "buffer_load_dwordx4 a[212:215], v240, s[20:23], s53 offen offset:3072 sc1\n"
    "s_addk_i32 s53, 4096\n"
    "v_mfma_f32_16x16x32_bf16 v[164:167], a[216:219], a[148:151], v[164:167]\n"
    "s_mov_b32 m0, s56\n"
    "s_xor_b32 s90, s90, s59\n"
    "v_mfma_f32_16x16x32_bf16 v[164:167], a[220:223], a[180:183], v[164:167]\n"
    "buffer_load_dwordx4 v240, s[32:35], s90 offen sc1 lds\n"
    "v_mfma_f32_16x16x32_bf16 v[132:135], a[220:223], a[176:179], v[132:135]\n"
    "v_mfma_f32_16x16x32_bf16 v[132:135], a[216:219], a[144:147], v[132:135]\n"
    "v_mfma_f32_16x16x32_bf16 v[100:103], a[216:219], a[140:143], v[100:103]\n"
    "v_mfma_f32_16x16x32_bf16 v[100:103], a[220:223], a[172:175], v[100:103]\n"
    "v_mfma_f32_16x16x32_bf16 v[68:71], a[220:223], a[168:171], v[68:71]\n"
    "v_mfma_f32_16x16x32_bf16 v[68:71], a[216:219], a[136:139], v[68:71]\n"
    "v_mfma_f32_16x16x32_bf16 v[36:39], a[216:219], a[132:135], v[36:39]\n"
    "v_mfma_f32_16x16x32_bf16 v[36:39], a[220:223], a[164:167], v[36:39]\n"
    "v_mfma_f32_16x16x32_bf16 v[4:7], a[220:223], a[160:163], v[4:7]\n"
    "v_mfma_f32_16x16x32_bf16 v[4:7], a[216:219], a[128:131], v[4:7]\n"
    "s_waitcnt vmcnt(8)\n"
    "v_mfma_f32_16x16x32_bf16 v[8:11], a[228:231], a[128:131], v[8:11]\n"
    "buffer_load_dwordx4 a[216:219], v240, s[20:23], s53 offen offset:0 sc1\n"
    "v_mfma_f32_16x16x32_bf16 v[8:11], a[232:235], a[160:163], v[8:11]\n"
    "buffer_load_dwordx4 a[220:223], v240, s[20:23], s53 offen offset:1024 sc1\n"
    "v_mfma_f32_16x16x32_bf16 v[40:43], a[232:235], a[164:167], v[40:43]\n"
    "buffer_load_dwordx4 a[224:227], v240, s[20:23], s53 offen offset:2048 sc1\n"
    "v_mfma_f32_16x16x32_bf16 v[40:43], a[228:231], a[132:135], v[40:43]\n"
    "v_mfma_f32_16x16x32_bf16 v[72:75], a[228:231], a[136:139], v[72:75]\n"
    "v_mfma_f32_16x16x32_bf16 v[72:75], a[232:235], a[168:171], v[72:75]\n"
    "v_mfma_f32_16x16x32_bf16 v[104:107], a[232:235], a[172:175], v[104:107]\n"
    "v_mfma_f32_16x16x32_bf16 v[104:107], a[228:231], a[140:143], v[104:107]\n"
    "v_mfma_f32_16x16x32_bf16 v[136:139], a[228:231], a[144:147], v[136:139]\n"
    "s_addk_i32 m0, 2048\n"
    "s_xor_b32 s90, s90, 0x800\n"
    "v_mfma_f32_16x16x32_bf16 v[136:139], a[232:235], a[176:179], v[136:139]\n"
    "buffer_load_dwordx4 v240, s[32:35], s90 offen sc1 lds\n"
    "v_mfma_f32_16x16x32_bf16 v[168:171], a[232:235], a[180:183], v[168:171]\n"
    "v_mfma_f32_16x16x32_bf16 v[168:171], a[228:231], a[148:151], v[168:171]\n"
    "v_mfma_f32_16x16x32_bf16 v[200:203], a[228:231], a[152:155], v[200:203]\n"
    "v_mfma_f32_16x16x32_bf16 v[200:203], a[232:235], a[184:187], v[200:203]\n"
    "v_mfma_f32_16x16x32_bf16 v[232:235], a[232:235], a[188:191], v[232:235]\n"
    "v_mfma_f32_16x16x32_bf16 v[232:235], a[228:231], a[156:159], v[232:235]\n"
    "s_waitcnt vmcnt(9)\n"
    "v_mfma_f32_16x16x32_bf16 v[236:239], a[236:239], a[156:159], v[236:239]\n"
    "buffer_load_dwordx4 a[228:231], v240, s[20:23], s53 offen offset:3072 sc1\n"
    "v_mfma_f32_16x16x32_bf16 v[236:239], a[192:195], a[188:191], v[236:239]\n"
    "s_addk_i32 s53, 4096\n"
    "v_mfma_f32_16x16x32_bf16 v[204:207], a[192:195], a[184:187], v[204:207]\n"
    "v_mfma_f32_16x16x32_bf16 v[204:207], a[236:239], a[152:155], v[204:207]\n"
    "v_mfma_f32_16x16x32_bf16 v[172:175], a[236:239], a[148:151], v[172:175]\n"
    "v_mfma_f32_16x16x32_bf16 v[172:175], a[192:195], a[180:183], v[172:175]\n"
    "v_mfma_f32_16x16x32_bf16 v[140:143], a[192:195], a[176:179], v[140:143]\n"
    "v_mfma_f32_16x16x32_bf16 v[140:143], a[236:239], a[144:147], v[140:143]\n"
    "v_mfma_f32_16x16x32_bf16 v[108:111], a[236:239], a[140:143], v[108:111]\n"
    "v_mfma_f32_16x16x32_bf16 v[108:111], a[192:195], a[172:175], v[108:111]\n"
    "v_mfma_f32_16x16x32_bf16 v[76:79], a[192:195], a[168:171], v[76:79]\n"
    "v_mfma_f32_16x16x32_bf16 v[76:79], a[236:239], a[136:139], v[76:79]\n"
    "v_mfma_f32_16x16x32_bf16 v[44:47], a[236:239], a[132:135], v[44:47]\n"
    "v_mfma_f32_16x16x32_bf16 v[44:47], a[192:195], a[164:167], v[44:47]\n"
    "v_mfma_f32_16x16x32_bf16 v[12:15], a[192:195], a[160:163], v[12:15]\n"
    "v_mfma_f32_16x16x32_bf16 v[12:15], a[236:239], a[128:131], v[12:15]\n"
    "s_waitcnt vmcnt(8)\n"
    "v_mfma_f32_16x16x32_bf16 v[16:19], a[196:199], a[128:131], v[16:19]\n"
    "buffer_load_dwordx4 a[236:239], v240, s[20:23], s53 offen offset:0 sc1\n"
    "v_mfma_f32_16x16x32_bf16 v[16:19], a[200:203], a[160:163], v[16:19]\n"
    "buffer_load_dwordx4 a[192:195], v240, s[20:23], s53 offen offset:1024 sc1\n"
    "v_mfma_f32_16x16x32_bf16 v[48:51], a[200:203], a[164:167], v[48:51]\n"
    "s_addk_i32 m0, 2048\n"
    "s_xor_b32 s90, s90, 0x101800\n"
    "v_mfma_f32_16x16x32_bf16 v[48:51], a[196:199], a[132:135], v[48:51]\n"
    "buffer_load_dwordx4 v240, s[32:35], s90 offen sc1 lds\n"
    "v_mfma_f32_16x16x32_bf16 v[80:83], a[196:199], a[136:139], v[80:83]\n"
    "v_mfma_f32_16x16x32_bf16 v[80:83], a[200:203], a[168:171], v[80:83]\n"
    "v_mfma_f32_16x16x32_bf16 v[112:115], a[200:203], a[172:175], v[112:115]\n"
    "v_mfma_f32_16x16x32_bf16 v[112:115], a[196:199], a[140:143], v[112:115]\n"
    "v_mfma_f32_16x16x32_bf16 v[144:147], a[196:199], a[144:147], v[144:147]\n"
    "v_mfma_f32_16x16x32_bf16 v[144:147], a[200:203], a[176:179], v[144:147]\n"
    "v_mfma_f32_16x16x32_bf16 v[176:179], a[200:203], a[180:183], v[176:179]\n"
    "v_mfma_f32_16x16x32_bf16 v[176:179], a[196:199], a[148:151], v[176:179]\n"
    "v_mfma_f32_16x16x32_bf16 v[208:211], a[196:199], a[152:155], v[208:211]\n"
    "v_mfma_f32_16x16x32_bf16 v[208:211], a[200:203], a[184:187], v[208:211]\n"
    "v_mfma_f32_16x16x32_bf16 a[240:243], a[200:203], a[188:191], a[240:243]\n"
    "s_add_u32 s62, s52, s64\n"
    "v_mov_b32 v242, s62\n"
    "s_mov_b32 s46, 260\n"
    "v_mfma_f32_16x16x32_bf16 a[240:243], a[196:199], a[156:159], a[240:243]\n"
    "s_waitcnt vmcnt(9)\n"
    "v_mfma_f32_16x16x32_bf16 a[244:247], a[208:211], a[156:159], a[244:247]\n"
    "buffer_store_dword v242, v240, s[44:47], 0 offen offset:256 sc1\n"
    "v_mfma_f32_16x16x32_bf16 a[244:247], a[212:215], a[188:191], a[244:247]\n"
    "buffer_load_dwordx4 a[196:199], v240, s[20:23], s53 offen offset:2048 sc1\n"
    "v_mfma_f32_16x16x32_bf16 v[212:215], a[212:215], a[184:187], v[212:215]\n"
    "buffer_load_dwordx4 a[200:203], v240, s[20:23], s53 offen offset:3072 sc1\n"
    "v_mfma_f32_16x16x32_bf16 v[212:215], a[208:211], a[152:155], v[212:215]\n"
    "s_addk_i32 s53, 4096\n"
    "v_mfma_f32_16x16x32_bf16 v[180:183], a[208:211], a[148:151], v[180:183]\n"
    "buffer_load_dwordx4 a[204:207], v240, s[20:23], s53 offen offset:0 sc1\n"
    "v_mfma_f32_16x16x32_bf16 v[180:183], a[212:215], a[180:183], v[180:183]\n"
    "v_mfma_f32_16x16x32_bf16 v[148:151], a[212:215], a[176:179], v[148:151]\n"
    "v_mfma_f32_16x16x32_bf16 v[148:151], a[208:211], a[144:147], v[148:151]\n"
    "v_mfma_f32_16x16x32_bf16 v[116:119], a[208:211], a[140:143], v[116:119]\n"
    "s_addk_i32 m0, 2048\n"
    "s_xor_b32 s90, s90, 0x800\n"
    "v_mfma_f32_16x16x32_bf16 v[116:119], a[212:215], a[172:175], v[116:119]\n"
    "buffer_load_dwordx4 v240, s[32:35], s90 offen sc1 lds\n"
    "v_mfma_f32_16x16x32_bf16 v[84:87], a[212:215], a[168:171], v[84:87]\n"
    "v_mfma_f32_16x16x32_bf16 v[84:87], a[208:211], a[136:139], v[84:87]\n"
    "v_mfma_f32_16x16x32_bf16 v[52:55], a[208:211], a[132:135], v[52:55]\n"
    "v_mfma_f32_16x16x32_bf16 v[52:55], a[212:215], a[164:167], v[52:55]\n"
    "v_mfma_f32_16x16x32_bf16 v[20:23], a[212:215], a[160:163], v[20:23]\n"
    "v_mfma_f32_16x16x32_bf16 v[20:23], a[208:211], a[128:131], v[20:23]\n"
    "s_waitcnt vmcnt(11)\n"
    "v_mfma_f32_16x16x32_bf16 v[24:27], a[216:219], a[128:131], v[24:27]\n"
    "buffer_load_dwordx4 a[208:211], v240, s[20:23], s53 offen offset:1024 sc1\n"
    "v_mfma_f32_16x16x32_bf16 v[24:27], a[220:223], a[160:163], v[24:27]\n"
    "v_mfma_f32_16x16x32_bf16 v[56:59], a[220:223], a[164:167], v[56:59]\n"
    "v_mfma_f32_16x16x32_bf16 v[56:59], a[216:219], a[132:135], v[56:59]\n"
    "v_mfma_f32_16x16x32_bf16 v[88:91], a[216:219], a[136:139], v[88:91]\n"
    "v_mfma_f32_16x16x32_bf16 v[88:91], a[220:223], a[168:171], v[88:91]\n"
    "v_mfma_f32_16x16x32_bf16 v[120:123], a[220:223], a[172:175], v[120:123]\n"
    "v_mfma_f32_16x16x32_bf16 v[120:123], a[216:219], a[140:143], v[120:123]\n"
    "v_mfma_f32_16x16x32_bf16 v[152:155], a[216:219], a[144:147], v[152:155]\n"
    "v_mfma_f32_16x16x32_bf16 v[152:155], a[220:223], a[176:179], v[152:155]\n"
    "v_mfma_f32_16x16x32_bf16 v[184:187], a[220:223], a[180:183], v[184:187]\n"
    "v_mfma_f32_16x16x32_bf16 v[184:187], a[216:219], a[148:151], v[184:187]\n"
    "v_mfma_f32_16x16x32_bf16 v[216:219], a[216:219], a[152:155], v[216:219]\n"
    "v_mfma_f32_16x16x32_bf16 v[216:219], a[220:223], a[184:187], v[216:219]\n"
    "s_barrier\n"
    "v_mfma_f32_16x16x32_bf16 a[248:251], a[220:223], a[188:191], a[248:251]\n"
    "v_mfma_f32_16x16x32_bf16 a[248:251], a[216:219], a[156:159], a[248:251]\n"
    "s_waitcnt vmcnt(9)\n"
    "v_mfma_f32_16x16x32_bf16 a[252:255], a[224:227], a[156:159], a[252:255]\n"
    "buffer_load_dwordx4 a[216:219], v240, s[20:23], s53 offen offset:2048 sc1\n"
    "v_mfma_f32_16x16x32_bf16 a[252:255], a[228:231], a[188:191], a[252:255]\n"
    "buffer_load_dwordx4 a[220:223], v240, s[20:23], s53 offen offset:3072 sc1\n"
    "v_mfma_f32_16x16x32_bf16 v[220:223], a[228:231], a[184:187], v[220:223]\n"
    "s_addk_i32 s53, 4096\n"
    "ds_read_b128 a[156:159], v241 offset:47104\n"
    "v_mfma_f32_16x16x32_bf16 v[220:223], a[224:227], a[152:155], v[220:223]\n"
    "ds_read_b128 a[188:191], v241 offset:48128\n"
    "v_mfma_f32_16x16x32_bf16 v[188:191], a[224:227], a[148:151], v[188:191]\n"
    "s_addk_i32 m0, 2048\n"
    "s_xor_b32 s90, s90, 0x185800\n"
    "v_mfma_f32_16x16x32_bf16 v[188:191], a[228:231], a[180:183], v[188:191]\n"
    "buffer_load_dwordx4 v240, s[32:35], s90 offen sc1 lds\n"
    "v_mfma_f32_16x16x32_bf16 v[156:159], a[228:231], a[176:179], v[156:159]\n"
    "ds_read_b128 a[184:187], v241 offset:46080\n"
    "ds_read_b128 a[152:155], v241 offset:45056\n"
    "v_mfma_f32_16x16x32_bf16 v[156:159], a[224:227], a[144:147], v[156:159]\n"
    "ds_read_b128 a[148:151], v241 offset:43008\n"
    "ds_read_b128 a[180:183], v241 offset:44032\n"
    "v_mfma_f32_16x16x32_bf16 v[124:127], a[224:227], a[140:143], v[124:127]\n"
    "ds_read_b128 a[176:179], v241 offset:41984\n"
    "v_mfma_f32_16x16x32_bf16 v[124:127], a[228:231], a[172:175], v[124:127]\n"
    "ds_read_b128 a[144:147], v241 offset:40960\n"
    "v_mfma_f32_16x16x32_bf16 v[92:95], a[228:231], a[168:171], v[92:95]\n"
    "ds_read_b128 a[140:143], v241 offset:38912\n"
    "v_mfma_f32_16x16x32_bf16 v[92:95], a[224:227], a[136:139], v[92:95]\n"
    "ds_read_b128 a[172:175], v241 offset:39936\n"
    "v_mfma_f32_16x16x32_bf16 v[60:63], a[224:227], a[132:135], v[60:63]\n"
    "ds_read_b128 a[168:171], v241 offset:37888\n"
    "v_mfma_f32_16x16x32_bf16 v[60:63], a[228:231], a[164:167], v[60:63]\n"
    "ds_read_b128 a[136:139], v241 offset:36864\n"
    "v_mfma_f32_16x16x32_bf16 v[28:31], a[228:231], a[160:163], v[28:31]\n"
    "ds_read_b128 a[132:135], v241 offset:34816\n"
    "v_mfma_f32_16x16x32_bf16 v[28:31], a[224:227], a[128:131], v[28:31]\n"
    "s_waitcnt vmcnt(10) lgkmcnt(11)\n"
    "ds_read_b128 a[164:167], v241 offset:35840\n"
    "v_mfma_f32_16x16x32_bf16 v[224:227], a[236:239], a[156:159], v[224:227]\n"
    "buffer_load_dwordx4 a[224:227], v240, s[20:23], s53 offen offset:0 sc1\n"
    "v_mfma_f32_16x16x32_bf16 v[224:227], a[192:195], a[188:191], v[224:227]\n"
    "s_waitcnt lgkmcnt(10)\n"
    "v_mfma_f32_16x16x32_bf16 v[192:195], a[192:195], a[184:187], v[192:195]\n"
    "buffer_load_dwordx4 a[228:231], v240, s[20:23], s53 offen offset:1024 sc1\n"
    "v_mfma_f32_16x16x32_bf16 v[192:195], a[236:239], a[152:155], v[192:195]\n"
    "s_waitcnt lgkmcnt(8)\n"
    "v_mfma_f32_16x16x32_bf16 v[160:163], a[236:239], a[148:151], v[160:163]\n"
    "buffer_load_dwordx4 a[232:235], v240, s[20:23], s53 offen offset:2048 sc1\n"
    "ds_read_b128 a[160:163], v241 offset:33792\n"
    "ds_read_b128 a[128:131], v241 offset:32768\n"
    "v_mfma_f32_16x16x32_bf16 v[160:163], a[192:195], a[180:183], v[160:163]\n"
    "s_waitcnt lgkmcnt(8)\n"
    "v_mfma_f32_16x16x32_bf16 v[128:131], a[192:195], a[176:179], v[128:131]\n"
    "v_mfma_f32_16x16x32_bf16 v[128:131], a[236:239], a[144:147], v[128:131]\n"
    "s_waitcnt lgkmcnt(6)\n"
    "v_mfma_f32_16x16x32_bf16 v[96:99], a[236:239], a[140:143], v[96:99]\n"
    "v_mfma_f32_16x16x32_bf16 v[96:99], a[192:195], a[172:175], v[96:99]\n"
    "s_waitcnt lgkmcnt(4)\n"
    "v_mfma_f32_16x16x32_bf16 v[64:67], a[192:195], a[168:171], v[64:67]\n"
    "v_mfma_f32_16x16x32_bf16 v[64:67], a[236:239], a[136:139], v[64:67]\n"
    "s_waitcnt lgkmcnt(2)\n"
    "v_mfma_f32_16x16x32_bf16 v[32:35], a[236:239], a[132:135], v[32:35]\n"
    "s_addk_i32 m0, 2048\n"
    "s_xor_b32 s90, s90, 0x800\n"
    "v_mfma_f32_16x16x32_bf16 v[32:35], a[192:195], a[164:167], v[32:35]\n"
    "s_waitcnt lgkmcnt(0)\n"
    "v_mfma_f32_16x16x32_bf16 v[0:3], a[192:195], a[160:163], v[0:3]\n"
    "buffer_load_dwordx4 v240, s[32:35], s90 offen sc1 lds\n"
    "v_mfma_f32_16x16x32_bf16 v[0:3], a[236:239], a[128:131], v[0:3]\n"
    "s_waitcnt vmcnt(10)\n"
    "v_mfma_f32_16x16x32_bf16 v[4:7], a[196:199], a[128:131], v[4:7]\n"
    "buffer_load_dwordx4 a[236:239], v240, s[20:23], s53 offen offset:3072 sc1\n"
    "v_mfma_f32_16x16x32_bf16 v[4:7], a[200:203], a[160:163], v[4:7]\n"
    "s_addk_i32 s53, 4096\n"
    "v_mfma_f32_16x16x32_bf16 v[36:39], a[200:203], a[164:167], v[36:39]\n"
    "buffer_load_dwordx4 a[192:195], v240, s[20:23], s53 offen offset:0 sc1\n"
    "v_mfma_f32_16x16x32_bf16 v[36:39], a[196:199], a[132:135], v[36:39]\n"
    "v_mfma_f32_16x16x32_bf16 v[68:71], a[196:199], a[136:139], v[68:71]\n"
    "v_mfma_f32_16x16x32_bf16 v[68:71], a[200:203], a[168:171], v[68:71]\n"
    "v_mfma_f32_16x16x32_bf16 v[100:103], a[200:203], a[172:175], v[100:103]\n"
    "v_mfma_f32_16x16x32_bf16 v[100:103], a[196:199], a[140:143], v[100:103]\n"
    "v_mfma_f32_16x16x32_bf16 v[132:135], a[196:199], a[144:147], v[132:135]\n"
    "v_mfma_f32_16x16x32_bf16 v[132:135], a[200:203], a[176:179], v[132:135]\n"
    "v_mfma_f32_16x16x32_bf16 v[164:167], a[200:203], a[180:183], v[164:167]\n"
    "v_mfma_f32_16x16x32_bf16 v[164:167], a[196:199], a[148:151], v[164:167]\n"
    "v_mfma_f32_16x16x32_bf16 v[196:199], a[196:199], a[152:155], v[196:199]\n"
    "v_mfma_f32_16x16x32_bf16 v[196:199], a[200:203], a[184:187], v[196:199]\n"
    "v_mfma_f32_16x16x32_bf16 v[228:231], a[200:203], a[188:191], v[228:231]\n"
    "v_mfma_f32_16x16x32_bf16 v[228:231], a[196:199], a[156:159], v[228:231]\n"
    "s_waitcnt vmcnt(9)\n"
    "v_mfma_f32_16x16x32_bf16 v[232:235], a[204:207], a[156:159], v[232:235]\n"
    "buffer_load_dwordx4 a[196:199], v240, s[20:23], s53 offen offset:1024 sc1\n"
    "v_mfma_f32_16x16x32_bf16 v[232:235], a[208:211], a[188:191], v[232:235]\n"
    "buffer_load_dwordx4 a[200:203], v240, s[20:23], s53 offen offset:2048 sc1\n"
    "v_mfma_f32_16x16x32_bf16 v[200:203], a[208:211], a[184:187], v[200:203]\n"
    "v_mfma_f32_16x16x32_bf16 v[200:203], a[204:207], a[152:155], v[200:203]\n"
    "v_mfma_f32_16x16x32_bf16 v[168:171], a[204:207], a[148:151], v[168:171]\n"
    "s_addk_i32 m0, 2048\n"
    "s_xor_b32 s90, s90, 0x101800\n"
    "v_mfma_f32_16x16x32_bf16 v[168:171], a[208:211], a[180:183], v[168:171]\n"
    "buffer_load_dwordx4 v240, s[32:35], s90 offen sc1 lds\n"
    "v_mfma_f32_16x16x32_bf16 v[136:139], a[208:211], a[176:179], v[136:139]\n"
    "v_mfma_f32_16x16x32_bf16 v[136:139], a[204:207], a[144:147], v[136:139]\n"
    "v_mfma_f32_16x16x32_bf16 v[104:107], a[204:207], a[140:143], v[104:107]\n"
    "v_mfma_f32_16x16x32_bf16 v[104:107], a[208:211], a[172:175], v[104:107]\n"
    "v_mfma_f32_16x16x32_bf16 v[72:75], a[208:211], a[168:171], v[72:75]\n"
    "v_mfma_f32_16x16x32_bf16 v[72:75], a[204:207], a[136:139], v[72:75]\n"
    "v_mfma_f32_16x16x32_bf16 v[40:43], a[204:207], a[132:135], v[40:43]\n"
    "v_mfma_f32_16x16x32_bf16 v[40:43], a[208:211], a[164:167], v[40:43]\n"
    "v_mfma_f32_16x16x32_bf16 v[8:11], a[208:211], a[160:163], v[8:11]\n"
    "v_mfma_f32_16x16x32_bf16 v[8:11], a[204:207], a[128:131], v[8:11]\n"
    "s_waitcnt vmcnt(10)\n"
    "v_mfma_f32_16x16x32_bf16 v[12:15], a[216:219], a[128:131], v[12:15]\n"
    "buffer_load_dwordx4 a[204:207], v240, s[20:23], s53 offen offset:3072 sc1\n"
    "v_mfma_f32_16x16x32_bf16 v[12:15], a[220:223], a[160:163], v[12:15]\n"
    "s_addk_i32 s53, 4096\n"
    "v_mfma_f32_16x16x32_bf16 v[44:47], a[220:223], a[164:167], v[44:47]\n"
    "buffer_load_dwordx4 a[208:211], v240, s[20:23], s53 offen offset:0 sc1\n"
    "v_mfma_f32_16x16x32_bf16 v[44:47], a[216:219], a[132:135], v[44:47]\n"
    "buffer_load_dwordx4 a[212:215], v240, s[20:23], s53 offen offset:1024 sc1\n"
    "v_mfma_f32_16x16x32_bf16 v[76:79], a[216:219], a[136:139], v[76:79]\n"
    "v_mfma_f32_16x16x32_bf16 v[76:79], a[220:223], a[168:171], v[76:79]\n"
    "v_mfma_f32_16x16x32_bf16 v[108:111], a[220:223], a[172:175], v[108:111]\n"
    "v_mfma_f32_16x16x32_bf16 v[108:111], a[216:219], a[140:143], v[108:111]\n"
    "v_mfma_f32_16x16x32_bf16 v[140:143], a[216:219], a[144:147], v[140:143]\n"
    "v_mfma_f32_16x16x32_bf16 v[140:143], a[220:223], a[176:179], v[140:143]\n"
    "v_mfma_f32_16x16x32_bf16 v[172:175], a[220:223], a[180:183], v[172:175]\n"
    "v_mfma_f32_16x16x32_bf16 v[172:175], a[216:219], a[148:151], v[172:175]\n"
    "v_mfma_f32_16x16x32_bf16 v[204:207], a[216:219], a[152:155], v[204:207]\n"
    "s_addk_i32 m0, 2048\n"
    "s_xor_b32 s90, s90, 0x800\n"
    "v_mfma_f32_16x16x32_bf16 v[204:207], a[220:223], a[184:187], v[204:207]\n"
    "buffer_load_dwordx4 v240, s[32:35], s90 offen sc1 lds\n"
    "v_mfma_f32_16x16x32_bf16 v[236:239], a[220:223], a[188:191], v[236:239]\n"
    "v_mfma_f32_16x16x32_bf16 v[236:239], a[216:219], a[156:159], v[236:239]\n"
    "s_waitcnt vmcnt(11)\n"
    "v_mfma_f32_16x16x32_bf16 a[240:243], a[224:227], a[156:159], a[240:243]\n"
    "buffer_load_dwordx4 a[216:219], v240, s[20:23], s53 offen offset:2048 sc1\n"
    "v_mfma_f32_16x16x32_bf16 a[240:243], a[228:231], a[188:191], a[240:243]\n"
    "buffer_load_dwordx4 a[220:223], v240, s[20:23], s53 offen offset:3072 sc1\n"
    "v_mfma_f32_16x16x32_bf16 v[208:211], a[228:231], a[184:187], v[208:211]\n"
    "s_addk_i32 s53, 4096\n"
    "v_mfma_f32_16x16x32_bf16 v[208:211], a[224:227], a[152:155], v[208:211]\n"
    "v_mfma_f32_16x16x32_bf16 v[176:179], a[224:227], a[148:151], v[176:179]\n"
    "v_mfma_f32_16x16x32_bf16 v[176:179], a[228:231], a[180:183], v[176:179]\n"
    "v_mfma_f32_16x16x32_bf16 v[144:147], a[228:231], a[176:179], v[144:147]\n"
    "v_mfma_f32_16x16x32_bf16 v[144:147], a[224:227], a[144:147], v[144:147]\n"
    "v_mfma_f32_16x16x32_bf16 v[112:115], a[224:227], a[140:143], v[112:115]\n"
    "v_mfma_f32_16x16x32_bf16 v[112:115], a[228:231], a[172:175], v[112:115]\n"
    "s_bitcmp1_b32 0x404, s52\n"
    "s_cbranch_scc1 OA_%=\n"
    "OB_%=:\n"
    "v_mfma_f32_16x16x32_bf16 v[80:83], a[228:231], a[168:171], v[80:83]\n"
    "v_mfma_f32_16x16x32_bf16 v[80:83], a[224:227], a[136:139], v[80:83]\n"
    "v_mfma_f32_16x16x32_bf16 v[48:51], a[224:227], a[132:135], v[48:51]\n"
    "v_mfma_f32_16x16x32_bf16 v[48:51], a[228:231], a[164:167], v[48:51]\n"
    "v_mfma_f32_16x16x32_bf16 v[16:19], a[228:231], a[160:163], v[16:19]\n"
    "v_mfma_f32_16x16x32_bf16 v[16:19], a[224:227], a[128:131], v[16:19]\n"
    "s_waitcnt vmcnt(10)\n"
    "v_mfma_f32_16x16x32_bf16 v[20:23], a[232:235], a[128:131], v[20:23]\n"
    "buffer_load_dwordx4 a[224:227], v240, s[20:23], s53 offen offset:0 sc1\n"
    "v_mfma_f32_16x16x32_bf16 v[20:23], a[236:239], a[160:163], v[20:23]\n"
    "buffer_load_dwordx4 a[228:231], v240, s[20:23], s53 offen offset:1024 sc1\n"
    "v_mfma_f32_16x16x32_bf16 v[52:55], a[236:239], a[164:167], v[52:55]\n"
    "v_mfma_f32_16x16x32_bf16 v[52:55], a[232:235], a[132:135], v[52:55]\n"
    "v_mfma_f32_16x16x32_bf16 v[84:87], a[232:235], a[136:139], v[84:87]\n"
    "v_mfma_f32_16x16x32_bf16 v[84:87], a[236:239], a[168:171], v[84:87]\n"
    "v_mfma_f32_16x16x32_bf16 v[116:119], a[236:239], a[172:175], v[116:119]\n"
    "v_mfma_f32_16x16x32_bf16 v[116:119], a[232:235], a[140:143], v[116:119]\n"
    "v_mfma_f32_16x16x32_bf16 v[148:151], a[232:235], a[144:147], v[148:151]\n"
    "s_mov_b32 m0, s71\n"
    "s_xor_b32 s90, s90, 0x195800\n"
    "v_mfma_f32_16x16x32_bf16 v[148:151], a[236:239], a[176:179], v[148:151]\n"
    "buffer_load_dwordx4 v240, s[32:35], s90 offen sc1 lds\n"
    "v_mfma_f32_16x16x32_bf16 v[180:183], a[236:239], a[180:183], v[180:183]\n"
    "v_mfma_f32_16x16x32_bf16 v[180:183], a[232:235], a[148:151], v[180:183]\n"
    "v_mfma_f32_16x16x32_bf16 v[212:215], a[232:235], a[152:155], v[212:215]\n"
    "v_mfma_f32_16x16x32_bf16 v[212:215], a[236:239], a[184:187], v[212:215]\n"
    "v_mfma_f32_16x16x32_bf16 a[244:247], a[236:239], a[188:191], a[244:247]\n"
    "v_mfma_f32_16x16x32_bf16 a[244:247], a[232:235], a[156:159], a[244:247]\n"
    "s_waitcnt vmcnt(11)\n"
    "v_mfma_f32_16x16x32_bf16 a[248:251], a[192:195], a[156:159], a[248:251]\n"
    "buffer_load_dwordx4 a[232:235], v240, s[20:23], s53 offen offset:2048 sc1\n"
    "v_mfma_f32_16x16x32_bf16 a[248:251], a[196:199], a[188:191], a[248:251]\n"
    "buffer_load_dwordx4 a[236:239], v240, s[20:23], s53 offen offset:3072 sc1\n"
    "v_mfma_f32_16x16x32_bf16 v[216:219], a[196:199], a[184:187], v[216:219]\n"
    "s_addk_i32 s53, 4096\n"
    "v_mfma_f32_16x16x32_bf16 v[216:219], a[192:195], a[152:155], v[216:219]\n"
    "v_mfma_f32_16x16x32_bf16 v[184:187], a[192:195], a[148:151], v[184:187]\n"
    "v_mfma_f32_16x16x32_bf16 v[184:187], a[196:199], a[180:183], v[184:187]\n"
    "v_mfma_f32_16x16x32_bf16 v[152:155], a[196:199], a[176:179], v[152:155]\n"
    "v_mfma_f32_16x16x32_bf16 v[152:155], a[192:195], a[144:147], v[152:155]\n"
    "v_mfma_f32_16x16x32_bf16 v[120:123], a[192:195], a[140:143], v[120:123]\n"
    "v_mfma_f32_16x16x32_bf16 v[120:123], a[196:199], a[172:175], v[120:123]\n"
    "v_mfma_f32_16x16x32_bf16 v[88:91], a[196:199], a[168:171], v[88:91]\n"
    "v_mfma_f32_16x16x32_bf16 v[88:91], a[192:195], a[136:139], v[88:91]\n"
    "s_addk_i32 m0, 2048\n"
    "s_xor_b32 s90, s90, 0x800\n"
    "v_mfma_f32_16x16x32_bf16 v[56:59], a[192:195], a[132:135], v[56:59]\n"
    "buffer_load_dwordx4 v240, s[32:35], s90 offen sc1 lds\n"
    "v_mfma_f32_16x16x32_bf16 v[56:59], a[196:199], a[164:167], v[56:59]\n"
    "v_mfma_f32_16x16x32_bf16 v[24:27], a[196:199], a[160:163], v[24:27]\n"
    "v_mfma_f32_16x16x32_bf16 v[24:27], a[192:195], a[128:131], v[24:27]\n"
    "s_waitcnt vmcnt(11)\n"
    "v_mfma_f32_16x16x32_bf16 v[28:31], a[200:203], a[128:131], v[28:31]\n"
    "buffer_load_dwordx4 a[192:195], v240, s[20:23], s53 offen offset:0 sc1\n"
    "v_mfma_f32_16x16x32_bf16 v[28:31], a[204:207], a[160:163], v[28:31]\n"
    "buffer_load_dwordx4 a[196:199], v240, s[20:23], s53 offen offset:1024 sc1\n"
    "v_mfma_f32_16x16x32_bf16 v[60:63], a[204:207], a[164:167], v[60:63]\n"
    "ds_read_b128 a[128:131], v241 offset:49152\n"
    "v_mfma_f32_16x16x32_bf16 v[60:63], a[200:203], a[132:135], v[60:63]\n"
    "ds_read_b128 a[160:163], v241 offset:50176\n"
    "v_mfma_f32_16x16x32_bf16 v[92:95], a[200:203], a[136:139], v[92:95]\n"
    "ds_read_b128 a[164:167], v241 offset:52224\n"
    "v_mfma_f32_16x16x32_bf16 v[92:95], a[204:207], a[168:171], v[92:95]\n"
    "ds_read_b128 a[132:135], v241 offset:51200\n"
    "v_mfma_f32_16x16x32_bf16 v[124:127], a[204:207], a[172:175], v[124:127]\n"
    "ds_read_b128 a[136:139], v241 offset:53248\n"
    "v_mfma_f32_16x16x32_bf16 v[124:127], a[200:203], a[140:143], v[124:127]\n"
    "ds_read_b128 a[168:171], v241 offset:54272\n"
    "v_mfma_f32_16x16x32_bf16 v[156:159], a[200:203], a[144:147], v[156:159]\n"
    "ds_read_b128 a[172:175], v241 offset:56320\n"
    "v_mfma_f32_16x16x32_bf16 v[156:159], a[204:207], a[176:179], v[156:159]\n"
    "ds_read_b128 a[140:143], v241 offset:55296\n"
    "v_mfma_f32_16x16x32_bf16 v[188:191], a[204:207], a[180:183], v[188:191]\n"
    "ds_read_b128 a[144:147], v241 offset:57344\n"
    "v_mfma_f32_16x16x32_bf16 v[188:191], a[200:203], a[148:151], v[188:191]\n"
    "ds_read_b128 a[176:179], v241 offset:58368\n"
    "v_mfma_f32_16x16x32_bf16 v[220:223], a[200:203], a[152:155], v[220:223]\n"
    "ds_read_b128 a[180:183], v241 offset:60416\n"
    "v_mfma_f32_16x16x32_bf16 v[220:223], a[204:207], a[184:187], v[220:223]\n"
    "ds_read_b128 a[148:151], v241 offset:59392\n"
    "v_mfma_f32_16x16x32_bf16 a[252:255], a[204:207], a[188:191], a[252:255]\n"
    "s_addk_i32 m0, 2048\n"
    "s_xor_b32 s90, s90, 0x101800\n"
    "v_mfma_f32_16x16x32_bf16 a[252:255], a[200:203], a[156:159], a[252:255]\n"
    "s_waitcnt vmcnt(11) lgkmcnt(10)\n"
    "v_mfma_f32_16x16x32_bf16 v[0:3], a[208:211], a[128:131], v[0:3]\n"
    "buffer_load_dwordx4 v240, s[32:35], s90 offen sc1 lds\n"
    "v_mfma_f32_16x16x32_bf16 v[0:3], a[212:215], a[160:163], v[0:3]\n"
    "s_waitcnt lgkmcnt(8)\n"
    "ds_read_b128 a[152:155], v241 offset:61440\n"
    "ds_read_b128 a[184:187], v241 offset:62464\n"
    "v_mfma_f32_16x16x32_bf16 v[32:35], a[212:215], a[164:167], v[32:35]\n"
    "buffer_load_dwordx4 a[200:203], v240, s[20:23], s53 offen offset:2048 sc1\n"
    "v_mfma_f32_16x16x32_bf16 v[32:35], a[208:211], a[132:135], v[32:35]\n"
    "s_waitcnt lgkmcnt(8)\n"
    "v_mfma_f32_16x16x32_bf16 v[64:67], a[208:211], a[136:139], v[64:67]\n"
    "buffer_load_dwordx4 a[204:207], v240, s[20:23], s53 offen offset:3072 sc1\n"
    "s_addk_i32 s53, 4096\n"
    "ds_read_b128 a[188:191], v241 offset:64512\n"
    "ds_read_b128 a[156:159], v241 offset:63488\n"
    "v_mfma_f32_16x16x32_bf16 v[64:67], a[212:215], a[168:171], v[64:67]\n"
    "s_waitcnt lgkmcnt(8)\n"
    "v_mfma_f32_16x16x32_bf16 v[96:99], a[212:215], a[172:175], v[96:99]\n"
    "v_mfma_f32_16x16x32_bf16 v[96:99], a[208:211], a[140:143], v[96:99]\n"
    "s_waitcnt lgkmcnt(6)\n"
    "v_mfma_f32_16x16x32_bf16 v[128:131], a[208:211], a[144:147], v[128:131]\n"
    "v_mfma_f32_16x16x32_bf16 v[128:131], a[212:215], a[176:179], v[128:131]\n"
    "s_waitcnt lgkmcnt(4)\n"
    "v_mfma_f32_16x16x32_bf16 v[160:163], a[212:215], a[180:183], v[160:163]\n"
    "v_mfma_f32_16x16x32_bf16 v[160:163], a[208:211], a[148:151], v[160:163]\n"
    "s_waitcnt lgkmcnt(2)\n"
    "v_mfma_f32_16x16x32_bf16 v[192:195], a[208:211], a[152:155], v[192:195]\n"
    "v_mfma_f32_16x16x32_bf16 v[192:195], a[212:215], a[184:187], v[192:195]\n"
    "s_waitcnt lgkmcnt(0)\n"
    "v_mfma_f32_16x16x32_bf16 v[224:227], a[212:215], a[188:191], v[224:227]\n"
    "v_mfma_f32_16x16x32_bf16 v[224:227], a[208:211], a[156:159], v[224:227]\n"
    "s_waitcnt vmcnt(11)\n"
    "v_mfma_f32_16x16x32_bf16 v[228:231], a[216:219], a[156:159], v[228:231]\n"
    "buffer_load_dwordx4 a[208:211], v240, s[20:23], s53 offen offset:0 sc1\n"
    "v_mfma_f32_16x16x32_bf16 v[228:231], a[220:223], a[188:191], v[228:231]\n"
    "buffer_load_dwordx4 a[212:215], v240, s[20:23], s53 offen offset:1024 sc1\n"
    "v_mfma_f32_16x16x32_bf16 v[196:199], a[220:223], a[184:187], v[196:199]\n"
    "s_addk_i32 m0, 2048\n"
    "s_xor_b32 s90, s90, 0x800\n"
    "v_mfma_f32_16x16x32_bf16 v[196:199], a[216:219], a[152:155], v[196:199]\n"
    "buffer_load_dwordx4 v240, s[32:35], s90 offen sc1 lds\n"
    "v_mfma_f32_16x16x32_bf16 v[164:167], a[216:219], a[148:151], v[164:167]\n"
    "v_mfma_f32_16x16x32_bf16 v[164:167], a[220:223], a[180:183], v[164:167]\n"
    "v_mfma_f32_16x16x32_bf16 v[132:135], a[220:223], a[176:179], v[132:135]\n"
    "v_mfma_f32_16x16x32_bf16 v[132:135], a[216:219], a[144:147], v[132:135]\n"
    "v_mfma_f32_16x16x32_bf16 v[100:103], a[216:219], a[140:143], v[100:103]\n"
    "v_mfma_f32_16x16x32_bf16 v[100:103], a[220:223], a[172:175], v[100:103]\n"
    "v_mfma_f32_16x16x32_bf16 v[68:71], a[220:223], a[168:171], v[68:71]\n"
    "v_mfma_f32_16x16x32_bf16 v[68:71], a[216:219], a[136:139], v[68:71]\n"
    "v_mfma_f32_16x16x32_bf16 v[36:39], a[216:219], a[132:135], v[36:39]\n"
    "v_mfma_f32_16x16x32_bf16 v[36:39], a[220:223], a[164:167], v[36:39]\n"
    "s_mov_b32 s50, 320\n"
    "v_mfma_f32_16x16x32_bf16 v[4:7], a[220:223], a[160:163], v[4:7]\n"
    "buffer_load_dword v242, v240, s[48:51], 0 offen offset:256 sc1\n"
    "v_mfma_f32_16x16x32_bf16 v[4:7], a[216:219], a[128:131], v[4:7]\n"
    "s_waitcnt vmcnt(13)\n"
    "v_mfma_f32_16x16x32_bf16 v[8:11], a[224:227], a[128:131], v[8:11]\n"
    "buffer_load_dwordx4 a[216:219], v240, s[20:23], s53 offen offset:2048 sc1\n"
    "v_mfma_f32_16x16x32_bf16 v[8:11], a[228:231], a[160:163], v[8:11]\n"
    "buffer_load_dwordx4 a[220:223], v240, s[20:23], s53 offen offset:3072 sc1\n"
    "v_mfma_f32_16x16x32_bf16 v[40:43], a[228:231], a[164:167], v[40:43]\n"
    "s_addk_i32 s53, 4096\n"
    "v_mfma_f32_16x16x32_bf16 v[40:43], a[224:227], a[132:135], v[40:43]\n"
    "v_mfma_f32_16x16x32_bf16 v[72:75], a[224:227], a[136:139], v[72:75]\n"
    "v_mfma_f32_16x16x32_bf16 v[72:75], a[228:231], a[168:171], v[72:75]\n"
    "v_mfma_f32_16x16x32_bf16 v[104:107], a[228:231], a[172:175], v[104:107]\n"
    "v_mfma_f32_16x16x32_bf16 v[104:107], a[224:227], a[140:143], v[104:107]\n"
    "v_mfma_f32_16x16x32_bf16 v[136:139], a[224:227], a[144:147], v[136:139]\n"
    "s_addk_i32 m0, 2048\n"
    "s_xor_b32 s90, s90, 0x185800\n"
    "v_mfma_f32_16x16x32_bf16 v[136:139], a[228:231], a[176:179], v[136:139]\n"
    "buffer_load_dwordx4 v240, s[32:35], s90 offen sc1 lds\n"
    "v_mfma_f32_16x16x32_bf16 v[168:171], a[228:231], a[180:183], v[168:171]\n"
    "v_mfma_f32_16x16x32_bf16 v[168:171], a[224:227], a[148:151], v[168:171]\n"
    "v_mfma_f32_16x16x32_bf16 v[200:203], a[224:227], a[152:155], v[200:203]\n"
    "v_mfma_f32_16x16x32_bf16 v[200:203], a[228:231], a[184:187], v[200:203]\n"
    "v_mfma_f32_16x16x32_bf16 v[232:235], a[228:231], a[188:191], v[232:235]\n"
    "v_mfma_f32_16x16x32_bf16 v[232:235], a[224:227], a[156:159], v[232:235]\n"
    "s_waitcnt vmcnt(13)\n"
    "v_mfma_f32_16x16x32_bf16 v[236:239], a[232:235], a[156:159], v[236:239]\n"
    "buffer_load_dwordx4 a[224:227], v240, s[20:23], s53 offen offset:0 sc1\n"
    "v_mfma_f32_16x16x32_bf16 v[236:239], a[236:239], a[188:191], v[236:239]\n"
    "buffer_load_dwordx4 a[228:231], v240, s[20:23], s53 offen offset:1024 sc1\n"
    "v_mfma_f32_16x16x32_bf16 v[204:207], a[236:239], a[184:187], v[204:207]\n"
    "v_mfma_f32_16x16x32_bf16 v[204:207], a[232:235], a[152:155], v[204:207]\n"
    "v_mfma_f32_16x16x32_bf16 v[172:175], a[232:235], a[148:151], v[172:175]\n"
    "v_mfma_f32_16x16x32_bf16 v[172:175], a[236:239], a[180:183], v[172:175]\n"
    "v_mfma_f32_16x16x32_bf16 v[140:143], a[236:239], a[176:179], v[140:143]\n"
    "v_mfma_f32_16x16x32_bf16 v[140:143], a[232:235], a[144:147], v[140:143]\n"
    "v_mfma_f32_16x16x32_bf16 v[108:111], a[232:235], a[140:143], v[108:111]\n"
    "v_mfma_f32_16x16x32_bf16 v[108:111], a[236:239], a[172:175], v[108:111]\n"
    "v_mfma_f32_16x16x32_bf16 v[76:79], a[236:239], a[168:171], v[76:79]\n"
    "v_mfma_f32_16x16x32_bf16 v[76:79], a[232:235], a[136:139], v[76:79]\n"
    "s_addk_i32 m0, 2048\n"
    "s_xor_b32 s90, s90, 0x800\n"
    "v_mfma_f32_16x16x32_bf16 v[44:47], a[232:235], a[132:135], v[44:47]\n"
    "buffer_load_dwordx4 v240, s[32:35], s90 offen sc1 lds\n"
    "v_mfma_f32_16x16x32_bf16 v[44:47], a[236:239], a[164:167], v[44:47]\n"
    "v_mfma_f32_16x16x32_bf16 v[12:15], a[236:239], a[160:163], v[12:15]\n"
    "v_mfma_f32_16x16x32_bf16 v[12:15], a[232:235], a[128:131], v[12:15]\n"
    "s_waitcnt vmcnt(13)\n"
    "v_mfma_f32_16x16x32_bf16 v[16:19], a[192:195], a[128:131], v[16:19]\n"
    "buffer_load_dwordx4 a[232:235], v240, s[20:23], s53 offen offset:2048 sc1\n"
    "v_mfma_f32_16x16x32_bf16 v[16:19], a[196:199], a[160:163], v[16:19]\n"
    "buffer_load_dwordx4 a[236:239], v240, s[20:23], s53 offen offset:3072 sc1\n"
    "v_mfma_f32_16x16x32_bf16 v[48:51], a[196:199], a[164:167], v[48:51]\n"
    "s_addk_i32 s53, 4096\n"
    "v_mfma_f32_16x16x32_bf16 v[48:51], a[192:195], a[132:135], v[48:51]\n"
    "v_mfma_f32_16x16x32_bf16 v[80:83], a[192:195], a[136:139], v[80:83]\n"
    "v_mfma_f32_16x16x32_bf16 v[80:83], a[196:199], a[168:171], v[80:83]\n"
    "v_mfma_f32_16x16x32_bf16 v[112:115], a[196:199], a[172:175], v[112:115]\n"
    "v_mfma_f32_16x16x32_bf16 v[112:115], a[192:195], a[140:143], v[112:115]\n"
    "v_mfma_f32_16x16x32_bf16 v[144:147], a[192:195], a[144:147], v[144:147]\n"
    "v_mfma_f32_16x16x32_bf16 v[144:147], a[196:199], a[176:179], v[144:147]\n"
    "v_mfma_f32_16x16x32_bf16 v[176:179], a[196:199], a[180:183], v[176:179]\n"
    "v_mfma_f32_16x16x32_bf16 v[176:179], a[192:195], a[148:151], v[176:179]\n"
    "v_mfma_f32_16x16x32_bf16 v[208:211], a[192:195], a[152:155], v[208:211]\n"
    "v_mfma_f32_16x16x32_bf16 v[208:211], a[196:199], a[184:187], v[208:211]\n"
    "v_mfma_f32_16x16x32_bf16 a[240:243], a[196:199], a[188:191], a[240:243]\n"
    "s_addk_i32 m0, 2048\n"
    "s_xor_b32 s90, s90, 0x101800\n"
    "v_mfma_f32_16x16x32_bf16 a[240:243], a[192:195], a[156:159], a[240:243]\n"
    "s_waitcnt vmcnt(12)\n"
    "v_mfma_f32_16x16x32_bf16 a[244:247], a[200:203], a[156:159], a[244:247]\n"
    "buffer_load_dwordx4 v240, s[32:35], s90 offen sc1 lds\n"
    "v_mfma_f32_16x16x32_bf16 a[244:247], a[204:207], a[188:191], a[244:247]\n"
    "buffer_load_dwordx4 a[192:195], v240, s[20:23], s53 offen offset:0 sc1\n"
    "v_mfma_f32_16x16x32_bf16 v[212:215], a[204:207], a[184:187], v[212:215]\n"
    "buffer_load_dwordx4 a[196:199], v240, s[20:23], s53 offen offset:1024 sc1\n"
    "v_mfma_f32_16x16x32_bf16 v[212:215], a[200:203], a[152:155], v[212:215]\n"
    "v_mfma_f32_16x16x32_bf16 v[180:183], a[200:203], a[148:151], v[180:183]\n"
    "v_mfma_f32_16x16x32_bf16 v[180:183], a[204:207], a[180:183], v[180:183]\n"
    "v_mfma_f32_16x16x32_bf16 v[148:151], a[204:207], a[176:179], v[148:151]\n"
    "v_mfma_f32_16x16x32_bf16 v[148:151], a[200:203], a[144:147], v[148:151]\n"
    "v_mfma_f32_16x16x32_bf16 v[116:119], a[200:203], a[140:143], v[116:119]\n"
    "v_mfma_f32_16x16x32_bf16 v[116:119], a[204:207], a[172:175], v[116:119]\n"
    "v_mfma_f32_16x16x32_bf16 v[84:87], a[204:207], a[168:171], v[84:87]\n"
    "v_mfma_f32_16x16x32_bf16 v[84:87], a[200:203], a[136:139], v[84:87]\n"
    "v_mfma_f32_16x16x32_bf16 v[52:55], a[200:203], a[132:135], v[52:55]\n"
    "v_mfma_f32_16x16x32_bf16 v[52:55], a[204:207], a[164:167], v[52:55]\n"
    "v_mfma_f32_16x16x32_bf16 v[20:23], a[204:207], a[160:163], v[20:23]\n"
    "v_mfma_f32_16x16x32_bf16 v[20:23], a[200:203], a[128:131], v[20:23]\n"
    "s_waitcnt vmcnt(13)\n"
    "v_mfma_f32_16x16x32_bf16 v[24:27], a[208:211], a[128:131], v[24:27]\n"
    "buffer_load_dwordx4 a[200:203], v240, s[20:23], s53 offen offset:2048 sc1\n"
    "v_mfma_f32_16x16x32_bf16 v[24:27], a[212:215], a[160:163], v[24:27]\n"
    "buffer_load_dwordx4 a[204:207], v240, s[20:23], s53 offen offset:3072 sc1\n"
    "v_mfma_f32_16x16x32_bf16 v[56:59], a[212:215], a[164:167], v[56:59]\n"
    "s_addk_i32 s53, 4096\n"
    "s_addk_i32 m0, 2048\n"
    "s_xor_b32 s90, s90, 0x800\n"
    "v_mfma_f32_16x16x32_bf16 v[56:59], a[208:211], a[132:135], v[56:59]\n"
    "buffer_load_dwordx4 v240, s[32:35], s90 offen sc1 lds\n"
    "v_mfma_f32_16x16x32_bf16 v[88:91], a[208:211], a[136:139], v[88:91]\n"
    "v_mfma_f32_16x16x32_bf16 v[88:91], a[212:215], a[168:171], v[88:91]\n"
    "v_mfma_f32_16x16x32_bf16 v[120:123], a[212:215], a[172:175], v[120:123]\n"
    "v_mfma_f32_16x16x32_bf16 v[120:123], a[208:211], a[140:143], v[120:123]\n"
    "v_mfma_f32_16x16x32_bf16 v[152:155], a[208:211], a[144:147], v[152:155]\n"
    "v_mfma_f32_16x16x32_bf16 v[152:155], a[212:215], a[176:179], v[152:155]\n"
    "v_mfma_f32_16x16x32_bf16 v[184:187], a[212:215], a[180:183], v[184:187]\n"
    "v_mfma_f32_16x16x32_bf16 v[184:187], a[208:211], a[148:151], v[184:187]\n"
    "v_mfma_f32_16x16x32_bf16 v[216:219], a[208:211], a[152:155], v[216:219]\n"
    "v_mfma_f32_16x16x32_bf16 v[216:219], a[212:215], a[184:187], v[216:219]\n"
    "s_barrier\n"
    "v_mfma_f32_16x16x32_bf16 a[248:251], a[212:215], a[188:191], a[248:251]\n"
    "v_mfma_f32_16x16x32_bf16 a[248:251], a[208:211], a[156:159], a[248:251]\n"
    "s_waitcnt vmcnt(12)\n"
    "v_mfma_f32_16x16x32_bf16 a[252:255], a[216:219], a[156:159], a[252:255]\n"
    "buffer_load_dwordx4 a[208:211], v240, s[20:23], s53 offen offset:0 sc1\n"
    "v_mfma_f32_16x16x32_bf16 a[252:255], a[220:223], a[188:191], a[252:255]\n"
    "buffer_load_dwordx4 a[212:215], v240, s[20:23], s53 offen offset:1024 sc1\n"
    "v_mfma_f32_16x16x32_bf16 v[220:223], a[220:223], a[184:187], v[220:223]\n"
    "v_add_u32 v241, s67, v240\n"
    "ds_read_b128 a[156:159], v241 offset:14336\n"
    "v_mfma_f32_16x16x32_bf16 v[220:223], a[216:219], a[152:155], v[220:223]\n"
    "ds_read_b128 a[188:191], v241 offset:15360\n"
    "v_mfma_f32_16x16x32_bf16 v[188:191], a[216:219], a[148:151], v[188:191]\n"
    "ds_read_b128 a[184:187], v241 offset:13312\n"
    "v_mfma_f32_16x16x32_bf16 v[188:191], a[220:223], a[180:183], v[188:191]\n"
    "ds_read_b128 a[152:155], v241 offset:12288\n"
    "v_mfma_f32_16x16x32_bf16 v[156:159], a[220:223], a[176:179], v[156:159]\n"
    "ds_read_b128 a[148:151], v241 offset:10240\n"
    "v_mfma_f32_16x16x32_bf16 v[156:159], a[216:219], a[144:147], v[156:159]\n"
    "v_cmp_le_u32 vcc, s52, v242\n"
    "s_nop 1\n"
    "s_and_b32 s63, vcc_lo, 15\n"
    "s_cmp_eq_u32 s63, 15\n"
    "s_cbranch_scc1 LA8_%=\n"
    "s_mov_b32 s69, 0x400000\n"
    "LA9_%=:\n"
    "s_sleep 1\n"
    "buffer_load_dword v242, v240, s[48:51], 0 offen offset:256 sc1\n"
    "s_waitcnt vmcnt(0)\n"
    "v_cmp_le_u32 vcc, s52, v242\n"
    "s_sub_u32 s69, s69, 1\n"
    "s_cbranch_scc1 LA8_%=\n"
    "s_and_b32 s63, vcc_lo, 15\n"
    "s_cmp_eq_u32 s63, 15\n"
    "s_cbranch_scc0 LA9_%=\n"
    "LA8_%=:\n"
    "v_mfma_f32_16x16x32_bf16 v[124:127], a[216:219], a[140:143], v[124:127]\n"
    "ds_read_b128 a[180:183], v241 offset:11264\n"
    "s_add_u32 m0, s71, 32768\n"
    "v_mfma_f32_16x16x32_bf16 v[124:127], a[220:223], a[172:175], v[124:127]\n"
    "s_xor_b32 s90, s90, 0x1b5800\n"
    "v_mfma_f32_16x16x32_bf16 v[92:95], a[220:223], a[168:171], v[92:95]\n"
    "buffer_load_dwordx4 v240, s[32:35], s90 offen sc1 lds\n"
    "v_mfma_f32_16x16x32_bf16 v[92:95], a[216:219], a[136:139], v[92:95]\n"
    "ds_read_b128 a[176:179], v241 offset:9216\n"
    "ds_read_b128 a[144:147], v241 offset:8192\n"
    "v_mfma_f32_16x16x32_bf16 v[60:63], a[216:219], a[132:135], v[60:63]\n"
    "ds_read_b128 a[140:143], v241 offset:6144\n"
    "ds_read_b128 a[172:175], v241 offset:7168\n"
    "v_mfma_f32_16x16x32_bf16 v[60:63], a[220:223], a[164:167], v[60:63]\n"
    "ds_read_b128 a[168:171], v241 offset:5120\n"
    "ds_read_b128 a[136:139], v241 offset:4096\n"
    "v_mfma_f32_16x16x32_bf16 v[28:31], a[220:223], a[160:163], v[28:31]\n"
    "ds_read_b128 a[132:135], v241 offset:2048\n"
    "v_mfma_f32_16x16x32_bf16 v[28:31], a[216:219], a[128:131], v[28:31]\n"
    "s_waitcnt vmcnt(12) lgkmcnt(11)\n"
    "ds_read_b128 a[164:167], v241 offset:3072\n"
    "v_mfma_f32_16x16x32_bf16 v[224:227], a[224:227], a[156:159], v[224:227]\n"
    "buffer_load_dwordx4 a[216:219], v240, s[20:23], s53 offen offset:2048 sc1\n"
    "v_mfma_f32_16x16x32_bf16 v[224:227], a[228:231], a[188:191], v[224:227]\n"
    "s_waitcnt lgkmcnt(10)\n"
    "v_mfma_f32_16x16x32_bf16 v[192:195], a[228:231], a[184:187], v[192:195]\n"
    "buffer_load_dwordx4 a[220:223], v240, s[20:23], s53 offen offset:3072 sc1\n"
    "v_mfma_f32_16x16x32_bf16 v[192:195], a[224:227], a[152:155], v[192:195]\n"
    "s_waitcnt lgkmcnt(8)\n"
    "s_addk_i32 s53, 4096\n"
    "ds_read_b128 a[160:163], v241 offset:1024\n"
    "v_mfma_f32_16x16x32_bf16 v[160:163], a[224:227], a[148:151], v[160:163]\n"
    "ds_read_b128 a[128:131], v241 offset:0\n"
    "v_mfma_f32_16x16x32_bf16 v[160:163], a[228:231], a[180:183], v[160:163]\n"
    "s_waitcnt lgkmcnt(8)\n"
    "v_mfma_f32_16x16x32_bf16 v[128:131], a[228:231], a[176:179], v[128:131]\n"
    "v_mfma_f32_16x16x32_bf16 v[128:131], a[224:227], a[144:147], v[128:131]\n"
    "s_waitcnt lgkmcnt(6)\n"
    "v_mfma_f32_16x16x32_bf16 v[96:99], a[224:227], a[140:143], v[96:99]\n"
    "s_addk_i32 m0, 2048\n"
    "s_xor_b32 s90, s90, 0x800\n"
    "v_mfma_f32_16x16x32_bf16 v[96:99], a[228:231], a[172:175], v[96:99]\n"
    "s_waitcnt lgkmcnt(4)\n"
    "v_mfma_f32_16x16x32_bf16 v[64:67], a[228:231], a[168:171], v[64:67]\n"
    "buffer_load_dwordx4 v240, s[32:35], s90 offen sc1 lds\n"
    "v_mfma_f32_16x16x32_bf16 v[64:67], a[224:227], a[136:139], v[64:67]\n"
    "s_waitcnt lgkmcnt(2)\n"
    "v_mfma_f32_16x16x32_bf16 v[32:35], a[224:227], a[132:135], v[32:35]\n"
    "v_mfma_f32_16x16x32_bf16 v[32:35], a[228:231], a[164:167], v[32:35]\n"
    "s_waitcnt lgkmcnt(0)\n"
    "v_mfma_f32_16x16x32_bf16 v[0:3], a[228:231], a[160:163], v[0:3]\n"
    "v_mfma_f32_16x16x32_bf16 v[0:3], a[224:227], a[128:131], v[0:3]\n"
    "s_waitcnt vmcnt(12)\n"
    "v_mfma_f32_16x16x32_bf16 v[4:7], a[232:235], a[128:131], v[4:7]\n"
    "buffer_load_dwordx4 a[224:227], v240, s[20:23], s53 offen offset:0 sc1\n"
    "v_mfma_f32_16x16x32_bf16 v[4:7], a[236:239], a[160:163], v[4:7]\n"
    "buffer_load_dwordx4 a[228:231], v240, s[20:23], s53 offen offset:1024 sc1\n"
    "v_mfma_f32_16x16x32_bf16 v[36:39], a[236:239], a[164:167], v[36:39]\n"
    "v_mfma_f32_16x16x32_bf16 v[36:39], a[232:235], a[132:135], v[36:39]\n"
    "v_mfma_f32_16x16x32_bf16 v[68:71], a[232:235], a[136:139], v[68:71]\n"
    "v_mfma_f32_16x16x32_bf16 v[68:71], a[236:239], a[168:171], v[68:71]\n"
    "v_mfma_f32_16x16x32_bf16 v[100:103], a[236:239], a[172:175], v[100:103]\n"
    "v_mfma_f32_16x16x32_bf16 v[100:103], a[232:235], a[140:143], v[100:103]\n"
    "v_mfma_f32_16x16x32_bf16 v[132:135], a[232:235], a[144:147], v[132:135]\n"
    "s_addk_i32 m0, 2048\n"
    "s_xor_b32 s90, s90, 0x101800\n"
    "v_mfma_f32_16x16x32_bf16 v[132:135], a[236:239], a[176:179], v[132:135]\n"
    "buffer_load_dwordx4 v240, s[32:35], s90 offen sc1 lds\n"
    "v_mfma_f32_16x16x32_bf16 v[164:167], a[236:239], a[180:183], v[164:167]\n"
    "v_mfma_f32_16x16x32_bf16 v[164:167], a[232:235], a[148:151], v[164:167]\n"
    "v_mfma_f32_16x16x32_bf16 v[196:199], a[232:235], a[152:155], v[196:199]\n"
    "v_mfma_f32_16x16x32_bf16 v[196:199], a[236:239], a[184:187], v[196:199]\n"
    "v_mfma_f32_16x16x32_bf16 v[228:231], a[236:239], a[188:191], v[228:231]\n"
    "v_mfma_f32_16x16x32_bf16 v[228:231], a[232:235], a[156:159], v[228:231]\n"
    "s_waitcnt vmcnt(12)\n"
    "v_mfma_f32_16x16x32_bf16 v[232:235], a[192:195], a[156:159], v[232:235]\n"
    "buffer_load_dwordx4 a[232:235], v240, s[20:23], s53 offen offset:2048 sc1\n"
    "v_mfma_f32_16x16x32_bf16 v[232:235], a[196:199], a[188:191], v[232:235]\n"
    "buffer_load_dwordx4 a[236:239], v240, s[20:23], s53 offen offset:3072 sc1\n"
    "v_mfma_f32_16x16x32_bf16 v[200:203], a[196:199], a[184:187], v[200:203]\n"
    "s_addk_i32 s53, 4096\n"
    "v_mfma_f32_16x16x32_bf16 v[200:203], a[192:195], a[152:155], v[200:203]\n"
    "v_mfma_f32_16x16x32_bf16 v[168:171], a[192:195], a[148:151], v[168:171]\n"
    "v_mfma_f32_16x16x32_bf16 v[168:171], a[196:199], a[180:183], v[168:171]\n"
    "v_mfma_f32_16x16x32_bf16 v[136:139], a[196:199], a[176:179], v[136:139]\n"
    "v_mfma_f32_16x16x32_bf16 v[136:139], a[192:195], a[144:147], v[136:139]\n"
    "v_mfma_f32_16x16x32_bf16 v[104:107], a[192:195], a[140:143], v[104:107]\n"
    "s_addk_i32 m0, 2048\n"
    "s_xor_b32 s90, s90, 0x800\n"
    "v_mfma_f32_16x16x32_bf16 v[104:107], a[196:199], a[172:175], v[104:107]\n"
    "buffer_load_dwordx4 v240, s[32:35], s90 offen sc1 lds\n"
    "v_mfma_f32_16x16x32_bf16 v[72:75], a[196:199], a[168:171], v[72:75]\n"
    "v_mfma_f32_16x16x32_bf16 v[72:75], a[192:195], a[136:139], v[72:75]\n"
    "v_mfma_f32_16x16x32_bf16 v[40:43], a[192:195], a[132:135], v[40:43]\n"
    "v_mfma_f32_16x16x32_bf16 v[40:43], a[196:199], a[164:167], v[40:43]\n"
    "v_mfma_f32_16x16x32_bf16 v[8:11], a[196:199], a[160:163], v[8:11]\n"
    "v_mfma_f32_16x16x32_bf16 v[8:11], a[192:195], a[128:131], v[8:11]\n"
    "s_waitcnt vmcnt(13)\n"
    "v_mfma_f32_16x16x32_bf16 v[12:15], a[200:203], a[128:131], v[12:15]\n"
    "buffer_load_dwordx4 a[192:195], v240, s[20:23], s53 offen offset:0 sc1\n"
    "v_mfma_f32_16x16x32_bf16 v[12:15], a[204:207], a[160:163], v[12:15]\n"
    "buffer_load_dwordx4 a[196:199], v240, s[20:23], s53 offen offset:1024 sc1\n"
    "v_mfma_f32_16x16x32_bf16 v[44:47], a[204:207], a[164:167], v[44:47]\n"
    "v_mfma_f32_16x16x32_bf16 v[44:47], a[200:203], a[132:135], v[44:47]\n"
    "v_mfma_f32_16x16x32_bf16 v[76:79], a[200:203], a[136:139], v[76:79]\n"
    "v_mfma_f32_16x16x32_bf16 v[76:79], a[204:207], a[168:171], v[76:79]\n"
    "v_mfma_f32_16x16x32_bf16 v[108:111], a[204:207], a[172:175], v[108:111]\n"
    "v_mfma_f32_16x16x32_bf16 v[108:111], a[200:203], a[140:143], v[108:111]\n"
    "v_mfma_f32_16x16x32_bf16 v[140:143], a[200:203], a[144:147], v[140:143]\n"
    "v_mfma_f32_16x16x32_bf16 v[140:143], a[204:207], a[176:179], v[140:143]\n"
    "v_mfma_f32_16x16x32_bf16 v[172:175], a[204:207], a[180:183], v[172:175]\n"
    "v_mfma_f32_16x16x32_bf16 v[172:175], a[200:203], a[148:151], v[172:175]\n"
    "v_mfma_f32_16x16x32_bf16 v[204:207], a[200:203], a[152:155], v[204:207]\n"
    "v_mfma_f32_16x16x32_bf16 v[204:207], a[204:207], a[184:187], v[204:207]\n"
    "v_mfma_f32_16x16x32_bf16 v[236:239], a[204:207], a[188:191], v[236:239]\n"
    "s_addk_i32 m0, 2048\n"
    "s_xor_b32 s90, s90, 0x185800\n"
    "v_mfma_f32_16x16x32_bf16 v[236:239], a[200:203], a[156:159], v[236:239]\n"
    "s_waitcnt vmcnt(12)\n"
    "v_mfma_f32_16x16x32_bf16 a[240:243], a[208:211], a[156:159], a[240:243]\n"
    "buffer_load_dwordx4 v240, s[32:35], s90 offen sc1 lds\n"
    "v_mfma_f32_16x16x32_bf16 a[240:243], a[212:215], a[188:191], a[240:243]\n"
    "buffer_load_dwordx4 a[200:203], v240, s[20:23], s53 offen offset:2048 sc1\n"
    "v_mfma_f32_16x16x32_bf16 v[208:211], a[212:215], a[184:187], v[208:211]\n"
    "buffer_load_dwordx4 a[204:207], v240, s[20:23], s53 offen offset:3072 sc1\n"
    "v_mfma_f32_16x16x32_bf16 v[208:211], a[208:211], a[152:155], v[208:211]\n"
    "s_addk_i32 s53, 4096\n"
    "v_mfma_f32_16x16x32_bf16 v[176:179], a[208:211], a[148:151], v[176:179]\n"
    "v_mfma_f32_16x16x32_bf16 v[176:179], a[212:215], a[180:183], v[176:179]\n"
    "v_mfma_f32_16x16x32_bf16 v[144:147], a[212:215], a[176:179], v[144:147]\n"
    "v_mfma_f32_16x16x32_bf16 v[144:147], a[208:211], a[144:147], v[144:147]\n"
    "v_mfma_f32_16x16x32_bf16 v[112:115], a[208:211], a[140:143], v[112:115]\n"
    "v_mfma_f32_16x16x32_bf16 v[112:115], a[212:215], a[172:175], v[112:115]\n"
    "v_mfma_f32_16x16x32_bf16 v[80:83], a[212:215], a[168:171], v[80:83]\n"
    "v_mfma_f32_16x16x32_bf16 v[80:83], a[208:211], a[136:139], v[80:83]\n"
    "v_mfma_f32_16x16x32_bf16 v[48:51], a[208:211], a[132:135], v[48:51]\n"
    "v_mfma_f32_16x16x32_bf16 v[48:51], a[212:215], a[164:167], v[48:51]\n"
    "v_mfma_f32_16x16x32_bf16 v[16:19], a[212:215], a[160:163], v[16:19]\n"
    "s_addk_i32 m0, 2048\n"
    "s_xor_b32 s90, s90, 0x800\n"
    "v_mfma_f32_16x16x32_bf16 v[16:19], a[208:211], a[128:131], v[16:19]\n"
    "s_waitcnt vmcnt(12)\n"
    "v_mfma_f32_16x16x32_bf16 v[20:23], a[216:219], a[128:131], v[20:23]\n"
    "buffer_load_dwordx4 v240, s[32:35], s90 offen sc1 lds\n"
    "v_mfma_f32_16x16x32_bf16 v[20:23], a[220:223], a[160:163], v[20:23]\n"
    "buffer_load_dwordx4 a[208:211], v240, s[20:23], s53 offen offset:0 sc1\n"
    "v_mfma_f32_16x16x32_bf16 v[52:55], a[220:223], a[164:167], v[52:55]\n"
    "buffer_load_dwordx4 a[212:215], v240, s[20:23], s53 offen offset:1024 sc1\n"
    "v_mfma_f32_16x16x32_bf16 v[52:55], a[216:219], a[132:135], v[52:55]\n"
    "v_mfma_f32_16x16x32_bf16 v[84:87], a[216:219], a[136:139], v[84:87]\n"
    "v_mfma_f32_16x16x32_bf16 v[84:87], a[220:223], a[168:171], v[84:87]\n"
    "v_mfma_f32_16x16x32_bf16 v[116:119], a[220:223], a[172:175], v[116:119]\n"
    "v_mfma_f32_16x16x32_bf16 v[116:119], a[216:219], a[140:143], v[116:119]\n"
    "v_mfma_f32_16x16x32_bf16 v[148:151], a[216:219], a[144:147], v[148:151]\n"
    "v_mfma_f32_16x16x32_bf16 v[148:151], a[220:223], a[176:179], v[148:151]\n"
    "v_mfma_f32_16x16x32_bf16 v[180:183], a[220:223], a[180:183], v[180:183]\n"
    "v_mfma_f32_16x16x32_bf16 v[180:183], a[216:219], a[148:151], v[180:183]\n"
    "v_mfma_f32_16x16x32_bf16 v[212:215], a[216:219], a[152:155], v[212:215]\n"
    "v_mfma_f32_16x16x32_bf16 v[212:215], a[220:223], a[184:187], v[212:215]\n"
    "v_mfma_f32_16x16x32_bf16 a[244:247], a[220:223], a[188:191], a[244:247]\n"
    "s_addk_i32 m0, 2048\n"
    "s_xor_b32 s90, s90, 0x101800\n"
    "v_mfma_f32_16x16x32_bf16 a[244:247], a[216:219], a[156:159], a[244:247]\n"
    "s_waitcnt vmcnt(12)\n"
    "v_mfma_f32_16x16x32_bf16 a[248:251], a[224:227], a[156:159], a[248:251]\n"
    "buffer_load_dwordx4 v240, s[32:35], s90 offen sc1 lds\n"
    "v_mfma_f32_16x16x32_bf16 a[248:251], a[228:231], a[188:191], a[248:251]\n"
    "buffer_load_dwordx4 a[216:219], v240, s[20:23], s53 offen offset:2048 sc1\n"
    "v_mfma_f32_16x16x32_bf16 v[216:219], a[228:231], a[184:187], v[216:219]\n"
    "buffer_load_dwordx4 a[220:223], v240, s[20:23], s53 offen offset:3072 sc1\n"
    "v_mfma_f32_16x16x32_bf16 v[216:219], a[224:227], a[152:155], v[216:219]\n"
    "s_addk_i32 s53, 4096\n"
    "v_mfma_f32_16x16x32_bf16 v[184:187], a[224:227], a[148:151], v[184:187]\n"
    "v_mfma_f32_16x16x32_bf16 v[184:187], a[228:231], a[180:183], v[184:187]\n"
    "v_mfma_f32_16x16x32_bf16 v[152:155], a[228:231], a[176:179], v[152:155]\n"
    "v_mfma_f32_16x16x32_bf16 v[152:155], a[224:227], a[144:147], v[152:155]\n"
    "v_mfma_f32_16x16x32_bf16 v[120:123], a[224:227], a[140:143], v[120:123]\n"
    "v_mfma_f32_16x16x32_bf16 v[120:123], a[228:231], a[172:175], v[120:123]\n"
    "v_mfma_f32_16x16x32_bf16 v[88:91], a[228:231], a[168:171], v[88:91]\n"
    "v_mfma_f32_16x16x32_bf16 v[88:91], a[224:227], a[136:139], v[88:91]\n"
    "v_mfma_f32_16x16x32_bf16 v[56:59], a[224:227], a[132:135], v[56:59]\n"
    "v_mfma_f32_16x16x32_bf16 v[56:59], a[228:231], a[164:167], v[56:59]\n"
    "v_mfma_f32_16x16x32_bf16 v[24:27], a[228:231], a[160:163], v[24:27]\n"
    "s_addk_i32 m0, 2048\n"
    "s_xor_b32 s90, s90, 0x800\n"
    "v_mfma_f32_16x16x32_bf16 v[24:27], a[224:227], a[128:131], v[24:27]\n"
    "s_waitcnt vmcnt(12)\n"
    "v_mfma_f32_16x16x32_bf16 v[28:31], a[232:235], a[128:131], v[28:31]\n"
    "buffer_load_dwordx4 v240, s[32:35], s90 offen sc1 lds\n"
    "v_mfma_f32_16x16x32_bf16 v[28:31], a[236:239], a[160:163], v[28:31]\n"
    "buffer_load_dwordx4 a[224:227], v240, s[20:23], s53 offen offset:0 sc1\n"
    "v_mfma_f32_16x16x32_bf16 v[60:63], a[236:239], a[164:167], v[60:63]\n"
    "buffer_load_dwordx4 a[228:231], v240, s[20:23], s53 offen offset:1024 sc1\n"
    "v_mfma_f32_16x16x32_bf16 v[60:63], a[232:235], a[132:135], v[60:63]\n"
    "ds_read_b128 a[128:131], v241 offset:16384\n"
    "ds_read_b128 a[160:163], v241 offset:17408\n"
    "v_mfma_f32_16x16x32_bf16 v[92:95], a[232:235], a[136:139], v[92:95]\n"
    "ds_read_b128 a[164:167], v241 offset:19456\n"
    "v_mfma_f32_16x16x32_bf16 v[92:95], a[236:239], a[168:171], v[92:95]\n"
    "ds_read_b128 a[132:135], v241 offset:18432\n"
    "v_mfma_f32_16x16x32_bf16 v[124:127], a[236:239], a[172:175], v[124:127]\n"
    "ds_read_b128 a[136:139], v241 offset:20480\n"
    "v_mfma_f32_16x16x32_bf16 v[124:127], a[232:235], a[140:143], v[124:127]\n"
    "ds_read_b128 a[168:171], v241 offset:21504\n"
    "v_mfma_f32_16x16x32_bf16 v[156:159], a[232:235], a[144:147], v[156:159]\n"
    "ds_read_b128 a[172:175], v241 offset:23552\n"
    "v_mfma_f32_16x16x32_bf16 v[156:159], a[236:239], a[176:179], v[156:159]\n"
    "ds_read_b128 a[140:143], v241 offset:22528\n"
    "v_mfma_f32_16x16x32_bf16 v[188:191], a[236:239], a[180:183], v[188:191]\n"
    "ds_read_b128 a[144:147], v241 offset:24576\n"
    "v_mfma_f32_16x16x32_bf16 v[188:191], a[232:235], a[148:151], v[188:191]\n"
    "ds_read_b128 a[176:179], v241 offset:25600\n"
    "v_mfma_f32_16x16x32_bf16 v[220:223], a[232:235], a[152:155], v[220:223]\n"
    "ds_read_b128 a[180:183], v241 offset:27648\n"
    "v_mfma_f32_16x16x32_bf16 v[220:223], a[236:239], a[184:187], v[220:223]\n"
    "ds_read_b128 a[148:151], v241 offset:26624\n"
    "v_mfma_f32_16x16x32_bf16 a[252:255], a[236:239], a[188:191], a[252:255]\n"
    "ds_read_b128 a[152:155], v241 offset:28672\n"
    "v_mfma_f32_16x16x32_bf16 a[252:255], a[232:235], a[156:159], a[252:255]\n"
    "s_waitcnt vmcnt(12) lgkmcnt(11)\n"
    "ds_read_b128 a[184:187], v241 offset:29696\n"
    "v_mfma_f32_16x16x32_bf16 v[0:3], a[192:195], a[128:131], v[0:3]\n"
    "buffer_load_dwordx4 a[232:235], v240, s[20:23], s53 offen offset:2048 sc1\n"
    "v_mfma_f32_16x16x32_bf16 v[0:3], a[196:199], a[160:163], v[0:3]\n"
    "s_waitcnt lgkmcnt(10)\n"
    "v_mfma_f32_16x16x32_bf16 v[32:35], a[196:199], a[164:167], v[32:35]\n"
    "buffer_load_dwordx4 a[236:239], v240, s[20:23], s53 offen offset:3072 sc1\n"
    "v_mfma_f32_16x16x32_bf16 v[32:35], a[192:195], a[132:135], v[32:35]\n"
    "s_waitcnt lgkmcnt(8)\n"
    "s_addk_i32 s53, 4096\n"
    "ds_read_b128 a[188:191], v241 offset:31744\n"
    "v_mfma_f32_16x16x32_bf16 v[64:67], a[192:195], a[136:139], v[64:67]\n"
    "ds_read_b128 a[156:159], v241 offset:30720\n"
    "v_mfma_f32_16x16x32_bf16 v[64:67], a[196:199], a[168:171], v[64:67]\n"
    "s_waitcnt lgkmcnt(8)\n"
    "v_mfma_f32_16x16x32_bf16 v[96:99], a[196:199], a[172:175], v[96:99]\n"
    "v_mfma_f32_16x16x32_bf16 v[96:99], a[192:195], a[140:143], v[96:99]\n"
    "s_waitcnt lgkmcnt(6)\n"
    "v_mfma_f32_16x16x32_bf16 v[128:131], a[192:195], a[144:147], v[128:131]\n"
    "v_mfma_f32_16x16x32_bf16 v[128:131], a[196:199], a[176:179], v[128:131]\n"
    "s_waitcnt lgkmcnt(4)\n"
    "v_mfma_f32_16x16x32_bf16 v[160:163], a[196:199], a[180:183], v[160:163]\n"
    "v_mfma_f32_16x16x32_bf16 v[160:163], a[192:195], a[148:151], v[160:163]\n"
    "s_waitcnt lgkmcnt(2)\n"
    "v_mfma_f32_16x16x32_bf16 v[192:195], a[192:195], a[152:155], v[192:195]\n"
    "v_mfma_f32_16x16x32_bf16 v[192:195], a[196:199], a[184:187], v[192:195]\n"
    "s_waitcnt lgkmcnt(0)\n"
    "v_mfma_f32_16x16x32_bf16 v[224:227], a[196:199], a[188:191], v[224:227]\n"
    "v_mfma_f32_16x16x32_bf16 v[224:227], a[192:195], a[156:159], v[224:227]\n"
    "s_waitcnt vmcnt(11)\n"
    "v_mfma_f32_16x16x32_bf16 v[228:231], a[200:203], a[156:159], v[228:231]\n"
    "buffer_load_dwordx4 a[192:195], v240, s[20:23], s53 offen offset:0 sc1\n"
    "v_mfma_f32_16x16x32_bf16 v[228:231], a[204:207], a[188:191], v[228:231]\n"
    "buffer_load_dwordx4 a[196:199], v240, s[20:23], s53 offen offset:1024 sc1\n"
    "v_mfma_f32_16x16x32_bf16 v[196:199], a[204:207], a[184:187], v[196:199]\n"
    "v_mfma_f32_16x16x32_bf16 v[196:199], a[200:203], a[152:155], v[196:199]\n"
    "v_mfma_f32_16x16x32_bf16 v[164:167], a[200:203], a[148:151], v[164:167]\n"
    "v_mfma_f32_16x16x32_bf16 v[164:167], a[204:207], a[180:183], v[164:167]\n"
    "v_mfma_f32_16x16x32_bf16 v[132:135], a[204:207], a[176:179], v[132:135]\n"
    "v_mfma_f32_16x16x32_bf16 v[132:135], a[200:203], a[144:147], v[132:135]\n"
    "v_mfma_f32_16x16x32_bf16 v[100:103], a[200:203], a[140:143], v[100:103]\n"
    "buffer_load_dword v253, v240, s[36:39], s55 offen offset:0 sc1\n"
    "v_mfma_f32_16x16x32_bf16 v[100:103], a[204:207], a[172:175], v[100:103]\n"
    "buffer_load_dword v252, v240, s[36:39], s55 offen offset:1024 sc1\n"
    "v_mfma_f32_16x16x32_bf16 v[68:71], a[204:207], a[168:171], v[68:71]\n"
    "v_mfma_f32_16x16x32_bf16 v[68:71], a[200:203], a[136:139], v[68:71]\n"
    "v_mfma_f32_16x16x32_bf16 v[36:39], a[200:203], a[132:135], v[36:39]\n"
    "v_mfma_f32_16x16x32_bf16 v[36:39], a[204:207], a[164:167], v[36:39]\n"
    "v_mfma_f32_16x16x32_bf16 v[4:7], a[204:207], a[160:163], v[4:7]\n"
    "v_mfma_f32_16x16x32_bf16 v[4:7], a[200:203], a[128:131], v[4:7]\n"
    "s_waitcnt vmcnt(12)\n"
    "v_mfma_f32_16x16x32_bf16 v[8:11], a[208:211], a[128:131], v[8:11]\n"
    "buffer_load_dwordx4 a[200:203], v240, s[20:23], s53 offen offset:2048 sc1\n"
    "v_mfma_f32_16x16x32_bf16 v[8:11], a[212:215], a[160:163], v[8:11]\n"
    "buffer_load_dwordx4 a[204:207], v240, s[20:23], s53 offen offset:3072 sc1\n"
    "v_mfma_f32_16x16x32_bf16 v[40:43], a[212:215], a[164:167], v[40:43]\n"
    "s_addk_i32 s53, 4096\n"
    "v_mfma_f32_16x16x32_bf16 v[40:43], a[208:211], a[132:135], v[40:43]\n"
    "v_mfma_f32_16x16x32_bf16 v[72:75], a[208:211], a[136:139], v[72:75]\n"
    "v_mfma_f32_16x16x32_bf16 v[72:75], a[212:215], a[168:171], v[72:75]\n"
    "v_mfma_f32_16x16x32_bf16 v[104:107], a[212:215], a[172:175], v[104:107]\n"
    "v_mfma_f32_16x16x32_bf16 v[104:107], a[208:211], a[140:143], v[104:107]\n"
    "v_mfma_f32_16x16x32_bf16 v[136:139], a[208:211], a[144:147], v[136:139]\n"
    "v_mfma_f32_16x16x32_bf16 v[136:139], a[212:215], a[176:179], v[136:139]\n"
    "v_mfma_f32_16x16x32_bf16 v[168:171], a[212:215], a[180:183], v[168:171]\n"
    "v_mfma_f32_16x16x32_bf16 v[168:171], a[208:211], a[148:151], v[168:171]\n"
    "v_mfma_f32_16x16x32_bf16 v[200:203], a[208:211], a[152:155], v[200:203]\n"
    "v_mfma_f32_16x16x32_bf16 v[200:203], a[212:215], a[184:187], v[200:203]\n"
    "v_mfma_f32_16x16x32_bf16 v[232:235], a[212:215], a[188:191], v[232:235]\n"
    "v_mfma_f32_16x16x32_bf16 v[232:235], a[208:211], a[156:159], v[232:235]\n"
    "s_waitcnt vmcnt(11)\n"
    "v_mfma_f32_16x16x32_bf16 v[236:239], a[216:219], a[156:159], v[236:239]\n"
    "buffer_load_dwordx4 a[208:211], v240, s[20:23], s53 offen offset:0 sc1\n"
    "v_mfma_f32_16x16x32_bf16 v[236:239], a[220:223], a[188:191], v[236:239]\n"
    "buffer_load_dwordx4 a[212:215], v240, s[20:23], s53 offen offset:1024 sc1\n"
    "v_mfma_f32_16x16x32_bf16 v[204:207], a[220:223], a[184:187], v[204:207]\n"
    "v_mfma_f32_16x16x32_bf16 v[204:207], a[216:219], a[152:155], v[204:207]\n"
    "v_mfma_f32_16x16x32_bf16 v[172:175], a[216:219], a[148:151], v[172:175]\n"
    "v_mfma_f32_16x16x32_bf16 v[172:175], a[220:223], a[180:183], v[172:175]\n"
    "v_mfma_f32_16x16x32_bf16 v[140:143], a[220:223], a[176:179], v[140:143]\n"
    "v_mfma_f32_16x16x32_bf16 v[140:143], a[216:219], a[144:147], v[140:143]\n"
    "v_mfma_f32_16x16x32_bf16 v[108:111], a[216:219], a[140:143], v[108:111]\n"
    "v_mfma_f32_16x16x32_bf16 v[108:111], a[220:223], a[172:175], v[108:111]\n"
    "v_mfma_f32_16x16x32_bf16 v[76:79], a[220:223], a[168:171], v[76:79]\n"
    "v_mfma_f32_16x16x32_bf16 v[76:79], a[216:219], a[136:139], v[76:79]\n"
    "v_mfma_f32_16x16x32_bf16 v[44:47], a[216:219], a[132:135], v[44:47]\n"
    "v_mfma_f32_16x16x32_bf16 v[44:47], a[220:223], a[164:167], v[44:47]\n"
    "v_mfma_f32_16x16x32_bf16 v[12:15], a[220:223], a[160:163], v[12:15]\n"
    "v_mfma_f32_16x16x32_bf16 v[12:15], a[216:219], a[128:131], v[12:15]\n"
    "s_waitcnt vmcnt(10)\n"
    "v_mfma_f32_16x16x32_bf16 v[16:19], a[224:227], a[128:131], v[16:19]\n"
    "buffer_load_dwordx4 a[216:219], v240, s[20:23], s53 offen offset:2048 sc1\n"
    "v_mfma_f32_16x16x32_bf16 v[16:19], a[228:231], a[160:163], v[16:19]\n"
    "buffer_load_dwordx4 a[220:223], v240, s[20:23], s53 offen offset:3072 sc1\n"
    "v_mfma_f32_16x16x32_bf16 v[48:51], a[228:231], a[164:167], v[48:51]\n"
    "s_addk_i32 s53, 4096\n"
    "v_mfma_f32_16x16x32_bf16 v[48:51], a[224:227], a[132:135], v[48:51]\n"
    "v_mfma_f32_16x16x32_bf16 v[80:83], a[224:227], a[136:139], v[80:83]\n"
    "v_mfma_f32_16x16x32_bf16 v[80:83], a[228:231], a[168:171], v[80:83]\n"
    "v_mfma_f32_16x16x32_bf16 v[112:115], a[228:231], a[172:175], v[112:115]\n"
    "v_mfma_f32_16x16x32_bf16 v[112:115], a[224:227], a[140:143], v[112:115]\n"
    "v_mfma_f32_16x16x32_bf16 v[144:147], a[224:227], a[144:147], v[144:147]\n"
    "v_mfma_f32_16x16x32_bf16 v[144:147], a[228:231], a[176:179], v[144:147]\n"
    "v_mfma_f32_16x16x32_bf16 v[176:179], a[228:231], a[180:183], v[176:179]\n"
    "v_mfma_f32_16x16x32_bf16 v[176:179], a[224:227], a[148:151], v[176:179]\n"
    "v_mfma_f32_16x16x32_bf16 v[208:211], a[224:227], a[152:155], v[208:211]\n"
    "v_mfma_f32_16x16x32_bf16 v[208:211], a[228:231], a[184:187], v[208:211]\n"
    "v_mfma_f32_16x16x32_bf16 a[240:243], a[228:231], a[188:191], a[240:243]\n"
    "v_mfma_f32_16x16x32_bf16 a[240:243], a[224:227], a[156:159], a[240:243]\n"
    "s_waitcnt vmcnt(10)\n"
    "v_mfma_f32_16x16x32_bf16 a[244:247], a[232:235], a[156:159], a[244:247]\n"
    "buffer_load_dwordx4 a[224:227], v240, s[20:23], s53 offen offset:0 sc1\n"
    "v_mfma_f32_16x16x32_bf16 a[244:247], a[236:239], a[188:191], a[244:247]\n"
    "buffer_load_dwordx4 a[228:231], v240, s[20:23], s53 offen offset:1024 sc1\n"
    "v_mfma_f32_16x16x32_bf16 v[212:215], a[236:239], a[184:187], v[212:215]\n"
    "v_mfma_f32_16x16x32_bf16 v[212:215], a[232:235], a[152:155], v[212:215]\n"
    "v_mfma_f32_16x16x32_bf16 v[180:183], a[232:235], a[148:151], v[180:183]\n"
    "v_mfma_f32_16x16x32_bf16 v[180:183], a[236:239], a[180:183], v[180:183]\n"
    "v_mfma_f32_16x16x32_bf16 v[148:151], a[236:239], a[176:179], v[148:151]\n"
    "v_mfma_f32_16x16x32_bf16 v[148:151], a[232:235], a[144:147], v[148:151]\n"
    "v_mfma_f32_16x16x32_bf16 v[116:119], a[232:235], a[140:143], v[116:119]\n"
    "v_mfma_f32_16x16x32_bf16 v[116:119], a[236:239], a[172:175], v[116:119]\n"
    "v_mfma_f32_16x16x32_bf16 v[84:87], a[236:239], a[168:171], v[84:87]\n"
    "v_mfma_f32_16x16x32_bf16 v[84:87], a[232:235], a[136:139], v[84:87]\n"
    "v_mfma_f32_16x16x32_bf16 v[52:55], a[232:235], a[132:135], v[52:55]\n"
    "v_mfma_f32_16x16x32_bf16 v[52:55], a[236:239], a[164:167], v[52:55]\n"
    "v_mfma_f32_16x16x32_bf16 v[20:23], a[236:239], a[160:163], v[20:23]\n"
    "v_mfma_f32_16x16x32_bf16 v[20:23], a[232:235], a[128:131], v[20:23]\n"
    "s_waitcnt vmcnt(10)\n"
    "v_mfma_f32_16x16x32_bf16 v[24:27], a[192:195], a[128:131], v[24:27]\n"
    "buffer_load_dwordx4 a[232:235], v240, s[20:23], s53 offen offset:2048 sc1\n"
    "v_mfma_f32_16x16x32_bf16 v[24:27], a[196:199], a[160:163], v[24:27]\n"
    "buffer_load_dwordx4 a[236:239], v240, s[20:23], s53 offen offset:3072 sc1\n"
    "v_mfma_f32_16x16x32_bf16 v[56:59], a[196:199], a[164:167], v[56:59]\n"
    "s_addk_i32 s53, 4096\n"
    "v_mfma_f32_16x16x32_bf16 v[56:59], a[192:195], a[132:135], v[56:59]\n"
    "v_mfma_f32_16x16x32_bf16 v[88:91], a[192:195], a[136:139], v[88:91]\n"
    "v_mfma_f32_16x16x32_bf16 v[88:91], a[196:199], a[168:171], v[88:91]\n"
    "v_mfma_f32_16x16x32_bf16 v[120:123], a[196:199], a[172:175], v[120:123]\n"
    "v_mfma_f32_16x16x32_bf16 v[120:123], a[192:195], a[140:143], v[120:123]\n"
    "v_mfma_f32_16x16x32_bf16 v[152:155], a[192:195], a[144:147], v[152:155]\n"
    "v_mfma_f32_16x16x32_bf16 v[152:155], a[196:199], a[176:179], v[152:155]\n"
    "v_mfma_f32_16x16x32_bf16 v[184:187], a[196:199], a[180:183], v[184:187]\n"
    "v_mfma_f32_16x16x32_bf16 v[184:187], a[192:195], a[148:151], v[184:187]\n"
    "v_mfma_f32_16x16x32_bf16 v[216:219], a[192:195], a[152:155], v[216:219]\n"
    "v_mfma_f32_16x16x32_bf16 v[216:219], a[196:199], a[184:187], v[216:219]\n"
    "s_barrier\n"
    "v_mfma_f32_16x16x32_bf16 a[248:251], a[196:199], a[188:191], a[248:251]\n"
    "v_mfma_f32_16x16x32_bf16 a[248:251], a[192:195], a[156:159], a[248:251]\n"
    "s_waitcnt vmcnt(8)\n"
    "v_mfma_f32_16x16x32_bf16 a[252:255], a[200:203], a[156:159], a[252:255]\n"
    "buffer_load_dwordx4 a[192:195], v240, s[20:23], s53 offen offset:0 sc1\n"
    "v_mfma_f32_16x16x32_bf16 a[252:255], a[204:207], a[188:191], a[252:255]\n"
    "buffer_load_dwordx4 a[196:199], v240, s[20:23], s53 offen offset:1024 sc1\n"
    "v_mfma_f32_16x16x32_bf16 v[220:223], a[204:207], a[184:187], v[220:223]\n"
    "v_add_u32 v241, s66, v240\n"
    "ds_read_b128 a[156:159], v241 offset:14336\n"
    "v_mfma_f32_16x16x32_bf16 v[220:223], a[200:203], a[152:155], v[220:223]\n"
    "ds_read_b128 a[188:191], v241 offset:15360\n"
    "v_mfma_f32_16x16x32_bf16 v[188:191], a[200:203], a[148:151], v[188:191]\n"
    "ds_read_b128 a[184:187], v241 offset:13312\n"
    "v_mfma_f32_16x16x32_bf16 v[188:191], a[204:207], a[180:183], v[188:191]\n"
    "ds_read_b128 a[152:155], v241 offset:12288\n"
    "v_mfma_f32_16x16x32_bf16 v[156:159], a[204:207], a[176:179], v[156:159]\n"
    "ds_read_b128 a[148:151], v241 offset:10240\n"
    "v_mfma_f32_16x16x32_bf16 v[156:159], a[200:203], a[144:147], v[156:159]\n"
    "ds_read_b128 a[180:183], v241 offset:11264\n"
    "v_mfma_f32_16x16x32_bf16 v[124:127], a[200:203], a[140:143], v[124:127]\n"
    "ds_read_b128 a[176:179], v241 offset:9216\n"
    "v_mfma_f32_16x16x32_bf16 v[124:127], a[204:207], a[172:175], v[124:127]\n"
    "ds_read_b128 a[144:147], v241 offset:8192\n"
    "v_mfma_f32_16x16x32_bf16 v[92:95], a[204:207], a[168:171], v[92:95]\n"
    "ds_read_b128 a[140:143], v241 offset:6144\n"
    "v_mfma_f32_16x16x32_bf16 v[92:95], a[200:203], a[136:139], v[92:95]\n"
    "ds_read_b128 a[172:175], v241 offset:7168\n"
    "v_mfma_f32_16x16x32_bf16 v[60:63], a[200:203], a[132:135], v[60:63]\n"
    "ds_read_b128 a[168:171], v241 offset:5120\n"
    "v_mfma_f32_16x16x32_bf16 v[60:63], a[204:207], a[164:167], v[60:63]\n"
    "ds_read_b128 a[136:139], v241 offset:4096\n"
    "v_mfma_f32_16x16x32_bf16 v[28:31], a[204:207], a[160:163], v[28:31]\n"
    "ds_read_b128 a[132:135], v241 offset:2048\n"
    "v_mfma_f32_16x16x32_bf16 v[28:31], a[200:203], a[128:131], v[28:31]\n"
    "ds_read_b128 a[164:167], v241 offset:3072\n"
    "ds_read_b128 a[160:163], v241 offset:1024\n"
    "ds_read_b128 a[128:131], v241 offset:0\n"
    "buffer_load_dwordx4 a[200:203], v240, s[20:23], s53 offen offset:2048 sc1\n"
    "buffer_load_dwordx4 a[204:207], v240, s[20:23], s53 offen offset:3072 sc1\n"
    "s_addk_i32 s53, 4096\n"
    "s_bitcmp1_b32 0x404, s52\n"
    "s_cbranch_scc1 OD_%=\n"
    "OE_%=:\n"
    "s_waitcnt vmcnt(10) lgkmcnt(14)\n"
    "v_mfma_f32_16x16x32_bf16 v[224:227], a[208:211], a[156:159], v[224:227]\n"
    "v_mfma_f32_16x16x32_bf16 v[224:227], a[212:215], a[188:191], v[224:227]\n"
    "s_waitcnt lgkmcnt(12)\n"
    "v_mfma_f32_16x16x32_bf16 v[192:195], a[212:215], a[184:187], v[192:195]\n"
    "v_mfma_f32_16x16x32_bf16 v[192:195], a[208:211], a[152:155], v[192:195]\n"
    "s_waitcnt lgkmcnt(10)\n"
    "v_mfma_f32_16x16x32_bf16 v[160:163], a[208:211], a[148:151], v[160:163]\n"
    "v_mfma_f32_16x16x32_bf16 v[160:163], a[212:215], a[180:183], v[160:163]\n"
    "s_waitcnt lgkmcnt(8)\n"
    "v_mfma_f32_16x16x32_bf16 v[128:131], a[212:215], a[176:179], v[128:131]\n"
    "s_mov_b32 m0, s56\n"
    "s_xor_b32 s90, s90, 0x195800\n"
    "v_mfma_f32_16x16x32_bf16 v[128:131], a[208:211], a[144:147], v[128:131]\n"
    "s_waitcnt lgkmcnt(6)\n"
    "v_mfma_f32_16x16x32_bf16 v[96:99], a[208:211], a[140:143], v[96:99]\n"
    "buffer_load_dwordx4 v240, s[32:35], s90 offen sc1 lds\n"
    "v_mfma_f32_16x16x32_bf16 v[96:99], a[212:215], a[172:175], v[96:99]\n"
    "s_waitcnt lgkmcnt(4)\n"
    "v_mfma_f32_16x16x32_bf16 v[64:67], a[212:215], a[168:171], v[64:67]\n"
    "v_mfma_f32_16x16x32_bf16 v[64:67], a[208:211], a[136:139], v[64:67]\n"
    "s_waitcnt lgkmcnt(2)\n"
    "v_mfma_f32_16x16x32_bf16 v[32:35], a[208:211], a[132:135], v[32:35]\n"
    "v_mfma_f32_16x16x32_bf16 v[32:35], a[212:215], a[164:167], v[32:35]\n"
    "s_waitcnt lgkmcnt(0)\n"
    "v_mfma_f32_16x16x32_bf16 v[0:3], a[212:215], a[160:163], v[0:3]\n"
    "v_mfma_f32_16x16x32_bf16 v[0:3], a[208:211], a[128:131], v[0:3]\n"
    "s_waitcnt vmcnt(9)\n"
    "v_mfma_f32_16x16x32_bf16 v[4:7], a[216:219], a[128:131], v[4:7]\n"
    "buffer_load_dwordx4 a[208:211], v240, s[20:23], s53 offen offset:0 sc1\n"
    "v_mfma_f32_16x16x32_bf16 v[4:7], a[220:223], a[160:163], v[4:7]\n"
    "buffer_load_dwordx4 a[212:215], v240, s[20:23], s53 offen offset:1024 sc1\n"
    "v_mfma_f32_16x16x32_bf16 v[36:39], a[220:223], a[164:167], v[36:39]\n"
    "v_mfma_f32_16x16x32_bf16 v[36:39], a[216:219], a[132:135], v[36:39]\n"
    "v_mfma_f32_16x16x32_bf16 v[68:71], a[216:219], a[136:139], v[68:71]\n"
    "v_mfma_f32_16x16x32_bf16 v[68:71], a[220:223], a[168:171], v[68:71]\n"
    "v_mfma_f32_16x16x32_bf16 v[100:103], a[220:223], a[172:175], v[100:103]\n"
    "v_mfma_f32_16x16x32_bf16 v[100:103], a[216:219], a[140:143], v[100:103]\n"
    "v_mfma_f32_16x16x32_bf16 v[132:135], a[216:219], a[144:147], v[132:135]\n"
    "v_mfma_f32_16x16x32_bf16 v[132:135], a[220:223], a[176:179], v[132:135]\n"
    "v_mfma_f32_16x16x32_bf16 v[164:167], a[220:223], a[180:183], v[164:167]\n"
    "v_mfma_f32_16x16x32_bf16 v[164:167], a[216:219], a[148:151], v[164:167]\n"
    "v_mfma_f32_16x16x32_bf16 v[196:199], a[216:219], a[152:155], v[196:199]\n"
    "v_mfma_f32_16x16x32_bf16 v[196:199], a[220:223], a[184:187], v[196:199]\n"
    "v_mfma_f32_16x16x32_bf16 v[228:231], a[220:223], a[188:191], v[228:231]\n"
    "s_addk_i32 m0, 2048\n"
    "s_xor_b32 s90, s90, 0x800\n"
    "v_mfma_f32_16x16x32_bf16 v[228:231], a[216:219], a[156:159], v[228:231]\n"
    "s_waitcnt vmcnt(9)\n"
    "v_mfma_f32_16x16x32_bf16 v[232:235], a[224:227], a[156:159], v[232:235]\n"
    "buffer_load_dwordx4 v240, s[32:35], s90 offen sc1 lds\n"
    "v_mfma_f32_16x16x32_bf16 v[232:235], a[228:231], a[188:191], v[232:235]\n"
    "buffer_load_dwordx4 a[216:219], v240, s[20:23], s53 offen offset:2048 sc1\n"
    "v_mfma_f32_16x16x32_bf16 v[200:203], a[228:231], a[184:187], v[200:203]\n"
    "buffer_load_dwordx4 a[220:223], v240, s[20:23], s53 offen offset:3072 sc1\n"
    "v_mfma_f32_16x16x32_bf16 v[200:203], a[224:227], a[152:155], v[200:203]\n"
    "s_addk_i32 s53, 4096\n"
    "v_mfma_f32_16x16x32_bf16 v[168:171], a[224:227], a[148:151], v[168:171]\n"
    "v_mfma_f32_16x16x32_bf16 v[168:171], a[228:231], a[180:183], v[168:171]\n"
    "v_mfma_f32_16x16x32_bf16 v[136:139], a[228:231], a[176:179], v[136:139]\n"
    "v_mfma_f32_16x16x32_bf16 v[136:139], a[224:227], a[144:147], v[136:139]\n"
    "v_mfma_f32_16x16x32_bf16 v[104:107], a[224:227], a[140:143], v[104:107]\n"
    "v_mfma_f32_16x16x32_bf16 v[104:107], a[228:231], a[172:175], v[104:107]\n"
    "v_mfma_f32_16x16x32_bf16 v[72:75], a[228:231], a[168:171], v[72:75]\n"
    "v_mfma_f32_16x16x32_bf16 v[72:75], a[224:227], a[136:139], v[72:75]\n"
    "v_mfma_f32_16x16x32_bf16 v[40:43], a[224:227], a[132:135], v[40:43]\n"
    "v_mfma_f32_16x16x32_bf16 v[40:43], a[228:231], a[164:167], v[40:43]\n"
    "v_mfma_f32_16x16x32_bf16 v[8:11], a[228:231], a[160:163], v[8:11]\n"
    "v_mfma_f32_16x16x32_bf16 v[8:11], a[224:227], a[128:131], v[8:11]\n"
    "s_waitcnt vmcnt(10)\n"
    "v_mfma_f32_16x16x32_bf16 v[12:15], a[232:235], a[128:131], v[12:15]\n"
    "buffer_load_dwordx4 a[224:227], v240, s[20:23], s53 offen offset:0 sc1\n"
    "v_mfma_f32_16x16x32_bf16 v[12:15], a[236:239], a[160:163], v[12:15]\n"
    "buffer_load_dwordx4 a[228:231], v240, s[20:23], s53 offen offset:1024 sc1\n"
    "v_mfma_f32_16x16x32_bf16 v[44:47], a[236:239], a[164:167], v[44:47]\n"
    "v_mfma_f32_16x16x32_bf16 v[44:47], a[232:235], a[132:135], v[44:47]\n"
    "v_mfma_f32_16x16x32_bf16 v[76:79], a[232:235], a[136:139], v[76:79]\n"
    "v_mfma_f32_16x16x32_bf16 v[76:79], a[236:239], a[168:171], v[76:79]\n"
    "v_mfma_f32_16x16x32_bf16 v[108:111], a[236:239], a[172:175], v[108:111]\n"
    "s_addk_i32 m0, 2048\n"
    "s_xor_b32 s90, s90, 0x101800\n"
    "v_mfma_f32_16x16x32_bf16 v[108:111], a[232:235], a[140:143], v[108:111]\n"
    "buffer_load_dwordx4 v240, s[32:35], s90 offen sc1 lds\n"
    "v_mfma_f32_16x16x32_bf16 v[140:143], a[232:235], a[144:147], v[140:143]\n"
    "v_mfma_f32_16x16x32_bf16 v[140:143], a[236:239], a[176:179], v[140:143]\n"
    "v_mfma_f32_16x16x32_bf16 v[172:175], a[236:239], a[180:183], v[172:175]\n"
    "v_mfma_f32_16x16x32_bf16 v[172:175], a[232:235], a[148:151], v[172:175]\n"
    "v_mfma_f32_16x16x32_bf16 v[204:207], a[232:235], a[152:155], v[204:207]\n"
    "v_mfma_f32_16x16x32_bf16 v[204:207], a[236:239], a[184:187], v[204:207]\n"
    "v_mfma_f32_16x16x32_bf16 v[236:239], a[236:239], a[188:191], v[236:239]\n"
    "v_mfma_f32_16x16x32_bf16 v[236:239], a[232:235], a[156:159], v[236:239]\n"
    "s_waitcnt vmcnt(11)\n"
    "v_mfma_f32_16x16x32_bf16 a[240:243], a[192:195], a[156:159], a[240:243]\n"
    "buffer_load_dwordx4 a[232:235], v240, s[20:23], s53 offen offset:2048 sc1\n"
    "v_mfma_f32_16x16x32_bf16 a[240:243], a[196:199], a[188:191], a[240:243]\n"
    "buffer_load_dwordx4 a[236:239], v240, s[20:23], s53 offen offset:3072 sc1\n"
    "v_mfma_f32_16x16x32_bf16 v[208:211], a[196:199], a[184:187], v[208:211]\n"
    "s_addk_i32 s53, 4096\n"
    "v_mfma_f32_16x16x32_bf16 v[208:211], a[192:195], a[152:155], v[208:211]\n"
    "v_mfma_f32_16x16x32_bf16 v[176:179], a[192:195], a[148:151], v[176:179]\n"
    "v_mfma_f32_16x16x32_bf16 v[176:179], a[196:199], a[180:183], v[176:179]\n"
    "v_mfma_f32_16x16x32_bf16 v[144:147], a[196:199], a[176:179], v[144:147]\n"
    "v_mfma_f32_16x16x32_bf16 v[144:147], a[192:195], a[144:147], v[144:147]\n"
    "v_mfma_f32_16x16x32_bf16 v[112:115], a[192:195], a[140:143], v[112:115]\n"
    "v_mfma_f32_16x16x32_bf16 v[112:115], a[196:199], a[172:175], v[112:115]\n"
    "v_mfma_f32_16x16x32_bf16 v[80:83], a[196:199], a[168:171], v[80:83]\n"
    "v_mfma_f32_16x16x32_bf16 v[80:83], a[192:195], a[136:139], v[80:83]\n"
    "v_mfma_f32_16x16x32_bf16 v[48:51], a[192:195], a[132:135], v[48:51]\n"
    "v_mfma_f32_16x16x32_bf16 v[48:51], a[196:199], a[164:167], v[48:51]\n"
    "v_mfma_f32_16x16x32_bf16 v[16:19], a[196:199], a[160:163], v[16:19]\n"
    "s_addk_i32 m0, 2048\n"
    "s_xor_b32 s90, s90, 0x800\n"
    "v_mfma_f32_16x16x32_bf16 v[16:19], a[192:195], a[128:131], v[16:19]\n"
    "s_waitcnt vmcnt(11)\n"
    "v_mfma_f32_16x16x32_bf16 v[20:23], a[200:203], a[128:131], v[20:23]\n"
    "buffer_load_dwordx4 v240, s[32:35], s90 offen sc1 lds\n"
    "v_mfma_f32_16x16x32_bf16 v[20:23], a[204:207], a[160:163], v[20:23]\n"
    "buffer_load_dwordx4 a[192:195], v240, s[20:23], s53 offen offset:0 sc1\n"
    "v_mfma_f32_16x16x32_bf16 v[52:55], a[204:207], a[164:167], v[52:55]\n"
    "buffer_load_dwordx4 a[196:199], v240, s[20:23], s53 offen offset:1024 sc1\n"
    "v_mfma_f32_16x16x32_bf16 v[52:55], a[200:203], a[132:135], v[52:55]\n"
    "v_mfma_f32_16x16x32_bf16 v[84:87], a[200:203], a[136:139], v[84:87]\n"
    "v_mfma_f32_16x16x32_bf16 v[84:87], a[204:207], a[168:171], v[84:87]\n"
    "v_mfma_f32_16x16x32_bf16 v[116:119], a[204:207], a[172:175], v[116:119]\n"
    "v_mfma_f32_16x16x32_bf16 v[116:119], a[200:203], a[140:143], v[116:119]\n"
    "v_mfma_f32_16x16x32_bf16 v[148:151], a[200:203], a[144:147], v[148:151]\n"
    "v_mfma_f32_16x16x32_bf16 v[148:151], a[204:207], a[176:179], v[148:151]\n"
    "v_mfma_f32_16x16x32_bf16 v[180:183], a[204:207], a[180:183], v[180:183]\n"
    "v_mfma_f32_16x16x32_bf16 v[180:183], a[200:203], a[148:151], v[180:183]\n"
    "v_mfma_f32_16x16x32_bf16 v[212:215], a[200:203], a[152:155], v[212:215]\n"
    "v_mfma_f32_16x16x32_bf16 v[212:215], a[204:207], a[184:187], v[212:215]\n"
    "v_mfma_f32_16x16x32_bf16 a[244:247], a[204:207], a[188:191], a[244:247]\n"
    "v_mfma_f32_16x16x32_bf16 a[244:247], a[200:203], a[156:159], a[244:247]\n"
    "s_waitcnt vmcnt(11)\n"
    "v_mfma_f32_16x16x32_bf16 a[248:251], a[208:211], a[156:159], a[248:251]\n"
    "buffer_load_dwordx4 a[200:203], v240, s[20:23], s53 offen offset:2048 sc1\n"
    "v_mfma_f32_16x16x32_bf16 a[248:251], a[212:215], a[188:191], a[248:251]\n"
    "buffer_load_dwordx4 a[204:207], v240, s[20:23], s53 offen offset:3072 sc1\n"
    "v_mfma_f32_16x16x32_bf16 v[216:219], a[212:215], a[184:187], v[216:219]\n"
    "s_addk_i32 s53, 4096\n"
    "v_mfma_f32_16x16x32_bf16 v[216:219], a[208:211], a[152:155], v[216:219]\n"
    "v_mfma_f32_16x16x32_bf16 v[184:187], a[208:211], a[148:151], v[184:187]\n"
    "v_mfma_f32_16x16x32_bf16 v[184:187], a[212:215], a[180:183], v[184:187]\n"
    "v_mfma_f32_16x16x32_bf16 v[152:155], a[212:215], a[176:179], v[152:155]\n"
    "v_mfma_f32_16x16x32_bf16 v[152:155], a[208:211], a[144:147], v[152:155]\n"
    "v_mfma_f32_16x16x32_bf16 v[120:123], a[208:211], a[140:143], v[120:123]\n"
    "v_mfma_f32_16x16x32_bf16 v[120:123], a[212:215], a[172:175], v[120:123]\n"
    "v_mfma_f32_16x16x32_bf16 v[88:91], a[212:215], a[168:171], v[88:91]\n"
    "s_addk_i32 m0, 2048\n"
    "s_xor_b32 s90, s90, 0x185800\n"
    "v_mfma_f32_16x16x32_bf16 v[88:91], a[208:211], a[136:139], v[88:91]\n"
    "buffer_load_dwordx4 v240, s[32:35], s90 offen sc1 lds\n"
    "v_mfma_f32_16x16x32_bf16 v[56:59], a[208:211], a[132:135], v[56:59]\n"
    "v_mfma_f32_16x16x32_bf16 v[56:59], a[212:215], a[164:167], v[56:59]\n"
    "v_mfma_f32_16x16x32_bf16 v[24:27], a[212:215], a[160:163], v[24:27]\n"
    "v_mfma_f32_16x16x32_bf16 v[24:27], a[208:211], a[128:131], v[24:27]\n"
    "s_waitcnt vmcnt(11)\n"
    "v_mfma_f32_16x16x32_bf16 v[28:31], a[216:219], a[128:131], v[28:31]\n"
    "buffer_load_dwordx4 a[208:211], v240, s[20:23], s53 offen offset:0 sc1\n"
    "v_mfma_f32_16x16x32_bf16 v[28:31], a[220:223], a[160:163], v[28:31]\n"
    "buffer_load_dwordx4 a[212:215], v240, s[20:23], s53 offen offset:1024 sc1\n"
    "v_mfma_f32_16x16x32_bf16 v[60:63], a[220:223], a[164:167], v[60:63]\n"
    "ds_read_b128 a[128:131], v241 offset:16384\n"
    "v_mfma_f32_16x16x32_bf16 v[60:63], a[216:219], a[132:135], v[60:63]\n"
    "ds_read_b128 a[160:163], v241 offset:17408\n"
    "v_mfma_f32_16x16x32_bf16 v[92:95], a[216:219], a[136:139], v[92:95]\n"
    "ds_read_b128 a[164:167], v241 offset:19456\n"
    "v_mfma_f32_16x16x32_bf16 v[92:95], a[220:223], a[168:171], v[92:95]\n"
    "ds_read_b128 a[132:135], v241 offset:18432\n"
    "v_mfma_f32_16x16x32_bf16 v[124:127], a[220:223], a[172:175], v[124:127]\n"
    "ds_read_b128 a[136:139], v241 offset:20480\n"
    "v_mfma_f32_16x16x32_bf16 v[124:127], a[216:219], a[140:143], v[124:127]\n"
    "ds_read_b128 a[168:171], v241 offset:21504\n"
    "v_mfma_f32_16x16x32_bf16 v[156:159], a[216:219], a[144:147], v[156:159]\n"
    "ds_read_b128 a[172:175], v241 offset:23552\n"
    "v_mfma_f32_16x16x32_bf16 v[156:159], a[220:223], a[176:179], v[156:159]\n"
    "ds_read_b128 a[140:143], v241 offset:22528\n"
    "v_mfma_f32_16x16x32_bf16 v[188:191], a[220:223], a[180:183], v[188:191]\n"
    "ds_read_b128 a[144:147], v241 offset:24576\n"
    "v_mfma_f32_16x16x32_bf16 v[188:191], a[216:219], a[148:151], v[188:191]\n"
    "ds_read_b128 a[176:179], v241 offset:25600\n"
    "v_mfma_f32_16x16x32_bf16 v[220:223], a[216:219], a[152:155], v[220:223]\n"
    "ds_read_b128 a[180:183], v241 offset:27648\n"
    "v_mfma_f32_16x16x32_bf16 v[220:223], a[220:223], a[184:187], v[220:223]\n"
    "ds_read_b128 a[148:151], v241 offset:26624\n"
    "v_mfma_f32_16x16x32_bf16 a[252:255], a[220:223], a[188:191], a[252:255]\n"
    "ds_read_b128 a[152:155], v241 offset:28672\n"
    "v_mfma_f32_16x16x32_bf16 a[252:255], a[216:219], a[156:159], a[252:255]\n"
    "s_waitcnt vmcnt(11) lgkmcnt(11)\n"
    "ds_read_b128 a[184:187], v241 offset:29696\n"
    "v_mfma_f32_16x16x32_bf16 v[0:3], a[224:227], a[128:131], v[0:3]\n"
    "buffer_load_dwordx4 a[216:219], v240, s[20:23], s53 offen offset:2048 sc1\n"
    "v_mfma_f32_16x16x32_bf16 v[0:3], a[228:231], a[160:163], v[0:3]\n"
    "s_waitcnt lgkmcnt(10)\n"
    "v_mfma_f32_16x16x32_bf16 v[32:35], a[228:231], a[164:167], v[32:35]\n"
    "buffer_load_dwordx4 a[220:223], v240, s[20:23], s53 offen offset:3072 sc1\n"
    "v_mfma_f32_16x16x32_bf16 v[32:35], a[224:227], a[132:135], v[32:35]\n"
    "s_waitcnt lgkmcnt(8)\n"
    "s_addk_i32 s53, 4096\n"
    "ds_read_b128 a[188:191], v241 offset:31744\n"
    "v_mfma_f32_16x16x32_bf16 v[64:67], a[224:227], a[136:139], v[64:67]\n"
    "ds_read_b128 a[156:159], v241 offset:30720\n"
    "s_addk_i32 m0, 2048\n"
    "v_mfma_f32_16x16x32_bf16 v[64:67], a[228:231], a[168:171], v[64:67]\n"
    "s_waitcnt lgkmcnt(8)\n"
    "s_xor_b32 s90, s90, 0x800\n"
    "v_mfma_f32_16x16x32_bf16 v[96:99], a[228:231], a[172:175], v[96:99]\n"
    "buffer_load_dwordx4 v240, s[32:35], s90 offen sc1 lds\n"
    "v_mfma_f32_16x16x32_bf16 v[96:99], a[224:227], a[140:143], v[96:99]\n"
    "s_waitcnt lgkmcnt(6)\n"
    "v_mfma_f32_16x16x32_bf16 v[128:131], a[224:227], a[144:147], v[128:131]\n"
    "v_mfma_f32_16x16x32_bf16 v[128:131], a[228:231], a[176:179], v[128:131]\n"
    "s_waitcnt lgkmcnt(4)\n"
    "v_mfma_f32_16x16x32_bf16 v[160:163], a[228:231], a[180:183], v[160:163]\n"
    "v_mfma_f32_16x16x32_bf16 v[160:163], a[224:227], a[148:151], v[160:163]\n"
    "s_waitcnt lgkmcnt(2)\n"
    "v_mfma_f32_16x16x32_bf16 v[192:195], a[224:227], a[152:155], v[192:195]\n"
    "v_mfma_f32_16x16x32_bf16 v[192:195], a[228:231], a[184:187], v[192:195]\n"
    "s_waitcnt lgkmcnt(0)\n"
    "v_mfma_f32_16x16x32_bf16 v[224:227], a[228:231], a[188:191], v[224:227]\n"
    "v_mfma_f32_16x16x32_bf16 v[224:227], a[224:227], a[156:159], v[224:227]\n"
    "s_waitcnt vmcnt(11)\n"
    "v_mfma_f32_16x16x32_bf16 v[228:231], a[232:235], a[156:159], v[228:231]\n"
    "buffer_load_dwordx4 a[224:227], v240, s[20:23], s53 offen offset:0 sc1\n"
    "v_mfma_f32_16x16x32_bf16 v[228:231], a[236:239], a[188:191], v[228:231]\n"
    "buffer_load_dwordx4 a[228:231], v240, s[20:23], s53 offen offset:1024 sc1\n"
    "v_mfma_f32_16x16x32_bf16 v[196:199], a[236:239], a[184:187], v[196:199]\n"
    "v_mfma_f32_16x16x32_bf16 v[196:199], a[232:235], a[152:155], v[196:199]\n"
    "v_mfma_f32_16x16x32_bf16 v[164:167], a[232:235], a[148:151], v[164:167]\n"
    "v_mfma_f32_16x16x32_bf16 v[164:167], a[236:239], a[180:183], v[164:167]\n"
    "v_mfma_f32_16x16x32_bf16 v[132:135], a[236:239], a[176:179], v[132:135]\n"
    "v_mfma_f32_16x16x32_bf16 v[132:135], a[232:235], a[144:147], v[132:135]\n"
    "v_mfma_f32_16x16x32_bf16 v[100:103], a[232:235], a[140:143], v[100:103]\n"
    "v_mfma_f32_16x16x32_bf16 v[100:103], a[236:239], a[172:175], v[100:103]\n"
    "v_mfma_f32_16x16x32_bf16 v[68:71], a[236:239], a[168:171], v[68:71]\n"
    "s_addk_i32 m0, 2048\n"
    "s_xor_b32 s90, s90, 0x101800\n"
    "v_mfma_f32_16x16x32_bf16 v[68:71], a[232:235], a[136:139], v[68:71]\n"
    "buffer_load_dwordx4 v240, s[32:35], s90 offen sc1 lds\n"
    "v_mfma_f32_16x16x32_bf16 v[36:39], a[232:235], a[132:135], v[36:39]\n"
    "v_mfma_f32_16x16x32_bf16 v[36:39], a[236:239], a[164:167], v[36:39]\n"
    "v_mfma_f32_16x16x32_bf16 v[4:7], a[236:239], a[160:163], v[4:7]\n"
    "v_mfma_f32_16x16x32_bf16 v[4:7], a[232:235], a[128:131], v[4:7]\n"
    "s_waitcnt vmcnt(11)\n"
    "v_mfma_f32_16x16x32_bf16 v[8:11], a[192:195], a[128:131], v[8:11]\n"
    "buffer_load_dwordx4 a[232:235], v240, s[20:23], s53 offen offset:2048 sc1\n"
    "v_mfma_f32_16x16x32_bf16 v[8:11], a[196:199], a[160:163], v[8:11]\n"
    "buffer_load_dwordx4 a[236:239], v240, s[20:23], s53 offen offset:3072 sc1\n"
    "v_mfma_f32_16x16x32_bf16 v[40:43], a[196:199], a[164:167], v[40:43]\n"
    "s_addk_i32 s53, 4096\n"
    "v_mfma_f32_16x16x32_bf16 v[40:43], a[192:195], a[132:135], v[40:43]\n"
    "v_mfma_f32_16x16x32_bf16 v[72:75], a[192:195], a[136:139], v[72:75]\n"
    "v_mfma_f32_16x16x32_bf16 v[72:75], a[196:199], a[168:171], v[72:75]\n"
    "v_mfma_f32_16x16x32_bf16 v[104:107], a[196:199], a[172:175], v[104:107]\n"
    "v_mfma_f32_16x16x32_bf16 v[104:107], a[192:195], a[140:143], v[104:107]\n"
    "v_mfma_f32_16x16x32_bf16 v[136:139], a[192:195], a[144:147], v[136:139]\n"
    "v_mfma_f32_16x16x32_bf16 v[136:139], a[196:199], a[176:179], v[136:139]\n"
    "v_mfma_f32_16x16x32_bf16 v[168:171], a[196:199], a[180:183], v[168:171]\n"
    "v_mfma_f32_16x16x32_bf16 v[168:171], a[192:195], a[148:151], v[168:171]\n"
    "v_mfma_f32_16x16x32_bf16 v[200:203], a[192:195], a[152:155], v[200:203]\n"
    "v_mfma_f32_16x16x32_bf16 v[200:203], a[196:199], a[184:187], v[200:203]\n"
    "v_mfma_f32_16x16x32_bf16 v[232:235], a[196:199], a[188:191], v[232:235]\n"
    "v_mfma_f32_16x16x32_bf16 v[232:235], a[192:195], a[156:159], v[232:235]\n"
    "s_waitcnt vmcnt(11)\n"
    "v_mfma_f32_16x16x32_bf16 v[236:239], a[200:203], a[156:159], v[236:239]\n"
    "buffer_load_dwordx4 a[192:195], v240, s[20:23], s53 offen offset:0 sc1\n"
    "v_mfma_f32_16x16x32_bf16 v[236:239], a[204:207], a[188:191], v[236:239]\n"
    "buffer_load_dwordx4 a[196:199], v240, s[20:23], s53 offen offset:1024 sc1\n"
    "v_mfma_f32_16x16x32_bf16 v[204:207], a[204:207], a[184:187], v[204:207]\n"
    "s_addk_i32 m0, 2048\n"
    "s_xor_b32 s90, s90, 0x800\n"
    "v_mfma_f32_16x16x32_bf16 v[204:207], a[200:203], a[152:155], v[204:207]\n"
    "buffer_load_dwordx4 v240, s[32:35], s90 offen sc1 lds\n"
    "v_mfma_f32_16x16x32_bf16 v[172:175], a[200:203], a[148:151], v[172:175]\n"
    "v_mfma_f32_16x16x32_bf16 v[172:175], a[204:207], a[180:183], v[172:175]\n"
    "v_mfma_f32_16x16x32_bf16 v[140:143], a[204:207], a[176:179], v[140:143]\n"
    "v_mfma_f32_16x16x32_bf16 v[140:143], a[200:203], a[144:147], v[140:143]\n"
    "v_mfma_f32_16x16x32_bf16 v[108:111], a[200:203], a[140:143], v[108:111]\n"
    "v_mfma_f32_16x16x32_bf16 v[108:111], a[204:207], a[172:175], v[108:111]\n"
    "v_mfma_f32_16x16x32_bf16 v[76:79], a[204:207], a[168:171], v[76:79]\n"
    "v_mfma_f32_16x16x32_bf16 v[76:79], a[200:203], a[136:139], v[76:79]\n"
    "v_mfma_f32_16x16x32_bf16 v[44:47], a[200:203], a[132:135], v[44:47]\n"
    "v_mfma_f32_16x16x32_bf16 v[44:47], a[204:207], a[164:167], v[44:47]\n"
    "v_mfma_f32_16x16x32_bf16 v[12:15], a[204:207], a[160:163], v[12:15]\n"
    "v_mfma_f32_16x16x32_bf16 v[12:15], a[200:203], a[128:131], v[12:15]\n"
    "s_waitcnt vmcnt(11)\n"
    "v_mfma_f32_16x16x32_bf16 v[16:19], a[208:211], a[128:131], v[16:19]\n"
    "buffer_load_dwordx4 a[200:203], v240, s[20:23], s53 offen offset:2048 sc1\n"
    "v_mfma_f32_16x16x32_bf16 v[16:19], a[212:215], a[160:163], v[16:19]\n"
    "buffer_load_dwordx4 a[204:207], v240, s[20:23], s53 offen offset:3072 sc1\n"
    "v_mfma_f32_16x16x32_bf16 v[48:51], a[212:215], a[164:167], v[48:51]\n"
    "s_addk_i32 s53, 4096\n"
    "v_mfma_f32_16x16x32_bf16 v[48:51], a[208:211], a[132:135], v[48:51]\n"
    "v_mfma_f32_16x16x32_bf16 v[80:83], a[208:211], a[136:139], v[80:83]\n"
    "v_mfma_f32_16x16x32_bf16 v[80:83], a[212:215], a[168:171], v[80:83]\n"
    "v_mfma_f32_16x16x32_bf16 v[112:115], a[212:215], a[172:175], v[112:115]\n"
    "v_mfma_f32_16x16x32_bf16 v[112:115], a[208:211], a[140:143], v[112:115]\n"
    "v_mfma_f32_16x16x32_bf16 v[144:147], a[208:211], a[144:147], v[144:147]\n"
    "v_mfma_f32_16x16x32_bf16 v[144:147], a[212:215], a[176:179], v[144:147]\n"
    "v_mfma_f32_16x16x32_bf16 v[176:179], a[212:215], a[180:183], v[176:179]\n"
    "v_mfma_f32_16x16x32_bf16 v[176:179], a[208:211], a[148:151], v[176:179]\n"
    "v_mfma_f32_16x16x32_bf16 v[208:211], a[208:211], a[152:155], v[208:211]\n"
    "v_mfma_f32_16x16x32_bf16 v[208:211], a[212:215], a[184:187], v[208:211]\n"
    "v_mfma_f32_16x16x32_bf16 a[240:243], a[212:215], a[188:191], a[240:243]\n"
    "v_mfma_f32_16x16x32_bf16 a[240:243], a[208:211], a[156:159], a[240:243]\n"
    "s_waitcnt vmcnt(11)\n"
    "v_mfma_f32_16x16x32_bf16 a[244:247], a[216:219], a[156:159], a[244:247]\n"
    "buffer_load_dwordx4 a[208:211], v240, s[20:23], s53 offen offset:0 sc1\n"
    "v_mfma_f32_16x16x32_bf16 a[244:247], a[220:223], a[188:191], a[244:247]\n"
    "buffer_load_dwordx4 a[212:215], v240, s[20:23], s53 offen offset:1024 sc1\n"
    "v_mfma_f32_16x16x32_bf16 v[212:215], a[220:223], a[184:187], v[212:215]\n"
    "v_mfma_f32_16x16x32_bf16 v[212:215], a[216:219], a[152:155], v[212:215]\n"
    "v_mfma_f32_16x16x32_bf16 v[180:183], a[216:219], a[148:151], v[180:183]\n"
    "v_mfma_f32_16x16x32_bf16 v[180:183], a[220:223], a[180:183], v[180:183]\n"
    "v_mfma_f32_16x16x32_bf16 v[148:151], a[220:223], a[176:179], v[148:151]\n"
    "v_mfma_f32_16x16x32_bf16 v[148:151], a[216:219], a[144:147], v[148:151]\n"
    "v_mfma_f32_16x16x32_bf16 v[116:119], a[216:219], a[140:143], v[116:119]\n"
    "v_mfma_f32_16x16x32_bf16 v[116:119], a[220:223], a[172:175], v[116:119]\n"
    "v_mfma_f32_16x16x32_bf16 v[84:87], a[220:223], a[168:171], v[84:87]\n"
    "v_mfma_f32_16x16x32_bf16 v[84:87], a[216:219], a[136:139], v[84:87]\n"
    "v_mfma_f32_16x16x32_bf16 v[52:55], a[216:219], a[132:135], v[52:55]\n"
    "v_mfma_f32_16x16x32_bf16 v[52:55], a[220:223], a[164:167], v[52:55]\n"
    "v_mfma_f32_16x16x32_bf16 v[20:23], a[220:223], a[160:163], v[20:23]\n"
    "v_mfma_f32_16x16x32_bf16 v[20:23], a[216:219], a[128:131], v[20:23]\n"
    "s_waitcnt vmcnt(10)\n"
    "v_mfma_f32_16x16x32_bf16 v[24:27], a[224:227], a[128:131], v[24:27]\n"
    "buffer_load_dwordx4 a[216:219], v240, s[20:23], s53 offen offset:2048 sc1\n"
    "v_mfma_f32_16x16x32_bf16 v[24:27], a[228:231], a[160:163], v[24:27]\n"
    "buffer_load_dwordx4 a[220:223], v240, s[20:23], s53 offen offset:3072 sc1\n"
    "v_mfma_f32_16x16x32_bf16 v[56:59], a[228:231], a[164:167], v[56:59]\n"
    "s_addk_i32 s53, 4096\n"
    "v_mfma_f32_16x16x32_bf16 v[56:59], a[224:227], a[132:135], v[56:59]\n"
    "v_mfma_f32_16x16x32_bf16 v[88:91], a[224:227], a[136:139], v[88:91]\n"
    "v_mfma_f32_16x16x32_bf16 v[88:91], a[228:231], a[168:171], v[88:91]\n"
    "v_mfma_f32_16x16x32_bf16 v[120:123], a[228:231], a[172:175], v[120:123]\n"
    "v_mfma_f32_16x16x32_bf16 v[120:123], a[224:227], a[140:143], v[120:123]\n"
    "v_mfma_f32_16x16x32_bf16 v[152:155], a[224:227], a[144:147], v[152:155]\n"
    "v_mfma_f32_16x16x32_bf16 v[152:155], a[228:231], a[176:179], v[152:155]\n"
    "v_mfma_f32_16x16x32_bf16 v[184:187], a[228:231], a[180:183], v[184:187]\n"
    "v_mfma_f32_16x16x32_bf16 v[184:187], a[224:227], a[148:151], v[184:187]\n"
    "v_mfma_f32_16x16x32_bf16 v[216:219], a[224:227], a[152:155], v[216:219]\n"
    "v_mfma_f32_16x16x32_bf16 v[216:219], a[228:231], a[184:187], v[216:219]\n"
    "v_mfma_f32_16x16x32_bf16 a[248:251], a[228:231], a[188:191], a[248:251]\n"
    "v_mfma_f32_16x16x32_bf16 a[248:251], a[224:227], a[156:159], a[248:251]\n"
    "s_waitcnt vmcnt(9)\n"
    "v_mfma_f32_16x16x32_bf16 a[252:255], a[232:235], a[156:159], a[252:255]\n"
    "buffer_load_dwordx4 a[224:227], v240, s[20:23], s53 offen offset:0 sc1\n"
    "v_mfma_f32_16x16x32_bf16 a[252:255], a[236:239], a[188:191], a[252:255]\n"
    "buffer_load_dwordx4 a[228:231], v240, s[20:23], s53 offen offset:1024 sc1\n"
    "v_mfma_f32_16x16x32_bf16 v[220:223], a[236:239], a[184:187], v[220:223]\n"
    "ds_read_b128 a[156:159], v241 offset:47104\n"
    "v_mfma_f32_16x16x32_bf16 v[220:223], a[232:235], a[152:155], v[220:223]\n"
    "ds_read_b128 a[188:191], v241 offset:48128\n"
    "v_mfma_f32_16x16x32_bf16 v[188:191], a[232:235], a[148:151], v[188:191]\n"
    "ds_read_b128 a[184:187], v241 offset:46080\n"
    "v_mfma_f32_16x16x32_bf16 v[188:191], a[236:239], a[180:183], v[188:191]\n"
    "ds_read_b128 a[152:155], v241 offset:45056\n"
    "v_mfma_f32_16x16x32_bf16 v[156:159], a[236:239], a[176:179], v[156:159]\n"
    "ds_read_b128 a[148:151], v241 offset:43008\n"
    "v_mfma_f32_16x16x32_bf16 v[156:159], a[232:235], a[144:147], v[156:159]\n"
    "ds_read_b128 a[180:183], v241 offset:44032\n"
    "v_mfma_f32_16x16x32_bf16 v[124:127], a[232:235], a[140:143], v[124:127]\n"
    "ds_read_b128 a[176:179], v241 offset:41984\n"
    "v_mfma_f32_16x16x32_bf16 v[124:127], a[236:239], a[172:175], v[124:127]\n"
    "ds_read_b128 a[144:147], v241 offset:40960\n"
    "v_mfma_f32_16x16x32_bf16 v[92:95], a[236:239], a[168:171], v[92:95]\n"
    "ds_read_b128 a[140:143], v241 offset:38912\n"
    "v_mfma_f32_16x16x32_bf16 v[92:95], a[232:235], a[136:139], v[92:95]\n"
    "ds_read_b128 a[172:175], v241 offset:39936\n"
    "v_mfma_f32_16x16x32_bf16 v[60:63], a[232:235], a[132:135], v[60:63]\n"
    "ds_read_b128 a[168:171], v241 offset:37888\n"
    "v_mfma_f32_16x16x32_bf16 v[60:63], a[236:239], a[164:167], v[60:63]\n"
    "ds_read_b128 a[136:139], v241 offset:36864\n"
    "v_mfma_f32_16x16x32_bf16 v[28:31], a[236:239], a[160:163], v[28:31]\n"
    "ds_read_b128 a[132:135], v241 offset:34816\n"
    "v_mfma_f32_16x16x32_bf16 v[28:31], a[232:235], a[128:131], v[28:31]\n"
    "s_waitcnt vmcnt(9) lgkmcnt(11)\n"
    "ds_read_b128 a[164:167], v241 offset:35840\n"
    "v_mfma_f32_16x16x32_bf16 v[224:227], a[192:195], a[156:159], v[224:227]\n"
    "buffer_load_dwordx4 a[232:235], v240, s[20:23], s53 offen offset:2048 sc1\n"
    "v_mfma_f32_16x16x32_bf16 v[224:227], a[196:199], a[188:191], v[224:227]\n"
    "s_waitcnt lgkmcnt(10)\n"
    "v_mfma_f32_16x16x32_bf16 v[192:195], a[196:199], a[184:187], v[192:195]\n"
    "buffer_load_dwordx4 a[236:239], v240, s[20:23], s53 offen offset:3072 sc1\n"
    "v_mfma_f32_16x16x32_bf16 v[192:195], a[192:195], a[152:155], v[192:195]\n"
    "s_waitcnt lgkmcnt(8)\n"
    "s_addk_i32 s53, 4096\n"
    "ds_read_b128 a[160:163], v241 offset:33792\n"
    "v_mfma_f32_16x16x32_bf16 v[160:163], a[192:195], a[148:151], v[160:163]\n"
    "ds_read_b128 a[128:131], v241 offset:32768\n"
    "v_mfma_f32_16x16x32_bf16 v[160:163], a[196:199], a[180:183], v[160:163]\n"
    "s_waitcnt lgkmcnt(8)\n"
    "v_mfma_f32_16x16x32_bf16 v[128:131], a[196:199], a[176:179], v[128:131]\n"
    "v_mfma_f32_16x16x32_bf16 v[128:131], a[192:195], a[144:147], v[128:131]\n"
    "s_waitcnt lgkmcnt(6)\n"
    "v_mfma_f32_16x16x32_bf16 v[96:99], a[192:195], a[140:143], v[96:99]\n"
    "v_mfma_f32_16x16x32_bf16 v[96:99], a[196:199], a[172:175], v[96:99]\n"
    "s_waitcnt lgkmcnt(4)\n"
    "v_mfma_f32_16x16x32_bf16 v[64:67], a[196:199], a[168:171], v[64:67]\n"
    "v_mfma_f32_16x16x32_bf16 v[64:67], a[192:195], a[136:139], v[64:67]\n"
    "s_waitcnt lgkmcnt(2)\n"
    "v_mfma_f32_16x16x32_bf16 v[32:35], a[192:195], a[132:135], v[32:35]\n"
    "v_mfma_f32_16x16x32_bf16 v[32:35], a[196:199], a[164:167], v[32:35]\n"
    "s_waitcnt lgkmcnt(0)\n"
    "v_mfma_f32_16x16x32_bf16 v[0:3], a[196:199], a[160:163], v[0:3]\n"
    "v_mfma_f32_16x16x32_bf16 v[0:3], a[192:195], a[128:131], v[0:3]\n"
    "s_waitcnt vmcnt(8)\n"
    "v_mfma_f32_16x16x32_bf16 v[4:7], a[200:203], a[128:131], v[4:7]\n"
    "buffer_load_dwordx4 a[192:195], v240, s[20:23], s53 offen offset:0 sc1\n"
    "v_mfma_f32_16x16x32_bf16 v[4:7], a[204:207], a[160:163], v[4:7]\n"
    "buffer_load_dwordx4 a[196:199], v240, s[20:23], s53 offen offset:1024 sc1\n"
    "v_mfma_f32_16x16x32_bf16 v[36:39], a[204:207], a[164:167], v[36:39]\n"
    "v_mfma_f32_16x16x32_bf16 v[36:39], a[200:203], a[132:135], v[36:39]\n"
    "v_mfma_f32_16x16x32_bf16 v[68:71], a[200:203], a[136:139], v[68:71]\n"
    "v_mfma_f32_16x16x32_bf16 v[68:71], a[204:207], a[168:171], v[68:71]\n"
    "v_mfma_f32_16x16x32_bf16 v[100:103], a[204:207], a[172:175], v[100:103]\n"
    "v_mfma_f32_16x16x32_bf16 v[100:103], a[200:203], a[140:143], v[100:103]\n"
    "v_mfma_f32_16x16x32_bf16 v[132:135], a[200:203], a[144:147], v[132:135]\n"
    "v_mfma_f32_16x16x32_bf16 v[132:135], a[204:207], a[176:179], v[132:135]\n"
    "v_mfma_f32_16x16x32_bf16 v[164:167], a[204:207], a[180:183], v[164:167]\n"
    "v_mfma_f32_16x16x32_bf16 v[164:167], a[200:203], a[148:151], v[164:167]\n"
    "v_mfma_f32_16x16x32_bf16 v[196:199], a[200:203], a[152:155], v[196:199]\n"
    "v_mfma_f32_16x16x32_bf16 v[196:199], a[204:207], a[184:187], v[196:199]\n"
    "v_mfma_f32_16x16x32_bf16 v[228:231], a[204:207], a[188:191], v[228:231]\n"
    "v_mfma_f32_16x16x32_bf16 v[228:231], a[200:203], a[156:159], v[228:231]\n"
    "s_waitcnt vmcnt(8)\n"
    "v_mfma_f32_16x16x32_bf16 v[232:235], a[208:211], a[156:159], v[232:235]\n"
    "buffer_load_dwordx4 a[200:203], v240, s[20:23], s53 offen offset:2048 sc1\n"
    "v_mfma_f32_16x16x32_bf16 v[232:235], a[212:215], a[188:191], v[232:235]\n"
    "buffer_load_dwordx4 a[204:207], v240, s[20:23], s53 offen offset:3072 sc1\n"
    "v_mfma_f32_16x16x32_bf16 v[200:203], a[212:215], a[184:187], v[200:203]\n"
    "s_addk_i32 s53, 4096\n"
    "v_mfma_f32_16x16x32_bf16 v[200:203], a[208:211], a[152:155], v[200:203]\n"
    "v_mfma_f32_16x16x32_bf16 v[168:171], a[208:211], a[148:151], v[168:171]\n"
    "v_mfma_f32_16x16x32_bf16 v[168:171], a[212:215], a[180:183], v[168:171]\n"
    "v_mfma_f32_16x16x32_bf16 v[136:139], a[212:215], a[176:179], v[136:139]\n"
    "v_mfma_f32_16x16x32_bf16 v[136:139], a[208:211], a[144:147], v[136:139]\n"
    "v_mfma_f32_16x16x32_bf16 v[104:107], a[208:211], a[140:143], v[104:107]\n"
    "v_mfma_f32_16x16x32_bf16 v[104:107], a[212:215], a[172:175], v[104:107]\n"
    "v_mfma_f32_16x16x32_bf16 v[72:75], a[212:215], a[168:171], v[72:75]\n"
    "v_mfma_f32_16x16x32_bf16 v[72:75], a[208:211], a[136:139], v[72:75]\n"
    "v_mfma_f32_16x16x32_bf16 v[40:43], a[208:211], a[132:135], v[40:43]\n"
    "v_mfma_f32_16x16x32_bf16 v[40:43], a[212:215], a[164:167], v[40:43]\n"
    "v_mfma_f32_16x16x32_bf16 v[8:11], a[212:215], a[160:163], v[8:11]\n"
    "v_mfma_f32_16x16x32_bf16 v[8:11], a[208:211], a[128:131], v[8:11]\n"
    "s_waitcnt vmcnt(8)\n"
    "v_mfma_f32_16x16x32_bf16 v[12:15], a[216:219], a[128:131], v[12:15]\n"
    "buffer_load_dwordx4 a[208:211], v240, s[20:23], s53 offen offset:0 sc1\n"
    "v_mfma_f32_16x16x32_bf16 v[12:15], a[220:223], a[160:163], v[12:15]\n"
    "buffer_load_dwordx4 a[212:215], v240, s[20:23], s53 offen offset:1024 sc1\n"
    "v_mfma_f32_16x16x32_bf16 v[44:47], a[220:223], a[164:167], v[44:47]\n"
    "v_mfma_f32_16x16x32_bf16 v[44:47], a[216:219], a[132:135], v[44:47]\n"
    "v_mfma_f32_16x16x32_bf16 v[76:79], a[216:219], a[136:139], v[76:79]\n"
    "v_mfma_f32_16x16x32_bf16 v[76:79], a[220:223], a[168:171], v[76:79]\n"
    "v_mfma_f32_16x16x32_bf16 v[108:111], a[220:223], a[172:175], v[108:111]\n"
    "v_mfma_f32_16x16x32_bf16 v[108:111], a[216:219], a[140:143], v[108:111]\n"
    "v_mfma_f32_16x16x32_bf16 v[140:143], a[216:219], a[144:147], v[140:143]\n"
    "v_mfma_f32_16x16x32_bf16 v[140:143], a[220:223], a[176:179], v[140:143]\n"
    "v_mfma_f32_16x16x32_bf16 v[172:175], a[220:223], a[180:183], v[172:175]\n"
    "v_mfma_f32_16x16x32_bf16 v[172:175], a[216:219], a[148:151], v[172:175]\n"
    "v_mfma_f32_16x16x32_bf16 v[204:207], a[216:219], a[152:155], v[204:207]\n"
    "v_mfma_f32_16x16x32_bf16 v[204:207], a[220:223], a[184:187], v[204:207]\n"
    "v_mfma_f32_16x16x32_bf16 v[236:239], a[220:223], a[188:191], v[236:239]\n"
    "v_mfma_f32_16x16x32_bf16 v[236:239], a[216:219], a[156:159], v[236:239]\n"
    "s_waitcnt vmcnt(8)\n"
    "v_mfma_f32_16x16x32_bf16 a[240:243], a[224:227], a[156:159], a[240:243]\n"
    "buffer_load_dwordx4 a[216:219], v240, s[20:23], s53 offen offset:2048 sc1\n"
    "v_mfma_f32_16x16x32_bf16 a[240:243], a[228:231], a[188:191], a[240:243]\n"
    "buffer_load_dwordx4 a[220:223], v240, s[20:23], s53 offen offset:3072 sc1\n"
    "v_mfma_f32_16x16x32_bf16 v[208:211], a[228:231], a[184:187], v[208:211]\n"
    "s_addk_i32 s53, 4096\n"
    "v_mfma_f32_16x16x32_bf16 v[208:211], a[224:227], a[152:155], v[208:211]\n"
    "v_mfma_f32_16x16x32_bf16 v[176:179], a[224:227], a[148:151], v[176:179]\n"
    "v_mfma_f32_16x16x32_bf16 v[176:179], a[228:231], a[180:183], v[176:179]\n"
    "v_mfma_f32_16x16x32_bf16 v[144:147], a[228:231], a[176:179], v[144:147]\n"
    "v_mfma_f32_16x16x32_bf16 v[144:147], a[224:227], a[144:147], v[144:147]\n"
    "v_mfma_f32_16x16x32_bf16 v[112:115], a[224:227], a[140:143], v[112:115]\n"
    "v_mfma_f32_16x16x32_bf16 v[112:115], a[228:231], a[172:175], v[112:115]\n"
    "v_mfma_f32_16x16x32_bf16 v[80:83], a[228:231], a[168:171], v[80:83]\n"
    "v_mfma_f32_16x16x32_bf16 v[80:83], a[224:227], a[136:139], v[80:83]\n"
    "v_mfma_f32_16x16x32_bf16 v[48:51], a[224:227], a[132:135], v[48:51]\n"
    "v_mfma_f32_16x16x32_bf16 v[48:51], a[228:231], a[164:167], v[48:51]\n"
    "v_mfma_f32_16x16x32_bf16 v[16:19], a[228:231], a[160:163], v[16:19]\n"
    "v_mfma_f32_16x16x32_bf16 v[16:19], a[224:227], a[128:131], v[16:19]\n"
    "s_waitcnt vmcnt(8)\n"
    "v_mfma_f32_16x16x32_bf16 v[20:23], a[232:235], a[128:131], v[20:23]\n"
    "buffer_load_dwordx4 a[224:227], v240, s[20:23], s53 offen offset:0 sc1\n"
    "v_mfma_f32_16x16x32_bf16 v[20:23], a[236:239], a[160:163], v[20:23]\n"
    "buffer_load_dwordx4 a[228:231], v240, s[20:23], s53 offen offset:1024 sc1\n"
    "v_mfma_f32_16x16x32_bf16 v[52:55], a[236:239], a[164:167], v[52:55]\n"
    "v_mfma_f32_16x16x32_bf16 v[52:55], a[232:235], a[132:135], v[52:55]\n"
    "v_mfma_f32_16x16x32_bf16 v[84:87], a[232:235], a[136:139], v[84:87]\n"
    "v_mfma_f32_16x16x32_bf16 v[84:87], a[236:239], a[168:171], v[84:87]\n"
    "v_mfma_f32_16x16x32_bf16 v[116:119], a[236:239], a[172:175], v[116:119]\n"
    "v_mfma_f32_16x16x32_bf16 v[116:119], a[232:235], a[140:143], v[116:119]\n"
    "v_mfma_f32_16x16x32_bf16 v[148:151], a[232:235], a[144:147], v[148:151]\n"
    "v_mfma_f32_16x16x32_bf16 v[148:151], a[236:239], a[176:179], v[148:151]\n"
    "v_mfma_f32_16x16x32_bf16 v[180:183], a[236:239], a[180:183], v[180:183]\n"
    "v_mfma_f32_16x16x32_bf16 v[180:183], a[232:235], a[148:151], v[180:183]\n"
    "v_mfma_f32_16x16x32_bf16 v[212:215], a[232:235], a[152:155], v[212:215]\n"
    "v_mfma_f32_16x16x32_bf16 v[212:215], a[236:239], a[184:187], v[212:215]\n"
    "v_mfma_f32_16x16x32_bf16 a[244:247], a[236:239], a[188:191], a[244:247]\n"
    "v_mfma_f32_16x16x32_bf16 a[244:247], a[232:235], a[156:159], a[244:247]\n"
    "s_waitcnt vmcnt(8)\n"
    "v_mfma_f32_16x16x32_bf16 a[248:251], a[192:195], a[156:159], a[248:251]\n"
    "buffer_load_dwordx4 a[232:235], v240, s[20:23], s53 offen offset:2048 sc1\n"
    "v_mfma_f32_16x16x32_bf16 a[248:251], a[196:199], a[188:191], a[248:251]\n"
    "buffer_load_dwordx4 a[236:239], v240, s[20:23], s53 offen offset:3072 sc1\n"
    "v_mfma_f32_16x16x32_bf16 v[216:219], a[196:199], a[184:187], v[216:219]\n"
    "s_addk_i32 s53, 4096\n"
    "v_mfma_f32_16x16x32_bf16 v[216:219], a[192:195], a[152:155], v[216:219]\n"
    "v_mfma_f32_16x16x32_bf16 v[184:187], a[192:195], a[148:151], v[184:187]\n"
    "v_mfma_f32_16x16x32_bf16 v[184:187], a[196:199], a[180:183], v[184:187]\n"
    "v_mfma_f32_16x16x32_bf16 v[152:155], a[196:199], a[176:179], v[152:155]\n"
    "v_mfma_f32_16x16x32_bf16 v[152:155], a[192:195], a[144:147], v[152:155]\n"
    "v_mfma_f32_16x16x32_bf16 v[120:123], a[192:195], a[140:143], v[120:123]\n"
    "v_mfma_f32_16x16x32_bf16 v[120:123], a[196:199], a[172:175], v[120:123]\n"
    "v_mfma_f32_16x16x32_bf16 v[88:91], a[196:199], a[168:171], v[88:91]\n"
    "v_mfma_f32_16x16x32_bf16 v[88:91], a[192:195], a[136:139], v[88:91]\n"
    "v_mfma_f32_16x16x32_bf16 v[56:59], a[192:195], a[132:135], v[56:59]\n"
    "v_mfma_f32_16x16x32_bf16 v[56:59], a[196:199], a[164:167], v[56:59]\n"
    "v_mfma_f32_16x16x32_bf16 v[24:27], a[196:199], a[160:163], v[24:27]\n"
    "v_mfma_f32_16x16x32_bf16 v[24:27], a[192:195], a[128:131], v[24:27]\n"
    "s_waitcnt vmcnt(8)\n"
    "v_mfma_f32_16x16x32_bf16 v[28:31], a[200:203], a[128:131], v[28:31]\n"
    "buffer_load_dwordx4 a[192:195], v240, s[20:23], s53 offen offset:0 sc1\n"
    "v_mfma_f32_16x16x32_bf16 v[28:31], a[204:207], a[160:163], v[28:31]\n"
    "buffer_load_dwordx4 a[196:199], v240, s[20:23], s53 offen offset:1024 sc1\n"
    "v_mfma_f32_16x16x32_bf16 v[60:63], a[204:207], a[164:167], v[60:63]\n"
    "ds_read_b128 a[128:131], v241 offset:49152\n"
    "v_mfma_f32_16x16x32_bf16 v[60:63], a[200:203], a[132:135], v[60:63]\n"
    "ds_read_b128 a[160:163], v241 offset:50176\n"
    "v_mfma_f32_16x16x32_bf16 v[92:95], a[200:203], a[136:139], v[92:95]\n"
    "ds_read_b128 a[164:167], v241 offset:52224\n"
    "v_mfma_f32_16x16x32_bf16 v[92:95], a[204:207], a[168:171], v[92:95]\n"
    "ds_read_b128 a[132:135], v241 offset:51200\n"
    "v_mfma_f32_16x16x32_bf16 v[124:127], a[204:207], a[172:175], v[124:127]\n"
    "ds_read_b128 a[136:139], v241 offset:53248\n"
    "v_mfma_f32_16x16x32_bf16 v[124:127], a[200:203], a[140:143], v[124:127]\n"
    "ds_read_b128 a[168:171], v241 offset:54272\n"
    "v_mfma_f32_16x16x32_bf16 v[156:159], a[200:203], a[144:147], v[156:159]\n"
    "ds_read_b128 a[172:175], v241 offset:56320\n"
    "v_mfma_f32_16x16x32_bf16 v[156:159], a[204:207], a[176:179], v[156:159]\n"
    "ds_read_b128 a[140:143], v241 offset:55296\n"
    "v_mfma_f32_16x16x32_bf16 v[188:191], a[204:207], a[180:183], v[188:191]\n"
    "ds_read_b128 a[144:147], v241 offset:57344\n"
    "v_mfma_f32_16x16x32_bf16 v[188:191], a[200:203], a[148:151], v[188:191]\n"
    "ds_read_b128 a[176:179], v241 offset:58368\n"
    "v_mfma_f32_16x16x32_bf16 v[220:223], a[200:203], a[152:155], v[220:223]\n"
    "ds_read_b128 a[180:183], v241 offset:60416\n"
    "v_mfma_f32_16x16x32_bf16 v[220:223], a[204:207], a[184:187], v[220:223]\n"
    "ds_read_b128 a[148:151], v241 offset:59392\n"
    "v_mfma_f32_16x16x32_bf16 a[252:255], a[204:207], a[188:191], a[252:255]\n"
    "ds_read_b128 a[152:155], v241 offset:61440\n"
    "v_mfma_f32_16x16x32_bf16 a[252:255], a[200:203], a[156:159], a[252:255]\n"
    "s_waitcnt vmcnt(8) lgkmcnt(11)\n"
    "ds_read_b128 a[184:187], v241 offset:62464\n"
    "v_mfma_f32_16x16x32_bf16 v[0:3], a[208:211], a[128:131], v[0:3]\n"
    "buffer_load_dwordx4 a[200:203], v240, s[20:23], s53 offen offset:2048 sc1\n"
    "v_mfma_f32_16x16x32_bf16 v[0:3], a[212:215], a[160:163], v[0:3]\n"
    "s_waitcnt lgkmcnt(10)\n"
    "v_mfma_f32_16x16x32_bf16 v[32:35], a[212:215], a[164:167], v[32:35]\n"
    "buffer_load_dwordx4 a[204:207], v240, s[20:23], s53 offen offset:3072 sc1\n"
    "v_mfma_f32_16x16x32_bf16 v[32:35], a[208:211], a[132:135], v[32:35]\n"
    "s_waitcnt lgkmcnt(8)\n"
    "s_addk_i32 s53, 4096\n"
    "ds_read_b128 a[188:191], v241 offset:64512\n"
    "v_mfma_f32_16x16x32_bf16 v[64:67], a[208:211], a[136:139], v[64:67]\n"
    "ds_read_b128 a[156:159], v241 offset:63488\n"
    "v_mfma_f32_16x16x32_bf16 v[64:67], a[212:215], a[168:171], v[64:67]\n"
    "s_waitcnt lgkmcnt(8)\n"
    "v_mfma_f32_16x16x32_bf16 v[96:99], a[212:215], a[172:175], v[96:99]\n"
    "v_mfma_f32_16x16x32_bf16 v[96:99], a[208:211], a[140:143], v[96:99]\n"
    "s_waitcnt lgkmcnt(6)\n"
    "v_mfma_f32_16x16x32_bf16 v[128:131], a[208:211], a[144:147], v[128:131]\n"
    "v_mfma_f32_16x16x32_bf16 v[128:131], a[212:215], a[176:179], v[128:131]\n"
    "s_waitcnt lgkmcnt(4)\n"
    "v_mfma_f32_16x16x32_bf16 v[160:163], a[212:215], a[180:183], v[160:163]\n"
    "v_mfma_f32_16x16x32_bf16 v[160:163], a[208:211], a[148:151], v[160:163]\n"
    "s_waitcnt lgkmcnt(2)\n"
    "v_mfma_f32_16x16x32_bf16 v[192:195], a[208:211], a[152:155], v[192:195]\n"
    "v_mfma_f32_16x16x32_bf16 v[192:195], a[212:215], a[184:187], v[192:195]\n"
    "s_waitcnt lgkmcnt(0)\n"
    "v_mfma_f32_16x16x32_bf16 v[224:227], a[212:215], a[188:191], v[224:227]\n"
    "v_mfma_f32_16x16x32_bf16 v[224:227], a[208:211], a[156:159], v[224:227]\n"
    "s_waitcnt vmcnt(8)\n"
    "v_mfma_f32_16x16x32_bf16 v[228:231], a[216:219], a[156:159], v[228:231]\n"
    "buffer_load_dwordx4 a[208:211], v240, s[20:23], s53 offen offset:0 sc1\n"
    "v_mfma_f32_16x16x32_bf16 v[228:231], a[220:223], a[188:191], v[228:231]\n"
    "buffer_load_dwordx4 a[212:215], v240, s[20:23], s53 offen offset:1024 sc1\n"
    "v_mfma_f32_16x16x32_bf16 v[196:199], a[220:223], a[184:187], v[196:199]\n"
    "v_mfma_f32_16x16x32_bf16 v[196:199], a[216:219], a[152:155], v[196:199]\n"
    "v_mfma_f32_16x16x32_bf16 v[164:167], a[216:219], a[148:151], v[164:167]\n"
    "v_mfma_f32_16x16x32_bf16 v[164:167], a[220:223], a[180:183], v[164:167]\n"
    "v_mfma_f32_16x16x32_bf16 v[132:135], a[220:223], a[176:179], v[132:135]\n"
    "v_mfma_f32_16x16x32_bf16 v[132:135], a[216:219], a[144:147], v[132:135]\n"
    "v_mfma_f32_16x16x32_bf16 v[100:103], a[216:219], a[140:143], v[100:103]\n"
    "v_mfma_f32_16x16x32_bf16 v[100:103], a[220:223], a[172:175], v[100:103]\n"
    "v_mfma_f32_16x16x32_bf16 v[68:71], a[220:223], a[168:171], v[68:71]\n"
    "v_mfma_f32_16x16x32_bf16 v[68:71], a[216:219], a[136:139], v[68:71]\n"
    "v_mfma_f32_16x16x32_bf16 v[36:39], a[216:219], a[132:135], v[36:39]\n"
    "v_mfma_f32_16x16x32_bf16 v[36:39], a[220:223], a[164:167], v[36:39]\n"
    "v_mfma_f32_16x16x32_bf16 v[4:7], a[220:223], a[160:163], v[4:7]\n"
    "v_mfma_f32_16x16x32_bf16 v[4:7], a[216:219], a[128:131], v[4:7]\n"
    "s_waitcnt vmcnt(8)\n"
    "v_mfma_f32_16x16x32_bf16 v[8:11], a[224:227], a[128:131], v[8:11]\n"
    "buffer_load_dwordx4 a[216:219], v240, s[20:23], s53 offen offset:2048 sc1\n"
    "v_mfma_f32_16x16x32_bf16 v[8:11], a[228:231], a[160:163], v[8:11]\n"
    "buffer_load_dwordx4 a[220:223], v240, s[20:23], s53 offen offset:3072 sc1\n"
    "v_mfma_f32_16x16x32_bf16 v[40:43], a[228:231], a[164:167], v[40:43]\n"
    "s_addk_i32 s53, 4096\n"
    "v_mfma_f32_16x16x32_bf16 v[40:43], a[224:227], a[132:135], v[40:43]\n"
    "v_mfma_f32_16x16x32_bf16 v[72:75], a[224:227], a[136:139], v[72:75]\n"
    "v_mfma_f32_16x16x32_bf16 v[72:75], a[228:231], a[168:171], v[72:75]\n"
    "v_mfma_f32_16x16x32_bf16 v[104:107], a[228:231], a[172:175], v[104:107]\n"
    "v_mfma_f32_16x16x32_bf16 v[104:107], a[224:227], a[140:143], v[104:107]\n"
    "v_mfma_f32_16x16x32_bf16 v[136:139], a[224:227], a[144:147], v[136:139]\n"
    "v_mfma_f32_16x16x32_bf16 v[136:139], a[228:231], a[176:179], v[136:139]\n"
    "v_mfma_f32_16x16x32_bf16 v[168:171], a[228:231], a[180:183], v[168:171]\n"
    "v_mfma_f32_16x16x32_bf16 v[168:171], a[224:227], a[148:151], v[168:171]\n"
    "v_mfma_f32_16x16x32_bf16 v[200:203], a[224:227], a[152:155], v[200:203]\n"
    "v_mfma_f32_16x16x32_bf16 v[200:203], a[228:231], a[184:187], v[200:203]\n"
    "v_mfma_f32_16x16x32_bf16 v[232:235], a[228:231], a[188:191], v[232:235]\n"
    "v_mfma_f32_16x16x32_bf16 v[232:235], a[224:227], a[156:159], v[232:235]\n"
    "s_waitcnt vmcnt(8)\n"
    "v_mfma_f32_16x16x32_bf16 v[236:239], a[232:235], a[156:159], v[236:239]\n"
    "buffer_load_dwordx4 a[224:227], v240, s[20:23], s53 offen offset:0 sc1\n"
    "v_mfma_f32_16x16x32_bf16 v[236:239], a[236:239], a[188:191], v[236:239]\n"
    "buffer_load_dwordx4 a[228:231], v240, s[20:23], s53 offen offset:1024 sc1\n"
    "v_mfma_f32_16x16x32_bf16 v[204:207], a[236:239], a[184:187], v[204:207]\n"
    "v_mfma_f32_16x16x32_bf16 v[204:207], a[232:235], a[152:155], v[204:207]\n"
    "v_mfma_f32_16x16x32_bf16 v[172:175], a[232:235], a[148:151], v[172:175]\n"
    "v_mfma_f32_16x16x32_bf16 v[172:175], a[236:239], a[180:183], v[172:175]\n"
    "v_mfma_f32_16x16x32_bf16 v[140:143], a[236:239], a[176:179], v[140:143]\n"
    "v_mfma_f32_16x16x32_bf16 v[140:143], a[232:235], a[144:147], v[140:143]\n"
    "v_mfma_f32_16x16x32_bf16 v[108:111], a[232:235], a[140:143], v[108:111]\n"
    "v_mfma_f32_16x16x32_bf16 v[108:111], a[236:239], a[172:175], v[108:111]\n"
    "v_mfma_f32_16x16x32_bf16 v[76:79], a[236:239], a[168:171], v[76:79]\n"
    "v_mfma_f32_16x16x32_bf16 v[76:79], a[232:235], a[136:139], v[76:79]\n"
    "v_mfma_f32_16x16x32_bf16 v[44:47], a[232:235], a[132:135], v[44:47]\n"
    "v_mfma_f32_16x16x32_bf16 v[44:47], a[236:239], a[164:167], v[44:47]\n"
    "v_mfma_f32_16x16x32_bf16 v[12:15], a[236:239], a[160:163], v[12:15]\n"
    "v_mfma_f32_16x16x32_bf16 v[12:15], a[232:235], a[128:131], v[12:15]\n"
    "s_waitcnt vmcnt(8)\n"
    "v_mfma_f32_16x16x32_bf16 v[16:19], a[192:195], a[128:131], v[16:19]\n"
    "buffer_load_dwordx4 a[232:235], v240, s[20:23], s53 offen offset:2048 sc1\n"
    "v_mfma_f32_16x16x32_bf16 v[16:19], a[196:199], a[160:163], v[16:19]\n"
    "buffer_load_dwordx4 a[236:239], v240, s[20:23], s53 offen offset:3072 sc1\n"
    "v_mfma_f32_16x16x32_bf16 v[48:51], a[196:199], a[164:167], v[48:51]\n"
    "s_addk_i32 s53, 4096\n"
    "v_mfma_f32_16x16x32_bf16 v[48:51], a[192:195], a[132:135], v[48:51]\n"
    "v_mfma_f32_16x16x32_bf16 v[80:83], a[192:195], a[136:139], v[80:83]\n"
    "v_mfma_f32_16x16x32_bf16 v[80:83], a[196:199], a[168:171], v[80:83]\n"
    "v_mfma_f32_16x16x32_bf16 v[112:115], a[196:199], a[172:175], v[112:115]\n"
    "v_mfma_f32_16x16x32_bf16 v[112:115], a[192:195], a[140:143], v[112:115]\n"
    "v_mfma_f32_16x16x32_bf16 v[144:147], a[192:195], a[144:147], v[144:147]\n"
    "v_mfma_f32_16x16x32_bf16 v[144:147], a[196:199], a[176:179], v[144:147]\n"
    "v_mfma_f32_16x16x32_bf16 v[176:179], a[196:199], a[180:183], v[176:179]\n"
    "v_mfma_f32_16x16x32_bf16 v[176:179], a[192:195], a[148:151], v[176:179]\n"
    "v_mfma_f32_16x16x32_bf16 v[208:211], a[192:195], a[152:155], v[208:211]\n"
    "v_mfma_f32_16x16x32_bf16 v[208:211], a[196:199], a[184:187], v[208:211]\n"
    "v_mfma_f32_16x16x32_bf16 a[240:243], a[196:199], a[188:191], a[240:243]\n"
    "v_mfma_f32_16x16x32_bf16 a[240:243], a[192:195], a[156:159], a[240:243]\n"
    "s_waitcnt vmcnt(8)\n"
    "v_mfma_f32_16x16x32_bf16 a[244:247], a[200:203], a[156:159], a[244:247]\n"
    "buffer_load_dwordx4 a[192:195], v240, s[20:23], s53 offen offset:0 sc1\n"
    "v_mfma_f32_16x16x32_bf16 a[244:247], a[204:207], a[188:191], a[244:247]\n"
    "buffer_load_dwordx4 a[196:199], v240, s[20:23], s53 offen offset:1024 sc1\n"
    "v_mfma_f32_16x16x32_bf16 v[212:215], a[204:207], a[184:187], v[212:215]\n"
    "v_mfma_f32_16x16x32_bf16 v[212:215], a[200:203], a[152:155], v[212:215]\n"
    "v_mfma_f32_16x16x32_bf16 v[180:183], a[200:203], a[148:151], v[180:183]\n"
    "v_mfma_f32_16x16x32_bf16 v[180:183], a[204:207], a[180:183], v[180:183]\n"
    "v_mfma_f32_16x16x32_bf16 v[148:151], a[204:207], a[176:179], v[148:151]\n"
    "v_mfma_f32_16x16x32_bf16 v[148:151], a[200:203], a[144:147], v[148:151]\n"
    "v_mfma_f32_16x16x32_bf16 v[116:119], a[200:203], a[140:143], v[116:119]\n"
    "v_mfma_f32_16x16x32_bf16 v[116:119], a[204:207], a[172:175], v[116:119]\n"
    "v_mfma_f32_16x16x32_bf16 v[84:87], a[204:207], a[168:171], v[84:87]\n"
    "v_mfma_f32_16x16x32_bf16 v[84:87], a[200:203], a[136:139], v[84:87]\n"
    "v_mfma_f32_16x16x32_bf16 v[52:55], a[200:203], a[132:135], v[52:55]\n"
    "v_mfma_f32_16x16x32_bf16 v[52:55], a[204:207], a[164:167], v[52:55]\n"
    "v_mfma_f32_16x16x32_bf16 v[20:23], a[204:207], a[160:163], v[20:23]\n"
    "v_mfma_f32_16x16x32_bf16 v[20:23], a[200:203], a[128:131], v[20:23]\n"
    "s_waitcnt vmcnt(8)\n"
    "v_mfma_f32_16x16x32_bf16 v[24:27], a[208:211], a[128:131], v[24:27]\n"
    "buffer_load_dwordx4 a[200:203], v240, s[20:23], s53 offen offset:2048 sc1\n"
    "v_mfma_f32_16x16x32_bf16 v[24:27], a[212:215], a[160:163], v[24:27]\n"
    "buffer_load_dwordx4 a[204:207], v240, s[20:23], s53 offen offset:3072 sc1\n"
    "v_mfma_f32_16x16x32_bf16 v[56:59], a[212:215], a[164:167], v[56:59]\n"
    "s_addk_i32 s53, 4096\n"
    "v_mfma_f32_16x16x32_bf16 v[56:59], a[208:211], a[132:135], v[56:59]\n"
    "v_mfma_f32_16x16x32_bf16 v[88:91], a[208:211], a[136:139], v[88:91]\n"
    "v_mfma_f32_16x16x32_bf16 v[88:91], a[212:215], a[168:171], v[88:91]\n"
    "v_mfma_f32_16x16x32_bf16 v[120:123], a[212:215], a[172:175], v[120:123]\n"
    "v_mfma_f32_16x16x32_bf16 v[120:123], a[208:211], a[140:143], v[120:123]\n"
    "v_mfma_f32_16x16x32_bf16 v[152:155], a[208:211], a[144:147], v[152:155]\n"
    "v_mfma_f32_16x16x32_bf16 v[152:155], a[212:215], a[176:179], v[152:155]\n"
    "v_mfma_f32_16x16x32_bf16 v[184:187], a[212:215], a[180:183], v[184:187]\n"
    "v_mfma_f32_16x16x32_bf16 v[184:187], a[208:211], a[148:151], v[184:187]\n"
    "v_mfma_f32_16x16x32_bf16 v[216:219], a[208:211], a[152:155], v[216:219]\n"
    "v_mfma_f32_16x16x32_bf16 v[216:219], a[212:215], a[184:187], v[216:219]\n"
    "s_barrier\n"
    "v_mfma_f32_16x16x32_bf16 a[248:251], a[212:215], a[188:191], a[248:251]\n"
    "v_mfma_f32_16x16x32_bf16 a[248:251], a[208:211], a[156:159], a[248:251]\n"
    "s_waitcnt vmcnt(8)\n"
    "v_mfma_f32_16x16x32_bf16 a[252:255], a[216:219], a[156:159], a[252:255]\n"
    "buffer_load_dwordx4 a[208:211], v240, s[20:23], s53 offen offset:0 sc1\n"
    "v_mfma_f32_16x16x32_bf16 a[252:255], a[220:223], a[188:191], a[252:255]\n"
    "buffer_load_dwordx4 a[212:215], v240, s[20:23], s53 offen offset:1024 sc1\n"
    "v_mfma_f32_16x16x32_bf16 v[220:223], a[220:223], a[184:187], v[220:223]\n"
    "v_add_u32 v241, s67, v240\n"
    "ds_read_b128 a[156:159], v241 offset:14336\n"
    "v_mfma_f32_16x16x32_bf16 v[220:223], a[216:219], a[152:155], v[220:223]\n"
    "ds_read_b128 a[188:191], v241 offset:15360\n"
    "v_mfma_f32_16x16x32_bf16 v[188:191], a[216:219], a[148:151], v[188:191]\n"
    "ds_read_b128 a[184:187], v241 offset:13312\n"
    "v_mfma_f32_16x16x32_bf16 v[188:191], a[220:223], a[180:183], v[188:191]\n"
    "ds_read_b128 a[152:155], v241 offset:12288\n"
    "v_mfma_f32_16x16x32_bf16 v[156:159], a[220:223], a[176:179], v[156:159]\n"
    "ds_read_b128 a[148:151], v241 offset:10240\n"
    "v_mfma_f32_16x16x32_bf16 v[156:159], a[216:219], a[144:147], v[156:159]\n"
    "ds_read_b128 a[180:183], v241 offset:11264\n"
    "v_mfma_f32_16x16x32_bf16 v[124:127], a[216:219], a[140:143], v[124:127]\n"
    "ds_read_b128 a[176:179], v241 offset:9216\n"
    "v_mfma_f32_16x16x32_bf16 v[124:127], a[220:223], a[172:175], v[124:127]\n"
    "ds_read_b128 a[144:147], v241 offset:8192\n"
    "v_mfma_f32_16x16x32_bf16 v[92:95], a[220:223], a[168:171], v[92:95]\n"
    "ds_read_b128 a[140:143], v241 offset:6144\n"
    "v_mfma_f32_16x16x32_bf16 v[92:95], a[216:219], a[136:139], v[92:95]\n"
    "ds_read_b128 a[172:175], v241 offset:7168\n"
    "v_mfma_f32_16x16x32_bf16 v[60:63], a[216:219], a[132:135], v[60:63]\n"
    "ds_read_b128 a[168:171], v241 offset:5120\n"
    "v_mfma_f32_16x16x32_bf16 v[60:63], a[220:223], a[164:167], v[60:63]\n"
    "ds_read_b128 a[136:139], v241 offset:4096\n"
    "v_mfma_f32_16x16x32_bf16 v[28:31], a[220:223], a[160:163], v[28:31]\n"
    "ds_read_b128 a[132:135], v241 offset:2048\n"
    "v_mfma_f32_16x16x32_bf16 v[28:31], a[216:219], a[128:131], v[28:31]\n"
    "s_waitcnt vmcnt(8) lgkmcnt(11)\n"
    "ds_read_b128 a[164:167], v241 offset:3072\n"
    "v_mfma_f32_16x16x32_bf16 v[224:227], a[224:227], a[156:159], v[224:227]\n"
    "buffer_load_dwordx4 a[216:219], v240, s[20:23], s53 offen offset:2048 sc1\n"
    "v_mfma_f32_16x16x32_bf16 v[224:227], a[228:231], a[188:191], v[224:227]\n"
    "s_waitcnt lgkmcnt(10)\n"
    "v_mfma_f32_16x16x32_bf16 v[192:195], a[228:231], a[184:187], v[192:195]\n"
    "buffer_load_dwordx4 a[220:223], v240, s[20:23], s53 offen offset:3072 sc1\n"
    "v_mfma_f32_16x16x32_bf16 v[192:195], a[224:227], a[152:155], v[192:195]\n"
    "s_waitcnt lgkmcnt(8)\n"
    "s_addk_i32 s53, 4096\n"
    "ds_read_b128 a[160:163], v241 offset:1024\n"
    "v_mfma_f32_16x16x32_bf16 v[160:163], a[224:227], a[148:151], v[160:163]\n"
    "ds_read_b128 a[128:131], v241 offset:0\n"
    "v_mfma_f32_16x16x32_bf16 v[160:163], a[228:231], a[180:183], v[160:163]\n"
    "s_waitcnt lgkmcnt(8)\n"
    "v_mfma_f32_16x16x32_bf16 v[128:131], a[228:231], a[176:179], v[128:131]\n"
    "v_mfma_f32_16x16x32_bf16 v[128:131], a[224:227], a[144:147], v[128:131]\n"
    "s_waitcnt lgkmcnt(6)\n"
    "v_mfma_f32_16x16x32_bf16 v[96:99], a[224:227], a[140:143], v[96:99]\n"
    "v_mfma_f32_16x16x32_bf16 v[96:99], a[228:231], a[172:175], v[96:99]\n"
    "s_waitcnt lgkmcnt(4)\n"
    "v_mfma_f32_16x16x32_bf16 v[64:67], a[228:231], a[168:171], v[64:67]\n"
    "v_mfma_f32_16x16x32_bf16 v[64:67], a[224:227], a[136:139], v[64:67]\n"
    "s_waitcnt lgkmcnt(2)\n"
    "v_mfma_f32_16x16x32_bf16 v[32:35], a[224:227], a[132:135], v[32:35]\n"
    "v_mfma_f32_16x16x32_bf16 v[32:35], a[228:231], a[164:167], v[32:35]\n"
    "s_waitcnt lgkmcnt(0)\n"
    "v_mfma_f32_16x16x32_bf16 v[0:3], a[228:231], a[160:163], v[0:3]\n"
    "v_mfma_f32_16x16x32_bf16 v[0:3], a[224:227], a[128:131], v[0:3]\n"
    "s_waitcnt vmcnt(8)\n"
    "v_mfma_f32_16x16x32_bf16 v[4:7], a[232:235], a[128:131], v[4:7]\n"
    "buffer_load_dwordx4 a[224:227], v240, s[20:23], s53 offen offset:0 sc1\n"
    "v_mfma_f32_16x16x32_bf16 v[4:7], a[236:239], a[160:163], v[4:7]\n"
    "buffer_load_dwordx4 a[228:231], v240, s[20:23], s53 offen offset:1024 sc1\n"
    "v_mfma_f32_16x16x32_bf16 v[36:39], a[236:239], a[164:167], v[36:39]\n"
    "v_mfma_f32_16x16x32_bf16 v[36:39], a[232:235], a[132:135], v[36:39]\n"
    "v_mfma_f32_16x16x32_bf16 v[68:71], a[232:235], a[136:139], v[68:71]\n"
    "v_mfma_f32_16x16x32_bf16 v[68:71], a[236:239], a[168:171], v[68:71]\n"
    "v_mfma_f32_16x16x32_bf16 v[100:103], a[236:239], a[172:175], v[100:103]\n"
    "v_mfma_f32_16x16x32_bf16 v[100:103], a[232:235], a[140:143], v[100:103]\n"
    "v_mfma_f32_16x16x32_bf16 v[132:135], a[232:235], a[144:147], v[132:135]\n"
    "v_mfma_f32_16x16x32_bf16 v[132:135], a[236:239], a[176:179], v[132:135]\n"
    "v_mfma_f32_16x16x32_bf16 v[164:167], a[236:239], a[180:183], v[164:167]\n"
    "v_mfma_f32_16x16x32_bf16 v[164:167], a[232:235], a[148:151], v[164:167]\n"
    "v_mfma_f32_16x16x32_bf16 v[196:199], a[232:235], a[152:155], v[196:199]\n"
    "v_mfma_f32_16x16x32_bf16 v[196:199], a[236:239], a[184:187], v[196:199]\n"
    "v_mfma_f32_16x16x32_bf16 v[228:231], a[236:239], a[188:191], v[228:231]\n"
    "v_mfma_f32_16x16x32_bf16 v[228:231], a[232:235], a[156:159], v[228:231]\n"
    "s_waitcnt vmcnt(8)\n"
    "v_mfma_f32_16x16x32_bf16 v[232:235], a[192:195], a[156:159], v[232:235]\n"
    "buffer_load_dwordx4 a[232:235], v240, s[20:23], s53 offen offset:2048 sc1\n"
    "v_mfma_f32_16x16x32_bf16 v[232:235], a[196:199], a[188:191], v[232:235]\n"
    "buffer_load_dwordx4 a[236:239], v240, s[20:23], s53 offen offset:3072 sc1\n"
    "v_mfma_f32_16x16x32_bf16 v[200:203], a[196:199], a[184:187], v[200:203]\n"
    "s_addk_i32 s53, 4096\n"
    "v_mfma_f32_16x16x32_bf16 v[200:203], a[192:195], a[152:155], v[200:203]\n"
    "v_mfma_f32_16x16x32_bf16 v[168:171], a[192:195], a[148:151], v[168:171]\n"
    "v_mfma_f32_16x16x32_bf16 v[168:171], a[196:199], a[180:183], v[168:171]\n"
    "v_mfma_f32_16x16x32_bf16 v[136:139], a[196:199], a[176:179], v[136:139]\n"
    "v_mfma_f32_16x16x32_bf16 v[136:139], a[192:195], a[144:147], v[136:139]\n"
    "v_mfma_f32_16x16x32_bf16 v[104:107], a[192:195], a[140:143], v[104:107]\n"
    "v_mfma_f32_16x16x32_bf16 v[104:107], a[196:199], a[172:175], v[104:107]\n"
    "v_mfma_f32_16x16x32_bf16 v[72:75], a[196:199], a[168:171], v[72:75]\n"
    "v_mfma_f32_16x16x32_bf16 v[72:75], a[192:195], a[136:139], v[72:75]\n"
    "v_mfma_f32_16x16x32_bf16 v[40:43], a[192:195], a[132:135], v[40:43]\n"
    "v_mfma_f32_16x16x32_bf16 v[40:43], a[196:199], a[164:167], v[40:43]\n"
    "v_mfma_f32_16x16x32_bf16 v[8:11], a[196:199], a[160:163], v[8:11]\n"
    "v_mfma_f32_16x16x32_bf16 v[8:11], a[192:195], a[128:131], v[8:11]\n"
    "s_waitcnt vmcnt(8)\n"
    "v_mfma_f32_16x16x32_bf16 v[12:15], a[200:203], a[128:131], v[12:15]\n"
    "buffer_load_dwordx4 a[192:195], v240, s[20:23], s53 offen offset:0 sc1\n"
    "v_mfma_f32_16x16x32_bf16 v[12:15], a[204:207], a[160:163], v[12:15]\n"
    "buffer_load_dwordx4 a[196:199], v240, s[20:23], s53 offen offset:1024 sc1\n"
    "v_mfma_f32_16x16x32_bf16 v[44:47], a[204:207], a[164:167], v[44:47]\n"
    "v_mfma_f32_16x16x32_bf16 v[44:47], a[200:203], a[132:135], v[44:47]\n"
    "v_mfma_f32_16x16x32_bf16 v[76:79], a[200:203], a[136:139], v[76:79]\n"
    "v_mfma_f32_16x16x32_bf16 v[76:79], a[204:207], a[168:171], v[76:79]\n"
    "v_mfma_f32_16x16x32_bf16 v[108:111], a[204:207], a[172:175], v[108:111]\n"
    "v_mfma_f32_16x16x32_bf16 v[108:111], a[200:203], a[140:143], v[108:111]\n"
    "v_mfma_f32_16x16x32_bf16 v[140:143], a[200:203], a[144:147], v[140:143]\n"
    "v_mfma_f32_16x16x32_bf16 v[140:143], a[204:207], a[176:179], v[140:143]\n"
    "v_mfma_f32_16x16x32_bf16 v[172:175], a[204:207], a[180:183], v[172:175]\n"
    "v_mfma_f32_16x16x32_bf16 v[172:175], a[200:203], a[148:151], v[172:175]\n"
    "v_mfma_f32_16x16x32_bf16 v[204:207], a[200:203], a[152:155], v[204:207]\n"
    "v_mfma_f32_16x16x32_bf16 v[204:207], a[204:207], a[184:187], v[204:207]\n"
    "v_mfma_f32_16x16x32_bf16 v[236:239], a[204:207], a[188:191], v[236:239]\n"
    "v_mfma_f32_16x16x32_bf16 v[236:239], a[200:203], a[156:159], v[236:239]\n"
    "s_waitcnt vmcnt(8)\n"
    "v_mfma_f32_16x16x32_bf16 a[240:243], a[208:211], a[156:159], a[240:243]\n"
    "buffer_load_dwordx4 a[200:203], v240, s[20:23], s53 offen offset:2048 sc1\n"
    "v_mfma_f32_16x16x32_bf16 a[240:243], a[212:215], a[188:191], a[240:243]\n"
    "buffer_load_dwordx4 a[204:207], v240, s[20:23], s53 offen offset:3072 sc1\n"
    "v_mfma_f32_16x16x32_bf16 v[208:211], a[212:215], a[184:187], v[208:211]\n"
    "s_addk_i32 s53, 4096\n"
    "v_mfma_f32_16x16x32_bf16 v[208:211], a[208:211], a[152:155], v[208:211]\n"
    "v_mfma_f32_16x16x32_bf16 v[176:179], a[208:211], a[148:151], v[176:179]\n"
    "v_mfma_f32_16x16x32_bf16 v[176:179], a[212:215], a[180:183], v[176:179]\n"
    "v_mfma_f32_16x16x32_bf16 v[144:147], a[212:215], a[176:179], v[144:147]\n"
    "v_mfma_f32_16x16x32_bf16 v[144:147], a[208:211], a[144:147], v[144:147]\n"
    "v_mfma_f32_16x16x32_bf16 v[112:115], a[208:211], a[140:143], v[112:115]\n"
    "v_mfma_f32_16x16x32_bf16 v[112:115], a[212:215], a[172:175], v[112:115]\n"
    "v_mfma_f32_16x16x32_bf16 v[80:83], a[212:215], a[168:171], v[80:83]\n"
    "v_mfma_f32_16x16x32_bf16 v[80:83], a[208:211], a[136:139], v[80:83]\n"
    "v_mfma_f32_16x16x32_bf16 v[48:51], a[208:211], a[132:135], v[48:51]\n"
    "v_mfma_f32_16x16x32_bf16 v[48:51], a[212:215], a[164:167], v[48:51]\n"
    "v_mfma_f32_16x16x32_bf16 v[16:19], a[212:215], a[160:163], v[16:19]\n"
    "v_mfma_f32_16x16x32_bf16 v[16:19], a[208:211], a[128:131], v[16:19]\n"
    "s_waitcnt vmcnt(8)\n"
    "v_mfma_f32_16x16x32_bf16 v[20:23], a[216:219], a[128:131], v[20:23]\n"
    "buffer_load_dwordx4 a[208:211], v240, s[20:23], s53 offen offset:0 sc1\n"
    "v_mfma_f32_16x16x32_bf16 v[20:23], a[220:223], a[160:163], v[20:23]\n"
    "buffer_load_dwordx4 a[212:215], v240, s[20:23], s53 offen offset:1024 sc1\n"
    "v_mfma_f32_16x16x32_bf16 v[52:55], a[220:223], a[164:167], v[52:55]\n"
    "v_mfma_f32_16x16x32_bf16 v[52:55], a[216:219], a[132:135], v[52:55]\n"
    "v_mfma_f32_16x16x32_bf16 v[84:87], a[216:219], a[136:139], v[84:87]\n"
    "v_mfma_f32_16x16x32_bf16 v[84:87], a[220:223], a[168:171], v[84:87]\n"
    "v_mfma_f32_16x16x32_bf16 v[116:119], a[220:223], a[172:175], v[116:119]\n"
    "v_mfma_f32_16x16x32_bf16 v[116:119], a[216:219], a[140:143], v[116:119]\n"
    "v_mfma_f32_16x16x32_bf16 v[148:151], a[216:219], a[144:147], v[148:151]\n"
    "v_mfma_f32_16x16x32_bf16 v[148:151], a[220:223], a[176:179], v[148:151]\n"
    "v_mfma_f32_16x16x32_bf16 v[180:183], a[220:223], a[180:183], v[180:183]\n"
    "v_mfma_f32_16x16x32_bf16 v[180:183], a[216:219], a[148:151], v[180:183]\n"
    "v_mfma_f32_16x16x32_bf16 v[212:215], a[216:219], a[152:155], v[212:215]\n"
    "v_mfma_f32_16x16x32_bf16 v[212:215], a[220:223], a[184:187], v[212:215]\n"
    "v_mfma_f32_16x16x32_bf16 a[244:247], a[220:223], a[188:191], a[244:247]\n"
    "v_mfma_f32_16x16x32_bf16 a[244:247], a[216:219], a[156:159], a[244:247]\n"
    "s_waitcnt vmcnt(8)\n"
    "v_mfma_f32_16x16x32_bf16 a[248:251], a[224:227], a[156:159], a[248:251]\n"
    "buffer_load_dwordx4 a[216:219], v240, s[20:23], s53 offen offset:2048 sc1\n"
    "v_mfma_f32_16x16x32_bf16 a[248:251], a[228:231], a[188:191], a[248:251]\n"
    "buffer_load_dwordx4 a[220:223], v240, s[20:23], s53 offen offset:3072 sc1\n"
    "v_mfma_f32_16x16x32_bf16 v[216:219], a[228:231], a[184:187], v[216:219]\n"
    "s_addk_i32 s53, 4096\n"
    "v_mfma_f32_16x16x32_bf16 v[216:219], a[224:227], a[152:155], v[216:219]\n"
    "v_mfma_f32_16x16x32_bf16 v[184:187], a[224:227], a[148:151], v[184:187]\n"
    "v_mfma_f32_16x16x32_bf16 v[184:187], a[228:231], a[180:183], v[184:187]\n"
    "v_mfma_f32_16x16x32_bf16 v[152:155], a[228:231], a[176:179], v[152:155]\n"
    "v_mfma_f32_16x16x32_bf16 v[152:155], a[224:227], a[144:147], v[152:155]\n"
    "v_mfma_f32_16x16x32_bf16 v[120:123], a[224:227], a[140:143], v[120:123]\n"
    "v_mfma_f32_16x16x32_bf16 v[120:123], a[228:231], a[172:175], v[120:123]\n"
    "v_mfma_f32_16x16x32_bf16 v[88:91], a[228:231], a[168:171], v[88:91]\n"
    "v_mfma_f32_16x16x32_bf16 v[88:91], a[224:227], a[136:139], v[88:91]\n"
    "v_mfma_f32_16x16x32_bf16 v[56:59], a[224:227], a[132:135], v[56:59]\n"
    "v_mfma_f32_16x16x32_bf16 v[56:59], a[228:231], a[164:167], v[56:59]\n"
    "v_mfma_f32_16x16x32_bf16 v[24:27], a[228:231], a[160:163], v[24:27]\n"
    "v_mfma_f32_16x16x32_bf16 v[24:27], a[224:227], a[128:131], v[24:27]\n"
    "s_waitcnt vmcnt(8)\n"
    "v_mfma_f32_16x16x32_bf16 v[28:31], a[232:235], a[128:131], v[28:31]\n"
    "buffer_load_dwordx4 a[224:227], v240, s[20:23], s53 offen offset:0 sc1\n"
    "v_mfma_f32_16x16x32_bf16 v[28:31], a[236:239], a[160:163], v[28:31]\n"
    "buffer_load_dwordx4 a[228:231], v240, s[20:23], s53 offen offset:1024 sc1\n"
    "v_mfma_f32_16x16x32_bf16 v[60:63], a[236:239], a[164:167], v[60:63]\n"
    "ds_read_b128 a[128:131], v241 offset:16384\n"
    "v_mfma_f32_16x16x32_bf16 v[60:63], a[232:235], a[132:135], v[60:63]\n"
    "ds_read_b128 a[160:163], v241 offset:17408\n"
    "v_mfma_f32_16x16x32_bf16 v[92:95], a[232:235], a[136:139], v[92:95]\n"
    "ds_read_b128 a[164:167], v241 offset:19456\n"
    "v_mfma_f32_16x16x32_bf16 v[92:95], a[236:239], a[168:171], v[92:95]\n"
    "ds_read_b128 a[132:135], v241 offset:18432\n"
    "v_mfma_f32_16x16x32_bf16 v[124:127], a[236:239], a[172:175], v[124:127]\n"
    "ds_read_b128 a[136:139], v241 offset:20480\n"
    "v_mfma_f32_16x16x32_bf16 v[124:127], a[232:235], a[140:143], v[124:127]\n"
    "ds_read_b128 a[168:171], v241 offset:21504\n"
    "v_mfma_f32_16x16x32_bf16 v[156:159], a[232:235], a[144:147], v[156:159]\n"
    "ds_read_b128 a[172:175], v241 offset:23552\n"
    "v_mfma_f32_16x16x32_bf16 v[156:159], a[236:239], a[176:179], v[156:159]\n"
    "ds_read_b128 a[140:143], v241 offset:22528\n"
    "v_mfma_f32_16x16x32_bf16 v[188:191], a[236:239], a[180:183], v[188:191]\n"
    "ds_read_b128 a[144:147], v241 offset:24576\n"
    "v_mfma_f32_16x16x32_bf16 v[188:191], a[232:235], a[148:151], v[188:191]\n"
    "ds_read_b128 a[176:179], v241 offset:25600\n"
    "v_mfma_f32_16x16x32_bf16 v[220:223], a[232:235], a[152:155], v[220:223]\n"
    "ds_read_b128 a[180:183], v241 offset:27648\n"
    "v_mfma_f32_16x16x32_bf16 v[220:223], a[236:239], a[184:187], v[220:223]\n"
    "ds_read_b128 a[148:151], v241 offset:26624\n"
    "v_mfma_f32_16x16x32_bf16 a[252:255], a[236:239], a[188:191], a[252:255]\n"
    "ds_read_b128 a[152:155], v241 offset:28672\n"
    "v_mfma_f32_16x16x32_bf16 a[252:255], a[232:235], a[156:159], a[252:255]\n"
    "s_waitcnt vmcnt(8) lgkmcnt(11)\n"
    "ds_read_b128 a[184:187], v241 offset:29696\n"
    "v_mfma_f32_16x16x32_bf16 v[0:3], a[192:195], a[128:131], v[0:3]\n"
    "buffer_load_dwordx4 a[232:235], v240, s[20:23], s53 offen offset:2048 sc1\n"
    "v_mfma_f32_16x16x32_bf16 v[0:3], a[196:199], a[160:163], v[0:3]\n"
    "s_waitcnt lgkmcnt(10)\n"
    "v_accvgpr_read_b32 v245, a0\n"
    "v_accvgpr_read_b32 v247, a1\n"
    "v_mfma_f32_16x16x32_bf16 v[32:35], a[196:199], a[164:167], v[32:35]\n"
    "buffer_load_dwordx4 a[236:239], v240, s[20:23], s53 offen offset:3072 sc1\n"
    "v_mfma_f32_16x16x32_bf16 v[32:35], a[192:195], a[132:135], v[32:35]\n"
    "s_waitcnt lgkmcnt(8)\n"
    "s_addk_i32 s53, 4096\n"
    "ds_read_b128 a[188:191], v241 offset:31744\n"
    "v_mfma_f32_16x16x32_bf16 v[64:67], a[192:195], a[136:139], v[64:67]\n"
    "ds_read_b128 a[156:159], v241 offset:30720\n"
    "v_lshlrev_b32 v244, 16, v245\n"
    "v_mfma_f32_16x16x32_bf16 v[64:67], a[196:199], a[168:171], v[64:67]\n"
    "s_waitcnt lgkmcnt(8)\n"
    "v_lshlrev_b32 v246, 16, v247\n"
    "v_and_b32 v245, s58, v245\n"
    "v_mfma_f32_16x16x32_bf16 v[96:99], a[196:199], a[172:175], v[96:99]\n"
    "v_and_b32 v247, s58, v247\n"
    "v_add_f32_dpp v254, v253, v0 row_newbcast:0 row_mask:0xf bank_mask:0xf\n"
    "v_mfma_f32_16x16x32_bf16 v[96:99], a[192:195], a[140:143], v[96:99]\n"
    "s_waitcnt lgkmcnt(6)\n"
    "v_add_f32_dpp v255, v253, v2 row_newbcast:2 row_mask:0xf bank_mask:0xf\n"
    "v_mul_f32 v244, v244, v254\n"
    "v_mfma_f32_16x16x32_bf16 v[128:131], a[192:195], a[144:147], v[128:131]\n"
    "v_mul_f32 v246, v246, v255\n"
    "v_add_f32_dpp v254, v253, v1 row_newbcast:1 row_mask:0xf bank_mask:0xf\n"
    "v_mfma_f32_16x16x32_bf16 v[128:131], a[196:199], a[176:179], v[128:131]\n"
    "s_waitcnt lgkmcnt(4)\n"
    "v_add_f32_dpp v255, v253, v3 row_newbcast:3 row_mask:0xf bank_mask:0xf\n"
    "v_mul_f32 v245, v245, v254\n"
    "v_mfma_f32_16x16x32_bf16 v[160:163], a[196:199], a[180:183], v[160:163]\n"
    "v_mul_f32 v247, v247, v255\n"
    "v_cvt_pk_bf16_f32 v248, v244, v245\n"
    "v_mfma_f32_16x16x32_bf16 v[160:163], a[192:195], a[148:151], v[160:163]\n"
    "s_waitcnt lgkmcnt(2)\n"
    "v_cvt_pk_bf16_f32 v249, v246, v247\n"
    "v_accvgpr_read_b32 v245, a2\n"
    "v_mfma_f32_16x16x32_bf16 v[192:195], a[192:195], a[152:155], v[192:195]\n"
    "v_accvgpr_read_b32 v247, a3\n"
    "v_lshlrev_b32 v244, 16, v245\n"
    "v_mfma_f32_16x16x32_bf16 v[192:195], a[196:199], a[184:187], v[192:195]\n"
    "s_waitcnt lgkmcnt(0)\n"
    "v_lshlrev_b32 v246, 16, v247\n"
    "v_and_b32 v245, s58, v245\n"
    "v_mfma_f32_16x16x32_bf16 v[224:227], a[196:199], a[188:191], v[224:227]\n"
    "v_and_b32 v247, s58, v247\n"
    "v_mfma_f32_16x16x32_bf16 v[224:227], a[192:195], a[156:159], v[224:227]\n"
    "s_waitcnt vmcnt(8)\n"
    "v_mfma_f32_16x16x32_bf16 v[228:231], a[200:203], a[156:159], v[228:231]\n"
    "buffer_load_dwordx4 a[192:195], v240, s[20:23], s53 offen offset:0 sc1\n"
    "v_mfma_f32_16x16x32_bf16 v[228:231], a[204:207], a[188:191], v[228:231]\n"
    "buffer_load_dwordx4 a[196:199], v240, s[20:23], s53 offen offset:1024 sc1\n"
    "v_mfma_f32_16x16x32_bf16 v[196:199], a[204:207], a[184:187], v[196:199]\n"
    "v_mfma_f32_16x16x32_bf16 v[196:199], a[200:203], a[152:155], v[196:199]\n"
    "v_mfma_f32_16x16x32_bf16 v[164:167], a[200:203], a[148:151], v[164:167]\n"
    "v_mfma_f32_16x16x32_bf16 v[164:167], a[204:207], a[180:183], v[164:167]\n"
    "v_mfma_f32_16x16x32_bf16 v[132:135], a[204:207], a[176:179], v[132:135]\n"
    "v_mfma_f32_16x16x32_bf16 v[132:135], a[200:203], a[144:147], v[132:135]\n"
    "v_mfma_f32_16x16x32_bf16 v[100:103], a[200:203], a[140:143], v[100:103]\n"
    "v_mfma_f32_16x16x32_bf16 v[100:103], a[204:207], a[172:175], v[100:103]\n"
    "v_mfma_f32_16x16x32_bf16 v[68:71], a[204:207], a[168:171], v[68:71]\n"
    "v_mfma_f32_16x16x32_bf16 v[68:71], a[200:203], a[136:139], v[68:71]\n"
    "v_mfma_f32_16x16x32_bf16 v[36:39], a[200:203], a[132:135], v[36:39]\n"
    "v_mfma_f32_16x16x32_bf16 v[36:39], a[204:207], a[164:167], v[36:39]\n"
    "v_mfma_f32_16x16x32_bf16 v[4:7], a[204:207], a[160:163], v[4:7]\n"
    "v_mfma_f32_16x16x32_bf16 v[4:7], a[200:203], a[128:131], v[4:7]\n"
    "s_waitcnt vmcnt(8)\n"
    "v_mfma_f32_16x16x32_bf16 v[8:11], a[208:211], a[128:131], v[8:11]\n"
    "buffer_load_dwordx4 a[200:203], v240, s[20:23], s53 offen offset:2048 sc1\n"
    "v_mfma_f32_16x16x32_bf16 v[8:11], a[212:215], a[160:163], v[8:11]\n"
    "buffer_load_dwordx4 a[204:207], v240, s[20:23], s53 offen offset:3072 sc1\n"
    "v_mfma_f32_16x16x32_bf16 v[40:43], a[212:215], a[164:167], v[40:43]\n"
    "s_addk_i32 s53, 4096\n"
    "s_xor_b32 s91, s54, 0x40000\n"
    "v_add_f32_dpp v254, v253, v4 row_newbcast:4 row_mask:0xf bank_mask:0xf\n"
    "v_mfma_f32_16x16x32_bf16 v[40:43], a[208:211], a[132:135], v[40:43]\n"
    "v_add_f32_dpp v255, v253, v6 row_newbcast:6 row_mask:0xf bank_mask:0xf\n"
    "v_mul_f32 v244, v244, v254\n"
    "v_mfma_f32_16x16x32_bf16 v[72:75], a[208:211], a[136:139], v[72:75]\n"
    "v_mul_f32 v246, v246, v255\n"
    "v_add_f32_dpp v254, v253, v5 row_newbcast:5 row_mask:0xf bank_mask:0xf\n"
    "v_mfma_f32_16x16x32_bf16 v[72:75], a[212:215], a[168:171], v[72:75]\n"
    "v_add_f32_dpp v255, v253, v7 row_newbcast:7 row_mask:0xf bank_mask:0xf\n"
    "v_mul_f32 v245, v245, v254\n"
    "v_mfma_f32_16x16x32_bf16 v[104:107], a[212:215], a[172:175], v[104:107]\n"
    "v_mul_f32 v247, v247, v255\n"
    "v_cvt_pk_bf16_f32 v250, v244, v245\n"
    "v_mfma_f32_16x16x32_bf16 v[104:107], a[208:211], a[140:143], v[104:107]\n"
    "v_cvt_pk_bf16_f32 v251, v246, v247\n"
    "v_accvgpr_read_b32 v245, a4\n"
    "v_mfma_f32_16x16x32_bf16 v[136:139], a[208:211], a[144:147], v[136:139]\n"
    "v_accvgpr_read_b32 v247, a5\n"
    "v_lshlrev_b32 v244, 16, v245\n"
    "v_mfma_f32_16x16x32_bf16 v[136:139], a[212:215], a[176:179], v[136:139]\n"
    "v_lshlrev_b32 v246, 16, v247\n"
    "v_and_b32 v245, s58, v245\n"
    "v_mfma_f32_16x16x32_bf16 v[168:171], a[212:215], a[180:183], v[168:171]\n"
    "v_and_b32 v247, s58, v247\n"
    "v_add_f32_dpp v254, v253, v32 row_newbcast:0 row_mask:0xf bank_mask:0xf\n"
    "v_mfma_f32_16x16x32_bf16 v[168:171], a[208:211], a[148:151], v[168:171]\n"
    "v_add_f32_dpp v255, v253, v34 row_newbcast:2 row_mask:0xf bank_mask:0xf\n"
    "v_mul_f32 v244, v244, v254\n"
    "v_mfma_f32_16x16x32_bf16 v[200:203], a[208:211], a[152:155], v[200:203]\n"
    "v_mul_f32 v246, v246, v255\n"
    "v_add_f32_dpp v254, v253, v33 row_newbcast:1 row_mask:0xf bank_mask:0xf\n"
    "v_mfma_f32_16x16x32_bf16 v[200:203], a[212:215], a[184:187], v[200:203]\n"
    "v_add_f32_dpp v255, v253, v35 row_newbcast:3 row_mask:0xf bank_mask:0xf\n"
    "v_mul_f32 v245, v245, v254\n"
    "v_mfma_f32_16x16x32_bf16 v[232:235], a[212:215], a[188:191], v[232:235]\n"
    "v_mul_f32 v247, v247, v255\n"
    "v_mfma_f32_16x16x32_bf16 v[232:235], a[208:211], a[156:159], v[232:235]\n"
    "s_waitcnt vmcnt(8)\n"
    "v_mfma_f32_16x16x32_bf16 v[236:239], a[216:219], a[156:159], v[236:239]\n"
    "buffer_load_dwordx4 a[208:211], v240, s[20:23], s53 offen offset:0 sc1\n"
    "v_mfma_f32_16x16x32_bf16 v[236:239], a[220:223], a[188:191], v[236:239]\n"
    "buffer_load_dwordx4 a[212:215], v240, s[20:23], s53 offen offset:1024 sc1\n"
    "v_mfma_f32_16x16x32_bf16 v[204:207], a[220:223], a[184:187], v[204:207]\n"
    "v_mfma_f32_16x16x32_bf16 v[204:207], a[216:219], a[152:155], v[204:207]\n"
    "buffer_store_dwordx4 v[248:251], v240, s[32:35], s91 offen sc1\n"
    "ds_write_b128 v243, v[248:251] offset:0\n"
    "v_mfma_f32_16x16x32_bf16 v[172:175], a[216:219], a[148:151], v[172:175]\n"
    "v_cvt_pk_bf16_f32 v248, v244, v245\n"
    "v_cvt_pk_bf16_f32 v249, v246, v247\n"
    "v_mfma_f32_16x16x32_bf16 v[172:175], a[220:223], a[180:183], v[172:175]\n"
    "v_accvgpr_read_b32 v245, a6\n"
    "v_accvgpr_read_b32 v247, a7\n"
    "v_mfma_f32_16x16x32_bf16 v[140:143], a[220:223], a[176:179], v[140:143]\n"
    "v_lshlrev_b32 v244, 16, v245\n"
    "v_lshlrev_b32 v246, 16, v247\n"
    "v_mfma_f32_16x16x32_bf16 v[140:143], a[216:219], a[144:147], v[140:143]\n"
    "v_and_b32 v245, s58, v245\n"
    "v_and_b32 v247, s58, v247\n"
    "v_mfma_f32_16x16x32_bf16 v[108:111], a[216:219], a[140:143], v[108:111]\n"
    "s_xor_b32 s91, s91, 0x800\n"
    "v_add_f32_dpp v254, v253, v36 row_newbcast:4 row_mask:0xf bank_mask:0xf\n"
    "v_add_f32_dpp v255, v253, v38 row_newbcast:6 row_mask:0xf bank_mask:0xf\n"
    "v_mfma_f32_16x16x32_bf16 v[108:111], a[220:223], a[172:175], v[108:111]\n"
    "v_mul_f32 v244, v244, v254\n"
    "v_mul_f32 v246, v246, v255\n"
    "v_mfma_f32_16x16x32_bf16 v[76:79], a[220:223], a[168:171], v[76:79]\n"
    "v_add_f32_dpp v254, v253, v37 row_newbcast:5 row_mask:0xf bank_mask:0xf\n"
    "v_add_f32_dpp v255, v253, v39 row_newbcast:7 row_mask:0xf bank_mask:0xf\n"
    "v_mfma_f32_16x16x32_bf16 v[76:79], a[216:219], a[136:139], v[76:79]\n"
    "v_mul_f32 v245, v245, v254\n"
    "v_mul_f32 v247, v247, v255\n"
    "v_mfma_f32_16x16x32_bf16 v[44:47], a[216:219], a[132:135], v[44:47]\n"
    "v_cvt_pk_bf16_f32 v250, v244, v245\n"
    "v_cvt_pk_bf16_f32 v251, v246, v247\n"
    "v_mfma_f32_16x16x32_bf16 v[44:47], a[220:223], a[164:167], v[44:47]\n"
    "v_accvgpr_read_b32 v245, a8\n"
    "v_accvgpr_read_b32 v247, a9\n"
    "v_mfma_f32_16x16x32_bf16 v[12:15], a[220:223], a[160:163], v[12:15]\n"
    "v_lshlrev_b32 v244, 16, v245\n"
    "v_lshlrev_b32 v246, 16, v247\n"
    "v_mfma_f32_16x16x32_bf16 v[12:15], a[216:219], a[128:131], v[12:15]\n"
    "s_waitcnt vmcnt(9)\n"
    "v_and_b32 v245, s58, v245\n"
    "v_and_b32 v247, s58, v247\n"
    "v_mfma_f32_16x16x32_bf16 v[16:19], a[224:227], a[128:131], v[16:19]\n"
    "buffer_load_dwordx4 a[216:219], v240, s[20:23], s53 offen offset:2048 sc1\n"
    "v_mfma_f32_16x16x32_bf16 v[16:19], a[228:231], a[160:163], v[16:19]\n"
    "buffer_load_dwordx4 a[220:223], v240, s[20:23], s53 offen offset:3072 sc1\n"
    "v_mfma_f32_16x16x32_bf16 v[48:51], a[228:231], a[164:167], v[48:51]\n"
    "s_addk_i32 s53, 4096\n"
    "v_add_f32_dpp v254, v253, v64 row_newbcast:0 row_mask:0xf bank_mask:0xf\n"
    "v_add_f32_dpp v255, v253, v66 row_newbcast:2 row_mask:0xf bank_mask:0xf\n"
    "v_mfma_f32_16x16x32_bf16 v[48:51], a[224:227], a[132:135], v[48:51]\n"
    "v_mul_f32 v244, v244, v254\n"
    "v_mul_f32 v246, v246, v255\n"
    "v_mfma_f32_16x16x32_bf16 v[80:83], a[224:227], a[136:139], v[80:83]\n"
    "v_add_f32_dpp v254, v253, v65 row_newbcast:1 row_mask:0xf bank_mask:0xf\n"
    "v_add_f32_dpp v255, v253, v67 row_newbcast:3 row_mask:0xf bank_mask:0xf\n"
    "v_mfma_f32_16x16x32_bf16 v[80:83], a[228:231], a[168:171], v[80:83]\n"
    "v_mul_f32 v245, v245, v254\n"
    "v_mul_f32 v247, v247, v255\n"
    "v_mfma_f32_16x16x32_bf16 v[112:115], a[228:231], a[172:175], v[112:115]\n"
    "v_mfma_f32_16x16x32_bf16 v[112:115], a[224:227], a[140:143], v[112:115]\n"
    "buffer_store_dwordx4 v[248:251], v240, s[32:35], s91 offen sc1\n"
    "ds_write_b128 v243, v[248:251] offset:2048\n"
    "v_mfma_f32_16x16x32_bf16 v[144:147], a[224:227], a[144:147], v[144:147]\n"
    "v_cvt_pk_bf16_f32 v248, v244, v245\n"
    "v_cvt_pk_bf16_f32 v249, v246, v247\n"
    "v_mfma_f32_16x16x32_bf16 v[144:147], a[228:231], a[176:179], v[144:147]\n"
    "v_accvgpr_read_b32 v245, a10\n"
    "v_accvgpr_read_b32 v247, a11\n"
    "v_mfma_f32_16x16x32_bf16 v[176:179], a[228:231], a[180:183], v[176:179]\n"
    "v_lshlrev_b32 v244, 16, v245\n"
    "v_lshlrev_b32 v246, 16, v247\n"
    "v_mfma_f32_16x16x32_bf16 v[176:179], a[224:227], a[148:151], v[176:179]\n"
    "v_and_b32 v245, s58, v245\n"
    "v_and_b32 v247, s58, v247\n"
    "v_mfma_f32_16x16x32_bf16 v[208:211], a[224:227], a[152:155], v[208:211]\n"
    "s_xor_b32 s91, s91, 0x101800\n"
    "v_add_f32_dpp v254, v253, v68 row_newbcast:4 row_mask:0xf bank_mask:0xf\n"
    "v_add_f32_dpp v255, v253, v70 row_newbcast:6 row_mask:0xf bank_mask:0xf\n"
    "v_mfma_f32_16x16x32_bf16 v[208:211], a[228:231], a[184:187], v[208:211]\n"
    "v_mul_f32 v244, v244, v254\n"
    "v_mul_f32 v246, v246, v255\n"
    "v_mfma_f32_16x16x32_bf16 a[240:243], a[228:231], a[188:191], a[240:243]\n"
    "v_add_f32_dpp v254, v253, v69 row_newbcast:5 row_mask:0xf bank_mask:0xf\n"
    "v_add_f32_dpp v255, v253, v71 row_newbcast:7 row_mask:0xf bank_mask:0xf\n"
    "v_mfma_f32_16x16x32_bf16 a[240:243], a[224:227], a[156:159], a[240:243]\n"
    "s_waitcnt vmcnt(10)\n"
    "v_mul_f32 v245, v245, v254\n"
    "v_mul_f32 v247, v247, v255\n"
    "v_mfma_f32_16x16x32_bf16 a[244:247], a[232:235], a[156:159], a[244:247]\n"
    "buffer_load_dwordx4 a[224:227], v240, s[20:23], s53 offen offset:0 sc1\n"
    "v_mfma_f32_16x16x32_bf16 a[244:247], a[236:239], a[188:191], a[244:247]\n"
    "buffer_load_dwordx4 a[228:231], v240, s[20:23], s53 offen offset:1024 sc1\n"
    "v_mfma_f32_16x16x32_bf16 v[212:215], a[236:239], a[184:187], v[212:215]\n"
    "v_cvt_pk_bf16_f32 v250, v244, v245\n"
    "v_cvt_pk_bf16_f32 v251, v246, v247\n"
    "v_mfma_f32_16x16x32_bf16 v[212:215], a[232:235], a[152:155], v[212:215]\n"
    "v_accvgpr_read_b32 v245, a12\n"
    "v_accvgpr_read_b32 v247, a13\n"
    "v_mfma_f32_16x16x32_bf16 v[180:183], a[232:235], a[148:151], v[180:183]\n"
    "v_lshlrev_b32 v244, 16, v245\n"
    "v_lshlrev_b32 v246, 16, v247\n"
    "v_mfma_f32_16x16x32_bf16 v[180:183], a[236:239], a[180:183], v[180:183]\n"
    "v_and_b32 v245, s58, v245\n"
    "v_and_b32 v247, s58, v247\n"
    "v_mfma_f32_16x16x32_bf16 v[148:151], a[236:239], a[176:179], v[148:151]\n"
    "v_add_f32_dpp v254, v253, v96 row_newbcast:0 row_mask:0xf bank_mask:0xf\n"
    "v_add_f32_dpp v255, v253, v98 row_newbcast:2 row_mask:0xf bank_mask:0xf\n"
    "v_mfma_f32_16x16x32_bf16 v[148:151], a[232:235], a[144:147], v[148:151]\n"
    "v_mul_f32 v244, v244, v254\n"
    "v_mul_f32 v246, v246, v255\n"
    "v_mfma_f32_16x16x32_bf16 v[116:119], a[232:235], a[140:143], v[116:119]\n"
    "v_add_f32_dpp v254, v253, v97 row_newbcast:1 row_mask:0xf bank_mask:0xf\n"
    "v_add_f32_dpp v255, v253, v99 row_newbcast:3 row_mask:0xf bank_mask:0xf\n"
    "v_mfma_f32_16x16x32_bf16 v[116:119], a[236:239], a[172:175], v[116:119]\n"
    "v_mul_f32 v245, v245, v254\n"
    "v_mul_f32 v247, v247, v255\n"
    "v_mfma_f32_16x16x32_bf16 v[84:87], a[236:239], a[168:171], v[84:87]\n"
    "v_mfma_f32_16x16x32_bf16 v[84:87], a[232:235], a[136:139], v[84:87]\n"
    "buffer_store_dwordx4 v[248:251], v240, s[32:35], s91 offen sc1\n"
    "ds_write_b128 v243, v[248:251] offset:4096\n"
    "v_mfma_f32_16x16x32_bf16 v[52:55], a[232:235], a[132:135], v[52:55]\n"
    "v_cvt_pk_bf16_f32 v248, v244, v245\n"
    "v_cvt_pk_bf16_f32 v249, v246, v247\n"
    "v_mfma_f32_16x16x32_bf16 v[52:55], a[236:239], a[164:167], v[52:55]\n"
    "v_accvgpr_read_b32 v245, a14\n"
    "v_accvgpr_read_b32 v247, a15\n"
    "v_mfma_f32_16x16x32_bf16 v[20:23], a[236:239], a[160:163], v[20:23]\n"
    "v_lshlrev_b32 v244, 16, v245\n"
    "v_lshlrev_b32 v246, 16, v247\n"
    "v_mfma_f32_16x16x32_bf16 v[20:23], a[232:235], a[128:131], v[20:23]\n"
    "s_waitcnt vmcnt(11)\n"
    "v_and_b32 v245, s58, v245\n"
    "v_and_b32 v247, s58, v247\n"
    "v_mfma_f32_16x16x32_bf16 v[24:27], a[192:195], a[128:131], v[24:27]\n"
    "buffer_load_dwordx4 a[232:235], v240, s[20:23], s53 offen offset:2048 sc1\n"
    "v_mfma_f32_16x16x32_bf16 v[24:27], a[196:199], a[160:163], v[24:27]\n"
    "buffer_load_dwordx4 a[236:239], v240, s[20:23], s53 offen offset:3072 sc1\n"
    "v_mfma_f32_16x16x32_bf16 v[56:59], a[196:199], a[164:167], v[56:59]\n"
    "s_addk_i32 s53, 4096\n"
    "s_xor_b32 s91, s91, 0x800\n"
    "v_add_f32_dpp v254, v253, v100 row_newbcast:4 row_mask:0xf bank_mask:0xf\n"
    "v_mfma_f32_16x16x32_bf16 v[56:59], a[192:195], a[132:135], v[56:59]\n"
    "v_add_f32_dpp v255, v253, v102 row_newbcast:6 row_mask:0xf bank_mask:0xf\n"
    "v_mul_f32 v244, v244, v254\n"
    "v_mfma_f32_16x16x32_bf16 v[88:91], a[192:195], a[136:139], v[88:91]\n"
    "v_mul_f32 v246, v246, v255\n"
    "v_add_f32_dpp v254, v253, v101 row_newbcast:5 row_mask:0xf bank_mask:0xf\n"
    "v_mfma_f32_16x16x32_bf16 v[88:91], a[196:199], a[168:171], v[88:91]\n"
    "v_add_f32_dpp v255, v253, v103 row_newbcast:7 row_mask:0xf bank_mask:0xf\n"
    "v_mul_f32 v245, v245, v254\n"
    "v_mfma_f32_16x16x32_bf16 v[120:123], a[196:199], a[172:175], v[120:123]\n"
    "v_mul_f32 v247, v247, v255\n"
    "v_cvt_pk_bf16_f32 v250, v244, v245\n"
    "v_mfma_f32_16x16x32_bf16 v[120:123], a[192:195], a[140:143], v[120:123]\n"
    "v_cvt_pk_bf16_f32 v251, v246, v247\n"
    "v_accvgpr_read_b32 v245, a16\n"
    "v_mfma_f32_16x16x32_bf16 v[152:155], a[192:195], a[144:147], v[152:155]\n"
    "v_accvgpr_read_b32 v247, a17\n"
    "v_lshlrev_b32 v244, 16, v245\n"
    "v_mfma_f32_16x16x32_bf16 v[152:155], a[196:199], a[176:179], v[152:155]\n"
    "v_lshlrev_b32 v246, 16, v247\n"
    "v_and_b32 v245, s58, v245\n"
    "v_mfma_f32_16x16x32_bf16 v[184:187], a[196:199], a[180:183], v[184:187]\n"
    "v_and_b32 v247, s58, v247\n"
    "v_add_f32_dpp v254, v253, v128 row_newbcast:0 row_mask:0xf bank_mask:0xf\n"
    "v_mfma_f32_16x16x32_bf16 v[184:187], a[192:195], a[148:151], v[184:187]\n"
    "v_add_f32_dpp v255, v253, v130 row_newbcast:2 row_mask:0xf bank_mask:0xf\n"
    "v_mul_f32 v244, v244, v254\n"
    "v_mfma_f32_16x16x32_bf16 v[216:219], a[192:195], a[152:155], v[216:219]\n"
    "v_mul_f32 v246, v246, v255\n"
    "v_add_f32_dpp v254, v253, v129 row_newbcast:1 row_mask:0xf bank_mask:0xf\n"
    "v_mfma_f32_16x16x32_bf16 v[216:219], a[196:199], a[184:187], v[216:219]\n"
    "v_add_f32_dpp v255, v253, v131 row_newbcast:3 row_mask:0xf bank_mask:0xf\n"
    "v_mul_f32 v245, v245, v254\n"
    "v_mfma_f32_16x16x32_bf16 a[248:251], a[196:199], a[188:191], a[248:251]\n"
    "v_mul_f32 v247, v247, v255\n"
    "v_mfma_f32_16x16x32_bf16 a[248:251], a[192:195], a[156:159], a[248:251]\n"
    "s_waitcnt vmcnt(11)\n"
    "v_mfma_f32_16x16x32_bf16 a[252:255], a[200:203], a[156:159], a[252:255]\n"
    "buffer_load_dwordx4 a[192:195], v240, s[20:23], s53 offen offset:0 sc1\n"
    "v_mfma_f32_16x16x32_bf16 a[252:255], a[204:207], a[188:191], a[252:255]\n"
    "buffer_load_dwordx4 a[196:199], v240, s[20:23], s53 offen offset:1024 sc1\n"
    "v_mfma_f32_16x16x32_bf16 v[220:223], a[204:207], a[184:187], v[220:223]\n"
    "v_add_u32 v241, s57, v240\n"
    "ds_read_b128 a[156:159], v241 offset:14336\n"
    "v_mfma_f32_16x16x32_bf16 v[220:223], a[200:203], a[152:155], v[220:223]\n"
    "buffer_store_dwordx4 v[248:251], v240, s[32:35], s91 offen sc1\n"
    "ds_write_b128 v243, v[248:251] offset:6144\n"
    "v_mfma_f32_16x16x32_bf16 v[188:191], a[200:203], a[148:151], v[188:191]\n"
    "ds_read_b128 a[188:191], v241 offset:15360\n"
    "ds_read_b128 a[184:187], v241 offset:13312\n"
    "v_mfma_f32_16x16x32_bf16 v[188:191], a[204:207], a[180:183], v[188:191]\n"
    "ds_read_b128 a[152:155], v241 offset:12288\n"
    "v_cvt_pk_bf16_f32 v248, v244, v245\n"
    "v_mfma_f32_16x16x32_bf16 v[156:159], a[204:207], a[176:179], v[156:159]\n"
    "ds_read_b128 a[148:151], v241 offset:10240\n"
    "v_cvt_pk_bf16_f32 v249, v246, v247\n"
    "v_mfma_f32_16x16x32_bf16 v[156:159], a[200:203], a[144:147], v[156:159]\n"
    "ds_read_b128 a[180:183], v241 offset:11264\n"
    "v_accvgpr_read_b32 v245, a18\n"
    "v_mfma_f32_16x16x32_bf16 v[124:127], a[200:203], a[140:143], v[124:127]\n"
    "ds_read_b128 a[176:179], v241 offset:9216\n"
    "v_accvgpr_read_b32 v247, a19\n"
    "v_mfma_f32_16x16x32_bf16 v[124:127], a[204:207], a[172:175], v[124:127]\n"
    "ds_read_b128 a[144:147], v241 offset:8192\n"
    "v_lshlrev_b32 v244, 16, v245\n"
    "v_mfma_f32_16x16x32_bf16 v[92:95], a[204:207], a[168:171], v[92:95]\n"
    "ds_read_b128 a[140:143], v241 offset:6144\n"
    "v_lshlrev_b32 v246, 16, v247\n"
    "v_mfma_f32_16x16x32_bf16 v[92:95], a[200:203], a[136:139], v[92:95]\n"
    "ds_read_b128 a[172:175], v241 offset:7168\n"
    "v_and_b32 v245, s58, v245\n"
    "v_mfma_f32_16x16x32_bf16 v[60:63], a[200:203], a[132:135], v[60:63]\n"
    "ds_read_b128 a[168:171], v241 offset:5120\n"
    "v_and_b32 v247, s58, v247\n"
    "v_mfma_f32_16x16x32_bf16 v[60:63], a[204:207], a[164:167], v[60:63]\n"
    "ds_read_b128 a[136:139], v241 offset:4096\n"
    "v_add_f32_dpp v254, v253, v132 row_newbcast:4 row_mask:0xf bank_mask:0xf\n"
    "v_mfma_f32_16x16x32_bf16 v[28:31], a[204:207], a[160:163], v[28:31]\n"
    "ds_read_b128 a[132:135], v241 offset:2048\n"
    "s_xor_b32 s91, s91, 0x185800\n"
    "v_mfma_f32_16x16x32_bf16 v[28:31], a[200:203], a[128:131], v[28:31]\n"
    "s_waitcnt vmcnt(12) lgkmcnt(11)\n"
    "ds_read_b128 a[164:167], v241 offset:3072\n"
    "v_add_f32_dpp v255, v253, v134 row_newbcast:6 row_mask:0xf bank_mask:0xf\n"
    "v_mfma_f32_16x16x32_bf16 a[240:243], a[208:211], a[156:159], a[240:243]\n"
    "buffer_load_dwordx4 a[200:203], v240, s[20:23], s53 offen offset:2048 sc1\n"
    "v_mfma_f32_16x16x32_bf16 a[240:243], a[212:215], a[188:191], a[240:243]\n"
    "s_waitcnt lgkmcnt(10)\n"
    "v_mul_f32 v244, v244, v254\n"
    "v_mul_f32 v246, v246, v255\n"
    "v_mfma_f32_16x16x32_bf16 v[208:211], a[212:215], a[184:187], v[208:211]\n"
    "buffer_load_dwordx4 a[204:207], v240, s[20:23], s53 offen offset:3072 sc1\n"
    "v_mfma_f32_16x16x32_bf16 v[208:211], a[208:211], a[152:155], v[208:211]\n"
    "s_waitcnt lgkmcnt(8)\n"
    "s_addk_i32 s53, 4096\n"
    "ds_read_b128 a[160:163], v241 offset:1024\n"
    "v_mfma_f32_16x16x32_bf16 v[176:179], a[208:211], a[148:151], v[176:179]\n"
    "ds_read_b128 a[128:131], v241 offset:0\n"
    "v_add_f32_dpp v254, v253, v133 row_newbcast:5 row_mask:0xf bank_mask:0xf\n"
    "v_mfma_f32_16x16x32_bf16 v[176:179], a[212:215], a[180:183], v[176:179]\n"
    "s_waitcnt lgkmcnt(8)\n"
    "v_add_f32_dpp v255, v253, v135 row_newbcast:7 row_mask:0xf bank_mask:0xf\n"
    "v_mul_f32 v245, v245, v254\n"
    "v_mfma_f32_16x16x32_bf16 v[144:147], a[212:215], a[176:179], v[144:147]\n"
    "v_mul_f32 v247, v247, v255\n"
    "v_cvt_pk_bf16_f32 v250, v244, v245\n"
    "v_mfma_f32_16x16x32_bf16 v[144:147], a[208:211], a[144:147], v[144:147]\n"
    "s_waitcnt lgkmcnt(6)\n"
    "v_cvt_pk_bf16_f32 v251, v246, v247\n"
    "v_accvgpr_read_b32 v245, a20\n"
    "v_mfma_f32_16x16x32_bf16 v[112:115], a[208:211], a[140:143], v[112:115]\n"
    "v_accvgpr_read_b32 v247, a21\n"
    "v_lshlrev_b32 v244, 16, v245\n"
    "v_mfma_f32_16x16x32_bf16 v[112:115], a[212:215], a[172:175], v[112:115]\n"
    "s_waitcnt lgkmcnt(4)\n"
    "v_lshlrev_b32 v246, 16, v247\n"
    "v_and_b32 v245, s58, v245\n"
    "v_mfma_f32_16x16x32_bf16 v[80:83], a[212:215], a[168:171], v[80:83]\n"
    "v_and_b32 v247, s58, v247\n"
    "v_add_f32_dpp v254, v253, v160 row_newbcast:0 row_mask:0xf bank_mask:0xf\n"
    "v_mfma_f32_16x16x32_bf16 v[80:83], a[208:211], a[136:139], v[80:83]\n"
    "s_waitcnt lgkmcnt(2)\n"
    "v_add_f32_dpp v255, v253, v162 row_newbcast:2 row_mask:0xf bank_mask:0xf\n"
    "v_mul_f32 v244, v244, v254\n"
    "v_mfma_f32_16x16x32_bf16 v[48:51], a[208:211], a[132:135], v[48:51]\n"
    "v_mul_f32 v246, v246, v255\n"
    "v_add_f32_dpp v254, v253, v161 row_newbcast:1 row_mask:0xf bank_mask:0xf\n"
    "v_mfma_f32_16x16x32_bf16 v[48:51], a[212:215], a[164:167], v[48:51]\n"
    "s_waitcnt lgkmcnt(0)\n"
    "v_add_f32_dpp v255, v253, v163 row_newbcast:3 row_mask:0xf bank_mask:0xf\n"
    "v_mul_f32 v245, v245, v254\n"
    "v_mfma_f32_16x16x32_bf16 v[16:19], a[212:215], a[160:163], v[16:19]\n"
    "v_mul_f32 v247, v247, v255\n"
    "v_mfma_f32_16x16x32_bf16 v[16:19], a[208:211], a[128:131], v[16:19]\n"
    "s_waitcnt vmcnt(11)\n"
    "v_mfma_f32_16x16x32_bf16 v[20:23], a[216:219], a[128:131], v[20:23]\n"
    "buffer_load_dwordx4 a[208:211], v240, s[20:23], s53 offen offset:0 sc1\n"
    "v_mfma_f32_16x16x32_bf16 v[20:23], a[220:223], a[160:163], v[20:23]\n"
    "buffer_load_dwordx4 a[212:215], v240, s[20:23], s53 offen offset:1024 sc1\n"
    "v_mfma_f32_16x16x32_bf16 v[52:55], a[220:223], a[164:167], v[52:55]\n"
    "v_mfma_f32_16x16x32_bf16 v[52:55], a[216:219], a[132:135], v[52:55]\n"
    "buffer_store_dwordx4 v[248:251], v240, s[32:35], s91 offen sc1\n"
    "ds_write_b128 v243, v[248:251] offset:8192\n"
    "v_mfma_f32_16x16x32_bf16 v[84:87], a[216:219], a[136:139], v[84:87]\n"
    "v_cvt_pk_bf16_f32 v248, v244, v245\n"
    "v_cvt_pk_bf16_f32 v249, v246, v247\n"
    "v_mfma_f32_16x16x32_bf16 v[84:87], a[220:223], a[168:171], v[84:87]\n"
    "v_accvgpr_read_b32 v245, a22\n"
    "v_accvgpr_read_b32 v247, a23\n"
    "v_mfma_f32_16x16x32_bf16 v[116:119], a[220:223], a[172:175], v[116:119]\n"
    "v_lshlrev_b32 v244, 16, v245\n"
    "v_lshlrev_b32 v246, 16, v247\n"
    "v_mfma_f32_16x16x32_bf16 v[116:119], a[216:219], a[140:143], v[116:119]\n"
    "v_and_b32 v245, s58, v245\n"
    "v_and_b32 v247, s58, v247\n"
    "v_mfma_f32_16x16x32_bf16 v[148:151], a[216:219], a[144:147], v[148:151]\n"
    "s_xor_b32 s91, s91, 0x800\n"
    "v_add_f32_dpp v254, v253, v164 row_newbcast:4 row_mask:0xf bank_mask:0xf\n"
    "v_add_f32_dpp v255, v253, v166 row_newbcast:6 row_mask:0xf bank_mask:0xf\n"
    "v_mfma_f32_16x16x32_bf16 v[148:151], a[220:223], a[176:179], v[148:151]\n"
    "v_mul_f32 v244, v244, v254\n"
    "v_mul_f32 v246, v246, v255\n"
    "v_mfma_f32_16x16x32_bf16 v[180:183], a[220:223], a[180:183], v[180:183]\n"
    "v_add_f32_dpp v254, v253, v165 row_newbcast:5 row_mask:0xf bank_mask:0xf\n"
    "v_add_f32_dpp v255, v253, v167 row_newbcast:7 row_mask:0xf bank_mask:0xf\n"
    "v_mfma_f32_16x16x32_bf16 v[180:183], a[216:219], a[148:151], v[180:183]\n"
    "v_mul_f32 v245, v245, v254\n"
    "v_mul_f32 v247, v247, v255\n"
    "v_mfma_f32_16x16x32_bf16 v[212:215], a[216:219], a[152:155], v[212:215]\n"
    "v_cvt_pk_bf16_f32 v250, v244, v245\n"
    "v_cvt_pk_bf16_f32 v251, v246, v247\n"
    "v_mfma_f32_16x16x32_bf16 v[212:215], a[220:223], a[184:187], v[212:215]\n"
    "v_accvgpr_read_b32 v245, a24\n"
    "v_accvgpr_read_b32 v247, a25\n"
    "v_mfma_f32_16x16x32_bf16 a[244:247], a[220:223], a[188:191], a[244:247]\n"
    "v_lshlrev_b32 v244, 16, v245\n"
    "v_lshlrev_b32 v246, 16, v247\n"
    "v_mfma_f32_16x16x32_bf16 a[244:247], a[216:219], a[156:159], a[244:247]\n"
    "s_waitcnt vmcnt(11)\n"
    "v_and_b32 v245, s58, v245\n"
    "v_and_b32 v247, s58, v247\n"
    "v_mfma_f32_16x16x32_bf16 a[248:251], a[224:227], a[156:159], a[248:251]\n"
    "buffer_load_dwordx4 a[216:219], v240, s[20:23], s53 offen offset:2048 sc1\n"
    "v_mfma_f32_16x16x32_bf16 a[248:251], a[228:231], a[188:191], a[248:251]\n"
    "buffer_load_dwordx4 a[220:223], v240, s[20:23], s53 offen offset:3072 sc1\n"
    "v_mfma_f32_16x16x32_bf16 v[216:219], a[228:231], a[184:187], v[216:219]\n"
    "s_addk_i32 s53, 4096\n"
    "v_add_f32_dpp v254, v253, v192 row_newbcast:0 row_mask:0xf bank_mask:0xf\n"
    "v_add_f32_dpp v255, v253, v194 row_newbcast:2 row_mask:0xf bank_mask:0xf\n"
    "v_mfma_f32_16x16x32_bf16 v[216:219], a[224:227], a[152:155], v[216:219]\n"
    "v_mul_f32 v244, v244, v254\n"
    "v_mul_f32 v246, v246, v255\n"
    "v_mfma_f32_16x16x32_bf16 v[184:187], a[224:227], a[148:151], v[184:187]\n"
    "v_add_f32_dpp v254, v253, v193 row_newbcast:1 row_mask:0xf bank_mask:0xf\n"
    "v_add_f32_dpp v255, v253, v195 row_newbcast:3 row_mask:0xf bank_mask:0xf\n"
    "v_mfma_f32_16x16x32_bf16 v[184:187], a[228:231], a[180:183], v[184:187]\n"
    "v_mul_f32 v245, v245, v254\n"
    "v_mul_f32 v247, v247, v255\n"
    "v_mfma_f32_16x16x32_bf16 v[152:155], a[228:231], a[176:179], v[152:155]\n"
    "buffer_store_dwordx4 v[248:251], v240, s[32:35], s91 offen sc1\n"
    "ds_write_b128 v243, v[248:251] offset:10240\n"
    "v_mfma_f32_16x16x32_bf16 v[152:155], a[224:227], a[144:147], v[152:155]\n"
    "v_cvt_pk_bf16_f32 v248, v244, v245\n"
    "v_cvt_pk_bf16_f32 v249, v246, v247\n"
    "v_mfma_f32_16x16x32_bf16 v[120:123], a[224:227], a[140:143], v[120:123]\n"
    "v_accvgpr_read_b32 v245, a26\n"
    "v_accvgpr_read_b32 v247, a27\n"
    "v_mfma_f32_16x16x32_bf16 v[120:123], a[228:231], a[172:175], v[120:123]\n"
    "v_lshlrev_b32 v244, 16, v245\n"
    "v_lshlrev_b32 v246, 16, v247\n"
    "v_mfma_f32_16x16x32_bf16 v[88:91], a[228:231], a[168:171], v[88:91]\n"
    "s_xor_b32 s91, s91, 0x101800\n"
    "v_and_b32 v245, s58, v245\n"
    "v_and_b32 v247, s58, v247\n"
    "v_mfma_f32_16x16x32_bf16 v[88:91], a[224:227], a[136:139], v[88:91]\n"
    "v_add_f32_dpp v254, v253, v196 row_newbcast:4 row_mask:0xf bank_mask:0xf\n"
    "v_add_f32_dpp v255, v253, v198 row_newbcast:6 row_mask:0xf bank_mask:0xf\n"
    "v_mfma_f32_16x16x32_bf16 v[56:59], a[224:227], a[132:135], v[56:59]\n"
    "v_mul_f32 v244, v244, v254\n"
    "v_mul_f32 v246, v246, v255\n"
    "v_mfma_f32_16x16x32_bf16 v[56:59], a[228:231], a[164:167], v[56:59]\n"
    "v_add_f32_dpp v254, v253, v197 row_newbcast:5 row_mask:0xf bank_mask:0xf\n"
    "v_add_f32_dpp v255, v253, v199 row_newbcast:7 row_mask:0xf bank_mask:0xf\n"
    "v_mfma_f32_16x16x32_bf16 v[24:27], a[228:231], a[160:163], v[24:27]\n"
    "v_mul_f32 v245, v245, v254\n"
    "v_mul_f32 v247, v247, v255\n"
    "v_mfma_f32_16x16x32_bf16 v[24:27], a[224:227], a[128:131], v[24:27]\n"
    "s_waitcnt vmcnt(11)\n"
    "v_cvt_pk_bf16_f32 v250, v244, v245\n"
    "v_cvt_pk_bf16_f32 v251, v246, v247\n"
    "v_mfma_f32_16x16x32_bf16 v[28:31], a[232:235], a[128:131], v[28:31]\n"
    "buffer_load_dwordx4 a[224:227], v240, s[20:23], s53 offen offset:0 sc1\n"
    "v_mfma_f32_16x16x32_bf16 v[28:31], a[236:239], a[160:163], v[28:31]\n"
    "buffer_load_dwordx4 a[228:231], v240, s[20:23], s53 offen offset:1024 sc1\n"
    "v_mfma_f32_16x16x32_bf16 v[60:63], a[236:239], a[164:167], v[60:63]\n"
    "ds_read_b128 a[128:131], v241 offset:16384\n"
    "v_accvgpr_read_b32 v245, a28\n"
    "v_mfma_f32_16x16x32_bf16 v[60:63], a[232:235], a[132:135], v[60:63]\n"
    "ds_read_b128 a[160:163], v241 offset:17408\n"
    "v_accvgpr_read_b32 v247, a29\n"
    "v_mfma_f32_16x16x32_bf16 v[92:95], a[232:235], a[136:139], v[92:95]\n"
    "ds_read_b128 a[164:167], v241 offset:19456\n"
    "v_lshlrev_b32 v244, 16, v245\n"
    "v_mfma_f32_16x16x32_bf16 v[92:95], a[236:239], a[168:171], v[92:95]\n"
    "ds_read_b128 a[132:135], v241 offset:18432\n"
    "v_lshlrev_b32 v246, 16, v247\n"
    "v_mfma_f32_16x16x32_bf16 v[124:127], a[236:239], a[172:175], v[124:127]\n"
    "ds_read_b128 a[136:139], v241 offset:20480\n"
    "v_and_b32 v245, s58, v245\n"
    "v_mfma_f32_16x16x32_bf16 v[124:127], a[232:235], a[140:143], v[124:127]\n"
    "ds_read_b128 a[168:171], v241 offset:21504\n"
    "v_and_b32 v247, s58, v247\n"
    "v_mfma_f32_16x16x32_bf16 v[156:159], a[232:235], a[144:147], v[156:159]\n"
    "ds_read_b128 a[172:175], v241 offset:23552\n"
    "v_add_f32_dpp v254, v253, v224 row_newbcast:0 row_mask:0xf bank_mask:0xf\n"
    "v_mfma_f32_16x16x32_bf16 v[156:159], a[236:239], a[176:179], v[156:159]\n"
    "ds_read_b128 a[140:143], v241 offset:22528\n"
    "v_add_f32_dpp v255, v253, v226 row_newbcast:2 row_mask:0xf bank_mask:0xf\n"
    "v_mfma_f32_16x16x32_bf16 v[188:191], a[236:239], a[180:183], v[188:191]\n"
    "ds_read_b128 a[144:147], v241 offset:24576\n"
    "v_mul_f32 v244, v244, v254\n"
    "v_mfma_f32_16x16x32_bf16 v[188:191], a[232:235], a[148:151], v[188:191]\n"
    "ds_read_b128 a[176:179], v241 offset:25600\n"
    "v_mul_f32 v246, v246, v255\n"
    "v_mfma_f32_16x16x32_bf16 v[220:223], a[232:235], a[152:155], v[220:223]\n"
    "ds_read_b128 a[180:183], v241 offset:27648\n"
    "v_add_f32_dpp v254, v253, v225 row_newbcast:1 row_mask:0xf bank_mask:0xf\n"
    "v_mfma_f32_16x16x32_bf16 v[220:223], a[236:239], a[184:187], v[220:223]\n"
    "ds_read_b128 a[148:151], v241 offset:26624\n"
    "v_add_f32_dpp v255, v253, v227 row_newbcast:3 row_mask:0xf bank_mask:0xf\n"
    "v_mfma_f32_16x16x32_bf16 a[252:255], a[236:239], a[188:191], a[252:255]\n"
    "ds_read_b128 a[152:155], v241 offset:28672\n"
    "v_mul_f32 v245, v245, v254\n"
    "v_mfma_f32_16x16x32_bf16 a[252:255], a[232:235], a[156:159], a[252:255]\n"
    "s_waitcnt vmcnt(11) lgkmcnt(11)\n"
    "ds_read_b128 a[184:187], v241 offset:29696\n"
    "v_mul_f32 v247, v247, v255\n"
    "v_mfma_f32_16x16x32_bf16 v[16:19], a[192:195], a[128:131], v[16:19]\n"
    "buffer_load_dwordx4 a[232:235], v240, s[20:23], s53 offen offset:2048 sc1\n"
    "v_mfma_f32_16x16x32_bf16 v[16:19], a[196:199], a[160:163], v[16:19]\n"
    "s_waitcnt lgkmcnt(10)\n"
    "v_mfma_f32_16x16x32_bf16 v[48:51], a[196:199], a[164:167], v[48:51]\n"
    "buffer_load_dwordx4 a[236:239], v240, s[20:23], s53 offen offset:3072 sc1\n"
    "v_mfma_f32_16x16x32_bf16 v[48:51], a[192:195], a[132:135], v[48:51]\n"
    "s_waitcnt lgkmcnt(8)\n"
    "s_addk_i32 s53, 4096\n"
    "ds_read_b128 a[188:191], v241 offset:31744\n"
    "v_mfma_f32_16x16x32_bf16 v[80:83], a[192:195], a[136:139], v[80:83]\n"
    "ds_read_b128 a[156:159], v241 offset:30720\n"
    "v_mfma_f32_16x16x32_bf16 v[80:83], a[196:199], a[168:171], v[80:83]\n"
    "s_waitcnt lgkmcnt(8)\n"
    "v_mfma_f32_16x16x32_bf16 v[112:115], a[196:199], a[172:175], v[112:115]\n"
    "buffer_store_dwordx4 v[248:251], v240, s[32:35], s91 offen sc1\n"
    "ds_write_b128 v243, v[248:251] offset:12288\n"
    "v_mfma_f32_16x16x32_bf16 v[112:115], a[192:195], a[140:143], v[112:115]\n"
    "s_waitcnt lgkmcnt(7)\n"
    "v_cvt_pk_bf16_f32 v248, v244, v245\n"
    "v_cvt_pk_bf16_f32 v249, v246, v247\n"
    "v_mfma_f32_16x16x32_bf16 v[144:147], a[192:195], a[144:147], v[144:147]\n"
    "v_accvgpr_read_b32 v245, a30\n"
    "v_accvgpr_read_b32 v247, a31\n"
    "v_mfma_f32_16x16x32_bf16 v[144:147], a[196:199], a[176:179], v[144:147]\n"
    "s_waitcnt lgkmcnt(5)\n"
    "v_lshlrev_b32 v244, 16, v245\n"
    "v_lshlrev_b32 v246, 16, v247\n"
    "v_mfma_f32_16x16x32_bf16 v[176:179], a[196:199], a[180:183], v[176:179]\n"
    "v_and_b32 v245, s58, v245\n"
    "v_and_b32 v247, s58, v247\n"
    "v_mfma_f32_16x16x32_bf16 v[176:179], a[192:195], a[148:151], v[176:179]\n"
    "s_waitcnt lgkmcnt(3)\n"
    "s_xor_b32 s91, s91, 0x800\n"
    "v_add_f32_dpp v254, v253, v228 row_newbcast:4 row_mask:0xf bank_mask:0xf\n"
    "v_mfma_f32_16x16x32_bf16 v[208:211], a[192:195], a[152:155], v[208:211]\n"
    "v_add_f32_dpp v255, v253, v230 row_newbcast:6 row_mask:0xf bank_mask:0xf\n"
    "v_mul_f32 v244, v244, v254\n"
    "v_mfma_f32_16x16x32_bf16 v[208:211], a[196:199], a[184:187], v[208:211]\n"
    "s_waitcnt lgkmcnt(1)\n"
    "v_mul_f32 v246, v246, v255\n"
    "v_add_f32_dpp v254, v253, v229 row_newbcast:5 row_mask:0xf bank_mask:0xf\n"
    "v_mfma_f32_16x16x32_bf16 a[240:243], a[196:199], a[188:191], a[240:243]\n"
    "v_add_f32_dpp v255, v253, v231 row_newbcast:7 row_mask:0xf bank_mask:0xf\n"
    "v_mul_f32 v245, v245, v254\n"
    "v_mfma_f32_16x16x32_bf16 a[240:243], a[192:195], a[156:159], a[240:243]\n"
    "s_waitcnt vmcnt(11)\n"
    "v_mul_f32 v247, v247, v255\n"
    "v_cvt_pk_bf16_f32 v250, v244, v245\n"
    "v_mfma_f32_16x16x32_bf16 a[244:247], a[200:203], a[156:159], a[244:247]\n"
    "buffer_load_dwordx4 a[192:195], v240, s[20:23], s53 offen offset:0 sc1\n"
    "v_mfma_f32_16x16x32_bf16 a[244:247], a[204:207], a[188:191], a[244:247]\n"
    "buffer_load_dwordx4 a[196:199], v240, s[20:23], s53 offen offset:1024 sc1\n"
    "v_mfma_f32_16x16x32_bf16 v[212:215], a[204:207], a[184:187], v[212:215]\n"
    "v_cvt_pk_bf16_f32 v251, v246, v247\n"
    "v_accvgpr_read_b32 v245, a32\n"
    "v_mfma_f32_16x16x32_bf16 v[212:215], a[200:203], a[152:155], v[212:215]\n"
    "buffer_store_dwordx4 v[248:251], v240, s[32:35], s91 offen sc1\n"
    "ds_write_b128 v243, v[248:251] offset:14336\n"
    "v_mfma_f32_16x16x32_bf16 v[180:183], a[200:203], a[148:151], v[180:183]\n"
    "v_accvgpr_read_b32 v247, a33\n"
    "v_lshlrev_b32 v244, 16, v245\n"
    "v_mfma_f32_16x16x32_bf16 v[180:183], a[204:207], a[180:183], v[180:183]\n"
    "v_lshlrev_b32 v246, 16, v247\n"
    "v_and_b32 v245, s58, v245\n"
    "v_mfma_f32_16x16x32_bf16 v[148:151], a[204:207], a[176:179], v[148:151]\n"
    "v_and_b32 v247, s58, v247\n"
    "v_add_f32_dpp v254, v253, v8 row_newbcast:8 row_mask:0xf bank_mask:0xf\n"
    "v_mfma_f32_16x16x32_bf16 v[148:151], a[200:203], a[144:147], v[148:151]\n"
    "v_add_f32_dpp v255, v253, v10 row_newbcast:10 row_mask:0xf bank_mask:0xf\n"
    "v_mul_f32 v244, v244, v254\n"
    "v_mfma_f32_16x16x32_bf16 v[116:119], a[200:203], a[140:143], v[116:119]\n"
    "v_mul_f32 v246, v246, v255\n"
    "v_add_f32_dpp v254, v253, v9 row_newbcast:9 row_mask:0xf bank_mask:0xf\n"
    "v_mfma_f32_16x16x32_bf16 v[116:119], a[204:207], a[172:175], v[116:119]\n"
    "v_add_f32_dpp v255, v253, v11 row_newbcast:11 row_mask:0xf bank_mask:0xf\n"
    "v_mul_f32 v245, v245, v254\n"
    "v_mfma_f32_16x16x32_bf16 v[84:87], a[204:207], a[168:171], v[84:87]\n"
    "s_xor_b32 s91, s91, 0x195800\n"
    "v_mul_f32 v247, v247, v255\n"
    "v_cvt_pk_bf16_f32 v248, v244, v245\n"
    "v_mfma_f32_16x16x32_bf16 v[84:87], a[200:203], a[136:139], v[84:87]\n"
    "v_cvt_pk_bf16_f32 v249, v246, v247\n"
    "v_accvgpr_read_b32 v245, a34\n"
    "v_mfma_f32_16x16x32_bf16 v[52:55], a[200:203], a[132:135], v[52:55]\n"
    "v_accvgpr_read_b32 v247, a35\n"
    "v_lshlrev_b32 v244, 16, v245\n"
    "v_mfma_f32_16x16x32_bf16 v[52:55], a[204:207], a[164:167], v[52:55]\n"
    "v_lshlrev_b32 v246, 16, v247\n"
    "v_and_b32 v245, s58, v245\n"
    "v_mfma_f32_16x16x32_bf16 v[20:23], a[204:207], a[160:163], v[20:23]\n"
    "v_and_b32 v247, s58, v247\n"
    "v_add_f32_dpp v254, v253, v12 row_newbcast:12 row_mask:0xf bank_mask:0xf\n"
    "v_mfma_f32_16x16x32_bf16 v[20:23], a[200:203], a[128:131], v[20:23]\n"
    "s_waitcnt vmcnt(12)\n"
    "v_add_f32_dpp v255, v253, v14 row_newbcast:14 row_mask:0xf bank_mask:0xf\n"
    "v_mul_f32 v244, v244, v254\n"
    "v_mfma_f32_16x16x32_bf16 v[24:27], a[208:211], a[128:131], v[24:27]\n"
    "buffer_load_dwordx4 a[200:203], v240, s[20:23], s53 offen offset:2048 sc1\n"
    "v_mfma_f32_16x16x32_bf16 v[24:27], a[212:215], a[160:163], v[24:27]\n"
    "buffer_load_dwordx4 a[204:207], v240, s[20:23], s53 offen offset:3072 sc1\n"
    "v_mfma_f32_16x16x32_bf16 v[56:59], a[212:215], a[164:167], v[56:59]\n"
    "s_addk_i32 s53, 4096\n"
    "v_mul_f32 v246, v246, v255\n"
    "v_add_f32_dpp v254, v253, v13 row_newbcast:13 row_mask:0xf bank_mask:0xf\n"
    "v_mfma_f32_16x16x32_bf16 v[56:59], a[208:211], a[132:135], v[56:59]\n"
    "v_add_f32_dpp v255, v253, v15 row_newbcast:15 row_mask:0xf bank_mask:0xf\n"
    "v_mul_f32 v245, v245, v254\n"
    "v_mfma_f32_16x16x32_bf16 v[88:91], a[208:211], a[136:139], v[88:91]\n"
    "v_mul_f32 v247, v247, v255\n"
    "v_cvt_pk_bf16_f32 v250, v244, v245\n"
    "v_mfma_f32_16x16x32_bf16 v[88:91], a[212:215], a[168:171], v[88:91]\n"
    "v_cvt_pk_bf16_f32 v251, v246, v247\n"
    "v_accvgpr_read_b32 v245, a36\n"
    "v_mfma_f32_16x16x32_bf16 v[120:123], a[212:215], a[172:175], v[120:123]\n"
    "buffer_store_dwordx4 v[248:251], v240, s[32:35], s91 offen sc1\n"
    "ds_write_b128 v243, v[248:251] offset:32768\n"
    "v_accvgpr_read_b32 v247, a37\n"
    "v_lshlrev_b32 v244, 16, v245\n"
    "v_lshlrev_b32 v246, 16, v247\n"
    "v_mfma_f32_16x16x32_bf16 v[120:123], a[208:211], a[140:143], v[120:123]\n"
    "s_xor_b32 s91, s91, 0x800\n"
    "v_and_b32 v245, s58, v245\n"
    "v_and_b32 v247, s58, v247\n"
    "v_add_f32_dpp v254, v253, v40 row_newbcast:8 row_mask:0xf bank_mask:0xf\n"
    "v_mfma_f32_16x16x32_bf16 v[152:155], a[208:211], a[144:147], v[152:155]\n"
    "v_add_f32_dpp v255, v253, v42 row_newbcast:10 row_mask:0xf bank_mask:0xf\n"
    "v_mul_f32 v244, v244, v254\n"
    "v_mfma_f32_16x16x32_bf16 v[152:155], a[212:215], a[176:179], v[152:155]\n"
    "v_mul_f32 v246, v246, v255\n"
    "v_add_f32_dpp v254, v253, v41 row_newbcast:9 row_mask:0xf bank_mask:0xf\n"
    "v_add_f32_dpp v255, v253, v43 row_newbcast:11 row_mask:0xf bank_mask:0xf\n"
    "v_mfma_f32_16x16x32_bf16 v[184:187], a[212:215], a[180:183], v[184:187]\n"
    "v_mul_f32 v245, v245, v254\n"
    "v_mul_f32 v247, v247, v255\n"
    "v_mfma_f32_16x16x32_bf16 v[184:187], a[208:211], a[148:151], v[184:187]\n"
    "v_cvt_pk_bf16_f32 v248, v244, v245\n"
    "v_cvt_pk_bf16_f32 v249, v246, v247\n"
    "v_mfma_f32_16x16x32_bf16 v[216:219], a[208:211], a[152:155], v[216:219]\n"
    "v_accvgpr_read_b32 v245, a38\n"
    "v_accvgpr_read_b32 v247, a39\n"
    "v_mfma_f32_16x16x32_bf16 v[216:219], a[212:215], a[184:187], v[216:219]\n"
    "v_lshlrev_b32 v244, 16, v245\n"
    "v_lshlrev_b32 v246, 16, v247\n"
    "v_and_b32 v245, s58, v245\n"
    "v_mfma_f32_16x16x32_bf16 a[248:251], a[212:215], a[188:191], a[248:251]\n"
    "v_and_b32 v247, s58, v247\n"
    "v_add_f32_dpp v254, v253, v44 row_newbcast:12 row_mask:0xf bank_mask:0xf\n"
    "v_mfma_f32_16x16x32_bf16 a[248:251], a[208:211], a[156:159], a[248:251]\n"
    "s_waitcnt vmcnt(12)\n"
    "v_add_f32_dpp v255, v253, v46 row_newbcast:14 row_mask:0xf bank_mask:0xf\n"
    "v_mul_f32 v244, v244, v254\n"
    "v_mfma_f32_16x16x32_bf16 a[252:255], a[216:219], a[156:159], a[252:255]\n"
    "buffer_load_dwordx4 a[208:211], v240, s[20:23], s53 offen offset:0 sc1\n"
    "v_mul_f32 v246, v246, v255\n"
    "v_add_f32_dpp v254, v253, v45 row_newbcast:13 row_mask:0xf bank_mask:0xf\n"
    "v_mfma_f32_16x16x32_bf16 a[252:255], a[220:223], a[188:191], a[252:255]\n"
    "buffer_load_dwordx4 a[212:215], v240, s[20:23], s53 offen offset:1024 sc1\n"
    "v_add_f32_dpp v255, v253, v47 row_newbcast:15 row_mask:0xf bank_mask:0xf\n"
    "v_mul_f32 v245, v245, v254\n"
    "v_mul_f32 v247, v247, v255\n"
    "v_mfma_f32_16x16x32_bf16 v[220:223], a[220:223], a[184:187], v[220:223]\n"
    "ds_read_b128 a[156:159], v241 offset:47104\n"
    "v_cvt_pk_bf16_f32 v250, v244, v245\n"
    "v_cvt_pk_bf16_f32 v251, v246, v247\n"
    "v_accvgpr_read_b32 v245, a40\n"
    "v_mfma_f32_16x16x32_bf16 v[220:223], a[216:219], a[152:155], v[220:223]\n"
    "ds_read_b128 a[188:191], v241 offset:48128\n"
    "v_accvgpr_read_b32 v247, a41\n"
    "v_lshlrev_b32 v244, 16, v245\n"
    "v_mfma_f32_16x16x32_bf16 v[188:191], a[216:219], a[148:151], v[188:191]\n"
    "ds_read_b128 a[184:187], v241 offset:46080\n"
    "v_lshlrev_b32 v246, 16, v247\n"
    "v_and_b32 v245, s58, v245\n"
    "v_and_b32 v247, s58, v247\n"
    "v_mfma_f32_16x16x32_bf16 v[188:191], a[220:223], a[180:183], v[188:191]\n"
    "ds_read_b128 a[152:155], v241 offset:45056\n"
    "v_add_f32_dpp v254, v253, v72 row_newbcast:8 row_mask:0xf bank_mask:0xf\n"
    "v_add_f32_dpp v255, v253, v74 row_newbcast:10 row_mask:0xf bank_mask:0xf\n"
    "v_mfma_f32_16x16x32_bf16 v[156:159], a[220:223], a[176:179], v[156:159]\n"
    "ds_read_b128 a[148:151], v241 offset:43008\n"
    "v_mul_f32 v244, v244, v254\n"
    "v_mul_f32 v246, v246, v255\n"
    "v_add_f32_dpp v254, v253, v73 row_newbcast:9 row_mask:0xf bank_mask:0xf\n"
    "v_mfma_f32_16x16x32_bf16 v[156:159], a[216:219], a[144:147], v[156:159]\n"
    "ds_read_b128 a[180:183], v241 offset:44032\n"
    "v_add_f32_dpp v255, v253, v75 row_newbcast:11 row_mask:0xf bank_mask:0xf\n"
    "v_mul_f32 v245, v245, v254\n"
    "v_mfma_f32_16x16x32_bf16 v[124:127], a[216:219], a[140:143], v[124:127]\n"
    "ds_read_b128 a[176:179], v241 offset:41984\n"
    "v_mul_f32 v247, v247, v255\n"
    "v_mfma_f32_16x16x32_bf16 v[124:127], a[220:223], a[172:175], v[124:127]\n"
    "buffer_store_dwordx4 v[248:251], v240, s[32:35], s91 offen sc1\n"
    "ds_write_b128 v243, v[248:251] offset:34816\n"
    "v_mfma_f32_16x16x32_bf16 v[92:95], a[220:223], a[168:171], v[92:95]\n"
    "ds_read_b128 a[144:147], v241 offset:40960\n"
    "ds_read_b128 a[140:143], v241 offset:38912\n"
    "v_mfma_f32_16x16x32_bf16 v[92:95], a[216:219], a[136:139], v[92:95]\n"
    "ds_read_b128 a[172:175], v241 offset:39936\n"
    "v_cvt_pk_bf16_f32 v248, v244, v245\n"
    "v_mfma_f32_16x16x32_bf16 v[60:63], a[216:219], a[132:135], v[60:63]\n"
    "ds_read_b128 a[168:171], v241 offset:37888\n"
    "v_cvt_pk_bf16_f32 v249, v246, v247\n"
    "v_mfma_f32_16x16x32_bf16 v[60:63], a[220:223], a[164:167], v[60:63]\n"
    "ds_read_b128 a[136:139], v241 offset:36864\n"
    "v_accvgpr_read_b32 v245, a42\n"
    "v_mfma_f32_16x16x32_bf16 v[28:31], a[220:223], a[160:163], v[28:31]\n"
    "ds_read_b128 a[132:135], v241 offset:34816\n"
    "v_accvgpr_read_b32 v247, a43\n"
    "v_mfma_f32_16x16x32_bf16 v[28:31], a[216:219], a[128:131], v[28:31]\n"
    "s_waitcnt vmcnt(12) lgkmcnt(12)\n"
    "ds_read_b128 a[164:167], v241 offset:35840\n"
    "v_lshlrev_b32 v244, 16, v245\n"
    "v_mfma_f32_16x16x32_bf16 a[240:243], a[224:227], a[156:159], a[240:243]\n"
    "buffer_load_dwordx4 a[216:219], v240, s[20:23], s53 offen offset:2048 sc1\n"
    "v_mfma_f32_16x16x32_bf16 a[240:243], a[228:231], a[188:191], a[240:243]\n"
    "s_waitcnt lgkmcnt(11)\n"
    "v_lshlrev_b32 v246, 16, v247\n"
    "v_and_b32 v245, s58, v245\n"
    "v_mfma_f32_16x16x32_bf16 v[208:211], a[228:231], a[184:187], v[208:211]\n"
    "buffer_load_dwordx4 a[220:223], v240, s[20:23], s53 offen offset:3072 sc1\n"
    "v_mfma_f32_16x16x32_bf16 v[208:211], a[224:227], a[152:155], v[208:211]\n"
    "s_waitcnt lgkmcnt(9)\n"
    "s_addk_i32 s53, 4096\n"
    "ds_read_b128 a[160:163], v241 offset:33792\n"
    "v_mfma_f32_16x16x32_bf16 v[176:179], a[224:227], a[148:151], v[176:179]\n"
    "ds_read_b128 a[128:131], v241 offset:32768\n"
    "v_and_b32 v247, s58, v247\n"
    "v_mfma_f32_16x16x32_bf16 v[176:179], a[228:231], a[180:183], v[176:179]\n"
    "s_waitcnt lgkmcnt(8)\n"
    "s_xor_b32 s91, s91, 0x101800\n"
    "v_add_f32_dpp v254, v253, v76 row_newbcast:12 row_mask:0xf bank_mask:0xf\n"
    "v_mfma_f32_16x16x32_bf16 v[144:147], a[228:231], a[176:179], v[144:147]\n"
    "v_add_f32_dpp v255, v253, v78 row_newbcast:14 row_mask:0xf bank_mask:0xf\n"
    "v_mul_f32 v244, v244, v254\n"
    "v_mfma_f32_16x16x32_bf16 v[144:147], a[224:227], a[144:147], v[144:147]\n"
    "s_waitcnt lgkmcnt(6)\n"
    "v_mul_f32 v246, v246, v255\n"
    "v_add_f32_dpp v254, v253, v77 row_newbcast:13 row_mask:0xf bank_mask:0xf\n"
    "v_mfma_f32_16x16x32_bf16 v[112:115], a[224:227], a[140:143], v[112:115]\n"
    "v_add_f32_dpp v255, v253, v79 row_newbcast:15 row_mask:0xf bank_mask:0xf\n"
    "v_mul_f32 v245, v245, v254\n"
    "v_mfma_f32_16x16x32_bf16 v[112:115], a[228:231], a[172:175], v[112:115]\n"
    "s_waitcnt lgkmcnt(4)\n"
    "v_mul_f32 v247, v247, v255\n"
    "v_cvt_pk_bf16_f32 v250, v244, v245\n"
    "v_mfma_f32_16x16x32_bf16 v[80:83], a[228:231], a[168:171], v[80:83]\n"
    "v_cvt_pk_bf16_f32 v251, v246, v247\n"
    "v_accvgpr_read_b32 v245, a44\n"
    "v_mfma_f32_16x16x32_bf16 v[80:83], a[224:227], a[136:139], v[80:83]\n"
    "s_waitcnt lgkmcnt(2)\n"
    "v_accvgpr_read_b32 v247, a45\n"
    "v_lshlrev_b32 v244, 16, v245\n"
    "v_mfma_f32_16x16x32_bf16 v[48:51], a[224:227], a[132:135], v[48:51]\n"
    "v_lshlrev_b32 v246, 16, v247\n"
    "v_and_b32 v245, s58, v245\n"
    "v_mfma_f32_16x16x32_bf16 v[48:51], a[228:231], a[164:167], v[48:51]\n"
    "s_waitcnt lgkmcnt(0)\n"
    "v_and_b32 v247, s58, v247\n"
    "v_add_f32_dpp v254, v253, v104 row_newbcast:8 row_mask:0xf bank_mask:0xf\n"
    "v_mfma_f32_16x16x32_bf16 v[16:19], a[228:231], a[160:163], v[16:19]\n"
    "v_add_f32_dpp v255, v253, v106 row_newbcast:10 row_mask:0xf bank_mask:0xf\n"
    "v_mul_f32 v244, v244, v254\n"
    "v_mfma_f32_16x16x32_bf16 v[16:19], a[224:227], a[128:131], v[16:19]\n"
    "s_waitcnt vmcnt(12)\n"
    "v_mul_f32 v246, v246, v255\n"
    "v_add_f32_dpp v254, v253, v105 row_newbcast:9 row_mask:0xf bank_mask:0xf\n"
    "v_mfma_f32_16x16x32_bf16 v[20:23], a[232:235], a[128:131], v[20:23]\n"
    "buffer_load_dwordx4 a[224:227], v240, s[20:23], s53 offen offset:0 sc1\n"
    "v_mfma_f32_16x16x32_bf16 v[20:23], a[236:239], a[160:163], v[20:23]\n"
    "buffer_load_dwordx4 a[228:231], v240, s[20:23], s53 offen offset:1024 sc1\n"
    "v_mfma_f32_16x16x32_bf16 v[52:55], a[236:239], a[164:167], v[52:55]\n"
    "buffer_store_dwordx4 v[248:251], v240, s[32:35], s91 offen sc1\n"
    "ds_write_b128 v243, v[248:251] offset:36864\n"
    "v_mfma_f32_16x16x32_bf16 v[52:55], a[232:235], a[132:135], v[52:55]\n"
    "v_add_f32_dpp v255, v253, v107 row_newbcast:11 row_mask:0xf bank_mask:0xf\n"
    "v_mul_f32 v245, v245, v254\n"
    "v_mfma_f32_16x16x32_bf16 v[84:87], a[232:235], a[136:139], v[84:87]\n"
    "v_mul_f32 v247, v247, v255\n"
    "v_cvt_pk_bf16_f32 v248, v244, v245\n"
    "v_mfma_f32_16x16x32_bf16 v[84:87], a[236:239], a[168:171], v[84:87]\n"
    "v_cvt_pk_bf16_f32 v249, v246, v247\n"
    "v_accvgpr_read_b32 v245, a46\n"
    "v_mfma_f32_16x16x32_bf16 v[116:119], a[236:239], a[172:175], v[116:119]\n"
    "s_xor_b32 s91, s91, 0x800\n"
    "v_accvgpr_read_b32 v247, a47\n"
    "v_lshlrev_b32 v244, 16, v245\n"
    "v_mfma_f32_16x16x32_bf16 v[116:119], a[232:235], a[140:143], v[116:119]\n"
    "v_lshlrev_b32 v246, 16, v247\n"
    "v_and_b32 v245, s58, v245\n"
    "v_mfma_f32_16x16x32_bf16 v[148:151], a[232:235], a[144:147], v[148:151]\n"
    "v_and_b32 v247, s58, v247\n"
    "v_add_f32_dpp v254, v253, v108 row_newbcast:12 row_mask:0xf bank_mask:0xf\n"
    "v_mfma_f32_16x16x32_bf16 v[148:151], a[236:239], a[176:179], v[148:151]\n"
    "v_add_f32_dpp v255, v253, v110 row_newbcast:14 row_mask:0xf bank_mask:0xf\n"
    "v_mul_f32 v244, v244, v254\n"
    "v_mfma_f32_16x16x32_bf16 v[180:183], a[236:239], a[180:183], v[180:183]\n"
    "v_mul_f32 v246, v246, v255\n"
    "v_add_f32_dpp v254, v253, v109 row_newbcast:13 row_mask:0xf bank_mask:0xf\n"
    "v_mfma_f32_16x16x32_bf16 v[180:183], a[232:235], a[148:151], v[180:183]\n"
    "v_add_f32_dpp v255, v253, v111 row_newbcast:15 row_mask:0xf bank_mask:0xf\n"
    "v_mul_f32 v245, v245, v254\n"
    "v_mfma_f32_16x16x32_bf16 v[212:215], a[232:235], a[152:155], v[212:215]\n"
    "v_mul_f32 v247, v247, v255\n"
    "v_cvt_pk_bf16_f32 v250, v244, v245\n"
    "v_mfma_f32_16x16x32_bf16 v[212:215], a[236:239], a[184:187], v[212:215]\n"
    "v_cvt_pk_bf16_f32 v251, v246, v247\n"
    "v_accvgpr_read_b32 v245, a48\n"
    "v_mfma_f32_16x16x32_bf16 a[244:247], a[236:239], a[188:191], a[244:247]\n"
    "v_accvgpr_read_b32 v247, a49\n"
    "v_lshlrev_b32 v244, 16, v245\n"
    "v_mfma_f32_16x16x32_bf16 a[244:247], a[232:235], a[156:159], a[244:247]\n"
    "s_waitcnt vmcnt(12)\n"
    "v_lshlrev_b32 v246, 16, v247\n"
    "v_and_b32 v245, s58, v245\n"
    "v_mfma_f32_16x16x32_bf16 a[248:251], a[192:195], a[156:159], a[248:251]\n"
    "buffer_store_dwordx4 v[248:251], v240, s[32:35], s91 offen sc1\n"
    "ds_write_b128 v243, v[248:251] offset:38912\n"
    "v_mfma_f32_16x16x32_bf16 a[248:251], a[196:199], a[188:191], a[248:251]\n"
    "buffer_load_dwordx4 a[232:235], v240, s[20:23], s53 offen offset:2048 sc1\n"
    "v_mfma_f32_16x16x32_bf16 v[216:219], a[196:199], a[184:187], v[216:219]\n"
    "buffer_load_dwordx4 a[236:239], v240, s[20:23], s53 offen offset:3072 sc1\n"
    "v_mfma_f32_16x16x32_bf16 v[216:219], a[192:195], a[152:155], v[216:219]\n"
    "s_addk_i32 s53, 4096\n"
    "v_and_b32 v247, s58, v247\n"
    "v_add_f32_dpp v254, v253, v136 row_newbcast:8 row_mask:0xf bank_mask:0xf\n"
    "v_mfma_f32_16x16x32_bf16 v[184:187], a[192:195], a[148:151], v[184:187]\n"
    "s_xor_b32 s91, s91, 0x185800\n"
    "v_add_f32_dpp v255, v253, v138 row_newbcast:10 row_mask:0xf bank_mask:0xf\n"
    "v_mul_f32 v244, v244, v254\n"
    "v_mfma_f32_16x16x32_bf16 v[184:187], a[196:199], a[180:183], v[184:187]\n"
    "v_mul_f32 v246, v246, v255\n"
    "v_add_f32_dpp v254, v253, v137 row_newbcast:9 row_mask:0xf bank_mask:0xf\n"
    "v_mfma_f32_16x16x32_bf16 v[152:155], a[196:199], a[176:179], v[152:155]\n"
    "v_add_f32_dpp v255, v253, v139 row_newbcast:11 row_mask:0xf bank_mask:0xf\n"
    "v_mul_f32 v245, v245, v254\n"
    "v_mfma_f32_16x16x32_bf16 v[152:155], a[192:195], a[144:147], v[152:155]\n"
    "v_mul_f32 v247, v247, v255\n"
    "v_cvt_pk_bf16_f32 v248, v244, v245\n"
    "v_mfma_f32_16x16x32_bf16 v[120:123], a[192:195], a[140:143], v[120:123]\n"
    "v_cvt_pk_bf16_f32 v249, v246, v247\n"
    "v_accvgpr_read_b32 v245, a50\n"
    "v_mfma_f32_16x16x32_bf16 v[120:123], a[196:199], a[172:175], v[120:123]\n"
    "v_accvgpr_read_b32 v247, a51\n"
    "v_lshlrev_b32 v244, 16, v245\n"
    "v_mfma_f32_16x16x32_bf16 v[88:91], a[196:199], a[168:171], v[88:91]\n"
    "v_lshlrev_b32 v246, 16, v247\n"
    "v_and_b32 v245, s58, v245\n"
    "v_mfma_f32_16x16x32_bf16 v[88:91], a[192:195], a[136:139], v[88:91]\n"
    "v_and_b32 v247, s58, v247\n"
    "v_add_f32_dpp v254, v253, v140 row_newbcast:12 row_mask:0xf bank_mask:0xf\n"
    "v_add_f32_dpp v255, v253, v142 row_newbcast:14 row_mask:0xf bank_mask:0xf\n"
    "v_mfma_f32_16x16x32_bf16 v[56:59], a[192:195], a[132:135], v[56:59]\n"
    "v_mul_f32 v244, v244, v254\n"
    "v_mul_f32 v246, v246, v255\n"
    "v_mfma_f32_16x16x32_bf16 v[56:59], a[196:199], a[164:167], v[56:59]\n"
    "v_add_f32_dpp v254, v253, v141 row_newbcast:13 row_mask:0xf bank_mask:0xf\n"
    "v_add_f32_dpp v255, v253, v143 row_newbcast:15 row_mask:0xf bank_mask:0xf\n"
    "v_mfma_f32_16x16x32_bf16 v[24:27], a[196:199], a[160:163], v[24:27]\n"
    "v_mul_f32 v245, v245, v254\n"
    "v_mul_f32 v247, v247, v255\n"
    "v_mfma_f32_16x16x32_bf16 v[24:27], a[192:195], a[128:131], v[24:27]\n"
    "s_waitcnt vmcnt(12)\n"
    "v_cvt_pk_bf16_f32 v250, v244, v245\n"
    "v_cvt_pk_bf16_f32 v251, v246, v247\n"
    "v_accvgpr_read_b32 v245, a52\n"
    "v_mfma_f32_16x16x32_bf16 v[28:31], a[200:203], a[128:131], v[28:31]\n"
    "buffer_store_dwordx4 v[248:251], v240, s[32:35], s91 offen sc1\n"
    "ds_write_b128 v243, v[248:251] offset:40960\n"
    "v_accvgpr_read_b32 v247, a53\n"
    "v_lshlrev_b32 v244, 16, v245\n"
    "v_lshlrev_b32 v246, 16, v247\n"
    "v_mfma_f32_16x16x32_bf16 v[28:31], a[204:207], a[160:163], v[28:31]\n"
    "buffer_load_dwordx4 a[192:195], v240, s[20:23], s53 offen offset:0 sc1\n"
    "v_and_b32 v245, s58, v245\n"
    "v_and_b32 v247, s58, v247\n"
    "v_mfma_f32_16x16x32_bf16 v[60:63], a[204:207], a[164:167], v[60:63]\n"
    "buffer_load_dwordx4 a[196:199], v240, s[20:23], s53 offen offset:1024 sc1\n"
    "v_add_f32_dpp v254, v253, v168 row_newbcast:8 row_mask:0xf bank_mask:0xf\n"
    "v_add_f32_dpp v255, v253, v170 row_newbcast:10 row_mask:0xf bank_mask:0xf\n"
    "v_mul_f32 v244, v244, v254\n"
    "v_mfma_f32_16x16x32_bf16 v[60:63], a[200:203], a[132:135], v[60:63]\n"
    "ds_read_b128 a[128:131], v241 offset:49152\n"
    "ds_read_b128 a[160:163], v241 offset:50176\n"
    "v_mul_f32 v246, v246, v255\n"
    "v_add_f32_dpp v254, v253, v169 row_newbcast:9 row_mask:0xf bank_mask:0xf\n"
    "v_mfma_f32_16x16x32_bf16 v[92:95], a[200:203], a[136:139], v[92:95]\n"
    "ds_read_b128 a[164:167], v241 offset:52224\n"
    "v_add_f32_dpp v255, v253, v171 row_newbcast:11 row_mask:0xf bank_mask:0xf\n"
    "v_mul_f32 v245, v245, v254\n"
    "v_mul_f32 v247, v247, v255\n"
    "v_mfma_f32_16x16x32_bf16 v[92:95], a[204:207], a[168:171], v[92:95]\n"
    "ds_read_b128 a[132:135], v241 offset:51200\n"
    "v_cvt_pk_bf16_f32 v248, v244, v245\n"
    "v_mfma_f32_16x16x32_bf16 v[124:127], a[204:207], a[172:175], v[124:127]\n"
    "ds_read_b128 a[136:139], v241 offset:53248\n"
    "v_cvt_pk_bf16_f32 v249, v246, v247\n"
    "v_accvgpr_read_b32 v245, a54\n"
    "v_accvgpr_read_b32 v247, a55\n"
    "v_mfma_f32_16x16x32_bf16 v[124:127], a[200:203], a[140:143], v[124:127]\n"
    "ds_read_b128 a[168:171], v241 offset:54272\n"
    "v_lshlrev_b32 v244, 16, v245\n"
    "v_lshlrev_b32 v246, 16, v247\n"
    "v_mfma_f32_16x16x32_bf16 v[156:159], a[200:203], a[144:147], v[156:159]\n"
    "ds_read_b128 a[172:175], v241 offset:56320\n"
    "v_and_b32 v245, s58, v245\n"
    "v_mfma_f32_16x16x32_bf16 v[156:159], a[204:207], a[176:179], v[156:159]\n"
    "ds_read_b128 a[140:143], v241 offset:55296\n"
    "v_and_b32 v247, s58, v247\n"
    "v_mfma_f32_16x16x32_bf16 v[188:191], a[204:207], a[180:183], v[188:191]\n"
    "ds_read_b128 a[144:147], v241 offset:57344\n"
    "v_add_f32_dpp v254, v253, v172 row_newbcast:12 row_mask:0xf bank_mask:0xf\n"
    "v_mfma_f32_16x16x32_bf16 v[188:191], a[200:203], a[148:151], v[188:191]\n"
    "ds_read_b128 a[176:179], v241 offset:58368\n"
    "s_xor_b32 s91, s91, 0x800\n"
    "v_mfma_f32_16x16x32_bf16 v[220:223], a[200:203], a[152:155], v[220:223]\n"
    "ds_read_b128 a[180:183], v241 offset:60416\n"
    "v_add_f32_dpp v255, v253, v174 row_newbcast:14 row_mask:0xf bank_mask:0xf\n"
    "v_mfma_f32_16x16x32_bf16 v[220:223], a[204:207], a[184:187], v[220:223]\n"
    "ds_read_b128 a[148:151], v241 offset:59392\n"
    "v_mul_f32 v244, v244, v254\n"
    "v_mfma_f32_16x16x32_bf16 a[252:255], a[204:207], a[188:191], a[252:255]\n"
    "ds_read_b128 a[152:155], v241 offset:61440\n"
    "v_mul_f32 v246, v246, v255\n"
    "v_mfma_f32_16x16x32_bf16 a[252:255], a[200:203], a[156:159], a[252:255]\n"
    "s_waitcnt vmcnt(12) lgkmcnt(11)\n"
    "ds_read_b128 a[184:187], v241 offset:62464\n"
    "v_add_f32_dpp v254, v253, v173 row_newbcast:13 row_mask:0xf bank_mask:0xf\n"
    "v_mfma_f32_16x16x32_bf16 v[16:19], a[208:211], a[128:131], v[16:19]\n"
    "buffer_load_dwordx4 a[200:203], v240, s[20:23], s53 offen offset:2048 sc1\n"
    "v_mfma_f32_16x16x32_bf16 v[16:19], a[212:215], a[160:163], v[16:19]\n"
    "s_waitcnt lgkmcnt(10)\n"
    "v_add_f32_dpp v255, v253, v175 row_newbcast:15 row_mask:0xf bank_mask:0xf\n"
    "v_mul_f32 v245, v245, v254\n"
    "v_mfma_f32_16x16x32_bf16 v[48:51], a[212:215], a[164:167], v[48:51]\n"
    "buffer_load_dwordx4 a[204:207], v240, s[20:23], s53 offen offset:3072 sc1\n"
    "v_mfma_f32_16x16x32_bf16 v[48:51], a[208:211], a[132:135], v[48:51]\n"
    "s_waitcnt lgkmcnt(8)\n"
    "s_addk_i32 s53, 4096\n"
    "ds_read_b128 a[188:191], v241 offset:64512\n"
    "v_mfma_f32_16x16x32_bf16 v[80:83], a[208:211], a[136:139], v[80:83]\n"
    "ds_read_b128 a[156:159], v241 offset:63488\n"
    "v_mul_f32 v247, v247, v255\n"
    "v_mfma_f32_16x16x32_bf16 v[80:83], a[212:215], a[168:171], v[80:83]\n"
    "s_waitcnt lgkmcnt(8)\n"
    "v_cvt_pk_bf16_f32 v250, v244, v245\n"
    "v_cvt_pk_bf16_f32 v251, v246, v247\n"
    "v_mfma_f32_16x16x32_bf16 v[112:115], a[212:215], a[172:175], v[112:115]\n"
    "v_accvgpr_read_b32 v245, a56\n"
    "v_accvgpr_read_b32 v247, a57\n"
    "v_mfma_f32_16x16x32_bf16 v[112:115], a[208:211], a[140:143], v[112:115]\n"
    "s_waitcnt lgkmcnt(6)\n"
    "v_lshlrev_b32 v244, 16, v245\n"
    "v_lshlrev_b32 v246, 16, v247\n"
    "v_mfma_f32_16x16x32_bf16 v[144:147], a[208:211], a[144:147], v[144:147]\n"
    "v_and_b32 v245, s58, v245\n"
    "v_and_b32 v247, s58, v247\n"
    "v_mfma_f32_16x16x32_bf16 v[144:147], a[212:215], a[176:179], v[144:147]\n"
    "s_waitcnt lgkmcnt(4)\n"
    "v_add_f32_dpp v254, v253, v200 row_newbcast:8 row_mask:0xf bank_mask:0xf\n"
    "v_add_f32_dpp v255, v253, v202 row_newbcast:10 row_mask:0xf bank_mask:0xf\n"
    "v_mfma_f32_16x16x32_bf16 v[176:179], a[212:215], a[180:183], v[176:179]\n"
    "v_mul_f32 v244, v244, v254\n"
    "v_mul_f32 v246, v246, v255\n"
    "v_mfma_f32_16x16x32_bf16 v[176:179], a[208:211], a[148:151], v[176:179]\n"
    "s_waitcnt lgkmcnt(2)\n"
    "v_add_f32_dpp v254, v253, v201 row_newbcast:9 row_mask:0xf bank_mask:0xf\n"
    "v_add_f32_dpp v255, v253, v203 row_newbcast:11 row_mask:0xf bank_mask:0xf\n"
    "v_mfma_f32_16x16x32_bf16 v[208:211], a[208:211], a[152:155], v[208:211]\n"
    "v_mul_f32 v245, v245, v254\n"
    "v_mul_f32 v247, v247, v255\n"
    "v_mfma_f32_16x16x32_bf16 v[208:211], a[212:215], a[184:187], v[208:211]\n"
    "s_waitcnt lgkmcnt(0)\n"
    "v_mfma_f32_16x16x32_bf16 a[240:243], a[212:215], a[188:191], a[240:243]\n"
    "buffer_store_dwordx4 v[248:251], v240, s[32:35], s91 offen sc1\n"
    "ds_write_b128 v243, v[248:251] offset:43008\n"
    "v_mfma_f32_16x16x32_bf16 a[240:243], a[208:211], a[156:159], a[240:243]\n"
    "s_waitcnt vmcnt(12)\n"
    "v_cvt_pk_bf16_f32 v248, v244, v245\n"
    "v_cvt_pk_bf16_f32 v249, v246, v247\n"
    "v_mfma_f32_16x16x32_bf16 a[244:247], a[216:219], a[156:159], a[244:247]\n"
    "buffer_load_dwordx4 a[208:211], v240, s[20:23], s53 offen offset:0 sc1\n"
    "v_mfma_f32_16x16x32_bf16 a[244:247], a[220:223], a[188:191], a[244:247]\n"
    "buffer_load_dwordx4 a[212:215], v240, s[20:23], s53 offen offset:1024 sc1\n"
    "v_mfma_f32_16x16x32_bf16 v[212:215], a[220:223], a[184:187], v[212:215]\n"
    "v_accvgpr_read_b32 v245, a58\n"
    "v_accvgpr_read_b32 v247, a59\n"
    "v_mfma_f32_16x16x32_bf16 v[212:215], a[216:219], a[152:155], v[212:215]\n"
    "s_xor_b32 s91, s91, 0x101800\n"
    "v_lshlrev_b32 v244, 16, v245\n"
    "v_lshlrev_b32 v246, 16, v247\n"
    "v_mfma_f32_16x16x32_bf16 v[180:183], a[216:219], a[148:151], v[180:183]\n"
    "v_and_b32 v245, s58, v245\n"
    "v_and_b32 v247, s58, v247\n"
    "v_mfma_f32_16x16x32_bf16 v[180:183], a[220:223], a[180:183], v[180:183]\n"
    "v_add_f32_dpp v254, v253, v204 row_newbcast:12 row_mask:0xf bank_mask:0xf\n"
    "v_add_f32_dpp v255, v253, v206 row_newbcast:14 row_mask:0xf bank_mask:0xf\n"
    "v_mfma_f32_16x16x32_bf16 v[148:151], a[220:223], a[176:179], v[148:151]\n"
    "v_mul_f32 v244, v244, v254\n"
    "v_mul_f32 v246, v246, v255\n"
    "v_mfma_f32_16x16x32_bf16 v[148:151], a[216:219], a[144:147], v[148:151]\n"
    "v_add_f32_dpp v254, v253, v205 row_newbcast:13 row_mask:0xf bank_mask:0xf\n"
    "v_add_f32_dpp v255, v253, v207 row_newbcast:15 row_mask:0xf bank_mask:0xf\n"
    "v_mfma_f32_16x16x32_bf16 v[116:119], a[216:219], a[140:143], v[116:119]\n"
    "v_mul_f32 v245, v245, v254\n"
    "v_mul_f32 v247, v247, v255\n"
    "v_mfma_f32_16x16x32_bf16 v[116:119], a[220:223], a[172:175], v[116:119]\n"
    "v_cvt_pk_bf16_f32 v250, v244, v245\n"
    "v_cvt_pk_bf16_f32 v251, v246, v247\n"
    "v_mfma_f32_16x16x32_bf16 v[84:87], a[220:223], a[168:171], v[84:87]\n"
    "v_accvgpr_read_b32 v245, a60\n"
    "v_accvgpr_read_b32 v247, a61\n"
    "v_mfma_f32_16x16x32_bf16 v[84:87], a[216:219], a[136:139], v[84:87]\n"
    "v_lshlrev_b32 v244, 16, v245\n"
    "v_lshlrev_b32 v246, 16, v247\n"
    "v_mfma_f32_16x16x32_bf16 v[52:55], a[216:219], a[132:135], v[52:55]\n"
    "buffer_store_dwordx4 v[248:251], v240, s[32:35], s91 offen sc1\n"
    "ds_write_b128 v243, v[248:251] offset:45056\n"
    "v_mfma_f32_16x16x32_bf16 v[52:55], a[220:223], a[164:167], v[52:55]\n"
    "v_and_b32 v245, s58, v245\n"
    "v_and_b32 v247, s58, v247\n"
    "v_mfma_f32_16x16x32_bf16 v[20:23], a[220:223], a[160:163], v[20:23]\n"
    "v_add_f32_dpp v254, v253, v232 row_newbcast:8 row_mask:0xf bank_mask:0xf\n"
    "v_add_f32_dpp v255, v253, v234 row_newbcast:10 row_mask:0xf bank_mask:0xf\n"
    "v_mfma_f32_16x16x32_bf16 v[20:23], a[216:219], a[128:131], v[20:23]\n"
    "s_waitcnt vmcnt(13)\n"
    "v_mul_f32 v244, v244, v254\n"
    "v_mul_f32 v246, v246, v255\n"
    "v_mfma_f32_16x16x32_bf16 v[24:27], a[224:227], a[128:131], v[24:27]\n"
    "buffer_load_dwordx4 a[216:219], v240, s[20:23], s53 offen offset:2048 sc1\n"
    "v_mfma_f32_16x16x32_bf16 v[24:27], a[228:231], a[160:163], v[24:27]\n"
    "buffer_load_dwordx4 a[220:223], v240, s[20:23], s53 offen offset:3072 sc1\n"
    "v_mfma_f32_16x16x32_bf16 v[56:59], a[228:231], a[164:167], v[56:59]\n"
    "s_addk_i32 s53, 4096\n"
    "s_xor_b32 s91, s91, 0x800\n"
    "v_add_f32_dpp v254, v253, v233 row_newbcast:9 row_mask:0xf bank_mask:0xf\n"
    "v_mfma_f32_16x16x32_bf16 v[56:59], a[224:227], a[132:135], v[56:59]\n"
    "v_add_f32_dpp v255, v253, v235 row_newbcast:11 row_mask:0xf bank_mask:0xf\n"
    "v_mul_f32 v245, v245, v254\n"
    "v_mfma_f32_16x16x32_bf16 v[88:91], a[224:227], a[136:139], v[88:91]\n"
    "v_mul_f32 v247, v247, v255\n"
    "v_cvt_pk_bf16_f32 v248, v244, v245\n"
    "v_mfma_f32_16x16x32_bf16 v[88:91], a[228:231], a[168:171], v[88:91]\n"
    "v_cvt_pk_bf16_f32 v249, v246, v247\n"
    "v_accvgpr_read_b32 v245, a62\n"
    "v_mfma_f32_16x16x32_bf16 v[120:123], a[228:231], a[172:175], v[120:123]\n"
    "v_accvgpr_read_b32 v247, a63\n"
    "v_lshlrev_b32 v244, 16, v245\n"
    "v_mfma_f32_16x16x32_bf16 v[120:123], a[224:227], a[140:143], v[120:123]\n"
    "v_lshlrev_b32 v246, 16, v247\n"
    "v_and_b32 v245, s58, v245\n"
    "v_and_b32 v247, s58, v247\n"
    "v_mfma_f32_16x16x32_bf16 v[152:155], a[224:227], a[144:147], v[152:155]\n"
    "v_add_f32_dpp v254, v253, v236 row_newbcast:12 row_mask:0xf bank_mask:0xf\n"
    "v_add_f32_dpp v255, v253, v238 row_newbcast:14 row_mask:0xf bank_mask:0xf\n"
    "v_mfma_f32_16x16x32_bf16 v[152:155], a[228:231], a[176:179], v[152:155]\n"
    "v_mul_f32 v244, v244, v254\n"
    "v_mul_f32 v246, v246, v255\n"
    "v_mfma_f32_16x16x32_bf16 v[184:187], a[228:231], a[180:183], v[184:187]\n"
    "v_add_f32_dpp v254, v253, v237 row_newbcast:13 row_mask:0xf bank_mask:0xf\n"
    "v_add_f32_dpp v255, v253, v239 row_newbcast:15 row_mask:0xf bank_mask:0xf\n"
    "v_mfma_f32_16x16x32_bf16 v[184:187], a[224:227], a[148:151], v[184:187]\n"
    "v_mul_f32 v245, v245, v254\n"
    "v_mul_f32 v247, v247, v255\n"
    "v_cvt_pk_bf16_f32 v250, v244, v245\n"
    "v_mfma_f32_16x16x32_bf16 v[216:219], a[224:227], a[152:155], v[216:219]\n"
    "v_cvt_pk_bf16_f32 v251, v246, v247\n"
    "v_mfma_f32_16x16x32_bf16 v[216:219], a[228:231], a[184:187], v[216:219]\n"
    "buffer_store_dwordx4 v[248:251], v240, s[32:35], s91 offen sc1\n"
    "ds_write_b128 v243, v[248:251] offset:47104\n"
    "s_waitcnt lgkmcnt(0)\n"
    "s_barrier\n"
    "v_mfma_f32_16x16x32_bf16 a[248:251], a[228:231], a[188:191], a[248:251]\n"
    "v_mfma_f32_16x16x32_bf16 a[248:251], a[224:227], a[156:159], a[248:251]\n"
    "s_waitcnt vmcnt(12)\n"
    "v_mfma_f32_16x16x32_bf16 a[252:255], a[232:235], a[156:159], a[252:255]\n"
    "buffer_load_dwordx4 a[224:227], v240, s[20:23], s53 offen offset:0 sc1\n"
    "v_mfma_f32_16x16x32_bf16 a[252:255], a[236:239], a[188:191], a[252:255]\n"
    "buffer_load_dwordx4 a[228:231], v240, s[20:23], s53 offen offset:1024 sc1\n"
    "v_mfma_f32_16x16x32_bf16 v[220:223], a[236:239], a[184:187], v[220:223]\n"
    "v_add_u32 v241, s66, v240\n"
    "ds_read_b128 a[156:159], v241 offset:14336\n"
    "v_mfma_f32_16x16x32_bf16 v[220:223], a[232:235], a[152:155], v[220:223]\n"
    "ds_read_b128 a[188:191], v241 offset:15360\n"
    "v_mfma_f32_16x16x32_bf16 v[188:191], a[232:235], a[148:151], v[188:191]\n"
    "ds_read_b128 a[184:187], v241 offset:13312\n"
    "v_mfma_f32_16x16x32_bf16 v[188:191], a[236:239], a[180:183], v[188:191]\n"
    "ds_read_b128 a[152:155], v241 offset:12288\n"
    "v_mfma_f32_16x16x32_bf16 v[156:159], a[236:239], a[176:179], v[156:159]\n"
    "ds_read_b128 a[148:151], v241 offset:10240\n"
    "v_mfma_f32_16x16x32_bf16 v[156:159], a[232:235], a[144:147], v[156:159]\n"
    "ds_read_b128 a[180:183], v241 offset:11264\n"
    "v_mfma_f32_16x16x32_bf16 v[124:127], a[232:235], a[140:143], v[124:127]\n"
    "ds_read_b128 a[176:179], v241 offset:9216\n"
    "v_mfma_f32_16x16x32_bf16 v[124:127], a[236:239], a[172:175], v[124:127]\n"
    "ds_read_b128 a[144:147], v241 offset:8192\n"
    "v_mfma_f32_16x16x32_bf16 v[92:95], a[236:239], a[168:171], v[92:95]\n"
    "ds_read_b128 a[140:143], v241 offset:6144\n"
    "v_mfma_f32_16x16x32_bf16 v[92:95], a[232:235], a[136:139], v[92:95]\n"
    "ds_read_b128 a[172:175], v241 offset:7168\n"
    "v_mfma_f32_16x16x32_bf16 v[60:63], a[232:235], a[132:135], v[60:63]\n"
    "ds_read_b128 a[168:171], v241 offset:5120\n"
    "v_mfma_f32_16x16x32_bf16 v[60:63], a[236:239], a[164:167], v[60:63]\n"
    "ds_read_b128 a[136:139], v241 offset:4096\n"
    "v_mfma_f32_16x16x32_bf16 v[28:31], a[236:239], a[160:163], v[28:31]\n"
    "ds_read_b128 a[132:135], v241 offset:2048\n"
    "v_mfma_f32_16x16x32_bf16 v[28:31], a[232:235], a[128:131], v[28:31]\n"
    "ds_read_b128 a[164:167], v241 offset:3072\n"
    "ds_read_b128 a[160:163], v241 offset:1024\n"
    "ds_read_b128 a[128:131], v241 offset:0\n"
    "buffer_load_dwordx4 a[232:235], v240, s[20:23], s53 offen offset:2048 sc1\n"
    "buffer_load_dwordx4 a[236:239], v240, s[20:23], s53 offen offset:3072 sc1\n"
    "s_addk_i32 s53, 4096\n"
    "s_branch LOOP_%=\n"
    "LOOPE_%=:\n"
    "CP0_%=:\n"
    "s_cmp_lg_u32 s84, 0\n"
    "s_cbranch_scc1 CQ0_%=\n"
    "ds_read_b128 a[0:3], v241 offset:0\n"
    "ds_read_b128 a[4:7], v241 offset:2048\n"
    "ds_read_b128 a[8:11], v241 offset:4096\n"
    "ds_read_b128 a[12:15], v241 offset:6144\n"
    "ds_read_b128 a[16:19], v241 offset:8192\n"
    "ds_read_b128 a[20:23], v241 offset:10240\n"
    "ds_read_b128 a[24:27], v241 offset:12288\n"
    "ds_read_b128 a[28:31], v241 offset:14336\n"
    "ds_read_b128 a[32:35], v242 offset:0\n"
    "ds_read_b128 a[36:39], v242 offset:2048\n"
    "ds_read_b128 a[40:43], v242 offset:4096\n"
    "ds_read_b128 a[44:47], v242 offset:6144\n"
    "ds_read_b128 a[48:51], v242 offset:8192\n"
    "ds_read_b128 a[52:55], v242 offset:10240\n"
    "ds_read_b128 a[56:59], v242 offset:12288\n"
    "ds_read_b128 a[60:63], v242 offset:14336\n"
    "s_branch CR0_%=\n"
    "CQ0_%=:\n"
    "ds_read_b128 a[64:67], v241 offset:0\n"
    "ds_read_b128 a[68:71], v241 offset:2048\n"
    "ds_read_b128 a[72:75], v241 offset:4096\n"
    "ds_read_b128 a[76:79], v241 offset:6144\n"
    "ds_read_b128 a[80:83], v241 offset:8192\n"
    "ds_read_b128 a[84:87], v241 offset:10240\n"
    "ds_read_b128 a[88:91], v241 offset:12288\n"
    "ds_read_b128 a[92:95], v241 offset:14336\n"
    "ds_read_b128 a[96:99], v242 offset:0\n"
    "ds_read_b128 a[100:103], v242 offset:2048\n"
    "ds_read_b128 a[104:107], v242 offset:4096\n"
    "ds_read_b128 a[108:111], v242 offset:6144\n"
    "ds_read_b128 a[112:115], v242 offset:8192\n"
    "ds_read_b128 a[116:119], v242 offset:10240\n"
    "ds_read_b128 a[120:123], v242 offset:12288\n"
    "ds_read_b128 a[124:127], v242 offset:14336\n"
    "s_branch CR0_%=\n"
    "CP1_%=:\n"
    "s_cmp_lg_u32 s84, 1\n"
    "s_cbranch_scc1 CQ1_%=\n"
    "ds_read_b128 a[0:3], v241 offset:16384\n"
    "ds_read_b128 a[4:7], v241 offset:18432\n"
    "ds_read_b128 a[8:11], v241 offset:20480\n"
    "ds_read_b128 a[12:15], v241 offset:22528\n"
    "ds_read_b128 a[16:19], v241 offset:24576\n"
    "ds_read_b128 a[20:23], v241 offset:26624\n"
    "ds_read_b128 a[24:27], v241 offset:28672\n"
    "ds_read_b128 a[28:31], v241 offset:30720\n"
    "ds_read_b128 a[32:35], v242 offset:16384\n"
    "ds_read_b128 a[36:39], v242 offset:18432\n"
    "ds_read_b128 a[40:43], v242 offset:20480\n"
    "ds_read_b128 a[44:47], v242 offset:22528\n"
    "ds_read_b128 a[48:51], v242 offset:24576\n"
    "ds_read_b128 a[52:55], v242 offset:26624\n"
    "ds_read_b128 a[56:59], v242 offset:28672\n"
    "ds_read_b128 a[60:63], v242 offset:30720\n"
    "s_branch CR1_%=\n"
    "CQ1_%=:\n"
    "ds_read_b128 a[64:67], v241 offset:16384\n"
    "ds_read_b128 a[68:71], v241 offset:18432\n"
    "ds_read_b128 a[72:75], v241 offset:20480\n"
    "ds_read_b128 a[76:79], v241 offset:22528\n"
    "ds_read_b128 a[80:83], v241 offset:24576\n"
    "ds_read_b128 a[84:87], v241 offset:26624\n"
    "ds_read_b128 a[88:91], v241 offset:28672\n"
    "ds_read_b128 a[92:95], v241 offset:30720\n"
    "ds_read_b128 a[96:99], v242 offset:16384\n"
    "ds_read_b128 a[100:103], v242 offset:18432\n"
    "ds_read_b128 a[104:107], v242 offset:20480\n"
    "ds_read_b128 a[108:111], v242 offset:22528\n"
    "ds_read_b128 a[112:115], v242 offset:24576\n"
    "ds_read_b128 a[116:119], v242 offset:26624\n"
    "ds_read_b128 a[120:123], v242 offset:28672\n"
    "ds_read_b128 a[124:127], v242 offset:30720\n"
    "s_branch CR1_%=\n"
    "CP2_%=:\n"
    "s_cmp_lg_u32 s84, 2\n"
    "s_cbranch_scc1 CQ2_%=\n"
    "ds_read_b128 a[0:3], v241 offset:32768\n"
    "ds_read_b128 a[4:7], v241 offset:34816\n"
    "ds_read_b128 a[8:11], v241 offset:36864\n"
    "ds_read_b128 a[12:15], v241 offset:38912\n"
    "ds_read_b128 a[16:19], v241 offset:40960\n"
    "ds_read_b128 a[20:23], v241 offset:43008\n"
    "ds_read_b128 a[24:27], v241 offset:45056\n"
    "ds_read_b128 a[28:31], v241 offset:47104\n"
    "ds_read_b128 a[32:35], v242 offset:32768\n"
    "ds_read_b128 a[36:39], v242 offset:34816\n"
    "ds_read_b128 a[40:43], v242 offset:36864\n"
    "ds_read_b128 a[44:47], v242 offset:38912\n"
    "ds_read_b128 a[48:51], v242 offset:40960\n"
    "ds_read_b128 a[52:55], v242 offset:43008\n"
    "ds_read_b128 a[56:59], v242 offset:45056\n"
    "ds_read_b128 a[60:63], v242 offset:47104\n"
    "s_branch CR2_%=\n"
    "CQ2_%=:\n"
    "ds_read_b128 a[64:67], v241 offset:32768\n"
    "ds_read_b128 a[68:71], v241 offset:34816\n"
    "ds_read_b128 a[72:75], v241 offset:36864\n"
    "ds_read_b128 a[76:79], v241 offset:38912\n"
    "ds_read_b128 a[80:83], v241 offset:40960\n"
    "ds_read_b128 a[84:87], v241 offset:43008\n"
    "ds_read_b128 a[88:91], v241 offset:45056\n"
    "ds_read_b128 a[92:95], v241 offset:47104\n"
    "ds_read_b128 a[96:99], v242 offset:32768\n"
    "ds_read_b128 a[100:103], v242 offset:34816\n"
    "ds_read_b128 a[104:107], v242 offset:36864\n"
    "ds_read_b128 a[108:111], v242 offset:38912\n"
    "ds_read_b128 a[112:115], v242 offset:40960\n"
    "ds_read_b128 a[116:119], v242 offset:43008\n"
    "ds_read_b128 a[120:123], v242 offset:45056\n"
    "ds_read_b128 a[124:127], v242 offset:47104\n"
    "s_branch CR2_%=\n"
    "CP3_%=:\n"
    "s_cmp_lg_u32 s84, 3\n"
    "s_cbranch_scc1 CQ3_%=\n"
    "ds_read_b128 a[0:3], v241 offset:0\n"
    "ds_read_b128 a[4:7], v241 offset:2048\n"
    "ds_read_b128 a[8:11], v241 offset:4096\n"
    "ds_read_b128 a[12:15], v241 offset:6144\n"
    "ds_read_b128 a[16:19], v241 offset:8192\n"
    "ds_read_b128 a[20:23], v241 offset:10240\n"
    "ds_read_b128 a[24:27], v241 offset:12288\n"
    "ds_read_b128 a[28:31], v241 offset:14336\n"
    "ds_read_b128 a[32:35], v242 offset:0\n"
    "ds_read_b128 a[36:39], v242 offset:2048\n"
    "ds_read_b128 a[40:43], v242 offset:4096\n"
    "ds_read_b128 a[44:47], v242 offset:6144\n"
    "ds_read_b128 a[48:51], v242 offset:8192\n"
    "ds_read_b128 a[52:55], v242 offset:10240\n"
    "ds_read_b128 a[56:59], v242 offset:12288\n"
    "ds_read_b128 a[60:63], v242 offset:14336\n"
    "s_branch CR3_%=\n"
    "CQ3_%=:\n"
    "ds_read_b128 a[64:67], v241 offset:0\n"
    "ds_read_b128 a[68:71], v241 offset:2048\n"
    "ds_read_b128 a[72:75], v241 offset:4096\n"
    "ds_read_b128 a[76:79], v241 offset:6144\n"
    "ds_read_b128 a[80:83], v241 offset:8192\n"
    "ds_read_b128 a[84:87], v241 offset:10240\n"
    "ds_read_b128 a[88:91], v241 offset:12288\n"
    "ds_read_b128 a[92:95], v241 offset:14336\n"
    "ds_read_b128 a[96:99], v242 offset:0\n"
    "ds_read_b128 a[100:103], v242 offset:2048\n"
    "ds_read_b128 a[104:107], v242 offset:4096\n"
    "ds_read_b128 a[108:111], v242 offset:6144\n"
    "ds_read_b128 a[112:115], v242 offset:8192\n"
    "ds_read_b128 a[116:119], v242 offset:10240\n"
    "ds_read_b128 a[120:123], v242 offset:12288\n"
    "ds_read_b128 a[124:127], v242 offset:14336\n"
    "s_branch CR3_%=\n"
    "CP4_%=:\n"
    "s_cmp_lg_u32 s84, 4\n"
    "s_cbranch_scc1 CQ4_%=\n"
    "ds_read_b128 a[0:3], v241 offset:16384\n"
    "ds_read_b128 a[4:7], v241 offset:18432\n"
    "ds_read_b128 a[8:11], v241 offset:20480\n"
    "ds_read_b128 a[12:15], v241 offset:22528\n"
    "ds_read_b128 a[16:19], v241 offset:24576\n"
    "ds_read_b128 a[20:23], v241 offset:26624\n"
    "ds_read_b128 a[24:27], v241 offset:28672\n"
    "ds_read_b128 a[28:31], v241 offset:30720\n"
    "ds_read_b128 a[32:35], v242 offset:16384\n"
    "ds_read_b128 a[36:39], v242 offset:18432\n"
    "ds_read_b128 a[40:43], v242 offset:20480\n"
    "ds_read_b128 a[44:47], v242 offset:22528\n"
    "ds_read_b128 a[48:51], v242 offset:24576\n"
    "ds_read_b128 a[52:55], v242 offset:26624\n"
    "ds_read_b128 a[56:59], v242 offset:28672\n"
    "ds_read_b128 a[60:63], v242 offset:30720\n"
    "s_branch CR4_%=\n"
    "CQ4_%=:\n"
    "ds_read_b128 a[64:67], v241 offset:16384\n"
    "ds_read_b128 a[68:71], v241 offset:18432\n"
    "ds_read_b128 a[72:75], v241 offset:20480\n"
    "ds_read_b128 a[76:79], v241 offset:22528\n"
    "ds_read_b128 a[80:83], v241 offset:24576\n"
    "ds_read_b128 a[84:87], v241 offset:26624\n"
    "ds_read_b128 a[88:91], v241 offset:28672\n"
    "ds_read_b128 a[92:95], v241 offset:30720\n"
    "ds_read_b128 a[96:99], v242 offset:16384\n"
    "ds_read_b128 a[100:103], v242 offset:18432\n"
    "ds_read_b128 a[104:107], v242 offset:20480\n"
    "ds_read_b128 a[108:111], v242 offset:22528\n"
    "ds_read_b128 a[112:115], v242 offset:24576\n"
    "ds_read_b128 a[116:119], v242 offset:26624\n"
    "ds_read_b128 a[120:123], v242 offset:28672\n"
    "ds_read_b128 a[124:127], v242 offset:30720\n"
    "s_branch CR4_%=\n"
    "CP5_%=:\n"
    "s_cmp_lg_u32 s84, 5\n"
    "s_cbranch_scc1 CQ5_%=\n"
    "ds_read_b128 a[0:3], v241 offset:32768\n"
    "ds_read_b128 a[4:7], v241 offset:34816\n"
    "ds_read_b128 a[8:11], v241 offset:36864\n"
    "ds_read_b128 a[12:15], v241 offset:38912\n"
    "ds_read_b128 a[16:19], v241 offset:40960\n"
    "ds_read_b128 a[20:23], v241 offset:43008\n"
    "ds_read_b128 a[24:27], v241 offset:45056\n"
    "ds_read_b128 a[28:31], v241 offset:47104\n"
    "ds_read_b128 a[32:35], v242 offset:32768\n"
    "ds_read_b128 a[36:39], v242 offset:34816\n"
    "ds_read_b128 a[40:43], v242 offset:36864\n"
    "ds_read_b128 a[44:47], v242 offset:38912\n"
    "ds_read_b128 a[48:51], v242 offset:40960\n"
    "ds_read_b128 a[52:55], v242 offset:43008\n"
    "ds_read_b128 a[56:59], v242 offset:45056\n"
    "ds_read_b128 a[60:63], v242 offset:47104\n"
    "s_branch CR5_%=\n"
    "CQ5_%=:\n"
    "ds_read_b128 a[64:67], v241 offset:32768\n"
    "ds_read_b128 a[68:71], v241 offset:34816\n"
    "ds_read_b128 a[72:75], v241 offset:36864\n"
    "ds_read_b128 a[76:79], v241 offset:38912\n"
    "ds_read_b128 a[80:83], v241 offset:40960\n"
    "ds_read_b128 a[84:87], v241 offset:43008\n"
    "ds_read_b128 a[88:91], v241 offset:45056\n"
    "ds_read_b128 a[92:95], v241 offset:47104\n"
    "ds_read_b128 a[96:99], v242 offset:32768\n"
    "ds_read_b128 a[100:103], v242 offset:34816\n"
    "ds_read_b128 a[104:107], v242 offset:36864\n"
    "ds_read_b128 a[108:111], v242 offset:38912\n"
    "ds_read_b128 a[112:115], v242 offset:40960\n"
    "ds_read_b128 a[116:119], v242 offset:43008\n"
    "ds_read_b128 a[120:123], v242 offset:45056\n"
    "ds_read_b128 a[124:127], v242 offset:47104\n"
    "s_branch CR5_%=\n"
    "CP6_%=:\n"
    "s_cmp_lg_u32 s84, 6\n"
    "s_cbranch_scc1 CQ6_%=\n"
    "ds_read_b128 a[0:3], v241 offset:0\n"
    "ds_read_b128 a[4:7], v241 offset:2048\n"
    "ds_read_b128 a[8:11], v241 offset:4096\n"
    "ds_read_b128 a[12:15], v241 offset:6144\n"
    "ds_read_b128 a[16:19], v241 offset:8192\n"
    "ds_read_b128 a[20:23], v241 offset:10240\n"
    "ds_read_b128 a[24:27], v241 offset:12288\n"
    "ds_read_b128 a[28:31], v241 offset:14336\n"
    "ds_read_b128 a[32:35], v242 offset:0\n"
    "ds_read_b128 a[36:39], v242 offset:2048\n"
    "ds_read_b128 a[40:43], v242 offset:4096\n"
    "ds_read_b128 a[44:47], v242 offset:6144\n"
    "ds_read_b128 a[48:51], v242 offset:8192\n"
    "ds_read_b128 a[52:55], v242 offset:10240\n"
    "ds_read_b128 a[56:59], v242 offset:12288\n"
    "ds_read_b128 a[60:63], v242 offset:14336\n"
    "s_branch CR6_%=\n"
    "CQ6_%=:\n"
    "ds_read_b128 a[64:67], v241 offset:0\n"
    "ds_read_b128 a[68:71], v241 offset:2048\n"
    "ds_read_b128 a[72:75], v241 offset:4096\n"
    "ds_read_b128 a[76:79], v241 offset:6144\n"
    "ds_read_b128 a[80:83], v241 offset:8192\n"
    "ds_read_b128 a[84:87], v241 offset:10240\n"
    "ds_read_b128 a[88:91], v241 offset:12288\n"
    "ds_read_b128 a[92:95], v241 offset:14336\n"
    "ds_read_b128 a[96:99], v242 offset:0\n"
    "ds_read_b128 a[100:103], v242 offset:2048\n"
    "ds_read_b128 a[104:107], v242 offset:4096\n"
    "ds_read_b128 a[108:111], v242 offset:6144\n"
    "ds_read_b128 a[112:115], v242 offset:8192\n"
    "ds_read_b128 a[116:119], v242 offset:10240\n"
    "ds_read_b128 a[120:123], v242 offset:12288\n"
    "ds_read_b128 a[124:127], v242 offset:14336\n"
    "s_branch CR6_%=\n"
    "CP7_%=:\n"
    "s_cmp_lg_u32 s84, 7\n"
    "s_cbranch_scc1 CQ7_%=\n"
    "ds_read_b128 a[0:3], v241 offset:16384\n"
    "ds_read_b128 a[4:7], v241 offset:18432\n"
    "ds_read_b128 a[8:11], v241 offset:20480\n"
    "ds_read_b128 a[12:15], v241 offset:22528\n"
    "ds_read_b128 a[16:19], v241 offset:24576\n"
    "ds_read_b128 a[20:23], v241 offset:26624\n"
    "ds_read_b128 a[24:27], v241 offset:28672\n"
    "ds_read_b128 a[28:31], v241 offset:30720\n"
    "ds_read_b128 a[32:35], v242 offset:16384\n"
    "ds_read_b128 a[36:39], v242 offset:18432\n"
    "ds_read_b128 a[40:43], v242 offset:20480\n"
    "ds_read_b128 a[44:47], v242 offset:22528\n"
    "ds_read_b128 a[48:51], v242 offset:24576\n"
    "ds_read_b128 a[52:55], v242 offset:26624\n"
    "ds_read_b128 a[56:59], v242 offset:28672\n"
    "ds_read_b128 a[60:63], v242 offset:30720\n"
    "s_branch CR7_%=\n"
    "CQ7_%=:\n"
    "ds_read_b128 a[64:67], v241 offset:16384\n"
    "ds_read_b128 a[68:71], v241 offset:18432\n"
    "ds_read_b128 a[72:75], v241 offset:20480\n"
    "ds_read_b128 a[76:79], v241 offset:22528\n"
    "ds_read_b128 a[80:83], v241 offset:24576\n"
    "ds_read_b128 a[84:87], v241 offset:26624\n"
    "ds_read_b128 a[88:91], v241 offset:28672\n"
    "ds_read_b128 a[92:95], v241 offset:30720\n"
    "ds_read_b128 a[96:99], v242 offset:16384\n"
    "ds_read_b128 a[100:103], v242 offset:18432\n"
    "ds_read_b128 a[104:107], v242 offset:20480\n"
    "ds_read_b128 a[108:111], v242 offset:22528\n"
    "ds_read_b128 a[112:115], v242 offset:24576\n"
    "ds_read_b128 a[116:119], v242 offset:26624\n"
    "ds_read_b128 a[120:123], v242 offset:28672\n"
    "ds_read_b128 a[124:127], v242 offset:30720\n"
    "s_branch CR7_%=\n"
    "CP8_%=:\n"
    "s_cmp_lg_u32 s84, 8\n"
    "s_cbranch_scc1 CQ8_%=\n"
    "ds_read_b128 a[0:3], v241 offset:32768\n"
    "ds_read_b128 a[4:7], v241 offset:34816\n"
    "ds_read_b128 a[8:11], v241 offset:36864\n"
    "ds_read_b128 a[12:15], v241 offset:38912\n"
    "ds_read_b128 a[16:19], v241 offset:40960\n"
    "ds_read_b128 a[20:23], v241 offset:43008\n"
    "ds_read_b128 a[24:27], v241 offset:45056\n"
    "ds_read_b128 a[28:31], v241 offset:47104\n"
    "ds_read_b128 a[32:35], v242 offset:32768\n"
    "ds_read_b128 a[36:39], v242 offset:34816\n"
    "ds_read_b128 a[40:43], v242 offset:36864\n"
    "ds_read_b128 a[44:47], v242 offset:38912\n"
    "ds_read_b128 a[48:51], v242 offset:40960\n"
    "ds_read_b128 a[52:55], v242 offset:43008\n"
    "ds_read_b128 a[56:59], v242 offset:45056\n"
    "ds_read_b128 a[60:63], v242 offset:47104\n"
    "s_branch CR8_%=\n"
    "CQ8_%=:\n"
    "ds_read_b128 a[64:67], v241 offset:32768\n"
    "ds_read_b128 a[68:71], v241 offset:34816\n"
    "ds_read_b128 a[72:75], v241 offset:36864\n"
    "ds_read_b128 a[76:79], v241 offset:38912\n"
    "ds_read_b128 a[80:83], v241 offset:40960\n"
    "ds_read_b128 a[84:87], v241 offset:43008\n"
    "ds_read_b128 a[88:91], v241 offset:45056\n"
    "ds_read_b128 a[92:95], v241 offset:47104\n"
    "ds_read_b128 a[96:99], v242 offset:32768\n"
    "ds_read_b128 a[100:103], v242 offset:34816\n"
    "ds_read_b128 a[104:107], v242 offset:36864\n"
    "ds_read_b128 a[108:111], v242 offset:38912\n"
    "ds_read_b128 a[112:115], v242 offset:40960\n"
    "ds_read_b128 a[116:119], v242 offset:43008\n"
    "ds_read_b128 a[120:123], v242 offset:45056\n"
    "ds_read_b128 a[124:127], v242 offset:47104\n"
    "s_branch CR8_%=\n"
    "CP9_%=:\n"
    "s_cmp_lg_u32 s84, 9\n"
    "s_cbranch_scc1 CQ9_%=\n"
    "ds_read_b128 a[0:3], v241 offset:0\n"
    "ds_read_b128 a[4:7], v241 offset:2048\n"
    "ds_read_b128 a[8:11], v241 offset:4096\n"
    "ds_read_b128 a[12:15], v241 offset:6144\n"
    "ds_read_b128 a[16:19], v241 offset:8192\n"
    "ds_read_b128 a[20:23], v241 offset:10240\n"
    "ds_read_b128 a[24:27], v241 offset:12288\n"
    "ds_read_b128 a[28:31], v241 offset:14336\n"
    "ds_read_b128 a[32:35], v242 offset:0\n"
    "ds_read_b128 a[36:39], v242 offset:2048\n"
    "ds_read_b128 a[40:43], v242 offset:4096\n"
    "ds_read_b128 a[44:47], v242 offset:6144\n"
    "ds_read_b128 a[48:51], v242 offset:8192\n"
    "ds_read_b128 a[52:55], v242 offset:10240\n"
    "ds_read_b128 a[56:59], v242 offset:12288\n"
    "ds_read_b128 a[60:63], v242 offset:14336\n"
    "s_branch CR9_%=\n"
    "CQ9_%=:\n"
    "ds_read_b128 a[64:67], v241 offset:0\n"
    "ds_read_b128 a[68:71], v241 offset:2048\n"
    "ds_read_b128 a[72:75], v241 offset:4096\n"
    "ds_read_b128 a[76:79], v241 offset:6144\n"
    "ds_read_b128 a[80:83], v241 offset:8192\n"
    "ds_read_b128 a[84:87], v241 offset:10240\n"
    "ds_read_b128 a[88:91], v241 offset:12288\n"
    "ds_read_b128 a[92:95], v241 offset:14336\n"
    "ds_read_b128 a[96:99], v242 offset:0\n"
    "ds_read_b128 a[100:103], v242 offset:2048\n"
    "ds_read_b128 a[104:107], v242 offset:4096\n"
    "ds_read_b128 a[108:111], v242 offset:6144\n"
    "ds_read_b128 a[112:115], v242 offset:8192\n"
    "ds_read_b128 a[116:119], v242 offset:10240\n"
    "ds_read_b128 a[120:123], v242 offset:12288\n"
    "ds_read_b128 a[124:127], v242 offset:14336\n"
    "s_branch CR9_%=\n"
    "CP10_%=:\n"
    "s_cmp_lg_u32 s84, 10\n"
    "s_cbranch_scc1 CQ10_%=\n"
    "ds_read_b128 a[0:3], v241 offset:16384\n"
    "ds_read_b128 a[4:7], v241 offset:18432\n"
    "ds_read_b128 a[8:11], v241 offset:20480\n"
    "ds_read_b128 a[12:15], v241 offset:22528\n"
    "ds_read_b128 a[16:19], v241 offset:24576\n"
    "ds_read_b128 a[20:23], v241 offset:26624\n"
    "ds_read_b128 a[24:27], v241 offset:28672\n"
    "ds_read_b128 a[28:31], v241 offset:30720\n"
    "ds_read_b128 a[32:35], v242 offset:16384\n"
    "ds_read_b128 a[36:39], v242 offset:18432\n"
    "ds_read_b128 a[40:43], v242 offset:20480\n"
    "ds_read_b128 a[44:47], v242 offset:22528\n"
    "ds_read_b128 a[48:51], v242 offset:24576\n"
    "ds_read_b128 a[52:55], v242 offset:26624\n"
    "ds_read_b128 a[56:59], v242 offset:28672\n"
    "ds_read_b128 a[60:63], v242 offset:30720\n"
    "s_branch CR10_%=\n"
    "CQ10_%=:\n"
    "ds_read_b128 a[64:67], v241 offset:16384\n"
    "ds_read_b128 a[68:71], v241 offset:18432\n"
    "ds_read_b128 a[72:75], v241 offset:20480\n"
    "ds_read_b128 a[76:79], v241 offset:22528\n"
    "ds_read_b128 a[80:83], v241 offset:24576\n"
    "ds_read_b128 a[84:87], v241 offset:26624\n"
    "ds_read_b128 a[88:91], v241 offset:28672\n"
    "ds_read_b128 a[92:95], v241 offset:30720\n"
    "ds_read_b128 a[96:99], v242 offset:16384\n"
    "ds_read_b128 a[100:103], v242 offset:18432\n"
    "ds_read_b128 a[104:107], v242 offset:20480\n"
    "ds_read_b128 a[108:111], v242 offset:22528\n"
    "ds_read_b128 a[112:115], v242 offset:24576\n"
    "ds_read_b128 a[116:119], v242 offset:26624\n"
    "ds_read_b128 a[120:123], v242 offset:28672\n"
    "ds_read_b128 a[124:127], v242 offset:30720\n"
    "s_branch CR10_%=\n"
    "CP11_%=:\n"
    "s_cmp_lg_u32 s84, 11\n"
    "s_cbranch_scc1 CQ11_%=\n"
    "ds_read_b128 a[0:3], v241 offset:32768\n"
    "ds_read_b128 a[4:7], v241 offset:34816\n"
    "ds_read_b128 a[8:11], v241 offset:36864\n"
    "ds_read_b128 a[12:15], v241 offset:38912\n"
    "ds_read_b128 a[16:19], v241 offset:40960\n"
    "ds_read_b128 a[20:23], v241 offset:43008\n"
    "ds_read_b128 a[24:27], v241 offset:45056\n"
    "ds_read_b128 a[28:31], v241 offset:47104\n"
    "ds_read_b128 a[32:35], v242 offset:32768\n"
    "ds_read_b128 a[36:39], v242 offset:34816\n"
    "ds_read_b128 a[40:43], v242 offset:36864\n"
    "ds_read_b128 a[44:47], v242 offset:38912\n"
    "ds_read_b128 a[48:51], v242 offset:40960\n"
    "ds_read_b128 a[52:55], v242 offset:43008\n"
    "ds_read_b128 a[56:59], v242 offset:45056\n"
    "ds_read_b128 a[60:63], v242 offset:47104\n"
    "s_branch CR11_%=\n"
    "CQ11_%=:\n"
    "ds_read_b128 a[64:67], v241 offset:32768\n"
    "ds_read_b128 a[68:71], v241 offset:34816\n"
    "ds_read_b128 a[72:75], v241 offset:36864\n"
    "ds_read_b128 a[76:79], v241 offset:38912\n"
    "ds_read_b128 a[80:83], v241 offset:40960\n"
    "ds_read_b128 a[84:87], v241 offset:43008\n"
    "ds_read_b128 a[88:91], v241 offset:45056\n"
    "ds_read_b128 a[92:95], v241 offset:47104\n"
    "ds_read_b128 a[96:99], v242 offset:32768\n"
    "ds_read_b128 a[100:103], v242 offset:34816\n"
    "ds_read_b128 a[104:107], v242 offset:36864\n"
    "ds_read_b128 a[108:111], v242 offset:38912\n"
    "ds_read_b128 a[112:115], v242 offset:40960\n"
    "ds_read_b128 a[116:119], v242 offset:43008\n"
    "ds_read_b128 a[120:123], v242 offset:45056\n"
    "ds_read_b128 a[124:127], v242 offset:47104\n"
    "s_branch CR11_%=\n"
    "CP12_%=:\n"
    "s_cmp_lg_u32 s84, 12\n"
    "s_cbranch_scc1 CQ12_%=\n"
    "ds_read_b128 a[0:3], v241 offset:0\n"
    "ds_read_b128 a[4:7], v241 offset:2048\n"
    "ds_read_b128 a[8:11], v241 offset:4096\n"
    "ds_read_b128 a[12:15], v241 offset:6144\n"
    "ds_read_b128 a[16:19], v241 offset:8192\n"
    "ds_read_b128 a[20:23], v241 offset:10240\n"
    "ds_read_b128 a[24:27], v241 offset:12288\n"
    "ds_read_b128 a[28:31], v241 offset:14336\n"
    "ds_read_b128 a[32:35], v242 offset:0\n"
    "ds_read_b128 a[36:39], v242 offset:2048\n"
    "ds_read_b128 a[40:43], v242 offset:4096\n"
    "ds_read_b128 a[44:47], v242 offset:6144\n"
    "ds_read_b128 a[48:51], v242 offset:8192\n"
    "ds_read_b128 a[52:55], v242 offset:10240\n"
    "ds_read_b128 a[56:59], v242 offset:12288\n"
    "ds_read_b128 a[60:63], v242 offset:14336\n"
    "s_branch CR12_%=\n"
    "CQ12_%=:\n"
    "ds_read_b128 a[64:67], v241 offset:0\n"
    "ds_read_b128 a[68:71], v241 offset:2048\n"
    "ds_read_b128 a[72:75], v241 offset:4096\n"
    "ds_read_b128 a[76:79], v241 offset:6144\n"
    "ds_read_b128 a[80:83], v241 offset:8192\n"
    "ds_read_b128 a[84:87], v241 offset:10240\n"
    "ds_read_b128 a[88:91], v241 offset:12288\n"
    "ds_read_b128 a[92:95], v241 offset:14336\n"
    "ds_read_b128 a[96:99], v242 offset:0\n"
    "ds_read_b128 a[100:103], v242 offset:2048\n"
    "ds_read_b128 a[104:107], v242 offset:4096\n"
    "ds_read_b128 a[108:111], v242 offset:6144\n"
    "ds_read_b128 a[112:115], v242 offset:8192\n"
    "ds_read_b128 a[116:119], v242 offset:10240\n"
    "ds_read_b128 a[120:123], v242 offset:12288\n"
    "ds_read_b128 a[124:127], v242 offset:14336\n"
    "s_branch CR12_%=\n"
    "CP13_%=:\n"
    "s_cmp_lg_u32 s84, 13\n"
    "s_cbranch_scc1 CQ13_%=\n"
    "ds_read_b128 a[0:3], v241 offset:16384\n"
    "ds_read_b128 a[4:7], v241 offset:18432\n"
    "ds_read_b128 a[8:11], v241 offset:20480\n"
    "ds_read_b128 a[12:15], v241 offset:22528\n"
    "ds_read_b128 a[16:19], v241 offset:24576\n"
    "ds_read_b128 a[20:23], v241 offset:26624\n"
    "ds_read_b128 a[24:27], v241 offset:28672\n"
    "ds_read_b128 a[28:31], v241 offset:30720\n"
    "ds_read_b128 a[32:35], v242 offset:16384\n"
    "ds_read_b128 a[36:39], v242 offset:18432\n"
    "ds_read_b128 a[40:43], v242 offset:20480\n"
    "ds_read_b128 a[44:47], v242 offset:22528\n"
    "ds_read_b128 a[48:51], v242 offset:24576\n"
    "ds_read_b128 a[52:55], v242 offset:26624\n"
    "ds_read_b128 a[56:59], v242 offset:28672\n"
    "ds_read_b128 a[60:63], v242 offset:30720\n"
    "s_branch CR13_%=\n"
    "CQ13_%=:\n"
    "ds_read_b128 a[64:67], v241 offset:16384\n"
    "ds_read_b128 a[68:71], v241 offset:18432\n"
    "ds_read_b128 a[72:75], v241 offset:20480\n"
    "ds_read_b128 a[76:79], v241 offset:22528\n"
    "ds_read_b128 a[80:83], v241 offset:24576\n"
    "ds_read_b128 a[84:87], v241 offset:26624\n"
    "ds_read_b128 a[88:91], v241 offset:28672\n"
    "ds_read_b128 a[92:95], v241 offset:30720\n"
    "ds_read_b128 a[96:99], v242 offset:16384\n"
    "ds_read_b128 a[100:103], v242 offset:18432\n"
    "ds_read_b128 a[104:107], v242 offset:20480\n"
    "ds_read_b128 a[108:111], v242 offset:22528\n"
    "ds_read_b128 a[112:115], v242 offset:24576\n"
    "ds_read_b128 a[116:119], v242 offset:26624\n"
    "ds_read_b128 a[120:123], v242 offset:28672\n"
    "ds_read_b128 a[124:127], v242 offset:30720\n"
    "s_branch CR13_%=\n"
    "CP14_%=:\n"
    "s_cmp_lg_u32 s84, 14\n"
    "s_cbranch_scc1 CQ14_%=\n"
    "ds_read_b128 a[0:3], v241 offset:49152\n"
    "ds_read_b128 a[4:7], v241 offset:51200\n"
    "ds_read_b128 a[8:11], v241 offset:53248\n"
    "ds_read_b128 a[12:15], v241 offset:55296\n"
    "ds_read_b128 a[16:19], v241 offset:57344\n"
    "ds_read_b128 a[20:23], v241 offset:59392\n"
    "ds_read_b128 a[24:27], v241 offset:61440\n"
    "ds_read_b128 a[28:31], v241 offset:63488\n"
    "ds_read_b128 a[32:35], v242 offset:49152\n"
    "ds_read_b128 a[36:39], v242 offset:51200\n"
    "ds_read_b128 a[40:43], v242 offset:53248\n"
    "ds_read_b128 a[44:47], v242 offset:55296\n"
    "ds_read_b128 a[48:51], v242 offset:57344\n"
    "ds_read_b128 a[52:55], v242 offset:59392\n"
    "ds_read_b128 a[56:59], v242 offset:61440\n"
    "ds_read_b128 a[60:63], v242 offset:63488\n"
    "s_branch CR14_%=\n"
    "CQ14_%=:\n"
    "ds_read_b128 a[64:67], v241 offset:49152\n"
    "ds_read_b128 a[68:71], v241 offset:51200\n"
    "ds_read_b128 a[72:75], v241 offset:53248\n"
    "ds_read_b128 a[76:79], v241 offset:55296\n"
    "ds_read_b128 a[80:83], v241 offset:57344\n"
    "ds_read_b128 a[84:87], v241 offset:59392\n"
    "ds_read_b128 a[88:91], v241 offset:61440\n"
    "ds_read_b128 a[92:95], v241 offset:63488\n"
    "ds_read_b128 a[96:99], v242 offset:49152\n"
    "ds_read_b128 a[100:103], v242 offset:51200\n"
    "ds_read_b128 a[104:107], v242 offset:53248\n"
    "ds_read_b128 a[108:111], v242 offset:55296\n"
    "ds_read_b128 a[112:115], v242 offset:57344\n"
    "ds_read_b128 a[116:119], v242 offset:59392\n"
    "ds_read_b128 a[120:123], v242 offset:61440\n"
    "ds_read_b128 a[124:127], v242 offset:63488\n"
    "s_branch CR14_%=\n"
    "CP15_%=:\n"
    "s_cmp_lg_u32 s84, 15\n"
    "s_cbranch_scc1 CQ15_%=\n"
    "ds_read_b128 a[0:3], v241 offset:32768\n"
    "ds_read_b128 a[4:7], v241 offset:34816\n"
    "ds_read_b128 a[8:11], v241 offset:36864\n"
    "ds_read_b128 a[12:15], v241 offset:38912\n"
    "ds_read_b128 a[16:19], v241 offset:40960\n"
    "ds_read_b128 a[20:23], v241 offset:43008\n"
    "ds_read_b128 a[24:27], v241 offset:45056\n"
    "ds_read_b128 a[28:31], v241 offset:47104\n"
    "ds_read_b128 a[32:35], v242 offset:32768\n"
    "ds_read_b128 a[36:39], v242 offset:34816\n"
    "ds_read_b128 a[40:43], v242 offset:36864\n"
    "ds_read_b128 a[44:47], v242 offset:38912\n"
    "ds_read_b128 a[48:51], v242 offset:40960\n"
    "ds_read_b128 a[52:55], v242 offset:43008\n"
    "ds_read_b128 a[56:59], v242 offset:45056\n"
    "ds_read_b128 a[60:63], v242 offset:47104\n"
    "s_branch CR15_%=\n"
    "CQ15_%=:\n"
    "ds_read_b128 a[64:67], v241 offset:32768\n"
    "ds_read_b128 a[68:71], v241 offset:34816\n"
    "ds_read_b128 a[72:75], v241 offset:36864\n"
    "ds_read_b128 a[76:79], v241 offset:38912\n"
    "ds_read_b128 a[80:83], v241 offset:40960\n"
    "ds_read_b128 a[84:87], v241 offset:43008\n"
    "ds_read_b128 a[88:91], v241 offset:45056\n"
    "ds_read_b128 a[92:95], v241 offset:47104\n"
    "ds_read_b128 a[96:99], v242 offset:32768\n"
    "ds_read_b128 a[100:103], v242 offset:34816\n"
    "ds_read_b128 a[104:107], v242 offset:36864\n"
    "ds_read_b128 a[108:111], v242 offset:38912\n"
    "ds_read_b128 a[112:115], v242 offset:40960\n"
    "ds_read_b128 a[116:119], v242 offset:43008\n"
    "ds_read_b128 a[120:123], v242 offset:45056\n"
    "ds_read_b128 a[124:127], v242 offset:47104\n"
    "s_branch CR15_%=\n"
    "OA_%=:\n"
    "s_cmp_eq_u32 s52, 2\n"
    "s_cbranch_scc1 OC_%=\n"
    "s_lshr_b32 s60, %3, 3\n"
    "s_lshl_b32 s60, s60, 2\n"
    "s_add_u32 s60, s60, s65\n"
    "s_lshl_b32 s60, s60, 10\n"
    "s_min_u32 s60, s60, CODE_END_%= - LAST_%= - 1024\n"
    "s_getpc_b64 s[62:63]\n"
    "LA9001_%=:\n"
    "s_add_u32 s62, s62, LAST_%= - LA9001_%=\n"
    "s_addc_u32 s63, s63, 0\n"
    "s_add_u32 s62, s62, s60\n"
    "s_addc_u32 s63, s63, 0\n"
    "global_load_dwordx4 v[244:247], v240, s[62:63]\n"
    "s_branch OB_%=\n"
    "OC_%=:\n"
    "buffer_load_dwordx4 v[248:251], v240, s[24:27], 0 offen sc1\n"
    "s_branch OB_%=\n"
    "OD_%=:\n"
    "s_cmp_eq_u32 s52, 10\n"
    "s_cbranch_scc1 LAST_%=\n"
    "v_and_b32 v244, v248, v249\n"
    "v_and_b32 v245, v250, v251\n"
    "v_and_b32 v244, v244, v245\n"
    "v_and_b32 v244, 1, v244\n"
    "v_cmp_ne_u32 vcc, 1, v244\n"
    "s_nop 1\n"
    "s_cmp_eq_u64 vcc, 0\n"
    "s_cbranch_scc1 LA9002_%=\n"
    "s_mov_b32 s69, 0x400000\n"
    "LA9003_%=:\n"
    "s_sleep 2\n"
    "buffer_load_dwordx4 v[248:251], v240, s[24:27], 0 offen sc1\n"
    "s_waitcnt vmcnt(0)\n"
    "s_sub_u32 s69, s69, 1\n"
    "s_cbranch_scc1 LA9002_%=\n"
    "v_and_b32 v244, v248, v249\n"
    "v_and_b32 v245, v250, v251\n"
    "v_and_b32 v244, v244, v245\n"
    "v_and_b32 v244, 1, v244\n"
    "v_cmp_ne_u32 vcc, 1, v244\n"
    "s_nop 1\n"
    "s_cmp_eq_u64 vcc, 0\n"
    "s_cbranch_scc0 LA9003_%=\n"
    "LA9002_%=:\n"
    "v_add_u32 v248, v248, v249\n"
    "v_add3_u32 v248, v248, v250, v251\n"
    "s_lshr_b32 s60, %0, 2\n"
    "s_xor_b32 s61, s60, 2\n"
    "v_and_b32 v249, 0xfffff, v248\n"
    "v_readlane_b32 s62, v248, s60\n"
    "v_readlane_b32 s63, v248, s61\n"
    "v_add_u32_dpp v249, v249, v249 row_ror:8 row_mask:0xf bank_mask:0xf\n"
    "s_nop 1\n"
    "v_add_u32_dpp v249, v249, v249 row_ror:4 row_mask:0xf bank_mask:0xf\n"
    "s_nop 1\n"
    "v_add_u32_dpp v249, v249, v249 row_ror:2 row_mask:0xf bank_mask:0xf\n"
    "s_nop 1\n"
    "v_add_u32_dpp v249, v249, v249 row_ror:1 row_mask:0xf bank_mask:0xf\n"
    "s_nop 1\n"
    "s_add_u32 s62, s62, s63\n"
    "s_lshr_b32 s62, s62, 20\n"
    "s_cmp_gt_u32 s62, 2048\n"
    "s_cselect_b32 s64, 1, 0\n"
    "v_readlane_b32 s62, v249, s60\n"
    "s_cmp_gt_u32 s62, 262208\n"
    "s_cselect_b32 s62, 524288, 0\n"
    "s_lshl_b32 s54, %3, 19\n"
    "s_xor_b32 s54, s54, s62\n"
    "s_xor_b32 s59, s54, 4194304\n"
    "s_lshl_b32 s62, s64, 13\n"
    "s_xor_b32 s54, s54, s62\n"
    "s_branch OE_%=\n"
    "LAST_%=:\n"
    "s_waitcnt vmcnt(10) lgkmcnt(14)\n"
    "v_mfma_f32_16x16x32_bf16 v[224:227], a[208:211], a[156:159], v[224:227]\n"
    "v_mfma_f32_16x16x32_bf16 v[224:227], a[212:215], a[188:191], v[224:227]\n"
    "s_waitcnt lgkmcnt(12)\n"
    "v_mfma_f32_16x16x32_bf16 v[192:195], a[212:215], a[184:187], v[192:195]\n"
    "v_mfma_f32_16x16x32_bf16 v[192:195], a[208:211], a[152:155], v[192:195]\n"
    "s_waitcnt lgkmcnt(10)\n"
    "v_mfma_f32_16x16x32_bf16 v[160:163], a[208:211], a[148:151], v[160:163]\n"
    "v_mfma_f32_16x16x32_bf16 v[160:163], a[212:215], a[180:183], v[160:163]\n"
    "s_waitcnt lgkmcnt(8)\n"
    "v_mfma_f32_16x16x32_bf16 v[128:131], a[212:215], a[176:179], v[128:131]\n"
    "s_mov_b32 m0, s56\n"
    "s_xor_b32 s90, s90, 0x195800\n"
    "v_mfma_f32_16x16x32_bf16 v[128:131], a[208:211], a[144:147], v[128:131]\n"
    "s_waitcnt lgkmcnt(6)\n"
    "v_mfma_f32_16x16x32_bf16 v[96:99], a[208:211], a[140:143], v[96:99]\n"
    "buffer_load_dwordx4 v240, s[32:35], s90 offen sc1 lds\n"
    "v_mfma_f32_16x16x32_bf16 v[96:99], a[212:215], a[172:175], v[96:99]\n"
    "s_waitcnt lgkmcnt(4)\n"
    "v_mfma_f32_16x16x32_bf16 v[64:67], a[212:215], a[168:171], v[64:67]\n"
    "v_mfma_f32_16x16x32_bf16 v[64:67], a[208:211], a[136:139], v[64:67]\n"
    "s_waitcnt lgkmcnt(2)\n"
    "v_mfma_f32_16x16x32_bf16 v[32:35], a[208:211], a[132:135], v[32:35]\n"
    "v_mfma_f32_16x16x32_bf16 v[32:35], a[212:215], a[164:167], v[32:35]\n"
    "s_waitcnt lgkmcnt(0)\n"
    "v_mfma_f32_16x16x32_bf16 v[0:3], a[212:215], a[160:163], v[0:3]\n"
    "v_mfma_f32_16x16x32_bf16 v[0:3], a[208:211], a[128:131], v[0:3]\n"
    "s_waitcnt vmcnt(9)\n"
    "v_mfma_f32_16x16x32_bf16 v[4:7], a[216:219], a[128:131], v[4:7]\n"
    "buffer_load_dwordx4 a[208:211], v240, s[20:23], s53 offen offset:0 sc1\n"
    "v_mfma_f32_16x16x32_bf16 v[4:7], a[220:223], a[160:163], v[4:7]\n"
    "buffer_load_dwordx4 a[212:215], v240, s[20:23], s53 offen offset:1024 sc1\n"
    "v_mfma_f32_16x16x32_bf16 v[36:39], a[220:223], a[164:167], v[36:39]\n"
    "v_mfma_f32_16x16x32_bf16 v[36:39], a[216:219], a[132:135], v[36:39]\n"
    "v_mfma_f32_16x16x32_bf16 v[68:71], a[216:219], a[136:139], v[68:71]\n"
    "v_mfma_f32_16x16x32_bf16 v[68:71], a[220:223], a[168:171], v[68:71]\n"
    "v_mfma_f32_16x16x32_bf16 v[100:103], a[220:223], a[172:175], v[100:103]\n"
    "s_addk_i32 m0, 2048\n"
    "s_xor_b32 s90, s90, 0x800\n"
    "v_mfma_f32_16x16x32_bf16 v[100:103], a[216:219], a[140:143], v[100:103]\n"
    "buffer_load_dwordx4 v240, s[32:35], s90 offen sc1 lds\n"
    "v_mfma_f32_16x16x32_bf16 v[132:135], a[216:219], a[144:147], v[132:135]\n"
    "v_mfma_f32_16x16x32_bf16 v[132:135], a[220:223], a[176:179], v[132:135]\n"
    "v_mfma_f32_16x16x32_bf16 v[164:167], a[220:223], a[180:183], v[164:167]\n"
    "v_mfma_f32_16x16x32_bf16 v[164:167], a[216:219], a[148:151], v[164:167]\n"
    "v_mfma_f32_16x16x32_bf16 v[196:199], a[216:219], a[152:155], v[196:199]\n"
    "v_mfma_f32_16x16x32_bf16 v[196:199], a[220:223], a[184:187], v[196:199]\n"
    "v_mfma_f32_16x16x32_bf16 v[228:231], a[220:223], a[188:191], v[228:231]\n"
    "v_mfma_f32_16x16x32_bf16 v[228:231], a[216:219], a[156:159], v[228:231]\n"
    "s_waitcnt vmcnt(10)\n"
    "v_mfma_f32_16x16x32_bf16 v[232:235], a[224:227], a[156:159], v[232:235]\n"
    "buffer_load_dwordx4 a[216:219], v240, s[20:23], s53 offen offset:2048 sc1\n"
    "v_mfma_f32_16x16x32_bf16 v[232:235], a[228:231], a[188:191], v[232:235]\n"
    "buffer_load_dwordx4 a[220:223], v240, s[20:23], s53 offen offset:3072 sc1\n"
    "v_mfma_f32_16x16x32_bf16 v[200:203], a[228:231], a[184:187], v[200:203]\n"
    "v_mfma_f32_16x16x32_bf16 v[200:203], a[224:227], a[152:155], v[200:203]\n"
    "v_mfma_f32_16x16x32_bf16 v[168:171], a[224:227], a[148:151], v[168:171]\n"
    "v_mfma_f32_16x16x32_bf16 v[168:171], a[228:231], a[180:183], v[168:171]\n"
    "v_mfma_f32_16x16x32_bf16 v[136:139], a[228:231], a[176:179], v[136:139]\n"
    "s_addk_i32 m0, 2048\n"
    "s_xor_b32 s90, s90, 0x101800\n"
    "v_mfma_f32_16x16x32_bf16 v[136:139], a[224:227], a[144:147], v[136:139]\n"
    "buffer_load_dwordx4 v240, s[32:35], s90 offen sc1 lds\n"
    "v_mfma_f32_16x16x32_bf16 v[104:107], a[224:227], a[140:143], v[104:107]\n"
    "v_mfma_f32_16x16x32_bf16 v[104:107], a[228:231], a[172:175], v[104:107]\n"
    "v_mfma_f32_16x16x32_bf16 v[72:75], a[228:231], a[168:171], v[72:75]\n"
    "v_mfma_f32_16x16x32_bf16 v[72:75], a[224:227], a[136:139], v[72:75]\n"
    "v_mfma_f32_16x16x32_bf16 v[40:43], a[224:227], a[132:135], v[40:43]\n"
    "v_mfma_f32_16x16x32_bf16 v[40:43], a[228:231], a[164:167], v[40:43]\n"
    "v_mfma_f32_16x16x32_bf16 v[8:11], a[228:231], a[160:163], v[8:11]\n"
    "v_mfma_f32_16x16x32_bf16 v[8:11], a[224:227], a[128:131], v[8:11]\n"
    "s_waitcnt vmcnt(11)\n"
    "s_addk_i32 s53, 4096\n"
    "v_mfma_f32_16x16x32_bf16 v[12:15], a[232:235], a[128:131], v[12:15]\n"
    "buffer_load_dwordx4 a[224:227], v240, s[20:23], s53 offen offset:0 sc1\n"
    "v_mfma_f32_16x16x32_bf16 v[12:15], a[236:239], a[160:163], v[12:15]\n"
    "buffer_load_dwordx4 a[228:231], v240, s[20:23], s53 offen offset:1024 sc1\n"
    "v_mfma_f32_16x16x32_bf16 v[44:47], a[236:239], a[164:167], v[44:47]\n"
    "v_mfma_f32_16x16x32_bf16 v[44:47], a[232:235], a[132:135], v[44:47]\n"
    "v_mfma_f32_16x16x32_bf16 v[76:79], a[232:235], a[136:139], v[76:79]\n"
    "v_mfma_f32_16x16x32_bf16 v[76:79], a[236:239], a[168:171], v[76:79]\n"
    "v_mfma_f32_16x16x32_bf16 v[108:111], a[236:239], a[172:175], v[108:111]\n"
    "s_addk_i32 m0, 2048\n"
    "s_xor_b32 s90, s90, 0x800\n"
    "v_mfma_f32_16x16x32_bf16 v[108:111], a[232:235], a[140:143], v[108:111]\n"
    "buffer_load_dwordx4 v240, s[32:35], s90 offen sc1 lds\n"
    "v_mfma_f32_16x16x32_bf16 v[140:143], a[232:235], a[144:147], v[140:143]\n"
    "v_mfma_f32_16x16x32_bf16 v[140:143], a[236:239], a[176:179], v[140:143]\n"
    "v_mfma_f32_16x16x32_bf16 v[172:175], a[236:239], a[180:183], v[172:175]\n"
    "v_mfma_f32_16x16x32_bf16 v[172:175], a[232:235], a[148:151], v[172:175]\n"
    "v_mfma_f32_16x16x32_bf16 v[204:207], a[232:235], a[152:155], v[204:207]\n"
    "v_mfma_f32_16x16x32_bf16 v[204:207], a[236:239], a[184:187], v[204:207]\n"
    "v_mfma_f32_16x16x32_bf16 v[236:239], a[236:239], a[188:191], v[236:239]\n"
    "v_mfma_f32_16x16x32_bf16 v[236:239], a[232:235], a[156:159], v[236:239]\n"
    "s_waitcnt vmcnt(12)\n"
    "v_mfma_f32_16x16x32_bf16 a[240:243], a[192:195], a[156:159], a[240:243]\n"
    "buffer_load_dwordx4 a[232:235], v240, s[20:23], s53 offen offset:2048 sc1\n"
    "v_mfma_f32_16x16x32_bf16 a[240:243], a[196:199], a[188:191], a[240:243]\n"
    "buffer_load_dwordx4 a[236:239], v240, s[20:23], s53 offen offset:3072 sc1\n"
    "v_mfma_f32_16x16x32_bf16 v[208:211], a[196:199], a[184:187], v[208:211]\n"
    "v_mfma_f32_16x16x32_bf16 v[208:211], a[192:195], a[152:155], v[208:211]\n"
    "v_mfma_f32_16x16x32_bf16 v[176:179], a[192:195], a[148:151], v[176:179]\n"
    "v_mfma_f32_16x16x32_bf16 v[176:179], a[196:199], a[180:183], v[176:179]\n"
    "v_mfma_f32_16x16x32_bf16 v[144:147], a[196:199], a[176:179], v[144:147]\n"
    "v_mfma_f32_16x16x32_bf16 v[144:147], a[192:195], a[144:147], v[144:147]\n"
    "v_mfma_f32_16x16x32_bf16 v[112:115], a[192:195], a[140:143], v[112:115]\n"
    "v_mfma_f32_16x16x32_bf16 v[112:115], a[196:199], a[172:175], v[112:115]\n"
    "v_mfma_f32_16x16x32_bf16 v[80:83], a[196:199], a[168:171], v[80:83]\n"
    "v_mfma_f32_16x16x32_bf16 v[80:83], a[192:195], a[136:139], v[80:83]\n"
    "v_mfma_f32_16x16x32_bf16 v[48:51], a[192:195], a[132:135], v[48:51]\n"
    "s_addk_i32 m0, 2048\n"
    "s_xor_b32 s90, s90, 0x185800\n"
    "v_mfma_f32_16x16x32_bf16 v[48:51], a[196:199], a[164:167], v[48:51]\n"
    "buffer_load_dwordx4 v240, s[32:35], s90 offen sc1 lds\n"
    "v_mfma_f32_16x16x32_bf16 v[16:19], a[196:199], a[160:163], v[16:19]\n"
    "v_mfma_f32_16x16x32_bf16 v[16:19], a[192:195], a[128:131], v[16:19]\n"
    "s_waitcnt vmcnt(13)\n"
    "s_addk_i32 s53, 4096\n"
    "v_mfma_f32_16x16x32_bf16 v[20:23], a[200:203], a[128:131], v[20:23]\n"
    "buffer_load_dwordx4 a[192:195], v240, s[20:23], s53 offen offset:0 sc1\n"
    "v_mfma_f32_16x16x32_bf16 v[20:23], a[204:207], a[160:163], v[20:23]\n"
    "buffer_load_dwordx4 a[196:199], v240, s[20:23], s53 offen offset:1024 sc1\n"
    "v_mfma_f32_16x16x32_bf16 v[52:55], a[204:207], a[164:167], v[52:55]\n"
    "v_mfma_f32_16x16x32_bf16 v[52:55], a[200:203], a[132:135], v[52:55]\n"
    "v_mfma_f32_16x16x32_bf16 v[84:87], a[200:203], a[136:139], v[84:87]\n"
    "v_mfma_f32_16x16x32_bf16 v[84:87], a[204:207], a[168:171], v[84:87]\n"
    "v_mfma_f32_16x16x32_bf16 v[116:119], a[204:207], a[172:175], v[116:119]\n"
    "v_mfma_f32_16x16x32_bf16 v[116:119], a[200:203], a[140:143], v[116:119]\n"
    "v_mfma_f32_16x16x32_bf16 v[148:151], a[200:203], a[144:147], v[148:151]\n"
    "v_mfma_f32_16x16x32_bf16 v[148:151], a[204:207], a[176:179], v[148:151]\n"
    "v_mfma_f32_16x16x32_bf16 v[180:183], a[204:207], a[180:183], v[180:183]\n"
    "v_mfma_f32_16x16x32_bf16 v[180:183], a[200:203], a[148:151], v[180:183]\n"
    "v_mfma_f32_16x16x32_bf16 v[212:215], a[200:203], a[152:155], v[212:215]\n"
    "s_addk_i32 m0, 2048\n"
    "s_xor_b32 s90, s90, 0x800\n"
    "v_mfma_f32_16x16x32_bf16 v[212:215], a[204:207], a[184:187], v[212:215]\n"
    "buffer_load_dwordx4 v240, s[32:35], s90 offen sc1 lds\n"
    "v_mfma_f32_16x16x32_bf16 a[244:247], a[204:207], a[188:191], a[244:247]\n"
    "v_mfma_f32_16x16x32_bf16 a[244:247], a[200:203], a[156:159], a[244:247]\n"
    "s_waitcnt vmcnt(13)\n"
    "v_mfma_f32_16x16x32_bf16 a[248:251], a[208:211], a[156:159], a[248:251]\n"
    "buffer_load_dwordx4 a[200:203], v240, s[20:23], s53 offen offset:2048 sc1\n"
    "v_mfma_f32_16x16x32_bf16 a[248:251], a[212:215], a[188:191], a[248:251]\n"
    "buffer_load_dwordx4 a[204:207], v240, s[20:23], s53 offen offset:3072 sc1\n"
    "v_mfma_f32_16x16x32_bf16 v[216:219], a[212:215], a[184:187], v[216:219]\n"
    "v_mfma_f32_16x16x32_bf16 v[216:219], a[208:211], a[152:155], v[216:219]\n"
    "v_mfma_f32_16x16x32_bf16 v[184:187], a[208:211], a[148:151], v[184:187]\n"
    "v_mfma_f32_16x16x32_bf16 v[184:187], a[212:215], a[180:183], v[184:187]\n"
    "v_mfma_f32_16x16x32_bf16 v[152:155], a[212:215], a[176:179], v[152:155]\n"
    "v_mfma_f32_16x16x32_bf16 v[152:155], a[208:211], a[144:147], v[152:155]\n"
    "v_mfma_f32_16x16x32_bf16 v[120:123], a[208:211], a[140:143], v[120:123]\n"
    "v_mfma_f32_16x16x32_bf16 v[120:123], a[212:215], a[172:175], v[120:123]\n"
    "v_mfma_f32_16x16x32_bf16 v[88:91], a[212:215], a[168:171], v[88:91]\n"
    "v_mfma_f32_16x16x32_bf16 v[88:91], a[208:211], a[136:139], v[88:91]\n"
    "v_mfma_f32_16x16x32_bf16 v[56:59], a[208:211], a[132:135], v[56:59]\n"
    "s_addk_i32 m0, 2048\n"
    "s_xor_b32 s90, s90, 0x101800\n"
    "v_mfma_f32_16x16x32_bf16 v[56:59], a[212:215], a[164:167], v[56:59]\n"
    "buffer_load_dwordx4 v240, s[32:35], s90 offen sc1 lds\n"
    "v_mfma_f32_16x16x32_bf16 v[24:27], a[212:215], a[160:163], v[24:27]\n"
    "v_mfma_f32_16x16x32_bf16 v[24:27], a[208:211], a[128:131], v[24:27]\n"
    "s_waitcnt vmcnt(13)\n"
    "s_addk_i32 s53, 12288\n"
    "v_mfma_f32_16x16x32_bf16 v[28:31], a[216:219], a[128:131], v[28:31]\n"
    "buffer_load_dwordx4 a[208:211], v240, s[20:23], s53 offen offset:0 sc1\n"
    "v_mfma_f32_16x16x32_bf16 v[28:31], a[220:223], a[160:163], v[28:31]\n"
    "buffer_load_dwordx4 a[212:215], v240, s[20:23], s53 offen offset:1024 sc1\n"
    "v_mfma_f32_16x16x32_bf16 v[60:63], a[220:223], a[164:167], v[60:63]\n"
    "ds_read_b128 a[128:131], v241 offset:16384\n"
    "v_mfma_f32_16x16x32_bf16 v[60:63], a[216:219], a[132:135], v[60:63]\n"
    "ds_read_b128 a[160:163], v241 offset:17408\n"
    "v_mfma_f32_16x16x32_bf16 v[92:95], a[216:219], a[136:139], v[92:95]\n"
    "ds_read_b128 a[164:167], v241 offset:19456\n"
    "v_mfma_f32_16x16x32_bf16 v[92:95], a[220:223], a[168:171], v[92:95]\n"
    "ds_read_b128 a[132:135], v241 offset:18432\n"
    "v_mfma_f32_16x16x32_bf16 v[124:127], a[220:223], a[172:175], v[124:127]\n"
    "ds_read_b128 a[136:139], v241 offset:20480\n"
    "v_mfma_f32_16x16x32_bf16 v[124:127], a[216:219], a[140:143], v[124:127]\n"
    "ds_read_b128 a[168:171], v241 offset:21504\n"
    "v_mfma_f32_16x16x32_bf16 v[156:159], a[216:219], a[144:147], v[156:159]\n"
    "ds_read_b128 a[172:175], v241 offset:23552\n"
    "v_mfma_f32_16x16x32_bf16 v[156:159], a[220:223], a[176:179], v[156:159]\n"
    "ds_read_b128 a[140:143], v241 offset:22528\n"
    "v_mfma_f32_16x16x32_bf16 v[188:191], a[220:223], a[180:183], v[188:191]\n"
    "ds_read_b128 a[144:147], v241 offset:24576\n"
    "v_mfma_f32_16x16x32_bf16 v[188:191], a[216:219], a[148:151], v[188:191]\n"
    "ds_read_b128 a[176:179], v241 offset:25600\n"
    "v_mfma_f32_16x16x32_bf16 v[220:223], a[216:219], a[152:155], v[220:223]\n"
    "s_addk_i32 m0, 2048\n"
    "s_xor_b32 s90, s90, 0x800\n"
    "v_mfma_f32_16x16x32_bf16 v[220:223], a[220:223], a[184:187], v[220:223]\n"
    "buffer_load_dwordx4 v240, s[32:35], s90 offen sc1 lds\n"
    "v_mfma_f32_16x16x32_bf16 a[252:255], a[220:223], a[188:191], a[252:255]\n"
    "ds_read_b128 a[180:183], v241 offset:27648\n"
    "ds_read_b128 a[148:151], v241 offset:26624\n"
    "v_mfma_f32_16x16x32_bf16 a[252:255], a[216:219], a[156:159], a[252:255]\n"
    "s_waitcnt vmcnt(13) lgkmcnt(10)\n"
    "ds_read_b128 a[152:155], v241 offset:28672\n"
    "ds_read_b128 a[184:187], v241 offset:29696\n"
    "v_mfma_f32_16x16x32_bf16 v[0:3], a[224:227], a[128:131], v[0:3]\n"
    "buffer_load_dwordx4 a[216:219], v240, s[20:23], s53 offen offset:2048 sc1\n"
    "v_mfma_f32_16x16x32_bf16 v[0:3], a[228:231], a[160:163], v[0:3]\n"
    "s_waitcnt lgkmcnt(10)\n"
    "v_mfma_f32_16x16x32_bf16 v[32:35], a[228:231], a[164:167], v[32:35]\n"
    "buffer_load_dwordx4 a[220:223], v240, s[20:23], s53 offen offset:3072 sc1\n"
    "v_mfma_f32_16x16x32_bf16 v[32:35], a[224:227], a[132:135], v[32:35]\n"
    "s_waitcnt lgkmcnt(8)\n"
    "ds_read_b128 a[188:191], v241 offset:31744\n"
    "ds_read_b128 a[156:159], v241 offset:30720\n"
    "v_mfma_f32_16x16x32_bf16 v[64:67], a[224:227], a[136:139], v[64:67]\n"
    "v_add_f32_dpp v16, v252, v16 row_newbcast:0 row_mask:0xf bank_mask:0xf\n"
    "v_add_f32_dpp v17, v252, v17 row_newbcast:1 row_mask:0xf bank_mask:0xf\n"
    "v_mfma_f32_16x16x32_bf16 v[64:67], a[228:231], a[168:171], v[64:67]\n"
    "s_waitcnt lgkmcnt(8)\n"
    "v_add_f32_dpp v18, v252, v18 row_newbcast:2 row_mask:0xf bank_mask:0xf\n"
    "v_mfma_f32_16x16x32_bf16 v[96:99], a[228:231], a[172:175], v[96:99]\n"
    "v_add_f32_dpp v19, v252, v19 row_newbcast:3 row_mask:0xf bank_mask:0xf\n"
    "v_mfma_f32_16x16x32_bf16 v[96:99], a[224:227], a[140:143], v[96:99]\n"
    "s_waitcnt lgkmcnt(6)\n"
    "v_add_f32_dpp v20, v252, v20 row_newbcast:4 row_mask:0xf bank_mask:0xf\n"
    "v_add_f32_dpp v21, v252, v21 row_newbcast:5 row_mask:0xf bank_mask:0xf\n"
    "v_mfma_f32_16x16x32_bf16 v[128:131], a[224:227], a[144:147], v[128:131]\n"
    "v_add_f32_dpp v22, v252, v22 row_newbcast:6 row_mask:0xf bank_mask:0xf\n"
    "v_mfma_f32_16x16x32_bf16 v[128:131], a[228:231], a[176:179], v[128:131]\n"
    "s_waitcnt lgkmcnt(4)\n"
    "v_add_f32_dpp v23, v252, v23 row_newbcast:7 row_mask:0xf bank_mask:0xf\n"
    "v_mfma_f32_16x16x32_bf16 v[160:163], a[228:231], a[180:183], v[160:163]\n"
    "v_add_f32_dpp v24, v252, v24 row_newbcast:8 row_mask:0xf bank_mask:0xf\n"
    "v_add_f32_dpp v25, v252, v25 row_newbcast:9 row_mask:0xf bank_mask:0xf\n"
    "v_mfma_f32_16x16x32_bf16 v[160:163], a[224:227], a[148:151], v[160:163]\n"
    "s_waitcnt lgkmcnt(2)\n"
    "v_add_f32_dpp v26, v252, v26 row_newbcast:10 row_mask:0xf bank_mask:0xf\n"
    "v_mfma_f32_16x16x32_bf16 v[192:195], a[224:227], a[152:155], v[192:195]\n"
    "v_add_f32_dpp v27, v252, v27 row_newbcast:11 row_mask:0xf bank_mask:0xf\n"
    "v_mfma_f32_16x16x32_bf16 v[192:195], a[228:231], a[184:187], v[192:195]\n"
    "s_waitcnt lgkmcnt(0)\n"
    "v_add_f32_dpp v28, v252, v28 row_newbcast:12 row_mask:0xf bank_mask:0xf\n"
    "v_add_f32_dpp v29, v252, v29 row_newbcast:13 row_mask:0xf bank_mask:0xf\n"
    "v_mfma_f32_16x16x32_bf16 v[224:227], a[228:231], a[188:191], v[224:227]\n"
    "v_add_f32_dpp v30, v252, v30 row_newbcast:14 row_mask:0xf bank_mask:0xf\n"
    "v_mfma_f32_16x16x32_bf16 v[224:227], a[224:227], a[156:159], v[224:227]\n"
    "s_waitcnt vmcnt(12)\n"
    "s_addk_i32 s53, 4096\n"
    "v_add_f32_dpp v31, v252, v31 row_newbcast:15 row_mask:0xf bank_mask:0xf\n"
    "v_mfma_f32_16x16x32_bf16 v[228:231], a[232:235], a[156:159], v[228:231]\n"
    "buffer_load_dwordx4 a[224:227], v240, s[20:23], s53 offen offset:0 sc1\n"
    "v_mfma_f32_16x16x32_bf16 v[228:231], a[236:239], a[188:191], v[228:231]\n"
    "buffer_load_dwordx4 a[228:231], v240, s[20:23], s53 offen offset:1024 sc1\n"
    "v_mfma_f32_16x16x32_bf16 v[196:199], a[236:239], a[184:187], v[196:199]\n"
    "v_add_f32_dpp v48, v252, v48 row_newbcast:0 row_mask:0xf bank_mask:0xf\n"
    "v_add_f32_dpp v49, v252, v49 row_newbcast:1 row_mask:0xf bank_mask:0xf\n"
    "v_mfma_f32_16x16x32_bf16 v[196:199], a[232:235], a[152:155], v[196:199]\n"
    "v_add_f32_dpp v50, v252, v50 row_newbcast:2 row_mask:0xf bank_mask:0xf\n"
    "v_add_f32_dpp v51, v252, v51 row_newbcast:3 row_mask:0xf bank_mask:0xf\n"
    "v_mfma_f32_16x16x32_bf16 v[164:167], a[232:235], a[148:151], v[164:167]\n"
    "v_add_f32_dpp v52, v252, v52 row_newbcast:4 row_mask:0xf bank_mask:0xf\n"
    "v_add_f32_dpp v53, v252, v53 row_newbcast:5 row_mask:0xf bank_mask:0xf\n"
    "v_mfma_f32_16x16x32_bf16 v[164:167], a[236:239], a[180:183], v[164:167]\n"
    "v_add_f32_dpp v54, v252, v54 row_newbcast:6 row_mask:0xf bank_mask:0xf\n"
    "v_add_f32_dpp v55, v252, v55 row_newbcast:7 row_mask:0xf bank_mask:0xf\n"
    "v_mfma_f32_16x16x32_bf16 v[132:135], a[236:239], a[176:179], v[132:135]\n"
    "v_add_f32_dpp v56, v252, v56 row_newbcast:8 row_mask:0xf bank_mask:0xf\n"
    "v_mfma_f32_16x16x32_bf16 v[132:135], a[232:235], a[144:147], v[132:135]\n"
    "v_add_f32_dpp v57, v252, v57 row_newbcast:9 row_mask:0xf bank_mask:0xf\n"
    "v_add_f32_dpp v58, v252, v58 row_newbcast:10 row_mask:0xf bank_mask:0xf\n"
    "v_mfma_f32_16x16x32_bf16 v[100:103], a[232:235], a[140:143], v[100:103]\n"
    "v_add_f32_dpp v59, v252, v59 row_newbcast:11 row_mask:0xf bank_mask:0xf\n"
    "v_mfma_f32_16x16x32_bf16 v[100:103], a[236:239], a[172:175], v[100:103]\n"
    "v_add_f32_dpp v60, v252, v60 row_newbcast:12 row_mask:0xf bank_mask:0xf\n"
    "v_mfma_f32_16x16x32_bf16 v[68:71], a[236:239], a[168:171], v[68:71]\n"
    "s_movk_i32 s60, 0x70\n"
    "v_add_f32_dpp v61, v252, v61 row_newbcast:13 row_mask:0xf bank_mask:0xf\n"
    "v_add_f32_dpp v62, v252, v62 row_newbcast:14 row_mask:0xf bank_mask:0xf\n"
    "v_mfma_f32_16x16x32_bf16 v[68:71], a[232:235], a[136:139], v[68:71]\n"
    "v_add_f32_dpp v63, v252, v63 row_newbcast:15 row_mask:0xf bank_mask:0xf\n"
    "v_mfma_f32_16x16x32_bf16 v[36:39], a[232:235], a[132:135], v[36:39]\n"
    "v_lshrrev_b32 v248, 4, v240\n"
    "v_xor_b32 v248, v248, v240\n"
    "v_mfma_f32_16x16x32_bf16 v[36:39], a[236:239], a[164:167], v[36:39]\n"
    "v_and_b32 v248, 0x70, v248\n"
    "v_and_b32 v249, 0xf0, v240\n"
    "v_mfma_f32_16x16x32_bf16 v[4:7], a[236:239], a[160:163], v[4:7]\n"
    "v_lshl_add_u32 v248, v249, 3, v248\n"
    "s_lshl_b32 s61, s65, 12\n"
    "v_mfma_f32_16x16x32_bf16 v[4:7], a[232:235], a[128:131], v[4:7]\n"
    "s_waitcnt vmcnt(11)\n"
    "s_add_u32 s61, s61, s66\n"
    "v_add_u32 v248, s61, v248\n"
    "v_xor_b32 v249, 64, v248\n"
    "v_lshrrev_b32 v242, 3, v240\n"
    "v_and_b32 v242, 0x70, v242\n"
    "v_xor_b32 v242, v242, v240\n"
    "v_add_u32 v242, s61, v242\n"
    "v_and_b32 v243, 0x380, v240\n"
    "v_lshlrev_b32 v243, 4, v243\n"
    "v_and_or_b32 v243, v240, s60, v243\n"
    "v_mfma_f32_16x16x32_bf16 v[8:11], a[192:195], a[128:131], v[8:11]\n"
    "s_add_u32 s91, s53, 0xf000\n"
    "v_add_f32_dpp v80, v252, v80 row_newbcast:0 row_mask:0xf bank_mask:0xf\n"
    "v_add_f32_dpp v81, v252, v81 row_newbcast:1 row_mask:0xf bank_mask:0xf\n"
    "v_mfma_f32_16x16x32_bf16 v[8:11], a[196:199], a[160:163], v[8:11]\n"
    "buffer_load_dwordx4 a[232:235], v240, s[20:23], s53 offen offset:2048 sc1\n"
    "v_mfma_f32_16x16x32_bf16 v[40:43], a[196:199], a[164:167], v[40:43]\n"
    "buffer_load_dwordx4 a[236:239], v240, s[20:23], s53 offen offset:3072 sc1\n"
    "v_mfma_f32_16x16x32_bf16 v[40:43], a[192:195], a[132:135], v[40:43]\n"
    "v_add_f32_dpp v82, v252, v82 row_newbcast:2 row_mask:0xf bank_mask:0xf\n"
    "v_add_f32_dpp v83, v252, v83 row_newbcast:3 row_mask:0xf bank_mask:0xf\n"
    "v_mfma_f32_16x16x32_bf16 v[72:75], a[192:195], a[136:139], v[72:75]\n"
    "v_add_f32_dpp v84, v252, v84 row_newbcast:4 row_mask:0xf bank_mask:0xf\n"
    "v_add_f32_dpp v85, v252, v85 row_newbcast:5 row_mask:0xf bank_mask:0xf\n"
    "v_mfma_f32_16x16x32_bf16 v[72:75], a[196:199], a[168:171], v[72:75]\n"
    "v_add_f32_dpp v86, v252, v86 row_newbcast:6 row_mask:0xf bank_mask:0xf\n"
    "v_add_f32_dpp v87, v252, v87 row_newbcast:7 row_mask:0xf bank_mask:0xf\n"
    "v_mfma_f32_16x16x32_bf16 v[104:107], a[196:199], a[172:175], v[104:107]\n"
    "v_add_f32_dpp v88, v252, v88 row_newbcast:8 row_mask:0xf bank_mask:0xf\n"
    "v_add_f32_dpp v89, v252, v89 row_newbcast:9 row_mask:0xf bank_mask:0xf\n"
    "v_mfma_f32_16x16x32_bf16 v[104:107], a[192:195], a[140:143], v[104:107]\n"
    "v_add_f32_dpp v90, v252, v90 row_newbcast:10 row_mask:0xf bank_mask:0xf\n"
    "v_add_f32_dpp v91, v252, v91 row_newbcast:11 row_mask:0xf bank_mask:0xf\n"
    "v_mfma_f32_16x16x32_bf16 v[136:139], a[192:195], a[144:147], v[136:139]\n"
    "v_add_f32_dpp v92, v252, v92 row_newbcast:12 row_mask:0xf bank_mask:0xf\n"
    "v_add_f32_dpp v93, v252, v93 row_newbcast:13 row_mask:0xf bank_mask:0xf\n"
    "v_mfma_f32_16x16x32_bf16 v[136:139], a[196:199], a[176:179], v[136:139]\n"
    "v_add_f32_dpp v94, v252, v94 row_newbcast:14 row_mask:0xf bank_mask:0xf\n"
    "v_add_f32_dpp v95, v252, v95 row_newbcast:15 row_mask:0xf bank_mask:0xf\n"
    "v_mfma_f32_16x16x32_bf16 v[168:171], a[196:199], a[180:183], v[168:171]\n"
    "v_add_f32_dpp v112, v252, v112 row_newbcast:0 row_mask:0xf bank_mask:0xf\n"
    "v_add_f32_dpp v113, v252, v113 row_newbcast:1 row_mask:0xf bank_mask:0xf\n"
    "v_mfma_f32_16x16x32_bf16 v[168:171], a[192:195], a[148:151], v[168:171]\n"
    "v_add_f32_dpp v114, v252, v114 row_newbcast:2 row_mask:0xf bank_mask:0xf\n"
    "v_add_f32_dpp v115, v252, v115 row_newbcast:3 row_mask:0xf bank_mask:0xf\n"
    "v_mfma_f32_16x16x32_bf16 v[200:203], a[192:195], a[152:155], v[200:203]\n"
    "v_add_f32_dpp v116, v252, v116 row_newbcast:4 row_mask:0xf bank_mask:0xf\n"
    "v_add_f32_dpp v117, v252, v117 row_newbcast:5 row_mask:0xf bank_mask:0xf\n"
    "v_mfma_f32_16x16x32_bf16 v[200:203], a[196:199], a[184:187], v[200:203]\n"
    "v_add_f32_dpp v118, v252, v118 row_newbcast:6 row_mask:0xf bank_mask:0xf\n"
    "v_add_f32_dpp v119, v252, v119 row_newbcast:7 row_mask:0xf bank_mask:0xf\n"
    "v_mfma_f32_16x16x32_bf16 v[232:235], a[196:199], a[188:191], v[232:235]\n"
    "v_add_f32_dpp v120, v252, v120 row_newbcast:8 row_mask:0xf bank_mask:0xf\n"
    "v_mfma_f32_16x16x32_bf16 v[232:235], a[192:195], a[156:159], v[232:235]\n"
    "s_waitcnt vmcnt(10)\n"
    "s_addk_i32 s53, 12288\n"
    "v_add_f32_dpp v121, v252, v121 row_newbcast:9 row_mask:0xf bank_mask:0xf\n"
    "v_mfma_f32_16x16x32_bf16 v[236:239], a[200:203], a[156:159], v[236:239]\n"
    "buffer_load_dwordx4 a[192:195], v240, s[20:23], s53 offen offset:0 sc1\n"
    "v_mfma_f32_16x16x32_bf16 v[236:239], a[204:207], a[188:191], v[236:239]\n"
    "buffer_load_dwordx4 a[196:199], v240, s[20:23], s53 offen offset:1024 sc1\n"
    "v_mfma_f32_16x16x32_bf16 v[204:207], a[204:207], a[184:187], v[204:207]\n"
    "ds_read_b128 a[156:159], v241 offset:47104\n"
    "v_add_f32_dpp v122, v252, v122 row_newbcast:10 row_mask:0xf bank_mask:0xf\n"
    "v_mfma_f32_16x16x32_bf16 v[204:207], a[200:203], a[152:155], v[204:207]\n"
    "ds_read_b128 a[188:191], v241 offset:48128\n"
    "v_add_f32_dpp v123, v252, v123 row_newbcast:11 row_mask:0xf bank_mask:0xf\n"
    "v_mfma_f32_16x16x32_bf16 v[172:175], a[200:203], a[148:151], v[172:175]\n"
    "ds_read_b128 a[184:187], v241 offset:46080\n"
    "v_add_f32_dpp v124, v252, v124 row_newbcast:12 row_mask:0xf bank_mask:0xf\n"
    "v_mfma_f32_16x16x32_bf16 v[172:175], a[204:207], a[180:183], v[172:175]\n"
    "ds_read_b128 a[152:155], v241 offset:45056\n"
    "v_add_f32_dpp v125, v252, v125 row_newbcast:13 row_mask:0xf bank_mask:0xf\n"
    "v_mfma_f32_16x16x32_bf16 v[140:143], a[204:207], a[176:179], v[140:143]\n"
    "ds_read_b128 a[148:151], v241 offset:43008\n"
    "v_add_f32_dpp v126, v252, v126 row_newbcast:14 row_mask:0xf bank_mask:0xf\n"
    "v_mfma_f32_16x16x32_bf16 v[140:143], a[200:203], a[144:147], v[140:143]\n"
    "ds_read_b128 a[180:183], v241 offset:44032\n"
    "v_add_f32_dpp v127, v252, v127 row_newbcast:15 row_mask:0xf bank_mask:0xf\n"
    "v_mfma_f32_16x16x32_bf16 v[108:111], a[200:203], a[140:143], v[108:111]\n"
    "ds_read_b128 a[176:179], v241 offset:41984\n"
    "v_add_f32_dpp v144, v252, v144 row_newbcast:0 row_mask:0xf bank_mask:0xf\n"
    "v_mfma_f32_16x16x32_bf16 v[108:111], a[204:207], a[172:175], v[108:111]\n"
    "ds_read_b128 a[144:147], v241 offset:40960\n"
    "v_add_f32_dpp v145, v252, v145 row_newbcast:1 row_mask:0xf bank_mask:0xf\n"
    "v_mfma_f32_16x16x32_bf16 v[76:79], a[204:207], a[168:171], v[76:79]\n"
    "ds_read_b128 a[140:143], v241 offset:38912\n"
    "v_add_f32_dpp v146, v252, v146 row_newbcast:2 row_mask:0xf bank_mask:0xf\n"
    "v_mfma_f32_16x16x32_bf16 v[76:79], a[200:203], a[136:139], v[76:79]\n"
    "ds_read_b128 a[172:175], v241 offset:39936\n"
    "v_add_f32_dpp v147, v252, v147 row_newbcast:3 row_mask:0xf bank_mask:0xf\n"
    "v_mfma_f32_16x16x32_bf16 v[44:47], a[200:203], a[132:135], v[44:47]\n"
    "ds_read_b128 a[168:171], v241 offset:37888\n"
    "v_add_f32_dpp v148, v252, v148 row_newbcast:4 row_mask:0xf bank_mask:0xf\n"
    "v_mfma_f32_16x16x32_bf16 v[44:47], a[204:207], a[164:167], v[44:47]\n"
    "ds_read_b128 a[136:139], v241 offset:36864\n"
    "v_add_f32_dpp v149, v252, v149 row_newbcast:5 row_mask:0xf bank_mask:0xf\n"
    "v_mfma_f32_16x16x32_bf16 v[12:15], a[204:207], a[160:163], v[12:15]\n"
    "ds_read_b128 a[132:135], v241 offset:34816\n"
    "v_add_f32_dpp v150, v252, v150 row_newbcast:6 row_mask:0xf bank_mask:0xf\n"
    "v_mfma_f32_16x16x32_bf16 v[12:15], a[200:203], a[128:131], v[12:15]\n"
    "s_waitcnt vmcnt(9) lgkmcnt(11)\n"
    "ds_read_b128 a[164:167], v241 offset:35840\n"
    "v_add_f32_dpp v151, v252, v151 row_newbcast:7 row_mask:0xf bank_mask:0xf\n"
    "v_mfma_f32_16x16x32_bf16 v[224:227], a[208:211], a[156:159], v[224:227]\n"
    "buffer_load_dwordx4 a[200:203], v240, s[20:23], s53 offen offset:2048 sc1\n"
    "v_mfma_f32_16x16x32_bf16 v[224:227], a[212:215], a[188:191], v[224:227]\n"
    "s_waitcnt lgkmcnt(10)\n"
    "v_add_f32_dpp v152, v252, v152 row_newbcast:8 row_mask:0xf bank_mask:0xf\n"
    "v_add_f32_dpp v153, v252, v153 row_newbcast:9 row_mask:0xf bank_mask:0xf\n"
    "v_mfma_f32_16x16x32_bf16 v[192:195], a[212:215], a[184:187], v[192:195]\n"
    "buffer_load_dwordx4 a[204:207], v240, s[20:23], s53 offen offset:3072 sc1\n"
    "v_mfma_f32_16x16x32_bf16 v[192:195], a[208:211], a[152:155], v[192:195]\n"
    "s_waitcnt lgkmcnt(8)\n"
    "ds_read_b128 a[160:163], v241 offset:33792\n"
    "ds_read_b128 a[128:131], v241 offset:32768\n"
    "v_mfma_f32_16x16x32_bf16 v[160:163], a[208:211], a[148:151], v[160:163]\n"
    "v_add_f32_dpp v154, v252, v154 row_newbcast:10 row_mask:0xf bank_mask:0xf\n"
    "v_add_f32_dpp v155, v252, v155 row_newbcast:11 row_mask:0xf bank_mask:0xf\n"
    "v_mfma_f32_16x16x32_bf16 v[160:163], a[212:215], a[180:183], v[160:163]\n"
    "s_waitcnt lgkmcnt(8)\n"
    "v_add_f32_dpp v156, v252, v156 row_newbcast:12 row_mask:0xf bank_mask:0xf\n"
    "v_add_f32_dpp v157, v252, v157 row_newbcast:13 row_mask:0xf bank_mask:0xf\n"
    "v_mfma_f32_16x16x32_bf16 v[128:131], a[212:215], a[176:179], v[128:131]\n"
    "v_add_f32_dpp v158, v252, v158 row_newbcast:14 row_mask:0xf bank_mask:0xf\n"
    "v_add_f32_dpp v159, v252, v159 row_newbcast:15 row_mask:0xf bank_mask:0xf\n"
    "v_mfma_f32_16x16x32_bf16 v[128:131], a[208:211], a[144:147], v[128:131]\n"
    "s_waitcnt lgkmcnt(6)\n"
    "v_add_f32_dpp v176, v252, v176 row_newbcast:0 row_mask:0xf bank_mask:0xf\n"
    "v_add_f32_dpp v177, v252, v177 row_newbcast:1 row_mask:0xf bank_mask:0xf\n"
    "v_mfma_f32_16x16x32_bf16 v[96:99], a[208:211], a[140:143], v[96:99]\n"
    "v_add_f32_dpp v178, v252, v178 row_newbcast:2 row_mask:0xf bank_mask:0xf\n"
    "v_add_f32_dpp v179, v252, v179 row_newbcast:3 row_mask:0xf bank_mask:0xf\n"
    "v_mfma_f32_16x16x32_bf16 v[96:99], a[212:215], a[172:175], v[96:99]\n"
    "s_waitcnt lgkmcnt(4)\n"
    "v_add_f32_dpp v180, v252, v180 row_newbcast:4 row_mask:0xf bank_mask:0xf\n"
    "v_add_f32_dpp v181, v252, v181 row_newbcast:5 row_mask:0xf bank_mask:0xf\n"
    "v_mfma_f32_16x16x32_bf16 v[64:67], a[212:215], a[168:171], v[64:67]\n"
    "v_add_f32_dpp v182, v252, v182 row_newbcast:6 row_mask:0xf bank_mask:0xf\n"
    "v_add_f32_dpp v183, v252, v183 row_newbcast:7 row_mask:0xf bank_mask:0xf\n"
    "v_mfma_f32_16x16x32_bf16 v[64:67], a[208:211], a[136:139], v[64:67]\n"
    "s_waitcnt lgkmcnt(2)\n"
    "v_add_f32_dpp v184, v252, v184 row_newbcast:8 row_mask:0xf bank_mask:0xf\n"
    "v_add_f32_dpp v185, v252, v185 row_newbcast:9 row_mask:0xf bank_mask:0xf\n"
    "v_mfma_f32_16x16x32_bf16 v[32:35], a[208:211], a[132:135], v[32:35]\n"
    "v_add_f32_dpp v186, v252, v186 row_newbcast:10 row_mask:0xf bank_mask:0xf\n"
    "v_add_f32_dpp v187, v252, v187 row_newbcast:11 row_mask:0xf bank_mask:0xf\n"
    "v_mfma_f32_16x16x32_bf16 v[32:35], a[212:215], a[164:167], v[32:35]\n"
    "s_waitcnt lgkmcnt(0)\n"
    "v_add_f32_dpp v188, v252, v188 row_newbcast:12 row_mask:0xf bank_mask:0xf\n"
    "v_add_f32_dpp v189, v252, v189 row_newbcast:13 row_mask:0xf bank_mask:0xf\n"
    "v_mfma_f32_16x16x32_bf16 v[0:3], a[212:215], a[160:163], v[0:3]\n"
    "v_add_f32_dpp v190, v252, v190 row_newbcast:14 row_mask:0xf bank_mask:0xf\n"
    "v_add_f32_dpp v191, v252, v191 row_newbcast:15 row_mask:0xf bank_mask:0xf\n"
    "v_mfma_f32_16x16x32_bf16 v[0:3], a[208:211], a[128:131], v[0:3]\n"
    "s_waitcnt vmcnt(8)\n"
    "s_addk_i32 s53, 4096\n"
    "v_add_f32_dpp v208, v252, v208 row_newbcast:0 row_mask:0xf bank_mask:0xf\n"
    "v_mfma_f32_16x16x32_bf16 v[4:7], a[216:219], a[128:131], v[4:7]\n"
    "buffer_load_dwordx4 a[208:211], v240, s[20:23], s53 offen offset:0 sc1\n"
    "v_mfma_f32_16x16x32_bf16 v[4:7], a[220:223], a[160:163], v[4:7]\n"
    "buffer_load_dwordx4 a[212:215], v240, s[20:23], s53 offen offset:1024 sc1\n"
    "v_mfma_f32_16x16x32_bf16 v[36:39], a[220:223], a[164:167], v[36:39]\n"
    "v_add_f32_dpp v209, v252, v209 row_newbcast:1 row_mask:0xf bank_mask:0xf\n"
    "v_add_f32_dpp v210, v252, v210 row_newbcast:2 row_mask:0xf bank_mask:0xf\n"
    "v_mfma_f32_16x16x32_bf16 v[36:39], a[216:219], a[132:135], v[36:39]\n"
    "v_add_f32_dpp v211, v252, v211 row_newbcast:3 row_mask:0xf bank_mask:0xf\n"
    "v_add_f32_dpp v212, v252, v212 row_newbcast:4 row_mask:0xf bank_mask:0xf\n"
    "v_mfma_f32_16x16x32_bf16 v[68:71], a[216:219], a[136:139], v[68:71]\n"
    "v_add_f32_dpp v213, v252, v213 row_newbcast:5 row_mask:0xf bank_mask:0xf\n"
    "v_add_f32_dpp v214, v252, v214 row_newbcast:6 row_mask:0xf bank_mask:0xf\n"
    "v_mfma_f32_16x16x32_bf16 v[68:71], a[220:223], a[168:171], v[68:71]\n"
    "v_add_f32_dpp v215, v252, v215 row_newbcast:7 row_mask:0xf bank_mask:0xf\n"
    "v_add_f32_dpp v216, v252, v216 row_newbcast:8 row_mask:0xf bank_mask:0xf\n"
    "v_mfma_f32_16x16x32_bf16 v[100:103], a[220:223], a[172:175], v[100:103]\n"
    "v_add_f32_dpp v217, v252, v217 row_newbcast:9 row_mask:0xf bank_mask:0xf\n"
    "v_add_f32_dpp v218, v252, v218 row_newbcast:10 row_mask:0xf bank_mask:0xf\n"
    "v_mfma_f32_16x16x32_bf16 v[100:103], a[216:219], a[140:143], v[100:103]\n"
    "v_add_f32_dpp v219, v252, v219 row_newbcast:11 row_mask:0xf bank_mask:0xf\n"
    "v_add_f32_dpp v220, v252, v220 row_newbcast:12 row_mask:0xf bank_mask:0xf\n"
    "v_mfma_f32_16x16x32_bf16 v[132:135], a[216:219], a[144:147], v[132:135]\n"
    "v_add_f32_dpp v221, v252, v221 row_newbcast:13 row_mask:0xf bank_mask:0xf\n"
    "v_add_f32_dpp v222, v252, v222 row_newbcast:14 row_mask:0xf bank_mask:0xf\n"
    "v_mfma_f32_16x16x32_bf16 v[132:135], a[220:223], a[176:179], v[132:135]\n"
    "v_add_f32_dpp v223, v252, v223 row_newbcast:15 row_mask:0xf bank_mask:0xf\n"
    "v_accvgpr_read_b32 v244, a240\n"
    "v_mfma_f32_16x16x32_bf16 v[164:167], a[220:223], a[180:183], v[164:167]\n"
    "v_accvgpr_read_b32 v245, a241\n"
    "v_accvgpr_read_b32 v246, a242\n"
    "v_mfma_f32_16x16x32_bf16 v[164:167], a[216:219], a[148:151], v[164:167]\n"
    "v_accvgpr_read_b32 v247, a243\n"
    "v_mfma_f32_16x16x32_bf16 v[196:199], a[216:219], a[152:155], v[196:199]\n"
    "v_add_f32_dpp v244, v252, v244 row_newbcast:0 row_mask:0xf bank_mask:0xf\n"
    "v_mfma_f32_16x16x32_bf16 v[196:199], a[220:223], a[184:187], v[196:199]\n"
    "v_add_f32_dpp v245, v252, v245 row_newbcast:1 row_mask:0xf bank_mask:0xf\n"
    "v_add_f32_dpp v246, v252, v246 row_newbcast:2 row_mask:0xf bank_mask:0xf\n"
    "v_mfma_f32_16x16x32_bf16 v[228:231], a[220:223], a[188:191], v[228:231]\n"
    "v_add_f32_dpp v247, v252, v247 row_newbcast:3 row_mask:0xf bank_mask:0xf\n"
    "v_mfma_f32_16x16x32_bf16 v[228:231], a[216:219], a[156:159], v[228:231]\n"
    "s_waitcnt vmcnt(8)\n"
    "v_accvgpr_write_b32 a240, v244\n"
    "v_mfma_f32_16x16x32_bf16 v[232:235], a[224:227], a[156:159], v[232:235]\n"
    "buffer_load_dwordx4 a[216:219], v240, s[20:23], s53 offen offset:2048 sc1\n"
    "v_mfma_f32_16x16x32_bf16 v[232:235], a[228:231], a[188:191], v[232:235]\n"
    "buffer_load_dwordx4 a[220:223], v240, s[20:23], s53 offen offset:3072 sc1\n"
    "v_mfma_f32_16x16x32_bf16 v[200:203], a[228:231], a[184:187], v[200:203]\n"
    "v_accvgpr_write_b32 a241, v245\n"
    "v_accvgpr_write_b32 a242, v246\n"
    "v_mfma_f32_16x16x32_bf16 v[200:203], a[224:227], a[152:155], v[200:203]\n"
    "v_accvgpr_write_b32 a243, v247\n"
    "v_accvgpr_read_b32 v244, a244\n"
    "v_mfma_f32_16x16x32_bf16 v[168:171], a[224:227], a[148:151], v[168:171]\n"
    "v_accvgpr_read_b32 v245, a245\n"
    "v_accvgpr_read_b32 v246, a246\n"
    "v_mfma_f32_16x16x32_bf16 v[168:171], a[228:231], a[180:183], v[168:171]\n"
    "v_accvgpr_read_b32 v247, a247\n"
    "v_add_f32_dpp v244, v252, v244 row_newbcast:4 row_mask:0xf bank_mask:0xf\n"
    "v_mfma_f32_16x16x32_bf16 v[136:139], a[228:231], a[176:179], v[136:139]\n"
    "v_add_f32_dpp v245, v252, v245 row_newbcast:5 row_mask:0xf bank_mask:0xf\n"
    "v_mfma_f32_16x16x32_bf16 v[136:139], a[224:227], a[144:147], v[136:139]\n"
    "v_add_f32_dpp v246, v252, v246 row_newbcast:6 row_mask:0xf bank_mask:0xf\n"
    "v_add_f32_dpp v247, v252, v247 row_newbcast:7 row_mask:0xf bank_mask:0xf\n"
    "v_mfma_f32_16x16x32_bf16 v[104:107], a[224:227], a[140:143], v[104:107]\n"
    "v_accvgpr_write_b32 a244, v244\n"
    "v_mfma_f32_16x16x32_bf16 v[104:107], a[228:231], a[172:175], v[104:107]\n"
    "v_accvgpr_write_b32 a245, v245\n"
    "v_mfma_f32_16x16x32_bf16 v[72:75], a[228:231], a[168:171], v[72:75]\n"
    "v_accvgpr_write_b32 a246, v246\n"
    "v_accvgpr_write_b32 a247, v247\n"
    "v_mfma_f32_16x16x32_bf16 v[72:75], a[224:227], a[136:139], v[72:75]\n"
    "v_accvgpr_read_b32 v244, a248\n"
    "v_mfma_f32_16x16x32_bf16 v[40:43], a[224:227], a[132:135], v[40:43]\n"
    "v_accvgpr_read_b32 v245, a249\n"
    "v_mfma_f32_16x16x32_bf16 v[40:43], a[228:231], a[164:167], v[40:43]\n"
    "v_accvgpr_read_b32 v246, a250\n"
    "v_accvgpr_read_b32 v247, a251\n"
    "v_mfma_f32_16x16x32_bf16 v[8:11], a[228:231], a[160:163], v[8:11]\n"
    "v_add_f32_dpp v244, v252, v244 row_newbcast:8 row_mask:0xf bank_mask:0xf\n"
    "v_mfma_f32_16x16x32_bf16 v[8:11], a[224:227], a[128:131], v[8:11]\n"
    "s_waitcnt vmcnt(8)\n"
    "s_addk_i32 s53, 12288\n"
    "v_add_f32_dpp v245, v252, v245 row_newbcast:9 row_mask:0xf bank_mask:0xf\n"
    "v_mfma_f32_16x16x32_bf16 v[12:15], a[232:235], a[128:131], v[12:15]\n"
    "buffer_load_dwordx4 a[224:227], v240, s[20:23], s53 offen offset:0 sc1\n"
    "v_mfma_f32_16x16x32_bf16 v[12:15], a[236:239], a[160:163], v[12:15]\n"
    "buffer_load_dwordx4 a[228:231], v240, s[20:23], s53 offen offset:1024 sc1\n"
    "v_mfma_f32_16x16x32_bf16 v[44:47], a[236:239], a[164:167], v[44:47]\n"
    "ds_read_b128 a[128:131], v241 offset:49152\n"
    "v_add_f32_dpp v246, v252, v246 row_newbcast:10 row_mask:0xf bank_mask:0xf\n"
    "v_mfma_f32_16x16x32_bf16 v[44:47], a[232:235], a[132:135], v[44:47]\n"
    "ds_read_b128 a[160:163], v241 offset:50176\n"
    "v_add_f32_dpp v247, v252, v247 row_newbcast:11 row_mask:0xf bank_mask:0xf\n"
    "v_mfma_f32_16x16x32_bf16 v[76:79], a[232:235], a[136:139], v[76:79]\n"
    "ds_read_b128 a[164:167], v241 offset:52224\n"
    "v_accvgpr_write_b32 a248, v244\n"
    "v_mfma_f32_16x16x32_bf16 v[76:79], a[236:239], a[168:171], v[76:79]\n"
    "ds_read_b128 a[132:135], v241 offset:51200\n"
    "v_accvgpr_write_b32 a249, v245\n"
    "v_mfma_f32_16x16x32_bf16 v[108:111], a[236:239], a[172:175], v[108:111]\n"
    "ds_read_b128 a[136:139], v241 offset:53248\n"
    "v_accvgpr_write_b32 a250, v246\n"
    "v_mfma_f32_16x16x32_bf16 v[108:111], a[232:235], a[140:143], v[108:111]\n"
    "ds_read_b128 a[168:171], v241 offset:54272\n"
    "v_accvgpr_write_b32 a251, v247\n"
    "v_mfma_f32_16x16x32_bf16 v[140:143], a[232:235], a[144:147], v[140:143]\n"
    "ds_read_b128 a[172:175], v241 offset:56320\n"
    "v_accvgpr_read_b32 v244, a252\n"
    "v_mfma_f32_16x16x32_bf16 v[140:143], a[236:239], a[176:179], v[140:143]\n"
    "ds_read_b128 a[140:143], v241 offset:55296\n"
    "v_accvgpr_read_b32 v245, a253\n"
    "v_mfma_f32_16x16x32_bf16 v[172:175], a[236:239], a[180:183], v[172:175]\n"
    "ds_read_b128 a[144:147], v241 offset:57344\n"
    "v_accvgpr_read_b32 v246, a254\n"
    "v_mfma_f32_16x16x32_bf16 v[172:175], a[232:235], a[148:151], v[172:175]\n"
    "ds_read_b128 a[176:179], v241 offset:58368\n"
    "v_accvgpr_read_b32 v247, a255\n"
    "v_mfma_f32_16x16x32_bf16 v[204:207], a[232:235], a[152:155], v[204:207]\n"
    "ds_read_b128 a[180:183], v241 offset:60416\n"
    "v_add_f32_dpp v244, v252, v244 row_newbcast:12 row_mask:0xf bank_mask:0xf\n"
    "v_mfma_f32_16x16x32_bf16 v[204:207], a[236:239], a[184:187], v[204:207]\n"
    "ds_read_b128 a[148:151], v241 offset:59392\n"
    "v_add_f32_dpp v245, v252, v245 row_newbcast:13 row_mask:0xf bank_mask:0xf\n"
    "v_mfma_f32_16x16x32_bf16 v[236:239], a[236:239], a[188:191], v[236:239]\n"
    "ds_read_b128 a[152:155], v241 offset:61440\n"
    "v_add_f32_dpp v246, v252, v246 row_newbcast:14 row_mask:0xf bank_mask:0xf\n"
    "v_mfma_f32_16x16x32_bf16 v[236:239], a[232:235], a[156:159], v[236:239]\n"
    "s_waitcnt vmcnt(8) lgkmcnt(11)\n"
    "ds_read_b128 a[184:187], v241 offset:62464\n"
    "v_add_f32_dpp v247, v252, v247 row_newbcast:15 row_mask:0xf bank_mask:0xf\n"
    "v_mfma_f32_16x16x32_bf16 v[0:3], a[192:195], a[128:131], v[0:3]\n"
    "buffer_load_dwordx4 a[232:235], v240, s[20:23], s53 offen offset:2048 sc1\n"
    "v_mfma_f32_16x16x32_bf16 v[0:3], a[196:199], a[160:163], v[0:3]\n"
    "s_waitcnt lgkmcnt(10)\n"
    "v_accvgpr_write_b32 a252, v244\n"
    "v_accvgpr_write_b32 a253, v245\n"
    "v_mfma_f32_16x16x32_bf16 v[32:35], a[196:199], a[164:167], v[32:35]\n"
    "buffer_load_dwordx4 a[236:239], v240, s[20:23], s53 offen offset:3072 sc1\n"
    "v_mfma_f32_16x16x32_bf16 v[32:35], a[192:195], a[132:135], v[32:35]\n"
    "s_waitcnt lgkmcnt(8)\n"
    "ds_read_b128 a[188:191], v241 offset:64512\n"
    "ds_read_b128 a[156:159], v241 offset:63488\n"
    "v_mfma_f32_16x16x32_bf16 v[64:67], a[192:195], a[136:139], v[64:67]\n"
    "v_accvgpr_write_b32 a254, v246\n"
    "v_add_f32_dpp v8, v253, v8 row_newbcast:8 row_mask:0xf bank_mask:0xf\n"
    "v_mfma_f32_16x16x32_bf16 v[64:67], a[196:199], a[168:171], v[64:67]\n"
    "s_waitcnt lgkmcnt(8)\n"
    "v_accvgpr_write_b32 a255, v247\n"
    "v_add_f32_dpp v9, v253, v9 row_newbcast:9 row_mask:0xf bank_mask:0xf\n"
    "v_mfma_f32_16x16x32_bf16 v[96:99], a[196:199], a[172:175], v[96:99]\n"
    "v_add_f32_dpp v10, v253, v10 row_newbcast:10 row_mask:0xf bank_mask:0xf\n"
    "v_add_f32_dpp v11, v253, v11 row_newbcast:11 row_mask:0xf bank_mask:0xf\n"
    "v_mfma_f32_16x16x32_bf16 v[96:99], a[192:195], a[140:143], v[96:99]\n"
    "s_waitcnt lgkmcnt(6)\n"
    "v_add_f32_dpp v40, v253, v40 row_newbcast:8 row_mask:0xf bank_mask:0xf\n"
    "v_add_f32_dpp v41, v253, v41 row_newbcast:9 row_mask:0xf bank_mask:0xf\n"
    "v_mfma_f32_16x16x32_bf16 v[128:131], a[192:195], a[144:147], v[128:131]\n"
    "v_add_f32_dpp v42, v253, v42 row_newbcast:10 row_mask:0xf bank_mask:0xf\n"
    "v_add_f32_dpp v43, v253, v43 row_newbcast:11 row_mask:0xf bank_mask:0xf\n"
    "v_mfma_f32_16x16x32_bf16 v[128:131], a[196:199], a[176:179], v[128:131]\n"
    "s_waitcnt lgkmcnt(4)\n"
    "v_add_f32_dpp v72, v253, v72 row_newbcast:8 row_mask:0xf bank_mask:0xf\n"
    "v_add_f32_dpp v73, v253, v73 row_newbcast:9 row_mask:0xf bank_mask:0xf\n"
    "v_mfma_f32_16x16x32_bf16 v[160:163], a[196:199], a[180:183], v[160:163]\n"
    "v_add_f32_dpp v74, v253, v74 row_newbcast:10 row_mask:0xf bank_mask:0xf\n"
    "v_add_f32_dpp v75, v253, v75 row_newbcast:11 row_mask:0xf bank_mask:0xf\n"
    "v_mfma_f32_16x16x32_bf16 v[160:163], a[192:195], a[148:151], v[160:163]\n"
    "s_waitcnt lgkmcnt(2)\n"
    "v_add_f32_dpp v104, v253, v104 row_newbcast:8 row_mask:0xf bank_mask:0xf\n"
    "v_add_f32_dpp v105, v253, v105 row_newbcast:9 row_mask:0xf bank_mask:0xf\n"
    "v_mfma_f32_16x16x32_bf16 v[192:195], a[192:195], a[152:155], v[192:195]\n"
    "v_add_f32_dpp v106, v253, v106 row_newbcast:10 row_mask:0xf bank_mask:0xf\n"
    "v_add_f32_dpp v107, v253, v107 row_newbcast:11 row_mask:0xf bank_mask:0xf\n"
    "v_mfma_f32_16x16x32_bf16 v[192:195], a[196:199], a[184:187], v[192:195]\n"
    "s_waitcnt lgkmcnt(0)\n"
    "v_add_f32_dpp v12, v253, v12 row_newbcast:12 row_mask:0xf bank_mask:0xf\n"
    "v_add_f32_dpp v13, v253, v13 row_newbcast:13 row_mask:0xf bank_mask:0xf\n"
    "v_mfma_f32_16x16x32_bf16 v[224:227], a[196:199], a[188:191], v[224:227]\n"
    "v_add_f32_dpp v14, v253, v14 row_newbcast:14 row_mask:0xf bank_mask:0xf\n"
    "v_mfma_f32_16x16x32_bf16 v[224:227], a[192:195], a[156:159], v[224:227]\n"
    "s_waitcnt vmcnt(8)\n"
    "s_addk_i32 s53, 4096\n"
    "v_add_f32_dpp v15, v253, v15 row_newbcast:15 row_mask:0xf bank_mask:0xf\n"
    "v_mfma_f32_16x16x32_bf16 v[228:231], a[200:203], a[156:159], v[228:231]\n"
    "buffer_load_dwordx4 a[192:195], v240, s[20:23], s53 offen offset:0 sc1\n"
    "v_mfma_f32_16x16x32_bf16 v[228:231], a[204:207], a[188:191], v[228:231]\n"
    "buffer_load_dwordx4 a[196:199], v240, s[20:23], s53 offen offset:1024 sc1\n"
    "v_mfma_f32_16x16x32_bf16 v[196:199], a[204:207], a[184:187], v[196:199]\n"
    "v_add_f32_dpp v44, v253, v44 row_newbcast:12 row_mask:0xf bank_mask:0xf\n"
    "v_add_f32_dpp v45, v253, v45 row_newbcast:13 row_mask:0xf bank_mask:0xf\n"
    "v_mfma_f32_16x16x32_bf16 v[196:199], a[200:203], a[152:155], v[196:199]\n"
    "v_add_f32_dpp v46, v253, v46 row_newbcast:14 row_mask:0xf bank_mask:0xf\n"
    "v_add_f32_dpp v47, v253, v47 row_newbcast:15 row_mask:0xf bank_mask:0xf\n"
    "v_mfma_f32_16x16x32_bf16 v[164:167], a[200:203], a[148:151], v[164:167]\n"
    "v_add_f32_dpp v76, v253, v76 row_newbcast:12 row_mask:0xf bank_mask:0xf\n"
    "v_mfma_f32_16x16x32_bf16 v[164:167], a[204:207], a[180:183], v[164:167]\n"
    "v_add_f32_dpp v77, v253, v77 row_newbcast:13 row_mask:0xf bank_mask:0xf\n"
    "v_mfma_f32_16x16x32_bf16 v[132:135], a[204:207], a[176:179], v[132:135]\n"
    "v_add_f32_dpp v78, v253, v78 row_newbcast:14 row_mask:0xf bank_mask:0xf\n"
    "v_mfma_f32_16x16x32_bf16 v[132:135], a[200:203], a[144:147], v[132:135]\n"
    "v_add_f32_dpp v79, v253, v79 row_newbcast:15 row_mask:0xf bank_mask:0xf\n"
    "v_mfma_f32_16x16x32_bf16 v[100:103], a[200:203], a[140:143], v[100:103]\n"
    "v_add_f32_dpp v108, v253, v108 row_newbcast:12 row_mask:0xf bank_mask:0xf\n"
    "v_mfma_f32_16x16x32_bf16 v[100:103], a[204:207], a[172:175], v[100:103]\n"
    "v_add_f32_dpp v109, v253, v109 row_newbcast:13 row_mask:0xf bank_mask:0xf\n"
    "v_mfma_f32_16x16x32_bf16 v[68:71], a[204:207], a[168:171], v[68:71]\n"
    "v_add_f32_dpp v110, v253, v110 row_newbcast:14 row_mask:0xf bank_mask:0xf\n"
    "v_mfma_f32_16x16x32_bf16 v[68:71], a[200:203], a[136:139], v[68:71]\n"
    "v_add_f32_dpp v111, v253, v111 row_newbcast:15 row_mask:0xf bank_mask:0xf\n"
    "v_mfma_f32_16x16x32_bf16 v[36:39], a[200:203], a[132:135], v[36:39]\n"
    "v_add_f32_dpp v0, v253, v0 row_newbcast:0 row_mask:0xf bank_mask:0xf\n"
    "v_mfma_f32_16x16x32_bf16 v[36:39], a[204:207], a[164:167], v[36:39]\n"
    "v_add_f32_dpp v1, v253, v1 row_newbcast:1 row_mask:0xf bank_mask:0xf\n"
    "v_mfma_f32_16x16x32_bf16 v[4:7], a[204:207], a[160:163], v[4:7]\n"
    "v_add_f32_dpp v2, v253, v2 row_newbcast:2 row_mask:0xf bank_mask:0xf\n"
    "v_mfma_f32_16x16x32_bf16 v[4:7], a[200:203], a[128:131], v[4:7]\n"
    "s_waitcnt vmcnt(8)\n"
    "v_add_f32_dpp v3, v253, v3 row_newbcast:3 row_mask:0xf bank_mask:0xf\n"
    "v_mfma_f32_16x16x32_bf16 v[8:11], a[208:211], a[128:131], v[8:11]\n"
    "buffer_load_dwordx4 a[200:203], v240, s[20:23], s53 offen offset:2048 sc1\n"
    "v_mfma_f32_16x16x32_bf16 v[8:11], a[212:215], a[160:163], v[8:11]\n"
    "buffer_load_dwordx4 a[204:207], v240, s[20:23], s53 offen offset:3072 sc1\n"
    "v_mfma_f32_16x16x32_bf16 v[40:43], a[212:215], a[164:167], v[40:43]\n"
    "v_add_f32_dpp v32, v253, v32 row_newbcast:0 row_mask:0xf bank_mask:0xf\n"
    "v_add_f32_dpp v33, v253, v33 row_newbcast:1 row_mask:0xf bank_mask:0xf\n"
    "v_mfma_f32_16x16x32_bf16 v[40:43], a[208:211], a[132:135], v[40:43]\n"
    "v_add_f32_dpp v34, v253, v34 row_newbcast:2 row_mask:0xf bank_mask:0xf\n"
    "v_add_f32_dpp v35, v253, v35 row_newbcast:3 row_mask:0xf bank_mask:0xf\n"
    "v_mfma_f32_16x16x32_bf16 v[72:75], a[208:211], a[136:139], v[72:75]\n"
    "v_add_f32_dpp v64, v253, v64 row_newbcast:0 row_mask:0xf bank_mask:0xf\n"
    "v_mfma_f32_16x16x32_bf16 v[72:75], a[212:215], a[168:171], v[72:75]\n"
    "v_add_f32_dpp v65, v253, v65 row_newbcast:1 row_mask:0xf bank_mask:0xf\n"
    "v_mfma_f32_16x16x32_bf16 v[104:107], a[212:215], a[172:175], v[104:107]\n"
    "v_add_f32_dpp v66, v253, v66 row_newbcast:2 row_mask:0xf bank_mask:0xf\n"
    "v_mfma_f32_16x16x32_bf16 v[104:107], a[208:211], a[140:143], v[104:107]\n"
    "v_add_f32_dpp v67, v253, v67 row_newbcast:3 row_mask:0xf bank_mask:0xf\n"
    "v_mfma_f32_16x16x32_bf16 v[136:139], a[208:211], a[144:147], v[136:139]\n"
    "v_add_f32_dpp v96, v253, v96 row_newbcast:0 row_mask:0xf bank_mask:0xf\n"
    "v_mfma_f32_16x16x32_bf16 v[136:139], a[212:215], a[176:179], v[136:139]\n"
    "v_add_f32_dpp v97, v253, v97 row_newbcast:1 row_mask:0xf bank_mask:0xf\n"
    "v_mfma_f32_16x16x32_bf16 v[168:171], a[212:215], a[180:183], v[168:171]\n"
    "v_add_f32_dpp v98, v253, v98 row_newbcast:2 row_mask:0xf bank_mask:0xf\n"
    "v_mfma_f32_16x16x32_bf16 v[168:171], a[208:211], a[148:151], v[168:171]\n"
    "v_add_f32_dpp v99, v253, v99 row_newbcast:3 row_mask:0xf bank_mask:0xf\n"
    "v_mfma_f32_16x16x32_bf16 v[200:203], a[208:211], a[152:155], v[200:203]\n"
    "v_add_f32_dpp v4, v253, v4 row_newbcast:4 row_mask:0xf bank_mask:0xf\n"
    "v_mfma_f32_16x16x32_bf16 v[200:203], a[212:215], a[184:187], v[200:203]\n"
    "s_barrier\n"
    "v_add_f32_dpp v5, v253, v5 row_newbcast:5 row_mask:0xf bank_mask:0xf\n"
    "v_mfma_f32_16x16x32_bf16 v[232:235], a[212:215], a[188:191], v[232:235]\n"
    "v_add_f32_dpp v6, v253, v6 row_newbcast:6 row_mask:0xf bank_mask:0xf\n"
    "v_mfma_f32_16x16x32_bf16 v[232:235], a[208:211], a[156:159], v[232:235]\n"
    "s_waitcnt vmcnt(8)\n"
    "s_addk_i32 s53, 12288\n"
    "v_add_f32_dpp v7, v253, v7 row_newbcast:7 row_mask:0xf bank_mask:0xf\n"
    "v_mfma_f32_16x16x32_bf16 v[236:239], a[216:219], a[156:159], v[236:239]\n"
    "buffer_load_dwordx4 a[208:211], v240, s[20:23], s53 offen offset:0 sc1\n"
    "v_mfma_f32_16x16x32_bf16 v[236:239], a[220:223], a[188:191], v[236:239]\n"
    "buffer_load_dwordx4 a[212:215], v240, s[20:23], s53 offen offset:1024 sc1\n"
    "v_mfma_f32_16x16x32_bf16 v[204:207], a[220:223], a[184:187], v[204:207]\n"
    "v_add_u32 v241, s67, v240\n"
    "ds_read_b128 a[156:159], v241 offset:14336\n"
    "v_mfma_f32_16x16x32_bf16 v[204:207], a[216:219], a[152:155], v[204:207]\n"
    "ds_read_b128 a[188:191], v241 offset:15360\n"
    "v_add_f32_dpp v36, v253, v36 row_newbcast:4 row_mask:0xf bank_mask:0xf\n"
    "v_mfma_f32_16x16x32_bf16 v[172:175], a[216:219], a[148:151], v[172:175]\n"
    "ds_read_b128 a[184:187], v241 offset:13312\n"
    "v_add_f32_dpp v37, v253, v37 row_newbcast:5 row_mask:0xf bank_mask:0xf\n"
    "v_mfma_f32_16x16x32_bf16 v[172:175], a[220:223], a[180:183], v[172:175]\n"
    "ds_read_b128 a[152:155], v241 offset:12288\n"
    "v_add_f32_dpp v38, v253, v38 row_newbcast:6 row_mask:0xf bank_mask:0xf\n"
    "v_mfma_f32_16x16x32_bf16 v[140:143], a[220:223], a[176:179], v[140:143]\n"
    "ds_read_b128 a[148:151], v241 offset:10240\n"
    "v_add_f32_dpp v39, v253, v39 row_newbcast:7 row_mask:0xf bank_mask:0xf\n"
    "v_mfma_f32_16x16x32_bf16 v[140:143], a[216:219], a[144:147], v[140:143]\n"
    "ds_read_b128 a[180:183], v241 offset:11264\n"
    "v_add_f32_dpp v68, v253, v68 row_newbcast:4 row_mask:0xf bank_mask:0xf\n"
    "v_mfma_f32_16x16x32_bf16 v[108:111], a[216:219], a[140:143], v[108:111]\n"
    "ds_read_b128 a[176:179], v241 offset:9216\n"
    "v_add_f32_dpp v69, v253, v69 row_newbcast:5 row_mask:0xf bank_mask:0xf\n"
    "v_mfma_f32_16x16x32_bf16 v[108:111], a[220:223], a[172:175], v[108:111]\n"
    "ds_read_b128 a[144:147], v241 offset:8192\n"
    "v_add_f32_dpp v70, v253, v70 row_newbcast:6 row_mask:0xf bank_mask:0xf\n"
    "v_mfma_f32_16x16x32_bf16 v[76:79], a[220:223], a[168:171], v[76:79]\n"
    "ds_read_b128 a[140:143], v241 offset:6144\n"
    "v_add_f32_dpp v71, v253, v71 row_newbcast:7 row_mask:0xf bank_mask:0xf\n"
    "v_mfma_f32_16x16x32_bf16 v[76:79], a[216:219], a[136:139], v[76:79]\n"
    "ds_read_b128 a[172:175], v241 offset:7168\n"
    "v_add_f32_dpp v100, v253, v100 row_newbcast:4 row_mask:0xf bank_mask:0xf\n"
    "v_mfma_f32_16x16x32_bf16 v[44:47], a[216:219], a[132:135], v[44:47]\n"
    "ds_read_b128 a[168:171], v241 offset:5120\n"
    "v_add_f32_dpp v101, v253, v101 row_newbcast:5 row_mask:0xf bank_mask:0xf\n"
    "v_mfma_f32_16x16x32_bf16 v[44:47], a[220:223], a[164:167], v[44:47]\n"
    "ds_read_b128 a[136:139], v241 offset:4096\n"
    "v_add_f32_dpp v102, v253, v102 row_newbcast:6 row_mask:0xf bank_mask:0xf\n"
    "v_mfma_f32_16x16x32_bf16 v[12:15], a[220:223], a[160:163], v[12:15]\n"
    "ds_read_b128 a[132:135], v241 offset:2048\n"
    "v_add_f32_dpp v103, v253, v103 row_newbcast:7 row_mask:0xf bank_mask:0xf\n"
    "v_mfma_f32_16x16x32_bf16 v[12:15], a[216:219], a[128:131], v[12:15]\n"
    "s_waitcnt vmcnt(8) lgkmcnt(11)\n"
    "ds_read_b128 a[164:167], v241 offset:3072\n"
    "v_add_f32_dpp v136, v253, v136 row_newbcast:8 row_mask:0xf bank_mask:0xf\n"
    "v_mfma_f32_16x16x32_bf16 v[224:227], a[224:227], a[156:159], v[224:227]\n"
    "buffer_load_dwordx4 a[216:219], v240, s[20:23], s53 offen offset:2048 sc1\n"
    "v_mfma_f32_16x16x32_bf16 v[224:227], a[228:231], a[188:191], v[224:227]\n"
    "s_waitcnt lgkmcnt(10)\n"
    "v_add_f32_dpp v137, v253, v137 row_newbcast:9 row_mask:0xf bank_mask:0xf\n"
    "v_add_f32_dpp v138, v253, v138 row_newbcast:10 row_mask:0xf bank_mask:0xf\n"
    "v_mfma_f32_16x16x32_bf16 v[192:195], a[228:231], a[184:187], v[192:195]\n"
    "buffer_load_dwordx4 a[220:223], v240, s[20:23], s53 offen offset:3072 sc1\n"
    "v_mfma_f32_16x16x32_bf16 v[192:195], a[224:227], a[152:155], v[192:195]\n"
    "s_waitcnt lgkmcnt(8)\n"
    "ds_read_b128 a[160:163], v241 offset:1024\n"
    "ds_read_b128 a[128:131], v241 offset:0\n"
    "v_mfma_f32_16x16x32_bf16 v[160:163], a[224:227], a[148:151], v[160:163]\n"
    "v_add_f32_dpp v139, v253, v139 row_newbcast:11 row_mask:0xf bank_mask:0xf\n"
    "v_add_f32_dpp v168, v253, v168 row_newbcast:8 row_mask:0xf bank_mask:0xf\n"
    "v_mfma_f32_16x16x32_bf16 v[160:163], a[228:231], a[180:183], v[160:163]\n"
    "s_waitcnt lgkmcnt(8)\n"
    "v_add_f32_dpp v169, v253, v169 row_newbcast:9 row_mask:0xf bank_mask:0xf\n"
    "v_add_f32_dpp v170, v253, v170 row_newbcast:10 row_mask:0xf bank_mask:0xf\n"
    "v_mfma_f32_16x16x32_bf16 v[128:131], a[228:231], a[176:179], v[128:131]\n"
    "v_add_f32_dpp v171, v253, v171 row_newbcast:11 row_mask:0xf bank_mask:0xf\n"
    "v_add_f32_dpp v200, v253, v200 row_newbcast:8 row_mask:0xf bank_mask:0xf\n"
    "v_mfma_f32_16x16x32_bf16 v[128:131], a[224:227], a[144:147], v[128:131]\n"
    "s_waitcnt lgkmcnt(6)\n"
    "v_add_f32_dpp v201, v253, v201 row_newbcast:9 row_mask:0xf bank_mask:0xf\n"
    "v_add_f32_dpp v202, v253, v202 row_newbcast:10 row_mask:0xf bank_mask:0xf\n"
    "v_mfma_f32_16x16x32_bf16 v[96:99], a[224:227], a[140:143], v[96:99]\n"
    "v_add_f32_dpp v203, v253, v203 row_newbcast:11 row_mask:0xf bank_mask:0xf\n"
    "v_add_f32_dpp v232, v253, v232 row_newbcast:8 row_mask:0xf bank_mask:0xf\n"
    "v_mfma_f32_16x16x32_bf16 v[96:99], a[228:231], a[172:175], v[96:99]\n"
    "s_waitcnt lgkmcnt(4)\n"
    "v_add_f32_dpp v233, v253, v233 row_newbcast:9 row_mask:0xf bank_mask:0xf\n"
    "v_mfma_f32_16x16x32_bf16 v[64:67], a[228:231], a[168:171], v[64:67]\n"
    "v_add_f32_dpp v234, v253, v234 row_newbcast:10 row_mask:0xf bank_mask:0xf\n"
    "v_mfma_f32_16x16x32_bf16 v[64:67], a[224:227], a[136:139], v[64:67]\n"
    "s_waitcnt lgkmcnt(2)\n"
    "v_add_f32_dpp v235, v253, v235 row_newbcast:11 row_mask:0xf bank_mask:0xf\n"
    "v_mfma_f32_16x16x32_bf16 v[32:35], a[224:227], a[132:135], v[32:35]\n"
    "v_add_f32_dpp v140, v253, v140 row_newbcast:12 row_mask:0xf bank_mask:0xf\n"
    "v_mfma_f32_16x16x32_bf16 v[32:35], a[228:231], a[164:167], v[32:35]\n"
    "s_waitcnt lgkmcnt(0)\n"
    "v_add_f32_dpp v141, v253, v141 row_newbcast:13 row_mask:0xf bank_mask:0xf\n"
    "v_mfma_f32_16x16x32_bf16 v[0:3], a[228:231], a[160:163], v[0:3]\n"
    "v_add_f32_dpp v142, v253, v142 row_newbcast:14 row_mask:0xf bank_mask:0xf\n"
    "v_mfma_f32_16x16x32_bf16 v[0:3], a[224:227], a[128:131], v[0:3]\n"
    "s_waitcnt vmcnt(8)\n"
    "s_addk_i32 s53, 4096\n"
    "v_add_f32_dpp v143, v253, v143 row_newbcast:15 row_mask:0xf bank_mask:0xf\n"
    "v_mfma_f32_16x16x32_bf16 v[4:7], a[232:235], a[128:131], v[4:7]\n"
    "buffer_load_dwordx4 a[224:227], v240, s[20:23], s53 offen offset:0 sc1\n"
    "v_mfma_f32_16x16x32_bf16 v[4:7], a[236:239], a[160:163], v[4:7]\n"
    "buffer_load_dwordx4 a[228:231], v240, s[20:23], s53 offen offset:1024 sc1\n"
    "v_mfma_f32_16x16x32_bf16 v[36:39], a[236:239], a[164:167], v[36:39]\n"
    "v_add_f32_dpp v172, v253, v172 row_newbcast:12 row_mask:0xf bank_mask:0xf\n"
    "v_add_f32_dpp v173, v253, v173 row_newbcast:13 row_mask:0xf bank_mask:0xf\n"
    "v_mfma_f32_16x16x32_bf16 v[36:39], a[232:235], a[132:135], v[36:39]\n"
    "v_add_f32_dpp v174, v253, v174 row_newbcast:14 row_mask:0xf bank_mask:0xf\n"
    "v_add_f32_dpp v175, v253, v175 row_newbcast:15 row_mask:0xf bank_mask:0xf\n"
    "v_mfma_f32_16x16x32_bf16 v[68:71], a[232:235], a[136:139], v[68:71]\n"
    "v_add_f32_dpp v204, v253, v204 row_newbcast:12 row_mask:0xf bank_mask:0xf\n"
    "v_mfma_f32_16x16x32_bf16 v[68:71], a[236:239], a[168:171], v[68:71]\n"
    "v_add_f32_dpp v205, v253, v205 row_newbcast:13 row_mask:0xf bank_mask:0xf\n"
    "v_mfma_f32_16x16x32_bf16 v[100:103], a[236:239], a[172:175], v[100:103]\n"
    "v_add_f32_dpp v206, v253, v206 row_newbcast:14 row_mask:0xf bank_mask:0xf\n"
    "v_mfma_f32_16x16x32_bf16 v[100:103], a[232:235], a[140:143], v[100:103]\n"
    "v_add_f32_dpp v207, v253, v207 row_newbcast:15 row_mask:0xf bank_mask:0xf\n"
    "v_mfma_f32_16x16x32_bf16 v[132:135], a[232:235], a[144:147], v[132:135]\n"
    "v_add_f32_dpp v236, v253, v236 row_newbcast:12 row_mask:0xf bank_mask:0xf\n"
    "v_mfma_f32_16x16x32_bf16 v[132:135], a[236:239], a[176:179], v[132:135]\n"
    "v_add_f32_dpp v237, v253, v237 row_newbcast:13 row_mask:0xf bank_mask:0xf\n"
    "v_mfma_f32_16x16x32_bf16 v[164:167], a[236:239], a[180:183], v[164:167]\n"
    "v_add_f32_dpp v238, v253, v238 row_newbcast:14 row_mask:0xf bank_mask:0xf\n"
    "v_mfma_f32_16x16x32_bf16 v[164:167], a[232:235], a[148:151], v[164:167]\n"
    "v_add_f32_dpp v239, v253, v239 row_newbcast:15 row_mask:0xf bank_mask:0xf\n"
    "v_mfma_f32_16x16x32_bf16 v[196:199], a[232:235], a[152:155], v[196:199]\n"
    "v_add_f32_dpp v128, v253, v128 row_newbcast:0 row_mask:0xf bank_mask:0xf\n"
    "v_mfma_f32_16x16x32_bf16 v[196:199], a[236:239], a[184:187], v[196:199]\n"
    "v_add_f32_dpp v129, v253, v129 row_newbcast:1 row_mask:0xf bank_mask:0xf\n"
    "v_mfma_f32_16x16x32_bf16 v[228:231], a[236:239], a[188:191], v[228:231]\n"
    "v_add_f32_dpp v130, v253, v130 row_newbcast:2 row_mask:0xf bank_mask:0xf\n"
    "v_mfma_f32_16x16x32_bf16 v[228:231], a[232:235], a[156:159], v[228:231]\n"
    "s_waitcnt vmcnt(8)\n"
    "v_add_f32_dpp v131, v253, v131 row_newbcast:3 row_mask:0xf bank_mask:0xf\n"
    "v_mfma_f32_16x16x32_bf16 v[232:235], a[192:195], a[156:159], v[232:235]\n"
    "buffer_load_dwordx4 a[232:235], v240, s[20:23], s53 offen offset:2048 sc1\n"
    "v_mfma_f32_16x16x32_bf16 v[232:235], a[196:199], a[188:191], v[232:235]\n"
    "buffer_load_dwordx4 a[236:239], v240, s[20:23], s53 offen offset:3072 sc1\n"
    "v_mfma_f32_16x16x32_bf16 v[200:203], a[196:199], a[184:187], v[200:203]\n"
    "v_add_f32_dpp v160, v253, v160 row_newbcast:0 row_mask:0xf bank_mask:0xf\n"
    "v_add_f32_dpp v161, v253, v161 row_newbcast:1 row_mask:0xf bank_mask:0xf\n"
    "v_mfma_f32_16x16x32_bf16 v[200:203], a[192:195], a[152:155], v[200:203]\n"
    "v_add_f32_dpp v162, v253, v162 row_newbcast:2 row_mask:0xf bank_mask:0xf\n"
    "v_add_f32_dpp v163, v253, v163 row_newbcast:3 row_mask:0xf bank_mask:0xf\n"
    "v_mfma_f32_16x16x32_bf16 v[168:171], a[192:195], a[148:151], v[168:171]\n"
    "v_add_f32_dpp v192, v253, v192 row_newbcast:0 row_mask:0xf bank_mask:0xf\n"
    "v_mfma_f32_16x16x32_bf16 v[168:171], a[196:199], a[180:183], v[168:171]\n"
    "v_add_f32_dpp v193, v253, v193 row_newbcast:1 row_mask:0xf bank_mask:0xf\n"
    "v_mfma_f32_16x16x32_bf16 v[136:139], a[196:199], a[176:179], v[136:139]\n"
    "v_add_f32_dpp v194, v253, v194 row_newbcast:2 row_mask:0xf bank_mask:0xf\n"
    "v_mfma_f32_16x16x32_bf16 v[136:139], a[192:195], a[144:147], v[136:139]\n"
    "v_add_f32_dpp v195, v253, v195 row_newbcast:3 row_mask:0xf bank_mask:0xf\n"
    "v_mfma_f32_16x16x32_bf16 v[104:107], a[192:195], a[140:143], v[104:107]\n"
    "v_add_f32_dpp v224, v253, v224 row_newbcast:0 row_mask:0xf bank_mask:0xf\n"
    "v_mfma_f32_16x16x32_bf16 v[104:107], a[196:199], a[172:175], v[104:107]\n"
    "v_add_f32_dpp v225, v253, v225 row_newbcast:1 row_mask:0xf bank_mask:0xf\n"
    "v_mfma_f32_16x16x32_bf16 v[72:75], a[196:199], a[168:171], v[72:75]\n"
    "v_add_f32_dpp v226, v253, v226 row_newbcast:2 row_mask:0xf bank_mask:0xf\n"
    "v_mfma_f32_16x16x32_bf16 v[72:75], a[192:195], a[136:139], v[72:75]\n"
    "v_add_f32_dpp v227, v253, v227 row_newbcast:3 row_mask:0xf bank_mask:0xf\n"
    "v_mfma_f32_16x16x32_bf16 v[40:43], a[192:195], a[132:135], v[40:43]\n"
    "v_add_f32_dpp v132, v253, v132 row_newbcast:4 row_mask:0xf bank_mask:0xf\n"
    "v_mfma_f32_16x16x32_bf16 v[40:43], a[196:199], a[164:167], v[40:43]\n"
    "v_add_f32_dpp v133, v253, v133 row_newbcast:5 row_mask:0xf bank_mask:0xf\n"
    "v_mfma_f32_16x16x32_bf16 v[8:11], a[196:199], a[160:163], v[8:11]\n"
    "v_add_f32_dpp v134, v253, v134 row_newbcast:6 row_mask:0xf bank_mask:0xf\n"
    "v_mfma_f32_16x16x32_bf16 v[8:11], a[192:195], a[128:131], v[8:11]\n"
    "s_waitcnt vmcnt(8)\n"
    "s_addk_i32 s53, 4096\n"
    "v_add_f32_dpp v135, v253, v135 row_newbcast:7 row_mask:0xf bank_mask:0xf\n"
    "v_mfma_f32_16x16x32_bf16 v[12:15], a[200:203], a[128:131], v[12:15]\n"
    "buffer_load_dwordx4 a[192:195], v240, s[20:23], s53 offen offset:0 sc1\n"
    "v_mfma_f32_16x16x32_bf16 v[12:15], a[204:207], a[160:163], v[12:15]\n"
    "buffer_load_dwordx4 a[196:199], v240, s[20:23], s53 offen offset:1024 sc1\n"
    "v_mfma_f32_16x16x32_bf16 v[44:47], a[204:207], a[164:167], v[44:47]\n"
    "ds_read_b128 a[128:131], v241 offset:16384\n"
    "v_add_f32_dpp v164, v253, v164 row_newbcast:4 row_mask:0xf bank_mask:0xf\n"
    "v_mfma_f32_16x16x32_bf16 v[44:47], a[200:203], a[132:135], v[44:47]\n"
    "ds_read_b128 a[160:163], v241 offset:17408\n"
    "v_add_f32_dpp v165, v253, v165 row_newbcast:5 row_mask:0xf bank_mask:0xf\n"
    "v_mfma_f32_16x16x32_bf16 v[76:79], a[200:203], a[136:139], v[76:79]\n"
    "ds_read_b128 a[164:167], v241 offset:19456\n"
    "v_add_f32_dpp v166, v253, v166 row_newbcast:6 row_mask:0xf bank_mask:0xf\n"
    "v_mfma_f32_16x16x32_bf16 v[76:79], a[204:207], a[168:171], v[76:79]\n"
    "ds_read_b128 a[132:135], v241 offset:18432\n"
    "v_add_f32_dpp v167, v253, v167 row_newbcast:7 row_mask:0xf bank_mask:0xf\n"
    "v_mfma_f32_16x16x32_bf16 v[108:111], a[204:207], a[172:175], v[108:111]\n"
    "ds_read_b128 a[136:139], v241 offset:20480\n"
    "v_add_f32_dpp v196, v253, v196 row_newbcast:4 row_mask:0xf bank_mask:0xf\n"
    "v_mfma_f32_16x16x32_bf16 v[108:111], a[200:203], a[140:143], v[108:111]\n"
    "ds_read_b128 a[168:171], v241 offset:21504\n"
    "v_add_f32_dpp v197, v253, v197 row_newbcast:5 row_mask:0xf bank_mask:0xf\n"
    "v_mfma_f32_16x16x32_bf16 v[140:143], a[200:203], a[144:147], v[140:143]\n"
    "ds_read_b128 a[172:175], v241 offset:23552\n"
    "v_add_f32_dpp v198, v253, v198 row_newbcast:6 row_mask:0xf bank_mask:0xf\n"
    "v_mfma_f32_16x16x32_bf16 v[140:143], a[204:207], a[176:179], v[140:143]\n"
    "ds_read_b128 a[140:143], v241 offset:22528\n"
    "v_add_f32_dpp v199, v253, v199 row_newbcast:7 row_mask:0xf bank_mask:0xf\n"
    "v_mfma_f32_16x16x32_bf16 v[172:175], a[204:207], a[180:183], v[172:175]\n"
    "ds_read_b128 a[144:147], v241 offset:24576\n"
    "v_add_f32_dpp v228, v253, v228 row_newbcast:4 row_mask:0xf bank_mask:0xf\n"
    "v_mfma_f32_16x16x32_bf16 v[172:175], a[200:203], a[148:151], v[172:175]\n"
    "ds_read_b128 a[176:179], v241 offset:25600\n"
    "v_add_f32_dpp v229, v253, v229 row_newbcast:5 row_mask:0xf bank_mask:0xf\n"
    "v_mfma_f32_16x16x32_bf16 v[204:207], a[200:203], a[152:155], v[204:207]\n"
    "ds_read_b128 a[180:183], v241 offset:27648\n"
    "v_add_f32_dpp v230, v253, v230 row_newbcast:6 row_mask:0xf bank_mask:0xf\n"
    "v_mfma_f32_16x16x32_bf16 v[204:207], a[204:207], a[184:187], v[204:207]\n"
    "ds_read_b128 a[148:151], v241 offset:26624\n"
    "v_add_f32_dpp v231, v253, v231 row_newbcast:7 row_mask:0xf bank_mask:0xf\n"
    "v_mfma_f32_16x16x32_bf16 v[236:239], a[204:207], a[188:191], v[236:239]\n"
    "ds_read_b128 a[152:155], v241 offset:28672\n"
    "v_mfma_f32_16x16x32_bf16 v[236:239], a[200:203], a[156:159], v[236:239]\n"
    "s_waitcnt vmcnt(8) lgkmcnt(11)\n"
    "ds_read_b128 a[184:187], v241 offset:29696\n"
    "v_mfma_f32_16x16x32_bf16 v[0:3], a[208:211], a[128:131], v[0:3]\n"
    "buffer_load_dwordx4 a[200:203], v240, s[20:23], s53 offen offset:2048 sc1\n"
    "v_mfma_f32_16x16x32_bf16 v[0:3], a[212:215], a[160:163], v[0:3]\n"
    "s_waitcnt lgkmcnt(10)\n"
    "v_mfma_f32_16x16x32_bf16 v[32:35], a[212:215], a[164:167], v[32:35]\n"
    "buffer_load_dwordx4 a[204:207], v240, s[20:23], s53 offen offset:3072 sc1\n"
    "v_mfma_f32_16x16x32_bf16 v[32:35], a[208:211], a[132:135], v[32:35]\n"
    "s_waitcnt lgkmcnt(8)\n"
    "ds_read_b128 a[188:191], v241 offset:31744\n"
    "ds_read_b128 a[156:159], v241 offset:30720\n"
    "v_mfma_f32_16x16x32_bf16 v[64:67], a[208:211], a[136:139], v[64:67]\n"
    "v_mfma_f32_16x16x32_bf16 v[64:67], a[212:215], a[168:171], v[64:67]\n"
    "s_waitcnt lgkmcnt(8)\n"
    "v_mfma_f32_16x16x32_bf16 v[96:99], a[212:215], a[172:175], v[96:99]\n"
    "v_mfma_f32_16x16x32_bf16 v[96:99], a[208:211], a[140:143], v[96:99]\n"
    "s_waitcnt lgkmcnt(6)\n"
    "v_mfma_f32_16x16x32_bf16 v[128:131], a[208:211], a[144:147], v[128:131]\n"
    "v_mfma_f32_16x16x32_bf16 v[128:131], a[212:215], a[176:179], v[128:131]\n"
    "s_waitcnt lgkmcnt(4)\n"
    "v_mfma_f32_16x16x32_bf16 v[160:163], a[212:215], a[180:183], v[160:163]\n"
    "v_mfma_f32_16x16x32_bf16 v[160:163], a[208:211], a[148:151], v[160:163]\n"
    "s_waitcnt lgkmcnt(2)\n"
    "v_mfma_f32_16x16x32_bf16 v[192:195], a[208:211], a[152:155], v[192:195]\n"
    "v_mfma_f32_16x16x32_bf16 v[192:195], a[212:215], a[184:187], v[192:195]\n"
    "s_waitcnt lgkmcnt(0)\n"
    "v_mfma_f32_16x16x32_bf16 v[224:227], a[212:215], a[188:191], v[224:227]\n"
    "v_mfma_f32_16x16x32_bf16 v[224:227], a[208:211], a[156:159], v[224:227]\n"
    "s_waitcnt vmcnt(8)\n"
    "s_addk_i32 s53, 4096\n"
    "v_mfma_f32_16x16x32_bf16 v[228:231], a[216:219], a[156:159], v[228:231]\n"
    "buffer_load_dwordx4 a[208:211], v240, s[20:23], s53 offen offset:0 sc1\n"
    "v_mfma_f32_16x16x32_bf16 v[228:231], a[220:223], a[188:191], v[228:231]\n"
    "buffer_load_dwordx4 a[212:215], v240, s[20:23], s53 offen offset:1024 sc1\n"
    "v_mfma_f32_16x16x32_bf16 v[196:199], a[220:223], a[184:187], v[196:199]\n"
    "v_accvgpr_read_b32 v245, a28\n"
    "v_accvgpr_read_b32 v247, a29\n"
    "v_mfma_f32_16x16x32_bf16 v[196:199], a[216:219], a[152:155], v[196:199]\n"
    "v_lshlrev_b32 v244, 16, v245\n"
    "v_lshlrev_b32 v246, 16, v247\n"
    "v_mfma_f32_16x16x32_bf16 v[164:167], a[216:219], a[148:151], v[164:167]\n"
    "v_and_b32 v245, s58, v245\n"
    "v_and_b32 v247, s58, v247\n"
    "v_mfma_f32_16x16x32_bf16 v[164:167], a[220:223], a[180:183], v[164:167]\n"
    "v_mul_f32 v244, v244, v224\n"
    "v_mul_f32 v246, v246, v226\n"
    "v_mfma_f32_16x16x32_bf16 v[132:135], a[220:223], a[176:179], v[132:135]\n"
    "v_mul_f32 v245, v245, v225\n"
    "v_mul_f32 v247, v247, v227\n"
    "v_mfma_f32_16x16x32_bf16 v[132:135], a[216:219], a[144:147], v[132:135]\n"
    "v_cvt_pk_bf16_f32 v224, v244, v245\n"
    "v_cvt_pk_bf16_f32 v225, v246, v247\n"
    "v_mfma_f32_16x16x32_bf16 v[100:103], a[216:219], a[140:143], v[100:103]\n"
    "v_accvgpr_read_b32 v245, a30\n"
    "v_accvgpr_read_b32 v247, a31\n"
    "v_mfma_f32_16x16x32_bf16 v[100:103], a[220:223], a[172:175], v[100:103]\n"
    "v_lshlrev_b32 v244, 16, v245\n"
    "v_lshlrev_b32 v246, 16, v247\n"
    "v_mfma_f32_16x16x32_bf16 v[68:71], a[220:223], a[168:171], v[68:71]\n"
    "v_and_b32 v245, s58, v245\n"
    "v_and_b32 v247, s58, v247\n"
    "v_mfma_f32_16x16x32_bf16 v[68:71], a[216:219], a[136:139], v[68:71]\n"
    "v_mul_f32 v244, v244, v228\n"
    "v_mul_f32 v246, v246, v230\n"
    "v_mfma_f32_16x16x32_bf16 v[36:39], a[216:219], a[132:135], v[36:39]\n"
    "v_mul_f32 v245, v245, v229\n"
    "v_mul_f32 v247, v247, v231\n"
    "v_mfma_f32_16x16x32_bf16 v[36:39], a[220:223], a[164:167], v[36:39]\n"
    "v_cvt_pk_bf16_f32 v226, v244, v245\n"
    "v_cvt_pk_bf16_f32 v227, v246, v247\n"
    "v_mfma_f32_16x16x32_bf16 v[4:7], a[220:223], a[160:163], v[4:7]\n"
    "ds_write_b128 v248, v[224:227] offset:2048\n"
    "v_mfma_f32_16x16x32_bf16 v[4:7], a[216:219], a[128:131], v[4:7]\n"
    "s_waitcnt vmcnt(8)\n"
    "v_accvgpr_read_b32 v245, a24\n"
    "v_accvgpr_read_b32 v247, a25\n"
    "v_mfma_f32_16x16x32_bf16 v[8:11], a[224:227], a[128:131], v[8:11]\n"
    "buffer_load_dwordx4 a[216:219], v240, s[20:23], s53 offen offset:2048 sc1\n"
    "v_mfma_f32_16x16x32_bf16 v[8:11], a[228:231], a[160:163], v[8:11]\n"
    "buffer_load_dwordx4 a[220:223], v240, s[20:23], s53 offen offset:3072 sc1\n"
    "v_mfma_f32_16x16x32_bf16 v[40:43], a[228:231], a[164:167], v[40:43]\n"
    "v_lshlrev_b32 v244, 16, v245\n"
    "v_lshlrev_b32 v246, 16, v247\n"
    "v_mfma_f32_16x16x32_bf16 v[40:43], a[224:227], a[132:135], v[40:43]\n"
    "v_and_b32 v245, s58, v245\n"
    "v_and_b32 v247, s58, v247\n"
    "v_mfma_f32_16x16x32_bf16 v[72:75], a[224:227], a[136:139], v[72:75]\n"
    "v_mul_f32 v244, v244, v192\n"
    "v_mul_f32 v246, v246, v194\n"
    "v_mfma_f32_16x16x32_bf16 v[72:75], a[228:231], a[168:171], v[72:75]\n"
    "v_mul_f32 v245, v245, v193\n"
    "v_mul_f32 v247, v247, v195\n"
    "v_mfma_f32_16x16x32_bf16 v[104:107], a[228:231], a[172:175], v[104:107]\n"
    "v_cvt_pk_bf16_f32 v192, v244, v245\n"
    "v_cvt_pk_bf16_f32 v193, v246, v247\n"
    "v_mfma_f32_16x16x32_bf16 v[104:107], a[224:227], a[140:143], v[104:107]\n"
    "v_accvgpr_read_b32 v245, a26\n"
    "v_accvgpr_read_b32 v247, a27\n"
    "v_mfma_f32_16x16x32_bf16 v[136:139], a[224:227], a[144:147], v[136:139]\n"
    "buffer_load_dwordx4 a[28:31], v240, s[20:23], s91 offen offset:0 sc1\n"
    "v_mfma_f32_16x16x32_bf16 v[136:139], a[228:231], a[176:179], v[136:139]\n"
    "v_lshlrev_b32 v244, 16, v245\n"
    "v_lshlrev_b32 v246, 16, v247\n"
    "v_mfma_f32_16x16x32_bf16 v[168:171], a[228:231], a[180:183], v[168:171]\n"
    "v_and_b32 v245, s58, v245\n"
    "v_and_b32 v247, s58, v247\n"
    "v_mfma_f32_16x16x32_bf16 v[168:171], a[224:227], a[148:151], v[168:171]\n"
    "v_mul_f32 v244, v244, v196\n"
    "v_mul_f32 v246, v246, v198\n"
    "v_mfma_f32_16x16x32_bf16 v[200:203], a[224:227], a[152:155], v[200:203]\n"
    "v_mul_f32 v245, v245, v197\n"
    "v_mul_f32 v247, v247, v199\n"
    "v_mfma_f32_16x16x32_bf16 v[200:203], a[228:231], a[184:187], v[200:203]\n"
    "v_cvt_pk_bf16_f32 v194, v244, v245\n"
    "v_cvt_pk_bf16_f32 v195, v246, v247\n"
    "v_mfma_f32_16x16x32_bf16 v[232:235], a[228:231], a[188:191], v[232:235]\n"
    "ds_write_b128 v248, v[192:195] offset:0\n"
    "v_mfma_f32_16x16x32_bf16 v[232:235], a[224:227], a[156:159], v[232:235]\n"
    "s_waitcnt vmcnt(9)\n"
    "s_addk_i32 s53, -20480\n"
    "v_mfma_f32_16x16x32_bf16 v[236:239], a[232:235], a[156:159], v[236:239]\n"
    "buffer_load_dwordx4 a[224:227], v240, s[20:23], s53 offen offset:0 sc1\n"
    "v_mfma_f32_16x16x32_bf16 v[236:239], a[236:239], a[188:191], v[236:239]\n"
    "buffer_load_dwordx4 a[228:231], v240, s[20:23], s53 offen offset:1024 sc1\n"
    "v_mfma_f32_16x16x32_bf16 v[204:207], a[236:239], a[184:187], v[204:207]\n"
    "v_accvgpr_read_b32 v245, a60\n"
    "v_accvgpr_read_b32 v247, a61\n"
    "v_mfma_f32_16x16x32_bf16 v[204:207], a[232:235], a[152:155], v[204:207]\n"
    "v_lshlrev_b32 v244, 16, v245\n"
    "v_lshlrev_b32 v246, 16, v247\n"
    "v_mfma_f32_16x16x32_bf16 v[172:175], a[232:235], a[148:151], v[172:175]\n"
    "v_and_b32 v245, s58, v245\n"
    "v_and_b32 v247, s58, v247\n"
    "v_mfma_f32_16x16x32_bf16 v[172:175], a[236:239], a[180:183], v[172:175]\n"
    "v_mul_f32 v244, v244, v232\n"
    "v_mul_f32 v246, v246, v234\n"
    "v_mfma_f32_16x16x32_bf16 v[140:143], a[236:239], a[176:179], v[140:143]\n"
    "v_mul_f32 v245, v245, v233\n"
    "v_mul_f32 v247, v247, v235\n"
    "v_mfma_f32_16x16x32_bf16 v[140:143], a[232:235], a[144:147], v[140:143]\n"
    "v_cvt_pk_bf16_f32 v232, v244, v245\n"
    "v_cvt_pk_bf16_f32 v233, v246, v247\n"
    "v_mfma_f32_16x16x32_bf16 v[108:111], a[232:235], a[140:143], v[108:111]\n"
    "buffer_load_dwordx4 v[224:227], v240, s[20:23], s91 offen offset:1024 sc1\n"
    "v_mfma_f32_16x16x32_bf16 v[108:111], a[236:239], a[172:175], v[108:111]\n"
    "v_accvgpr_read_b32 v245, a62\n"
    "v_accvgpr_read_b32 v247, a63\n"
    "v_mfma_f32_16x16x32_bf16 v[76:79], a[236:239], a[168:171], v[76:79]\n"
    "v_lshlrev_b32 v244, 16, v245\n"
    "v_lshlrev_b32 v246, 16, v247\n"
    "v_mfma_f32_16x16x32_bf16 v[76:79], a[232:235], a[136:139], v[76:79]\n"
    "v_and_b32 v245, s58, v245\n"
    "v_and_b32 v247, s58, v247\n"
    "v_mfma_f32_16x16x32_bf16 v[44:47], a[232:235], a[132:135], v[44:47]\n"
    "v_mul_f32 v244, v244, v236\n"
    "v_mul_f32 v246, v246, v238\n"
    "v_mfma_f32_16x16x32_bf16 v[44:47], a[236:239], a[164:167], v[44:47]\n"
    "v_mul_f32 v245, v245, v237\n"
    "v_mul_f32 v247, v247, v239\n"
    "v_mfma_f32_16x16x32_bf16 v[12:15], a[236:239], a[160:163], v[12:15]\n"
    "v_cvt_pk_bf16_f32 v234, v244, v245\n"
    "v_cvt_pk_bf16_f32 v235, v246, v247\n"
    "v_mfma_f32_16x16x32_bf16 v[12:15], a[232:235], a[128:131], v[12:15]\n"
    "s_waitcnt vmcnt(10)\n"
    "v_mfma_f32_16x16x32_bf16 v[16:19], a[192:195], a[128:131], v[16:19]\n"
    "ds_write_b128 v249, v[232:235] offset:2048\n"
    "v_mfma_f32_16x16x32_bf16 v[16:19], a[196:199], a[160:163], v[16:19]\n"
    "buffer_load_dwordx4 a[232:235], v240, s[20:23], s53 offen offset:2048 sc1\n"
    "v_mfma_f32_16x16x32_bf16 v[48:51], a[196:199], a[164:167], v[48:51]\n"
    "buffer_load_dwordx4 a[236:239], v240, s[20:23], s53 offen offset:3072 sc1\n"
    "v_mfma_f32_16x16x32_bf16 v[48:51], a[192:195], a[132:135], v[48:51]\n"
    "ds_read_b128 v[228:231], v242 offset:2048\n"
    "ds_read_b128 v[236:239], v242 offset:3072\n"
    "v_mfma_f32_16x16x32_bf16 v[80:83], a[192:195], a[136:139], v[80:83]\n"
    "v_accvgpr_read_b32 v245, a56\n"
    "v_accvgpr_read_b32 v247, a57\n"
    "v_mfma_f32_16x16x32_bf16 v[80:83], a[196:199], a[168:171], v[80:83]\n"
    "v_lshlrev_b32 v244, 16, v245\n"
    "v_lshlrev_b32 v246, 16, v247\n"
    "v_mfma_f32_16x16x32_bf16 v[112:115], a[196:199], a[172:175], v[112:115]\n"
    "v_and_b32 v245, s58, v245\n"
    "v_and_b32 v247, s58, v247\n"
    "v_mfma_f32_16x16x32_bf16 v[112:115], a[192:195], a[140:143], v[112:115]\n"
    "s_add_u32 s63, s70, 229376\n"
    "s_waitcnt lgkmcnt(1)\n"
    "v_mul_f32 v244, v244, v200\n"
    "v_mfma_f32_16x16x32_bf16 v[144:147], a[192:195], a[144:147], v[144:147]\n"
    "buffer_store_dwordx4 v[228:231], v243, s[40:43], s63 offen nt\n"
    "buffer_load_dwordx4 a[24:27], v240, s[20:23], s91 offen offset:2048 sc1\n"
    "v_mfma_f32_16x16x32_bf16 v[144:147], a[196:199], a[176:179], v[144:147]\n"
    "v_mul_f32 v246, v246, v202\n"
    "v_mul_f32 v245, v245, v201\n"
    "v_mfma_f32_16x16x32_bf16 v[176:179], a[196:199], a[180:183], v[176:179]\n"
    "v_mul_f32 v247, v247, v203\n"
    "v_cvt_pk_bf16_f32 v200, v244, v245\n"
    "v_mfma_f32_16x16x32_bf16 v[176:179], a[192:195], a[148:151], v[176:179]\n"
    "v_cvt_pk_bf16_f32 v201, v246, v247\n"
    "v_accvgpr_read_b32 v245, a58\n"
    "v_mfma_f32_16x16x32_bf16 v[208:211], a[192:195], a[152:155], v[208:211]\n"
    "v_accvgpr_read_b32 v247, a59\n"
    "v_lshlrev_b32 v244, 16, v245\n"
    "v_mfma_f32_16x16x32_bf16 v[208:211], a[196:199], a[184:187], v[208:211]\n"
    "v_lshlrev_b32 v246, 16, v247\n"
    "v_and_b32 v245, s58, v245\n"
    "v_mfma_f32_16x16x32_bf16 a[240:243], a[196:199], a[188:191], a[240:243]\n"
    "v_and_b32 v247, s58, v247\n"
    "v_mul_f32 v244, v244, v204\n"
    "v_mfma_f32_16x16x32_bf16 a[240:243], a[192:195], a[156:159], a[240:243]\n"
    "s_waitcnt vmcnt(12)\n"
    "s_addk_i32 s53, 4096\n"
    "v_mul_f32 v246, v246, v206\n"
    "v_mfma_f32_16x16x32_bf16 a[244:247], a[200:203], a[156:159], a[244:247]\n"
    "buffer_load_dwordx4 a[192:195], v240, s[20:23], s53 offen offset:0 sc1\n"
    "v_mfma_f32_16x16x32_bf16 a[244:247], a[204:207], a[188:191], a[244:247]\n"
    "buffer_load_dwordx4 a[196:199], v240, s[20:23], s53 offen offset:1024 sc1\n"
    "v_mfma_f32_16x16x32_bf16 v[212:215], a[204:207], a[184:187], v[212:215]\n"
    "v_mul_f32 v245, v245, v205\n"
    "v_mul_f32 v247, v247, v207\n"
    "v_mfma_f32_16x16x32_bf16 v[212:215], a[200:203], a[152:155], v[212:215]\n"
    "s_add_u32 s63, s70, 245760\n"
    "s_waitcnt lgkmcnt(0)\n"
    "v_cvt_pk_bf16_f32 v202, v244, v245\n"
    "v_mfma_f32_16x16x32_bf16 v[180:183], a[200:203], a[148:151], v[180:183]\n"
    "buffer_store_dwordx4 v[236:239], v243, s[40:43], s63 offen nt\n"
    "v_cvt_pk_bf16_f32 v203, v246, v247\n"
    "v_mfma_f32_16x16x32_bf16 v[180:183], a[204:207], a[180:183], v[180:183]\n"
    "ds_write_b128 v249, v[200:203] offset:0\n"
    "v_mfma_f32_16x16x32_bf16 v[148:151], a[204:207], a[176:179], v[148:151]\n"
    "ds_read_b128 v[196:199], v242 offset:0\n"
    "ds_read_b128 v[204:207], v242 offset:1024\n"
    "v_mfma_f32_16x16x32_bf16 v[148:151], a[200:203], a[144:147], v[148:151]\n"
    "v_accvgpr_read_b32 v245, a20\n"
    "v_accvgpr_read_b32 v247, a21\n"
    "v_mfma_f32_16x16x32_bf16 v[116:119], a[200:203], a[140:143], v[116:119]\n"
    "buffer_load_dwordx4 v[192:195], v240, s[20:23], s91 offen offset:3072 sc1\n"
    "v_mfma_f32_16x16x32_bf16 v[116:119], a[204:207], a[172:175], v[116:119]\n"
    "s_addk_i32 s91, 4096\n"
    "v_lshlrev_b32 v244, 16, v245\n"
    "v_lshlrev_b32 v246, 16, v247\n"
    "v_mfma_f32_16x16x32_bf16 v[84:87], a[204:207], a[168:171], v[84:87]\n"
    "v_and_b32 v245, s58, v245\n"
    "v_and_b32 v247, s58, v247\n"
    "v_mfma_f32_16x16x32_bf16 v[84:87], a[200:203], a[136:139], v[84:87]\n"
    "v_mul_f32 v244, v244, v160\n"
    "v_mul_f32 v246, v246, v162\n"
    "v_mfma_f32_16x16x32_bf16 v[52:55], a[200:203], a[132:135], v[52:55]\n"
    "v_mul_f32 v245, v245, v161\n"
    "v_mul_f32 v247, v247, v163\n"
    "v_mfma_f32_16x16x32_bf16 v[52:55], a[204:207], a[164:167], v[52:55]\n"
    "v_cvt_pk_bf16_f32 v160, v244, v245\n"
    "v_cvt_pk_bf16_f32 v161, v246, v247\n"
    "v_mfma_f32_16x16x32_bf16 v[20:23], a[204:207], a[160:163], v[20:23]\n"
    "v_accvgpr_read_b32 v245, a22\n"
    "v_accvgpr_read_b32 v247, a23\n"
    "v_mfma_f32_16x16x32_bf16 v[20:23], a[200:203], a[128:131], v[20:23]\n"
    "s_waitcnt vmcnt(14)\n"
    "v_lshlrev_b32 v244, 16, v245\n"
    "v_lshlrev_b32 v246, 16, v247\n"
    "v_mfma_f32_16x16x32_bf16 v[24:27], a[208:211], a[128:131], v[24:27]\n"
    "buffer_load_dwordx4 a[200:203], v240, s[20:23], s53 offen offset:2048 sc1\n"
    "v_mfma_f32_16x16x32_bf16 v[24:27], a[212:215], a[160:163], v[24:27]\n"
    "buffer_load_dwordx4 a[204:207], v240, s[20:23], s53 offen offset:3072 sc1\n"
    "v_mfma_f32_16x16x32_bf16 v[56:59], a[212:215], a[164:167], v[56:59]\n"
    "v_and_b32 v245, s58, v245\n"
    "v_and_b32 v247, s58, v247\n"
    "v_mfma_f32_16x16x32_bf16 v[56:59], a[208:211], a[132:135], v[56:59]\n"
    "s_add_u32 s63, s70, 196608\n"
    "s_waitcnt lgkmcnt(1)\n"
    "v_mul_f32 v244, v244, v164\n"
    "v_mfma_f32_16x16x32_bf16 v[88:91], a[208:211], a[136:139], v[88:91]\n"
    "buffer_store_dwordx4 v[196:199], v243, s[40:43], s63 offen nt\n"
    "v_mul_f32 v246, v246, v166\n"
    "v_mul_f32 v245, v245, v165\n"
    "v_mul_f32 v247, v247, v167\n"
    "v_mfma_f32_16x16x32_bf16 v[88:91], a[212:215], a[168:171], v[88:91]\n"
    "v_cvt_pk_bf16_f32 v162, v244, v245\n"
    "v_cvt_pk_bf16_f32 v163, v246, v247\n"
    "v_mfma_f32_16x16x32_bf16 v[120:123], a[212:215], a[172:175], v[120:123]\n"
    "ds_write_b128 v248, v[160:163] offset:2048\n"
    "v_mfma_f32_16x16x32_bf16 v[120:123], a[208:211], a[140:143], v[120:123]\n"
    "v_accvgpr_read_b32 v245, a52\n"
    "v_accvgpr_read_b32 v247, a53\n"
    "v_mfma_f32_16x16x32_bf16 v[152:155], a[208:211], a[144:147], v[152:155]\n"
    "buffer_load_dwordx4 a[60:63], v240, s[20:23], s91 offen offset:0 sc1\n"
    "v_mfma_f32_16x16x32_bf16 v[152:155], a[212:215], a[176:179], v[152:155]\n"
    "v_lshlrev_b32 v244, 16, v245\n"
    "v_lshlrev_b32 v246, 16, v247\n"
    "v_mfma_f32_16x16x32_bf16 v[184:187], a[212:215], a[180:183], v[184:187]\n"
    "v_and_b32 v245, s58, v245\n"
    "v_and_b32 v247, s58, v247\n"
    "v_mfma_f32_16x16x32_bf16 v[184:187], a[208:211], a[148:151], v[184:187]\n"
    "v_mul_f32 v244, v244, v168\n"
    "v_mul_f32 v246, v246, v170\n"
    "v_mfma_f32_16x16x32_bf16 v[216:219], a[208:211], a[152:155], v[216:219]\n"
    "v_mul_f32 v245, v245, v169\n"
    "v_mul_f32 v247, v247, v171\n"
    "v_mfma_f32_16x16x32_bf16 v[216:219], a[212:215], a[184:187], v[216:219]\n"
    "v_cvt_pk_bf16_f32 v168, v244, v245\n"
    "v_cvt_pk_bf16_f32 v169, v246, v247\n"
    "v_mfma_f32_16x16x32_bf16 a[248:251], a[212:215], a[188:191], a[248:251]\n"
    "v_accvgpr_read_b32 v245, a54\n"
    "v_accvgpr_read_b32 v247, a55\n"
    "v_mfma_f32_16x16x32_bf16 a[248:251], a[208:211], a[156:159], a[248:251]\n"
    "s_waitcnt vmcnt(16)\n"
    "s_addk_i32 s53, -20480\n"
    "v_lshlrev_b32 v244, 16, v245\n"
    "v_mfma_f32_16x16x32_bf16 a[252:255], a[216:219], a[156:159], a[252:255]\n"
    "buffer_load_dwordx4 a[208:211], v240, s[20:23], s53 offen offset:0 sc1\n"
    "v_mfma_f32_16x16x32_bf16 a[252:255], a[220:223], a[188:191], a[252:255]\n"
    "buffer_load_dwordx4 a[212:215], v240, s[20:23], s53 offen offset:1024 sc1\n"
    "v_mfma_f32_16x16x32_bf16 v[220:223], a[220:223], a[184:187], v[220:223]\n"
    "ds_read_b128 a[156:159], v241 offset:14336\n"
    "v_lshlrev_b32 v246, 16, v247\n"
    "v_mfma_f32_16x16x32_bf16 v[220:223], a[216:219], a[152:155], v[220:223]\n"
    "s_add_u32 s63, s70, 212992\n"
    "s_waitcnt lgkmcnt(2)\n"
    "v_and_b32 v245, s58, v245\n"
    "v_mfma_f32_16x16x32_bf16 v[188:191], a[216:219], a[148:151], v[188:191]\n"
    "buffer_store_dwordx4 v[204:207], v243, s[40:43], s63 offen nt\n"
    "ds_read_b128 a[188:191], v241 offset:15360\n"
    "ds_read_b128 a[184:187], v241 offset:13312\n"
    "v_and_b32 v247, s58, v247\n"
    "v_mfma_f32_16x16x32_bf16 v[188:191], a[220:223], a[180:183], v[188:191]\n"
    "ds_read_b128 a[152:155], v241 offset:12288\n"
    "v_mul_f32 v244, v244, v172\n"
    "v_mfma_f32_16x16x32_bf16 v[156:159], a[220:223], a[176:179], v[156:159]\n"
    "ds_read_b128 a[148:151], v241 offset:10240\n"
    "v_mul_f32 v246, v246, v174\n"
    "v_mfma_f32_16x16x32_bf16 v[156:159], a[216:219], a[144:147], v[156:159]\n"
    "ds_read_b128 a[180:183], v241 offset:11264\n"
    "v_mul_f32 v245, v245, v173\n"
    "v_mfma_f32_16x16x32_bf16 v[124:127], a[216:219], a[140:143], v[124:127]\n"
    "buffer_load_dwordx4 v[232:235], v240, s[20:23], s91 offen offset:1024 sc1\n"
    "v_mfma_f32_16x16x32_bf16 v[124:127], a[220:223], a[172:175], v[124:127]\n"
    "ds_read_b128 a[176:179], v241 offset:9216\n"
    "ds_read_b128 a[144:147], v241 offset:8192\n"
    "v_mfma_f32_16x16x32_bf16 v[92:95], a[220:223], a[168:171], v[92:95]\n"
    "ds_read_b128 a[140:143], v241 offset:6144\n"
    "v_mul_f32 v247, v247, v175\n"
    "v_mfma_f32_16x16x32_bf16 v[92:95], a[216:219], a[136:139], v[92:95]\n"
    "ds_read_b128 a[172:175], v241 offset:7168\n"
    "v_cvt_pk_bf16_f32 v170, v244, v245\n"
    "v_mfma_f32_16x16x32_bf16 v[60:63], a[216:219], a[132:135], v[60:63]\n"
    "ds_read_b128 a[168:171], v241 offset:5120\n"
    "v_cvt_pk_bf16_f32 v171, v246, v247\n"
    "v_mfma_f32_16x16x32_bf16 v[60:63], a[220:223], a[164:167], v[60:63]\n"
    "ds_write_b128 v249, v[168:171] offset:2048\n"
    "v_mfma_f32_16x16x32_bf16 v[28:31], a[220:223], a[160:163], v[28:31]\n"
    "ds_read_b128 a[136:139], v241 offset:4096\n"
    "ds_read_b128 v[164:167], v242 offset:2048\n"
    "v_mfma_f32_16x16x32_bf16 v[28:31], a[216:219], a[128:131], v[28:31]\n"
    "s_waitcnt vmcnt(17) lgkmcnt(12)\n"
    "ds_read_b128 v[172:175], v242 offset:3072\n"
    "ds_read_b128 a[132:135], v241 offset:2048\n"
    "v_mfma_f32_16x16x32_bf16 a[240:243], a[224:227], a[156:159], a[240:243]\n"
    "ds_read_b128 a[164:167], v241 offset:3072\n"
    "v_accvgpr_read_b32 v245, a16\n"
    "v_mfma_f32_16x16x32_bf16 a[240:243], a[228:231], a[188:191], a[240:243]\n"
    "s_waitcnt lgkmcnt(13)\n"
    "v_accvgpr_read_b32 v247, a17\n"
    "v_lshlrev_b32 v244, 16, v245\n"
    "v_mfma_f32_16x16x32_bf16 v[208:211], a[228:231], a[184:187], v[208:211]\n"
    "buffer_load_dwordx4 a[216:219], v240, s[20:23], s53 offen offset:2048 sc1\n"
    "v_mfma_f32_16x16x32_bf16 v[208:211], a[224:227], a[152:155], v[208:211]\n"
    "s_waitcnt lgkmcnt(11)\n"
    "v_lshlrev_b32 v246, 16, v247\n"
    "v_and_b32 v245, s58, v245\n"
    "v_mfma_f32_16x16x32_bf16 v[176:179], a[224:227], a[148:151], v[176:179]\n"
    "buffer_load_dwordx4 a[220:223], v240, s[20:23], s53 offen offset:3072 sc1\n"
    "ds_read_b128 a[160:163], v241 offset:1024\n"
    "ds_read_b128 a[128:131], v241 offset:0\n"
    "v_mfma_f32_16x16x32_bf16 v[176:179], a[228:231], a[180:183], v[176:179]\n"
    "v_and_b32 v247, s58, v247\n"
    "s_add_u32 s63, s70, 163840\n"
    "s_waitcnt lgkmcnt(5)\n"
    "v_mfma_f32_16x16x32_bf16 v[144:147], a[228:231], a[176:179], v[144:147]\n"
    "buffer_store_dwordx4 v[164:167], v243, s[40:43], s63 offen nt\n"
    "v_mul_f32 v244, v244, v128\n"
    "v_mul_f32 v246, v246, v130\n"
    "v_mul_f32 v245, v245, v129\n"
    "v_mfma_f32_16x16x32_bf16 v[144:147], a[224:227], a[144:147], v[144:147]\n"
    "v_mul_f32 v247, v247, v131\n"
    "v_cvt_pk_bf16_f32 v128, v244, v245\n"
    "v_mfma_f32_16x16x32_bf16 v[112:115], a[224:227], a[140:143], v[112:115]\n"
    "buffer_load_dwordx4 v[228:231], v240, s[20:23], s91 offen offset:2048 sc1\n"
    "v_mfma_f32_16x16x32_bf16 v[112:115], a[228:231], a[172:175], v[112:115]\n"
    "v_cvt_pk_bf16_f32 v129, v246, v247\n"
    "v_accvgpr_read_b32 v245, a18\n"
    "v_mfma_f32_16x16x32_bf16 v[80:83], a[228:231], a[168:171], v[80:83]\n"
    "v_accvgpr_read_b32 v247, a19\n"
    "v_lshlrev_b32 v244, 16, v245\n"
    "v_mfma_f32_16x16x32_bf16 v[80:83], a[224:227], a[136:139], v[80:83]\n"
    "s_waitcnt lgkmcnt(2)\n"
    "v_lshlrev_b32 v246, 16, v247\n"
    "v_and_b32 v245, s58, v245\n"
    "v_mfma_f32_16x16x32_bf16 v[48:51], a[224:227], a[132:135], v[48:51]\n"
    "v_and_b32 v247, s58, v247\n"
    "v_mul_f32 v244, v244, v132\n"
    "v_mfma_f32_16x16x32_bf16 v[48:51], a[228:231], a[164:167], v[48:51]\n"
    "s_waitcnt lgkmcnt(0)\n"
    "v_mul_f32 v246, v246, v134\n"
    "v_mul_f32 v245, v245, v133\n"
    "v_mfma_f32_16x16x32_bf16 v[16:19], a[228:231], a[160:163], v[16:19]\n"
    "v_mul_f32 v247, v247, v135\n"
    "v_cvt_pk_bf16_f32 v130, v244, v245\n"
    "v_mfma_f32_16x16x32_bf16 v[16:19], a[224:227], a[128:131], v[16:19]\n"
    "s_waitcnt vmcnt(18)\n"
    "s_addk_i32 s53, 4096\n"
    "v_cvt_pk_bf16_f32 v131, v246, v247\n"
    "v_mfma_f32_16x16x32_bf16 v[20:23], a[232:235], a[128:131], v[20:23]\n"
    "buffer_load_dwordx4 a[224:227], v240, s[20:23], s53 offen offset:0 sc1\n"
    "v_mfma_f32_16x16x32_bf16 v[20:23], a[236:239], a[160:163], v[20:23]\n"
    "buffer_load_dwordx4 a[228:231], v240, s[20:23], s53 offen offset:1024 sc1\n"
    "v_mfma_f32_16x16x32_bf16 v[52:55], a[236:239], a[164:167], v[52:55]\n"
    "ds_write_b128 v248, v[128:131] offset:0\n"
    "v_mfma_f32_16x16x32_bf16 v[52:55], a[232:235], a[132:135], v[52:55]\n"
    "s_add_u32 s63, s70, 180224\n"
    "v_accvgpr_read_b32 v245, a48\n"
    "v_mfma_f32_16x16x32_bf16 v[84:87], a[232:235], a[136:139], v[84:87]\n"
    "buffer_store_dwordx4 v[172:175], v243, s[40:43], s63 offen nt\n"
    "v_accvgpr_read_b32 v247, a49\n"
    "v_lshlrev_b32 v244, 16, v245\n"
    "v_lshlrev_b32 v246, 16, v247\n"
    "v_mfma_f32_16x16x32_bf16 v[84:87], a[236:239], a[168:171], v[84:87]\n"
    "v_and_b32 v245, s58, v245\n"
    "v_and_b32 v247, s58, v247\n"
    "v_mfma_f32_16x16x32_bf16 v[116:119], a[236:239], a[172:175], v[116:119]\n"
    "v_mul_f32 v244, v244, v136\n"
    "v_mul_f32 v246, v246, v138\n"
    "v_mfma_f32_16x16x32_bf16 v[116:119], a[232:235], a[140:143], v[116:119]\n"
    "v_mul_f32 v245, v245, v137\n"
    "v_mul_f32 v247, v247, v139\n"
    "v_mfma_f32_16x16x32_bf16 v[148:151], a[232:235], a[144:147], v[148:151]\n"
    "buffer_load_dwordx4 v[236:239], v240, s[20:23], s91 offen offset:3072 sc1\n"
    "v_mfma_f32_16x16x32_bf16 v[148:151], a[236:239], a[176:179], v[148:151]\n"
    "s_addk_i32 s91, 4096\n"
    "v_cvt_pk_bf16_f32 v136, v244, v245\n"
    "v_cvt_pk_bf16_f32 v137, v246, v247\n"
    "v_mfma_f32_16x16x32_bf16 v[180:183], a[236:239], a[180:183], v[180:183]\n"
    "v_accvgpr_read_b32 v245, a50\n"
    "v_accvgpr_read_b32 v247, a51\n"
    "v_mfma_f32_16x16x32_bf16 v[180:183], a[232:235], a[148:151], v[180:183]\n"
    "v_lshlrev_b32 v244, 16, v245\n"
    "v_lshlrev_b32 v246, 16, v247\n"
    "v_mfma_f32_16x16x32_bf16 v[212:215], a[232:235], a[152:155], v[212:215]\n"
    "v_and_b32 v245, s58, v245\n"
    "v_and_b32 v247, s58, v247\n"
    "v_mfma_f32_16x16x32_bf16 v[212:215], a[236:239], a[184:187], v[212:215]\n"
    "v_mul_f32 v244, v244, v140\n"
    "v_mul_f32 v246, v246, v142\n"
    "v_mfma_f32_16x16x32_bf16 a[244:247], a[236:239], a[188:191], a[244:247]\n"
    "v_mul_f32 v245, v245, v141\n"
    "v_mul_f32 v247, v247, v143\n"
    "v_mfma_f32_16x16x32_bf16 a[244:247], a[232:235], a[156:159], a[244:247]\n"
    "s_waitcnt vmcnt(18)\n"
    "v_cvt_pk_bf16_f32 v138, v244, v245\n"
    "v_cvt_pk_bf16_f32 v139, v246, v247\n"
    "v_mfma_f32_16x16x32_bf16 a[248:251], a[192:195], a[156:159], a[248:251]\n"
    "buffer_load_dwordx4 a[232:235], v240, s[20:23], s53 offen offset:2048 sc1\n"
    "v_mfma_f32_16x16x32_bf16 a[248:251], a[196:199], a[188:191], a[248:251]\n"
    "buffer_load_dwordx4 a[236:239], v240, s[20:23], s53 offen offset:3072 sc1\n"
    "v_mfma_f32_16x16x32_bf16 v[216:219], a[196:199], a[184:187], v[216:219]\n"
    "ds_write_b128 v249, v[136:139] offset:0\n"
    "v_mfma_f32_16x16x32_bf16 v[216:219], a[192:195], a[152:155], v[216:219]\n"
    "ds_read_b128 v[132:135], v242 offset:0\n"
    "ds_read_b128 v[140:143], v242 offset:1024\n"
    "v_mfma_f32_16x16x32_bf16 v[184:187], a[192:195], a[148:151], v[184:187]\n"
    "v_accvgpr_read_b32 v245, a12\n"
    "v_accvgpr_read_b32 v247, a13\n"
    "v_mfma_f32_16x16x32_bf16 v[184:187], a[196:199], a[180:183], v[184:187]\n"
    "v_lshlrev_b32 v244, 16, v245\n"
    "v_lshlrev_b32 v246, 16, v247\n"
    "v_mfma_f32_16x16x32_bf16 v[152:155], a[196:199], a[176:179], v[152:155]\n"
    "v_and_b32 v245, s58, v245\n"
    "v_and_b32 v247, s58, v247\n"
    "v_mfma_f32_16x16x32_bf16 v[152:155], a[192:195], a[144:147], v[152:155]\n"
    "v_mul_f32 v244, v244, v96\n"
    "v_mul_f32 v246, v246, v98\n"
    "v_mfma_f32_16x16x32_bf16 v[120:123], a[192:195], a[140:143], v[120:123]\n"
    "buffer_load_dwordx4 a[56:59], v240, s[20:23], s91 offen offset:0 sc1\n"
    "v_mfma_f32_16x16x32_bf16 v[120:123], a[196:199], a[172:175], v[120:123]\n"
    "v_mul_f32 v245, v245, v97\n"
    "v_mul_f32 v247, v247, v99\n"
    "v_mfma_f32_16x16x32_bf16 v[88:91], a[196:199], a[168:171], v[88:91]\n"
    "v_cvt_pk_bf16_f32 v96, v244, v245\n"
    "v_cvt_pk_bf16_f32 v97, v246, v247\n"
    "v_mfma_f32_16x16x32_bf16 v[88:91], a[192:195], a[136:139], v[88:91]\n"
    "v_accvgpr_read_b32 v245, a14\n"
    "v_accvgpr_read_b32 v247, a15\n"
    "v_mfma_f32_16x16x32_bf16 v[56:59], a[192:195], a[132:135], v[56:59]\n"
    "v_lshlrev_b32 v244, 16, v245\n"
    "v_lshlrev_b32 v246, 16, v247\n"
    "v_mfma_f32_16x16x32_bf16 v[56:59], a[196:199], a[164:167], v[56:59]\n"
    "v_and_b32 v245, s58, v245\n"
    "v_and_b32 v247, s58, v247\n"
    "v_mfma_f32_16x16x32_bf16 v[24:27], a[196:199], a[160:163], v[24:27]\n"
    "v_mul_f32 v244, v244, v100\n"
    "v_mul_f32 v246, v246, v102\n"
    "v_mfma_f32_16x16x32_bf16 v[24:27], a[192:195], a[128:131], v[24:27]\n"
    "s_waitcnt vmcnt(17)\n"
    "s_addk_i32 s53, -20480\n"
    "v_mul_f32 v245, v245, v101\n"
    "v_mfma_f32_16x16x32_bf16 v[28:31], a[200:203], a[128:131], v[28:31]\n"
    "buffer_load_dwordx4 a[192:195], v240, s[20:23], s53 offen offset:0 sc1\n"
    "v_mfma_f32_16x16x32_bf16 v[28:31], a[204:207], a[160:163], v[28:31]\n"
    "buffer_load_dwordx4 a[196:199], v240, s[20:23], s53 offen offset:1024 sc1\n"
    "v_mfma_f32_16x16x32_bf16 v[60:63], a[204:207], a[164:167], v[60:63]\n"
    "v_add_u32 v241, s66, v240\n"
    "ds_read_b128 a[128:131], v241 offset:49152\n"
    "v_mfma_f32_16x16x32_bf16 v[60:63], a[200:203], a[132:135], v[60:63]\n"
    "s_add_u32 s63, s70, 131072\n"
    "s_waitcnt lgkmcnt(2)\n"
    "v_mul_f32 v247, v247, v103\n"
    "v_mfma_f32_16x16x32_bf16 v[92:95], a[200:203], a[136:139], v[92:95]\n"
    "buffer_store_dwordx4 v[132:135], v243, s[40:43], s63 offen nt\n"
    "ds_read_b128 a[160:163], v241 offset:50176\n"
    "ds_read_b128 a[164:167], v241 offset:52224\n"
    "v_cvt_pk_bf16_f32 v98, v244, v245\n"
    "v_mfma_f32_16x16x32_bf16 v[92:95], a[204:207], a[168:171], v[92:95]\n"
    "ds_read_b128 a[132:135], v241 offset:51200\n"
    "v_cvt_pk_bf16_f32 v99, v246, v247\n"
    "v_mfma_f32_16x16x32_bf16 v[124:127], a[204:207], a[172:175], v[124:127]\n"
    "ds_write_b128 v248, v[96:99] offset:2048\n"
    "v_mfma_f32_16x16x32_bf16 v[124:127], a[200:203], a[140:143], v[124:127]\n"
    "ds_read_b128 a[136:139], v241 offset:53248\n"
    "ds_read_b128 a[168:171], v241 offset:54272\n"
    "v_mfma_f32_16x16x32_bf16 v[156:159], a[200:203], a[144:147], v[156:159]\n"
    "buffer_load_dwordx4 v[200:203], v240, s[20:23], s91 offen offset:1024 sc1\n"
    "v_mfma_f32_16x16x32_bf16 v[156:159], a[204:207], a[176:179], v[156:159]\n"
    "ds_read_b128 a[172:175], v241 offset:56320\n"
    "ds_read_b128 a[140:143], v241 offset:55296\n"
    "v_mfma_f32_16x16x32_bf16 v[188:191], a[204:207], a[180:183], v[188:191]\n"
    "ds_read_b128 a[144:147], v241 offset:57344\n"
    "v_accvgpr_read_b32 v245, a44\n"
    "v_mfma_f32_16x16x32_bf16 v[188:191], a[200:203], a[148:151], v[188:191]\n"
    "ds_read_b128 a[176:179], v241 offset:58368\n"
    "v_accvgpr_read_b32 v247, a45\n"
    "v_mfma_f32_16x16x32_bf16 v[220:223], a[200:203], a[152:155], v[220:223]\n"
    "ds_read_b128 a[180:183], v241 offset:60416\n"
    "v_lshlrev_b32 v244, 16, v245\n"
    "v_mfma_f32_16x16x32_bf16 v[220:223], a[204:207], a[184:187], v[220:223]\n"
    "ds_read_b128 a[148:151], v241 offset:59392\n"
    "v_lshlrev_b32 v246, 16, v247\n"
    "v_mfma_f32_16x16x32_bf16 a[252:255], a[204:207], a[188:191], a[252:255]\n"
    "ds_read_b128 a[152:155], v241 offset:61440\n"
    "v_and_b32 v245, s58, v245\n"
    "v_mfma_f32_16x16x32_bf16 a[252:255], a[200:203], a[156:159], a[252:255]\n"
    "s_waitcnt vmcnt(17) lgkmcnt(12)\n"
    "ds_read_b128 a[184:187], v241 offset:62464\n"
    "v_and_b32 v247, s58, v247\n"
    "v_mfma_f32_16x16x32_bf16 v[16:19], a[208:211], a[128:131], v[16:19]\n"
    "buffer_load_dwordx4 a[200:203], v240, s[20:23], s53 offen offset:2048 sc1\n"
    "v_mfma_f32_16x16x32_bf16 v[16:19], a[212:215], a[160:163], v[16:19]\n"
    "s_waitcnt lgkmcnt(11)\n"
    "v_mul_f32 v244, v244, v104\n"
    "v_mul_f32 v246, v246, v106\n"
    "v_mfma_f32_16x16x32_bf16 v[48:51], a[212:215], a[164:167], v[48:51]\n"
    "buffer_load_dwordx4 a[204:207], v240, s[20:23], s53 offen offset:3072 sc1\n"
    "v_mfma_f32_16x16x32_bf16 v[48:51], a[208:211], a[132:135], v[48:51]\n"
    "s_waitcnt lgkmcnt(8)\n"
    "ds_read_b128 a[188:191], v241 offset:64512\n"
    "ds_read_b128 a[156:159], v241 offset:63488\n"
    "v_mfma_f32_16x16x32_bf16 v[80:83], a[208:211], a[136:139], v[80:83]\n"
    "s_add_u32 s63, s70, 147456\n"
    "v_mul_f32 v245, v245, v105\n"
    "v_mfma_f32_16x16x32_bf16 v[80:83], a[212:215], a[168:171], v[80:83]\n"
    "s_waitcnt lgkmcnt(8)\n"
    "v_mfma_f32_16x16x32_bf16 v[112:115], a[212:215], a[172:175], v[112:115]\n"
    "buffer_store_dwordx4 v[140:143], v243, s[40:43], s63 offen nt\n"
    "v_mul_f32 v247, v247, v107\n"
    "v_cvt_pk_bf16_f32 v104, v244, v245\n"
    "v_cvt_pk_bf16_f32 v105, v246, v247\n"
    "v_mfma_f32_16x16x32_bf16 v[112:115], a[208:211], a[140:143], v[112:115]\n"
    "s_waitcnt lgkmcnt(6)\n"
    "v_accvgpr_read_b32 v245, a46\n"
    "v_accvgpr_read_b32 v247, a47\n"
    "v_mfma_f32_16x16x32_bf16 v[144:147], a[208:211], a[144:147], v[144:147]\n"
    "buffer_load_dwordx4 v[196:199], v240, s[20:23], s91 offen offset:2048 sc1\n"
    "v_mfma_f32_16x16x32_bf16 v[144:147], a[212:215], a[176:179], v[144:147]\n"
    "s_waitcnt lgkmcnt(4)\n"
    "v_lshlrev_b32 v244, 16, v245\n"
    "v_lshlrev_b32 v246, 16, v247\n"
    "v_mfma_f32_16x16x32_bf16 v[176:179], a[212:215], a[180:183], v[176:179]\n"
    "v_and_b32 v245, s58, v245\n"
    "v_and_b32 v247, s58, v247\n"
    "v_mfma_f32_16x16x32_bf16 v[176:179], a[208:211], a[148:151], v[176:179]\n"
    "s_waitcnt lgkmcnt(2)\n"
    "v_mul_f32 v244, v244, v108\n"
    "v_mul_f32 v246, v246, v110\n"
    "v_mfma_f32_16x16x32_bf16 v[208:211], a[208:211], a[152:155], v[208:211]\n"
    "v_mul_f32 v245, v245, v109\n"
    "v_mul_f32 v247, v247, v111\n"
    "v_mfma_f32_16x16x32_bf16 v[208:211], a[212:215], a[184:187], v[208:211]\n"
    "s_waitcnt lgkmcnt(0)\n"
    "v_cvt_pk_bf16_f32 v106, v244, v245\n"
    "v_cvt_pk_bf16_f32 v107, v246, v247\n"
    "v_mfma_f32_16x16x32_bf16 a[240:243], a[212:215], a[188:191], a[240:243]\n"
    "ds_write_b128 v249, v[104:107] offset:2048\n"
    "v_mfma_f32_16x16x32_bf16 a[240:243], a[208:211], a[156:159], a[240:243]\n"
    "s_waitcnt vmcnt(17)\n"
    "ds_read_b128 v[100:103], v242 offset:2048\n"
    "ds_read_b128 v[108:111], v242 offset:3072\n"
    "v_mfma_f32_16x16x32_bf16 a[244:247], a[216:219], a[156:159], a[244:247]\n"
    "s_addk_i32 s53, 4096\n"
    "v_accvgpr_read_b32 v245, a8\n"
    "v_accvgpr_read_b32 v247, a9\n"
    "v_mfma_f32_16x16x32_bf16 a[244:247], a[220:223], a[188:191], a[244:247]\n"
    "buffer_load_dwordx4 a[208:211], v240, s[20:23], s53 offen offset:0 sc1\n"
    "v_mfma_f32_16x16x32_bf16 v[212:215], a[220:223], a[184:187], v[212:215]\n"
    "buffer_load_dwordx4 a[212:215], v240, s[20:23], s53 offen offset:1024 sc1\n"
    "v_mfma_f32_16x16x32_bf16 v[212:215], a[216:219], a[152:155], v[212:215]\n"
    "v_lshlrev_b32 v244, 16, v245\n"
    "v_lshlrev_b32 v246, 16, v247\n"
    "v_mfma_f32_16x16x32_bf16 v[180:183], a[216:219], a[148:151], v[180:183]\n"
    "v_and_b32 v245, s58, v245\n"
    "v_and_b32 v247, s58, v247\n"
    "v_mfma_f32_16x16x32_bf16 v[180:183], a[220:223], a[180:183], v[180:183]\n"
    "s_add_u32 s63, s70, 98304\n"
    "s_waitcnt lgkmcnt(1)\n"
    "v_mul_f32 v244, v244, v64\n"
    "v_mfma_f32_16x16x32_bf16 v[148:151], a[220:223], a[176:179], v[148:151]\n"
    "buffer_store_dwordx4 v[100:103], v243, s[40:43], s63 offen nt\n"
    "v_mul_f32 v246, v246, v66\n"
    "v_mul_f32 v245, v245, v65\n"
    "v_mul_f32 v247, v247, v67\n"
    "v_mfma_f32_16x16x32_bf16 v[148:151], a[216:219], a[144:147], v[148:151]\n"
    "v_cvt_pk_bf16_f32 v64, v244, v245\n"
    "v_cvt_pk_bf16_f32 v65, v246, v247\n"
    "v_mfma_f32_16x16x32_bf16 v[116:119], a[216:219], a[140:143], v[116:119]\n"
    "buffer_load_dwordx4 a[20:23], v240, s[20:23], s91 offen offset:3072 sc1\n"
    "v_mfma_f32_16x16x32_bf16 v[116:119], a[220:223], a[172:175], v[116:119]\n"
    "s_addk_i32 s91, 4096\n"
    "v_accvgpr_read_b32 v245, a10\n"
    "v_accvgpr_read_b32 v247, a11\n"
    "v_mfma_f32_16x16x32_bf16 v[84:87], a[220:223], a[168:171], v[84:87]\n"
    "v_lshlrev_b32 v244, 16, v245\n"
    "v_lshlrev_b32 v246, 16, v247\n"
    "v_mfma_f32_16x16x32_bf16 v[84:87], a[216:219], a[136:139], v[84:87]\n"
    "v_and_b32 v245, s58, v245\n"
    "v_and_b32 v247, s58, v247\n"
    "v_mfma_f32_16x16x32_bf16 v[52:55], a[216:219], a[132:135], v[52:55]\n"
    "v_mul_f32 v244, v244, v68\n"
    "v_mul_f32 v246, v246, v70\n"
    "v_mfma_f32_16x16x32_bf16 v[52:55], a[220:223], a[164:167], v[52:55]\n"
    "v_mul_f32 v245, v245, v69\n"
    "v_mul_f32 v247, v247, v71\n"
    "v_mfma_f32_16x16x32_bf16 v[20:23], a[220:223], a[160:163], v[20:23]\n"
    "v_cvt_pk_bf16_f32 v66, v244, v245\n"
    "v_cvt_pk_bf16_f32 v67, v246, v247\n"
    "v_mfma_f32_16x16x32_bf16 v[20:23], a[216:219], a[128:131], v[20:23]\n"
    "s_waitcnt vmcnt(17)\n"
    "v_mfma_f32_16x16x32_bf16 v[24:27], a[224:227], a[128:131], v[24:27]\n"
    "ds_write_b128 v248, v[64:67] offset:0\n"
    "v_mfma_f32_16x16x32_bf16 v[24:27], a[228:231], a[160:163], v[24:27]\n"
    "buffer_load_dwordx4 a[216:219], v240, s[20:23], s53 offen offset:2048 sc1\n"
    "v_mfma_f32_16x16x32_bf16 v[56:59], a[228:231], a[164:167], v[56:59]\n"
    "buffer_load_dwordx4 a[220:223], v240, s[20:23], s53 offen offset:3072 sc1\n"
    "v_mfma_f32_16x16x32_bf16 v[56:59], a[224:227], a[132:135], v[56:59]\n"
    "s_add_u32 s63, s70, 114688\n"
    "s_waitcnt lgkmcnt(1)\n"
    "v_accvgpr_read_b32 v245, a40\n"
    "v_mfma_f32_16x16x32_bf16 v[88:91], a[224:227], a[136:139], v[88:91]\n"
    "buffer_store_dwordx4 v[108:111], v243, s[40:43], s63 offen nt\n"
    "v_accvgpr_read_b32 v247, a41\n"
    "v_lshlrev_b32 v244, 16, v245\n"
    "v_lshlrev_b32 v246, 16, v247\n"
    "v_mfma_f32_16x16x32_bf16 v[88:91], a[228:231], a[168:171], v[88:91]\n"
    "v_and_b32 v245, s58, v245\n"
    "v_and_b32 v247, s58, v247\n"
    "v_mfma_f32_16x16x32_bf16 v[120:123], a[228:231], a[172:175], v[120:123]\n"
    "v_mul_f32 v244, v244, v72\n"
    "v_mul_f32 v246, v246, v74\n"
    "v_mfma_f32_16x16x32_bf16 v[120:123], a[224:227], a[140:143], v[120:123]\n"
    "v_mul_f32 v245, v245, v73\n"
    "v_mul_f32 v247, v247, v75\n"
    "v_mfma_f32_16x16x32_bf16 v[152:155], a[224:227], a[144:147], v[152:155]\n"
    "buffer_load_dwordx4 v[160:163], v240, s[20:23], s91 offen offset:0 sc1\n"
    "v_mfma_f32_16x16x32_bf16 v[152:155], a[228:231], a[176:179], v[152:155]\n"
    "v_cvt_pk_bf16_f32 v72, v244, v245\n"
    "v_cvt_pk_bf16_f32 v73, v246, v247\n"
    "v_mfma_f32_16x16x32_bf16 v[184:187], a[228:231], a[180:183], v[184:187]\n"
    "v_accvgpr_read_b32 v245, a42\n"
    "v_accvgpr_read_b32 v247, a43\n"
    "v_mfma_f32_16x16x32_bf16 v[184:187], a[224:227], a[148:151], v[184:187]\n"
    "v_lshlrev_b32 v244, 16, v245\n"
    "v_lshlrev_b32 v246, 16, v247\n"
    "v_mfma_f32_16x16x32_bf16 v[216:219], a[224:227], a[152:155], v[216:219]\n"
    "v_and_b32 v245, s58, v245\n"
    "v_and_b32 v247, s58, v247\n"
    "v_mfma_f32_16x16x32_bf16 v[216:219], a[228:231], a[184:187], v[216:219]\n"
    "v_mul_f32 v244, v244, v76\n"
    "v_mul_f32 v246, v246, v78\n"
    "v_mfma_f32_16x16x32_bf16 a[248:251], a[228:231], a[188:191], a[248:251]\n"
    "v_mul_f32 v245, v245, v77\n"
    "v_mul_f32 v247, v247, v79\n"
    "v_mfma_f32_16x16x32_bf16 a[248:251], a[224:227], a[156:159], a[248:251]\n"
    "s_waitcnt vmcnt(17)\n"
    "s_addk_i32 s53, -20480\n"
    "v_cvt_pk_bf16_f32 v74, v244, v245\n"
    "v_mfma_f32_16x16x32_bf16 a[252:255], a[232:235], a[156:159], a[252:255]\n"
    "buffer_load_dwordx4 a[224:227], v240, s[20:23], s53 offen offset:0 sc1\n"
    "v_mfma_f32_16x16x32_bf16 a[252:255], a[236:239], a[188:191], a[252:255]\n"
    "buffer_load_dwordx4 a[228:231], v240, s[20:23], s53 offen offset:1024 sc1\n"
    "v_mfma_f32_16x16x32_bf16 v[220:223], a[236:239], a[184:187], v[220:223]\n"
    "ds_read_b128 a[156:159], v241 offset:47104\n"
    "v_cvt_pk_bf16_f32 v75, v246, v247\n"
    "v_mfma_f32_16x16x32_bf16 v[220:223], a[232:235], a[152:155], v[220:223]\n"
    "ds_write_b128 v249, v[72:75] offset:0\n"
    "v_mfma_f32_16x16x32_bf16 v[188:191], a[232:235], a[148:151], v[188:191]\n"
    "ds_read_b128 a[188:191], v241 offset:48128\n"
    "ds_read_b128 v[68:71], v242 offset:0\n"
    "v_mfma_f32_16x16x32_bf16 v[188:191], a[236:239], a[180:183], v[188:191]\n"
    "ds_read_b128 v[76:79], v242 offset:1024\n"
    "ds_read_b128 a[184:187], v241 offset:46080\n"
    "v_mfma_f32_16x16x32_bf16 v[156:159], a[236:239], a[176:179], v[156:159]\n"
    "ds_read_b128 a[152:155], v241 offset:45056\n"
    "ds_read_b128 a[148:151], v241 offset:43008\n"
    "v_mfma_f32_16x16x32_bf16 v[156:159], a[232:235], a[144:147], v[156:159]\n"
    "ds_read_b128 a[180:183], v241 offset:44032\n"
    "v_accvgpr_read_b32 v245, a4\n"
    "v_mfma_f32_16x16x32_bf16 v[124:127], a[232:235], a[140:143], v[124:127]\n"
    "buffer_load_dwordx4 v[204:207], v240, s[20:23], s91 offen offset:1024 sc1\n"
    "v_mfma_f32_16x16x32_bf16 v[124:127], a[236:239], a[172:175], v[124:127]\n"
    "ds_read_b128 a[176:179], v241 offset:41984\n"
    "ds_read_b128 a[144:147], v241 offset:40960\n"
    "v_mfma_f32_16x16x32_bf16 v[92:95], a[236:239], a[168:171], v[92:95]\n"
    "ds_read_b128 a[140:143], v241 offset:38912\n"
    "v_accvgpr_read_b32 v247, a5\n"
    "v_mfma_f32_16x16x32_bf16 v[92:95], a[232:235], a[136:139], v[92:95]\n"
    "ds_read_b128 a[172:175], v241 offset:39936\n"
    "v_lshlrev_b32 v244, 16, v245\n"
    "v_mfma_f32_16x16x32_bf16 v[60:63], a[232:235], a[132:135], v[60:63]\n"
    "ds_read_b128 a[168:171], v241 offset:37888\n"
    "v_lshlrev_b32 v246, 16, v247\n"
    "v_mfma_f32_16x16x32_bf16 v[60:63], a[236:239], a[164:167], v[60:63]\n"
    "ds_read_b128 a[136:139], v241 offset:36864\n"
    "v_and_b32 v245, s58, v245\n"
    "v_mfma_f32_16x16x32_bf16 v[28:31], a[236:239], a[160:163], v[28:31]\n"
    "ds_read_b128 a[132:135], v241 offset:34816\n"
    "v_and_b32 v247, s58, v247\n"
    "v_mfma_f32_16x16x32_bf16 v[28:31], a[232:235], a[128:131], v[28:31]\n"
    "s_waitcnt vmcnt(17) lgkmcnt(13)\n"
    "ds_read_b128 a[164:167], v241 offset:35840\n"
    "v_mul_f32 v244, v244, v32\n"
    "v_mfma_f32_16x16x32_bf16 a[240:243], a[192:195], a[156:159], a[240:243]\n"
    "buffer_load_dwordx4 a[232:235], v240, s[20:23], s53 offen offset:2048 sc1\n"
    "v_mfma_f32_16x16x32_bf16 a[240:243], a[196:199], a[188:191], a[240:243]\n"
    "s_waitcnt lgkmcnt(10)\n"
    "v_mul_f32 v246, v246, v34\n"
    "v_mul_f32 v245, v245, v33\n"
    "v_mfma_f32_16x16x32_bf16 v[208:211], a[196:199], a[184:187], v[208:211]\n"
    "buffer_load_dwordx4 a[236:239], v240, s[20:23], s53 offen offset:3072 sc1\n"
    "v_mfma_f32_16x16x32_bf16 v[208:211], a[192:195], a[152:155], v[208:211]\n"
    "s_waitcnt lgkmcnt(8)\n"
    "ds_read_b128 a[160:163], v241 offset:33792\n"
    "ds_read_b128 a[128:131], v241 offset:32768\n"
    "v_mfma_f32_16x16x32_bf16 v[176:179], a[192:195], a[148:151], v[176:179]\n"
    "s_add_u32 s63, s70, 65536\n"
    "v_mul_f32 v247, v247, v35\n"
    "v_mfma_f32_16x16x32_bf16 v[176:179], a[196:199], a[180:183], v[176:179]\n"
    "s_waitcnt lgkmcnt(8)\n"
    "v_mfma_f32_16x16x32_bf16 v[144:147], a[196:199], a[176:179], v[144:147]\n"
    "buffer_store_dwordx4 v[68:71], v243, s[40:43], s63 offen nt\n"
    "v_cvt_pk_bf16_f32 v32, v244, v245\n"
    "v_cvt_pk_bf16_f32 v33, v246, v247\n"
    "v_accvgpr_read_b32 v245, a6\n"
    "v_mfma_f32_16x16x32_bf16 v[144:147], a[192:195], a[144:147], v[144:147]\n"
    "s_waitcnt lgkmcnt(6)\n"
    "v_accvgpr_read_b32 v247, a7\n"
    "v_lshlrev_b32 v244, 16, v245\n"
    "v_mfma_f32_16x16x32_bf16 v[112:115], a[192:195], a[140:143], v[112:115]\n"
    "buffer_load_dwordx4 a[52:55], v240, s[20:23], s91 offen offset:2048 sc1\n"
    "v_mfma_f32_16x16x32_bf16 v[112:115], a[196:199], a[172:175], v[112:115]\n"
    "s_waitcnt lgkmcnt(4)\n"
    "v_lshlrev_b32 v246, 16, v247\n"
    "v_and_b32 v245, s58, v245\n"
    "v_mfma_f32_16x16x32_bf16 v[80:83], a[196:199], a[168:171], v[80:83]\n"
    "v_and_b32 v247, s58, v247\n"
    "v_mul_f32 v244, v244, v36\n"
    "v_mfma_f32_16x16x32_bf16 v[80:83], a[192:195], a[136:139], v[80:83]\n"
    "s_waitcnt lgkmcnt(2)\n"
    "v_mul_f32 v246, v246, v38\n"
    "v_mul_f32 v245, v245, v37\n"
    "v_mfma_f32_16x16x32_bf16 v[48:51], a[192:195], a[132:135], v[48:51]\n"
    "v_mul_f32 v247, v247, v39\n"
    "v_cvt_pk_bf16_f32 v34, v244, v245\n"
    "v_mfma_f32_16x16x32_bf16 v[48:51], a[196:199], a[164:167], v[48:51]\n"
    "s_waitcnt lgkmcnt(0)\n"
    "v_cvt_pk_bf16_f32 v35, v246, v247\n"
    "v_mfma_f32_16x16x32_bf16 v[16:19], a[196:199], a[160:163], v[16:19]\n"
    "ds_write_b128 v248, v[32:35] offset:2048\n"
    "v_mfma_f32_16x16x32_bf16 v[16:19], a[192:195], a[128:131], v[16:19]\n"
    "s_waitcnt vmcnt(17)\n"
    "s_addk_i32 s53, 4096\n"
    "v_accvgpr_read_b32 v245, a36\n"
    "v_mfma_f32_16x16x32_bf16 v[20:23], a[200:203], a[128:131], v[20:23]\n"
    "buffer_load_dwordx4 a[192:195], v240, s[20:23], s53 offen offset:0 sc1\n"
    "v_mfma_f32_16x16x32_bf16 v[20:23], a[204:207], a[160:163], v[20:23]\n"
    "buffer_load_dwordx4 a[196:199], v240, s[20:23], s53 offen offset:1024 sc1\n"
    "v_mfma_f32_16x16x32_bf16 v[52:55], a[204:207], a[164:167], v[52:55]\n"
    "v_accvgpr_read_b32 v247, a37\n"
    "v_lshlrev_b32 v244, 16, v245\n"
    "v_mfma_f32_16x16x32_bf16 v[52:55], a[200:203], a[132:135], v[52:55]\n"
    "s_add_u32 s63, s70, 81920\n"
    "v_lshlrev_b32 v246, 16, v247\n"
    "v_mfma_f32_16x16x32_bf16 v[84:87], a[200:203], a[136:139], v[84:87]\n"
    "buffer_store_dwordx4 v[76:79], v243, s[40:43], s63 offen nt\n"
    "v_and_b32 v245, s58, v245\n"
    "v_and_b32 v247, s58, v247\n"
    "v_mul_f32 v244, v244, v40\n"
    "v_mfma_f32_16x16x32_bf16 v[84:87], a[204:207], a[168:171], v[84:87]\n"
    "v_mul_f32 v246, v246, v42\n"
    "v_mul_f32 v245, v245, v41\n"
    "v_mfma_f32_16x16x32_bf16 v[116:119], a[204:207], a[172:175], v[116:119]\n"
    "v_mul_f32 v247, v247, v43\n"
    "v_cvt_pk_bf16_f32 v40, v244, v245\n"
    "v_mfma_f32_16x16x32_bf16 v[116:119], a[200:203], a[140:143], v[116:119]\n"
    "v_cvt_pk_bf16_f32 v41, v246, v247\n"
    "v_accvgpr_read_b32 v245, a38\n"
    "v_mfma_f32_16x16x32_bf16 v[148:151], a[200:203], a[144:147], v[148:151]\n"
    "buffer_load_dwordx4 v[168:171], v240, s[20:23], s91 offen offset:3072 sc1\n"
    "v_mfma_f32_16x16x32_bf16 v[148:151], a[204:207], a[176:179], v[148:151]\n"
    "s_addk_i32 s91, 4096\n"
    "v_accvgpr_read_b32 v247, a39\n"
    "v_lshlrev_b32 v244, 16, v245\n"
    "v_mfma_f32_16x16x32_bf16 v[180:183], a[204:207], a[180:183], v[180:183]\n"
    "v_lshlrev_b32 v246, 16, v247\n"
    "v_and_b32 v245, s58, v245\n"
    "v_mfma_f32_16x16x32_bf16 v[180:183], a[200:203], a[148:151], v[180:183]\n"
    "v_and_b32 v247, s58, v247\n"
    "v_mul_f32 v244, v244, v44\n"
    "v_mfma_f32_16x16x32_bf16 v[212:215], a[200:203], a[152:155], v[212:215]\n"
    "v_mul_f32 v246, v246, v46\n"
    "v_mul_f32 v245, v245, v45\n"
    "v_mfma_f32_16x16x32_bf16 v[212:215], a[204:207], a[184:187], v[212:215]\n"
    "v_mul_f32 v247, v247, v47\n"
    "v_cvt_pk_bf16_f32 v42, v244, v245\n"
    "v_mfma_f32_16x16x32_bf16 a[244:247], a[204:207], a[188:191], a[244:247]\n"
    "v_cvt_pk_bf16_f32 v43, v246, v247\n"
    "v_mfma_f32_16x16x32_bf16 a[244:247], a[200:203], a[156:159], a[244:247]\n"
    "s_waitcnt vmcnt(17)\n"
    "v_mfma_f32_16x16x32_bf16 a[248:251], a[208:211], a[156:159], a[248:251]\n"
    "ds_write_b128 v249, v[40:43] offset:2048\n"
    "v_mfma_f32_16x16x32_bf16 a[248:251], a[212:215], a[188:191], a[248:251]\n"
    "buffer_load_dwordx4 a[200:203], v240, s[20:23], s53 offen offset:2048 sc1\n"
    "v_mfma_f32_16x16x32_bf16 v[216:219], a[212:215], a[184:187], v[216:219]\n"
    "buffer_load_dwordx4 a[204:207], v240, s[20:23], s53 offen offset:3072 sc1\n"
    "v_mfma_f32_16x16x32_bf16 v[216:219], a[208:211], a[152:155], v[216:219]\n"
    "buffer_load_dwordx4 v[164:167], v240, s[20:23], s91 offen offset:0 sc1\n"
    "ds_read_b128 v[36:39], v242 offset:2048\n"
    "ds_read_b128 v[44:47], v242 offset:3072\n"
    "v_mfma_f32_16x16x32_bf16 v[184:187], a[208:211], a[148:151], v[184:187]\n"
    "buffer_load_dwordx4 a[16:19], v240, s[20:23], s91 offen offset:1024 sc1\n"
    "v_mfma_f32_16x16x32_bf16 v[184:187], a[212:215], a[180:183], v[184:187]\n"
    "v_accvgpr_read_b32 v245, a0\n"
    "v_accvgpr_read_b32 v247, a1\n"
    "v_mfma_f32_16x16x32_bf16 v[152:155], a[212:215], a[176:179], v[152:155]\n"
    "v_lshlrev_b32 v244, 16, v245\n"
    "v_lshlrev_b32 v246, 16, v247\n"
    "v_mfma_f32_16x16x32_bf16 v[152:155], a[208:211], a[144:147], v[152:155]\n"
    "s_add_u32 s63, s70, 32768\n"
    "s_waitcnt lgkmcnt(1)\n"
    "v_and_b32 v245, s58, v245\n"
    "v_mfma_f32_16x16x32_bf16 v[120:123], a[208:211], a[140:143], v[120:123]\n"
    "buffer_store_dwordx4 v[36:39], v243, s[40:43], s63 offen nt\n"
    "buffer_load_dwordx4 v[128:131], v240, s[20:23], s91 offen offset:2048 sc1\n"
    "v_mfma_f32_16x16x32_bf16 v[120:123], a[212:215], a[172:175], v[120:123]\n"
    "v_and_b32 v247, s58, v247\n"
    "v_mul_f32 v244, v244, v0\n"
    "v_mfma_f32_16x16x32_bf16 v[88:91], a[212:215], a[168:171], v[88:91]\n"
    "v_mul_f32 v246, v246, v2\n"
    "v_mul_f32 v245, v245, v1\n"
    "v_mfma_f32_16x16x32_bf16 v[88:91], a[208:211], a[136:139], v[88:91]\n"
    "v_mul_f32 v247, v247, v3\n"
    "v_cvt_pk_bf16_f32 v0, v244, v245\n"
    "v_mfma_f32_16x16x32_bf16 v[56:59], a[208:211], a[132:135], v[56:59]\n"
    "buffer_load_dwordx4 v[172:175], v240, s[20:23], s91 offen offset:3072 sc1\n"
    "v_mfma_f32_16x16x32_bf16 v[56:59], a[212:215], a[164:167], v[56:59]\n"
    "s_addk_i32 s91, 4096\n"
    "v_cvt_pk_bf16_f32 v1, v246, v247\n"
    "v_accvgpr_read_b32 v245, a2\n"
    "v_mfma_f32_16x16x32_bf16 v[24:27], a[212:215], a[160:163], v[24:27]\n"
    "v_accvgpr_read_b32 v247, a3\n"
    "v_lshlrev_b32 v244, 16, v245\n"
    "v_mfma_f32_16x16x32_bf16 v[24:27], a[208:211], a[128:131], v[24:27]\n"
    "s_waitcnt vmcnt(20)\n"
    "v_lshlrev_b32 v246, 16, v247\n"
    "v_and_b32 v245, s58, v245\n"
    "v_mfma_f32_16x16x32_bf16 v[28:31], a[216:219], a[128:131], v[28:31]\n"
    "buffer_load_dwordx4 a[48:51], v240, s[20:23], s91 offen offset:0 sc1\n"
    "v_mfma_f32_16x16x32_bf16 v[28:31], a[220:223], a[160:163], v[28:31]\n"
    "s_add_u32 s63, s70, 49152\n"
    "s_waitcnt lgkmcnt(0)\n"
    "v_and_b32 v247, s58, v247\n"
    "v_mfma_f32_16x16x32_bf16 v[60:63], a[220:223], a[164:167], v[60:63]\n"
    "buffer_store_dwordx4 v[44:47], v243, s[40:43], s63 offen nt\n"
    "ds_read_b128 a[128:131], v241 offset:16384\n"
    "v_mul_f32 v244, v244, v4\n"
    "v_mul_f32 v246, v246, v6\n"
    "v_mfma_f32_16x16x32_bf16 v[60:63], a[216:219], a[132:135], v[60:63]\n"
    "ds_read_b128 a[160:163], v241 offset:17408\n"
    "v_mul_f32 v245, v245, v5\n"
    "v_mfma_f32_16x16x32_bf16 v[92:95], a[216:219], a[136:139], v[92:95]\n"
    "buffer_load_dwordx4 v[136:139], v240, s[20:23], s91 offen offset:1024 sc1\n"
    "v_mfma_f32_16x16x32_bf16 v[92:95], a[220:223], a[168:171], v[92:95]\n"
    "ds_read_b128 a[164:167], v241 offset:19456\n"
    "ds_read_b128 a[132:135], v241 offset:18432\n"
    "v_mfma_f32_16x16x32_bf16 v[124:127], a[220:223], a[172:175], v[124:127]\n"
    "ds_read_b128 a[136:139], v241 offset:20480\n"
    "v_mul_f32 v247, v247, v7\n"
    "v_mfma_f32_16x16x32_bf16 v[124:127], a[216:219], a[140:143], v[124:127]\n"
    "ds_read_b128 a[168:171], v241 offset:21504\n"
    "v_cvt_pk_bf16_f32 v2, v244, v245\n"
    "v_mfma_f32_16x16x32_bf16 v[156:159], a[216:219], a[144:147], v[156:159]\n"
    "buffer_load_dwordx4 v[132:135], v240, s[20:23], s91 offen offset:2048 sc1\n"
    "v_mfma_f32_16x16x32_bf16 v[156:159], a[220:223], a[176:179], v[156:159]\n"
    "ds_read_b128 a[172:175], v241 offset:23552\n"
    "ds_read_b128 a[140:143], v241 offset:22528\n"
    "v_mfma_f32_16x16x32_bf16 v[188:191], a[220:223], a[180:183], v[188:191]\n"
    "ds_read_b128 a[144:147], v241 offset:24576\n"
    "v_cvt_pk_bf16_f32 v3, v246, v247\n"
    "v_mfma_f32_16x16x32_bf16 v[188:191], a[216:219], a[148:151], v[188:191]\n"
    "ds_write_b128 v248, v[0:3] offset:0\n"
    "v_mfma_f32_16x16x32_bf16 v[220:223], a[216:219], a[152:155], v[220:223]\n"
    "ds_read_b128 a[176:179], v241 offset:25600\n"
    "v_accvgpr_read_b32 v245, a32\n"
    "v_mfma_f32_16x16x32_bf16 v[220:223], a[220:223], a[184:187], v[220:223]\n"
    "buffer_load_dwordx4 a[12:15], v240, s[20:23], s91 offen offset:3072 sc1\n"
    "v_mfma_f32_16x16x32_bf16 a[252:255], a[220:223], a[188:191], a[252:255]\n"
    "s_addk_i32 s91, 4096\n"
    "ds_read_b128 a[180:183], v241 offset:27648\n"
    "ds_read_b128 a[148:151], v241 offset:26624\n"
    "v_mfma_f32_16x16x32_bf16 a[252:255], a[216:219], a[156:159], a[252:255]\n"
    "s_waitcnt vmcnt(21) lgkmcnt(11)\n"
    "ds_read_b128 a[152:155], v241 offset:28672\n"
    "ds_read_b128 a[184:187], v241 offset:29696\n"
    "v_mfma_f32_16x16x32_bf16 v[16:19], a[224:227], a[128:131], v[16:19]\n"
    "buffer_load_dwordx4 v[96:99], v240, s[20:23], s91 offen offset:0 sc1\n"
    "v_mfma_f32_16x16x32_bf16 v[16:19], a[228:231], a[160:163], v[16:19]\n"
    "s_waitcnt lgkmcnt(11)\n"
    "ds_read_b128 a[188:191], v241 offset:31744\n"
    "ds_read_b128 a[156:159], v241 offset:30720\n"
    "v_mfma_f32_16x16x32_bf16 v[48:51], a[228:231], a[164:167], v[48:51]\n"
    "v_accvgpr_read_b32 v247, a33\n"
    "v_lshlrev_b32 v244, 16, v245\n"
    "v_mfma_f32_16x16x32_bf16 v[48:51], a[224:227], a[132:135], v[48:51]\n"
    "s_waitcnt lgkmcnt(11)\n"
    "v_lshlrev_b32 v246, 16, v247\n"
    "v_and_b32 v245, s58, v245\n"
    "v_mfma_f32_16x16x32_bf16 v[80:83], a[224:227], a[136:139], v[80:83]\n"
    "buffer_load_dwordx4 v[140:143], v240, s[20:23], s91 offen offset:1024 sc1\n"
    "v_mfma_f32_16x16x32_bf16 v[80:83], a[228:231], a[168:171], v[80:83]\n"
    "s_waitcnt lgkmcnt(9)\n"
    "v_and_b32 v247, s58, v247\n"
    "v_mul_f32 v244, v244, v8\n"
    "v_mfma_f32_16x16x32_bf16 v[112:115], a[228:231], a[172:175], v[112:115]\n"
    "v_mul_f32 v246, v246, v10\n"
    "v_mul_f32 v245, v245, v9\n"
    "v_mfma_f32_16x16x32_bf16 v[112:115], a[224:227], a[140:143], v[112:115]\n"
    "s_waitcnt lgkmcnt(6)\n"
    "v_mul_f32 v247, v247, v11\n"
    "v_cvt_pk_bf16_f32 v8, v244, v245\n"
    "v_mfma_f32_16x16x32_bf16 v[144:147], a[224:227], a[144:147], v[144:147]\n"
    "buffer_load_dwordx4 a[44:47], v240, s[20:23], s91 offen offset:2048 sc1\n"
    "v_mfma_f32_16x16x32_bf16 v[144:147], a[228:231], a[176:179], v[144:147]\n"
    "s_waitcnt lgkmcnt(4)\n"
    "v_cvt_pk_bf16_f32 v9, v246, v247\n"
    "v_accvgpr_read_b32 v245, a34\n"
    "v_mfma_f32_16x16x32_bf16 v[176:179], a[228:231], a[180:183], v[176:179]\n"
    "v_accvgpr_read_b32 v247, a35\n"
    "v_lshlrev_b32 v244, 16, v245\n"
    "v_mfma_f32_16x16x32_bf16 v[176:179], a[224:227], a[148:151], v[176:179]\n"
    "s_waitcnt lgkmcnt(2)\n"
    "v_lshlrev_b32 v246, 16, v247\n"
    "v_and_b32 v245, s58, v245\n"
    "v_mfma_f32_16x16x32_bf16 v[208:211], a[224:227], a[152:155], v[208:211]\n"
    "buffer_load_dwordx4 v[104:107], v240, s[20:23], s91 offen offset:3072 sc1\n"
    "v_mfma_f32_16x16x32_bf16 v[208:211], a[228:231], a[184:187], v[208:211]\n"
    "s_waitcnt lgkmcnt(0)\n"
    "s_addk_i32 s91, 4096\n"
    "v_and_b32 v247, s58, v247\n"
    "v_mfma_f32_16x16x32_bf16 a[240:243], a[228:231], a[188:191], a[240:243]\n"
    "v_mul_f32 v244, v244, v12\n"
    "v_mul_f32 v246, v246, v14\n"
    "v_mfma_f32_16x16x32_bf16 a[240:243], a[224:227], a[156:159], a[240:243]\n"
    "s_waitcnt vmcnt(22)\n"
    "v_mul_f32 v245, v245, v13\n"
    "v_mul_f32 v247, v247, v15\n"
    "v_mfma_f32_16x16x32_bf16 a[244:247], a[232:235], a[156:159], a[244:247]\n"
    "buffer_load_dwordx4 v[100:103], v240, s[20:23], s91 offen offset:0 sc1\n"
    "v_mfma_f32_16x16x32_bf16 a[244:247], a[236:239], a[188:191], a[244:247]\n"
    "v_cvt_pk_bf16_f32 v10, v244, v245\n"
    "v_cvt_pk_bf16_f32 v11, v246, v247\n"
    "v_mfma_f32_16x16x32_bf16 v[212:215], a[236:239], a[184:187], v[212:215]\n"
    "ds_write_b128 v249, v[8:11] offset:0\n"
    "v_mfma_f32_16x16x32_bf16 v[212:215], a[232:235], a[152:155], v[212:215]\n"
    "ds_read_b128 v[4:7], v242 offset:0\n"
    "ds_read_b128 v[12:15], v242 offset:1024\n"
    "v_mfma_f32_16x16x32_bf16 v[180:183], a[232:235], a[148:151], v[180:183]\n"
    "buffer_load_dwordx4 a[8:11], v240, s[20:23], s91 offen offset:1024 sc1\n"
    "v_mfma_f32_16x16x32_bf16 v[180:183], a[236:239], a[180:183], v[180:183]\n"
    "v_mfma_f32_16x16x32_bf16 v[148:151], a[236:239], a[176:179], v[148:151]\n"
    "v_mfma_f32_16x16x32_bf16 v[148:151], a[232:235], a[144:147], v[148:151]\n"
    "v_mfma_f32_16x16x32_bf16 v[116:119], a[232:235], a[140:143], v[116:119]\n"
    "buffer_load_dwordx4 v[64:67], v240, s[20:23], s91 offen offset:2048 sc1\n"
    "v_mfma_f32_16x16x32_bf16 v[116:119], a[236:239], a[172:175], v[116:119]\n"
    "v_mfma_f32_16x16x32_bf16 v[84:87], a[236:239], a[168:171], v[84:87]\n"
    "v_mfma_f32_16x16x32_bf16 v[84:87], a[232:235], a[136:139], v[84:87]\n"
    "v_mfma_f32_16x16x32_bf16 v[52:55], a[232:235], a[132:135], v[52:55]\n"
    "buffer_load_dwordx4 v[108:111], v240, s[20:23], s91 offen offset:3072 sc1\n"
    "v_mfma_f32_16x16x32_bf16 v[52:55], a[236:239], a[164:167], v[52:55]\n"
    "s_addk_i32 s91, 4096\n"
    "s_add_u32 s63, s70, 0\n"
    "s_waitcnt lgkmcnt(1)\n"
    "v_mfma_f32_16x16x32_bf16 v[20:23], a[236:239], a[160:163], v[20:23]\n"
    "buffer_store_dwordx4 v[4:7], v243, s[40:43], s63 offen nt\n"
    "v_mfma_f32_16x16x32_bf16 v[20:23], a[232:235], a[128:131], v[20:23]\n"
    "s_waitcnt vmcnt(23)\n"
    "v_mfma_f32_16x16x32_bf16 v[24:27], a[192:195], a[128:131], v[24:27]\n"
    "v_mfma_f32_16x16x32_bf16 v[24:27], a[196:199], a[160:163], v[24:27]\n"
    "s_add_u32 s63, s70, 16384\n"
    "s_waitcnt lgkmcnt(0)\n"
    "v_mfma_f32_16x16x32_bf16 v[56:59], a[196:199], a[164:167], v[56:59]\n"
    "buffer_store_dwordx4 v[12:15], v243, s[40:43], s63 offen nt\n"
    "v_mfma_f32_16x16x32_bf16 v[56:59], a[192:195], a[132:135], v[56:59]\n"
    "v_mfma_f32_16x16x32_bf16 v[88:91], a[192:195], a[136:139], v[88:91]\n"
    "v_mfma_f32_16x16x32_bf16 v[88:91], a[196:199], a[168:171], v[88:91]\n"
    "v_mfma_f32_16x16x32_bf16 v[120:123], a[196:199], a[172:175], v[120:123]\n"
    "v_mfma_f32_16x16x32_bf16 v[120:123], a[192:195], a[140:143], v[120:123]\n"
    "v_mfma_f32_16x16x32_bf16 v[152:155], a[192:195], a[144:147], v[152:155]\n"
    "v_mfma_f32_16x16x32_bf16 v[152:155], a[196:199], a[176:179], v[152:155]\n"
    "v_mfma_f32_16x16x32_bf16 v[184:187], a[196:199], a[180:183], v[184:187]\n"
    "v_mfma_f32_16x16x32_bf16 v[184:187], a[192:195], a[148:151], v[184:187]\n"
    "v_mfma_f32_16x16x32_bf16 v[216:219], a[192:195], a[152:155], v[216:219]\n"
    "v_mfma_f32_16x16x32_bf16 v[216:219], a[196:199], a[184:187], v[216:219]\n"
    "v_mfma_f32_16x16x32_bf16 a[248:251], a[196:199], a[188:191], a[248:251]\n"
    "v_mfma_f32_16x16x32_bf16 a[248:251], a[192:195], a[156:159], a[248:251]\n"
    "s_waitcnt vmcnt(20)\n"
    "v_mfma_f32_16x16x32_bf16 a[252:255], a[200:203], a[156:159], a[252:255]\n"
    "v_mfma_f32_16x16x32_bf16 a[252:255], a[204:207], a[188:191], a[252:255]\n"
    "v_mfma_f32_16x16x32_bf16 v[220:223], a[204:207], a[184:187], v[220:223]\n"
    "v_add_u32 v241, s57, v240\n"
    "ds_read_b128 a[156:159], v241 offset:0\n"
    "v_mfma_f32_16x16x32_bf16 v[220:223], a[200:203], a[152:155], v[220:223]\n"
    "ds_read_b128 a[188:191], v241 offset:1024\n"
    "v_mfma_f32_16x16x32_bf16 v[188:191], a[200:203], a[148:151], v[188:191]\n"
    "ds_read_b128 a[184:187], v241 offset:16384\n"
    "v_mfma_f32_16x16x32_bf16 v[188:191], a[204:207], a[180:183], v[188:191]\n"
    "ds_read_b128 a[152:155], v241 offset:17408\n"
    "v_mfma_f32_16x16x32_bf16 v[156:159], a[204:207], a[176:179], v[156:159]\n"
    "ds_read_b128 a[148:151], v241 offset:32768\n"
    "v_mfma_f32_16x16x32_bf16 v[156:159], a[200:203], a[144:147], v[156:159]\n"
    "ds_read_b128 a[180:183], v241 offset:33792\n"
    "v_mfma_f32_16x16x32_bf16 v[124:127], a[200:203], a[140:143], v[124:127]\n"
    "ds_read_b128 a[176:179], v241 offset:49152\n"
    "v_mfma_f32_16x16x32_bf16 v[124:127], a[204:207], a[172:175], v[124:127]\n"
    "ds_read_b128 a[144:147], v241 offset:50176\n"
    "v_mfma_f32_16x16x32_bf16 v[92:95], a[204:207], a[168:171], v[92:95]\n"
    "v_mfma_f32_16x16x32_bf16 v[92:95], a[200:203], a[136:139], v[92:95]\n"
    "v_mfma_f32_16x16x32_bf16 v[60:63], a[200:203], a[132:135], v[60:63]\n"
    "v_mfma_f32_16x16x32_bf16 v[60:63], a[204:207], a[164:167], v[60:63]\n"
    "v_mfma_f32_16x16x32_bf16 v[28:31], a[204:207], a[160:163], v[28:31]\n"
    "v_mfma_f32_16x16x32_bf16 v[28:31], a[200:203], a[128:131], v[28:31]\n"
    "s_waitcnt lgkmcnt(7)\n"
    "v_mfma_f32_16x16x32_bf16 v[16:19], a[28:31], a[156:159], v[16:19]\n"
    "s_waitcnt lgkmcnt(6)\n"
    "v_mfma_f32_16x16x32_bf16 v[16:19], v[224:227], a[188:191], v[16:19]\n"
    "s_waitcnt lgkmcnt(5)\n"
    "ds_read_b128 a[140:143], v241 offset:2048\n"
    "v_mfma_f32_16x16x32_bf16 v[16:19], a[56:59], a[184:187], v[16:19]\n"
    "s_waitcnt lgkmcnt(5)\n"
    "v_mfma_f32_16x16x32_bf16 v[16:19], v[200:203], a[152:155], v[16:19]\n"
    "s_waitcnt vmcnt(19) lgkmcnt(4)\n"
    "v_mfma_f32_16x16x32_bf16 v[16:19], v[164:167], a[148:151], v[16:19]\n"
    "s_waitcnt vmcnt(18) lgkmcnt(3)\n"
    "ds_read_b128 a[172:175], v241 offset:3072\n"
    "v_mfma_f32_16x16x32_bf16 v[16:19], a[16:19], a[180:183], v[16:19]\n"
    "s_waitcnt vmcnt(9) lgkmcnt(3)\n"
    "v_mfma_f32_16x16x32_bf16 v[16:19], v[96:99], a[176:179], v[16:19]\n"
    "s_waitcnt vmcnt(8) lgkmcnt(2)\n"
    "v_mfma_f32_16x16x32_bf16 v[16:19], v[140:143], a[144:147], v[16:19]\n"
    "ds_read_b128 a[168:171], v241 offset:18432\n"
    "v_mfma_f32_16x16x32_bf16 v[20:23], a[24:27], a[156:159], v[20:23]\n"
    "v_mfma_f32_16x16x32_bf16 v[20:23], v[192:195], a[188:191], v[20:23]\n"
    "v_accvgpr_read_b32 v245, a64\n"
    "v_accvgpr_read_b32 v247, a65\n"
    "v_mfma_f32_16x16x32_bf16 v[20:23], v[196:199], a[184:187], v[20:23]\n"
    "ds_read_b128 a[136:139], v241 offset:19456\n"
    "v_lshlrev_b32 v244, 16, v245\n"
    "v_mfma_f32_16x16x32_bf16 v[20:23], a[20:23], a[152:155], v[20:23]\n"
    "v_lshlrev_b32 v246, 16, v247\n"
    "v_and_b32 v245, s58, v245\n"
    "v_mfma_f32_16x16x32_bf16 v[20:23], v[128:131], a[148:151], v[20:23]\n"
    "v_and_b32 v247, s58, v247\n"
    "v_mul_f32 v244, v244, v16\n"
    "v_mfma_f32_16x16x32_bf16 v[20:23], v[172:175], a[180:183], v[20:23]\n"
    "s_waitcnt vmcnt(7)\n"
    "ds_read_b128 a[132:135], v241 offset:34816\n"
    "v_mul_f32 v246, v246, v18\n"
    "v_mfma_f32_16x16x32_bf16 v[20:23], a[44:47], a[176:179], v[20:23]\n"
    "s_waitcnt vmcnt(6)\n"
    "v_mul_f32 v245, v245, v17\n"
    "v_mul_f32 v247, v247, v19\n"
    "v_mfma_f32_16x16x32_bf16 v[20:23], v[104:107], a[144:147], v[20:23]\n"
    "v_cvt_pk_bf16_f32 v16, v244, v245\n"
    "v_cvt_pk_bf16_f32 v17, v246, v247\n"
    "v_mfma_f32_16x16x32_bf16 v[24:27], a[60:63], a[156:159], v[24:27]\n"
    "ds_read_b128 a[164:167], v241 offset:35840\n"
    "v_mfma_f32_16x16x32_bf16 v[24:27], v[232:235], a[188:191], v[24:27]\n"
    "v_accvgpr_read_b32 v245, a66\n"
    "v_accvgpr_read_b32 v247, a67\n"
    "v_mfma_f32_16x16x32_bf16 v[24:27], v[160:163], a[184:187], v[24:27]\n"
    "v_lshlrev_b32 v244, 16, v245\n"
    "v_lshlrev_b32 v246, 16, v247\n"
    "v_mfma_f32_16x16x32_bf16 v[24:27], v[204:207], a[152:155], v[24:27]\n"
    "ds_read_b128 a[160:163], v241 offset:51200\n"
    "v_and_b32 v245, s58, v245\n"
    "v_mfma_f32_16x16x32_bf16 v[24:27], a[48:51], a[148:151], v[24:27]\n"
    "v_and_b32 v247, s58, v247\n"
    "v_mul_f32 v244, v244, v20\n"
    "v_mfma_f32_16x16x32_bf16 v[24:27], v[136:139], a[180:183], v[24:27]\n"
    "s_waitcnt vmcnt(5)\n"
    "v_mul_f32 v246, v246, v22\n"
    "v_mul_f32 v245, v245, v21\n"
    "v_mfma_f32_16x16x32_bf16 v[24:27], v[100:103], a[176:179], v[24:27]\n"
    "s_waitcnt vmcnt(4)\n"
    "ds_read_b128 a[128:131], v241 offset:52224\n"
    "v_mul_f32 v247, v247, v23\n"
    "v_mfma_f32_16x16x32_bf16 v[24:27], a[8:11], a[144:147], v[24:27]\n"
    "v_cvt_pk_bf16_f32 v18, v244, v245\n"
    "v_cvt_pk_bf16_f32 v19, v246, v247\n"
    "v_mfma_f32_16x16x32_bf16 v[28:31], v[228:231], a[156:159], v[28:31]\n"
    "ds_write_b128 v248, v[16:19] offset:0\n"
    "v_mfma_f32_16x16x32_bf16 v[28:31], v[236:239], a[188:191], v[28:31]\n"
    "v_accvgpr_read_b32 v245, a96\n"
    "v_accvgpr_read_b32 v247, a97\n"
    "v_mfma_f32_16x16x32_bf16 v[28:31], a[52:55], a[184:187], v[28:31]\n"
    "v_lshlrev_b32 v244, 16, v245\n"
    "v_lshlrev_b32 v246, 16, v247\n"
    "v_mfma_f32_16x16x32_bf16 v[28:31], v[168:171], a[152:155], v[28:31]\n"
    "v_and_b32 v245, s58, v245\n"
    "v_and_b32 v247, s58, v247\n"
    "v_mfma_f32_16x16x32_bf16 v[28:31], v[132:135], a[148:151], v[28:31]\n"
    "v_mul_f32 v244, v244, v24\n"
    "v_mul_f32 v246, v246, v26\n"
    "v_mfma_f32_16x16x32_bf16 v[28:31], a[12:15], a[180:183], v[28:31]\n"
    "s_waitcnt vmcnt(3)\n"
    "v_mul_f32 v245, v245, v25\n"
    "v_mul_f32 v247, v247, v27\n"
    "v_mfma_f32_16x16x32_bf16 v[28:31], v[64:67], a[176:179], v[28:31]\n"
    "s_waitcnt vmcnt(2)\n"
    "v_cvt_pk_bf16_f32 v24, v244, v245\n"
    "v_cvt_pk_bf16_f32 v25, v246, v247\n"
    "v_mfma_f32_16x16x32_bf16 v[28:31], v[108:111], a[144:147], v[28:31]\n"
    "s_waitcnt lgkmcnt(8)\n"
    "v_mfma_f32_16x16x32_bf16 v[48:51], a[28:31], a[140:143], v[48:51]\n"
    "s_waitcnt lgkmcnt(7)\n"
    "v_mfma_f32_16x16x32_bf16 v[48:51], v[224:227], a[172:175], v[48:51]\n"
    "s_waitcnt lgkmcnt(6)\n"
    "ds_read_b128 a[156:159], v241 offset:4096\n"
    "v_accvgpr_read_b32 v245, a98\n"
    "v_mfma_f32_16x16x32_bf16 v[48:51], a[56:59], a[168:171], v[48:51]\n"
    "s_waitcnt lgkmcnt(6)\n"
    "v_accvgpr_read_b32 v247, a99\n"
    "v_lshlrev_b32 v244, 16, v245\n"
    "v_mfma_f32_16x16x32_bf16 v[48:51], v[200:203], a[136:139], v[48:51]\n"
    "s_waitcnt lgkmcnt(5)\n"
    "v_lshlrev_b32 v246, 16, v247\n"
    "v_and_b32 v245, s58, v245\n"
    "v_mfma_f32_16x16x32_bf16 v[48:51], v[164:167], a[132:135], v[48:51]\n"
    "s_waitcnt lgkmcnt(4)\n"
    "ds_read_b128 a[188:191], v241 offset:5120\n"
    "v_and_b32 v247, s58, v247\n"
    "v_mfma_f32_16x16x32_bf16 v[48:51], a[16:19], a[164:167], v[48:51]\n"
    "s_waitcnt lgkmcnt(4)\n"
    "v_mul_f32 v244, v244, v28\n"
    "v_mul_f32 v246, v246, v30\n"
    "v_mfma_f32_16x16x32_bf16 v[48:51], v[96:99], a[160:163], v[48:51]\n"
    "s_waitcnt lgkmcnt(3)\n"
    "v_mul_f32 v245, v245, v29\n"
    "v_mul_f32 v247, v247, v31\n"
    "v_mfma_f32_16x16x32_bf16 v[48:51], v[140:143], a[128:131], v[48:51]\n"
    "ds_read_b128 a[184:187], v241 offset:20480\n"
    "v_cvt_pk_bf16_f32 v26, v244, v245\n"
    "v_mfma_f32_16x16x32_bf16 v[52:55], a[24:27], a[140:143], v[52:55]\n"
    "v_cvt_pk_bf16_f32 v27, v246, v247\n"
    "v_mfma_f32_16x16x32_bf16 v[52:55], v[192:195], a[172:175], v[52:55]\n"
    "ds_write_b128 v249, v[24:27] offset:0\n"
    "v_mfma_f32_16x16x32_bf16 v[52:55], v[196:199], a[168:171], v[52:55]\n"
    "ds_read_b128 v[20:23], v242 offset:0\n"
    "ds_read_b128 v[28:31], v242 offset:1024\n"
    "v_mfma_f32_16x16x32_bf16 v[52:55], a[20:23], a[136:139], v[52:55]\n"
    "ds_read_b128 a[152:155], v241 offset:21504\n"
    "v_accvgpr_read_b32 v245, a68\n"
    "v_mfma_f32_16x16x32_bf16 v[52:55], v[128:131], a[132:135], v[52:55]\n"
    "v_accvgpr_read_b32 v247, a69\n"
    "v_lshlrev_b32 v244, 16, v245\n"
    "v_mfma_f32_16x16x32_bf16 v[52:55], v[172:175], a[164:167], v[52:55]\n"
    "ds_read_b128 a[148:151], v241 offset:36864\n"
    "v_lshlrev_b32 v246, 16, v247\n"
    "v_mfma_f32_16x16x32_bf16 v[52:55], a[44:47], a[160:163], v[52:55]\n"
    "v_and_b32 v245, s58, v245\n"
    "v_and_b32 v247, s58, v247\n"
    "v_mfma_f32_16x16x32_bf16 v[52:55], v[104:107], a[128:131], v[52:55]\n"
    "s_add_u32 s63, s70, 512\n"
    "s_waitcnt lgkmcnt(3)\n"
    "v_mul_f32 v244, v244, v48\n"
    "v_mfma_f32_16x16x32_bf16 v[56:59], a[60:63], a[140:143], v[56:59]\n"
    "buffer_store_dwordx4 v[20:23], v243, s[40:43], s63 offen sc1\n"
    "ds_read_b128 a[180:183], v241 offset:37888\n"
    "v_mul_f32 v246, v246, v50\n"
    "v_mul_f32 v245, v245, v49\n"
    "v_mfma_f32_16x16x32_bf16 v[56:59], v[232:235], a[172:175], v[56:59]\n"
    "s_add_u32 s63, s70, 16896\n"
    "s_waitcnt lgkmcnt(3)\n"
    "v_mul_f32 v247, v247, v51\n"
    "v_mfma_f32_16x16x32_bf16 v[56:59], v[160:163], a[168:171], v[56:59]\n"
    "buffer_store_dwordx4 v[28:31], v243, s[40:43], s63 offen sc1\n"
    "v_cvt_pk_bf16_f32 v48, v244, v245\n"
    "v_cvt_pk_bf16_f32 v49, v246, v247\n"
    "v_accvgpr_read_b32 v245, a70\n"
    "v_mfma_f32_16x16x32_bf16 v[56:59], v[204:207], a[136:139], v[56:59]\n"
    "ds_read_b128 a[176:179], v241 offset:53248\n"
    "v_accvgpr_read_b32 v247, a71\n"
    "v_mfma_f32_16x16x32_bf16 v[56:59], a[48:51], a[132:135], v[56:59]\n"
    "v_lshlrev_b32 v244, 16, v245\n"
    "v_lshlrev_b32 v246, 16, v247\n"
    "v_mfma_f32_16x16x32_bf16 v[56:59], v[136:139], a[164:167], v[56:59]\n"
    "v_and_b32 v245, s58, v245\n"
    "v_and_b32 v247, s58, v247\n"
    "v_mfma_f32_16x16x32_bf16 v[56:59], v[100:103], a[160:163], v[56:59]\n"
    "ds_read_b128 a[144:147], v241 offset:54272\n"
    "v_mul_f32 v244, v244, v52\n"
    "v_mfma_f32_16x16x32_bf16 v[56:59], a[8:11], a[128:131], v[56:59]\n"
    "v_mul_f32 v246, v246, v54\n"
    "v_mul_f32 v245, v245, v53\n"
    "v_mfma_f32_16x16x32_bf16 v[60:63], v[228:231], a[140:143], v[60:63]\n"
    "v_mul_f32 v247, v247, v55\n"
    "v_cvt_pk_bf16_f32 v50, v244, v245\n"
    "v_mfma_f32_16x16x32_bf16 v[60:63], v[236:239], a[172:175], v[60:63]\n"
    "v_cvt_pk_bf16_f32 v51, v246, v247\n"
    "v_mfma_f32_16x16x32_bf16 v[60:63], a[52:55], a[168:171], v[60:63]\n"
    "ds_write_b128 v248, v[48:51] offset:2048\n"
    "v_mfma_f32_16x16x32_bf16 v[60:63], v[168:171], a[136:139], v[60:63]\n"
    "v_accvgpr_read_b32 v245, a100\n"
    "v_accvgpr_read_b32 v247, a101\n"
    "v_mfma_f32_16x16x32_bf16 v[60:63], v[132:135], a[132:135], v[60:63]\n"
    "v_lshlrev_b32 v244, 16, v245\n"
    "v_lshlrev_b32 v246, 16, v247\n"
    "v_mfma_f32_16x16x32_bf16 v[60:63], a[12:15], a[164:167], v[60:63]\n"
    "v_and_b32 v245, s58, v245\n"
    "v_and_b32 v247, s58, v247\n"
    "v_mfma_f32_16x16x32_bf16 v[60:63], v[64:67], a[160:163], v[60:63]\n"
    "v_mul_f32 v244, v244, v56\n"
    "v_mul_f32 v246, v246, v58\n"
    "v_mfma_f32_16x16x32_bf16 v[60:63], v[108:111], a[128:131], v[60:63]\n"
    "v_mul_f32 v245, v245, v57\n"
    "v_mul_f32 v247, v247, v59\n"
    "v_mfma_f32_16x16x32_bf16 v[80:83], a[28:31], a[156:159], v[80:83]\n"
    "v_cvt_pk_bf16_f32 v56, v244, v245\n"
    "v_cvt_pk_bf16_f32 v57, v246, v247\n"
    "v_mfma_f32_16x16x32_bf16 v[80:83], v[224:227], a[188:191], v[80:83]\n"
    "ds_read_b128 a[140:143], v241 offset:14336\n"
    "v_accvgpr_read_b32 v245, a102\n"
    "v_mfma_f32_16x16x32_bf16 v[80:83], a[56:59], a[184:187], v[80:83]\n"
    "s_waitcnt lgkmcnt(6)\n"
    "v_accvgpr_read_b32 v247, a103\n"
    "v_lshlrev_b32 v244, 16, v245\n"
    "v_mfma_f32_16x16x32_bf16 v[80:83], v[200:203], a[152:155], v[80:83]\n"
    "s_waitcnt lgkmcnt(5)\n"
    "v_lshlrev_b32 v246, 16, v247\n"
    "v_and_b32 v245, s58, v245\n"
    "v_mfma_f32_16x16x32_bf16 v[80:83], v[164:167], a[148:151], v[80:83]\n"
    "s_waitcnt lgkmcnt(4)\n"
    "ds_read_b128 a[172:175], v241 offset:15360\n"
    "v_and_b32 v247, s58, v247\n"
    "v_mfma_f32_16x16x32_bf16 v[80:83], a[16:19], a[180:183], v[80:83]\n"
    "s_waitcnt lgkmcnt(4)\n"
    "v_mul_f32 v244, v244, v60\n"
    "v_mul_f32 v246, v246, v62\n"
    "v_mfma_f32_16x16x32_bf16 v[80:83], v[96:99], a[176:179], v[80:83]\n"
    "s_waitcnt lgkmcnt(3)\n"
    "v_mul_f32 v245, v245, v61\n"
    "v_mul_f32 v247, v247, v63\n"
    "v_mfma_f32_16x16x32_bf16 v[80:83], v[140:143], a[144:147], v[80:83]\n"
    "ds_read_b128 a[168:171], v241 offset:30720\n"
    "v_cvt_pk_bf16_f32 v58, v244, v245\n"
    "v_mfma_f32_16x16x32_bf16 v[84:87], a[24:27], a[156:159], v[84:87]\n"
    "v_cvt_pk_bf16_f32 v59, v246, v247\n"
    "v_mfma_f32_16x16x32_bf16 v[84:87], v[192:195], a[188:191], v[84:87]\n"
    "ds_write_b128 v249, v[56:59] offset:2048\n"
    "v_mfma_f32_16x16x32_bf16 v[84:87], v[196:199], a[184:187], v[84:87]\n"
    "ds_read_b128 v[52:55], v242 offset:2048\n"
    "ds_read_b128 v[60:63], v242 offset:3072\n"
    "v_mfma_f32_16x16x32_bf16 v[84:87], a[20:23], a[152:155], v[84:87]\n"
    "ds_read_b128 a[136:139], v241 offset:31744\n"
    "v_accvgpr_read_b32 v245, a72\n"
    "v_mfma_f32_16x16x32_bf16 v[84:87], v[128:131], a[148:151], v[84:87]\n"
    "v_accvgpr_read_b32 v247, a73\n"
    "v_lshlrev_b32 v244, 16, v245\n"
    "v_mfma_f32_16x16x32_bf16 v[84:87], v[172:175], a[180:183], v[84:87]\n"
    "ds_read_b128 a[132:135], v241 offset:47104\n"
    "v_lshlrev_b32 v246, 16, v247\n"
    "v_mfma_f32_16x16x32_bf16 v[84:87], a[44:47], a[176:179], v[84:87]\n"
    "v_and_b32 v245, s58, v245\n"
    "v_and_b32 v247, s58, v247\n"
    "v_mfma_f32_16x16x32_bf16 v[84:87], v[104:107], a[144:147], v[84:87]\n"
    "s_add_u32 s63, s70, 33280\n"
    "s_waitcnt lgkmcnt(3)\n"
    "v_mul_f32 v244, v244, v80\n"
    "v_mfma_f32_16x16x32_bf16 v[88:91], a[60:63], a[156:159], v[88:91]\n"
    "buffer_store_dwordx4 v[52:55], v243, s[40:43], s63 offen sc1\n"
    "ds_read_b128 a[164:167], v241 offset:48128\n"
    "v_mul_f32 v246, v246, v82\n"
    "v_mul_f32 v245, v245, v81\n"
    "v_mfma_f32_16x16x32_bf16 v[88:91], v[232:235], a[188:191], v[88:91]\n"
    "s_add_u32 s63, s70, 49664\n"
    "s_waitcnt lgkmcnt(3)\n"
    "v_mul_f32 v247, v247, v83\n"
    "v_mfma_f32_16x16x32_bf16 v[88:91], v[160:163], a[184:187], v[88:91]\n"
    "buffer_store_dwordx4 v[60:63], v243, s[40:43], s63 offen sc1\n"
    "v_cvt_pk_bf16_f32 v80, v244, v245\n"
    "v_cvt_pk_bf16_f32 v81, v246, v247\n"
    "v_accvgpr_read_b32 v245, a74\n"
    "v_mfma_f32_16x16x32_bf16 v[88:91], v[204:207], a[152:155], v[88:91]\n"
    "ds_read_b128 a[160:163], v241 offset:63488\n"
    "v_accvgpr_read_b32 v247, a75\n"
    "v_mfma_f32_16x16x32_bf16 v[88:91], a[48:51], a[148:151], v[88:91]\n"
    "v_lshlrev_b32 v244, 16, v245\n"
    "v_lshlrev_b32 v246, 16, v247\n"
    "v_mfma_f32_16x16x32_bf16 v[88:91], v[136:139], a[180:183], v[88:91]\n"
    "v_and_b32 v245, s58, v245\n"
    "v_and_b32 v247, s58, v247\n"
    "v_mfma_f32_16x16x32_bf16 v[88:91], v[100:103], a[176:179], v[88:91]\n"
    "ds_read_b128 a[128:131], v241 offset:64512\n"
    "v_mul_f32 v244, v244, v84\n"
    "v_mfma_f32_16x16x32_bf16 v[88:91], a[8:11], a[144:147], v[88:91]\n"
    "v_mul_f32 v246, v246, v86\n"
    "v_mul_f32 v245, v245, v85\n"
    "v_mfma_f32_16x16x32_bf16 v[92:95], v[228:231], a[156:159], v[92:95]\n"
    "v_mul_f32 v247, v247, v87\n"
    "v_cvt_pk_bf16_f32 v82, v244, v245\n"
    "v_mfma_f32_16x16x32_bf16 v[92:95], v[236:239], a[188:191], v[92:95]\n"
    "v_cvt_pk_bf16_f32 v83, v246, v247\n"
    "v_mfma_f32_16x16x32_bf16 v[92:95], a[52:55], a[184:187], v[92:95]\n"
    "ds_write_b128 v248, v[80:83] offset:0\n"
    "v_mfma_f32_16x16x32_bf16 v[92:95], v[168:171], a[152:155], v[92:95]\n"
    "v_accvgpr_read_b32 v245, a104\n"
    "v_accvgpr_read_b32 v247, a105\n"
    "v_mfma_f32_16x16x32_bf16 v[92:95], v[132:135], a[148:151], v[92:95]\n"
    "v_lshlrev_b32 v244, 16, v245\n"
    "v_lshlrev_b32 v246, 16, v247\n"
    "v_mfma_f32_16x16x32_bf16 v[92:95], a[12:15], a[180:183], v[92:95]\n"
    "v_and_b32 v245, s58, v245\n"
    "v_and_b32 v247, s58, v247\n"
    "v_mfma_f32_16x16x32_bf16 v[92:95], v[64:67], a[176:179], v[92:95]\n"
    "v_mul_f32 v244, v244, v88\n"
    "v_mul_f32 v246, v246, v90\n"
    "v_mfma_f32_16x16x32_bf16 v[92:95], v[108:111], a[144:147], v[92:95]\n"
    "v_mul_f32 v245, v245, v89\n"
    "v_mul_f32 v247, v247, v91\n"
    "v_mfma_f32_16x16x32_bf16 a[240:243], a[28:31], a[140:143], a[240:243]\n"
    "v_cvt_pk_bf16_f32 v88, v244, v245\n"
    "v_cvt_pk_bf16_f32 v89, v246, v247\n"
    "v_mfma_f32_16x16x32_bf16 a[240:243], v[224:227], a[172:175], a[240:243]\n"
    "ds_read_b128 a[156:159], v241 offset:6144\n"
    "v_accvgpr_read_b32 v245, a106\n"
    "v_mfma_f32_16x16x32_bf16 a[240:243], a[56:59], a[168:171], a[240:243]\n"
    "s_waitcnt lgkmcnt(6)\n"
    "v_accvgpr_read_b32 v247, a107\n"
    "v_lshlrev_b32 v244, 16, v245\n"
    "v_mfma_f32_16x16x32_bf16 a[240:243], v[200:203], a[136:139], a[240:243]\n"
    "s_waitcnt lgkmcnt(5)\n"
    "v_lshlrev_b32 v246, 16, v247\n"
    "v_and_b32 v245, s58, v245\n"
    "v_mfma_f32_16x16x32_bf16 a[240:243], v[164:167], a[132:135], a[240:243]\n"
    "s_waitcnt lgkmcnt(4)\n"
    "ds_read_b128 a[188:191], v241 offset:7168\n"
    "v_and_b32 v247, s58, v247\n"
    "v_mfma_f32_16x16x32_bf16 a[240:243], a[16:19], a[164:167], a[240:243]\n"
    "s_waitcnt lgkmcnt(4)\n"
    "v_mul_f32 v244, v244, v92\n"
    "v_mul_f32 v246, v246, v94\n"
    "v_mfma_f32_16x16x32_bf16 a[240:243], v[96:99], a[160:163], a[240:243]\n"
    "s_waitcnt lgkmcnt(3)\n"
    "v_mul_f32 v245, v245, v93\n"
    "v_mul_f32 v247, v247, v95\n"
    "v_mfma_f32_16x16x32_bf16 a[240:243], v[140:143], a[128:131], a[240:243]\n"
    "ds_read_b128 a[184:187], v241 offset:22528\n"
    "v_cvt_pk_bf16_f32 v90, v244, v245\n"
    "v_mfma_f32_16x16x32_bf16 a[244:247], a[24:27], a[140:143], a[244:247]\n"
    "v_cvt_pk_bf16_f32 v91, v246, v247\n"
    "v_mfma_f32_16x16x32_bf16 a[244:247], v[192:195], a[172:175], a[244:247]\n"
    "ds_write_b128 v249, v[88:91] offset:0\n"
    "v_mfma_f32_16x16x32_bf16 a[244:247], v[196:199], a[168:171], a[244:247]\n"
    "ds_read_b128 v[84:87], v242 offset:0\n"
    "ds_read_b128 v[92:95], v242 offset:1024\n"
    "v_mfma_f32_16x16x32_bf16 a[244:247], a[20:23], a[136:139], a[244:247]\n"
    "ds_read_b128 a[152:155], v241 offset:23552\n"
    "v_accvgpr_read_b32 v245, a92\n"
    "v_mfma_f32_16x16x32_bf16 a[244:247], v[128:131], a[132:135], a[244:247]\n"
    "v_accvgpr_read_b32 v247, a93\n"
    "v_lshlrev_b32 v244, 16, v245\n"
    "v_mfma_f32_16x16x32_bf16 a[244:247], v[172:175], a[164:167], a[244:247]\n"
    "ds_read_b128 a[148:151], v241 offset:38912\n"
    "v_lshlrev_b32 v246, 16, v247\n"
    "v_mfma_f32_16x16x32_bf16 a[244:247], a[44:47], a[160:163], a[244:247]\n"
    "v_and_b32 v245, s58, v245\n"
    "v_and_b32 v247, s58, v247\n"
    "v_mfma_f32_16x16x32_bf16 a[244:247], v[104:107], a[128:131], a[244:247]\n"
    "s_add_u32 s63, s70, 66048\n"
    "s_waitcnt lgkmcnt(3)\n"
    "v_accvgpr_read_b32 v254, a240\n"
    "v_mfma_f32_16x16x32_bf16 a[248:251], a[60:63], a[140:143], a[248:251]\n"
    "buffer_store_dwordx4 v[84:87], v243, s[40:43], s63 offen sc1\n"
    "ds_read_b128 a[180:183], v241 offset:39936\n"
    "v_accvgpr_read_b32 v255, a242\n"
    "v_mul_f32 v244, v244, v254\n"
    "v_mfma_f32_16x16x32_bf16 a[248:251], v[232:235], a[172:175], a[248:251]\n"
    "s_add_u32 s63, s70, 82432\n"
    "s_waitcnt lgkmcnt(3)\n"
    "v_mul_f32 v246, v246, v255\n"
    "v_mfma_f32_16x16x32_bf16 a[248:251], v[160:163], a[168:171], a[248:251]\n"
    "buffer_store_dwordx4 v[92:95], v243, s[40:43], s63 offen sc1\n"
    "v_accvgpr_read_b32 v254, a241\n"
    "v_accvgpr_read_b32 v255, a243\n"
    "v_mul_f32 v245, v245, v254\n"
    "v_mfma_f32_16x16x32_bf16 a[248:251], v[204:207], a[136:139], a[248:251]\n"
    "ds_read_b128 a[176:179], v241 offset:55296\n"
    "v_mul_f32 v247, v247, v255\n"
    "v_mfma_f32_16x16x32_bf16 a[248:251], a[48:51], a[132:135], a[248:251]\n"
    "v_cvt_pk_bf16_f32 v16, v244, v245\n"
    "v_cvt_pk_bf16_f32 v17, v246, v247\n"
    "v_mfma_f32_16x16x32_bf16 a[248:251], v[136:139], a[164:167], a[248:251]\n"
    "v_accvgpr_read_b32 v245, a94\n"
    "v_accvgpr_read_b32 v247, a95\n"
    "v_mfma_f32_16x16x32_bf16 a[248:251], v[100:103], a[160:163], a[248:251]\n"
    "ds_read_b128 a[144:147], v241 offset:56320\n"
    "v_lshlrev_b32 v244, 16, v245\n"
    "v_mfma_f32_16x16x32_bf16 a[248:251], a[8:11], a[128:131], a[248:251]\n"
    "v_lshlrev_b32 v246, 16, v247\n"
    "v_and_b32 v245, s58, v245\n"
    "v_mfma_f32_16x16x32_bf16 a[252:255], v[228:231], a[140:143], a[252:255]\n"
    "v_and_b32 v247, s58, v247\n"
    "v_accvgpr_read_b32 v254, a244\n"
    "v_mfma_f32_16x16x32_bf16 a[252:255], v[236:239], a[172:175], a[252:255]\n"
    "v_accvgpr_read_b32 v255, a246\n"
    "v_mul_f32 v244, v244, v254\n"
    "v_mfma_f32_16x16x32_bf16 a[252:255], a[52:55], a[168:171], a[252:255]\n"
    "v_mul_f32 v246, v246, v255\n"
    "v_accvgpr_read_b32 v254, a245\n"
    "v_mfma_f32_16x16x32_bf16 a[252:255], v[168:171], a[136:139], a[252:255]\n"
    "v_accvgpr_read_b32 v255, a247\n"
    "v_mul_f32 v245, v245, v254\n"
    "v_mfma_f32_16x16x32_bf16 a[252:255], v[132:135], a[132:135], a[252:255]\n"
    "v_mul_f32 v247, v247, v255\n"
    "v_cvt_pk_bf16_f32 v18, v244, v245\n"
    "v_mfma_f32_16x16x32_bf16 a[252:255], a[12:15], a[164:167], a[252:255]\n"
    "v_cvt_pk_bf16_f32 v19, v246, v247\n"
    "v_mfma_f32_16x16x32_bf16 a[252:255], v[64:67], a[160:163], a[252:255]\n"
    "ds_write_b128 v248, v[16:19] offset:2048\n"
    "v_mfma_f32_16x16x32_bf16 a[252:255], v[108:111], a[128:131], a[252:255]\n"
    "v_accvgpr_read_b32 v245, a124\n"
    "v_accvgpr_read_b32 v247, a125\n"
    "v_mfma_f32_16x16x32_bf16 v[112:115], a[28:31], a[156:159], v[112:115]\n"
    "v_lshlrev_b32 v244, 16, v245\n"
    "v_lshlrev_b32 v246, 16, v247\n"
    "v_mfma_f32_16x16x32_bf16 v[112:115], v[224:227], a[188:191], v[112:115]\n"
    "ds_read_b128 a[140:143], v241 offset:8192\n"
    "v_and_b32 v245, s58, v245\n"
    "v_mfma_f32_16x16x32_bf16 v[112:115], a[56:59], a[184:187], v[112:115]\n"
    "s_waitcnt lgkmcnt(6)\n"
    "v_and_b32 v247, s58, v247\n"
    "v_accvgpr_read_b32 v254, a248\n"
    "v_mfma_f32_16x16x32_bf16 v[112:115], v[200:203], a[152:155], v[112:115]\n"
    "s_waitcnt lgkmcnt(5)\n"
    "v_accvgpr_read_b32 v255, a250\n"
    "v_mul_f32 v244, v244, v254\n"
    "v_mfma_f32_16x16x32_bf16 v[112:115], v[164:167], a[148:151], v[112:115]\n"
    "s_waitcnt lgkmcnt(4)\n"
    "ds_read_b128 a[172:175], v241 offset:9216\n"
    "v_mul_f32 v246, v246, v255\n"
    "v_mfma_f32_16x16x32_bf16 v[112:115], a[16:19], a[180:183], v[112:115]\n"
    "s_waitcnt lgkmcnt(4)\n"
    "v_accvgpr_read_b32 v254, a249\n"
    "v_accvgpr_read_b32 v255, a251\n"
    "v_mfma_f32_16x16x32_bf16 v[112:115], v[96:99], a[176:179], v[112:115]\n"
    "s_waitcnt lgkmcnt(3)\n"
    "v_mul_f32 v245, v245, v254\n"
    "v_mul_f32 v247, v247, v255\n"
    "v_mfma_f32_16x16x32_bf16 v[112:115], v[140:143], a[144:147], v[112:115]\n"
    "ds_read_b128 a[168:171], v241 offset:24576\n"
    "v_cvt_pk_bf16_f32 v24, v244, v245\n"
    "v_mfma_f32_16x16x32_bf16 v[116:119], a[24:27], a[156:159], v[116:119]\n"
    "v_cvt_pk_bf16_f32 v25, v246, v247\n"
    "v_accvgpr_read_b32 v245, a126\n"
    "v_mfma_f32_16x16x32_bf16 v[116:119], v[192:195], a[188:191], v[116:119]\n"
    "v_accvgpr_read_b32 v247, a127\n"
    "v_lshlrev_b32 v244, 16, v245\n"
    "v_mfma_f32_16x16x32_bf16 v[116:119], v[196:199], a[184:187], v[116:119]\n"
    "ds_read_b128 a[136:139], v241 offset:25600\n"
    "v_lshlrev_b32 v246, 16, v247\n"
    "v_mfma_f32_16x16x32_bf16 v[116:119], a[20:23], a[152:155], v[116:119]\n"
    "v_and_b32 v245, s58, v245\n"
    "v_and_b32 v247, s58, v247\n"
    "v_mfma_f32_16x16x32_bf16 v[116:119], v[128:131], a[148:151], v[116:119]\n"
    "v_accvgpr_read_b32 v254, a252\n"
    "v_accvgpr_read_b32 v255, a254\n"
    "v_mfma_f32_16x16x32_bf16 v[116:119], v[172:175], a[180:183], v[116:119]\n"
    "ds_read_b128 a[132:135], v241 offset:40960\n"
    "v_mul_f32 v244, v244, v254\n"
    "v_mfma_f32_16x16x32_bf16 v[116:119], a[44:47], a[176:179], v[116:119]\n"
    "v_mul_f32 v246, v246, v255\n"
    "v_accvgpr_read_b32 v254, a253\n"
    "v_mfma_f32_16x16x32_bf16 v[116:119], v[104:107], a[144:147], v[116:119]\n"
    "v_accvgpr_read_b32 v255, a255\n"
    "v_mul_f32 v245, v245, v254\n"
    "v_mfma_f32_16x16x32_bf16 v[120:123], a[60:63], a[156:159], v[120:123]\n"
    "ds_read_b128 a[164:167], v241 offset:41984\n"
    "v_mul_f32 v247, v247, v255\n"
    "v_mfma_f32_16x16x32_bf16 v[120:123], v[232:235], a[188:191], v[120:123]\n"
    "v_cvt_pk_bf16_f32 v26, v244, v245\n"
    "v_cvt_pk_bf16_f32 v27, v246, v247\n"
    "v_mfma_f32_16x16x32_bf16 v[120:123], v[160:163], a[184:187], v[120:123]\n"
    "ds_write_b128 v249, v[24:27] offset:2048\n"
    "v_mfma_f32_16x16x32_bf16 v[120:123], v[204:207], a[152:155], v[120:123]\n"
    "ds_read_b128 a[244:247], v242 offset:2048\n"
    "ds_read_b128 a[252:255], v242 offset:3072\n"
    "v_mfma_f32_16x16x32_bf16 v[120:123], a[48:51], a[148:151], v[120:123]\n"
    "ds_read_b128 a[160:163], v241 offset:57344\n"
    "v_accvgpr_read_b32 v245, a76\n"
    "v_mfma_f32_16x16x32_bf16 v[120:123], v[136:139], a[180:183], v[120:123]\n"
    "v_accvgpr_read_b32 v247, a77\n"
    "v_lshlrev_b32 v244, 16, v245\n"
    "v_mfma_f32_16x16x32_bf16 v[120:123], v[100:103], a[176:179], v[120:123]\n"
    "ds_read_b128 a[128:131], v241 offset:58368\n"
    "v_lshlrev_b32 v246, 16, v247\n"
    "v_mfma_f32_16x16x32_bf16 v[120:123], a[8:11], a[144:147], v[120:123]\n"
    "v_and_b32 v245, s58, v245\n"
    "v_and_b32 v247, s58, v247\n"
    "v_mfma_f32_16x16x32_bf16 v[124:127], v[228:231], a[156:159], v[124:127]\n"
    "v_mul_f32 v244, v244, v112\n"
    "v_mul_f32 v246, v246, v114\n"
    "v_mfma_f32_16x16x32_bf16 v[124:127], v[236:239], a[188:191], v[124:127]\n"
    "s_add_u32 s63, s70, 229888\n"
    "s_waitcnt lgkmcnt(3)\n"
    "v_mul_f32 v245, v245, v113\n"
    "v_mfma_f32_16x16x32_bf16 v[124:127], a[52:55], a[184:187], v[124:127]\n"
    "buffer_store_dwordx4 a[244:247], v243, s[40:43], s63 offen sc1\n"
    "v_mul_f32 v247, v247, v115\n"
    "v_cvt_pk_bf16_f32 v112, v244, v245\n"
    "v_cvt_pk_bf16_f32 v113, v246, v247\n"
    "v_mfma_f32_16x16x32_bf16 v[124:127], v[168:171], a[152:155], v[124:127]\n"
    "v_accvgpr_read_b32 v245, a78\n"
    "v_accvgpr_read_b32 v247, a79\n"
    "v_mfma_f32_16x16x32_bf16 v[124:127], v[132:135], a[148:151], v[124:127]\n"
    "v_lshlrev_b32 v244, 16, v245\n"
    "v_lshlrev_b32 v246, 16, v247\n"
    "v_mfma_f32_16x16x32_bf16 v[124:127], a[12:15], a[180:183], v[124:127]\n"
    "v_and_b32 v245, s58, v245\n"
    "v_and_b32 v247, s58, v247\n"
    "v_mfma_f32_16x16x32_bf16 v[124:127], v[64:67], a[176:179], v[124:127]\n"
    "v_mul_f32 v244, v244, v116\n"
    "v_mul_f32 v246, v246, v118\n"
    "v_mfma_f32_16x16x32_bf16 v[124:127], v[108:111], a[144:147], v[124:127]\n"
    "s_add_u32 s63, s70, 246272\n"
    "s_waitcnt lgkmcnt(2)\n"
    "v_mul_f32 v245, v245, v117\n"
    "v_mfma_f32_16x16x32_bf16 v[144:147], a[28:31], a[140:143], v[144:147]\n"
    "buffer_store_dwordx4 a[252:255], v243, s[40:43], s63 offen sc1\n"
    "v_mul_f32 v247, v247, v119\n"
    "v_cvt_pk_bf16_f32 v114, v244, v245\n"
    "v_cvt_pk_bf16_f32 v115, v246, v247\n"
    "v_mfma_f32_16x16x32_bf16 v[144:147], v[224:227], a[172:175], v[144:147]\n"
    "ds_write_b128 v248, v[112:115] offset:2048\n"
    "v_mfma_f32_16x16x32_bf16 v[144:147], a[56:59], a[168:171], v[144:147]\n"
    "ds_read_b128 a[156:159], v241 offset:10240\n"
    "v_accvgpr_read_b32 v245, a108\n"
    "v_mfma_f32_16x16x32_bf16 v[144:147], v[200:203], a[136:139], v[144:147]\n"
    "v_accvgpr_read_b32 v247, a109\n"
    "v_lshlrev_b32 v244, 16, v245\n"
    "v_mfma_f32_16x16x32_bf16 v[144:147], v[164:167], a[132:135], v[144:147]\n"
    "ds_read_b128 a[188:191], v241 offset:11264\n"
    "v_lshlrev_b32 v246, 16, v247\n"
    "v_mfma_f32_16x16x32_bf16 v[144:147], a[16:19], a[164:167], v[144:147]\n"
    "s_waitcnt lgkmcnt(4)\n"
    "v_and_b32 v245, s58, v245\n"
    "v_and_b32 v247, s58, v247\n"
    "v_mfma_f32_16x16x32_bf16 v[144:147], v[96:99], a[160:163], v[144:147]\n"
    "s_waitcnt lgkmcnt(3)\n"
    "v_mul_f32 v244, v244, v120\n"
    "v_mul_f32 v246, v246, v122\n"
    "v_mfma_f32_16x16x32_bf16 v[144:147], v[140:143], a[128:131], v[144:147]\n"
    "ds_read_b128 a[184:187], v241 offset:26624\n"
    "v_mul_f32 v245, v245, v121\n"
    "v_mfma_f32_16x16x32_bf16 v[148:151], a[24:27], a[140:143], v[148:151]\n"
    "v_mul_f32 v247, v247, v123\n"
    "v_cvt_pk_bf16_f32 v120, v244, v245\n"
    "v_mfma_f32_16x16x32_bf16 v[148:151], v[192:195], a[172:175], v[148:151]\n"
    "v_cvt_pk_bf16_f32 v121, v246, v247\n"
    "v_accvgpr_read_b32 v245, a110\n"
    "v_mfma_f32_16x16x32_bf16 v[148:151], v[196:199], a[168:171], v[148:151]\n"
    "ds_read_b128 a[152:155], v241 offset:27648\n"
    "v_accvgpr_read_b32 v247, a111\n"
    "v_mfma_f32_16x16x32_bf16 v[148:151], a[20:23], a[136:139], v[148:151]\n"
    "v_lshlrev_b32 v244, 16, v245\n"
    "v_lshlrev_b32 v246, 16, v247\n"
    "v_mfma_f32_16x16x32_bf16 v[148:151], v[128:131], a[132:135], v[148:151]\n"
    "v_and_b32 v245, s58, v245\n"
    "v_and_b32 v247, s58, v247\n"
    "v_mfma_f32_16x16x32_bf16 v[148:151], v[172:175], a[164:167], v[148:151]\n"
    "ds_read_b128 a[148:151], v241 offset:43008\n"
    "v_mul_f32 v244, v244, v124\n"
    "v_mfma_f32_16x16x32_bf16 v[148:151], a[44:47], a[160:163], v[148:151]\n"
    "v_mul_f32 v246, v246, v126\n"
    "v_mul_f32 v245, v245, v125\n"
    "v_mfma_f32_16x16x32_bf16 v[148:151], v[104:107], a[128:131], v[148:151]\n"
    "v_mul_f32 v247, v247, v127\n"
    "v_cvt_pk_bf16_f32 v122, v244, v245\n"
    "v_mfma_f32_16x16x32_bf16 v[152:155], a[60:63], a[140:143], v[152:155]\n"
    "ds_read_b128 a[180:183], v241 offset:44032\n"
    "v_cvt_pk_bf16_f32 v123, v246, v247\n"
    "v_mfma_f32_16x16x32_bf16 v[152:155], v[232:235], a[172:175], v[152:155]\n"
    "ds_write_b128 v249, v[120:123] offset:2048\n"
    "v_mfma_f32_16x16x32_bf16 v[152:155], v[160:163], a[168:171], v[152:155]\n"
    "ds_read_b128 v[116:119], v242 offset:2048\n"
    "ds_read_b128 v[124:127], v242 offset:3072\n"
    "v_mfma_f32_16x16x32_bf16 v[152:155], v[204:207], a[136:139], v[152:155]\n"
    "ds_read_b128 a[176:179], v241 offset:59392\n"
    "v_accvgpr_read_b32 v245, a80\n"
    "v_mfma_f32_16x16x32_bf16 v[152:155], a[48:51], a[132:135], v[152:155]\n"
    "v_accvgpr_read_b32 v247, a81\n"
    "v_lshlrev_b32 v244, 16, v245\n"
    "v_mfma_f32_16x16x32_bf16 v[152:155], v[136:139], a[164:167], v[152:155]\n"
    "v_lshlrev_b32 v246, 16, v247\n"
    "v_and_b32 v245, s58, v245\n"
    "v_mfma_f32_16x16x32_bf16 v[152:155], v[100:103], a[160:163], v[152:155]\n"
    "ds_read_b128 a[144:147], v241 offset:60416\n"
    "v_and_b32 v247, s58, v247\n"
    "v_mfma_f32_16x16x32_bf16 v[152:155], a[8:11], a[128:131], v[152:155]\n"
    "s_add_u32 s63, s70, 98816\n"
    "s_waitcnt lgkmcnt(3)\n"
    "v_mul_f32 v244, v244, v144\n"
    "v_mfma_f32_16x16x32_bf16 v[156:159], v[228:231], a[140:143], v[156:159]\n"
    "buffer_store_dwordx4 v[116:119], v243, s[40:43], s63 offen sc1\n"
    "v_mul_f32 v246, v246, v146\n"
    "v_mul_f32 v245, v245, v145\n"
    "v_mul_f32 v247, v247, v147\n"
    "v_mfma_f32_16x16x32_bf16 v[156:159], v[236:239], a[172:175], v[156:159]\n"
    "v_cvt_pk_bf16_f32 v144, v244, v245\n"
    "v_cvt_pk_bf16_f32 v145, v246, v247\n"
    "v_mfma_f32_16x16x32_bf16 v[156:159], a[52:55], a[168:171], v[156:159]\n"
    "v_accvgpr_read_b32 v245, a82\n"
    "v_accvgpr_read_b32 v247, a83\n"
    "v_mfma_f32_16x16x32_bf16 v[156:159], v[168:171], a[136:139], v[156:159]\n"
    "v_lshlrev_b32 v244, 16, v245\n"
    "v_lshlrev_b32 v246, 16, v247\n"
    "v_mfma_f32_16x16x32_bf16 v[156:159], v[132:135], a[132:135], v[156:159]\n"
    "v_and_b32 v245, s58, v245\n"
    "v_and_b32 v247, s58, v247\n"
    "v_mfma_f32_16x16x32_bf16 v[156:159], a[12:15], a[164:167], v[156:159]\n"
    "s_add_u32 s63, s70, 115200\n"
    "s_waitcnt lgkmcnt(2)\n"
    "v_mul_f32 v244, v244, v148\n"
    "v_mfma_f32_16x16x32_bf16 v[156:159], v[64:67], a[160:163], v[156:159]\n"
    "buffer_store_dwordx4 v[124:127], v243, s[40:43], s63 offen sc1\n"
    "v_mul_f32 v246, v246, v150\n"
    "v_mul_f32 v245, v245, v149\n"
    "v_mul_f32 v247, v247, v151\n"
    "v_mfma_f32_16x16x32_bf16 v[156:159], v[108:111], a[128:131], v[156:159]\n"
    "v_cvt_pk_bf16_f32 v146, v244, v245\n"
    "v_cvt_pk_bf16_f32 v147, v246, v247\n"
    "v_mfma_f32_16x16x32_bf16 v[176:179], a[28:31], a[156:159], v[176:179]\n"
    "ds_write_b128 v248, v[144:147] offset:0\n"
    "v_mfma_f32_16x16x32_bf16 v[176:179], v[224:227], a[188:191], v[176:179]\n"
    "ds_read_b128 a[140:143], v241 offset:12288\n"
    "v_accvgpr_read_b32 v245, a112\n"
    "v_mfma_f32_16x16x32_bf16 v[176:179], a[56:59], a[184:187], v[176:179]\n"
    "v_accvgpr_read_b32 v247, a113\n"
    "v_lshlrev_b32 v244, 16, v245\n"
    "v_mfma_f32_16x16x32_bf16 v[176:179], v[200:203], a[152:155], v[176:179]\n"
    "v_lshlrev_b32 v246, 16, v247\n"
    "v_and_b32 v245, s58, v245\n"
    "v_mfma_f32_16x16x32_bf16 v[176:179], v[164:167], a[148:151], v[176:179]\n"
    "ds_read_b128 a[172:175], v241 offset:13312\n"
    "v_and_b32 v247, s58, v247\n"
    "v_mfma_f32_16x16x32_bf16 v[176:179], a[16:19], a[180:183], v[176:179]\n"
    "s_waitcnt lgkmcnt(4)\n"
    "v_mul_f32 v244, v244, v152\n"
    "v_mul_f32 v246, v246, v154\n"
    "v_mfma_f32_16x16x32_bf16 v[176:179], v[96:99], a[176:179], v[176:179]\n"
    "s_waitcnt lgkmcnt(3)\n"
    "v_mul_f32 v245, v245, v153\n"
    "v_mul_f32 v247, v247, v155\n"
    "v_mfma_f32_16x16x32_bf16 v[176:179], v[140:143], a[144:147], v[176:179]\n"
    "ds_read_b128 a[168:171], v241 offset:28672\n"
    "v_cvt_pk_bf16_f32 v152, v244, v245\n"
    "v_mfma_f32_16x16x32_bf16 v[180:183], a[24:27], a[156:159], v[180:183]\n"
    "v_cvt_pk_bf16_f32 v153, v246, v247\n"
    "v_accvgpr_read_b32 v245, a114\n"
    "v_mfma_f32_16x16x32_bf16 v[180:183], v[192:195], a[188:191], v[180:183]\n"
    "v_accvgpr_read_b32 v247, a115\n"
    "v_lshlrev_b32 v244, 16, v245\n"
    "v_mfma_f32_16x16x32_bf16 v[180:183], v[196:199], a[184:187], v[180:183]\n"
    "ds_read_b128 a[136:139], v241 offset:29696\n"
    "v_lshlrev_b32 v246, 16, v247\n"
    "v_mfma_f32_16x16x32_bf16 v[180:183], a[20:23], a[152:155], v[180:183]\n"
    "v_and_b32 v245, s58, v245\n"
    "v_and_b32 v247, s58, v247\n"
    "v_mfma_f32_16x16x32_bf16 v[180:183], v[128:131], a[148:151], v[180:183]\n"
    "v_mul_f32 v244, v244, v156\n"
    "v_mul_f32 v246, v246, v158\n"
    "v_mfma_f32_16x16x32_bf16 v[180:183], v[172:175], a[180:183], v[180:183]\n"
    "ds_read_b128 a[132:135], v241 offset:45056\n"
    "v_mul_f32 v245, v245, v157\n"
    "v_mfma_f32_16x16x32_bf16 v[180:183], a[44:47], a[176:179], v[180:183]\n"
    "v_mul_f32 v247, v247, v159\n"
    "v_cvt_pk_bf16_f32 v154, v244, v245\n"
    "v_mfma_f32_16x16x32_bf16 v[180:183], v[104:107], a[144:147], v[180:183]\n"
    "v_cvt_pk_bf16_f32 v155, v246, v247\n"
    "v_mfma_f32_16x16x32_bf16 v[184:187], a[60:63], a[156:159], v[184:187]\n"
    "ds_write_b128 v249, v[152:155] offset:0\n"
    "v_mfma_f32_16x16x32_bf16 v[184:187], v[232:235], a[188:191], v[184:187]\n"
    "ds_read_b128 a[164:167], v241 offset:46080\n"
    "ds_read_b128 v[148:151], v242 offset:0\n"
    "v_mfma_f32_16x16x32_bf16 v[184:187], v[160:163], a[184:187], v[184:187]\n"
    "ds_read_b128 v[156:159], v242 offset:1024\n"
    "v_accvgpr_read_b32 v245, a84\n"
    "v_mfma_f32_16x16x32_bf16 v[184:187], v[204:207], a[152:155], v[184:187]\n"
    "ds_read_b128 a[160:163], v241 offset:61440\n"
    "v_accvgpr_read_b32 v247, a85\n"
    "v_mfma_f32_16x16x32_bf16 v[184:187], a[48:51], a[148:151], v[184:187]\n"
    "v_lshlrev_b32 v244, 16, v245\n"
    "v_lshlrev_b32 v246, 16, v247\n"
    "v_mfma_f32_16x16x32_bf16 v[184:187], v[136:139], a[180:183], v[184:187]\n"
    "v_and_b32 v245, s58, v245\n"
    "v_and_b32 v247, s58, v247\n"
    "v_mfma_f32_16x16x32_bf16 v[184:187], v[100:103], a[176:179], v[184:187]\n"
    "ds_read_b128 a[128:131], v241 offset:62464\n"
    "v_mul_f32 v244, v244, v176\n"
    "v_mfma_f32_16x16x32_bf16 v[184:187], a[8:11], a[144:147], v[184:187]\n"
    "s_add_u32 s63, s70, 131584\n"
    "s_waitcnt lgkmcnt(3)\n"
    "v_mul_f32 v246, v246, v178\n"
    "v_mfma_f32_16x16x32_bf16 v[188:191], v[228:231], a[156:159], v[188:191]\n"
    "buffer_store_dwordx4 v[148:151], v243, s[40:43], s63 offen sc1\n"
    "v_mul_f32 v245, v245, v177\n"
    "v_mul_f32 v247, v247, v179\n"
    "v_cvt_pk_bf16_f32 v176, v244, v245\n"
    "v_mfma_f32_16x16x32_bf16 v[188:191], v[236:239], a[188:191], v[188:191]\n"
    "v_cvt_pk_bf16_f32 v177, v246, v247\n"
    "v_accvgpr_read_b32 v245, a86\n"
    "v_mfma_f32_16x16x32_bf16 v[188:191], a[52:55], a[184:187], v[188:191]\n"
    "v_accvgpr_read_b32 v247, a87\n"
    "v_lshlrev_b32 v244, 16, v245\n"
    "v_mfma_f32_16x16x32_bf16 v[188:191], v[168:171], a[152:155], v[188:191]\n"
    "v_lshlrev_b32 v246, 16, v247\n"
    "v_and_b32 v245, s58, v245\n"
    "v_mfma_f32_16x16x32_bf16 v[188:191], v[132:135], a[148:151], v[188:191]\n"
    "v_and_b32 v247, s58, v247\n"
    "v_mul_f32 v244, v244, v180\n"
    "v_mfma_f32_16x16x32_bf16 v[188:191], a[12:15], a[180:183], v[188:191]\n"
    "s_add_u32 s63, s70, 147968\n"
    "s_waitcnt lgkmcnt(2)\n"
    "v_mul_f32 v246, v246, v182\n"
    "v_mfma_f32_16x16x32_bf16 v[188:191], v[64:67], a[176:179], v[188:191]\n"
    "buffer_store_dwordx4 v[156:159], v243, s[40:43], s63 offen sc1\n"
    "v_mul_f32 v245, v245, v181\n"
    "v_mul_f32 v247, v247, v183\n"
    "v_cvt_pk_bf16_f32 v178, v244, v245\n"
    "v_mfma_f32_16x16x32_bf16 v[188:191], v[108:111], a[144:147], v[188:191]\n"
    "v_cvt_pk_bf16_f32 v179, v246, v247\n"
    "v_mfma_f32_16x16x32_bf16 v[208:211], a[28:31], a[140:143], v[208:211]\n"
    "ds_write_b128 v248, v[176:179] offset:2048\n"
    "v_mfma_f32_16x16x32_bf16 v[208:211], v[224:227], a[172:175], v[208:211]\n"
    "v_accvgpr_read_b32 v245, a116\n"
    "v_accvgpr_read_b32 v247, a117\n"
    "v_mfma_f32_16x16x32_bf16 v[208:211], a[56:59], a[168:171], v[208:211]\n"
    "v_lshlrev_b32 v244, 16, v245\n"
    "v_lshlrev_b32 v246, 16, v247\n"
    "v_mfma_f32_16x16x32_bf16 v[208:211], v[200:203], a[136:139], v[208:211]\n"
    "v_and_b32 v245, s58, v245\n"
    "v_and_b32 v247, s58, v247\n"
    "v_mfma_f32_16x16x32_bf16 v[208:211], v[164:167], a[132:135], v[208:211]\n"
    "v_mul_f32 v244, v244, v184\n"
    "v_mul_f32 v246, v246, v186\n"
    "v_mfma_f32_16x16x32_bf16 v[208:211], a[16:19], a[164:167], v[208:211]\n"
    "s_waitcnt lgkmcnt(2)\n"
    "v_mul_f32 v245, v245, v185\n"
    "v_mul_f32 v247, v247, v187\n"
    "v_mfma_f32_16x16x32_bf16 v[208:211], v[96:99], a[160:163], v[208:211]\n"
    "s_waitcnt lgkmcnt(1)\n"
    "v_cvt_pk_bf16_f32 v184, v244, v245\n"
    "v_cvt_pk_bf16_f32 v185, v246, v247\n"
    "v_mfma_f32_16x16x32_bf16 v[208:211], v[140:143], a[128:131], v[208:211]\n"
    "v_accvgpr_read_b32 v245, a118\n"
    "v_accvgpr_read_b32 v247, a119\n"
    "v_mfma_f32_16x16x32_bf16 v[212:215], a[24:27], a[140:143], v[212:215]\n"
    "v_lshlrev_b32 v244, 16, v245\n"
    "v_lshlrev_b32 v246, 16, v247\n"
    "v_mfma_f32_16x16x32_bf16 v[212:215], v[192:195], a[172:175], v[212:215]\n"
    "v_and_b32 v245, s58, v245\n"
    "v_and_b32 v247, s58, v247\n"
    "v_mfma_f32_16x16x32_bf16 v[212:215], v[196:199], a[168:171], v[212:215]\n"
    "v_mul_f32 v244, v244, v188\n"
    "v_mul_f32 v246, v246, v190\n"
    "v_mfma_f32_16x16x32_bf16 v[212:215], a[20:23], a[136:139], v[212:215]\n"
    "v_mul_f32 v245, v245, v189\n"
    "v_mul_f32 v247, v247, v191\n"
    "v_mfma_f32_16x16x32_bf16 v[212:215], v[128:131], a[132:135], v[212:215]\n"
    "v_cvt_pk_bf16_f32 v186, v244, v245\n"
    "v_cvt_pk_bf16_f32 v187, v246, v247\n"
    "v_mfma_f32_16x16x32_bf16 v[212:215], v[172:175], a[164:167], v[212:215]\n"
    "ds_write_b128 v249, v[184:187] offset:2048\n"
    "v_mfma_f32_16x16x32_bf16 v[212:215], a[44:47], a[160:163], v[212:215]\n"
    "ds_read_b128 v[180:183], v242 offset:2048\n"
    "ds_read_b128 v[188:191], v242 offset:3072\n"
    "v_mfma_f32_16x16x32_bf16 v[212:215], v[104:107], a[128:131], v[212:215]\n"
    "v_accvgpr_read_b32 v245, a88\n"
    "v_accvgpr_read_b32 v247, a89\n"
    "v_mfma_f32_16x16x32_bf16 v[216:219], a[60:63], a[140:143], v[216:219]\n"
    "v_lshlrev_b32 v244, 16, v245\n"
    "v_lshlrev_b32 v246, 16, v247\n"
    "v_mfma_f32_16x16x32_bf16 v[216:219], v[232:235], a[172:175], v[216:219]\n"
    "v_and_b32 v245, s58, v245\n"
    "v_and_b32 v247, s58, v247\n"
    "v_mfma_f32_16x16x32_bf16 v[216:219], v[160:163], a[168:171], v[216:219]\n"
    "v_mul_f32 v244, v244, v208\n"
    "v_mul_f32 v246, v246, v210\n"
    "v_mfma_f32_16x16x32_bf16 v[216:219], v[204:207], a[136:139], v[216:219]\n"
    "s_add_u32 s63, s70, 164352\n"
    "s_waitcnt lgkmcnt(1)\n"
    "v_mul_f32 v245, v245, v209\n"
    "v_mfma_f32_16x16x32_bf16 v[216:219], a[48:51], a[132:135], v[216:219]\n"
    "buffer_store_dwordx4 v[180:183], v243, s[40:43], s63 offen sc1\n"
    "v_mul_f32 v247, v247, v211\n"
    "v_cvt_pk_bf16_f32 v208, v244, v245\n"
    "v_cvt_pk_bf16_f32 v209, v246, v247\n"
    "v_mfma_f32_16x16x32_bf16 v[216:219], v[136:139], a[164:167], v[216:219]\n"
    "v_accvgpr_read_b32 v245, a90\n"
    "v_accvgpr_read_b32 v247, a91\n"
    "v_mfma_f32_16x16x32_bf16 v[216:219], v[100:103], a[160:163], v[216:219]\n"
    "v_lshlrev_b32 v244, 16, v245\n"
    "v_lshlrev_b32 v246, 16, v247\n"
    "v_mfma_f32_16x16x32_bf16 v[216:219], a[8:11], a[128:131], v[216:219]\n"
    "v_and_b32 v245, s58, v245\n"
    "v_and_b32 v247, s58, v247\n"
    "v_mfma_f32_16x16x32_bf16 v[220:223], v[228:231], a[140:143], v[220:223]\n"
    "v_mul_f32 v244, v244, v212\n"
    "v_mul_f32 v246, v246, v214\n"
    "v_mfma_f32_16x16x32_bf16 v[220:223], v[236:239], a[172:175], v[220:223]\n"
    "s_add_u32 s63, s70, 180736\n"
    "s_waitcnt lgkmcnt(0)\n"
    "v_mul_f32 v245, v245, v213\n"
    "v_mfma_f32_16x16x32_bf16 v[220:223], a[52:55], a[168:171], v[220:223]\n"
    "buffer_store_dwordx4 v[188:191], v243, s[40:43], s63 offen sc1\n"
    "v_mul_f32 v247, v247, v215\n"
    "v_cvt_pk_bf16_f32 v210, v244, v245\n"
    "v_cvt_pk_bf16_f32 v211, v246, v247\n"
    "v_mfma_f32_16x16x32_bf16 v[220:223], v[168:171], a[136:139], v[220:223]\n"
    "ds_write_b128 v248, v[208:211] offset:0\n"
    "v_mfma_f32_16x16x32_bf16 v[220:223], v[132:135], a[132:135], v[220:223]\n"
    "v_accvgpr_read_b32 v245, a120\n"
    "v_accvgpr_read_b32 v247, a121\n"
    "v_mfma_f32_16x16x32_bf16 v[220:223], a[12:15], a[164:167], v[220:223]\n"
    "v_lshlrev_b32 v244, 16, v245\n"
    "v_lshlrev_b32 v246, 16, v247\n"
    "v_mfma_f32_16x16x32_bf16 v[220:223], v[64:67], a[160:163], v[220:223]\n"
    "v_and_b32 v245, s58, v245\n"
    "v_and_b32 v247, s58, v247\n"
    "v_mfma_f32_16x16x32_bf16 v[220:223], v[108:111], a[128:131], v[220:223]\n"
    "v_mul_f32 v244, v244, v216\n"
    "s_nop 7\n"
    "v_mul_f32 v246, v246, v218\n"
    "v_mul_f32 v245, v245, v217\n"
    "v_mul_f32 v247, v247, v219\n"
    "v_cvt_pk_bf16_f32 v216, v244, v245\n"
    "v_cvt_pk_bf16_f32 v217, v246, v247\n"
    "v_accvgpr_read_b32 v245, a122\n"
    "v_accvgpr_read_b32 v247, a123\n"
    "v_lshlrev_b32 v244, 16, v245\n"
    "v_lshlrev_b32 v246, 16, v247\n"
    "v_and_b32 v245, s58, v245\n"
    "v_and_b32 v247, s58, v247\n"
    "v_mul_f32 v244, v244, v220\n"
    "v_mul_f32 v246, v246, v222\n"
    "v_mul_f32 v245, v245, v221\n"
    "v_mul_f32 v247, v247, v223\n"
    "v_cvt_pk_bf16_f32 v218, v244, v245\n"
    "v_cvt_pk_bf16_f32 v219, v246, v247\n"
    "ds_write_b128 v249, v[216:219] offset:0\n"
    "ds_read_b128 v[212:215], v242 offset:0\n"
    "ds_read_b128 v[220:223], v242 offset:1024\n"
    "s_add_u32 s63, s70, 197120\n"
    "s_waitcnt lgkmcnt(1)\n"
    "buffer_store_dwordx4 v[212:215], v243, s[40:43], s63 offen sc1\n"
    "s_add_u32 s63, s70, 213504\n"
    "s_waitcnt lgkmcnt(0)\n"
    "buffer_store_dwordx4 v[220:223], v243, s[40:43], s63 offen sc1\n"
    "CODE_END_%=:\n"
    :
    : "s"(vb), "s"(wave), "s"(ldsb), "s"(pb), "s"(__builtin_amdgcn_kernarg_segment_ptr())
    : "v0", "v1", "v2", "v3", "v4", "v5", "v6", "v7", "v8", "v9", "v10", "v11", "v12", "v13", "v14", "v15", "v16", "v17", "v18", "v19", "v20", "v21", "v22", "v23", "v24", "v25", "v26", "v27", "v28", "v29", "v30", "v31", "v32", "v33", "v34", "v35", "v36", "v37", "v38", "v39", "v40", "v41", "v42", "v43", "v44", "v45", "v46", "v47", "v48", "v49", "v50", "v51", "v52", "v53", "v54", "v55", "v56", "v57", "v58", "v59", "v60", "v61", "v62", "v63", "v64", "v65", "v66", "v67", "v68", "v69", "v70", "v71", "v72", "v73", "v74", "v75", "v76", "v77", "v78", "v79", "v80", "v81", "v82", "v83", "v84", "v85", "v86", "v87", "v88", "v89", "v90", "v91", "v92", "v93", "v94", "v95", "v96", "v97", "v98", "v99", "v100", "v101", "v102", "v103", "v104", "v105", "v106", "v107", "v108", "v109", "v110", "v111", "v112", "v113", "v114", "v115", "v116", "v117", "v118", "v119", "v120", "v121", "v122", "v123", "v124", "v125", "v126", "v127", "v128", "v129", "v130", "v131", "v132", "v133", "v134", "v135", "v136", "v137", "v138", "v139", "v140", "v141", "v142", "v143", "v144", "v145", "v146", "v147", "v148", "v149", "v150", "v151", "v152", "v153", "v154", "v155", "v156", "v157", "v158", "v159", "v160", "v161", "v162", "v163", "v164", "v165", "v166", "v167", "v168", "v169", "v170", "v171", "v172", "v173", "v174", "v175", "v176", "v177", "v178", "v179", "v180", "v181", "v182", "v183", "v184", "v185", "v186", "v187", "v188", "v189", "v190", "v191", "v192", "v193", "v194", "v195", "v196", "v197", "v198", "v199", "v200", "v201", "v202", "v203", "v204", "v205", "v206", "v207", "v208", "v209", "v210", "v211", "v212", "v213", "v214", "v215", "v216", "v217", "v218", "v219", "v220", "v221", "v222", "v223", "v224", "v225", "v226", "v227", "v228", "v229", "v230", "v231", "v232", "v233", "v234", "v235", "v236", "v237", "v238", "v239", "v240", "v241", "v242", "v243", "v244", "v245", "v246", "v247", "v248", "v249", "v250", "v251", "v252", "v253", "v254", "v255", "a0", "a1", "a2", "a3", "a4", "a5", "a6", "a7", "a8", "a9", "a10", "a11", "a12", "a13", "a14", "a15", "a16", "a17", "a18", "a19", "a20", "a21", "a22", "a23", "a24", "a25", "a26", "a27", "a28", "a29", "a30", "a31", "a32", "a33", "a34", "a35", "a36", "a37", "a38", "a39", "a40", "a41", "a42", "a43", "a44", "a45", "a46", "a47", "a48", "a49", "a50", "a51", "a52", "a53", "a54", "a55", "a56", "a57", "a58", "a59", "a60", "a61", "a62", "a63", "a64", "a65", "a66", "a67", "a68", "a69", "a70", "a71", "a72", "a73", "a74", "a75", "a76", "a77", "a78", "a79", "a80", "a81", "a82", "a83", "a84", "a85", "a86", "a87", "a88", "a89", "a90", "a91", "a92", "a93", "a94", "a95", "a96", "a97", "a98", "a99", "a100", "a101", "a102", "a103", "a104", "a105", "a106", "a107", "a108", "a109", "a110", "a111", "a112", "a113", "a114", "a115", "a116", "a117", "a118", "a119", "a120", "a121", "a122", "a123", "a124", "a125", "a126", "a127", "a128", "a129", "a130", "a131", "a132", "a133", "a134", "a135", "a136", "a137", "a138", "a139", "a140", "a141", "a142", "a143", "a144", "a145", "a146", "a147", "a148", "a149", "a150", "a151", "a152", "a153", "a154", "a155", "a156", "a157", "a158", "a159", "a160", "a161", "a162", "a163", "a164", "a165", "a166", "a167", "a168", "a169", "a170", "a171", "a172", "a173", "a174", "a175", "a176", "a177", "a178", "a179", "a180", "a181", "a182", "a183", "a184", "a185", "a186", "a187", "a188", "a189", "a190", "a191", "a192", "a193", "a194", "a195", "a196", "a197", "a198", "a199", "a200", "a201", "a202", "a203", "a204", "a205", "a206", "a207", "a208", "a209", "a210", "a211", "a212", "a213", "a214", "a215", "a216", "a217", "a218", "a219", "a220", "a221", "a222", "a223", "a224", "a225", "a226", "a227", "a228", "a229", "a230", "a231", "a232", "a233", "a234", "a235", "a236", "a237", "a238", "a239", "a240", "a241", "a242", "a243", "a244", "a245", "a246", "a247", "a248", "a249", "a250", "a251", "a252", "a253", "a254", "a255", "s4", "s5", "s6", "s7", "s8", "s9", "s10", "s11", "s12", "s13", "s14", "s15", "s20", "s21", "s22", "s23", "s24", "s25", "s26", "s27", "s28", "s29", "s30", "s31", "s32", "s33", "s34", "s35", "s36", "s37", "s38", "s39", "s40", "s41", "s42", "s43", "s44", "s45", "s46", "s47", "s48", "s49", "s50", "s51", "s52", "s53", "s54", "s55", "s56", "s57", "s58", "s59", "s60", "s61", "s62", "s63", "s64", "s65", "s66", "s67", "s68", "s69", "s70", "s71", "s72", "s73", "s74", "s75", "s76", "s77", "s78", "s79", "s80", "s81", "s82", "s83", "s84", "s85", "s86", "s87", "s88", "s89", "s90", "s91", "s92", "s93", "s94", "s95", "s96", "s97", "s98", "s99", "vcc", "scc", "memory");
}

extern "C" void kernel_launch(void* const* d_in, const int* in_sizes, int n_in,
                              void* d_out, int out_size, void* d_ws, size_t ws_size,
                              hipStream_t stream) {
  const u16* x0 = (const u16*)d_in[0];
  const u16* ws = (const u16*)d_in[1];
  const u16* bs = (const u16*)d_in[2];
  char* base = (char*)d_ws + 0;
  unsigned* flags = (unsigned*)base;
  u16* wt = (u16*)(base + FLAG_BYTES + ((size_t)226 << 20));
  u16* ex = (u16*)(base + ((size_t)32 << 20));
  dcn_pair<<<256, 256, 0, stream>>>(x0, wt, bs, ex, flags, (u16*)d_out, ws);
}
```

```cpp
#include <hip/hip_runtime.h>
#include <stdint.h>
typedef unsigned short u16;
constexpr size_t WT_BYTES = (size_t)6 * 1024 * 1024 * 2 + 98304;
constexpr size_t FLAG_BYTES = 256 * 1024 + 8192;

__global__ __launch_bounds__(256, 1) void dcn_pair(const u16* __restrict__ x0, u16* __restrict__ wt, const u16* __restrict__ bs,
                                                   u16* __restrict__ ex0, unsigned* __restrict__ flags, u16* __restrict__ out,
                                                   const u16* __restrict__ w) {
  __shared__ __attribute__((aligned(16))) unsigned char lds[163840];
  const int wave = __builtin_amdgcn_readfirstlane(threadIdx.x >> 6);
  const unsigned ldsb = (unsigned)(size_t)lds;
  const unsigned pb = blockIdx.x, pk = 3;
  const unsigned vb = ((pb & ~(8u | (1u << pk))) | (((pb >> pk) & 1u) << 3) | (((pb >> 3) & 1u) << pk)) ^ 0u;
  asm volatile(
    "s_load_dwordx2 s[72:73], %4, 0x30\n"
    "s_load_dwordx2 s[24:25], %4, 0\n"
    "s_load_dwordx8 s[4:11], %4, 0\n"
    "s_load_dwordx4 s[12:15], %4, 0x20\n"
    "s_mov_b32 s65, %1\n"
    "v_mbcnt_lo_u32_b32 v0, -1, 0\n"
    "v_mbcnt_hi_u32_b32 v0, -1, v0\n"
    "v_lshlrev_b32 v240, 4, v0\n"
    "s_getpc_b64 s[48:49]\n"
    "s_and_b32 s49, s49, 0xffff\n"
    "s_mov_b32 s50, CODE_END_%= - . - 64\n"
    "s_mov_b32 s51, 0x20000\n"
    "s_lshr_b32 s60, %3, 3\n"
    "s_lshl_b32 s60, s60, 2\n"
    "s_add_u32 s60, s60, s65\n"
    "s_lshl_b32 s60, s60, 10\n"
    "buffer_load_dwordx4 v[248:251], v240, s[48:51], s60 offen\n"
    "s_bfe_u32 s64, %0, 0x10003\n"
    "s_lshr_b32 s62, %0, 4\n"
    "s_and_b32 s61, %0, 7\n"
    "s_lshl_b32 s60, s62, 3\n"
    "s_add_u32 s60, s60, s61\n"
    "s_lshl_b32 s40, s60, 18\n"
    "s_mov_b32 s26, 262144\n"
    "s_mov_b32 s27, 0x20000\n"
    "s_mov_b32 s74, 12582912\n"
    "s_mov_b32 s75, s27\n"
    "s_lshl_b32 s60, s65, 12\n"
    "s_add_u32 s68, %2, s60\n"
    "s_add_u32 s79, s68, s60\n"
    "s_lshl_b32 s61, s64, 10\n"
    "s_lshl_b32 s60, s65, 7\n"
    "s_add_u32 s70, s61, s60\n"
    "s_lshl_b32 s66, s65, 16\n"
    "s_mov_b32 s42, 0x20a86420\n"
    "s_mov_b32 s43, 0x6420a864\n"
    "s_lshl_b32 s63, s62, 2\n"
    "s_lshr_b64 s[42:43], s[42:43], s63\n"
    "s_and_b32 s41, s42, 15\n"
    "s_lshl_b32 s67, s41, 7\n"
    "s_lshl_b32 s57, s41, 17\n"
    "s_add_u32 s57, s57, s70\n"
    "v_lshrrev_b32 v1, 3, v0\n"
    "v_lshlrev_b32 v1, 11, v1\n"
    "v_lshrrev_b32 v2, 4, v0\n"
    "v_and_b32 v3, 7, v0\n"
    "v_xor_b32 v4, v3, v2\n"
    "v_lshl_add_u32 v255, v4, 4, v1\n"
    "v_xor_b32 v243, 64, v255\n"
    "v_and_b32 v4, 1, v2\n"
    "v_lshlrev_b32 v4, 2, v4\n"
    "v_xor_b32 v4, v4, v3\n"
    "v_lshl_add_u32 v254, v4, 4, v1\n"
    "v_and_b32 v8, 3, v0\n"
    "v_lshlrev_b32 v8, 4, v8\n"
    "v_lshrrev_b32 v9, 2, v0\n"
    "v_and_b32 v9, 3, v9\n"
    "v_lshl_add_u32 v10, v2, 3, v9\n"
    "v_lshrrev_b32 v11, 1, v9\n"
    "v_lshlrev_b32 v11, 6, v11\n"
    "v_lshlrev_b32 v12, 7, v10\n"
    "v_add3_u32 v252, v12, v11, v8\n"
    "v_add_u32 v252, s79, v252\n"
    "v_xor_b32 v253, 64, v252\n"
    "v_and_b32 v5, 15, v0\n"
    "v_lshrrev_b32 v6, 1, v5\n"
    "v_xor_b32 v6, v6, v2\n"
    "v_lshlrev_b32 v5, 7, v5\n"
    "v_lshl_add_u32 v5, v6, 4, v5\n"
    "s_add_u32 s60, %2, 65536\n"
    "v_add_u32 v241, s60, v5\n"
    "v_xor_b32 v242, 64, v241\n"
    "v_mov_b32 v244, 0x3f803f80\n"
    "v_mov_b32 v245, 0x3f80\n"
    "v_mov_b32 v246, 0x3eaa4400\n"
    "v_mov_b32 v247, 0x0\n"
    "s_waitcnt lgkmcnt(0)\n"
    "s_and_b32 s73, s73, 0xffff\n"
    "s_add_u32 m0, s79, 0\n"
    "s_add_u32 s54, s57, 0\n"
    "buffer_load_dwordx4 v254, s[72:75], s54 offen  lds\n"
    "s_addk_i32 m0, 1024\n"
    "s_addk_i32 s54, 16384\n"
    "buffer_load_dwordx4 v254, s[72:75], s54 offen  lds\n"
    "s_addk_i32 m0, 1024\n"
    "s_addk_i32 s54, 16384\n"
    "buffer_load_dwordx4 v254, s[72:75], s54 offen  lds\n"
    "s_addk_i32 m0, 1024\n"
    "s_addk_i32 s54, 16384\n"
    "buffer_load_dwordx4 v254, s[72:75], s54 offen  lds\n"
    "s_add_u32 s24, s24, s40\n"
    "s_addc_u32 s25, s25, 0\n"
    "s_and_b32 s25, s25, 0xffff\n"
    "s_xor_b32 s59, s66, s67\n"
    "s_addk_i32 s67, 128\n"
    "s_cmpk_eq_u32 s67, 1536\n"
    "s_cselect_b32 s67, 0, s67\n"
    "s_add_u32 m0, s68, 65536\n"
    "s_nop 0\n"
    "buffer_load_dwordx4 v255, s[24:27], s59 offen nt lds\n"
    "s_add_u32 m0, s68, 66560\n"
    "s_addk_i32 s59, 16384\n"
    "buffer_load_dwordx4 v243, s[24:27], s59 offen nt lds\n"
    "s_add_u32 m0, s68, 67584\n"
    "s_addk_i32 s59, 16384\n"
    "buffer_load_dwordx4 v255, s[24:27], s59 offen nt lds\n"
    "s_add_u32 m0, s68, 68608\n"
    "s_addk_i32 s59, 16384\n"
    "buffer_load_dwordx4 v243, s[24:27], s59 offen nt lds\n"
    "s_lshl_b32 s84, s64, 3\n"
    "s_add_u32 s84, s84, s65\n"
    "s_add_u32 s85, s84, 4\n"
    "s_sub_u32 s60, s84, s41\n"
    "s_add_u32 s61, s60, 12\n"
    "s_cmp_lt_u32 s84, s41\n"
    "s_cselect_b32 s60, s61, s60\n"
    "s_cmp_lt_u32 s84, 12\n"
    "s_cselect_b32 s84, s60, s84\n"
    "s_sub_u32 s60, s85, s41\n"
    "s_add_u32 s61, s60, 12\n"
    "s_cmp_lt_u32 s85, s41\n"
    "s_cselect_b32 s60, s61, s60\n"
    "s_cmp_lt_u32 s85, 12\n"
    "s_cselect_b32 s85, s60, s85\n"
    "s_cmp_ge_u32 s84, 4\n"
    "s_cselect_b32 s60, s64, 0\n"
    "s_xor_b32 s84, s84, s60\n"
    "s_xor_b32 s85, s85, s64\n"
    "s_lshl_b32 s85, 1, s85\n"
    "s_lshl_b32 s60, 1, s84\n"
    "s_or_b32 s85, s85, s60\n"
    "s_cmp_eq_u32 s65, 0\n"
    "s_cselect_b32 s86, -1, 0\n"
    "s_cmp_eq_u32 %0, 0\n"
    "s_cselect_b32 s87, 4096, 0\n"
    "s_mov_b64 s[62:63], s[12:13]\n"
    "s_add_u32 s62, s62, 262144\n"
    "s_addc_u32 s63, s63, 0\n"
    "s_mov_b32 s80, s62\n"
    "s_and_b32 s81, s63, 0xffff\n"
    "s_mov_b32 s82, 0\n"
    "s_mov_b32 s83, 0x20000\n"
    "s_lshl_b32 s76, %0, 3\n"
    "s_lshl_b32 s77, %0, 2\n"
    "s_lshl_b32 s61, %0, 10\n"
    "s_lshl_b32 s60, s65, 8\n"
    "s_add_u32 s61, s61, s60\n"
    "s_mov_b64 s[62:63], s[12:13]\n"
    "s_add_u32 s62, s62, s61\n"
    "s_addc_u32 s63, s63, 0\n"
    "s_mov_b32 s44, s62\n"
    "s_and_b32 s45, s63, 0xffff\n"
    "s_mov_b32 s46, 256\n"
    "s_mov_b32 s47, 0x20000\n"
    "s_sub_u32 s90, s68, s79\n"
    "s_add_u32 s90, s90, 114688\n"
    "s_mov_b32 s60, s40\n"
    "s_mov_b64 s[62:63], s[14:15]\n"
    "s_add_u32 s62, s62, s60\n"
    "s_addc_u32 s63, s63, 0\n"
    "s_mov_b32 s40, s62\n"
    "s_and_b32 s41, s63, 0xffff\n"
    "s_mov_b32 s42, 262144\n"
    "s_mov_b32 s43, 0x20000\n"
    "s_add_u32 s60, s77, s65\n"
    "s_lshr_b32 s20, s60, 9\n"
    "s_bfe_u32 s21, s60, 0x50004\n"
    "s_and_b32 s22, s60, 15\n"
    "s_lshr_b32 s23, s22, 3\n"
    "s_bfe_u32 s32, s22, 0x10002\n"
    "s_and_b32 s33, s22, 3\n"
    "s_and_b32 s34, s21, 1\n"
    "s_lshl_b32 s34, s34, 1\n"
    "s_bfe_u32 s35, s21, 0x10002\n"
    "s_or_b32 s34, s34, s35\n"
    "s_lshl_b32 s35, s34, 4\n"
    "s_lshl_b32 s36, s32, 3\n"
    "s_add_u32 s35, s35, s36\n"
    "s_bfe_u32 s36, s21, 0x10003\n"
    "s_lshl_b32 s36, s36, 6\n"
    "s_add_u32 s36, s36, s35\n"
    "s_add_u32 s36, s36, 96\n"
    "s_add_u32 s37, s35, 32\n"
    "s_mul_i32 s39, s32, 224\n"
    "s_lshl_b32 s34, s34, 3\n"
    "s_add_u32 s39, s39, s34\n"
    "s_bitcmp1_b32 s21, 3\n"
    "s_cselect_b32 s39, s37, s39\n"
    "s_lshr_b32 s37, s21, 4\n"
    "s_cmp_eq_u32 s37, s23\n"
    "s_cselect_b32 s39, s39, s36\n"
    "s_bfe_u32 s36, s21, 0x10001\n"
    "s_add_u32 s39, s39, s36\n"
    "s_lshr_b32 s34, s21, 1\n"
    "s_xor_b32 s34, s34, s23\n"
    "s_lshl_b32 s35, s34, 3\n"
    "s_add_u32 s36, s35, 96\n"
    "s_sub_u32 s37, 344, s35\n"
    "s_cmp_eq_u32 s32, 0\n"
    "s_cselect_b32 s38, s36, s37\n"
    "s_cmp_lt_u32 s34, 12\n"
    "s_cselect_b32 s71, 1, 0\n"
    "s_andn2_b32 s71, s71, s20\n"
    "s_lshl_b32 s71, s71, 30\n"
    "s_and_b32 s36, s21, 1\n"
    "s_add_u32 s38, s38, s36\n"
    "s_cmp_eq_u32 s20, 0\n"
    "s_cselect_b32 s38, s38, s39\n"
    "s_lshl_b32 s35, s23, 2\n"
    "s_add_u32 s35, s35, s33\n"
    "s_mul_i32 s35, s35, 6\n"
    "s_add_u32 s35, s35, s20\n"
    "s_lshl_b32 s35, s35, 18\n"
    "s_lshl_b32 s38, s38, 10\n"
    "s_add_u32 s89, s35, s38\n"
    "s_or_b32 s89, s89, s71\n"
    "s_lshl_b32 s39, s39, 10\n"
    "s_add_u32 s91, s35, s39\n"
    "s_lshl_b32 s35, s20, 21\n"
    "s_lshl_b32 s36, s21, 16\n"
    "s_add_u32 s35, s35, s36\n"
    "s_lshl_b32 s36, s22, 7\n"
    "s_add_u32 s78, s35, s36\n"
    "s_cmp_ge_u32 s6, 0xff200000\n"
    "s_cselect_b32 s62, 234881024, 0\n"
    "s_sub_u32 s6, s6, s62\n"
    "s_subb_u32 s7, s7, 0\n"
    "s_mov_b64 s[62:63], s[6:7]\n"
    "s_add_u32 s62, s62, 0\n"
    "s_addc_u32 s63, s63, 0\n"
    "s_mov_b32 s28, s62\n"
    "s_and_b32 s29, s63, 0xffff\n"
    "s_mov_b32 s30, 12582912\n"
    "s_mov_b32 s31, 0x20000\n"
    "s_mov_b64 s[62:63], s[8:9]\n"
    "s_add_u32 s62, s62, 0\n"
    "s_addc_u32 s63, s63, 0\n"
    "s_mov_b32 s20, s62\n"
    "s_and_b32 s21, s63, 0xffff\n"
    "s_mov_b32 s22, 12288\n"
    "s_mov_b32 s23, 0x20000\n"
    "s_mov_b64 s[62:63], s[6:7]\n"
    "s_add_u32 s62, s62, 12582912\n"
    "s_addc_u32 s63, s63, 0\n"
    "s_mov_b32 s36, s62\n"
    "s_and_b32 s37, s63, 0xffff\n"
    "s_mov_b32 s38, 98304\n"
    "s_mov_b32 s39, 0x20000\n"
    "s_mov_b64 s[62:63], s[10:11]\n"
    "s_add_u32 s62, s62, 0x1fffff\n"
    "s_addc_u32 s63, s63, 0\n"
    "s_mov_b32 s32, s62\n"
    "s_and_b32 s33, s63, 0xffff\n"
    "s_mov_b32 s34, 201326592\n"
    "s_mov_b32 s35, 0x20000\n"
    "s_and_b32 s32, s32, 0xffe00000\n"
    "s_mov_b32 s58, 0xffff0000\n"
    "s_mov_b32 s52, 0\n"
    "s_xor_b32 s60, %0, 8\n"
    "s_lshl_b32 s61, s60, 10\n"
    "s_mov_b64 s[62:63], s[12:13]\n"
    "s_add_u32 s62, s62, s61\n"
    "s_addc_u32 s63, s63, 0\n"
    "s_mov_b32 s48, s62\n"
    "s_and_b32 s49, s63, 0xffff\n"
    "s_mov_b32 s50, 64\n"
    "s_mov_b32 s51, 0x20000\n"
    "s_add_u32 m0, s79, 4096\n"
    "s_addk_i32 s54, 16384\n"
    "buffer_load_dwordx4 v254, s[72:75], s54 offen  lds\n"
    "s_addk_i32 m0, 1024\n"
    "s_addk_i32 s54, 16384\n"
    "buffer_load_dwordx4 v254, s[72:75], s54 offen  lds\n"
    "s_addk_i32 m0, 1024\n"
    "s_addk_i32 s54, 16384\n"
    "buffer_load_dwordx4 v254, s[72:75], s54 offen  lds\n"
    "s_addk_i32 m0, 1024\n"
    "s_addk_i32 s54, 16384\n"
    "buffer_load_dwordx4 v254, s[72:75], s54 offen  lds\n"
    "v_mfma_f32_16x16x4_4b_bf16 v[0:15], v[244:245], v[246:247], 0\n"
    "v_mfma_f32_16x16x4_4b_bf16 v[32:47], v[244:245], v[246:247], 0\n"
    "s_add_u32 m0, s79, 32768\n"
    "s_add_u32 s54, s57, 512\n"
    "buffer_load_dwordx4 v254, s[72:75], s54 offen  lds\n"
    "v_mfma_f32_16x16x4_4b_bf16 v[64:79], v[244:245], v[246:247], 0\n"
    "v_mfma_f32_16x16x4_4b_bf16 v[96:111], v[244:245], v[246:247], 0\n"
    "s_addk_i32 m0, 1024\n"
    "s_addk_i32 s54, 16384\n"
    "buffer_load_dwordx4 v254, s[72:75], s54 offen  lds\n"
    "v_mfma_f32_16x16x4_4b_bf16 v[128:143], v[244:245], v[246:247], 0\n"
    "v_mfma_f32_16x16x4_4b_bf16 v[160:175], v[244:245], v[246:247], 0\n"
    "s_addk_i32 m0, 1024\n"
    "s_addk_i32 s54, 16384\n"
    "buffer_load_dwordx4 v254, s[72:75], s54 offen  lds\n"
    "v_mfma_f32_16x16x4_4b_bf16 v[192:207], v[244:245], v[246:247], 0\n"
    "v_mfma_f32_16x16x4_4b_bf16 v[224:239], v[244:245], v[246:247], 0\n"
    "s_addk_i32 m0, 1024\n"
    "s_addk_i32 s54, 16384\n"
    "buffer_load_dwordx4 v254, s[72:75], s54 offen  lds\n"
    "v_mfma_f32_16x16x4_4b_bf16 v[16:31], v[244:245], v[246:247], 0\n"
    "v_mfma_f32_16x16x4_4b_bf16 v[48:63], v[244:245], v[246:247], 0\n"
    "s_addk_i32 m0, 1024\n"
    "s_addk_i32 s54, 16384\n"
    "buffer_load_dwordx4 v254, s[72:75], s54 offen  lds\n"
    "v_mfma_f32_16x16x4_4b_bf16 v[80:95], v[244:245], v[246:247], 0\n"
    "v_mfma_f32_16x16x4_4b_bf16 v[112:127], v[244:245], v[246:247], 0\n"
    "s_addk_i32 m0, 1024\n"
    "s_addk_i32 s54, 16384\n"
    "buffer_load_dwordx4 v254, s[72:75], s54 offen  lds\n"
    "v_mfma_f32_16x16x4_4b_bf16 v[144:159], v[244:245], v[246:247], 0\n"
    "v_mfma_f32_16x16x4_4b_bf16 v[176:191], v[244:245], v[246:247], 0\n"
    "s_addk_i32 m0, 1024\n"
    "s_addk_i32 s54, 16384\n"
    "buffer_load_dwordx4 v254, s[72:75], s54 offen  lds\n"
    "v_mfma_f32_16x16x4_4b_bf16 v[208:223], v[244:245], v[246:247], 0\n"
    "v_mfma_f32_16x16x4_4b_bf16 a[240:255], v[244:245], v[246:247], 0\n"
    "s_addk_i32 m0, 1024\n"
    "s_addk_i32 s54, 16384\n"
    "buffer_load_dwordx4 v254, s[72:75], s54 offen  lds\n"
    "s_add_u32 s57, s57, 131072\n"
    "s_cmp_ge_u32 s57, 1572864\n"
    "s_cselect_b32 s57, s70, s57\n"
    "s_waitcnt vmcnt(16)\n"
    "ds_read_b64_tr_b16 a[224:225], v252 offset:0\n"
    "ds_read_b64_tr_b16 a[226:227], v252 offset:512\n"
    "ds_read_b64_tr_b16 a[228:229], v252 offset:8\n"
    "ds_read_b64_tr_b16 a[230:231], v252 offset:520\n"
    "ds_read_b64_tr_b16 a[232:233], v253 offset:0\n"
    "ds_read_b64_tr_b16 a[234:235], v253 offset:512\n"
    "ds_read_b64_tr_b16 a[236:237], v253 offset:8\n"
    "ds_read_b64_tr_b16 a[238:239], v253 offset:520\n"
    "s_waitcnt vmcnt(12)\n"
    "s_barrier\n"
    "ds_read_b128 a[128:131], v241 offset:0\n"
    "s_waitcnt lgkmcnt(0)\n"
    "v_mfma_f32_16x16x32_bf16 v[0:3], a[224:227], a[128:131], v[0:3]\n"
    "ds_read_b128 a[132:135], v241 offset:2048\n"
    "s_waitcnt lgkmcnt(0)\n"
    "v_mfma_f32_16x16x32_bf16 v[32:35], a[224:227], a[132:135], v[32:35]\n"
    "ds_read_b128 a[136:139], v241 offset:4096\n"
    "s_waitcnt lgkmcnt(0)\n"
    "v_mfma_f32_16x16x32_bf16 v[64:67], a[224:227], a[136:139], v[64:67]\n"
    "ds_read_b128 a[140:143], v241 offset:6144\n"
    "s_waitcnt lgkmcnt(0)\n"
    "ds_read_b128 a[144:147], v241 offset:8192\n"
    "ds_read_b128 a[148:151], v241 offset:10240\n"
    "ds_read_b128 a[152:155], v241 offset:12288\n"
    "ds_read_b128 a[156:159], v241 offset:14336\n"
    "v_mfma_f32_16x16x32_bf16 v[96:99], a[224:227], a[140:143], v[96:99]\n"
    "s_waitcnt lgkmcnt(3)\n"
    "v_mfma_f32_16x16x32_bf16 v[128:131], a[224:227], a[144:147], v[128:131]\n"
    "s_waitcnt lgkmcnt(2)\n"
    "v_mfma_f32_16x16x32_bf16 v[160:163], a[224:227], a[148:151], v[160:163]\n"
    "s_waitcnt lgkmcnt(1)\n"
    "v_mfma_f32_16x16x32_bf16 v[192:195], a[224:227], a[152:155], v[192:195]\n"
    "s_waitcnt lgkmcnt(0)\n"
    "ds_read_b128 a[160:163], v242 offset:0\n"
    "v_mfma_f32_16x16x32_bf16 v[224:227], a[224:227], a[156:159], v[224:227]\n"
    "ds_read_b128 a[164:167], v242 offset:2048\n"
    "v_mfma_f32_16x16x32_bf16 v[228:231], a[228:231], a[156:159], v[228:231]\n"
    "ds_read_b128 a[168:171], v242 offset:4096\n"
    "v_mfma_f32_16x16x32_bf16 v[196:199], a[228:231], a[152:155], v[196:199]\n"
    "ds_read_b128 a[172:175], v242 offset:6144\n"
    "v_mfma_f32_16x16x32_bf16 v[164:167], a[228:231], a[148:151], v[164:167]\n"
    "ds_read_b128 a[176:179], v242 offset:8192\n"
    "v_mfma_f32_16x16x32_bf16 v[132:135], a[228:231], a[144:147], v[132:135]\n"
    "ds_read_b128 a[180:183], v242 offset:10240\n"
    "v_mfma_f32_16x16x32_bf16 v[100:103], a[228:231], a[140:143], v[100:103]\n"
    "ds_read_b128 a[184:187], v242 offset:12288\n"
    "v_mfma_f32_16x16x32_bf16 v[68:71], a[228:231], a[136:139], v[68:71]\n"
    "ds_read_b128 a[188:191], v242 offset:14336\n"
    "s_add_u32 m0, s79, 131072\n"
    "v_mfma_f32_16x16x32_bf16 v[36:39], a[228:231], a[132:135], v[36:39]\n"
    "s_add_u32 s54, s57, 0\n"
    "v_mfma_f32_16x16x32_bf16 v[4:7], a[228:231], a[128:131], v[4:7]\n"
    "buffer_load_dwordx4 v254, s[72:75], s54 offen  lds\n"
    "v_mfma_f32_16x16x32_bf16 v[8:11], a[232:235], a[128:131], v[8:11]\n"
    "s_addk_i32 m0, 1024\n"
    "s_addk_i32 s54, 16384\n"
    "v_mfma_f32_16x16x32_bf16 v[40:43], a[232:235], a[132:135], v[40:43]\n"
    "buffer_load_dwordx4 v254, s[72:75], s54 offen  lds\n"
    "v_mfma_f32_16x16x32_bf16 v[72:75], a[232:235], a[136:139], v[72:75]\n"
    "s_addk_i32 m0, 1024\n"
    "s_addk_i32 s54, 16384\n"
    "v_mfma_f32_16x16x32_bf16 v[104:107], a[232:235], a[140:143], v[104:107]\n"
    "buffer_load_dwordx4 v254, s[72:75], s54 offen  lds\n"
    "v_mfma_f32_16x16x32_bf16 v[136:139], a[232:235], a[144:147], v[136:139]\n"
    "s_addk_i32 m0, 1024\n"
    "s_addk_i32 s54, 16384\n"
    "v_mfma_f32_16x16x32_bf16 v[168:171], a[232:235], a[148:151], v[168:171]\n"
    "buffer_load_dwordx4 v254, s[72:75], s54 offen  lds\n"
    "v_mfma_f32_16x16x32_bf16 v[200:203], a[232:235], a[152:155], v[200:203]\n"
    "s_addk_i32 m0, 1024\n"
    "s_addk_i32 s54, 16384\n"
    "v_mfma_f32_16x16x32_bf16 v[232:235], a[232:235], a[156:159], v[232:235]\n"
    "buffer_load_dwordx4 v254, s[72:75], s54 offen  lds\n"
    "v_mfma_f32_16x16x32_bf16 v[236:239], a[236:239], a[156:159], v[236:239]\n"
    "s_waitcnt vmcnt(13)\n"
    "ds_read_b64_tr_b16 a[192:193], v252 offset:4096\n"
    "ds_read_b64_tr_b16 a[194:195], v252 offset:4608\n"
    "s_addk_i32 m0, 1024\n"
    "s_addk_i32 s54, 16384\n"
    "buffer_load_dwordx4 v254, s[72:75], s54 offen  lds\n"
    "ds_read_b64_tr_b16 a[196:197], v252 offset:4104\n"
    "ds_read_b64_tr_b16 a[198:199], v252 offset:4616\n"
    "s_bitcmp1_b32 s85, 0\n"
    "s_cbranch_scc1 CP0_%=\n"
    "CR0_%=:\n"
    "v_mfma_f32_16x16x32_bf16 v[204:207], a[236:239], a[152:155], v[204:207]\n"
    "s_addk_i32 m0, 1024\n"
    "s_addk_i32 s54, 16384\n"
    "v_mfma_f32_16x16x32_bf16 v[172:175], a[236:239], a[148:151], v[172:175]\n"
    "buffer_load_dwordx4 v254, s[72:75], s54 offen  lds\n"
    "v_mfma_f32_16x16x32_bf16 v[140:143], a[236:239], a[144:147], v[140:143]\n"
    "ds_read_b64_tr_b16 a[200:201], v253 offset:4096\n"
    "ds_read_b64_tr_b16 a[202:203], v253 offset:4608\n"
    "v_mfma_f32_16x16x32_bf16 v[108:111], a[236:239], a[140:143], v[108:111]\n"
    "s_addk_i32 m0, 1024\n"
    "s_addk_i32 s54, 16384\n"
    "v_mfma_f32_16x16x32_bf16 v[76:79], a[236:239], a[136:139], v[76:79]\n"
    "buffer_load_dwordx4 v254, s[72:75], s54 offen  lds\n"
    "v_mfma_f32_16x16x32_bf16 v[44:47], a[236:239], a[132:135], v[44:47]\n"
    "ds_read_b64_tr_b16 a[204:205], v253 offset:4104\n"
    "ds_read_b64_tr_b16 a[206:207], v253 offset:4616\n"
    "v_mfma_f32_16x16x32_bf16 v[12:15], a[236:239], a[128:131], v[12:15]\n"
    "s_waitcnt lgkmcnt(6)\n"
    "s_xor_b32 s59, s66, s67\n"
    "s_addk_i32 s67, 128\n"
    "v_mfma_f32_16x16x32_bf16 v[0:3], a[192:195], a[160:163], v[0:3]\n"
    "s_cmpk_eq_u32 s67, 1536\n"
    "s_cselect_b32 s67, 0, s67\n"
    "s_add_u32 m0, s68, 81920\n"
    "v_mfma_f32_16x16x32_bf16 v[32:35], a[192:195], a[164:167], v[32:35]\n"
    "s_nop 0\n"
    "v_mfma_f32_16x16x32_bf16 v[64:67], a[192:195], a[168:171], v[64:67]\n"
    "buffer_load_dwordx4 v255, s[24:27], s59 offen nt lds\n"
    "v_mfma_f32_16x16x32_bf16 v[96:99], a[192:195], a[172:175], v[96:99]\n"
    "s_add_u32 m0, s68, 82944\n"
    "s_addk_i32 s59, 16384\n"
    "v_mfma_f32_16x16x32_bf16 v[128:131], a[192:195], a[176:179], v[128:131]\n"
    "buffer_load_dwordx4 v243, s[24:27], s59 offen nt lds\n"
    "s_add_u32 s82, s77, 3076\n"
    "s_and_b32 s82, s82, s86\n"
    "v_mfma_f32_16x16x32_bf16 v[160:163], a[192:195], a[180:183], v[160:163]\n"
    "buffer_load_dword v245, v240, s[80:83], s77 offen offset:3072 sc1\n"
    "v_lshrrev_b32 v246, 1, v240\n"
    "s_lshl_b32 s61, s65, 9\n"
    "s_mov_b32 s82, s87\n"
    "v_mfma_f32_16x16x32_bf16 v[192:195], a[192:195], a[184:187], v[192:195]\n"
    "buffer_load_dwordx2 v[246:247], v246, s[80:83], s61 offen sc1\n"
    "v_mov_b32 v248, 0\n"
    "v_mov_b32 v249, 0\n"
    "v_mov_b32 v250, 0\n"
    "v_mov_b32 v251, 0\n"
    "v_mfma_f32_16x16x32_bf16 v[224:227], a[192:195], a[188:191], v[224:227]\n"
    "s_waitcnt lgkmcnt(4)\n"
    "v_mfma_f32_16x16x32_bf16 v[228:231], a[196:199], a[188:191], v[228:231]\n"
    "buffer_store_dwordx4 v[248:251], v240, s[44:47], 0 offen sc1\n"
    "s_lshl_b32 s60, s65, 8\n"
    "s_sub_u32 s44, s44, s60\n"
    "s_subb_u32 s45, s45, 0\n"
    "s_lshl_b32 s60, s65, 4\n"
    "s_add_u32 s44, s44, s60\n"
    "s_addc_u32 s45, s45, 0\n"
    "s_mov_b32 s46, 4\n"
    "s_add_u32 m0, s68, 83968\n"
    "s_addk_i32 s59, 16384\n"
    "v_mfma_f32_16x16x32_bf16 v[196:199], a[196:199], a[184:187], v[196:199]\n"
    "buffer_load_dwordx4 v255, s[24:27], s59 offen nt lds\n"
    "s_add_u32 m0, s68, 84992\n"
    "s_addk_i32 s59, 16384\n"
    "v_mfma_f32_16x16x32_bf16 v[164:167], a[196:199], a[180:183], v[164:167]\n"
    "buffer_load_dwordx4 v243, s[24:27], s59 offen nt lds\n"
    "v_mfma_f32_16x16x32_bf16 v[132:135], a[196:199], a[176:179], v[132:135]\n"
    "v_mfma_f32_16x16x32_bf16 v[100:103], a[196:199], a[172:175], v[100:103]\n"
    "v_mfma_f32_16x16x32_bf16 v[68:71], a[196:199], a[168:171], v[68:71]\n"
    "v_mfma_f32_16x16x32_bf16 v[36:39], a[196:199], a[164:167], v[36:39]\n"
    "v_mfma_f32_16x16x32_bf16 v[4:7], a[196:199], a[160:163], v[4:7]\n"
    "s_waitcnt lgkmcnt(2)\n"
    "s_waitcnt vmcnt(15)\n"
    "ds_read_b64_tr_b16 a[208:209], v252 offset:32768\n"
    "v_mfma_f32_16x16x32_bf16 v[8:11], a[200:203], a[160:163], v[8:11]\n"
    "ds_read_b64_tr_b16 a[210:211], v252 offset:33280\n"
    "s_add_u32 s60, s77, 4096\n"
    "v_mfma_f32_16x16x32_bf16 v[40:43], a[200:203], a[164:167], v[40:43]\n"
    "s_add_u32 s82, s60, 4\n"
    "s_and_b32 s82, s82, s86\n"
    "v_mfma_f32_16x16x32_bf16 v[72:75], a[200:203], a[168:171], v[72:75]\n"
    "buffer_store_dword v248, v240, s[80:83], s60 offen sc1\n"
    "v_mfma_f32_16x16x32_bf16 v[104:107], a[200:203], a[172:175], v[104:107]\n"
    "ds_read_b64_tr_b16 a[212:213], v252 offset:36864\n"
    "ds_read_b64_tr_b16 a[214:215], v252 offset:37376\n"
    "v_mfma_f32_16x16x32_bf16 v[136:139], a[200:203], a[176:179], v[136:139]\n"
    "ds_read_b64_tr_b16 a[216:217], v252 offset:32776\n"
    "ds_read_b64_tr_b16 a[218:219], v252 offset:33288\n"
    "v_mfma_f32_16x16x32_bf16 v[168:171], a[200:203], a[180:183], v[168:171]\n"
    "ds_read_b64_tr_b16 a[220:221], v252 offset:36872\n"
    "ds_read_b64_tr_b16 a[222:223], v252 offset:37384\n"
    "v_mfma_f32_16x16x32_bf16 v[200:203], a[200:203], a[184:187], v[200:203]\n"
    "v_mfma_f32_16x16x32_bf16 v[232:235], a[200:203], a[188:191], v[232:235]\n"
    "s_waitcnt lgkmcnt(8)\n"
    "ds_read_b64_tr_b16 a[224:225], v253 offset:32768\n"
    "ds_read_b64_tr_b16 a[226:227], v253 offset:33280\n"
    "v_mfma_f32_16x16x32_bf16 v[236:239], a[204:207], a[188:191], v[236:239]\n"
    "v_mfma_f32_16x16x32_bf16 v[204:207], a[204:207], a[184:187], v[204:207]\n"
    "ds_read_b64_tr_b16 a[228:229], v253 offset:36864\n"
    "ds_read_b64_tr_b16 a[230:231], v253 offset:37376\n"
    "v_mfma_f32_16x16x32_bf16 v[172:175], a[204:207], a[180:183], v[172:175]\n"
    "v_mfma_f32_16x16x32_bf16 v[140:143], a[204:207], a[176:179], v[140:143]\n"
    "ds_read_b64_tr_b16 a[232:233], v253 offset:32776\n"
    "ds_read_b64_tr_b16 a[234:235], v253 offset:33288\n"
    "v_mfma_f32_16x16x32_bf16 v[108:111], a[204:207], a[172:175], v[108:111]\n"
    "v_mfma_f32_16x16x32_bf16 v[76:79], a[204:207], a[168:171], v[76:79]\n"
    "ds_read_b64_tr_b16 a[236:237], v253 offset:36872\n"
    "ds_read_b64_tr_b16 a[238:239], v253 offset:37384\n"
    "v_mfma_f32_16x16x32_bf16 v[44:47], a[204:207], a[164:167], v[44:47]\n"
    "v_mfma_f32_16x16x32_bf16 v[12:15], a[204:207], a[160:163], v[12:15]\n"
    "s_waitcnt lgkmcnt(12)\n"
    "v_lshrrev_b32 v250, 3, v240\n"
    "v_and_b32 v250, 14, v250\n"
    "v_mfma_f32_16x16x32_bf16 v[16:19], a[208:211], a[128:131], v[16:19]\n"
    "v_lshrrev_b32 v248, 4, v240\n"
    "v_and_b32 v248, 0x30, v248\n"
    "v_mfma_f32_16x16x32_bf16 v[16:19], a[212:215], a[160:163], v[16:19]\n"
    "v_or_b32 v250, v248, v250\n"
    "v_lshrrev_b32 v248, 1, v240\n"
    "v_mfma_f32_16x16x32_bf16 v[48:51], a[212:215], a[164:167], v[48:51]\n"
    "v_and_b32 v248, 64, v248\n"
    "v_or_b32 v250, v248, v250\n"
    "v_mfma_f32_16x16x32_bf16 v[48:51], a[208:211], a[132:135], v[48:51]\n"
    "s_add_u32 s60, s77, s65\n"
    "s_lshr_b32 s61, s60, 4\n"
    "s_min_u32 s61, s61, 5\n"
    "s_lshl_b32 s61, s61, 11\n"
    "s_add_u32 s22, s61, 2048\n"
    "s_bfe_u32 s61, s60, 0x10003\n"
    "s_lshl_b32 s61, s61, 10\n"
    "s_and_b32 s62, s60, 1\n"
    "s_lshl_b32 s62, s62, 9\n"
    "s_add_u32 s61, s61, s62\n"
    "s_bfe_u32 s62, s60, 0x20001\n"
    "s_lshl_b32 s62, s62, 7\n"
    "s_add_u32 s61, s61, s62\n"
    "v_add_u32 v250, s61, v250\n"
    "v_mfma_f32_16x16x32_bf16 v[80:83], a[208:211], a[136:139], v[80:83]\n"
    "buffer_load_ushort v248, v250, s[20:23], 0 offen\n"
    "v_mfma_f32_16x16x32_bf16 v[80:83], a[212:215], a[168:171], v[80:83]\n"
    "buffer_load_ushort v249, v250, s[20:23], 0 offen offset:2048\n"
    "v_add_u32 v250, 0x1000, v250\n"
    "v_mfma_f32_16x16x32_bf16 v[112:115], a[212:215], a[172:175], v[112:115]\n"
    "buffer_load_ushort v251, v250, s[20:23], 0 offen\n"
    "v_mfma_f32_16x16x32_bf16 v[112:115], a[208:211], a[140:143], v[112:115]\n"
    "buffer_load_ushort v244, v250, s[20:23], 0 offen offset:2048\n"
    "v_add_u32 v250, 0x1000, v250\n"
    "v_mfma_f32_16x16x32_bf16 v[144:147], a[208:211], a[144:147], v[144:147]\n"
    "v_mfma_f32_16x16x32_bf16 v[144:147], a[212:215], a[176:179], v[144:147]\n"
    "v_mfma_f32_16x16x32_bf16 v[176:179], a[212:215], a[180:183], v[176:179]\n"
    "v_mfma_f32_16x16x32_bf16 v[176:179], a[208:211], a[148:151], v[176:179]\n"
    "v_mfma_f32_16x16x32_bf16 v[208:211], a[208:211], a[152:155], v[208:211]\n"
    "v_mfma_f32_16x16x32_bf16 v[208:211], a[212:215], a[184:187], v[208:211]\n"
    "v_mfma_f32_16x16x32_bf16 a[240:243], a[212:215], a[188:191], a[240:243]\n"
    "v_mfma_f32_16x16x32_bf16 a[240:243], a[208:211], a[156:159], a[240:243]\n"
    "s_waitcnt lgkmcnt(8)\n"
    "v_mfma_f32_16x16x32_bf16 a[244:247], a[216:219], a[156:159], a[244:247]\n"
    "v_mfma_f32_16x16x32_bf16 a[244:247], a[220:223], a[188:191], a[244:247]\n"
    "v_mfma_f32_16x16x32_bf16 v[212:215], a[220:223], a[184:187], v[212:215]\n"
    "v_mfma_f32_16x16x32_bf16 v[212:215], a[216:219], a[152:155], v[212:215]\n"
    "v_mfma_f32_16x16x32_bf16 v[180:183], a[216:219], a[148:151], v[180:183]\n"
    "v_mfma_f32_16x16x32_bf16 v[180:183], a[220:223], a[180:183], v[180:183]\n"
    "v_mfma_f32_16x16x32_bf16 v[148:151], a[220:223], a[176:179], v[148:151]\n"
    "v_mfma_f32_16x16x32_bf16 v[148:151], a[216:219], a[144:147], v[148:151]\n"
    "s_xor_b32 s59, s66, s67\n"
    "s_addk_i32 s67, 128\n"
    "s_cmpk_eq_u32 s67, 1536\n"
    "v_mfma_f32_16x16x32_bf16 v[116:119], a[216:219], a[140:143], v[116:119]\n"
    "s_cselect_b32 s67, 0, s67\n"
    "s_add_u32 m0, s68, 98304\n"
    "s_nop 0\n"
    "v_mfma_f32_16x16x32_bf16 v[116:119], a[220:223], a[172:175], v[116:119]\n"
    "buffer_load_dwordx4 v255, s[24:27], s59 offen  lds\n"
    "v_mfma_f32_16x16x32_bf16 v[84:87], a[220:223], a[168:171], v[84:87]\n"
    "v_mfma_f32_16x16x32_bf16 v[84:87], a[216:219], a[136:139], v[84:87]\n"
    "s_add_u32 m0, s68, 99328\n"
    "s_addk_i32 s59, 16384\n"
    "v_mfma_f32_16x16x32_bf16 v[52:55], a[216:219], a[132:135], v[52:55]\n"
    "buffer_load_dwordx4 v243, s[24:27], s59 offen  lds\n"
    "v_mfma_f32_16x16x32_bf16 v[52:55], a[220:223], a[164:167], v[52:55]\n"
    "v_mfma_f32_16x16x32_bf16 v[20:23], a[220:223], a[160:163], v[20:23]\n"
    "v_mfma_f32_16x16x32_bf16 v[20:23], a[216:219], a[128:131], v[20:23]\n"
    "s_waitcnt lgkmcnt(4)\n"
    "v_add_u32 v252, 0x20000, v252\n"
    "v_add_u32 v253, 0x20000, v253\n"
    "v_mfma_f32_16x16x32_bf16 v[24:27], a[224:227], a[128:131], v[24:27]\n"
    "s_waitcnt vmcnt(14)\n"
    "ds_read_b64_tr_b16 a[192:193], v252 offset:0\n"
    "ds_read_b64_tr_b16 a[194:195], v252 offset:512\n"
    "v_mfma_f32_16x16x32_bf16 v[24:27], a[228:231], a[160:163], v[24:27]\n"
    "s_add_u32 m0, s68, 100352\n"
    "s_addk_i32 s59, 16384\n"
    "v_mfma_f32_16x16x32_bf16 v[56:59], a[228:231], a[164:167], v[56:59]\n"
    "buffer_load_dwordx4 v255, s[24:27], s59 offen  lds\n"
    "v_mfma_f32_16x16x32_bf16 v[56:59], a[224:227], a[132:135], v[56:59]\n"
    "ds_read_b64_tr_b16 a[196:197], v252 offset:4096\n"
    "ds_read_b64_tr_b16 a[198:199], v252 offset:4608\n"
    "v_mfma_f32_16x16x32_bf16 v[88:91], a[224:227], a[136:139], v[88:91]\n"
    "ds_read_b64_tr_b16 a[200:201], v252 offset:8\n"
    "ds_read_b64_tr_b16 a[202:203], v252 offset:520\n"
    "v_mfma_f32_16x16x32_bf16 v[88:91], a[228:231], a[168:171], v[88:91]\n"
    "s_add_u32 m0, s68, 101376\n"
    "s_addk_i32 s59, 16384\n"
    "v_mfma_f32_16x16x32_bf16 v[120:123], a[228:231], a[172:175], v[120:123]\n"
    "buffer_load_dwordx4 v243, s[24:27], s59 offen  lds\n"
    "v_mfma_f32_16x16x32_bf16 v[120:123], a[224:227], a[140:143], v[120:123]\n"
    "ds_read_b64_tr_b16 a[204:205], v252 offset:4104\n"
    "ds_read_b64_tr_b16 a[206:207], v252 offset:4616\n"
    "v_mfma_f32_16x16x32_bf16 v[152:155], a[224:227], a[144:147], v[152:155]\n"
    "ds_read_b64_tr_b16 a[208:209], v253 offset:0\n"
    "ds_read_b64_tr_b16 a[210:211], v253 offset:512\n"
    "v_mfma_f32_16x16x32_bf16 v[152:155], a[228:231], a[176:179], v[152:155]\n"
    "s_waitcnt lgkmcnt(2)\n"
    "s_add_u32 m0, s79, 0\n"
    "s_add_u32 s54, s57, 512\n"
    "v_mfma_f32_16x16x32_bf16 v[184:187], a[228:231], a[180:183], v[184:187]\n"
    "s_mov_b32 s94, 65536\n"
    "s_mov_b32 s95, s75\n"
    "s_add_u32 s92, s72, s54\n"
    "v_mfma_f32_16x16x32_bf16 v[184:187], a[224:227], a[148:151], v[184:187]\n"
    "s_addc_u32 s93, s73, 0\n"
    "buffer_load_dwordx4 v254, s[92:95], 0 offen lds\n"
    "v_mfma_f32_16x16x32_bf16 v[216:219], a[224:227], a[152:155], v[216:219]\n"
    "ds_read_b64_tr_b16 a[212:213], v253 offset:4096\n"
    "ds_read_b64_tr_b16 a[214:215], v253 offset:4608\n"
    "v_mfma_f32_16x16x32_bf16 v[216:219], a[228:231], a[184:187], v[216:219]\n"
    "ds_read_b64_tr_b16 a[216:217], v253 offset:8\n"
    "ds_read_b64_tr_b16 a[218:219], v253 offset:520\n"
    "s_addk_i32 m0, 1024\n"
    "s_add_u32 s92, s92, 16384\n"
    "s_addc_u32 s93, s93, 0\n"
    "buffer_load_dwordx4 v254, s[92:95], 0 offen lds\n"
    "s_waitcnt vmcnt(11)\n"
    "s_barrier\n"
    "v_mfma_f32_16x16x32_bf16 a[248:251], a[228:231], a[188:191], a[248:251]\n"
    "ds_read_b64_tr_b16 a[220:221], v253 offset:4104\n"
    "ds_read_b64_tr_b16 a[222:223], v253 offset:4616\n"
    "v_mfma_f32_16x16x32_bf16 a[248:251], a[224:227], a[156:159], a[248:251]\n"
    "s_addk_i32 m0, 1024\n"
    "s_add_u32 s92, s92, 16384\n"
    "s_addc_u32 s93, s93, 0\n"
    "v_mfma_f32_16x16x32_bf16 a[252:255], a[232:235], a[156:159], a[252:255]\n"
    "buffer_load_dwordx4 v254, s[92:95], 0 offen lds\n"
    "v_mfma_f32_16x16x32_bf16 a[252:255], a[236:239], a[188:191], a[252:255]\n"
    "v_mfma_f32_16x16x32_bf16 v[220:223], a[236:239], a[184:187], v[220:223]\n"
    "ds_read_b128 a[156:159], v241 offset:30720\n"
    "v_mfma_f32_16x16x32_bf16 v[220:223], a[232:235], a[152:155], v[220:223]\n"
    "s_addk_i32 m0, 1024\n"
    "s_add_u32 s92, s92, 16384\n"
    "s_addc_u32 s93, s93, 0\n"
    "v_mfma_f32_16x16x32_bf16 v[188:191], a[232:235], a[148:151], v[188:191]\n"
    "buffer_load_dwordx4 v254, s[92:95], 0 offen lds\n"
    "ds_read_b128 a[188:191], v242 offset:30720\n"
    "v_mfma_f32_16x16x32_bf16 v[188:191], a[236:239], a[180:183], v[188:191]\n"
    "ds_read_b128 a[184:187], v242 offset:28672\n"
    "v_mfma_f32_16x16x32_bf16 v[156:159], a[236:239], a[176:179], v[156:159]\n"
    "ds_read_b128 a[152:155], v241 offset:28672\n"
    "v_mfma_f32_16x16x32_bf16 v[156:159], a[232:235], a[144:147], v[156:159]\n"
    "ds_read_b128 a[148:151], v241 offset:26624\n"
    "s_addk_i32 m0, 1024\n"
    "v_mfma_f32_16x16x32_bf16 v[124:127], a[232:235], a[140:143], v[124:127]\n"
    "s_add_u32 s92, s92, 16384\n"
    "s_addc_u32 s93, s93, 0\n"
    "v_mfma_f32_16x16x32_bf16 v[124:127], a[236:239], a[172:175], v[124:127]\n"
    "buffer_load_dwordx4 v254, s[92:95], 0 offen lds\n"
    "ds_read_b128 a[180:183], v242 offset:26624\n"
    "v_mfma_f32_16x16x32_bf16 v[92:95], a[236:239], a[168:171], v[92:95]\n"
    "ds_read_b128 a[176:179], v242 offset:24576\n"
    "v_mfma_f32_16x16x32_bf16 v[92:95], a[232:235], a[136:139], v[92:95]\n"
    "ds_read_b128 a[144:147], v241 offset:24576\n"
    "v_mfma_f32_16x16x32_bf16 v[60:63], a[232:235], a[132:135], v[60:63]\n"
    "ds_read_b128 a[140:143], v241 offset:22528\n"
    "s_addk_i32 m0, 1024\n"
    "v_mfma_f32_16x16x32_bf16 v[60:63], a[236:239], a[164:167], v[60:63]\n"
    "s_add_u32 s92, s92, 16384\n"
    "s_addc_u32 s93, s93, 0\n"
    "v_mfma_f32_16x16x32_bf16 v[28:31], a[236:239], a[160:163], v[28:31]\n"
    "buffer_load_dwordx4 v254, s[92:95], 0 offen lds\n"
    "ds_read_b128 a[172:175], v242 offset:22528\n"
    "v_mfma_f32_16x16x32_bf16 v[28:31], a[232:235], a[128:131], v[28:31]\n"
    "s_waitcnt lgkmcnt(8)\n"
    "ds_read_b128 a[168:171], v242 offset:20480\n"
    "v_mfma_f32_16x16x32_bf16 v[224:227], a[192:195], a[156:159], v[224:227]\n"
    "ds_read_b128 a[136:139], v241 offset:20480\n"
    "v_mfma_f32_16x16x32_bf16 v[224:227], a[196:199], a[188:191], v[224:227]\n"
    "s_waitcnt lgkmcnt(8)\n"
    "ds_read_b128 a[132:135], v241 offset:18432\n"
    "s_addk_i32 m0, 1024\n"
    "v_mfma_f32_16x16x32_bf16 v[192:195], a[196:199], a[184:187], v[192:195]\n"
    "s_add_u32 s92, s92, 16384\n"
    "s_addc_u32 s93, s93, 0\n"
    "v_mfma_f32_16x16x32_bf16 v[192:195], a[192:195], a[152:155], v[192:195]\n"
    "s_waitcnt lgkmcnt(7)\n"
    "buffer_load_dwordx4 v254, s[92:95], 0 offen lds\n"
    "ds_read_b128 a[164:167], v242 offset:18432\n"
    "ds_read_b128 a[160:163], v242 offset:16384\n"
    "ds_read_b128 a[128:131], v241 offset:16384\n"
    "v_mfma_f32_16x16x32_bf16 v[160:163], a[192:195], a[148:151], v[160:163]\n"
    "s_addk_i32 m0, 1024\n"
    "s_add_u32 s92, s92, 16384\n"
    "s_addc_u32 s93, s93, 0\n"
    "v_mfma_f32_16x16x32_bf16 v[160:163], a[196:199], a[180:183], v[160:163]\n"
    "s_waitcnt lgkmcnt(8)\n"
    "buffer_load_dwordx4 v254, s[92:95], 0 offen lds\n"
    "v_mfma_f32_16x16x32_bf16 v[128:131], a[196:199], a[176:179], v[128:131]\n"
    "s_add_u32 s57, s57, 131072\n"
    "s_cmp_ge_u32 s57, 1572864\n"
    "s_cselect_b32 s57, s70, s57\n"
    "v_mfma_f32_16x16x32_bf16 v[128:131], a[192:195], a[144:147], v[128:131]\n"
    "s_waitcnt lgkmcnt(6)\n"
    "s_add_u32 m0, s79, 32768\n"
    "s_add_u32 s54, s57, 0\n"
    "v_mfma_f32_16x16x32_bf16 v[96:99], a[192:195], a[140:143], v[96:99]\n"
    "s_add_u32 s92, s72, s54\n"
    "s_addc_u32 s93, s73, 0\n"
    "v_mfma_f32_16x16x32_bf16 v[96:99], a[196:199], a[172:175], v[96:99]\n"
    "s_waitcnt lgkmcnt(4)\n"
    "buffer_load_dwordx4 v254, s[92:95], 0 offen lds\n"
    "v_mfma_f32_16x16x32_bf16 v[64:67], a[196:199], a[168:171], v[64:67]\n"
    "v_mfma_f32_16x16x32_bf16 v[64:67], a[192:195], a[136:139], v[64:67]\n"
    "s_waitcnt lgkmcnt(2)\n"
    "s_addk_i32 m0, 1024\n"
    "s_add_u32 s92, s92, 16384\n"
    "v_mfma_f32_16x16x32_bf16 v[32:35], a[192:195], a[132:135], v[32:35]\n"
    "s_addc_u32 s93, s93, 0\n"
    "buffer_load_dwordx4 v254, s[92:95], 0 offen lds\n"
    "v_mfma_f32_16x16x32_bf16 v[32:35], a[196:199], a[164:167], v[32:35]\n"
    "s_waitcnt lgkmcnt(0)\n"
    "v_mfma_f32_16x16x32_bf16 v[0:3], a[196:199], a[160:163], v[0:3]\n"
    "v_mfma_f32_16x16x32_bf16 v[0:3], a[192:195], a[128:131], v[0:3]\n"
    "s_addk_i32 m0, 1024\n"
    "s_add_u32 s92, s92, 16384\n"
    "s_addc_u32 s93, s93, 0\n"
    "v_mfma_f32_16x16x32_bf16 v[4:7], a[200:203], a[128:131], v[4:7]\n"
    "buffer_load_dwordx4 v254, s[92:95], 0 offen lds\n"
    "v_mfma_f32_16x16x32_bf16 v[4:7], a[204:207], a[160:163], v[4:7]\n"
    "v_mfma_f32_16x16x32_bf16 v[36:39], a[204:207], a[164:167], v[36:39]\n"
    "v_mfma_f32_16x16x32_bf16 v[36:39], a[200:203], a[132:135], v[36:39]\n"
    "s_addk_i32 m0, 1024\n"
    "s_add_u32 s92, s92, 16384\n"
    "s_addc_u32 s93, s93, 0\n"
    "v_mfma_f32_16x16x32_bf16 v[68:71], a[200:203], a[136:139], v[68:71]\n"
    "buffer_load_dwordx4 v254, s[92:95], 0 offen lds\n"
    "v_mfma_f32_16x16x32_bf16 v[68:71], a[204:207], a[168:171], v[68:71]\n"
    "v_mfma_f32_16x16x32_bf16 v[100:103], a[204:207], a[172:175], v[100:103]\n"
    "v_mfma_f32_16x16x32_bf16 v[100:103], a[200:203], a[140:143], v[100:103]\n"
    "s_addk_i32 m0, 1024\n"
    "s_add_u32 s92, s92, 16384\n"
    "s_addc_u32 s93, s93, 0\n"
    "v_mfma_f32_16x16x32_bf16 v[132:135], a[200:203], a[144:147], v[132:135]\n"
    "buffer_load_dwordx4 v254, s[92:95], 0 offen lds\n"
    "s_bitcmp1_b32 s85, 1\n"
    "s_cbranch_scc1 CP1_%=\n"
    "CR1_%=:\n"
    "v_mfma_f32_16x16x32_bf16 v[132:135], a[204:207], a[176:179], v[132:135]\n"
    "v_mfma_f32_16x16x32_bf16 v[164:167], a[204:207], a[180:183], v[164:167]\n"
    "v_mfma_f32_16x16x32_bf16 v[164:167], a[200:203], a[148:151], v[164:167]\n"
    "s_addk_i32 m0, 1024\n"
    "s_add_u32 s92, s92, 16384\n"
    "s_addc_u32 s93, s93, 0\n"
    "v_mfma_f32_16x16x32_bf16 v[196:199], a[200:203], a[152:155], v[196:199]\n"
    "buffer_load_dwordx4 v254, s[92:95], 0 offen lds\n"
    "v_mfma_f32_16x16x32_bf16 v[196:199], a[204:207], a[184:187], v[196:199]\n"
    "v_mfma_f32_16x16x32_bf16 v[228:231], a[204:207], a[188:191], v[228:231]\n"
    "v_mfma_f32_16x16x32_bf16 v[228:231], a[200:203], a[156:159], v[228:231]\n"
    "v_add_u32 v252, 0xfffe0000, v252\n"
    "v_add_u32 v253, 0xfffe0000, v253\n"
    "v_mfma_f32_16x16x32_bf16 v[232:235], a[208:211], a[156:159], v[232:235]\n"
    "s_waitcnt vmcnt(6)\n"
    "ds_read_b64_tr_b16 a[224:225], v252 offset:0\n"
    "ds_read_b64_tr_b16 a[226:227], v252 offset:512\n"
    "v_mfma_f32_16x16x32_bf16 v[232:235], a[212:215], a[188:191], v[232:235]\n"
    "s_addk_i32 m0, 1024\n"
    "s_add_u32 s92, s92, 16384\n"
    "s_addc_u32 s93, s93, 0\n"
    "v_mfma_f32_16x16x32_bf16 v[200:203], a[212:215], a[184:187], v[200:203]\n"
    "buffer_load_dwordx4 v254, s[92:95], 0 offen lds\n"
    "v_lshlrev_b32 v248, 16, v248\n"
    "v_lshlrev_b32 v249, 16, v249\n"
    "v_add_f32 v248, v249, v248\n"
    "v_lshlrev_b32 v251, 16, v251\n"
    "v_add_f32 v248, v251, v248\n"
    "v_lshlrev_b32 v244, 16, v244\n"
    "v_add_f32 v248, v244, v248\n"
    "v_mfma_f32_16x16x32_bf16 v[200:203], a[208:211], a[152:155], v[200:203]\n"
    "buffer_load_ushort v249, v250, s[20:23], 0 offen\n"
    "v_mfma_f32_16x16x32_bf16 v[168:171], a[208:211], a[148:151], v[168:171]\n"
    "buffer_load_ushort v251, v250, s[20:23], 0 offen offset:2048\n"
    "v_add_u32 v250, 0x1000, v250\n"
    "ds_read_b64_tr_b16 a[228:229], v252 offset:4096\n"
    "ds_read_b64_tr_b16 a[230:231], v252 offset:4608\n"
    "v_mfma_f32_16x16x32_bf16 v[168:171], a[212:215], a[180:183], v[168:171]\n"
    "ds_read_b64_tr_b16 a[232:233], v252 offset:8\n"
    "ds_read_b64_tr_b16 a[234:235], v252 offset:520\n"
    "v_mfma_f32_16x16x32_bf16 v[136:139], a[212:215], a[176:179], v[136:139]\n"
    "s_addk_i32 m0, 1024\n"
    "s_add_u32 s92, s92, 16384\n"
    "s_addc_u32 s93, s93, 0\n"
    "v_mfma_f32_16x16x32_bf16 v[136:139], a[208:211], a[144:147], v[136:139]\n"
    "buffer_load_dwordx4 v254, s[92:95], 0 offen lds\n"
    "ds_read_b64_tr_b16 a[236:237], v252 offset:4104\n"
    "v_mfma_f32_16x16x32_bf16 v[104:107], a[208:211], a[140:143], v[104:107]\n"
    "ds_read_b64_tr_b16 a[238:239], v252 offset:4616\n"
    "ds_read_b64_tr_b16 a[192:193], v253 offset:0\n"
    "v_mfma_f32_16x16x32_bf16 v[104:107], a[212:215], a[172:175], v[104:107]\n"
    "ds_read_b64_tr_b16 a[194:195], v253 offset:512\n"
    "s_add_u32 m0, s68, 114688\n"
    "v_mfma_f32_16x16x32_bf16 v[72:75], a[212:215], a[168:171], v[72:75]\n"
    "s_add_u32 s60, s78, 0\n"
    "s_or_b32 s60, s60, s71\n"
    "v_mfma_f32_16x16x32_bf16 v[72:75], a[208:211], a[136:139], v[72:75]\n"
    "buffer_load_dwordx4 v254, s[72:75], s60 offen nt lds\n"
    "v_mfma_f32_16x16x32_bf16 v[40:43], a[208:211], a[132:135], v[40:43]\n"
    "ds_read_b64_tr_b16 a[196:197], v253 offset:4096\n"
    "ds_read_b64_tr_b16 a[198:199], v253 offset:4608\n"
    "v_mfma_f32_16x16x32_bf16 v[40:43], a[212:215], a[164:167], v[40:43]\n"
    "ds_read_b64_tr_b16 a[200:201], v253 offset:8\n"
    "ds_read_b64_tr_b16 a[202:203], v253 offset:520\n"
    "v_mfma_f32_16x16x32_bf16 v[8:11], a[212:215], a[160:163], v[8:11]\n"
    "s_add_u32 m0, s68, 115712\n"
    "s_add_u32 s60, s78, 16384\n"
    "s_or_b32 s60, s60, s71\n"
    "v_mfma_f32_16x16x32_bf16 v[8:11], a[208:211], a[128:131], v[8:11]\n"
    "buffer_load_dwordx4 v254, s[72:75], s60 offen nt lds\n"
    "v_mfma_f32_16x16x32_bf16 v[12:15], a[216:219], a[128:131], v[12:15]\n"
    "ds_read_b64_tr_b16 a[204:205], v253 offset:4104\n"
    "ds_read_b64_tr_b16 a[206:207], v253 offset:4616\n"
    "v_mfma_f32_16x16x32_bf16 v[12:15], a[220:223], a[160:163], v[12:15]\n"
    "s_add_u32 m0, s68, 116736\n"
    "s_add_u32 s60, s78, 32768\n"
    "s_or_b32 s60, s60, s71\n"
    "v_mfma_f32_16x16x32_bf16 v[44:47], a[220:223], a[164:167], v[44:47]\n"
    "buffer_load_dwordx4 v254, s[72:75], s60 offen nt lds\n"
    "v_mfma_f32_16x16x32_bf16 v[44:47], a[216:219], a[132:135], v[44:47]\n"
    "s_add_u32 m0, s68, 117760\n"
    "s_add_u32 s60, s78, 49152\n"
    "s_or_b32 s60, s60, s71\n"
    "v_mfma_f32_16x16x32_bf16 v[76:79], a[216:219], a[136:139], v[76:79]\n"
    "buffer_load_dwordx4 v254, s[72:75], s60 offen nt lds\n"
    "v_mfma_f32_16x16x32_bf16 v[76:79], a[220:223], a[168:171], v[76:79]\n"
    "v_mfma_f32_16x16x32_bf16 v[108:111], a[220:223], a[172:175], v[108:111]\n"
    "v_mfma_f32_16x16x32_bf16 v[108:111], a[216:219], a[140:143], v[108:111]\n"
    "s_xor_b32 s59, s66, s67\n"
    "s_addk_i32 s67, 128\n"
    "s_cmpk_eq_u32 s67, 1536\n"
    "v_mfma_f32_16x16x32_bf16 v[140:143], a[216:219], a[144:147], v[140:143]\n"
    "s_cselect_b32 s67, 0, s67\n"
    "s_add_u32 m0, s68, 65536\n"
    "s_nop 0\n"
    "v_mfma_f32_16x16x32_bf16 v[140:143], a[220:223], a[176:179], v[140:143]\n"
    "buffer_load_dwordx4 v255, s[24:27], s59 offen  lds\n"
    "v_mfma_f32_16x16x32_bf16 v[172:175], a[220:223], a[180:183], v[172:175]\n"
    "v_mfma_f32_16x16x32_bf16 v[172:175], a[216:219], a[148:151], v[172:175]\n"
    "s_add_u32 m0, s68, 66560\n"
    "s_addk_i32 s59, 16384\n"
    "v_mfma_f32_16x16x32_bf16 v[204:207], a[216:219], a[152:155], v[204:207]\n"
    "buffer_load_dwordx4 v243, s[24:27], s59 offen  lds\n"
    "v_mfma_f32_16x16x32_bf16 v[204:207], a[220:223], a[184:187], v[204:207]\n"
    "v_mfma_f32_16x16x32_bf16 v[236:239], a[220:223], a[188:191], v[236:239]\n"
    "v_mfma_f32_16x16x32_bf16 v[236:239], a[216:219], a[156:159], v[236:239]\n"
    "s_waitcnt lgkmcnt(12)\n"
    "s_add_u32 m0, s68, 67584\n"
    "s_addk_i32 s59, 16384\n"
    "v_mfma_f32_16x16x32_bf16 a[240:243], a[224:227], a[156:159], a[240:243]\n"
    "buffer_load_dwordx4 v255, s[24:27], s59 offen  lds\n"
    "v_mfma_f32_16x16x32_bf16 a[240:243], a[228:231], a[188:191], a[240:243]\n"
    "v_add_u32 v245, 1, v245\n"
    "v_add_u32 v246, 1, v246\n"
    "v_mfma_f32_16x16x32_bf16 v[208:211], a[228:231], a[184:187], v[208:211]\n"
    "v_readfirstlane_b32 s88, v245\n"
    "v_lshrrev_b32 v247, 2, v240\n"
    "v_mfma_f32_16x16x32_bf16 v[208:211], a[224:227], a[152:155], v[208:211]\n"
    "s_lshl_b32 s61, s65, 8\n"
    "s_mov_b32 s82, s87\n"
    "v_mfma_f32_16x16x32_bf16 v[176:179], a[224:227], a[148:151], v[176:179]\n"
    "buffer_store_dword v246, v247, s[80:83], s61 offen offset:2048 sc1\n"
    "v_mfma_f32_16x16x32_bf16 v[176:179], a[228:231], a[180:183], v[176:179]\n"
    "s_add_u32 m0, s68, 68608\n"
    "s_addk_i32 s59, 16384\n"
    "v_mfma_f32_16x16x32_bf16 v[144:147], a[228:231], a[176:179], v[144:147]\n"
    "buffer_load_dwordx4 v243, s[24:27], s59 offen  lds\n"
    "v_mfma_f32_16x16x32_bf16 v[144:147], a[224:227], a[144:147], v[144:147]\n"
    "s_add_u32 m0, s79, 131072\n"
    "s_add_u32 s54, s57, 512\n"
    "s_add_u32 s92, s72, s54\n"
    "v_mfma_f32_16x16x32_bf16 v[112:115], a[224:227], a[140:143], v[112:115]\n"
    "s_addc_u32 s93, s73, 0\n"
    "buffer_load_dwordx4 v254, s[92:95], 0 offen lds\n"
    "v_mfma_f32_16x16x32_bf16 v[112:115], a[228:231], a[172:175], v[112:115]\n"
    "v_mfma_f32_16x16x32_bf16 v[80:83], a[228:231], a[168:171], v[80:83]\n"
    "v_mfma_f32_16x16x32_bf16 v[80:83], a[224:227], a[136:139], v[80:83]\n"
    "s_addk_i32 m0, 1024\n"
    "s_add_u32 s92, s92, 16384\n"
    "s_addc_u32 s93, s93, 0\n"
    "v_mfma_f32_16x16x32_bf16 v[48:51], a[224:227], a[132:135], v[48:51]\n"
    "buffer_load_dwordx4 v254, s[92:95], 0 offen lds\n"
    "v_mfma_f32_16x16x32_bf16 v[48:51], a[228:231], a[164:167], v[48:51]\n"
    "v_mfma_f32_16x16x32_bf16 v[16:19], a[228:231], a[160:163], v[16:19]\n"
    "v_mfma_f32_16x16x32_bf16 v[16:19], a[224:227], a[128:131], v[16:19]\n"
    "s_waitcnt lgkmcnt(8)\n"
    "s_addk_i32 m0, 1024\n"
    "s_add_u32 s92, s92, 16384\n"
    "v_mfma_f32_16x16x32_bf16 v[20:23], a[232:235], a[128:131], v[20:23]\n"
    "s_addc_u32 s93, s93, 0\n"
    "buffer_load_dwordx4 v254, s[92:95], 0 offen lds\n"
    "v_mfma_f32_16x16x32_bf16 v[20:23], a[236:239], a[160:163], v[20:23]\n"
    "v_mfma_f32_16x16x32_bf16 v[52:55], a[236:239], a[164:167], v[52:55]\n"
    "v_mfma_f32_16x16x32_bf16 v[52:55], a[232:235], a[132:135], v[52:55]\n"
    "s_addk_i32 m0, 1024\n"
    "s_add_u32 s92, s92, 16384\n"
    "s_addc_u32 s93, s93, 0\n"
    "v_mfma_f32_16x16x32_bf16 v[84:87], a[232:235], a[136:139], v[84:87]\n"
    "buffer_load_dwordx4 v254, s[92:95], 0 offen lds\n"
    "v_mfma_f32_16x16x32_bf16 v[84:87], a[236:239], a[168:171], v[84:87]\n"
    "v_mfma_f32_16x16x32_bf16 v[116:119], a[236:239], a[172:175], v[116:119]\n"
    "v_mfma_f32_16x16x32_bf16 v[116:119], a[232:235], a[140:143], v[116:119]\n"
    "s_addk_i32 m0, 1024\n"
    "s_add_u32 s92, s92, 16384\n"
    "s_addc_u32 s93, s93, 0\n"
    "v_mfma_f32_16x16x32_bf16 v[148:151], a[232:235], a[144:147], v[148:151]\n"
    "buffer_load_dwordx4 v254, s[92:95], 0 offen lds\n"
    "v_mfma_f32_16x16x32_bf16 v[148:151], a[236:239], a[176:179], v[148:151]\n"
    "v_mfma_f32_16x16x32_bf16 v[180:183], a[236:239], a[180:183], v[180:183]\n"
    "v_mfma_f32_16x16x32_bf16 v[180:183], a[232:235], a[148:151], v[180:183]\n"
    "s_addk_i32 m0, 1024\n"
    "s_add_u32 s92, s92, 16384\n"
    "s_addc_u32 s93, s93, 0\n"
    "v_mfma_f32_16x16x32_bf16 v[212:215], a[232:235], a[152:155], v[212:215]\n"
    "buffer_load_dwordx4 v254, s[92:95], 0 offen lds\n"
    "v_mfma_f32_16x16x32_bf16 v[212:215], a[236:239], a[184:187], v[212:215]\n"
    "v_mfma_f32_16x16x32_bf16 a[244:247], a[236:239], a[188:191], a[244:247]\n"
    "v_mfma_f32_16x16x32_bf16 a[244:247], a[232:235], a[156:159], a[244:247]\n"
    "s_waitcnt lgkmcnt(4)\n"
    "s_waitcnt vmcnt(15)\n"
    "ds_read_b64_tr_b16 a[208:209], v252 offset:32768\n"
    "v_mfma_f32_16x16x32_bf16 a[248:251], a[192:195], a[156:159], a[248:251]\n"
    "ds_read_b64_tr_b16 a[210:211], v252 offset:33280\n"
    "s_addk_i32 m0, 1024\n"
    "v_mfma_f32_16x16x32_bf16 a[248:251], a[196:199], a[188:191], a[248:251]\n"
    "s_add_u32 s92, s92, 16384\n"
    "s_addc_u32 s93, s93, 0\n"
    "v_mfma_f32_16x16x32_bf16 v[216:219], a[196:199], a[184:187], v[216:219]\n"
    "buffer_load_dwordx4 v254, s[92:95], 0 offen lds\n"
    "ds_read_b64_tr_b16 a[212:213], v252 offset:36864\n"
    "v_mfma_f32_16x16x32_bf16 v[216:219], a[192:195], a[152:155], v[216:219]\n"
    "ds_read_b64_tr_b16 a[214:215], v252 offset:37376\n"
    "ds_read_b64_tr_b16 a[216:217], v252 offset:32776\n"
    "v_mfma_f32_16x16x32_bf16 v[184:187], a[192:195], a[148:151], v[184:187]\n"
    "ds_read_b64_tr_b16 a[218:219], v252 offset:33288\n"
    "s_addk_i32 m0, 1024\n"
    "v_mfma_f32_16x16x32_bf16 v[184:187], a[196:199], a[180:183], v[184:187]\n"
    "s_add_u32 s92, s92, 16384\n"
    "s_addc_u32 s93, s93, 0\n"
    "v_mfma_f32_16x16x32_bf16 v[152:155], a[196:199], a[176:179], v[152:155]\n"
    "buffer_load_dwordx4 v254, s[92:95], 0 offen lds\n"
    "s_add_u32 s57, s57, 131072\n"
    "v_mfma_f32_16x16x32_bf16 v[152:155], a[192:195], a[144:147], v[152:155]\n"
    "s_cmp_ge_u32 s57, 1572864\n"
    "s_cselect_b32 s57, s70, s57\n"
    "ds_read_b64_tr_b16 a[220:221], v252 offset:36872\n"
    "v_mfma_f32_16x16x32_bf16 v[120:123], a[192:195], a[140:143], v[120:123]\n"
    "ds_read_b64_tr_b16 a[222:223], v252 offset:37384\n"
    "ds_read_b64_tr_b16 a[224:225], v253 offset:32768\n"
    "v_mfma_f32_16x16x32_bf16 v[120:123], a[196:199], a[172:175], v[120:123]\n"
    "ds_read_b64_tr_b16 a[226:227], v253 offset:33280\n"
    "s_waitcnt lgkmcnt(10)\n"
    "v_mfma_f32_16x16x32_bf16 v[88:91], a[196:199], a[168:171], v[88:91]\n"
    "s_add_u32 m0, s79, 0\n"
    "s_add_u32 s54, s57, 0\n"
    "s_add_u32 s92, s72, s54\n"
    "v_mfma_f32_16x16x32_bf16 v[88:91], a[192:195], a[136:139], v[88:91]\n"
    "s_addc_u32 s93, s73, 0\n"
    "buffer_load_dwordx4 v254, s[92:95], 0 offen lds\n"
    "v_mfma_f32_16x16x32_bf16 v[56:59], a[192:195], a[132:135], v[56:59]\n"
    "ds_read_b64_tr_b16 a[228:229], v253 offset:36864\n"
    "ds_read_b64_tr_b16 a[230:231], v253 offset:37376\n"
    "v_mfma_f32_16x16x32_bf16 v[56:59], a[196:199], a[164:167], v[56:59]\n"
    "ds_read_b64_tr_b16 a[232:233], v253 offset:32776\n"
    "ds_read_b64_tr_b16 a[234:235], v253 offset:33288\n"
    "s_addk_i32 m0, 1024\n"
    "s_add_u32 s92, s92, 16384\n"
    "s_addc_u32 s93, s93, 0\n"
    "buffer_load_dwordx4 v254, s[92:95], 0 offen lds\n"
    "s_barrier\n"
    "v_mfma_f32_16x16x32_bf16 v[24:27], a[196:199], a[160:163], v[24:27]\n"
    "ds_read_b64_tr_b16 a[236:237], v253 offset:36872\n"
    "ds_read_b64_tr_b16 a[238:239], v253 offset:37384\n"
    "v_mfma_f32_16x16x32_bf16 v[24:27], a[192:195], a[128:131], v[24:27]\n"
    "s_addk_i32 m0, 1024\n"
    "s_add_u32 s92, s92, 16384\n"
    "s_addc_u32 s93, s93, 0\n"
    "v_mfma_f32_16x16x32_bf16 v[28:31], a[200:203], a[128:131], v[28:31]\n"
    "buffer_load_dwordx4 v254, s[92:95], 0 offen lds\n"
    "s_add_u32 s60, s77, s65\n"
    "v_mfma_f32_16x16x32_bf16 v[28:31], a[204:207], a[160:163], v[28:31]\n"
    "s_bfe_u32 s61, s60, 0x30001\n"
    "s_mul_i32 s61, s61, 6\n"
    "s_lshr_b32 s62, s60, 4\n"
    "v_mfma_f32_16x16x32_bf16 v[60:63], a[204:207], a[164:167], v[60:63]\n"
    "s_add_u32 s61, s61, s62\n"
    "s_lshl_b32 s61, s61, 1\n"
    "s_and_b32 s62, s60, 1\n"
    "v_mfma_f32_16x16x32_bf16 v[60:63], a[200:203], a[132:135], v[60:63]\n"
    "s_add_u32 s61, s61, s62\n"
    "s_lshl_b32 s61, s61, 10\n"
    "s_cmp_lt_u32 s60, 96\n"
    "s_cselect_b32 s61, s61, 98304\n"
    "v_lshlrev_b32 v249, 16, v249\n"
    "v_add_f32 v248, v249, v248\n"
    "v_lshlrev_b32 v251, 16, v251\n"
    "v_add_f32 v248, v251, v248\n"
    "v_add_f32 v248, 0xc3ffaa80, v248\n"
    "v_mfma_f32_16x16x32_bf16 v[92:95], a[200:203], a[136:139], v[92:95]\n"
    "buffer_store_dwordx4 v[248:251], v240, s[36:39], s61 offen sc1\n"
    "s_lshl_b32 s61, s64, 2\n"
    "s_add_u32 s61, s61, s65\n"
    "s_mul_i32 s55, s61, 12288\n"
    "s_mul_i32 s61, s61, 1572864\n"
    "s_add_u32 s61, s61, 196608\n"
    "s_mov_b64 s[20:21], s[6:7]\n"
    "s_add_u32 s20, s20, s61\n"
    "s_addc_u32 s21, s21, 0\n"
    "s_and_b32 s21, s21, 0xffff\n"
    "s_mov_b32 s22, 8192\n"
    "v_mfma_f32_16x16x32_bf16 v[92:95], a[204:207], a[168:171], v[92:95]\n"
    "ds_read_b128 a[128:131], v241 offset:32768\n"
    "s_addk_i32 m0, 1024\n"
    "v_mfma_f32_16x16x32_bf16 v[124:127], a[204:207], a[172:175], v[124:127]\n"
    "s_add_u32 s92, s92, 16384\n"
    "s_addc_u32 s93, s93, 0\n"
    "v_mfma_f32_16x16x32_bf16 v[124:127], a[200:203], a[140:143], v[124:127]\n"
    "buffer_load_dwordx4 v254, s[92:95], 0 offen lds\n"
    "ds_read_b128 a[160:163], v242 offset:32768\n"
    "ds_read_b128 a[164:167], v242 offset:34816\n"
    "v_mfma_f32_16x16x32_bf16 v[156:159], a[200:203], a[144:147], v[156:159]\n"
    "ds_read_b128 a[132:135], v241 offset:34816\n"
    "v_mfma_f32_16x16x32_bf16 v[156:159], a[204:207], a[176:179], v[156:159]\n"
    "ds_read_b128 a[136:139], v241 offset:36864\n"
    "s_addk_i32 m0, 1024\n"
    "v_mfma_f32_16x16x32_bf16 v[188:191], a[204:207], a[180:183], v[188:191]\n"
    "s_add_u32 s92, s92, 16384\n"
    "s_addc_u32 s93, s93, 0\n"
    "v_mfma_f32_16x16x32_bf16 v[188:191], a[200:203], a[148:151], v[188:191]\n"
    "buffer_load_dwordx4 v254, s[92:95], 0 offen lds\n"
    "ds_read_b128 a[168:171], v242 offset:36864\n"
    "ds_read_b128 a[172:175], v242 offset:38912\n"
    "v_mfma_f32_16x16x32_bf16 v[220:223], a[200:203], a[152:155], v[220:223]\n"
    "ds_read_b128 a[140:143], v241 offset:38912\n"
    "v_mfma_f32_16x16x32_bf16 v[220:223], a[204:207], a[184:187], v[220:223]\n"
    "ds_read_b128 a[144:147], v241 offset:40960\n"
    "s_addk_i32 m0, 1024\n"
    "v_mfma_f32_16x16x32_bf16 a[252:255], a[204:207], a[188:191], a[252:255]\n"
    "s_add_u32 s92, s92, 16384\n"
    "s_addc_u32 s93, s93, 0\n"
    "v_mfma_f32_16x16x32_bf16 a[252:255], a[200:203], a[156:159], a[252:255]\n"
    "s_waitcnt lgkmcnt(7)\n"
    "buffer_load_dwordx4 v254, s[92:95], 0 offen lds\n"
    "ds_read_b128 a[176:179], v242 offset:40960\n"
    "ds_read_b128 a[180:183], v242 offset:43008\n"
    "v_mfma_f32_16x16x32_bf16 v[0:3], a[208:211], a[128:131], v[0:3]\n"
    "ds_read_b128 a[148:151], v241 offset:43008\n"
    "v_mfma_f32_16x16x32_bf16 v[0:3], a[212:215], a[160:163], v[0:3]\n"
    "s_waitcnt lgkmcnt(8)\n"
    "ds_read_b128 a[152:155], v241 offset:45056\n"
    "s_addk_i32 m0, 1024\n"
    "v_mfma_f32_16x16x32_bf16 v[32:35], a[212:215], a[164:167], v[32:35]\n"
    "s_add_u32 s92, s92, 16384\n"
    "s_addc_u32 s93, s93, 0\n"
    "v_mfma_f32_16x16x32_bf16 v[32:35], a[208:211], a[132:135], v[32:35]\n"
    "s_waitcnt lgkmcnt(7)\n"
    "buffer_load_dwordx4 v254, s[92:95], 0 offen lds\n"
    "ds_read_b128 a[184:187], v242 offset:45056\n"
    "ds_read_b128 a[188:191], v242 offset:47104\n"
    "ds_read_b128 a[156:159], v241 offset:47104\n"
    "v_mfma_f32_16x16x32_bf16 v[64:67], a[208:211], a[136:139], v[64:67]\n"
    "s_addk_i32 m0, 1024\n"
    "s_add_u32 s92, s92, 16384\n"
    "s_addc_u32 s93, s93, 0\n"
    "v_mfma_f32_16x16x32_bf16 v[64:67], a[212:215], a[168:171], v[64:67]\n"
    "s_waitcnt lgkmcnt(8)\n"
    "buffer_load_dwordx4 v254, s[92:95], 0 offen lds\n"
    "v_mfma_f32_16x16x32_bf16 v[96:99], a[212:215], a[172:175], v[96:99]\n"
    "v_mfma_f32_16x16x32_bf16 v[96:99], a[208:211], a[140:143], v[96:99]\n"
    "s_waitcnt lgkmcnt(6)\n"
    "s_add_u32 m0, s79, 32768\n"
    "s_add_u32 s54, s57, 512\n"
    "v_mfma_f32_16x16x32_bf16 v[128:131], a[208:211], a[144:147], v[128:131]\n"
    "s_add_u32 s92, s72, s54\n"
    "s_addc_u32 s93, s73, 0\n"
    "v_mfma_f32_16x16x32_bf16 v[128:131], a[212:215], a[176:179], v[128:131]\n"
    "s_waitcnt lgkmcnt(4)\n"
    "buffer_load_dwordx4 v254, s[92:95], 0 offen lds\n"
    "v_mfma_f32_16x16x32_bf16 v[160:163], a[212:215], a[180:183], v[160:163]\n"
    "v_mfma_f32_16x16x32_bf16 v[160:163], a[208:211], a[148:151], v[160:163]\n"
    "s_waitcnt lgkmcnt(2)\n"
    "s_addk_i32 m0, 1024\n"
    "s_add_u32 s92, s92, 16384\n"
    "v_mfma_f32_16x16x32_bf16 v[192:195], a[208:211], a[152:155], v[192:195]\n"
    "s_addc_u32 s93, s93, 0\n"
    "buffer_load_dwordx4 v254, s[92:95], 0 offen lds\n"
    "v_mfma_f32_16x16x32_bf16 v[192:195], a[212:215], a[184:187], v[192:195]\n"
    "s_waitcnt lgkmcnt(0)\n"
    "v_mfma_f32_16x16x32_bf16 v[224:227], a[212:215], a[188:191], v[224:227]\n"
    "v_mfma_f32_16x16x32_bf16 v[224:227], a[208:211], a[156:159], v[224:227]\n"
    "s_addk_i32 m0, 1024\n"
    "s_add_u32 s92, s92, 16384\n"
    "s_addc_u32 s93, s93, 0\n"
    "v_mfma_f32_16x16x32_bf16 v[228:231], a[216:219], a[156:159], v[228:231]\n"
    "buffer_load_dwordx4 v254, s[92:95], 0 offen lds\n"
    "v_mfma_f32_16x16x32_bf16 v[228:231], a[220:223], a[188:191], v[228:231]\n"
    "v_mfma_f32_16x16x32_bf16 v[196:199], a[220:223], a[184:187], v[196:199]\n"
    "v_mfma_f32_16x16x32_bf16 v[196:199], a[216:219], a[152:155], v[196:199]\n"
    "s_addk_i32 m0, 1024\n"
    "s_add_u32 s92, s92, 16384\n"
    "s_addc_u32 s93, s93, 0\n"
    "v_mfma_f32_16x16x32_bf16 v[164:167], a[216:219], a[148:151], v[164:167]\n"
    "buffer_load_dwordx4 v254, s[92:95], 0 offen lds\n"
    "s_lshl_b32 s59, s64, 7\n"
    "v_mfma_f32_16x16x32_bf16 v[164:167], a[220:223], a[180:183], v[164:167]\n"
    "s_or_b32 s66, s66, s59\n"
    "s_xor_b32 s59, s66, s67\n"
    "s_addk_i32 s67, 128\n"
    "v_mfma_f32_16x16x32_bf16 v[132:135], a[220:223], a[176:179], v[132:135]\n"
    "s_cmpk_eq_u32 s67, 1536\n"
    "s_cselect_b32 s67, 0, s67\n"
    "s_add_u32 m0, s68, 81920\n"
    "v_mfma_f32_16x16x32_bf16 v[132:135], a[216:219], a[144:147], v[132:135]\n"
    "s_mov_b32 s98, 65536\n"
    "s_mov_b32 s99, s27\n"
    "s_add_u32 s96, s24, s59\n"
    "v_mfma_f32_16x16x32_bf16 v[100:103], a[216:219], a[140:143], v[100:103]\n"
    "s_addc_u32 s97, s25, 0\n"
    "buffer_load_dwordx4 v255, s[96:99], 0 offen lds\n"
    "s_add_u32 m0, s68, 82944\n"
    "s_add_u32 s96, s96, 16384\n"
    "s_addc_u32 s97, s97, 0\n"
    "buffer_load_dwordx4 v243, s[96:99], 0 offen lds\n"
    "s_bitcmp1_b32 s85, 2\n"
    "s_cbranch_scc1 CP2_%=\n"
    "CR2_%=:\n"
    "v_mfma_f32_16x16x32_bf16 v[100:103], a[220:223], a[172:175], v[100:103]\n"
    "v_mfma_f32_16x16x32_bf16 v[68:71], a[220:223], a[168:171], v[68:71]\n"
    "s_add_u32 m0, s68, 83968\n"
    "s_add_u32 s96, s96, 16384\n"
    "s_addc_u32 s97, s97, 0\n"
    "v_mfma_f32_16x16x32_bf16 v[68:71], a[216:219], a[136:139], v[68:71]\n"
    "buffer_load_dwordx4 v255, s[96:99], 0 offen lds\n"
    "v_mfma_f32_16x16x32_bf16 v[36:39], a[216:219], a[132:135], v[36:39]\n"
    "v_mfma_f32_16x16x32_bf16 v[36:39], a[220:223], a[164:167], v[36:39]\n"
    "s_add_u32 m0, s68, 84992\n"
    "s_add_u32 s96, s96, 16384\n"
    "s_addc_u32 s97, s97, 0\n"
    "v_mfma_f32_16x16x32_bf16 v[4:7], a[220:223], a[160:163], v[4:7]\n"
    "buffer_load_dwordx4 v243, s[96:99], 0 offen lds\n"
    "v_mfma_f32_16x16x32_bf16 v[4:7], a[216:219], a[128:131], v[4:7]\n"
    "v_add_u32 v252, 0x20000, v252\n"
    "v_add_u32 v253, 0x20000, v253\n"
    "v_mfma_f32_16x16x32_bf16 v[8:11], a[224:227], a[128:131], v[8:11]\n"
    "s_waitcnt vmcnt(17)\n"
    "ds_read_b64_tr_b16 a[192:193], v252 offset:0\n"
    "ds_read_b64_tr_b16 a[194:195], v252 offset:512\n"
    "v_mfma_f32_16x16x32_bf16 v[8:11], a[228:231], a[160:163], v[8:11]\n"
    "s_add_u32 m0, s79, 36864\n"
    "s_add_u32 s92, s92, 16384\n"
    "s_addc_u32 s93, s93, 0\n"
    "v_mfma_f32_16x16x32_bf16 v[40:43], a[228:231], a[164:167], v[40:43]\n"
    "buffer_load_dwordx4 v254, s[92:95], 0 offen lds\n"
    "ds_read_b64_tr_b16 a[196:197], v252 offset:4096\n"
    "v_mfma_f32_16x16x32_bf16 v[40:43], a[224:227], a[132:135], v[40:43]\n"
    "ds_read_b64_tr_b16 a[198:199], v252 offset:4608\n"
    "v_add_u32 v246, s90, v252\n"
    "v_mfma_f32_16x16x32_bf16 v[72:75], a[224:227], a[136:139], v[72:75]\n"
    "v_add_u32 v246, 0xfffe0000, v246\n"
    "ds_read_b64_tr_b16 v[248:249], v246 offset:0\n"
    "v_mfma_f32_16x16x32_bf16 v[72:75], a[228:231], a[168:171], v[72:75]\n"
    "ds_read_b64_tr_b16 v[250:251], v246 offset:512\n"
    "ds_read_b64_tr_b16 a[200:201], v252 offset:8\n"
    "v_mfma_f32_16x16x32_bf16 v[104:107], a[228:231], a[172:175], v[104:107]\n"
    "ds_read_b64_tr_b16 a[202:203], v252 offset:520\n"
    "s_addk_i32 m0, 1024\n"
    "v_mfma_f32_16x16x32_bf16 v[104:107], a[224:227], a[140:143], v[104:107]\n"
    "s_add_u32 s92, s92, 16384\n"
    "s_addc_u32 s93, s93, 0\n"
    "v_mfma_f32_16x16x32_bf16 v[136:139], a[224:227], a[144:147], v[136:139]\n"
    "buffer_load_dwordx4 v254, s[92:95], 0 offen lds\n"
    "ds_read_b64_tr_b16 a[204:205], v252 offset:4104\n"
    "ds_read_b64_tr_b16 a[206:207], v252 offset:4616\n"
    "v_mfma_f32_16x16x32_bf16 v[136:139], a[228:231], a[176:179], v[136:139]\n"
    "ds_read_b64_tr_b16 a[208:209], v253 offset:0\n"
    "ds_read_b64_tr_b16 a[210:211], v253 offset:512\n"
    "v_mfma_f32_16x16x32_bf16 v[168:171], a[228:231], a[180:183], v[168:171]\n"
    "ds_read_b64_tr_b16 a[212:213], v253 offset:4096\n"
    "ds_read_b64_tr_b16 a[214:215], v253 offset:4608\n"
    "v_mfma_f32_16x16x32_bf16 v[168:171], a[224:227], a[148:151], v[168:171]\n"
    "ds_read_b64_tr_b16 a[216:217], v253 offset:8\n"
    "ds_read_b64_tr_b16 a[218:219], v253 offset:520\n"
    "v_mfma_f32_16x16x32_bf16 v[200:203], a[224:227], a[152:155], v[200:203]\n"
    "s_add_u32 s62, s89, 0\n"
    "s_waitcnt lgkmcnt(10)\n"
    "v_mfma_f32_16x16x32_bf16 v[200:203], a[228:231], a[184:187], v[200:203]\n"
    "buffer_store_dwordx4 v[248:251], v240, s[28:31], s62 offen offset:0 sc1\n"
    "s_nop 0\n"
    "ds_read_b64_tr_b16 a[220:221], v253 offset:4104\n"
    "ds_read_b64_tr_b16 a[222:223], v253 offset:4616\n"
    "v_mfma_f32_16x16x32_bf16 v[232:235], a[228:231], a[188:191], v[232:235]\n"
    "v_mfma_f32_16x16x32_bf16 v[232:235], a[224:227], a[156:159], v[232:235]\n"
    "v_mfma_f32_16x16x32_bf16 v[236:239], a[232:235], a[156:159], v[236:239]\n"
    "v_mfma_f32_16x16x32_bf16 v[236:239], a[236:239], a[188:191], v[236:239]\n"
    "v_mfma_f32_16x16x32_bf16 v[204:207], a[236:239], a[184:187], v[204:207]\n"
    "v_add_u32 v246, s90, v252\n"
    "v_add_u32 v246, 0xfffe0000, v246\n"
    "v_mfma_f32_16x16x32_bf16 v[204:207], a[232:235], a[152:155], v[204:207]\n"
    "ds_read_b64_tr_b16 v[248:249], v246 offset:8\n"
    "ds_read_b64_tr_b16 v[250:251], v246 offset:520\n"
    "v_mfma_f32_16x16x32_bf16 v[172:175], a[232:235], a[148:151], v[172:175]\n"
    "v_mfma_f32_16x16x32_bf16 v[172:175], a[236:239], a[180:183], v[172:175]\n"
    "v_mfma_f32_16x16x32_bf16 v[140:143], a[236:239], a[176:179], v[140:143]\n"
    "v_mfma_f32_16x16x32_bf16 v[140:143], a[232:235], a[144:147], v[140:143]\n"
    "v_mfma_f32_16x16x32_bf16 v[108:111], a[232:235], a[140:143], v[108:111]\n"
    "v_mfma_f32_16x16x32_bf16 v[108:111], a[236:239], a[172:175], v[108:111]\n"
    "v_mfma_f32_16x16x32_bf16 v[76:79], a[236:239], a[168:171], v[76:79]\n"
    "v_mfma_f32_16x16x32_bf16 v[76:79], a[232:235], a[136:139], v[76:79]\n"
    "v_mfma_f32_16x16x32_bf16 v[44:47], a[232:235], a[132:135], v[44:47]\n"
    "s_add_u32 s62, s89, 0\n"
    "s_waitcnt lgkmcnt(0)\n"
    "v_mfma_f32_16x16x32_bf16 v[44:47], a[236:239], a[164:167], v[44:47]\n"
    "buffer_store_dwordx4 v[248:251], v240, s[28:31], s62 offen offset:2048 sc1\n"
    "s_nop 0\n"
    "v_mfma_f32_16x16x32_bf16 v[12:15], a[236:239], a[160:163], v[12:15]\n"
    "v_mfma_f32_16x16x32_bf16 v[12:15], a[232:235], a[128:131], v[12:15]\n"
    "v_mfma_f32_16x16x32_bf16 v[16:19], a[192:195], a[128:131], v[16:19]\n"
    "v_mfma_f32_16x16x32_bf16 v[16:19], a[196:199], a[160:163], v[16:19]\n"
    "v_mfma_f32_16x16x32_bf16 v[48:51], a[196:199], a[164:167], v[48:51]\n"
    "v_add_u32 v246, s90, v253\n"
    "v_add_u32 v246, 0xfffe0000, v246\n"
    "v_mfma_f32_16x16x32_bf16 v[48:51], a[192:195], a[132:135], v[48:51]\n"
    "ds_read_b64_tr_b16 v[248:249], v246 offset:0\n"
    "ds_read_b64_tr_b16 v[250:251], v246 offset:512\n"
    "v_mfma_f32_16x16x32_bf16 v[80:83], a[192:195], a[136:139], v[80:83]\n"
    "v_mfma_f32_16x16x32_bf16 v[80:83], a[196:199], a[168:171], v[80:83]\n"
    "v_mfma_f32_16x16x32_bf16 v[112:115], a[196:199], a[172:175], v[112:115]\n"
    "v_mfma_f32_16x16x32_bf16 v[112:115], a[192:195], a[140:143], v[112:115]\n"
    "v_mfma_f32_16x16x32_bf16 v[144:147], a[192:195], a[144:147], v[144:147]\n"
    "v_mfma_f32_16x16x32_bf16 v[144:147], a[196:199], a[176:179], v[144:147]\n"
    "v_mfma_f32_16x16x32_bf16 v[176:179], a[196:199], a[180:183], v[176:179]\n"
    "v_mfma_f32_16x16x32_bf16 v[176:179], a[192:195], a[148:151], v[176:179]\n"
    "v_mfma_f32_16x16x32_bf16 v[208:211], a[192:195], a[152:155], v[208:211]\n"
    "s_add_u32 s62, s89, 4096\n"
    "s_waitcnt lgkmcnt(0)\n"
    "v_mfma_f32_16x16x32_bf16 v[208:211], a[196:199], a[184:187], v[208:211]\n"
    "buffer_store_dwordx4 v[248:251], v240, s[28:31], s62 offen offset:0 sc1\n"
    "s_nop 0\n"
    "v_mfma_f32_16x16x32_bf16 a[240:243], a[196:199], a[188:191], a[240:243]\n"
    "v_mfma_f32_16x16x32_bf16 a[240:243], a[192:195], a[156:159], a[240:243]\n"
    "v_mfma_f32_16x16x32_bf16 a[244:247], a[200:203], a[156:159], a[244:247]\n"
    "v_mfma_f32_16x16x32_bf16 a[244:247], a[204:207], a[188:191], a[244:247]\n"
    "v_mfma_f32_16x16x32_bf16 v[212:215], a[204:207], a[184:187], v[212:215]\n"
    "v_add_u32 v246, s90, v253\n"
    "v_add_u32 v246, 0xfffe0000, v246\n"
    "v_mfma_f32_16x16x32_bf16 v[212:215], a[200:203], a[152:155], v[212:215]\n"
    "ds_read_b64_tr_b16 v[248:249], v246 offset:8\n"
    "ds_read_b64_tr_b16 v[250:251], v246 offset:520\n"
    "v_mfma_f32_16x16x32_bf16 v[180:183], a[200:203], a[148:151], v[180:183]\n"
    "v_mfma_f32_16x16x32_bf16 v[180:183], a[204:207], a[180:183], v[180:183]\n"
    "v_mfma_f32_16x16x32_bf16 v[148:151], a[204:207], a[176:179], v[148:151]\n"
    "v_mfma_f32_16x16x32_bf16 v[148:151], a[200:203], a[144:147], v[148:151]\n"
    "v_mfma_f32_16x16x32_bf16 v[116:119], a[200:203], a[140:143], v[116:119]\n"
    "v_mfma_f32_16x16x32_bf16 v[116:119], a[204:207], a[172:175], v[116:119]\n"
    "v_mfma_f32_16x16x32_bf16 v[84:87], a[204:207], a[168:171], v[84:87]\n"
    "v_mfma_f32_16x16x32_bf16 v[84:87], a[200:203], a[136:139], v[84:87]\n"
    "v_mfma_f32_16x16x32_bf16 v[52:55], a[200:203], a[132:135], v[52:55]\n"
    "s_add_u32 s62, s89, 4096\n"
    "s_waitcnt lgkmcnt(0)\n"
    "v_mfma_f32_16x16x32_bf16 v[52:55], a[204:207], a[164:167], v[52:55]\n"
    "buffer_store_dwordx4 v[248:251], v240, s[28:31], s62 offen offset:2048 sc1\n"
    "s_nop 0\n"
    "v_mfma_f32_16x16x32_bf16 v[20:23], a[204:207], a[160:163], v[20:23]\n"
    "v_mfma_f32_16x16x32_bf16 v[20:23], a[200:203], a[128:131], v[20:23]\n"
    "v_add_u32 v252, 0xfffe0000, v252\n"
    "v_add_u32 v253, 0xfffe0000, v253\n"
    "v_mfma_f32_16x16x32_bf16 v[24:27], a[208:211], a[128:131], v[24:27]\n"
    "s_waitcnt vmcnt(14)\n"
    "ds_read_b64_tr_b16 a[224:225], v252 offset:0\n"
    "ds_read_b64_tr_b16 a[226:227], v252 offset:512\n"
    "v_mfma_f32_16x16x32_bf16 v[24:27], a[212:215], a[160:163], v[24:27]\n"
    "ds_read_b64_tr_b16 a[228:229], v252 offset:4096\n"
    "ds_read_b64_tr_b16 a[230:231], v252 offset:4608\n"
    "v_mfma_f32_16x16x32_bf16 v[56:59], a[212:215], a[164:167], v[56:59]\n"
    "v_mfma_f32_16x16x32_bf16 v[56:59], a[208:211], a[132:135], v[56:59]\n"
    "ds_read_b64_tr_b16 a[232:233], v252 offset:8\n"
    "ds_read_b64_tr_b16 a[234:235], v252 offset:520\n"
    "v_mfma_f32_16x16x32_bf16 v[88:91], a[208:211], a[136:139], v[88:91]\n"
    "v_mfma_f32_16x16x32_bf16 v[88:91], a[212:215], a[168:171], v[88:91]\n"
    "ds_read_b64_tr_b16 a[236:237], v252 offset:4104\n"
    "ds_read_b64_tr_b16 a[238:239], v252 offset:4616\n"
    "v_mfma_f32_16x16x32_bf16 v[120:123], a[212:215], a[172:175], v[120:123]\n"
    "v_mfma_f32_16x16x32_bf16 v[120:123], a[208:211], a[140:143], v[120:123]\n"
    "ds_read_b64_tr_b16 a[192:193], v253 offset:0\n"
    "ds_read_b64_tr_b16 a[194:195], v253 offset:512\n"
    "v_mfma_f32_16x16x32_bf16 v[152:155], a[208:211], a[144:147], v[152:155]\n"
    "v_mfma_f32_16x16x32_bf16 v[152:155], a[212:215], a[176:179], v[152:155]\n"
    "ds_read_b64_tr_b16 a[196:197], v253 offset:4096\n"
    "ds_read_b64_tr_b16 a[198:199], v253 offset:4608\n"
    "v_mfma_f32_16x16x32_bf16 v[184:187], a[212:215], a[180:183], v[184:187]\n"
    "v_mfma_f32_16x16x32_bf16 v[184:187], a[208:211], a[148:151], v[184:187]\n"
    "ds_read_b64_tr_b16 a[200:201], v253 offset:8\n"
    "ds_read_b64_tr_b16 a[202:203], v253 offset:520\n"
    "v_mfma_f32_16x16x32_bf16 v[216:219], a[208:211], a[152:155], v[216:219]\n"
    "v_mfma_f32_16x16x32_bf16 v[216:219], a[212:215], a[184:187], v[216:219]\n"
    "s_barrier\n"
    "ds_read_b64_tr_b16 a[204:205], v253 offset:4104\n"
    "ds_read_b64_tr_b16 a[206:207], v253 offset:4616\n"
    "v_mfma_f32_16x16x32_bf16 a[248:251], a[212:215], a[188:191], a[248:251]\n"
    "v_mfma_f32_16x16x32_bf16 a[248:251], a[208:211], a[156:159], a[248:251]\n"
    "v_mfma_f32_16x16x32_bf16 a[252:255], a[216:219], a[156:159], a[252:255]\n"
    "v_mfma_f32_16x16x32_bf16 a[252:255], a[220:223], a[188:191], a[252:255]\n"
    "v_mfma_f32_16x16x32_bf16 v[220:223], a[220:223], a[184:187], v[220:223]\n"
    "ds_read_b128 a[156:159], v241 offset:14336\n"
    "v_mfma_f32_16x16x32_bf16 v[220:223], a[216:219], a[152:155], v[220:223]\n"
    "ds_read_b128 a[188:191], v242 offset:14336\n"
    "v_mfma_f32_16x16x32_bf16 v[188:191], a[216:219], a[148:151], v[188:191]\n"
    "ds_read_b128 a[184:187], v242 offset:12288\n"
    "v_mfma_f32_16x16x32_bf16 v[188:191], a[220:223], a[180:183], v[188:191]\n"
    "ds_read_b128 a[152:155], v241 offset:12288\n"
    "v_mfma_f32_16x16x32_bf16 v[156:159], a[220:223], a[176:179], v[156:159]\n"
    "ds_read_b128 a[148:151], v241 offset:10240\n"
    "v_mfma_f32_16x16x32_bf16 v[156:159], a[216:219], a[144:147], v[156:159]\n"
    "ds_read_b128 a[180:183], v242 offset:10240\n"
    "v_mfma_f32_16x16x32_bf16 v[124:127], a[216:219], a[140:143], v[124:127]\n"
    "ds_read_b128 a[176:179], v242 offset:8192\n"
    "v_mfma_f32_16x16x32_bf16 v[124:127], a[220:223], a[172:175], v[124:127]\n"
    "ds_read_b128 a[144:147], v241 offset:8192\n"
    "v_mfma_f32_16x16x32_bf16 v[92:95], a[220:223], a[168:171], v[92:95]\n"
    "ds_read_b128 a[140:143], v241 offset:6144\n"
    "v_mfma_f32_16x16x32_bf16 v[92:95], a[216:219], a[136:139], v[92:95]\n"
    "s_addk_i32 m0, 1024\n"
    "s_add_u32 s92, s92, 16384\n"
    "s_addc_u32 s93, s93, 0\n"
    "v_mfma_f32_16x16x32_bf16 v[60:63], a[216:219], a[132:135], v[60:63]\n"
    "buffer_load_dwordx4 v254, s[92:95], 0 offen lds\n"
    "ds_read_b128 a[172:175], v242 offset:6144\n"
    "v_mfma_f32_16x16x32_bf16 v[60:63], a[220:223], a[164:167], v[60:63]\n"
    "ds_read_b128 a[168:171], v242 offset:4096\n"
    "v_mfma_f32_16x16x32_bf16 v[28:31], a[220:223], a[160:163], v[28:31]\n"
    "ds_read_b128 a[136:139], v241 offset:4096\n"
    "v_mfma_f32_16x16x32_bf16 v[28:31], a[216:219], a[128:131], v[28:31]\n"
    "s_waitcnt lgkmcnt(10)\n"
    "ds_read_b128 a[132:135], v241 offset:2048\n"
    "s_addk_i32 m0, 1024\n"
    "v_mfma_f32_16x16x32_bf16 v[224:227], a[224:227], a[156:159], v[224:227]\n"
    "s_add_u32 s92, s92, 16384\n"
    "s_addc_u32 s93, s93, 0\n"
    "v_mfma_f32_16x16x32_bf16 v[224:227], a[228:231], a[188:191], v[224:227]\n"
    "s_waitcnt lgkmcnt(9)\n"
    "buffer_load_dwordx4 v254, s[92:95], 0 offen lds\n"
    "v_mfma_f32_16x16x32_bf16 v[192:195], a[228:231], a[184:187], v[192:195]\n"
    "s_add_u32 s57, s57, 131072\n"
    "s_cmp_ge_u32 s57, 1572864\n"
    "s_cselect_b32 s57, s70, s57\n"
    "ds_read_b128 a[164:167], v242 offset:2048\n"
    "ds_read_b128 a[160:163], v242 offset:0\n"
    "ds_read_b128 a[128:131], v241 offset:0\n"
    "v_mfma_f32_16x16x32_bf16 v[192:195], a[224:227], a[152:155], v[192:195]\n"
    "s_waitcnt lgkmcnt(10)\n"
    "s_add_u32 m0, s79, 131072\n"
    "s_lshl_b32 s54, s64, 17\n"
    "v_mfma_f32_16x16x32_bf16 v[160:163], a[224:227], a[148:151], v[160:163]\n"
    "s_xor_b32 s54, s54, s57\n"
    "s_add_u32 s92, s72, s54\n"
    "s_addc_u32 s93, s73, 0\n"
    "v_mfma_f32_16x16x32_bf16 v[160:163], a[228:231], a[180:183], v[160:163]\n"
    "s_waitcnt lgkmcnt(8)\n"
    "buffer_load_dwordx4 v254, s[92:95], 0 offen lds\n"
    "v_mfma_f32_16x16x32_bf16 v[128:131], a[228:231], a[176:179], v[128:131]\n"
    "v_mfma_f32_16x16x32_bf16 v[128:131], a[224:227], a[144:147], v[128:131]\n"
    "s_waitcnt lgkmcnt(6)\n"
    "s_addk_i32 m0, 1024\n"
    "s_add_u32 s92, s92, 16384\n"
    "v_mfma_f32_16x16x32_bf16 v[96:99], a[224:227], a[140:143], v[96:99]\n"
    "s_addc_u32 s93, s93, 0\n"
    "buffer_load_dwordx4 v254, s[92:95], 0 offen lds\n"
    "v_mfma_f32_16x16x32_bf16 v[96:99], a[228:231], a[172:175], v[96:99]\n"
    "s_waitcnt lgkmcnt(4)\n"
    "v_mfma_f32_16x16x32_bf16 v[64:67], a[228:231], a[168:171], v[64:67]\n"
    "v_mfma_f32_16x16x32_bf16 v[64:67], a[224:227], a[136:139], v[64:67]\n"
    "s_waitcnt lgkmcnt(2)\n"
    "s_addk_i32 m0, 1024\n"
    "s_add_u32 s92, s92, 16384\n"
    "v_mfma_f32_16x16x32_bf16 v[32:35], a[224:227], a[132:135], v[32:35]\n"
    "s_addc_u32 s93, s93, 0\n"
    "buffer_load_dwordx4 v254, s[92:95], 0 offen lds\n"
    "v_mfma_f32_16x16x32_bf16 v[32:35], a[228:231], a[164:167], v[32:35]\n"
    "s_waitcnt lgkmcnt(0)\n"
    "v_mfma_f32_16x16x32_bf16 v[0:3], a[228:231], a[160:163], v[0:3]\n"
    "v_mfma_f32_16x16x32_bf16 v[0:3], a[224:227], a[128:131], v[0:3]\n"
    "s_addk_i32 m0, 1024\n"
    "s_add_u32 s92, s92, 16384\n"
    "s_addc_u32 s93, s93, 0\n"
    "v_mfma_f32_16x16x32_bf16 v[4:7], a[232:235], a[128:131], v[4:7]\n"
    "buffer_load_dwordx4 v254, s[92:95], 0 offen lds\n"
    "s_add_u32 s82, s77, 2052\n"
    "v_mfma_f32_16x16x32_bf16 v[4:7], a[236:239], a[160:163], v[4:7]\n"
    "s_and_b32 s82, s82, s86\n"
    "v_mfma_f32_16x16x32_bf16 v[36:39], a[236:239], a[164:167], v[36:39]\n"
    "buffer_load_dword v244, v240, s[80:83], s77 offen offset:2048 sc1\n"
    "v_mfma_f32_16x16x32_bf16 v[36:39], a[232:235], a[132:135], v[36:39]\n"
    "s_addk_i32 m0, 1024\n"
    "s_add_u32 s92, s92, 16384\n"
    "s_addc_u32 s93, s93, 0\n"
    "v_mfma_f32_16x16x32_bf16 v[68:71], a[232:235], a[136:139], v[68:71]\n"
    "buffer_load_dwordx4 v254, s[92:95], 0 offen lds\n"
    "v_mfma_f32_16x16x32_bf16 v[68:71], a[236:239], a[168:171], v[68:71]\n"
    "v_mfma_f32_16x16x32_bf16 v[100:103], a[236:239], a[172:175], v[100:103]\n"
    "v_mfma_f32_16x16x32_bf16 v[100:103], a[232:235], a[140:143], v[100:103]\n"
    "s_addk_i32 m0, 1024\n"
    "s_add_u32 s92, s92, 16384\n"
    "s_addc_u32 s93, s93, 0\n"
    "v_mfma_f32_16x16x32_bf16 v[132:135], a[232:235], a[144:147], v[132:135]\n"
    "buffer_load_dwordx4 v254, s[92:95], 0 offen lds\n"
    "s_bitcmp1_b32 s85, 3\n"
    "s_cbranch_scc1 CP3_%=\n"
    "CR3_%=:\n"
    "v_mfma_f32_16x16x32_bf16 v[132:135], a[236:239], a[176:179], v[132:135]\n"
    "v_mfma_f32_16x16x32_bf16 v[164:167], a[236:239], a[180:183], v[164:167]\n"
    "v_mfma_f32_16x16x32_bf16 v[164:167], a[232:235], a[148:151], v[164:167]\n"
    "s_addk_i32 m0, 1024\n"
    "s_add_u32 s92, s92, 16384\n"
    "s_addc_u32 s93, s93, 0\n"
    "v_mfma_f32_16x16x32_bf16 v[196:199], a[232:235], a[152:155], v[196:199]\n"
    "buffer_load_dwordx4 v254, s[92:95], 0 offen lds\n"
    "v_mfma_f32_16x16x32_bf16 v[196:199], a[236:239], a[184:187], v[196:199]\n"
    "v_mfma_f32_16x16x32_bf16 v[228:231], a[236:239], a[188:191], v[228:231]\n"
    "v_mfma_f32_16x16x32_bf16 v[228:231], a[232:235], a[156:159], v[228:231]\n"
    "s_waitcnt vmcnt(8)\n"
    "ds_read_b64_tr_b16 a[208:209], v252 offset:32768\n"
    "ds_read_b64_tr_b16 a[210:211], v252 offset:33280\n"
    "v_mfma_f32_16x16x32_bf16 v[232:235], a[192:195], a[156:159], v[232:235]\n"
    "s_addk_i32 m0, 1024\n"
    "s_add_u32 s92, s92, 16384\n"
    "s_addc_u32 s93, s93, 0\n"
    "v_mfma_f32_16x16x32_bf16 v[232:235], a[196:199], a[188:191], v[232:235]\n"
    "buffer_load_dwordx4 v254, s[92:95], 0 offen lds\n"
    "ds_read_b64_tr_b16 a[212:213], v252 offset:36864\n"
    "v_mfma_f32_16x16x32_bf16 v[200:203], a[196:199], a[184:187], v[200:203]\n"
    "ds_read_b64_tr_b16 a[214:215], v252 offset:37376\n"
    "v_mfma_f32_16x16x32_bf16 v[200:203], a[192:195], a[152:155], v[200:203]\n"
    "ds_read_b64_tr_b16 a[216:217], v252 offset:32776\n"
    "ds_read_b64_tr_b16 a[218:219], v252 offset:33288\n"
    "v_mfma_f32_16x16x32_bf16 v[168:171], a[192:195], a[148:151], v[168:171]\n"
    "s_xor_b32 s59, s66, s67\n"
    "s_addk_i32 s67, 128\n"
    "s_cmpk_eq_u32 s67, 1536\n"
    "v_mfma_f32_16x16x32_bf16 v[168:171], a[196:199], a[180:183], v[168:171]\n"
    "s_cselect_b32 s67, 0, s67\n"
    "s_add_u32 m0, s68, 98304\n"
    "s_add_u32 s96, s24, s59\n"
    "v_mfma_f32_16x16x32_bf16 v[136:139], a[196:199], a[176:179], v[136:139]\n"
    "s_addc_u32 s97, s25, 0\n"
    "buffer_load_dwordx4 v255, s[96:99], 0 offen lds\n"
    "v_mfma_f32_16x16x32_bf16 v[136:139], a[192:195], a[144:147], v[136:139]\n"
    "ds_read_b64_tr_b16 a[220:221], v252 offset:36872\n"
    "ds_read_b64_tr_b16 a[222:223], v252 offset:37384\n"
    "v_mfma_f32_16x16x32_bf16 v[104:107], a[192:195], a[140:143], v[104:107]\n"
    "ds_read_b64_tr_b16 a[224:225], v253 offset:32768\n"
    "ds_read_b64_tr_b16 a[226:227], v253 offset:33280\n"
    "v_mfma_f32_16x16x32_bf16 v[104:107], a[196:199], a[172:175], v[104:107]\n"
    "s_add_u32 m0, s68, 99328\n"
    "s_add_u32 s96, s96, 16384\n"
    "s_addc_u32 s97, s97, 0\n"
    "v_mfma_f32_16x16x32_bf16 v[72:75], a[196:199], a[168:171], v[72:75]\n"
    "buffer_load_dwordx4 v243, s[96:99], 0 offen lds\n"
    "ds_read_b64_tr_b16 a[228:229], v253 offset:36864\n"
    "v_mfma_f32_16x16x32_bf16 v[72:75], a[192:195], a[136:139], v[72:75]\n"
    "ds_read_b64_tr_b16 a[230:231], v253 offset:37376\n"
    "ds_read_b64_tr_b16 a[232:233], v253 offset:32776\n"
    "v_mfma_f32_16x16x32_bf16 v[40:43], a[192:195], a[132:135], v[40:43]\n"
    "ds_read_b64_tr_b16 a[234:235], v253 offset:33288\n"
    "s_add_u32 m0, s68, 100352\n"
    "v_mfma_f32_16x16x32_bf16 v[40:43], a[196:199], a[164:167], v[40:43]\n"
    "s_add_u32 s96, s96, 16384\n"
    "s_addc_u32 s97, s97, 0\n"
    "v_mfma_f32_16x16x32_bf16 v[8:11], a[196:199], a[160:163], v[8:11]\n"
    "buffer_load_dwordx4 v255, s[96:99], 0 offen lds\n"
    "ds_read_b64_tr_b16 a[236:237], v253 offset:36872\n"
    "v_mfma_f32_16x16x32_bf16 v[8:11], a[192:195], a[128:131], v[8:11]\n"
    "ds_read_b64_tr_b16 a[238:239], v253 offset:37384\n"
    "s_add_u32 m0, s68, 101376\n"
    "v_mfma_f32_16x16x32_bf16 v[12:15], a[200:203], a[128:131], v[12:15]\n"
    "s_add_u32 s96, s96, 16384\n"
    "s_addc_u32 s97, s97, 0\n"
    "v_mfma_f32_16x16x32_bf16 v[12:15], a[204:207], a[160:163], v[12:15]\n"
    "buffer_load_dwordx4 v243, s[96:99], 0 offen lds\n"
    "v_mfma_f32_16x16x32_bf16 v[44:47], a[204:207], a[164:167], v[44:47]\n"
    "v_mfma_f32_16x16x32_bf16 v[44:47], a[200:203], a[132:135], v[44:47]\n"
    "s_add_u32 m0, s79, 0\n"
    "s_lshl_b32 s54, s64, 17\n"
    "s_xor_b32 s54, s54, s57\n"
    "v_mfma_f32_16x16x32_bf16 v[76:79], a[200:203], a[136:139], v[76:79]\n"
    "s_addk_i32 s54, 512\n"
    "s_add_u32 s92, s72, s54\n"
    "s_addc_u32 s93, s73, 0\n"
    "v_mfma_f32_16x16x32_bf16 v[76:79], a[204:207], a[168:171], v[76:79]\n"
    "buffer_load_dwordx4 v254, s[92:95], 0 offen lds\n"
    "v_mfma_f32_16x16x32_bf16 v[108:111], a[204:207], a[172:175], v[108:111]\n"
    "v_mfma_f32_16x16x32_bf16 v[108:111], a[200:203], a[140:143], v[108:111]\n"
    "s_addk_i32 m0, 1024\n"
    "s_add_u32 s92, s92, 16384\n"
    "s_addc_u32 s93, s93, 0\n"
    "v_mfma_f32_16x16x32_bf16 v[140:143], a[200:203], a[144:147], v[140:143]\n"
    "buffer_load_dwordx4 v254, s[92:95], 0 offen lds\n"
    "v_mfma_f32_16x16x32_bf16 v[140:143], a[204:207], a[176:179], v[140:143]\n"
    "v_mfma_f32_16x16x32_bf16 v[172:175], a[204:207], a[180:183], v[172:175]\n"
    "v_mfma_f32_16x16x32_bf16 v[172:175], a[200:203], a[148:151], v[172:175]\n"
    "s_addk_i32 m0, 1024\n"
    "s_add_u32 s92, s92, 16384\n"
    "s_addc_u32 s93, s93, 0\n"
    "v_mfma_f32_16x16x32_bf16 v[204:207], a[200:203], a[152:155], v[204:207]\n"
    "buffer_load_dwordx4 v254, s[92:95], 0 offen lds\n"
    "v_mfma_f32_16x16x32_bf16 v[204:207], a[204:207], a[184:187], v[204:207]\n"
    "v_mfma_f32_16x16x32_bf16 v[236:239], a[204:207], a[188:191], v[236:239]\n"
    "v_mfma_f32_16x16x32_bf16 v[236:239], a[200:203], a[156:159], v[236:239]\n"
    "s_waitcnt lgkmcnt(12)\n"
    "s_addk_i32 m0, 1024\n"
    "s_add_u32 s92, s92, 16384\n"
    "v_mfma_f32_16x16x32_bf16 a[240:243], a[208:211], a[156:159], a[240:243]\n"
    "s_addc_u32 s93, s93, 0\n"
    "buffer_load_dwordx4 v254, s[92:95], 0 offen lds\n"
    "v_mfma_f32_16x16x32_bf16 a[240:243], a[212:215], a[188:191], a[240:243]\n"
    "v_mfma_f32_16x16x32_bf16 v[208:211], a[212:215], a[184:187], v[208:211]\n"
    "v_mfma_f32_16x16x32_bf16 v[208:211], a[208:211], a[152:155], v[208:211]\n"
    "s_addk_i32 m0, 1024\n"
    "s_add_u32 s92, s92, 16384\n"
    "s_addc_u32 s93, s93, 0\n"
    "v_mfma_f32_16x16x32_bf16 v[176:179], a[208:211], a[148:151], v[176:179]\n"
    "buffer_load_dwordx4 v254, s[92:95], 0 offen lds\n"
    "v_mfma_f32_16x16x32_bf16 v[176:179], a[212:215], a[180:183], v[176:179]\n"
    "v_mfma_f32_16x16x32_bf16 v[144:147], a[212:215], a[176:179], v[144:147]\n"
    "v_mfma_f32_16x16x32_bf16 v[144:147], a[208:211], a[144:147], v[144:147]\n"
    "s_addk_i32 m0, 1024\n"
    "s_add_u32 s92, s92, 16384\n"
    "s_addc_u32 s93, s93, 0\n"
    "v_mfma_f32_16x16x32_bf16 v[112:115], a[208:211], a[140:143], v[112:115]\n"
    "buffer_load_dwordx4 v254, s[92:95], 0 offen lds\n"
    "v_mfma_f32_16x16x32_bf16 v[112:115], a[212:215], a[172:175], v[112:115]\n"
    "v_mfma_f32_16x16x32_bf16 v[80:83], a[212:215], a[168:171], v[80:83]\n"
    "v_mfma_f32_16x16x32_bf16 v[80:83], a[208:211], a[136:139], v[80:83]\n"
    "s_addk_i32 m0, 1024\n"
    "s_add_u32 s92, s92, 16384\n"
    "s_addc_u32 s93, s93, 0\n"
    "v_mfma_f32_16x16x32_bf16 v[48:51], a[208:211], a[132:135], v[48:51]\n"
    "buffer_load_dwordx4 v254, s[92:95], 0 offen lds\n"
    "v_mfma_f32_16x16x32_bf16 v[48:51], a[212:215], a[164:167], v[48:51]\n"
    "v_mfma_f32_16x16x32_bf16 v[16:19], a[212:215], a[160:163], v[16:19]\n"
    "v_mfma_f32_16x16x32_bf16 v[16:19], a[208:211], a[128:131], v[16:19]\n"
    "s_waitcnt lgkmcnt(8)\n"
    "s_addk_i32 m0, 1024\n"
    "s_add_u32 s92, s92, 16384\n"
    "v_mfma_f32_16x16x32_bf16 v[20:23], a[216:219], a[128:131], v[20:23]\n"
    "s_addc_u32 s93, s93, 0\n"
    "buffer_load_dwordx4 v254, s[92:95], 0 offen lds\n"
    "v_mfma_f32_16x16x32_bf16 v[20:23], a[220:223], a[160:163], v[20:23]\n"
    "s_add_u32 s57, s57, 131072\n"
    "s_cmp_ge_u32 s57, 1572864\n"
    "s_cselect_b32 s57, s70, s57\n"
    "v_mfma_f32_16x16x32_bf16 v[52:55], a[220:223], a[164:167], v[52:55]\n"
    "v_mfma_f32_16x16x32_bf16 v[52:55], a[216:219], a[132:135], v[52:55]\n"
    "s_waitcnt lgkmcnt(0)\n"
    "s_add_u32 m0, s79, 32768\n"
    "s_lshl_b32 s54, s64, 17\n"
    "v_mfma_f32_16x16x32_bf16 v[84:87], a[216:219], a[136:139], v[84:87]\n"
    "s_xor_b32 s54, s54, s57\n"
    "s_add_u32 s92, s72, s54\n"
    "s_addc_u32 s93, s73, 0\n"
    "v_mfma_f32_16x16x32_bf16 v[84:87], a[220:223], a[168:171], v[84:87]\n"
    "buffer_load_dwordx4 v254, s[92:95], 0 offen lds\n"
    "v_mfma_f32_16x16x32_bf16 v[116:119], a[220:223], a[172:175], v[116:119]\n"
    "v_mfma_f32_16x16x32_bf16 v[116:119], a[216:219], a[140:143], v[116:119]\n"
    "s_addk_i32 m0, 1024\n"
    "s_add_u32 s92, s92, 16384\n"
    "s_addc_u32 s93, s93, 0\n"
    "v_mfma_f32_16x16x32_bf16 v[148:151], a[216:219], a[144:147], v[148:151]\n"
    "buffer_load_dwordx4 v254, s[92:95], 0 offen lds\n"
    "v_mfma_f32_16x16x32_bf16 v[148:151], a[220:223], a[176:179], v[148:151]\n"
    "v_mfma_f32_16x16x32_bf16 v[180:183], a[220:223], a[180:183], v[180:183]\n"
    "v_mfma_f32_16x16x32_bf16 v[180:183], a[216:219], a[148:151], v[180:183]\n"
    "s_addk_i32 m0, 1024\n"
    "s_add_u32 s92, s92, 16384\n"
    "s_addc_u32 s93, s93, 0\n"
    "v_mfma_f32_16x16x32_bf16 v[212:215], a[216:219], a[152:155], v[212:215]\n"
    "buffer_load_dwordx4 v254, s[92:95], 0 offen lds\n"
    "v_mfma_f32_16x16x32_bf16 v[212:215], a[220:223], a[184:187], v[212:215]\n"
    "v_mfma_f32_16x16x32_bf16 a[244:247], a[220:223], a[188:191], a[244:247]\n"
    "v_mfma_f32_16x16x32_bf16 a[244:247], a[216:219], a[156:159], a[244:247]\n"
    "v_add_u32 v252, 0x20000, v252\n"
    "v_add_u32 v253, 0x20000, v253\n"
    "v_mfma_f32_16x16x32_bf16 a[248:251], a[224:227], a[156:159], a[248:251]\n"
    "s_waitcnt vmcnt(15)\n"
    "ds_read_b64_tr_b16 a[192:193], v252 offset:0\n"
    "ds_read_b64_tr_b16 a[194:195], v252 offset:512\n"
    "v_mfma_f32_16x16x32_bf16 a[248:251], a[228:231], a[188:191], a[248:251]\n"
    "s_addk_i32 m0, 1024\n"
    "s_add_u32 s92, s92, 16384\n"
    "s_addc_u32 s93, s93, 0\n"
    "v_mfma_f32_16x16x32_bf16 v[216:219], a[228:231], a[184:187], v[216:219]\n"
    "buffer_load_dwordx4 v254, s[92:95], 0 offen lds\n"
    "ds_read_b64_tr_b16 a[196:197], v252 offset:4096\n"
    "v_mfma_f32_16x16x32_bf16 v[216:219], a[224:227], a[152:155], v[216:219]\n"
    "ds_read_b64_tr_b16 a[198:199], v252 offset:4608\n"
    "ds_read_b64_tr_b16 a[200:201], v252 offset:8\n"
    "v_mfma_f32_16x16x32_bf16 v[184:187], a[224:227], a[148:151], v[184:187]\n"
    "ds_read_b64_tr_b16 a[202:203], v252 offset:520\n"
    "s_addk_i32 m0, 1024\n"
    "v_mfma_f32_16x16x32_bf16 v[184:187], a[228:231], a[180:183], v[184:187]\n"
    "s_add_u32 s92, s92, 16384\n"
    "s_addc_u32 s93, s93, 0\n"
    "v_mfma_f32_16x16x32_bf16 v[152:155], a[228:231], a[176:179], v[152:155]\n"
    "buffer_load_dwordx4 v254, s[92:95], 0 offen lds\n"
    "ds_read_b64_tr_b16 a[204:205], v252 offset:4104\n"
    "v_mfma_f32_16x16x32_bf16 v[152:155], a[224:227], a[144:147], v[152:155]\n"
    "ds_read_b64_tr_b16 a[206:207], v252 offset:4616\n"
    "ds_read_b64_tr_b16 a[208:209], v253 offset:0\n"
    "v_mfma_f32_16x16x32_bf16 v[120:123], a[224:227], a[140:143], v[120:123]\n"
    "ds_read_b64_tr_b16 a[210:211], v253 offset:512\n"
    "s_addk_i32 m0, 1024\n"
    "v_mfma_f32_16x16x32_bf16 v[120:123], a[228:231], a[172:175], v[120:123]\n"
    "s_add_u32 s92, s92, 16384\n"
    "s_addc_u32 s93, s93, 0\n"
    "v_mfma_f32_16x16x32_bf16 v[88:91], a[228:231], a[168:171], v[88:91]\n"
    "buffer_load_dwordx4 v254, s[92:95], 0 offen lds\n"
    "ds_read_b64_tr_b16 a[212:213], v253 offset:4096\n"
    "v_mfma_f32_16x16x32_bf16 v[88:91], a[224:227], a[136:139], v[88:91]\n"
    "ds_read_b64_tr_b16 a[214:215], v253 offset:4608\n"
    "ds_read_b64_tr_b16 a[216:217], v253 offset:8\n"
    "v_mfma_f32_16x16x32_bf16 v[56:59], a[224:227], a[132:135], v[56:59]\n"
    "ds_read_b64_tr_b16 a[218:219], v253 offset:520\n"
    "s_addk_i32 m0, 1024\n"
    "v_mfma_f32_16x16x32_bf16 v[56:59], a[228:231], a[164:167], v[56:59]\n"
    "s_add_u32 s92, s92, 16384\n"
    "s_addc_u32 s93, s93, 0\n"
    "buffer_load_dwordx4 v254, s[92:95], 0 offen lds\n"
    "s_barrier\n"
    "v_mfma_f32_16x16x32_bf16 v[24:27], a[228:231], a[160:163], v[24:27]\n"
    "ds_read_b64_tr_b16 a[220:221], v253 offset:4104\n"
    "ds_read_b64_tr_b16 a[222:223], v253 offset:4616\n"
    "v_mfma_f32_16x16x32_bf16 v[24:27], a[224:227], a[128:131], v[24:27]\n"
    "s_addk_i32 m0, 1024\n"
    "s_add_u32 s92, s92, 16384\n"
    "s_addc_u32 s93, s93, 0\n"
    "v_mfma_f32_16x16x32_bf16 v[28:31], a[232:235], a[128:131], v[28:31]\n"
    "buffer_load_dwordx4 v254, s[92:95], 0 offen lds\n"
    "v_mfma_f32_16x16x32_bf16 v[28:31], a[236:239], a[160:163], v[28:31]\n"
    "v_mfma_f32_16x16x32_bf16 v[60:63], a[236:239], a[164:167], v[60:63]\n"
    "ds_read_b128 a[128:131], v241 offset:16384\n"
    "v_mfma_f32_16x16x32_bf16 v[60:63], a[232:235], a[132:135], v[60:63]\n"
    "s_waitcnt lgkmcnt(1)\n"
    "s_add_u32 m0, s79, 131072\n"
    "s_lshl_b32 s54, s64, 17\n"
    "v_mfma_f32_16x16x32_bf16 v[92:95], a[232:235], a[136:139], v[92:95]\n"
    "s_xor_b32 s54, s54, s57\n"
    "s_addk_i32 s54, 512\n"
    "s_add_u32 s92, s72, s54\n"
    "v_mfma_f32_16x16x32_bf16 v[92:95], a[236:239], a[168:171], v[92:95]\n"
    "s_addc_u32 s93, s73, 0\n"
    "buffer_load_dwordx4 v254, s[92:95], 0 offen lds\n"
    "v_mfma_f32_16x16x32_bf16 v[124:127], a[236:239], a[172:175], v[124:127]\n"
    "ds_read_b128 a[160:163], v242 offset:16384\n"
    "v_mfma_f32_16x16x32_bf16 v[124:127], a[232:235], a[140:143], v[124:127]\n"
    "ds_read_b128 a[164:167], v242 offset:18432\n"
    "v_mfma_f32_16x16x32_bf16 v[156:159], a[232:235], a[144:147], v[156:159]\n"
    "ds_read_b128 a[132:135], v241 offset:18432\n"
    "v_mfma_f32_16x16x32_bf16 v[156:159], a[236:239], a[176:179], v[156:159]\n"
    "ds_read_b128 a[136:139], v241 offset:20480\n"
    "s_addk_i32 m0, 1024\n"
    "v_mfma_f32_16x16x32_bf16 v[188:191], a[236:239], a[180:183], v[188:191]\n"
    "s_add_u32 s92, s92, 16384\n"
    "s_addc_u32 s93, s93, 0\n"
    "v_mfma_f32_16x16x32_bf16 v[188:191], a[232:235], a[148:151], v[188:191]\n"
    "buffer_load_dwordx4 v254, s[92:95], 0 offen lds\n"
    "ds_read_b128 a[168:171], v242 offset:20480\n"
    "ds_read_b128 a[172:175], v242 offset:22528\n"
    "v_mfma_f32_16x16x32_bf16 v[220:223], a[232:235], a[152:155], v[220:223]\n"
    "ds_read_b128 a[140:143], v241 offset:22528\n"
    "v_mfma_f32_16x16x32_bf16 v[220:223], a[236:239], a[184:187], v[220:223]\n"
    "ds_read_b128 a[144:147], v241 offset:24576\n"
    "s_addk_i32 m0, 1024\n"
    "v_mfma_f32_16x16x32_bf16 a[252:255], a[236:239], a[188:191], a[252:255]\n"
    "s_add_u32 s92, s92, 16384\n"
    "s_addc_u32 s93, s93, 0\n"
    "v_mfma_f32_16x16x32_bf16 a[252:255], a[232:235], a[156:159], a[252:255]\n"
    "s_waitcnt lgkmcnt(7)\n"
    "buffer_load_dwordx4 v254, s[92:95], 0 offen lds\n"
    "ds_read_b128 a[176:179], v242 offset:24576\n"
    "ds_read_b128 a[180:183], v242 offset:26624\n"
    "v_mfma_f32_16x16x32_bf16 v[0:3], a[192:195], a[128:131], v[0:3]\n"
    "ds_read_b128 a[148:151], v241 offset:26624\n"
    "v_mfma_f32_16x16x32_bf16 v[0:3], a[196:199], a[160:163], v[0:3]\n"
    "s_waitcnt lgkmcnt(8)\n"
    "ds_read_b128 a[152:155], v241 offset:28672\n"
    "s_addk_i32 m0, 1024\n"
    "v_mfma_f32_16x16x32_bf16 v[32:35], a[196:199], a[164:167], v[32:35]\n"
    "s_add_u32 s92, s92, 16384\n"
    "s_addc_u32 s93, s93, 0\n"
    "v_mfma_f32_16x16x32_bf16 v[32:35], a[192:195], a[132:135], v[32:35]\n"
    "s_waitcnt lgkmcnt(7)\n"
    "buffer_load_dwordx4 v254, s[92:95], 0 offen lds\n"
    "ds_read_b128 a[184:187], v242 offset:28672\n"
    "ds_read_b128 a[188:191], v242 offset:30720\n"
    "ds_read_b128 a[156:159], v241 offset:30720\n"
    "s_add_u32 s82, s76, 8\n"
    "s_and_b32 s82, s82, s86\n"
    "v_mfma_f32_16x16x32_bf16 v[64:67], a[192:195], a[136:139], v[64:67]\n"
    "buffer_store_dwordx2 v[244:245], v240, s[80:83], s76 offen sc1\n"
    "v_mfma_f32_16x16x32_bf16 v[64:67], a[196:199], a[168:171], v[64:67]\n"
    "s_waitcnt lgkmcnt(8)\n"
    "s_addk_i32 m0, 1024\n"
    "s_add_u32 s92, s92, 16384\n"
    "v_mfma_f32_16x16x32_bf16 v[96:99], a[196:199], a[172:175], v[96:99]\n"
    "s_addc_u32 s93, s93, 0\n"
    "buffer_load_dwordx4 v254, s[92:95], 0 offen lds\n"
    "v_mfma_f32_16x16x32_bf16 v[96:99], a[192:195], a[140:143], v[96:99]\n"
    "s_waitcnt lgkmcnt(6)\n"
    "s_addk_i32 m0, 1024\n"
    "s_add_u32 s92, s92, 16384\n"
    "v_mfma_f32_16x16x32_bf16 v[128:131], a[192:195], a[144:147], v[128:131]\n"
    "s_addc_u32 s93, s93, 0\n"
    "buffer_load_dwordx4 v254, s[92:95], 0 offen lds\n"
    "v_mfma_f32_16x16x32_bf16 v[128:131], a[196:199], a[176:179], v[128:131]\n"
    "s_waitcnt lgkmcnt(4)\n"
    "v_mfma_f32_16x16x32_bf16 v[160:163], a[196:199], a[180:183], v[160:163]\n"
    "v_mfma_f32_16x16x32_bf16 v[160:163], a[192:195], a[148:151], v[160:163]\n"
    "s_waitcnt lgkmcnt(2)\n"
    "s_addk_i32 m0, 1024\n"
    "s_add_u32 s92, s92, 16384\n"
    "v_mfma_f32_16x16x32_bf16 v[192:195], a[192:195], a[152:155], v[192:195]\n"
    "s_addc_u32 s93, s93, 0\n"
    "buffer_load_dwordx4 v254, s[92:95], 0 offen lds\n"
    "v_mfma_f32_16x16x32_bf16 v[192:195], a[196:199], a[184:187], v[192:195]\n"
    "s_waitcnt lgkmcnt(0)\n"
    "v_mfma_f32_16x16x32_bf16 v[224:227], a[196:199], a[188:191], v[224:227]\n"
    "v_mfma_f32_16x16x32_bf16 v[224:227], a[192:195], a[156:159], v[224:227]\n"
    "s_addk_i32 m0, 1024\n"
    "s_add_u32 s92, s92, 16384\n"
    "s_addc_u32 s93, s93, 0\n"
    "v_mfma_f32_16x16x32_bf16 v[228:231], a[200:203], a[156:159], v[228:231]\n"
    "buffer_load_dwordx4 v254, s[92:95], 0 offen lds\n"
    "s_add_u32 s57, s57, 131072\n"
    "v_mfma_f32_16x16x32_bf16 v[228:231], a[204:207], a[188:191], v[228:231]\n"
    "s_cmp_ge_u32 s57, 1572864\n"
    "s_cselect_b32 s57, s70, s57\n"
    "v_mfma_f32_16x16x32_bf16 v[196:199], a[204:207], a[184:187], v[196:199]\n"
    "v_mfma_f32_16x16x32_bf16 v[196:199], a[200:203], a[152:155], v[196:199]\n"
    "v_mfma_f32_16x16x32_bf16 v[164:167], a[200:203], a[148:151], v[164:167]\n"
    "v_mfma_f32_16x16x32_bf16 v[164:167], a[204:207], a[180:183], v[164:167]\n"
    "v_mfma_f32_16x16x32_bf16 v[132:135], a[204:207], a[176:179], v[132:135]\n"
    "s_xor_b32 s59, s66, s67\n"
    "s_addk_i32 s67, 128\n"
    "s_cmpk_eq_u32 s67, 1536\n"
    "v_mfma_f32_16x16x32_bf16 v[132:135], a[200:203], a[144:147], v[132:135]\n"
    "s_cselect_b32 s67, 0, s67\n"
    "s_add_u32 m0, s68, 65536\n"
    "s_add_u32 s96, s24, s59\n"
    "v_mfma_f32_16x16x32_bf16 v[100:103], a[200:203], a[140:143], v[100:103]\n"
    "s_addc_u32 s97, s25, 0\n"
    "buffer_load_dwordx4 v255, s[96:99], 0 offen lds\n"
    "s_bitcmp1_b32 s85, 4\n"
    "s_cbranch_scc1 CP4_%=\n"
    "CR4_%=:\n"
    "v_mfma_f32_16x16x32_bf16 v[100:103], a[204:207], a[172:175], v[100:103]\n"
    "s_add_u32 m0, s68, 66560\n"
    "s_add_u32 s96, s96, 16384\n"
    "s_addc_u32 s97, s97, 0\n"
    "v_mfma_f32_16x16x32_bf16 v[68:71], a[204:207], a[168:171], v[68:71]\n"
    "buffer_load_dwordx4 v243, s[96:99], 0 offen lds\n"
    "v_mfma_f32_16x16x32_bf16 v[68:71], a[200:203], a[136:139], v[68:71]\n"
    "v_mfma_f32_16x16x32_bf16 v[36:39], a[200:203], a[132:135], v[36:39]\n"
    "s_add_u32 m0, s68, 67584\n"
    "s_add_u32 s96, s96, 16384\n"
    "s_addc_u32 s97, s97, 0\n"
    "v_mfma_f32_16x16x32_bf16 v[36:39], a[204:207], a[164:167], v[36:39]\n"
    "buffer_load_dwordx4 v255, s[96:99], 0 offen lds\n"
    "v_mfma_f32_16x16x32_bf16 v[4:7], a[204:207], a[160:163], v[4:7]\n"
    "v_mfma_f32_16x16x32_bf16 v[4:7], a[200:203], a[128:131], v[4:7]\n"
    "v_add_u32 v252, 0xfffe0000, v252\n"
    "v_add_u32 v253, 0xfffe0000, v253\n"
    "v_mfma_f32_16x16x32_bf16 v[8:11], a[208:211], a[128:131], v[8:11]\n"
    "s_waitcnt vmcnt(20)\n"
    "ds_read_b64_tr_b16 a[224:225], v252 offset:0\n"
    "ds_read_b64_tr_b16 a[226:227], v252 offset:512\n"
    "v_mfma_f32_16x16x32_bf16 v[8:11], a[212:215], a[160:163], v[8:11]\n"
    "s_add_u32 m0, s68, 68608\n"
    "s_add_u32 s96, s96, 16384\n"
    "s_addc_u32 s97, s97, 0\n"
    "v_mfma_f32_16x16x32_bf16 v[40:43], a[212:215], a[164:167], v[40:43]\n"
    "buffer_load_dwordx4 v243, s[96:99], 0 offen lds\n"
    "ds_read_b64_tr_b16 a[228:229], v252 offset:4096\n"
    "v_mfma_f32_16x16x32_bf16 v[40:43], a[208:211], a[132:135], v[40:43]\n"
    "ds_read_b64_tr_b16 a[230:231], v252 offset:4608\n"
    "s_add_u32 m0, s68, 114688\n"
    "v_mfma_f32_16x16x32_bf16 v[72:75], a[208:211], a[136:139], v[72:75]\n"
    "s_add_u32 s60, s78, 4194304\n"
    "v_mfma_f32_16x16x32_bf16 v[72:75], a[212:215], a[168:171], v[72:75]\n"
    "buffer_load_dwordx4 v254, s[72:75], s60 offen nt lds\n"
    "v_mfma_f32_16x16x32_bf16 v[104:107], a[212:215], a[172:175], v[104:107]\n"
    "ds_read_b64_tr_b16 a[232:233], v252 offset:8\n"
    "ds_read_b64_tr_b16 a[234:235], v252 offset:520\n"
    "v_mfma_f32_16x16x32_bf16 v[104:107], a[208:211], a[140:143], v[104:107]\n"
    "ds_read_b64_tr_b16 a[236:237], v252 offset:4104\n"
    "ds_read_b64_tr_b16 a[238:239], v252 offset:4616\n"
    "v_mfma_f32_16x16x32_bf16 v[136:139], a[208:211], a[144:147], v[136:139]\n"
    "s_add_u32 m0, s68, 115712\n"
    "s_add_u32 s60, s78, 4210688\n"
    "v_mfma_f32_16x16x32_bf16 v[136:139], a[212:215], a[176:179], v[136:139]\n"
    "buffer_load_dwordx4 v254, s[72:75], s60 offen nt lds\n"
    "v_mfma_f32_16x16x32_bf16 v[168:171], a[212:215], a[180:183], v[168:171]\n"
    "ds_read_b64_tr_b16 a[192:193], v253 offset:0\n"
    "ds_read_b64_tr_b16 a[194:195], v253 offset:512\n"
    "v_mfma_f32_16x16x32_bf16 v[168:171], a[208:211], a[148:151], v[168:171]\n"
    "s_add_u32 m0, s68, 116736\n"
    "s_add_u32 s60, s78, 4227072\n"
    "v_mfma_f32_16x16x32_bf16 v[200:203], a[208:211], a[152:155], v[200:203]\n"
    "buffer_load_dwordx4 v254, s[72:75], s60 offen nt lds\n"
    "ds_read_b64_tr_b16 a[196:197], v253 offset:4096\n"
    "ds_read_b64_tr_b16 a[198:199], v253 offset:4608\n"
    "v_mfma_f32_16x16x32_bf16 v[200:203], a[212:215], a[184:187], v[200:203]\n"
    "ds_read_b64_tr_b16 a[200:201], v253 offset:8\n"
    "ds_read_b64_tr_b16 a[202:203], v253 offset:520\n"
    "v_mfma_f32_16x16x32_bf16 v[232:235], a[212:215], a[188:191], v[232:235]\n"
    "s_add_u32 m0, s68, 117760\n"
    "s_add_u32 s60, s78, 4243456\n"
    "v_mfma_f32_16x16x32_bf16 v[232:235], a[208:211], a[156:159], v[232:235]\n"
    "buffer_load_dwordx4 v254, s[72:75], s60 offen nt lds\n"
    "v_mfma_f32_16x16x32_bf16 v[236:239], a[216:219], a[156:159], v[236:239]\n"
    "ds_read_b64_tr_b16 a[204:205], v253 offset:4104\n"
    "ds_read_b64_tr_b16 a[206:207], v253 offset:4616\n"
    "v_mfma_f32_16x16x32_bf16 v[236:239], a[220:223], a[188:191], v[236:239]\n"
    "v_mfma_f32_16x16x32_bf16 v[204:207], a[220:223], a[184:187], v[204:207]\n"
    "v_mfma_f32_16x16x32_bf16 v[204:207], a[216:219], a[152:155], v[204:207]\n"
    "v_mfma_f32_16x16x32_bf16 v[172:175], a[216:219], a[148:151], v[172:175]\n"
    "v_mfma_f32_16x16x32_bf16 v[172:175], a[220:223], a[180:183], v[172:175]\n"
    "v_mfma_f32_16x16x32_bf16 v[140:143], a[220:223], a[176:179], v[140:143]\n"
    "v_mfma_f32_16x16x32_bf16 v[140:143], a[216:219], a[144:147], v[140:143]\n"
    "v_mfma_f32_16x16x32_bf16 v[108:111], a[216:219], a[140:143], v[108:111]\n"
    "v_mfma_f32_16x16x32_bf16 v[108:111], a[220:223], a[172:175], v[108:111]\n"
    "v_mfma_f32_16x16x32_bf16 v[76:79], a[220:223], a[168:171], v[76:79]\n"
    "v_mfma_f32_16x16x32_bf16 v[76:79], a[216:219], a[136:139], v[76:79]\n"
    "v_mfma_f32_16x16x32_bf16 v[44:47], a[216:219], a[132:135], v[44:47]\n"
    "v_mfma_f32_16x16x32_bf16 v[44:47], a[220:223], a[164:167], v[44:47]\n"
    "v_mfma_f32_16x16x32_bf16 v[12:15], a[220:223], a[160:163], v[12:15]\n"
    "v_mfma_f32_16x16x32_bf16 v[12:15], a[216:219], a[128:131], v[12:15]\n"
    "s_waitcnt lgkmcnt(12)\n"
    "s_lshr_b32 s60, %3, 2\n"
    "s_lshl_b32 s60, s60, 21\n"
    "v_mfma_f32_16x16x32_bf16 v[16:19], a[224:227], a[128:131], v[16:19]\n"
    "s_and_b32 s61, %3, 3\n"
    "s_lshl_b32 s61, s61, 15\n"
    "s_add_u32 s60, s60, s61\n"
    "v_mfma_f32_16x16x32_bf16 v[16:19], a[228:231], a[160:163], v[16:19]\n"
    "s_mul_i32 s61, s65, 0x2400\n"
    "s_and_b32 s61, s61, 0x6400\n"
    "s_add_u32 s60, s60, s61\n"
    "v_mfma_f32_16x16x32_bf16 v[48:51], a[228:231], a[164:167], v[48:51]\n"
    "buffer_store_dwordx4 v[240:243], v240, s[32:35], s60 offen sc1\n"
    "s_xor_b32 s60, s60, 8192\n"
    "v_mfma_f32_16x16x32_bf16 v[48:51], a[224:227], a[132:135], v[48:51]\n"
    "buffer_store_dwordx4 v[240:243], v240, s[32:35], s60 offen sc1\n"
    "v_mfma_f32_16x16x32_bf16 v[80:83], a[224:227], a[136:139], v[80:83]\n"
    "v_mfma_f32_16x16x32_bf16 v[80:83], a[228:231], a[168:171], v[80:83]\n"
    "v_mfma_f32_16x16x32_bf16 v[112:115], a[228:231], a[172:175], v[112:115]\n"
    "v_mfma_f32_16x16x32_bf16 v[112:115], a[224:227], a[140:143], v[112:115]\n"
    "v_mfma_f32_16x16x32_bf16 v[144:147], a[224:227], a[144:147], v[144:147]\n"
    "v_mfma_f32_16x16x32_bf16 v[144:147], a[228:231], a[176:179], v[144:147]\n"
    "v_mfma_f32_16x16x32_bf16 v[176:179], a[228:231], a[180:183], v[176:179]\n"
    "v_mfma_f32_16x16x32_bf16 v[176:179], a[224:227], a[148:151], v[176:179]\n"
    "v_mfma_f32_16x16x32_bf16 v[208:211], a[224:227], a[152:155], v[208:211]\n"
    "v_mfma_f32_16x16x32_bf16 v[208:211], a[228:231], a[184:187], v[208:211]\n"
    "v_mfma_f32_16x16x32_bf16 a[240:243], a[228:231], a[188:191], a[240:243]\n"
    "v_mfma_f32_16x16x32_bf16 a[240:243], a[224:227], a[156:159], a[240:243]\n"
    "s_waitcnt lgkmcnt(8)\n"
    "v_mfma_f32_16x16x32_bf16 a[244:247], a[232:235], a[156:159], a[244:247]\n"
    "v_mfma_f32_16x16x32_bf16 a[244:247], a[236:239], a[188:191], a[244:247]\n"
    "v_mfma_f32_16x16x32_bf16 v[212:215], a[236:239], a[184:187], v[212:215]\n"
    "v_mfma_f32_16x16x32_bf16 v[212:215], a[232:235], a[152:155], v[212:215]\n"
    "v_mfma_f32_16x16x32_bf16 v[180:183], a[232:235], a[148:151], v[180:183]\n"
    "v_mfma_f32_16x16x32_bf16 v[180:183], a[236:239], a[180:183], v[180:183]\n"
    "v_mfma_f32_16x16x32_bf16 v[148:151], a[236:239], a[176:179], v[148:151]\n"
    "v_mfma_f32_16x16x32_bf16 v[148:151], a[232:235], a[144:147], v[148:151]\n"
    "v_mfma_f32_16x16x32_bf16 v[116:119], a[232:235], a[140:143], v[116:119]\n"
    "v_mfma_f32_16x16x32_bf16 v[116:119], a[236:239], a[172:175], v[116:119]\n"
    "v_mfma_f32_16x16x32_bf16 v[84:87], a[236:239], a[168:171], v[84:87]\n"
    "v_mfma_f32_16x16x32_bf16 v[84:87], a[232:235], a[136:139], v[84:87]\n"
    "v_mfma_f32_16x16x32_bf16 v[52:55], a[232:235], a[132:135], v[52:55]\n"
    "v_mfma_f32_16x16x32_bf16 v[52:55], a[236:239], a[164:167], v[52:55]\n"
    "v_mfma_f32_16x16x32_bf16 v[20:23], a[236:239], a[160:163], v[20:23]\n"
    "v_mfma_f32_16x16x32_bf16 v[20:23], a[232:235], a[128:131], v[20:23]\n"
    "s_waitcnt lgkmcnt(4)\n"
    "s_waitcnt vmcnt(19)\n"
    "ds_read_b64_tr_b16 a[208:209], v252 offset:32768\n"
    "v_mfma_f32_16x16x32_bf16 v[24:27], a[192:195], a[128:131], v[24:27]\n"
    "ds_read_b64_tr_b16 a[210:211], v252 offset:33280\n"
    "v_mfma_f32_16x16x32_bf16 v[24:27], a[196:199], a[160:163], v[24:27]\n"
    "ds_read_b64_tr_b16 a[212:213], v252 offset:36864\n"
    "ds_read_b64_tr_b16 a[214:215], v252 offset:37376\n"
    "v_mfma_f32_16x16x32_bf16 v[56:59], a[196:199], a[164:167], v[56:59]\n"
    "v_mfma_f32_16x16x32_bf16 v[56:59], a[192:195], a[132:135], v[56:59]\n"
    "ds_read_b64_tr_b16 a[216:217], v252 offset:32776\n"
    "ds_read_b64_tr_b16 a[218:219], v252 offset:33288\n"
    "v_mfma_f32_16x16x32_bf16 v[88:91], a[192:195], a[136:139], v[88:91]\n"
    "v_mfma_f32_16x16x32_bf16 v[88:91], a[196:199], a[168:171], v[88:91]\n"
    "ds_read_b64_tr_b16 a[220:221], v252 offset:36872\n"
    "ds_read_b64_tr_b16 a[222:223], v252 offset:37384\n"
    "v_mfma_f32_16x16x32_bf16 v[120:123], a[196:199], a[172:175], v[120:123]\n"
    "v_mfma_f32_16x16x32_bf16 v[120:123], a[192:195], a[140:143], v[120:123]\n"
    "ds_read_b64_tr_b16 a[224:225], v253 offset:32768\n"
    "ds_read_b64_tr_b16 a[226:227], v253 offset:33280\n"
    "v_mfma_f32_16x16x32_bf16 v[152:155], a[192:195], a[144:147], v[152:155]\n"
    "v_mfma_f32_16x16x32_bf16 v[152:155], a[196:199], a[176:179], v[152:155]\n"
    "ds_read_b64_tr_b16 a[228:229], v253 offset:36864\n"
    "ds_read_b64_tr_b16 a[230:231], v253 offset:37376\n"
    "v_mfma_f32_16x16x32_bf16 v[184:187], a[196:199], a[180:183], v[184:187]\n"
    "v_mfma_f32_16x16x32_bf16 v[184:187], a[192:195], a[148:151], v[184:187]\n"
    "ds_read_b64_tr_b16 a[232:233], v253 offset:32776\n"
    "ds_read_b64_tr_b16 a[234:235], v253 offset:33288\n"
    "v_mfma_f32_16x16x32_bf16 v[216:219], a[192:195], a[152:155], v[216:219]\n"
    "v_mfma_f32_16x16x32_bf16 v[216:219], a[196:199], a[184:187], v[216:219]\n"
    "s_barrier\n"
    "ds_read_b64_tr_b16 a[236:237], v253 offset:36872\n"
    "ds_read_b64_tr_b16 a[238:239], v253 offset:37384\n"
    "v_mfma_f32_16x16x32_bf16 a[248:251], a[196:199], a[188:191], a[248:251]\n"
    "v_mfma_f32_16x16x32_bf16 a[248:251], a[192:195], a[156:159], a[248:251]\n"
    "s_waitcnt lgkmcnt(15)\n"
    "v_mfma_f32_16x16x32_bf16 a[252:255], a[200:203], a[156:159], a[252:255]\n"
    "v_mfma_f32_16x16x32_bf16 a[252:255], a[204:207], a[188:191], a[252:255]\n"
    "v_mfma_f32_16x16x32_bf16 v[220:223], a[204:207], a[184:187], v[220:223]\n"
    "ds_read_b128 a[156:159], v241 offset:47104\n"
    "v_mfma_f32_16x16x32_bf16 v[220:223], a[200:203], a[152:155], v[220:223]\n"
    "ds_read_b128 a[188:191], v242 offset:47104\n"
    "v_mfma_f32_16x16x32_bf16 v[188:191], a[200:203], a[148:151], v[188:191]\n"
    "ds_read_b128 a[184:187], v242 offset:45056\n"
    "v_mfma_f32_16x16x32_bf16 v[188:191], a[204:207], a[180:183], v[188:191]\n"
    "ds_read_b128 a[152:155], v241 offset:45056\n"
    "v_mfma_f32_16x16x32_bf16 v[156:159], a[204:207], a[176:179], v[156:159]\n"
    "ds_read_b128 a[148:151], v241 offset:43008\n"
    "v_mfma_f32_16x16x32_bf16 v[156:159], a[200:203], a[144:147], v[156:159]\n"
    "ds_read_b128 a[180:183], v242 offset:43008\n"
    "v_mfma_f32_16x16x32_bf16 v[124:127], a[200:203], a[140:143], v[124:127]\n"
    "ds_read_b128 a[176:179], v242 offset:40960\n"
    "v_mfma_f32_16x16x32_bf16 v[124:127], a[204:207], a[172:175], v[124:127]\n"
    "ds_read_b128 a[144:147], v241 offset:40960\n"
    "v_mfma_f32_16x16x32_bf16 v[92:95], a[204:207], a[168:171], v[92:95]\n"
    "ds_read_b128 a[140:143], v241 offset:38912\n"
    "v_mfma_f32_16x16x32_bf16 v[92:95], a[200:203], a[136:139], v[92:95]\n"
    "s_add_u32 m0, s79, 0\n"
    "s_lshl_b32 s54, s64, 17\n"
    "s_xor_b32 s54, s54, s57\n"
    "v_mfma_f32_16x16x32_bf16 v[60:63], a[200:203], a[132:135], v[60:63]\n"
    "s_add_u32 s92, s72, s54\n"
    "s_addc_u32 s93, s73, 0\n"
    "v_mfma_f32_16x16x32_bf16 v[60:63], a[204:207], a[164:167], v[60:63]\n"
    "buffer_load_dwordx4 v254, s[92:95], 0 offen lds\n"
    "ds_read_b128 a[172:175], v242 offset:38912\n"
    "v_mfma_f32_16x16x32_bf16 v[28:31], a[204:207], a[160:163], v[28:31]\n"
    "ds_read_b128 a[168:171], v242 offset:36864\n"
    "v_mfma_f32_16x16x32_bf16 v[28:31], a[200:203], a[128:131], v[28:31]\n"
    "s_waitcnt lgkmcnt(9)\n"
    "ds_read_b128 a[136:139], v241 offset:36864\n"
    "v_mfma_f32_16x16x32_bf16 v[224:227], a[208:211], a[156:159], v[224:227]\n"
    "ds_read_b128 a[132:135], v241 offset:34816\n"
    "s_addk_i32 m0, 1024\n"
    "v_mfma_f32_16x16x32_bf16 v[224:227], a[212:215], a[188:191], v[224:227]\n"
    "s_waitcnt lgkmcnt(9)\n"
    "s_add_u32 s92, s92, 16384\n"
    "s_addc_u32 s93, s93, 0\n"
    "v_mfma_f32_16x16x32_bf16 v[192:195], a[212:215], a[184:187], v[192:195]\n"
    "buffer_load_dwordx4 v254, s[92:95], 0 offen lds\n"
    "ds_read_b128 a[164:167], v242 offset:34816\n"
    "ds_read_b128 a[160:163], v242 offset:32768\n"
    "ds_read_b128 a[128:131], v241 offset:32768\n"
    "v_mfma_f32_16x16x32_bf16 v[192:195], a[208:211], a[152:155], v[192:195]\n"
    "s_waitcnt lgkmcnt(10)\n"
    "s_addk_i32 m0, 1024\n"
    "s_add_u32 s92, s92, 16384\n"
    "v_mfma_f32_16x16x32_bf16 v[160:163], a[208:211], a[148:151], v[160:163]\n"
    "s_addc_u32 s93, s93, 0\n"
    "buffer_load_dwordx4 v254, s[92:95], 0 offen lds\n"
    "v_mfma_f32_16x16x32_bf16 v[160:163], a[212:215], a[180:183], v[160:163]\n"
    "s_waitcnt lgkmcnt(8)\n"
    "v_mfma_f32_16x16x32_bf16 v[128:131], a[212:215], a[176:179], v[128:131]\n"
    "v_mfma_f32_16x16x32_bf16 v[128:131], a[208:211], a[144:147], v[128:131]\n"
    "s_waitcnt lgkmcnt(6)\n"
    "s_addk_i32 m0, 1024\n"
    "s_add_u32 s92, s92, 16384\n"
    "v_mfma_f32_16x16x32_bf16 v[96:99], a[208:211], a[140:143], v[96:99]\n"
    "s_addc_u32 s93, s93, 0\n"
    "buffer_load_dwordx4 v254, s[92:95], 0 offen lds\n"
    "v_mfma_f32_16x16x32_bf16 v[96:99], a[212:215], a[172:175], v[96:99]\n"
    "s_waitcnt lgkmcnt(4)\n"
    "v_mfma_f32_16x16x32_bf16 v[64:67], a[212:215], a[168:171], v[64:67]\n"
    "v_mfma_f32_16x16x32_bf16 v[64:67], a[208:211], a[136:139], v[64:67]\n"
    "s_waitcnt lgkmcnt(2)\n"
    "s_addk_i32 m0, 1024\n"
    "s_add_u32 s92, s92, 16384\n"
    "v_mfma_f32_16x16x32_bf16 v[32:35], a[208:211], a[132:135], v[32:35]\n"
    "s_addc_u32 s93, s93, 0\n"
    "buffer_load_dwordx4 v254, s[92:95], 0 offen lds\n"
    "v_mfma_f32_16x16x32_bf16 v[32:35], a[212:215], a[164:167], v[32:35]\n"
    "s_waitcnt lgkmcnt(0)\n"
    "v_mfma_f32_16x16x32_bf16 v[0:3], a[212:215], a[160:163], v[0:3]\n"
    "v_mfma_f32_16x16x32_bf16 v[0:3], a[208:211], a[128:131], v[0:3]\n"
    "s_addk_i32 m0, 1024\n"
    "s_add_u32 s92, s92, 16384\n"
    "s_addc_u32 s93, s93, 0\n"
    "v_mfma_f32_16x16x32_bf16 v[4:7], a[216:219], a[128:131], v[4:7]\n"
    "buffer_load_dwordx4 v254, s[92:95], 0 offen lds\n"
    "v_mfma_f32_16x16x32_bf16 v[4:7], a[220:223], a[160:163], v[4:7]\n"
    "v_mfma_f32_16x16x32_bf16 v[36:39], a[220:223], a[164:167], v[36:39]\n"
    "v_mfma_f32_16x16x32_bf16 v[36:39], a[216:219], a[132:135], v[36:39]\n"
    "s_addk_i32 m0, 1024\n"
    "s_add_u32 s92, s92, 16384\n"
    "s_addc_u32 s93, s93, 0\n"
    "v_mfma_f32_16x16x32_bf16 v[68:71], a[216:219], a[136:139], v[68:71]\n"
    "buffer_load_dwordx4 v254, s[92:95], 0 offen lds\n"
    "v_mfma_f32_16x16x32_bf16 v[68:71], a[220:223], a[168:171], v[68:71]\n"
    "v_mfma_f32_16x16x32_bf16 v[100:103], a[220:223], a[172:175], v[100:103]\n"
    "v_mfma_f32_16x16x32_bf16 v[100:103], a[216:219], a[140:143], v[100:103]\n"
    "s_addk_i32 m0, 1024\n"
    "s_add_u32 s92, s92, 16384\n"
    "s_addc_u32 s93, s93, 0\n"
    "v_mfma_f32_16x16x32_bf16 v[132:135], a[216:219], a[144:147], v[132:135]\n"
    "buffer_load_dwordx4 v254, s[92:95], 0 offen lds\n"
    "s_bitcmp1_b32 s85, 5\n"
    "s_cbranch_scc1 CP5_%=\n"
    "CR5_%=:\n"
    "v_mfma_f32_16x16x32_bf16 v[132:135], a[220:223], a[176:179], v[132:135]\n"
    "v_mfma_f32_16x16x32_bf16 v[164:167], a[220:223], a[180:183], v[164:167]\n"
    "v_mfma_f32_16x16x32_bf16 v[164:167], a[216:219], a[148:151], v[164:167]\n"
    "s_xor_b32 s59, s66, s67\n"
    "s_addk_i32 s67, 128\n"
    "s_cmpk_eq_u32 s67, 1536\n"
    "v_mfma_f32_16x16x32_bf16 v[196:199], a[216:219], a[152:155], v[196:199]\n"
    "s_cselect_b32 s67, 0, s67\n"
    "s_add_u32 m0, s68, 81920\n"
    "s_add_u32 s96, s24, s59\n"
    "v_mfma_f32_16x16x32_bf16 v[196:199], a[220:223], a[184:187], v[196:199]\n"
    "s_addc_u32 s97, s25, 0\n"
    "buffer_load_dwordx4 v255, s[96:99], 0 offen lds\n"
    "v_mfma_f32_16x16x32_bf16 v[228:231], a[220:223], a[188:191], v[228:231]\n"
    "v_mfma_f32_16x16x32_bf16 v[228:231], a[216:219], a[156:159], v[228:231]\n"
    "v_add_u32 v252, 0x20000, v252\n"
    "v_add_u32 v253, 0x20000, v253\n"
    "v_mfma_f32_16x16x32_bf16 v[232:235], a[224:227], a[156:159], v[232:235]\n"
    "s_waitcnt vmcnt(19)\n"
    "ds_read_b64_tr_b16 a[192:193], v252 offset:0\n"
    "ds_read_b64_tr_b16 a[194:195], v252 offset:512\n"
    "v_mfma_f32_16x16x32_bf16 v[232:235], a[228:231], a[188:191], v[232:235]\n"
    "s_add_u32 m0, s68, 82944\n"
    "s_add_u32 s96, s96, 16384\n"
    "s_addc_u32 s97, s97, 0\n"
    "v_mfma_f32_16x16x32_bf16 v[200:203], a[228:231], a[184:187], v[200:203]\n"
    "buffer_load_dwordx4 v243, s[96:99], 0 offen lds\n"
    "ds_read_b64_tr_b16 a[196:197], v252 offset:4096\n"
    "v_mfma_f32_16x16x32_bf16 v[200:203], a[224:227], a[152:155], v[200:203]\n"
    "ds_read_b64_tr_b16 a[198:199], v252 offset:4608\n"
    "ds_read_b64_tr_b16 a[200:201], v252 offset:8\n"
    "v_mfma_f32_16x16x32_bf16 v[168:171], a[224:227], a[148:151], v[168:171]\n"
    "ds_read_b64_tr_b16 a[202:203], v252 offset:520\n"
    "s_add_u32 m0, s68, 83968\n"
    "v_mfma_f32_16x16x32_bf16 v[168:171], a[228:231], a[180:183], v[168:171]\n"
    "s_add_u32 s96, s96, 16384\n"
    "s_addc_u32 s97, s97, 0\n"
    "v_mfma_f32_16x16x32_bf16 v[136:139], a[228:231], a[176:179], v[136:139]\n"
    "buffer_load_dwordx4 v255, s[96:99], 0 offen lds\n"
    "ds_read_b64_tr_b16 a[204:205], v252 offset:4104\n"
    "v_mfma_f32_16x16x32_bf16 v[136:139], a[224:227], a[144:147], v[136:139]\n"
    "ds_read_b64_tr_b16 a[206:207], v252 offset:4616\n"
    "ds_read_b64_tr_b16 a[208:209], v253 offset:0\n"
    "v_mfma_f32_16x16x32_bf16 v[104:107], a[224:227], a[140:143], v[104:107]\n"
    "ds_read_b64_tr_b16 a[210:211], v253 offset:512\n"
    "s_add_u32 m0, s68, 84992\n"
    "v_mfma_f32_16x16x32_bf16 v[104:107], a[228:231], a[172:175], v[104:107]\n"
    "s_add_u32 s96, s96, 16384\n"
    "s_addc_u32 s97, s97, 0\n"
    "v_mfma_f32_16x16x32_bf16 v[72:75], a[228:231], a[168:171], v[72:75]\n"
    "buffer_load_dwordx4 v243, s[96:99], 0 offen lds\n"
    "ds_read_b64_tr_b16 a[212:213], v253 offset:4096\n"
    "v_mfma_f32_16x16x32_bf16 v[72:75], a[224:227], a[136:139], v[72:75]\n"
    "ds_read_b64_tr_b16 a[214:215], v253 offset:4608\n"
    "ds_read_b64_tr_b16 a[216:217], v253 offset:8\n"
    "v_mfma_f32_16x16x32_bf16 v[40:43], a[224:227], a[132:135], v[40:43]\n"
    "ds_read_b64_tr_b16 a[218:219], v253 offset:520\n"
    "s_add_u32 m0, s79, 32768\n"
    "v_mfma_f32_16x16x32_bf16 v[40:43], a[228:231], a[164:167], v[40:43]\n"
    "s_lshl_b32 s54, s64, 17\n"
    "s_xor_b32 s54, s54, s57\n"
    "s_addk_i32 s54, 512\n"
    "v_mfma_f32_16x16x32_bf16 v[8:11], a[228:231], a[160:163], v[8:11]\n"
    "s_add_u32 s92, s72, s54\n"
    "s_addc_u32 s93, s73, 0\n"
    "v_mfma_f32_16x16x32_bf16 v[8:11], a[224:227], a[128:131], v[8:11]\n"
    "buffer_load_dwordx4 v254, s[92:95], 0 offen lds\n"
    "ds_read_b64_tr_b16 a[220:221], v253 offset:4104\n"
    "v_mfma_f32_16x16x32_bf16 v[12:15], a[232:235], a[128:131], v[12:15]\n"
    "ds_read_b64_tr_b16 a[222:223], v253 offset:4616\n"
    "s_addk_i32 m0, 1024\n"
    "v_mfma_f32_16x16x32_bf16 v[12:15], a[236:239], a[160:163], v[12:15]\n"
    "s_add_u32 s92, s92, 16384\n"
    "s_addc_u32 s93, s93, 0\n"
    "v_mfma_f32_16x16x32_bf16 v[44:47], a[236:239], a[164:167], v[44:47]\n"
    "buffer_load_dwordx4 v254, s[92:95], 0 offen lds\n"
    "v_mfma_f32_16x16x32_bf16 v[44:47], a[232:235], a[132:135], v[44:47]\n"
    "s_addk_i32 m0, 1024\n"
    "s_add_u32 s92, s92, 16384\n"
    "s_addc_u32 s93, s93, 0\n"
    "v_mfma_f32_16x16x32_bf16 v[76:79], a[232:235], a[136:139], v[76:79]\n"
    "buffer_load_dwordx4 v254, s[92:95], 0 offen lds\n"
    "v_mfma_f32_16x16x32_bf16 v[76:79], a[236:239], a[168:171], v[76:79]\n"
    "v_mfma_f32_16x16x32_bf16 v[108:111], a[236:239], a[172:175], v[108:111]\n"
    "v_mfma_f32_16x16x32_bf16 v[108:111], a[232:235], a[140:143], v[108:111]\n"
    "s_addk_i32 m0, 1024\n"
    "s_add_u32 s92, s92, 16384\n"
    "s_addc_u32 s93, s93, 0\n"
    "v_mfma_f32_16x16x32_bf16 v[140:143], a[232:235], a[144:147], v[140:143]\n"
    "buffer_load_dwordx4 v254, s[92:95], 0 offen lds\n"
    "v_mfma_f32_16x16x32_bf16 v[140:143], a[236:239], a[176:179], v[140:143]\n"
    "v_mfma_f32_16x16x32_bf16 v[172:175], a[236:239], a[180:183], v[172:175]\n"
    "v_mfma_f32_16x16x32_bf16 v[172:175], a[232:235], a[148:151], v[172:175]\n"
    "s_addk_i32 m0, 1024\n"
    "s_add_u32 s92, s92, 16384\n"
    "s_addc_u32 s93, s93, 0\n"
    "v_mfma_f32_16x16x32_bf16 v[204:207], a[232:235], a[152:155], v[204:207]\n"
    "buffer_load_dwordx4 v254, s[92:95], 0 offen lds\n"
    "v_mfma_f32_16x16x32_bf16 v[204:207], a[236:239], a[184:187], v[204:207]\n"
    "v_mfma_f32_16x16x32_bf16 v[236:239], a[236:239], a[188:191], v[236:239]\n"
    "v_mfma_f32_16x16x32_bf16 v[236:239], a[232:235], a[156:159], v[236:239]\n"
    "s_waitcnt lgkmcnt(12)\n"
    "s_addk_i32 m0, 1024\n"
    "s_add_u32 s92, s92, 16384\n"
    "v_mfma_f32_16x16x32_bf16 a[240:243], a[192:195], a[156:159], a[240:243]\n"
    "s_addc_u32 s93, s93, 0\n"
    "buffer_load_dwordx4 v254, s[92:95], 0 offen lds\n"
    "v_mfma_f32_16x16x32_bf16 a[240:243], a[196:199], a[188:191], a[240:243]\n"
    "v_mfma_f32_16x16x32_bf16 v[208:211], a[196:199], a[184:187], v[208:211]\n"
    "v_mfma_f32_16x16x32_bf16 v[208:211], a[192:195], a[152:155], v[208:211]\n"
    "s_addk_i32 m0, 1024\n"
    "s_add_u32 s92, s92, 16384\n"
    "s_addc_u32 s93, s93, 0\n"
    "v_mfma_f32_16x16x32_bf16 v[176:179], a[192:195], a[148:151], v[176:179]\n"
    "buffer_load_dwordx4 v254, s[92:95], 0 offen lds\n"
    "v_mfma_f32_16x16x32_bf16 v[176:179], a[196:199], a[180:183], v[176:179]\n"
    "v_mfma_f32_16x16x32_bf16 v[144:147], a[196:199], a[176:179], v[144:147]\n"
    "v_mfma_f32_16x16x32_bf16 v[144:147], a[192:195], a[144:147], v[144:147]\n"
    "s_addk_i32 m0, 1024\n"
    "s_add_u32 s92, s92, 16384\n"
    "s_addc_u32 s93, s93, 0\n"
    "v_mfma_f32_16x16x32_bf16 v[112:115], a[192:195], a[140:143], v[112:115]\n"
    "buffer_load_dwordx4 v254, s[92:95], 0 offen lds\n"
    "s_add_u32 s57, s57, 131072\n"
    "v_mfma_f32_16x16x32_bf16 v[112:115], a[196:199], a[172:175], v[112:115]\n"
    "s_cmp_ge_u32 s57, 1572864\n"
    "s_cselect_b32 s57, s70, s57\n"
    "v_mfma_f32_16x16x32_bf16 v[80:83], a[196:199], a[168:171], v[80:83]\n"
    "v_mfma_f32_16x16x32_bf16 v[80:83], a[192:195], a[136:139], v[80:83]\n"
    "s_waitcnt lgkmcnt(0)\n"
    "s_add_u32 m0, s79, 131072\n"
    "s_lshl_b32 s54, s64, 17\n"
    "v_mfma_f32_16x16x32_bf16 v[48:51], a[192:195], a[132:135], v[48:51]\n"
    "s_xor_b32 s54, s54, s57\n"
    "s_add_u32 s92, s72, s54\n"
    "s_addc_u32 s93, s73, 0\n"
    "v_mfma_f32_16x16x32_bf16 v[48:51], a[196:199], a[164:167], v[48:51]\n"
    "buffer_load_dwordx4 v254, s[92:95], 0 offen lds\n"
    "v_mfma_f32_16x16x32_bf16 v[16:19], a[196:199], a[160:163], v[16:19]\n"
    "v_mfma_f32_16x16x32_bf16 v[16:19], a[192:195], a[128:131], v[16:19]\n"
    "s_addk_i32 m0, 1024\n"
    "s_add_u32 s92, s92, 16384\n"
    "s_addc_u32 s93, s93, 0\n"
    "v_mfma_f32_16x16x32_bf16 v[20:23], a[200:203], a[128:131], v[20:23]\n"
    "buffer_load_dwordx4 v254, s[92:95], 0 offen lds\n"
    "v_mfma_f32_16x16x32_bf16 v[20:23], a[204:207], a[160:163], v[20:23]\n"
    "v_mfma_f32_16x16x32_bf16 v[52:55], a[204:207], a[164:167], v[52:55]\n"
    "v_mfma_f32_16x16x32_bf16 v[52:55], a[200:203], a[132:135], v[52:55]\n"
    "s_addk_i32 m0, 1024\n"
    "s_add_u32 s92, s92, 16384\n"
    "s_addc_u32 s93, s93, 0\n"
    "v_mfma_f32_16x16x32_bf16 v[84:87], a[200:203], a[136:139], v[84:87]\n"
    "buffer_load_dwordx4 v254, s[92:95], 0 offen lds\n"
    "v_mfma_f32_16x16x32_bf16 v[84:87], a[204:207], a[168:171], v[84:87]\n"
    "v_mfma_f32_16x16x32_bf16 v[116:119], a[204:207], a[172:175], v[116:119]\n"
    "v_mfma_f32_16x16x32_bf16 v[116:119], a[200:203], a[140:143], v[116:119]\n"
    "s_addk_i32 m0, 1024\n"
    "s_add_u32 s92, s92, 16384\n"
    "s_addc_u32 s93, s93, 0\n"
    "v_mfma_f32_16x16x32_bf16 v[148:151], a[200:203], a[144:147], v[148:151]\n"
    "buffer_load_dwordx4 v254, s[92:95], 0 offen lds\n"
    "v_mfma_f32_16x16x32_bf16 v[148:151], a[204:207], a[176:179], v[148:151]\n"
    "v_mfma_f32_16x16x32_bf16 v[180:183], a[204:207], a[180:183], v[180:183]\n"
    "v_mfma_f32_16x16x32_bf16 v[180:183], a[200:203], a[148:151], v[180:183]\n"
    "s_addk_i32 m0, 1024\n"
    "s_add_u32 s92, s92, 16384\n"
    "s_addc_u32 s93, s93, 0\n"
    "v_mfma_f32_16x16x32_bf16 v[212:215], a[200:203], a[152:155], v[212:215]\n"
    "buffer_load_dwordx4 v254, s[92:95], 0 offen lds\n"
    "v_mfma_f32_16x16x32_bf16 v[212:215], a[204:207], a[184:187], v[212:215]\n"
    "v_mfma_f32_16x16x32_bf16 a[244:247], a[204:207], a[188:191], a[244:247]\n"
    "v_mfma_f32_16x16x32_bf16 a[244:247], a[200:203], a[156:159], a[244:247]\n"
    "v_add_u32 v252, 0xfffe0000, v252\n"
    "v_add_u32 v253, 0xfffe0000, v253\n"
    "v_mfma_f32_16x16x32_bf16 a[248:251], a[208:211], a[156:159], a[248:251]\n"
    "s_waitcnt vmcnt(17)\n"
    "ds_read_b64_tr_b16 a[224:225], v252 offset:0\n"
    "ds_read_b64_tr_b16 a[226:227], v252 offset:512\n"
    "v_mfma_f32_16x16x32_bf16 a[248:251], a[212:215], a[188:191], a[248:251]\n"
    "s_addk_i32 m0, 1024\n"
    "s_add_u32 s92, s92, 16384\n"
    "s_addc_u32 s93, s93, 0\n"
    "v_mfma_f32_16x16x32_bf16 v[216:219], a[212:215], a[184:187], v[216:219]\n"
    "buffer_load_dwordx4 v254, s[92:95], 0 offen lds\n"
    "ds_read_b64_tr_b16 a[228:229], v252 offset:4096\n"
    "v_mfma_f32_16x16x32_bf16 v[216:219], a[208:211], a[152:155], v[216:219]\n"
    "ds_read_b64_tr_b16 a[230:231], v252 offset:4608\n"
    "ds_read_b64_tr_b16 a[232:233], v252 offset:8\n"
    "v_mfma_f32_16x16x32_bf16 v[184:187], a[208:211], a[148:151], v[184:187]\n"
    "ds_read_b64_tr_b16 a[234:235], v252 offset:520\n"
    "s_addk_i32 m0, 1024\n"
    "v_mfma_f32_16x16x32_bf16 v[184:187], a[212:215], a[180:183], v[184:187]\n"
    "s_add_u32 s92, s92, 16384\n"
    "s_addc_u32 s93, s93, 0\n"
    "v_mfma_f32_16x16x32_bf16 v[152:155], a[212:215], a[176:179], v[152:155]\n"
    "buffer_load_dwordx4 v254, s[92:95], 0 offen lds\n"
    "ds_read_b64_tr_b16 a[236:237], v252 offset:4104\n"
    "v_mfma_f32_16x16x32_bf16 v[152:155], a[208:211], a[144:147], v[152:155]\n"
    "ds_read_b64_tr_b16 a[238:239], v252 offset:4616\n"
    "ds_read_b64_tr_b16 a[192:193], v253 offset:0\n"
    "v_mfma_f32_16x16x32_bf16 v[120:123], a[208:211], a[140:143], v[120:123]\n"
    "ds_read_b64_tr_b16 a[194:195], v253 offset:512\n"
    "s_addk_i32 m0, 1024\n"
    "v_mfma_f32_16x16x32_bf16 v[120:123], a[212:215], a[172:175], v[120:123]\n"
    "s_add_u32 s92, s92, 16384\n"
    "s_addc_u32 s93, s93, 0\n"
    "v_mfma_f32_16x16x32_bf16 v[88:91], a[212:215], a[168:171], v[88:91]\n"
    "buffer_load_dwordx4 v254, s[92:95], 0 offen lds\n"
    "ds_read_b64_tr_b16 a[196:197], v253 offset:4096\n"
    "v_mfma_f32_16x16x32_bf16 v[88:91], a[208:211], a[136:139], v[88:91]\n"
    "ds_read_b64_tr_b16 a[198:199], v253 offset:4608\n"
    "ds_read_b64_tr_b16 a[200:201], v253 offset:8\n"
    "v_mfma_f32_16x16x32_bf16 v[56:59], a[208:211], a[132:135], v[56:59]\n"
    "ds_read_b64_tr_b16 a[202:203], v253 offset:520\n"
    "v_mfma_f32_16x16x32_bf16 v[56:59], a[212:215], a[164:167], v[56:59]\n"
    "s_barrier\n"
    "ds_read_b64_tr_b16 a[204:205], v253 offset:4104\n"
    "ds_read_b64_tr_b16 a[206:207], v253 offset:4616\n"
    "v_mfma_f32_16x16x32_bf16 v[24:27], a[212:215], a[160:163], v[24:27]\n"
    "v_mfma_f32_16x16x32_bf16 v[24:27], a[208:211], a[128:131], v[24:27]\n"
    "v_mfma_f32_16x16x32_bf16 v[28:31], a[216:219], a[128:131], v[28:31]\n"
    "v_mfma_f32_16x16x32_bf16 v[28:31], a[220:223], a[160:163], v[28:31]\n"
    "v_mfma_f32_16x16x32_bf16 v[60:63], a[220:223], a[164:167], v[60:63]\n"
    "ds_read_b128 a[128:131], v241 offset:0\n"
    "v_mfma_f32_16x16x32_bf16 v[60:63], a[216:219], a[132:135], v[60:63]\n"
    "s_waitcnt lgkmcnt(1)\n"
    "s_add_u32 m0, s79, 0\n"
    "s_lshl_b32 s54, s64, 17\n"
    "v_mfma_f32_16x16x32_bf16 v[92:95], a[216:219], a[136:139], v[92:95]\n"
    "s_xor_b32 s54, s54, s57\n"
    "s_addk_i32 s54, 512\n"
    "s_add_u32 s92, s72, s54\n"
    "v_mfma_f32_16x16x32_bf16 v[92:95], a[220:223], a[168:171], v[92:95]\n"
    "s_addc_u32 s93, s73, 0\n"
    "buffer_load_dwordx4 v254, s[92:95], 0 offen lds\n"
    "v_mfma_f32_16x16x32_bf16 v[124:127], a[220:223], a[172:175], v[124:127]\n"
    "ds_read_b128 a[160:163], v242 offset:0\n"
    "v_mfma_f32_16x16x32_bf16 v[124:127], a[216:219], a[140:143], v[124:127]\n"
    "ds_read_b128 a[164:167], v242 offset:2048\n"
    "v_mfma_f32_16x16x32_bf16 v[156:159], a[216:219], a[144:147], v[156:159]\n"
    "ds_read_b128 a[132:135], v241 offset:2048\n"
    "v_mfma_f32_16x16x32_bf16 v[156:159], a[220:223], a[176:179], v[156:159]\n"
    "ds_read_b128 a[136:139], v241 offset:4096\n"
    "s_addk_i32 m0, 1024\n"
    "v_mfma_f32_16x16x32_bf16 v[188:191], a[220:223], a[180:183], v[188:191]\n"
    "s_add_u32 s92, s92, 16384\n"
    "s_addc_u32 s93, s93, 0\n"
    "v_mfma_f32_16x16x32_bf16 v[188:191], a[216:219], a[148:151], v[188:191]\n"
    "buffer_load_dwordx4 v254, s[92:95], 0 offen lds\n"
    "ds_read_b128 a[168:171], v242 offset:4096\n"
    "ds_read_b128 a[172:175], v242 offset:6144\n"
    "v_mfma_f32_16x16x32_bf16 v[220:223], a[216:219], a[152:155], v[220:223]\n"
    "ds_read_b128 a[140:143], v241 offset:6144\n"
    "v_mfma_f32_16x16x32_bf16 v[220:223], a[220:223], a[184:187], v[220:223]\n"
    "ds_read_b128 a[144:147], v241 offset:8192\n"
    "s_addk_i32 m0, 1024\n"
    "v_mfma_f32_16x16x32_bf16 a[252:255], a[220:223], a[188:191], a[252:255]\n"
    "s_add_u32 s92, s92, 16384\n"
    "s_addc_u32 s93, s93, 0\n"
    "v_mfma_f32_16x16x32_bf16 a[252:255], a[216:219], a[156:159], a[252:255]\n"
    "s_waitcnt lgkmcnt(7)\n"
    "buffer_load_dwordx4 v254, s[92:95], 0 offen lds\n"
    "ds_read_b128 a[176:179], v242 offset:8192\n"
    "ds_read_b128 a[180:183], v242 offset:10240\n"
    "v_mfma_f32_16x16x32_bf16 v[0:3], a[224:227], a[128:131], v[0:3]\n"
    "ds_read_b128 a[148:151], v241 offset:10240\n"
    "v_mfma_f32_16x16x32_bf16 v[0:3], a[228:231], a[160:163], v[0:3]\n"
    "s_waitcnt lgkmcnt(8)\n"
    "ds_read_b128 a[152:155], v241 offset:12288\n"
    "s_addk_i32 m0, 1024\n"
    "v_mfma_f32_16x16x32_bf16 v[32:35], a[228:231], a[164:167], v[32:35]\n"
    "s_add_u32 s92, s92, 16384\n"
    "s_addc_u32 s93, s93, 0\n"
    "v_mfma_f32_16x16x32_bf16 v[32:35], a[224:227], a[132:135], v[32:35]\n"
    "s_waitcnt lgkmcnt(7)\n"
    "buffer_load_dwordx4 v254, s[92:95], 0 offen lds\n"
    "ds_read_b128 a[184:187], v242 offset:12288\n"
    "ds_read_b128 a[188:191], v242 offset:14336\n"
    "ds_read_b128 a[156:159], v241 offset:14336\n"
    "v_mfma_f32_16x16x32_bf16 v[64:67], a[224:227], a[136:139], v[64:67]\n"
    "s_addk_i32 m0, 1024\n"
    "s_add_u32 s92, s92, 16384\n"
    "s_addc_u32 s93, s93, 0\n"
    "v_mfma_f32_16x16x32_bf16 v[64:67], a[228:231], a[168:171], v[64:67]\n"
    "s_waitcnt lgkmcnt(8)\n"
    "buffer_load_dwordx4 v254, s[92:95], 0 offen lds\n"
    "v_mfma_f32_16x16x32_bf16 v[96:99], a[228:231], a[172:175], v[96:99]\n"
    "v_mfma_f32_16x16x32_bf16 v[96:99], a[224:227], a[140:143], v[96:99]\n"
    "s_waitcnt lgkmcnt(6)\n"
    "s_addk_i32 m0, 1024\n"
    "s_add_u32 s92, s92, 16384\n"
    "v_mfma_f32_16x16x32_bf16 v[128:131], a[224:227], a[144:147], v[128:131]\n"
    "s_addc_u32 s93, s93, 0\n"
    "buffer_load_dwordx4 v254, s[92:95], 0 offen lds\n"
    "v_mfma_f32_16x16x32_bf16 v[128:131], a[228:231], a[176:179], v[128:131]\n"
    "s_waitcnt lgkmcnt(4)\n"
    "v_mfma_f32_16x16x32_bf16 v[160:163], a[228:231], a[180:183], v[160:163]\n"
    "v_mfma_f32_16x16x32_bf16 v[160:163], a[224:227], a[148:151], v[160:163]\n"
    "s_waitcnt lgkmcnt(2)\n"
    "s_addk_i32 m0, 1024\n"
    "s_add_u32 s92, s92, 16384\n"
    "v_mfma_f32_16x16x32_bf16 v[192:195], a[224:227], a[152:155], v[192:195]\n"
    "s_addc_u32 s93, s93, 0\n"
    "buffer_load_dwordx4 v254, s[92:95], 0 offen lds\n"
    "v_mfma_f32_16x16x32_bf16 v[192:195], a[228:231], a[184:187], v[192:195]\n"
    "s_waitcnt lgkmcnt(0)\n"
    "v_mfma_f32_16x16x32_bf16 v[224:227], a[228:231], a[188:191], v[224:227]\n"
    "v_mfma_f32_16x16x32_bf16 v[224:227], a[224:227], a[156:159], v[224:227]\n"
    "s_addk_i32 m0, 1024\n"
    "s_add_u32 s92, s92, 16384\n"
    "s_addc_u32 s93, s93, 0\n"
    "v_mfma_f32_16x16x32_bf16 v[228:231], a[232:235], a[156:159], v[228:231]\n"
    "buffer_load_dwordx4 v254, s[92:95], 0 offen lds\n"
    "s_add_u32 s57, s57, 131072\n"
    "v_mfma_f32_16x16x32_bf16 v[228:231], a[236:239], a[188:191], v[228:231]\n"
    "s_cmp_ge_u32 s57, 1572864\n"
    "s_cselect_b32 s57, s70, s57\n"
    "v_mfma_f32_16x16x32_bf16 v[196:199], a[236:239], a[184:187], v[196:199]\n"
    "v_mfma_f32_16x16x32_bf16 v[196:199], a[232:235], a[152:155], v[196:199]\n"
    "v_mfma_f32_16x16x32_bf16 v[164:167], a[232:235], a[148:151], v[164:167]\n"
    "v_mfma_f32_16x16x32_bf16 v[164:167], a[236:239], a[180:183], v[164:167]\n"
    "s_xor_b32 s59, s66, s67\n"
    "s_addk_i32 s67, 128\n"
    "s_cmpk_eq_u32 s67, 1536\n"
    "v_mfma_f32_16x16x32_bf16 v[132:135], a[236:239], a[176:179], v[132:135]\n"
    "s_cselect_b32 s67, 0, s67\n"
    "s_add_u32 m0, s68, 98304\n"
    "s_add_u32 s96, s24, s59\n"
    "v_mfma_f32_16x16x32_bf16 v[132:135], a[232:235], a[144:147], v[132:135]\n"
    "s_addc_u32 s97, s25, 0\n"
    "buffer_load_dwordx4 v255, s[96:99], 0 offen lds\n"
    "v_mfma_f32_16x16x32_bf16 v[100:103], a[232:235], a[140:143], v[100:103]\n"
    "s_bitcmp1_b32 s85, 6\n"
    "s_cbranch_scc1 CP6_%=\n"
    "CR6_%=:\n"
    "s_add_u32 m0, s68, 99328\n"
    "v_mfma_f32_16x16x32_bf16 v[100:103], a[236:239], a[172:175], v[100:103]\n"
    "s_add_u32 s96, s96, 16384\n"
    "s_addc_u32 s97, s97, 0\n"
    "v_mfma_f32_16x16x32_bf16 v[68:71], a[236:239], a[168:171], v[68:71]\n"
    "buffer_load_dwordx4 v243, s[96:99], 0 offen lds\n"
    "v_mfma_f32_16x16x32_bf16 v[68:71], a[232:235], a[136:139], v[68:71]\n"
    "s_add_u32 m0, s68, 100352\n"
    "s_add_u32 s96, s96, 16384\n"
    "s_addc_u32 s97, s97, 0\n"
    "v_mfma_f32_16x16x32_bf16 v[36:39], a[232:235], a[132:135], v[36:39]\n"
    "buffer_load_dwordx4 v255, s[96:99], 0 offen lds\n"
    "v_mfma_f32_16x16x32_bf16 v[36:39], a[236:239], a[164:167], v[36:39]\n"
    "v_mfma_f32_16x16x32_bf16 v[4:7], a[236:239], a[160:163], v[4:7]\n"
    "s_add_u32 m0, s68, 101376\n"
    "s_add_u32 s96, s96, 16384\n"
    "s_addc_u32 s97, s97, 0\n"
    "v_mfma_f32_16x16x32_bf16 v[4:7], a[232:235], a[128:131], v[4:7]\n"
    "buffer_load_dwordx4 v243, s[96:99], 0 offen lds\n"
    "s_waitcnt vmcnt(20)\n"
    "v_mfma_f32_16x16x32_bf16 v[8:11], a[192:195], a[128:131], v[8:11]\n"
    "ds_read_b64_tr_b16 a[208:209], v252 offset:32768\n"
    "ds_read_b64_tr_b16 a[210:211], v252 offset:33280\n"
    "v_mfma_f32_16x16x32_bf16 v[8:11], a[196:199], a[160:163], v[8:11]\n"
    "ds_read_b64_tr_b16 a[212:213], v252 offset:36864\n"
    "ds_read_b64_tr_b16 a[214:215], v252 offset:37376\n"
    "v_mfma_f32_16x16x32_bf16 v[40:43], a[196:199], a[164:167], v[40:43]\n"
    "v_add_u32 v246, s90, v252\n"
    "ds_read_b64_tr_b16 v[248:249], v246 offset:0\n"
    "v_mfma_f32_16x16x32_bf16 v[40:43], a[192:195], a[132:135], v[40:43]\n"
    "ds_read_b64_tr_b16 v[250:251], v246 offset:512\n"
    "ds_read_b64_tr_b16 a[216:217], v252 offset:32776\n"
    "v_mfma_f32_16x16x32_bf16 v[72:75], a[192:195], a[136:139], v[72:75]\n"
    "ds_read_b64_tr_b16 a[218:219], v252 offset:33288\n"
    "v_mfma_f32_16x16x32_bf16 v[72:75], a[196:199], a[168:171], v[72:75]\n"
    "ds_read_b64_tr_b16 a[220:221], v252 offset:36872\n"
    "ds_read_b64_tr_b16 a[222:223], v252 offset:37384\n"
    "v_mfma_f32_16x16x32_bf16 v[104:107], a[196:199], a[172:175], v[104:107]\n"
    "v_mfma_f32_16x16x32_bf16 v[104:107], a[192:195], a[140:143], v[104:107]\n"
    "ds_read_b64_tr_b16 a[224:225], v253 offset:32768\n"
    "ds_read_b64_tr_b16 a[226:227], v253 offset:33280\n"
    "v_mfma_f32_16x16x32_bf16 v[136:139], a[192:195], a[144:147], v[136:139]\n"
    "v_mfma_f32_16x16x32_bf16 v[136:139], a[196:199], a[176:179], v[136:139]\n"
    "ds_read_b64_tr_b16 a[228:229], v253 offset:36864\n"
    "ds_read_b64_tr_b16 a[230:231], v253 offset:37376\n"
    "v_mfma_f32_16x16x32_bf16 v[168:171], a[196:199], a[180:183], v[168:171]\n"
    "v_mfma_f32_16x16x32_bf16 v[168:171], a[192:195], a[148:151], v[168:171]\n"
    "ds_read_b64_tr_b16 a[232:233], v253 offset:32776\n"
    "ds_read_b64_tr_b16 a[234:235], v253 offset:33288\n"
    "v_mfma_f32_16x16x32_bf16 v[200:203], a[192:195], a[152:155], v[200:203]\n"
    "s_add_u32 s62, s91, 524288\n"
    "s_waitcnt lgkmcnt(10)\n"
    "v_mfma_f32_16x16x32_bf16 v[200:203], a[196:199], a[184:187], v[200:203]\n"
    "buffer_store_dwordx4 v[248:251], v240, s[28:31], s62 offen offset:0 sc1\n"
    "s_nop 0\n"
    "ds_read_b64_tr_b16 a[236:237], v253 offset:36872\n"
    "ds_read_b64_tr_b16 a[238:239], v253 offset:37384\n"
    "v_mfma_f32_16x16x32_bf16 v[232:235], a[196:199], a[188:191], v[232:235]\n"
    "v_mfma_f32_16x16x32_bf16 v[232:235], a[192:195], a[156:159], v[232:235]\n"
    "v_mfma_f32_16x16x32_bf16 v[236:239], a[200:203], a[156:159], v[236:239]\n"
    "v_mfma_f32_16x16x32_bf16 v[236:239], a[204:207], a[188:191], v[236:239]\n"
    "v_mfma_f32_16x16x32_bf16 v[204:207], a[204:207], a[184:187], v[204:207]\n"
    "v_add_u32 v246, s90, v252\n"
    "ds_read_b64_tr_b16 v[248:249], v246 offset:8\n"
    "v_mfma_f32_16x16x32_bf16 v[204:207], a[200:203], a[152:155], v[204:207]\n"
    "ds_read_b64_tr_b16 v[250:251], v246 offset:520\n"
    "s_waitcnt lgkmcnt(2)\n"
    "v_mfma_f32_16x16x32_bf16 v[172:175], a[200:203], a[148:151], v[172:175]\n"
    "s_add_u32 m0, s79, 32768\n"
    "s_lshl_b32 s54, s64, 17\n"
    "s_xor_b32 s54, s54, s57\n"
    "v_mfma_f32_16x16x32_bf16 v[172:175], a[204:207], a[180:183], v[172:175]\n"
    "s_add_u32 s92, s72, s54\n"
    "s_addc_u32 s93, s73, 0\n"
    "v_mfma_f32_16x16x32_bf16 v[140:143], a[204:207], a[176:179], v[140:143]\n"
    "buffer_load_dwordx4 v254, s[92:95], 0 offen lds\n"
    "v_mfma_f32_16x16x32_bf16 v[140:143], a[200:203], a[144:147], v[140:143]\n"
    "s_addk_i32 m0, 1024\n"
    "s_add_u32 s92, s92, 16384\n"
    "s_addc_u32 s93, s93, 0\n"
    "v_mfma_f32_16x16x32_bf16 v[108:111], a[200:203], a[140:143], v[108:111]\n"
    "buffer_load_dwordx4 v254, s[92:95], 0 offen lds\n"
    "v_mfma_f32_16x16x32_bf16 v[108:111], a[204:207], a[172:175], v[108:111]\n"
    "v_mfma_f32_16x16x32_bf16 v[76:79], a[204:207], a[168:171], v[76:79]\n"
    "v_mfma_f32_16x16x32_bf16 v[76:79], a[200:203], a[136:139], v[76:79]\n"
    "v_mfma_f32_16x16x32_bf16 v[44:47], a[200:203], a[132:135], v[44:47]\n"
    "s_add_u32 s62, s91, 524288\n"
    "s_waitcnt lgkmcnt(0)\n"
    "v_mfma_f32_16x16x32_bf16 v[44:47], a[204:207], a[164:167], v[44:47]\n"
    "buffer_store_dwordx4 v[248:251], v240, s[28:31], s62 offen offset:2048 sc1\n"
    "s_nop 0\n"
    "v_mfma_f32_16x16x32_bf16 v[12:15], a[204:207], a[160:163], v[12:15]\n"
    "v_mfma_f32_16x16x32_bf16 v[12:15], a[200:203], a[128:131], v[12:15]\n"
    "v_mfma_f32_16x16x32_bf16 v[16:19], a[208:211], a[128:131], v[16:19]\n"
    "v_mfma_f32_16x16x32_bf16 v[16:19], a[212:215], a[160:163], v[16:19]\n"
    "v_mfma_f32_16x16x32_bf16 v[48:51], a[212:215], a[164:167], v[48:51]\n"
    "v_add_u32 v246, s90, v253\n"
    "ds_read_b64_tr_b16 v[248:249], v246 offset:0\n"
    "v_mfma_f32_16x16x32_bf16 v[48:51], a[208:211], a[132:135], v[48:51]\n"
    "ds_read_b64_tr_b16 v[250:251], v246 offset:512\n"
    "v_mfma_f32_16x16x32_bf16 v[80:83], a[208:211], a[136:139], v[80:83]\n"
    "v_mfma_f32_16x16x32_bf16 v[80:83], a[212:215], a[168:171], v[80:83]\n"
    "v_mfma_f32_16x16x32_bf16 v[112:115], a[212:215], a[172:175], v[112:115]\n"
    "v_mfma_f32_16x16x32_bf16 v[112:115], a[208:211], a[140:143], v[112:115]\n"
    "v_mfma_f32_16x16x32_bf16 v[144:147], a[208:211], a[144:147], v[144:147]\n"
    "v_mfma_f32_16x16x32_bf16 v[144:147], a[212:215], a[176:179], v[144:147]\n"
    "v_mfma_f32_16x16x32_bf16 v[176:179], a[212:215], a[180:183], v[176:179]\n"
    "v_mfma_f32_16x16x32_bf16 v[176:179], a[208:211], a[148:151], v[176:179]\n"
    "v_mfma_f32_16x16x32_bf16 v[208:211], a[208:211], a[152:155], v[208:211]\n"
    "s_add_u32 s62, s91, 528384\n"
    "s_waitcnt lgkmcnt(0)\n"
    "v_mfma_f32_16x16x32_bf16 v[208:211], a[212:215], a[184:187], v[208:211]\n"
    "buffer_store_dwordx4 v[248:251], v240, s[28:31], s62 offen offset:0 sc1\n"
    "s_nop 0\n"
    "v_mfma_f32_16x16x32_bf16 a[240:243], a[212:215], a[188:191], a[240:243]\n"
    "v_mfma_f32_16x16x32_bf16 a[240:243], a[208:211], a[156:159], a[240:243]\n"
    "v_mfma_f32_16x16x32_bf16 a[244:247], a[216:219], a[156:159], a[244:247]\n"
    "v_mfma_f32_16x16x32_bf16 a[244:247], a[220:223], a[188:191], a[244:247]\n"
    "v_mfma_f32_16x16x32_bf16 v[212:215], a[220:223], a[184:187], v[212:215]\n"
    "v_add_u32 v246, s90, v253\n"
    "ds_read_b64_tr_b16 v[248:249], v246 offset:8\n"
    "v_mfma_f32_16x16x32_bf16 v[212:215], a[216:219], a[152:155], v[212:215]\n"
    "ds_read_b64_tr_b16 v[250:251], v246 offset:520\n"
    "v_mfma_f32_16x16x32_bf16 v[180:183], a[216:219], a[148:151], v[180:183]\n"
    "v_mfma_f32_16x16x32_bf16 v[180:183], a[220:223], a[180:183], v[180:183]\n"
    "v_mfma_f32_16x16x32_bf16 v[148:151], a[220:223], a[176:179], v[148:151]\n"
    "v_mfma_f32_16x16x32_bf16 v[148:151], a[216:219], a[144:147], v[148:151]\n"
    "v_mfma_f32_16x16x32_bf16 v[116:119], a[216:219], a[140:143], v[116:119]\n"
    "v_mfma_f32_16x16x32_bf16 v[116:119], a[220:223], a[172:175], v[116:119]\n"
    "v_mfma_f32_16x16x32_bf16 v[84:87], a[220:223], a[168:171], v[84:87]\n"
    "v_mfma_f32_16x16x32_bf16 v[84:87], a[216:219], a[136:139], v[84:87]\n"
    "v_mfma_f32_16x16x32_bf16 v[52:55], a[216:219], a[132:135], v[52:55]\n"
    "s_add_u32 s62, s91, 528384\n"
    "s_waitcnt lgkmcnt(0)\n"
    "v_mfma_f32_16x16x32_bf16 v[52:55], a[220:223], a[164:167], v[52:55]\n"
    "buffer_store_dwordx4 v[248:251], v240, s[28:31], s62 offen offset:2048 sc1\n"
    "s_nop 0\n"
    "v_mfma_f32_16x16x32_bf16 v[20:23], a[220:223], a[160:163], v[20:23]\n"
    "v_mfma_f32_16x16x32_bf16 v[20:23], a[216:219], a[128:131], v[20:23]\n"
    "v_add_u32 v252, 0x20000, v252\n"
    "v_add_u32 v253, 0x20000, v253\n"
    "v_mfma_f32_16x16x32_bf16 v[24:27], a[224:227], a[128:131], v[24:27]\n"
    "s_waitcnt vmcnt(18)\n"
    "ds_read_b64_tr_b16 a[192:193], v252 offset:0\n"
    "ds_read_b64_tr_b16 a[194:195], v252 offset:512\n"
    "v_mfma_f32_16x16x32_bf16 v[24:27], a[228:231], a[160:163], v[24:27]\n"
    "ds_read_b64_tr_b16 a[196:197], v252 offset:4096\n"
    "ds_read_b64_tr_b16 a[198:199], v252 offset:4608\n"
    "v_mfma_f32_16x16x32_bf16 v[56:59], a[228:231], a[164:167], v[56:59]\n"
    "v_mfma_f32_16x16x32_bf16 v[56:59], a[224:227], a[132:135], v[56:59]\n"
    "ds_read_b64_tr_b16 a[200:201], v252 offset:8\n"
    "ds_read_b64_tr_b16 a[202:203], v252 offset:520\n"
    "v_mfma_f32_16x16x32_bf16 v[88:91], a[224:227], a[136:139], v[88:91]\n"
    "v_mfma_f32_16x16x32_bf16 v[88:91], a[228:231], a[168:171], v[88:91]\n"
    "ds_read_b64_tr_b16 a[204:205], v252 offset:4104\n"
    "ds_read_b64_tr_b16 a[206:207], v252 offset:4616\n"
    "v_mfma_f32_16x16x32_bf16 v[120:123], a[228:231], a[172:175], v[120:123]\n"
    "v_mfma_f32_16x16x32_bf16 v[120:123], a[224:227], a[140:143], v[120:123]\n"
    "ds_read_b64_tr_b16 a[208:209], v253 offset:0\n"
    "ds_read_b64_tr_b16 a[210:211], v253 offset:512\n"
    "v_mfma_f32_16x16x32_bf16 v[152:155], a[224:227], a[144:147], v[152:155]\n"
    "v_mfma_f32_16x16x32_bf16 v[152:155], a[228:231], a[176:179], v[152:155]\n"
    "ds_read_b64_tr_b16 a[212:213], v253 offset:4096\n"
    "ds_read_b64_tr_b16 a[214:215], v253 offset:4608\n"
    "v_mfma_f32_16x16x32_bf16 v[184:187], a[228:231], a[180:183], v[184:187]\n"
    "v_mfma_f32_16x16x32_bf16 v[184:187], a[224:227], a[148:151], v[184:187]\n"
    "ds_read_b64_tr_b16 a[216:217], v253 offset:8\n"
    "ds_read_b64_tr_b16 a[218:219], v253 offset:520\n"
    "v_mfma_f32_16x16x32_bf16 v[216:219], a[224:227], a[152:155], v[216:219]\n"
    "v_mfma_f32_16x16x32_bf16 v[216:219], a[228:231], a[184:187], v[216:219]\n"
    "s_barrier\n"
    "ds_read_b64_tr_b16 a[220:221], v253 offset:4104\n"
    "ds_read_b64_tr_b16 a[222:223], v253 offset:4616\n"
    "v_mfma_f32_16x16x32_bf16 a[248:251], a[228:231], a[188:191], a[248:251]\n"
    "v_mfma_f32_16x16x32_bf16 a[248:251], a[224:227], a[156:159], a[248:251]\n"
    "v_lshrrev_b32 v246, 1, v240\n"
    "s_lshl_b32 s61, s65, 9\n"
    "v_mfma_f32_16x16x32_bf16 a[252:255], a[232:235], a[156:159], a[252:255]\n"
    "s_mov_b32 s82, s87\n"
    "v_mfma_f32_16x16x32_bf16 a[252:255], a[236:239], a[188:191], a[252:255]\n"
    "buffer_load_dwordx2 v[246:247], v246, s[80:83], s61 offen sc1\n"
    "v_mfma_f32_16x16x32_bf16 v[220:223], a[236:239], a[184:187], v[220:223]\n"
    "v_lshrrev_b32 v248, 2, v240\n"
    "s_lshl_b32 s61, s65, 8\n"
    "v_mfma_f32_16x16x32_bf16 v[220:223], a[232:235], a[152:155], v[220:223]\n"
    "s_mov_b32 s82, s87\n"
    "v_mfma_f32_16x16x32_bf16 v[188:191], a[232:235], a[148:151], v[188:191]\n"
    "buffer_load_dword v248, v248, s[80:83], s61 offen offset:2048 sc1\n"
    "v_mfma_f32_16x16x32_bf16 v[188:191], a[236:239], a[180:183], v[188:191]\n"
    "ds_read_b128 a[156:159], v241 offset:30720\n"
    "v_mfma_f32_16x16x32_bf16 v[156:159], a[236:239], a[176:179], v[156:159]\n"
    "ds_read_b128 a[188:191], v242 offset:30720\n"
    "v_mfma_f32_16x16x32_bf16 v[156:159], a[232:235], a[144:147], v[156:159]\n"
    "ds_read_b128 a[184:187], v242 offset:28672\n"
    "v_mfma_f32_16x16x32_bf16 v[124:127], a[232:235], a[140:143], v[124:127]\n"
    "ds_read_b128 a[152:155], v241 offset:28672\n"
    "v_mfma_f32_16x16x32_bf16 v[124:127], a[236:239], a[172:175], v[124:127]\n"
    "ds_read_b128 a[148:151], v241 offset:26624\n"
    "v_mfma_f32_16x16x32_bf16 v[92:95], a[236:239], a[168:171], v[92:95]\n"
    "ds_read_b128 a[180:183], v242 offset:26624\n"
    "v_mfma_f32_16x16x32_bf16 v[92:95], a[232:235], a[136:139], v[92:95]\n"
    "ds_read_b128 a[176:179], v242 offset:24576\n"
    "v_mfma_f32_16x16x32_bf16 v[60:63], a[232:235], a[132:135], v[60:63]\n"
    "ds_read_b128 a[144:147], v241 offset:24576\n"
    "v_mfma_f32_16x16x32_bf16 v[60:63], a[236:239], a[164:167], v[60:63]\n"
    "ds_read_b128 a[140:143], v241 offset:22528\n"
    "s_addk_i32 m0, 1024\n"
    "v_mfma_f32_16x16x32_bf16 v[28:31], a[236:239], a[160:163], v[28:31]\n"
    "s_add_u32 s92, s92, 16384\n"
    "s_addc_u32 s93, s93, 0\n"
    "v_mfma_f32_16x16x32_bf16 v[28:31], a[232:235], a[128:131], v[28:31]\n"
    "s_waitcnt lgkmcnt(7)\n"
    "buffer_load_dwordx4 v254, s[92:95], 0 offen lds\n"
    "ds_read_b128 a[172:175], v242 offset:22528\n"
    "ds_read_b128 a[168:171], v242 offset:20480\n"
    "v_mfma_f32_16x16x32_bf16 v[224:227], a[192:195], a[156:159], v[224:227]\n"
    "ds_read_b128 a[136:139], v241 offset:20480\n"
    "v_mfma_f32_16x16x32_bf16 v[224:227], a[196:199], a[188:191], v[224:227]\n"
    "s_waitcnt lgkmcnt(8)\n"
    "ds_read_b128 a[132:135], v241 offset:18432\n"
    "s_addk_i32 m0, 1024\n"
    "v_mfma_f32_16x16x32_bf16 v[192:195], a[196:199], a[184:187], v[192:195]\n"
    "s_add_u32 s92, s92, 16384\n"
    "s_addc_u32 s93, s93, 0\n"
    "v_mfma_f32_16x16x32_bf16 v[192:195], a[192:195], a[152:155], v[192:195]\n"
    "s_waitcnt lgkmcnt(7)\n"
    "buffer_load_dwordx4 v254, s[92:95], 0 offen lds\n"
    "ds_read_b128 a[164:167], v242 offset:18432\n"
    "ds_read_b128 a[160:163], v242 offset:16384\n"
    "ds_read_b128 a[128:131], v241 offset:16384\n"
    "v_mfma_f32_16x16x32_bf16 v[160:163], a[192:195], a[148:151], v[160:163]\n"
    "s_addk_i32 m0, 1024\n"
    "s_add_u32 s92, s92, 16384\n"
    "s_addc_u32 s93, s93, 0\n"
    "v_mfma_f32_16x16x32_bf16 v[160:163], a[196:199], a[180:183], v[160:163]\n"
    "s_waitcnt lgkmcnt(8)\n"
    "buffer_load_dwordx4 v254, s[92:95], 0 offen lds\n"
    "v_mfma_f32_16x16x32_bf16 v[128:131], a[196:199], a[176:179], v[128:131]\n"
    "v_mfma_f32_16x16x32_bf16 v[128:131], a[192:195], a[144:147], v[128:131]\n"
    "s_waitcnt lgkmcnt(6)\n"
    "s_addk_i32 m0, 1024\n"
    "s_add_u32 s92, s92, 16384\n"
    "v_mfma_f32_16x16x32_bf16 v[96:99], a[192:195], a[140:143], v[96:99]\n"
    "s_addc_u32 s93, s93, 0\n"
    "buffer_load_dwordx4 v254, s[92:95], 0 offen lds\n"
    "v_mfma_f32_16x16x32_bf16 v[96:99], a[196:199], a[172:175], v[96:99]\n"
    "s_waitcnt lgkmcnt(4)\n"
    "v_mfma_f32_16x16x32_bf16 v[64:67], a[196:199], a[168:171], v[64:67]\n"
    "v_mfma_f32_16x16x32_bf16 v[64:67], a[192:195], a[136:139], v[64:67]\n"
    "s_waitcnt lgkmcnt(2)\n"
    "s_addk_i32 m0, 1024\n"
    "s_add_u32 s92, s92, 16384\n"
    "v_mfma_f32_16x16x32_bf16 v[32:35], a[192:195], a[132:135], v[32:35]\n"
    "s_addc_u32 s93, s93, 0\n"
    "buffer_load_dwordx4 v254, s[92:95], 0 offen lds\n"
    "v_mfma_f32_16x16x32_bf16 v[32:35], a[196:199], a[164:167], v[32:35]\n"
    "s_waitcnt lgkmcnt(0)\n"
    "v_mfma_f32_16x16x32_bf16 v[0:3], a[196:199], a[160:163], v[0:3]\n"
    "v_mfma_f32_16x16x32_bf16 v[0:3], a[192:195], a[128:131], v[0:3]\n"
    "s_addk_i32 m0, 1024\n"
    "s_add_u32 s92, s92, 16384\n"
    "s_addc_u32 s93, s93, 0\n"
    "v_mfma_f32_16x16x32_bf16 v[4:7], a[200:203], a[128:131], v[4:7]\n"
    "buffer_load_dwordx4 v254, s[92:95], 0 offen lds\n"
    "v_mfma_f32_16x16x32_bf16 v[4:7], a[204:207], a[160:163], v[4:7]\n"
    "v_mfma_f32_16x16x32_bf16 v[36:39], a[204:207], a[164:167], v[36:39]\n"
    "v_mfma_f32_16x16x32_bf16 v[36:39], a[200:203], a[132:135], v[36:39]\n"
    "s_xor_b32 s59, s66, s67\n"
    "s_addk_i32 s67, 128\n"
    "s_cmpk_eq_u32 s67, 1536\n"
    "v_mfma_f32_16x16x32_bf16 v[68:71], a[200:203], a[136:139], v[68:71]\n"
    "s_cselect_b32 s67, 0, s67\n"
    "s_add_u32 m0, s68, 65536\n"
    "s_add_u32 s96, s24, s59\n"
    "v_mfma_f32_16x16x32_bf16 v[68:71], a[204:207], a[168:171], v[68:71]\n"
    "s_addc_u32 s97, s25, 0\n"
    "buffer_load_dwordx4 v255, s[96:99], 0 offen lds\n"
    "v_mfma_f32_16x16x32_bf16 v[100:103], a[204:207], a[172:175], v[100:103]\n"
    "v_mfma_f32_16x16x32_bf16 v[100:103], a[200:203], a[140:143], v[100:103]\n"
    "s_add_u32 m0, s68, 66560\n"
    "s_add_u32 s96, s96, 16384\n"
    "s_addc_u32 s97, s97, 0\n"
    "v_mfma_f32_16x16x32_bf16 v[132:135], a[200:203], a[144:147], v[132:135]\n"
    "buffer_load_dwordx4 v243, s[96:99], 0 offen lds\n"
    "s_bitcmp1_b32 s85, 7\n"
    "s_cbranch_scc1 CP7_%=\n"
    "CR7_%=:\n"
    "v_mfma_f32_16x16x32_bf16 v[132:135], a[204:207], a[176:179], v[132:135]\n"
    "v_mfma_f32_16x16x32_bf16 v[164:167], a[204:207], a[180:183], v[164:167]\n"
    "v_mfma_f32_16x16x32_bf16 v[164:167], a[200:203], a[148:151], v[164:167]\n"
    "s_add_u32 m0, s68, 67584\n"
    "s_add_u32 s96, s96, 16384\n"
    "s_addc_u32 s97, s97, 0\n"
    "v_mfma_f32_16x16x32_bf16 v[196:199], a[200:203], a[152:155], v[196:199]\n"
    "buffer_load_dwordx4 v255, s[96:99], 0 offen lds\n"
    "v_mfma_f32_16x16x32_bf16 v[196:199], a[204:207], a[184:187], v[196:199]\n"
    "v_mfma_f32_16x16x32_bf16 v[228:231], a[204:207], a[188:191], v[228:231]\n"
    "v_mfma_f32_16x16x32_bf16 v[228:231], a[200:203], a[156:159], v[228:231]\n"
    "v_add_u32 v252, 0xfffe0000, v252\n"
    "v_add_u32 v253, 0xfffe0000, v253\n"
    "v_mfma_f32_16x16x32_bf16 v[232:235], a[208:211], a[156:159], v[232:235]\n"
    "s_waitcnt vmcnt(21)\n"
    "ds_read_b64_tr_b16 a[224:225], v252 offset:0\n"
    "ds_read_b64_tr_b16 a[226:227], v252 offset:512\n"
    "v_mfma_f32_16x16x32_bf16 v[232:235], a[212:215], a[188:191], v[232:235]\n"
    "s_add_u32 m0, s68, 68608\n"
    "s_add_u32 s96, s96, 16384\n"
    "s_addc_u32 s97, s97, 0\n"
    "v_mfma_f32_16x16x32_bf16 v[200:203], a[212:215], a[184:187], v[200:203]\n"
    "buffer_load_dwordx4 v243, s[96:99], 0 offen lds\n"
    "ds_read_b64_tr_b16 a[228:229], v252 offset:4096\n"
    "v_mfma_f32_16x16x32_bf16 v[200:203], a[208:211], a[152:155], v[200:203]\n"
    "ds_read_b64_tr_b16 a[230:231], v252 offset:4608\n"
    "ds_read_b64_tr_b16 a[232:233], v252 offset:8\n"
    "v_mfma_f32_16x16x32_bf16 v[168:171], a[208:211], a[148:151], v[168:171]\n"
    "ds_read_b64_tr_b16 a[234:235], v252 offset:520\n"
    "s_add_u32 m0, s79, 131072\n"
    "v_mfma_f32_16x16x32_bf16 v[168:171], a[212:215], a[180:183], v[168:171]\n"
    "s_lshl_b32 s54, s64, 17\n"
    "s_xor_b32 s54, s54, s57\n"
    "s_addk_i32 s54, 512\n"
    "v_mfma_f32_16x16x32_bf16 v[136:139], a[212:215], a[176:179], v[136:139]\n"
    "s_add_u32 s92, s72, s54\n"
    "s_addc_u32 s93, s73, 0\n"
    "v_mfma_f32_16x16x32_bf16 v[136:139], a[208:211], a[144:147], v[136:139]\n"
    "buffer_load_dwordx4 v254, s[92:95], 0 offen lds\n"
    "ds_read_b64_tr_b16 a[236:237], v252 offset:4104\n"
    "v_mfma_f32_16x16x32_bf16 v[104:107], a[208:211], a[140:143], v[104:107]\n"
    "ds_read_b64_tr_b16 a[238:239], v252 offset:4616\n"
    "ds_read_b64_tr_b16 a[192:193], v253 offset:0\n"
    "v_mfma_f32_16x16x32_bf16 v[104:107], a[212:215], a[172:175], v[104:107]\n"
    "ds_read_b64_tr_b16 a[194:195], v253 offset:512\n"
    "s_addk_i32 m0, 1024\n"
    "v_mfma_f32_16x16x32_bf16 v[72:75], a[212:215], a[168:171], v[72:75]\n"
    "s_add_u32 s92, s92, 16384\n"
    "s_addc_u32 s93, s93, 0\n"
    "v_mfma_f32_16x16x32_bf16 v[72:75], a[208:211], a[136:139], v[72:75]\n"
    "buffer_load_dwordx4 v254, s[92:95], 0 offen lds\n"
    "ds_read_b64_tr_b16 a[196:197], v253 offset:4096\n"
    "v_mfma_f32_16x16x32_bf16 v[40:43], a[208:211], a[132:135], v[40:43]\n"
    "ds_read_b64_tr_b16 a[198:199], v253 offset:4608\n"
    "ds_read_b64_tr_b16 a[200:201], v253 offset:8\n"
    "v_mfma_f32_16x16x32_bf16 v[40:43], a[212:215], a[164:167], v[40:43]\n"
    "ds_read_b64_tr_b16 a[202:203], v253 offset:520\n"
    "s_addk_i32 m0, 1024\n"
    "v_mfma_f32_16x16x32_bf16 v[8:11], a[212:215], a[160:163], v[8:11]\n"
    "s_add_u32 s92, s92, 16384\n"
    "s_addc_u32 s93, s93, 0\n"
    "v_mfma_f32_16x16x32_bf16 v[8:11], a[208:211], a[128:131], v[8:11]\n"
    "buffer_load_dwordx4 v254, s[92:95], 0 offen lds\n"
    "ds_read_b64_tr_b16 a[204:205], v253 offset:4104\n"
    "v_mfma_f32_16x16x32_bf16 v[12:15], a[216:219], a[128:131], v[12:15]\n"
    "ds_read_b64_tr_b16 a[206:207], v253 offset:4616\n"
    "s_addk_i32 m0, 1024\n"
    "v_mfma_f32_16x16x32_bf16 v[12:15], a[220:223], a[160:163], v[12:15]\n"
    "s_add_u32 s92, s92, 16384\n"
    "s_addc_u32 s93, s93, 0\n"
    "v_mfma_f32_16x16x32_bf16 v[44:47], a[220:223], a[164:167], v[44:47]\n"
    "buffer_load_dwordx4 v254, s[92:95], 0 offen lds\n"
    "v_mfma_f32_16x16x32_bf16 v[44:47], a[216:219], a[132:135], v[44:47]\n"
    "s_addk_i32 m0, 1024\n"
    "s_add_u32 s92, s92, 16384\n"
    "s_addc_u32 s93, s93, 0\n"
    "v_mfma_f32_16x16x32_bf16 v[76:79], a[216:219], a[136:139], v[76:79]\n"
    "buffer_load_dwordx4 v254, s[92:95], 0 offen lds\n"
    "v_mfma_f32_16x16x32_bf16 v[76:79], a[220:223], a[168:171], v[76:79]\n"
    "v_mfma_f32_16x16x32_bf16 v[108:111], a[220:223], a[172:175], v[108:111]\n"
    "v_mfma_f32_16x16x32_bf16 v[108:111], a[216:219], a[140:143], v[108:111]\n"
    "s_addk_i32 m0, 1024\n"
    "s_add_u32 s92, s92, 16384\n"
    "s_addc_u32 s93, s93, 0\n"
    "v_mfma_f32_16x16x32_bf16 v[140:143], a[216:219], a[144:147], v[140:143]\n"
    "buffer_load_dwordx4 v254, s[92:95], 0 offen lds\n"
    "v_mfma_f32_16x16x32_bf16 v[140:143], a[220:223], a[176:179], v[140:143]\n"
    "v_mfma_f32_16x16x32_bf16 v[172:175], a[220:223], a[180:183], v[172:175]\n"
    "v_mfma_f32_16x16x32_bf16 v[172:175], a[216:219], a[148:151], v[172:175]\n"
    "s_addk_i32 m0, 1024\n"
    "s_add_u32 s92, s92, 16384\n"
    "s_addc_u32 s93, s93, 0\n"
    "v_mfma_f32_16x16x32_bf16 v[204:207], a[216:219], a[152:155], v[204:207]\n"
    "buffer_load_dwordx4 v254, s[92:95], 0 offen lds\n"
    "v_mfma_f32_16x16x32_bf16 v[204:207], a[220:223], a[184:187], v[204:207]\n"
    "v_mfma_f32_16x16x32_bf16 v[236:239], a[220:223], a[188:191], v[236:239]\n"
    "v_mfma_f32_16x16x32_bf16 v[236:239], a[216:219], a[156:159], v[236:239]\n"
    "s_waitcnt lgkmcnt(12)\n"
    "s_addk_i32 m0, 1024\n"
    "s_add_u32 s92, s92, 16384\n"
    "v_mfma_f32_16x16x32_bf16 a[240:243], a[224:227], a[156:159], a[240:243]\n"
    "s_addc_u32 s93, s93, 0\n"
    "buffer_load_dwordx4 v254, s[92:95], 0 offen lds\n"
    "v_mfma_f32_16x16x32_bf16 a[240:243], a[228:231], a[188:191], a[240:243]\n"
    "s_add_u32 s57, s57, 131072\n"
    "s_cmp_ge_u32 s57, 1572864\n"
    "s_cselect_b32 s57, s70, s57\n"
    "v_mfma_f32_16x16x32_bf16 v[208:211], a[228:231], a[184:187], v[208:211]\n"
    "v_mfma_f32_16x16x32_bf16 v[208:211], a[224:227], a[152:155], v[208:211]\n"
    "s_waitcnt lgkmcnt(0)\n"
    "s_add_u32 m0, s79, 0\n"
    "s_lshl_b32 s54, s64, 17\n"
    "v_mfma_f32_16x16x32_bf16 v[176:179], a[224:227], a[148:151], v[176:179]\n"
    "s_xor_b32 s54, s54, s57\n"
    "s_add_u32 s92, s72, s54\n"
    "s_addc_u32 s93, s73, 0\n"
    "v_mfma_f32_16x16x32_bf16 v[176:179], a[228:231], a[180:183], v[176:179]\n"
    "buffer_load_dwordx4 v254, s[92:95], 0 offen lds\n"
    "v_mfma_f32_16x16x32_bf16 v[144:147], a[228:231], a[176:179], v[144:147]\n"
    "v_mfma_f32_16x16x32_bf16 v[144:147], a[224:227], a[144:147], v[144:147]\n"
    "s_addk_i32 m0, 1024\n"
    "s_add_u32 s92, s92, 16384\n"
    "s_addc_u32 s93, s93, 0\n"
    "v_mfma_f32_16x16x32_bf16 v[112:115], a[224:227], a[140:143], v[112:115]\n"
    "buffer_load_dwordx4 v254, s[92:95], 0 offen lds\n"
    "v_mfma_f32_16x16x32_bf16 v[112:115], a[228:231], a[172:175], v[112:115]\n"
    "v_mfma_f32_16x16x32_bf16 v[80:83], a[228:231], a[168:171], v[80:83]\n"
    "v_mfma_f32_16x16x32_bf16 v[80:83], a[224:227], a[136:139], v[80:83]\n"
    "s_addk_i32 m0, 1024\n"
    "s_add_u32 s92, s92, 16384\n"
    "s_addc_u32 s93, s93, 0\n"
    "v_mfma_f32_16x16x32_bf16 v[48:51], a[224:227], a[132:135], v[48:51]\n"
    "buffer_load_dwordx4 v254, s[92:95], 0 offen lds\n"
    "v_mfma_f32_16x16x32_bf16 v[48:51], a[228:231], a[164:167], v[48:51]\n"
    "v_mfma_f32_16x16x32_bf16 v[16:19], a[228:231], a[160:163], v[16:19]\n"
    "v_mfma_f32_16x16x32_bf16 v[16:19], a[224:227], a[128:131], v[16:19]\n"
    "s_addk_i32 m0, 1024\n"
    "s_add_u32 s92, s92, 16384\n"
    "s_addc_u32 s93, s93, 0\n"
    "v_mfma_f32_16x16x32_bf16 v[20:23], a[232:235], a[128:131], v[20:23]\n"
    "buffer_load_dwordx4 v254, s[92:95], 0 offen lds\n"
    "v_mfma_f32_16x16x32_bf16 v[20:23], a[236:239], a[160:163], v[20:23]\n"
    "v_mfma_f32_16x16x32_bf16 v[52:55], a[236:239], a[164:167], v[52:55]\n"
    "v_mfma_f32_16x16x32_bf16 v[52:55], a[232:235], a[132:135], v[52:55]\n"
    "s_addk_i32 m0, 1024\n"
    "s_add_u32 s92, s92, 16384\n"
    "s_addc_u32 s93, s93, 0\n"
    "v_mfma_f32_16x16x32_bf16 v[84:87], a[232:235], a[136:139], v[84:87]\n"
    "buffer_load_dwordx4 v254, s[92:95], 0 offen lds\n"
    "v_mfma_f32_16x16x32_bf16 v[84:87], a[236:239], a[168:171], v[84:87]\n"
    "v_mfma_f32_16x16x32_bf16 v[116:119], a[236:239], a[172:175], v[116:119]\n"
    "v_mfma_f32_16x16x32_bf16 v[116:119], a[232:235], a[140:143], v[116:119]\n"
    "s_addk_i32 m0, 1024\n"
    "s_add_u32 s92, s92, 16384\n"
    "s_addc_u32 s93, s93, 0\n"
    "v_mfma_f32_16x16x32_bf16 v[148:151], a[232:235], a[144:147], v[148:151]\n"
    "buffer_load_dwordx4 v254, s[92:95], 0 offen lds\n"
    "v_mfma_f32_16x16x32_bf16 v[148:151], a[236:239], a[176:179], v[148:151]\n"
    "v_mfma_f32_16x16x32_bf16 v[180:183], a[236:239], a[180:183], v[180:183]\n"
    "v_mfma_f32_16x16x32_bf16 v[180:183], a[232:235], a[148:151], v[180:183]\n"
    "s_addk_i32 m0, 1024\n"
    "s_add_u32 s92, s92, 16384\n"
    "s_addc_u32 s93, s93, 0\n"
    "v_mfma_f32_16x16x32_bf16 v[212:215], a[232:235], a[152:155], v[212:215]\n"
    "buffer_load_dwordx4 v254, s[92:95], 0 offen lds\n"
    "v_mfma_f32_16x16x32_bf16 v[212:215], a[236:239], a[184:187], v[212:215]\n"
    "v_mfma_f32_16x16x32_bf16 a[244:247], a[236:239], a[188:191], a[244:247]\n"
    "v_mfma_f32_16x16x32_bf16 a[244:247], a[232:235], a[156:159], a[244:247]\n"
    "s_waitcnt vmcnt(19)\n"
    "ds_read_b64_tr_b16 a[208:209], v252 offset:32768\n"
    "ds_read_b64_tr_b16 a[210:211], v252 offset:33280\n"
    "v_mfma_f32_16x16x32_bf16 a[248:251], a[192:195], a[156:159], a[248:251]\n"
    "s_addk_i32 m0, 1024\n"
    "s_add_u32 s92, s92, 16384\n"
    "s_addc_u32 s93, s93, 0\n"
    "v_mfma_f32_16x16x32_bf16 a[248:251], a[196:199], a[188:191], a[248:251]\n"
    "buffer_load_dwordx4 v254, s[92:95], 0 offen lds\n"
    "ds_read_b64_tr_b16 a[212:213], v252 offset:36864\n"
    "v_mfma_f32_16x16x32_bf16 v[216:219], a[196:199], a[184:187], v[216:219]\n"
    "ds_read_b64_tr_b16 a[214:215], v252 offset:37376\n"
    "v_mfma_f32_16x16x32_bf16 v[216:219], a[192:195], a[152:155], v[216:219]\n"
    "ds_read_b64_tr_b16 a[216:217], v252 offset:32776\n"
    "ds_read_b64_tr_b16 a[218:219], v252 offset:33288\n"
    "v_mfma_f32_16x16x32_bf16 v[184:187], a[192:195], a[148:151], v[184:187]\n"
    "v_mfma_f32_16x16x32_bf16 v[184:187], a[196:199], a[180:183], v[184:187]\n"
    "ds_read_b64_tr_b16 a[220:221], v252 offset:36872\n"
    "ds_read_b64_tr_b16 a[222:223], v252 offset:37384\n"
    "v_mfma_f32_16x16x32_bf16 v[152:155], a[196:199], a[176:179], v[152:155]\n"
    "v_mfma_f32_16x16x32_bf16 v[152:155], a[192:195], a[144:147], v[152:155]\n"
    "ds_read_b64_tr_b16 a[224:225], v253 offset:32768\n"
    "ds_read_b64_tr_b16 a[226:227], v253 offset:33280\n"
    "v_mfma_f32_16x16x32_bf16 v[120:123], a[192:195], a[140:143], v[120:123]\n"
    "v_mfma_f32_16x16x32_bf16 v[120:123], a[196:199], a[172:175], v[120:123]\n"
    "ds_read_b64_tr_b16 a[228:229], v253 offset:36864\n"
    "ds_read_b64_tr_b16 a[230:231], v253 offset:37376\n"
    "v_mfma_f32_16x16x32_bf16 v[88:91], a[196:199], a[168:171], v[88:91]\n"
    "v_mfma_f32_16x16x32_bf16 v[88:91], a[192:195], a[136:139], v[88:91]\n"
    "ds_read_b64_tr_b16 a[232:233], v253 offset:32776\n"
    "ds_read_b64_tr_b16 a[234:235], v253 offset:33288\n"
    "v_mfma_f32_16x16x32_bf16 v[56:59], a[192:195], a[132:135], v[56:59]\n"
    "v_mfma_f32_16x16x32_bf16 v[56:59], a[196:199], a[164:167], v[56:59]\n"
    "v_cmp_ne_u32 vcc, v246, v248\n"
    "s_and_b32 s61, s86, 1\n"
    "s_nop 0\n"
    "s_andn2_b32 vcc_lo, vcc_lo, s61\n"
    "s_cmp_eq_u64 vcc, 0\n"
    "s_cbranch_scc1 LA1_%=\n"
    "s_mov_b32 s69, 0x400000\n"
    "LA2_%=:\n"
    "s_sleep 2\n"
    "v_lshrrev_b32 v246, 1, v240\n"
    "s_lshl_b32 s61, s65, 9\n"
    "s_mov_b32 s82, s87\n"
    "buffer_load_dwordx2 v[246:247], v246, s[80:83], s61 offen sc1\n"
    "s_waitcnt vmcnt(0)\n"
    "s_sub_u32 s69, s69, 1\n"
    "s_cbranch_scc1 LA1_%=\n"
    "v_cmp_ne_u32 vcc, v246, v248\n"
    "s_and_b32 s61, s86, 1\n"
    "s_nop 0\n"
    "s_andn2_b32 vcc_lo, vcc_lo, s61\n"
    "s_cmp_eq_u64 vcc, 0\n"
    "s_cbranch_scc0 LA2_%=\n"
    "LA1_%=:\n"
    "s_barrier\n"
    "v_mfma_f32_16x16x32_bf16 v[24:27], a[196:199], a[160:163], v[24:27]\n"
    "ds_read_b64_tr_b16 a[236:237], v253 offset:36872\n"
    "ds_read_b64_tr_b16 a[238:239], v253 offset:37384\n"
    "v_mfma_f32_16x16x32_bf16 v[24:27], a[192:195], a[128:131], v[24:27]\n"
    "v_mfma_f32_16x16x32_bf16 v[28:31], a[200:203], a[128:131], v[28:31]\n"
    "v_mfma_f32_16x16x32_bf16 v[28:31], a[204:207], a[160:163], v[28:31]\n"
    "v_mfma_f32_16x16x32_bf16 v[60:63], a[204:207], a[164:167], v[60:63]\n"
    "ds_read_b128 a[128:131], v241 offset:32768\n"
    "v_mfma_f32_16x16x32_bf16 v[60:63], a[200:203], a[132:135], v[60:63]\n"
    "s_waitcnt lgkmcnt(1)\n"
    "s_add_u32 m0, s79, 32768\n"
    "s_lshl_b32 s54, s64, 17\n"
    "v_mfma_f32_16x16x32_bf16 v[92:95], a[200:203], a[136:139], v[92:95]\n"
    "s_xor_b32 s54, s54, s57\n"
    "s_addk_i32 s54, 512\n"
    "s_add_u32 s92, s72, s54\n"
    "v_mfma_f32_16x16x32_bf16 v[92:95], a[204:207], a[168:171], v[92:95]\n"
    "s_addc_u32 s93, s73, 0\n"
    "buffer_load_dwordx4 v254, s[92:95], 0 offen lds\n"
    "v_mfma_f32_16x16x32_bf16 v[124:127], a[204:207], a[172:175], v[124:127]\n"
    "ds_read_b128 a[160:163], v242 offset:32768\n"
    "v_mfma_f32_16x16x32_bf16 v[124:127], a[200:203], a[140:143], v[124:127]\n"
    "ds_read_b128 a[164:167], v242 offset:34816\n"
    "v_mfma_f32_16x16x32_bf16 v[156:159], a[200:203], a[144:147], v[156:159]\n"
    "ds_read_b128 a[132:135], v241 offset:34816\n"
    "v_mfma_f32_16x16x32_bf16 v[156:159], a[204:207], a[176:179], v[156:159]\n"
    "ds_read_b128 a[136:139], v241 offset:36864\n"
    "s_addk_i32 m0, 1024\n"
    "v_mfma_f32_16x16x32_bf16 v[188:191], a[204:207], a[180:183], v[188:191]\n"
    "s_add_u32 s92, s92, 16384\n"
    "s_addc_u32 s93, s93, 0\n"
    "v_mfma_f32_16x16x32_bf16 v[188:191], a[200:203], a[148:151], v[188:191]\n"
    "buffer_load_dwordx4 v254, s[92:95], 0 offen lds\n"
    "ds_read_b128 a[168:171], v242 offset:36864\n"
    "ds_read_b128 a[172:175], v242 offset:38912\n"
    "v_mfma_f32_16x16x32_bf16 v[220:223], a[200:203], a[152:155], v[220:223]\n"
    "ds_read_b128 a[140:143], v241 offset:38912\n"
    "v_mfma_f32_16x16x32_bf16 v[220:223], a[204:207], a[184:187], v[220:223]\n"
    "ds_read_b128 a[144:147], v241 offset:40960\n"
    "s_addk_i32 m0, 1024\n"
    "v_mfma_f32_16x16x32_bf16 a[252:255], a[204:207], a[188:191], a[252:255]\n"
    "s_add_u32 s92, s92, 16384\n"
    "s_addc_u32 s93, s93, 0\n"
    "v_mfma_f32_16x16x32_bf16 a[252:255], a[200:203], a[156:159], a[252:255]\n"
    "s_waitcnt lgkmcnt(7)\n"
    "buffer_load_dwordx4 v254, s[92:95], 0 offen lds\n"
    "ds_read_b128 a[176:179], v242 offset:40960\n"
    "ds_read_b128 a[180:183], v242 offset:43008\n"
    "v_mfma_f32_16x16x32_bf16 v[0:3], a[208:211], a[128:131], v[0:3]\n"
    "ds_read_b128 a[148:151], v241 offset:43008\n"
    "v_mfma_f32_16x16x32_bf16 v[0:3], a[212:215], a[160:163], v[0:3]\n"
    "s_waitcnt lgkmcnt(8)\n"
    "ds_read_b128 a[152:155], v241 offset:45056\n"
    "s_addk_i32 m0, 1024\n"
    "v_mfma_f32_16x16x32_bf16 v[32:35], a[212:215], a[164:167], v[32:35]\n"
    "s_add_u32 s92, s92, 16384\n"
    "s_addc_u32 s93, s93, 0\n"
    "v_mfma_f32_16x16x32_bf16 v[32:35], a[208:211], a[132:135], v[32:35]\n"
    "s_waitcnt lgkmcnt(7)\n"
    "buffer_load_dwordx4 v254, s[92:95], 0 offen lds\n"
    "ds_read_b128 a[184:187], v242 offset:45056\n"
    "ds_read_b128 a[188:191], v242 offset:47104\n"
    "ds_read_b128 a[156:159], v241 offset:47104\n"
    "v_lshrrev_b32 v246, 2, v240\n"
    "s_lshl_b32 s61, s65, 8\n"
    "s_mov_b32 s82, s87\n"
    "v_mfma_f32_16x16x32_bf16 v[64:67], a[208:211], a[136:139], v[64:67]\n"
    "buffer_store_dword v247, v246, s[80:83], s61 offen offset:3072 sc1\n"
    "v_mfma_f32_16x16x32_bf16 v[64:67], a[212:215], a[168:171], v[64:67]\n"
    "s_waitcnt lgkmcnt(8)\n"
    "s_addk_i32 m0, 1024\n"
    "s_add_u32 s92, s92, 16384\n"
    "v_mfma_f32_16x16x32_bf16 v[96:99], a[212:215], a[172:175], v[96:99]\n"
    "s_addc_u32 s93, s93, 0\n"
    "buffer_load_dwordx4 v254, s[92:95], 0 offen lds\n"
    "v_mfma_f32_16x16x32_bf16 v[96:99], a[208:211], a[140:143], v[96:99]\n"
    "s_waitcnt lgkmcnt(6)\n"
    "s_addk_i32 m0, 1024\n"
    "s_add_u32 s92, s92, 16384\n"
    "v_mfma_f32_16x16x32_bf16 v[128:131], a[208:211], a[144:147], v[128:131]\n"
    "s_addc_u32 s93, s93, 0\n"
    "buffer_load_dwordx4 v254, s[92:95], 0 offen lds\n"
    "v_mfma_f32_16x16x32_bf16 v[128:131], a[212:215], a[176:179], v[128:131]\n"
    "s_waitcnt lgkmcnt(4)\n"
    "v_mfma_f32_16x16x32_bf16 v[160:163], a[212:215], a[180:183], v[160:163]\n"
    "v_mfma_f32_16x16x32_bf16 v[160:163], a[208:211], a[148:151], v[160:163]\n"
    "s_waitcnt lgkmcnt(2)\n"
    "s_addk_i32 m0, 1024\n"
    "s_add_u32 s92, s92, 16384\n"
    "v_mfma_f32_16x16x32_bf16 v[192:195], a[208:211], a[152:155], v[192:195]\n"
    "s_addc_u32 s93, s93, 0\n"
    "buffer_load_dwordx4 v254, s[92:95], 0 offen lds\n"
    "v_mfma_f32_16x16x32_bf16 v[192:195], a[212:215], a[184:187], v[192:195]\n"
    "s_waitcnt lgkmcnt(0)\n"
    "v_mfma_f32_16x16x32_bf16 v[224:227], a[212:215], a[188:191], v[224:227]\n"
    "v_mfma_f32_16x16x32_bf16 v[224:227], a[208:211], a[156:159], v[224:227]\n"
    "s_addk_i32 m0, 1024\n"
    "s_add_u32 s92, s92, 16384\n"
    "s_addc_u32 s93, s93, 0\n"
    "v_mfma_f32_16x16x32_bf16 v[228:231], a[216:219], a[156:159], v[228:231]\n"
    "buffer_load_dwordx4 v254, s[92:95], 0 offen lds\n"
    "s_add_u32 s57, s57, 131072\n"
    "v_mfma_f32_16x16x32_bf16 v[228:231], a[220:223], a[188:191], v[228:231]\n"
    "s_cmp_ge_u32 s57, 1572864\n"
    "s_cselect_b32 s57, s70, s57\n"
    "v_mfma_f32_16x16x32_bf16 v[196:199], a[220:223], a[184:187], v[196:199]\n"
    "v_mfma_f32_16x16x32_bf16 v[196:199], a[216:219], a[152:155], v[196:199]\n"
    "v_mfma_f32_16x16x32_bf16 v[164:167], a[216:219], a[148:151], v[164:167]\n"
    "s_xor_b32 s59, s66, s67\n"
    "s_addk_i32 s67, 128\n"
    "s_cmpk_eq_u32 s67, 1536\n"
    "v_mfma_f32_16x16x32_bf16 v[164:167], a[220:223], a[180:183], v[164:167]\n"
    "s_cselect_b32 s67, 0, s67\n"
    "s_add_u32 m0, s68, 81920\n"
    "s_add_u32 s96, s24, s59\n"
    "v_mfma_f32_16x16x32_bf16 v[132:135], a[220:223], a[176:179], v[132:135]\n"
    "s_addc_u32 s97, s25, 0\n"
    "buffer_load_dwordx4 v255, s[96:99], 0 offen lds\n"
    "v_mfma_f32_16x16x32_bf16 v[132:135], a[216:219], a[144:147], v[132:135]\n"
    "s_add_u32 m0, s68, 82944\n"
    "s_add_u32 s96, s96, 16384\n"
    "s_addc_u32 s97, s97, 0\n"
    "v_mfma_f32_16x16x32_bf16 v[100:103], a[216:219], a[140:143], v[100:103]\n"
    "buffer_load_dwordx4 v243, s[96:99], 0 offen lds\n"
    "s_bitcmp1_b32 s85, 8\n"
    "s_cbranch_scc1 CP8_%=\n"
    "CR8_%=:\n"
    "v_mfma_f32_16x16x32_bf16 v[100:103], a[220:223], a[172:175], v[100:103]\n"
    "v_mfma_f32_16x16x32_bf16 v[68:71], a[220:223], a[168:171], v[68:71]\n"
    "s_add_u32 m0, s68, 83968\n"
    "s_add_u32 s96, s96, 16384\n"
    "s_addc_u32 s97, s97, 0\n"
    "v_mfma_f32_16x16x32_bf16 v[68:71], a[216:219], a[136:139], v[68:71]\n"
    "buffer_load_dwordx4 v255, s[96:99], 0 offen lds\n"
    "v_mfma_f32_16x16x32_bf16 v[36:39], a[216:219], a[132:135], v[36:39]\n"
    "v_mfma_f32_16x16x32_bf16 v[36:39], a[220:223], a[164:167], v[36:39]\n"
    "s_add_u32 m0, s68, 84992\n"
    "s_add_u32 s96, s96, 16384\n"
    "s_addc_u32 s97, s97, 0\n"
    "v_mfma_f32_16x16x32_bf16 v[4:7], a[220:223], a[160:163], v[4:7]\n"
    "buffer_load_dwordx4 v243, s[96:99], 0 offen lds\n"
    "v_mfma_f32_16x16x32_bf16 v[4:7], a[216:219], a[128:131], v[4:7]\n"
    "v_add_u32 v252, 0x20000, v252\n"
    "v_add_u32 v253, 0x20000, v253\n"
    "v_mfma_f32_16x16x32_bf16 v[8:11], a[224:227], a[128:131], v[8:11]\n"
    "s_waitcnt vmcnt(21)\n"
    "ds_read_b64_tr_b16 a[192:193], v252 offset:0\n"
    "ds_read_b64_tr_b16 a[194:195], v252 offset:512\n"
    "v_mfma_f32_16x16x32_bf16 v[8:11], a[228:231], a[160:163], v[8:11]\n"
    "s_add_u32 m0, s68, 114688\n"
    "s_add_u32 s60, s78, 8388608\n"
    "v_mfma_f32_16x16x32_bf16 v[40:43], a[228:231], a[164:167], v[40:43]\n"
    "buffer_load_dwordx4 v254, s[72:75], s60 offen nt lds\n"
    "v_mfma_f32_16x16x32_bf16 v[40:43], a[224:227], a[132:135], v[40:43]\n"
    "ds_read_b64_tr_b16 a[196:197], v252 offset:4096\n"
    "ds_read_b64_tr_b16 a[198:199], v252 offset:4608\n"
    "v_mfma_f32_16x16x32_bf16 v[72:75], a[224:227], a[136:139], v[72:75]\n"
    "ds_read_b64_tr_b16 a[200:201], v252 offset:8\n"
    "ds_read_b64_tr_b16 a[202:203], v252 offset:520\n"
    "v_mfma_f32_16x16x32_bf16 v[72:75], a[228:231], a[168:171], v[72:75]\n"
    "s_add_u32 m0, s68, 115712\n"
    "s_add_u32 s60, s78, 8404992\n"
    "v_mfma_f32_16x16x32_bf16 v[104:107], a[228:231], a[172:175], v[104:107]\n"
    "buffer_load_dwordx4 v254, s[72:75], s60 offen nt lds\n"
    "v_mfma_f32_16x16x32_bf16 v[104:107], a[224:227], a[140:143], v[104:107]\n"
    "ds_read_b64_tr_b16 a[204:205], v252 offset:4104\n"
    "ds_read_b64_tr_b16 a[206:207], v252 offset:4616\n"
    "v_mfma_f32_16x16x32_bf16 v[136:139], a[224:227], a[144:147], v[136:139]\n"
    "s_add_u32 m0, s68, 116736\n"
    "s_add_u32 s60, s78, 8421376\n"
    "v_mfma_f32_16x16x32_bf16 v[136:139], a[228:231], a[176:179], v[136:139]\n"
    "buffer_load_dwordx4 v254, s[72:75], s60 offen nt lds\n"
    "v_mfma_f32_16x16x32_bf16 v[168:171], a[228:231], a[180:183], v[168:171]\n"
    "ds_read_b64_tr_b16 a[208:209], v253 offset:0\n"
    "ds_read_b64_tr_b16 a[210:211], v253 offset:512\n"
    "v_mfma_f32_16x16x32_bf16 v[168:171], a[224:227], a[148:151], v[168:171]\n"
    "ds_read_b64_tr_b16 a[212:213], v253 offset:4096\n"
    "ds_read_b64_tr_b16 a[214:215], v253 offset:4608\n"
    "v_mfma_f32_16x16x32_bf16 v[200:203], a[224:227], a[152:155], v[200:203]\n"
    "s_add_u32 m0, s68, 117760\n"
    "s_add_u32 s60, s78, 8437760\n"
    "v_mfma_f32_16x16x32_bf16 v[200:203], a[228:231], a[184:187], v[200:203]\n"
    "buffer_load_dwordx4 v254, s[72:75], s60 offen nt lds\n"
    "v_mfma_f32_16x16x32_bf16 v[232:235], a[228:231], a[188:191], v[232:235]\n"
    "ds_read_b64_tr_b16 a[216:217], v253 offset:8\n"
    "ds_read_b64_tr_b16 a[218:219], v253 offset:520\n"
    "v_mfma_f32_16x16x32_bf16 v[232:235], a[224:227], a[156:159], v[232:235]\n"
    "ds_read_b64_tr_b16 a[220:221], v253 offset:4104\n"
    "ds_read_b64_tr_b16 a[222:223], v253 offset:4616\n"
    "v_mfma_f32_16x16x32_bf16 v[236:239], a[232:235], a[156:159], v[236:239]\n"
    "v_mfma_f32_16x16x32_bf16 v[236:239], a[236:239], a[188:191], v[236:239]\n"
    "v_mfma_f32_16x16x32_bf16 v[204:207], a[236:239], a[184:187], v[204:207]\n"
    "v_mfma_f32_16x16x32_bf16 v[204:207], a[232:235], a[152:155], v[204:207]\n"
    "v_mfma_f32_16x16x32_bf16 v[172:175], a[232:235], a[148:151], v[172:175]\n"
    "v_mfma_f32_16x16x32_bf16 v[172:175], a[236:239], a[180:183], v[172:175]\n"
    "v_mfma_f32_16x16x32_bf16 v[140:143], a[236:239], a[176:179], v[140:143]\n"
    "v_mfma_f32_16x16x32_bf16 v[140:143], a[232:235], a[144:147], v[140:143]\n"
    "v_mfma_f32_16x16x32_bf16 v[108:111], a[232:235], a[140:143], v[108:111]\n"
    "v_mfma_f32_16x16x32_bf16 v[108:111], a[236:239], a[172:175], v[108:111]\n"
    "v_mfma_f32_16x16x32_bf16 v[76:79], a[236:239], a[168:171], v[76:79]\n"
    "v_mfma_f32_16x16x32_bf16 v[76:79], a[232:235], a[136:139], v[76:79]\n"
    "v_mfma_f32_16x16x32_bf16 v[44:47], a[232:235], a[132:135], v[44:47]\n"
    "v_mfma_f32_16x16x32_bf16 v[44:47], a[236:239], a[164:167], v[44:47]\n"
    "v_mfma_f32_16x16x32_bf16 v[12:15], a[236:239], a[160:163], v[12:15]\n"
    "v_mfma_f32_16x16x32_bf16 v[12:15], a[232:235], a[128:131], v[12:15]\n"
    "s_waitcnt lgkmcnt(12)\n"
    "v_mfma_f32_16x16x32_bf16 v[16:19], a[192:195], a[128:131], v[16:19]\n"
    "v_mfma_f32_16x16x32_bf16 v[16:19], a[196:199], a[160:163], v[16:19]\n"
    "v_mfma_f32_16x16x32_bf16 v[48:51], a[196:199], a[164:167], v[48:51]\n"
    "v_mfma_f32_16x16x32_bf16 v[48:51], a[192:195], a[132:135], v[48:51]\n"
    "v_mfma_f32_16x16x32_bf16 v[80:83], a[192:195], a[136:139], v[80:83]\n"
    "v_mfma_f32_16x16x32_bf16 v[80:83], a[196:199], a[168:171], v[80:83]\n"
    "v_mfma_f32_16x16x32_bf16 v[112:115], a[196:199], a[172:175], v[112:115]\n"
    "v_mfma_f32_16x16x32_bf16 v[112:115], a[192:195], a[140:143], v[112:115]\n"
    "v_mfma_f32_16x16x32_bf16 v[144:147], a[192:195], a[144:147], v[144:147]\n"
    "v_mfma_f32_16x16x32_bf16 v[144:147], a[196:199], a[176:179], v[144:147]\n"
    "v_mfma_f32_16x16x32_bf16 v[176:179], a[196:199], a[180:183], v[176:179]\n"
    "v_mfma_f32_16x16x32_bf16 v[176:179], a[192:195], a[148:151], v[176:179]\n"
    "v_mfma_f32_16x16x32_bf16 v[208:211], a[192:195], a[152:155], v[208:211]\n"
    "v_mfma_f32_16x16x32_bf16 v[208:211], a[196:199], a[184:187], v[208:211]\n"
    "v_mfma_f32_16x16x32_bf16 a[240:243], a[196:199], a[188:191], a[240:243]\n"
    "v_mfma_f32_16x16x32_bf16 a[240:243], a[192:195], a[156:159], a[240:243]\n"
    "s_waitcnt lgkmcnt(8)\n"
    "v_mfma_f32_16x16x32_bf16 a[244:247], a[200:203], a[156:159], a[244:247]\n"
    "v_mfma_f32_16x16x32_bf16 a[244:247], a[204:207], a[188:191], a[244:247]\n"
    "v_mfma_f32_16x16x32_bf16 v[212:215], a[204:207], a[184:187], v[212:215]\n"
    "v_mfma_f32_16x16x32_bf16 v[212:215], a[200:203], a[152:155], v[212:215]\n"
    "v_mfma_f32_16x16x32_bf16 v[180:183], a[200:203], a[148:151], v[180:183]\n"
    "v_mfma_f32_16x16x32_bf16 v[180:183], a[204:207], a[180:183], v[180:183]\n"
    "v_mfma_f32_16x16x32_bf16 v[148:151], a[204:207], a[176:179], v[148:151]\n"
    "v_mfma_f32_16x16x32_bf16 v[148:151], a[200:203], a[144:147], v[148:151]\n"
    "v_mfma_f32_16x16x32_bf16 v[116:119], a[200:203], a[140:143], v[116:119]\n"
    "v_mfma_f32_16x16x32_bf16 v[116:119], a[204:207], a[172:175], v[116:119]\n"
    "v_mfma_f32_16x16x32_bf16 v[84:87], a[204:207], a[168:171], v[84:87]\n"
    "v_mfma_f32_16x16x32_bf16 v[84:87], a[200:203], a[136:139], v[84:87]\n"
    "v_mfma_f32_16x16x32_bf16 v[52:55], a[200:203], a[132:135], v[52:55]\n"
    "v_mfma_f32_16x16x32_bf16 v[52:55], a[204:207], a[164:167], v[52:55]\n"
    "v_mfma_f32_16x16x32_bf16 v[20:23], a[204:207], a[160:163], v[20:23]\n"
    "v_mfma_f32_16x16x32_bf16 v[20:23], a[200:203], a[128:131], v[20:23]\n"
    "s_waitcnt lgkmcnt(4)\n"
    "v_add_u32 v252, 0xfffe0000, v252\n"
    "v_add_u32 v253, 0xfffe0000, v253\n"
    "v_mfma_f32_16x16x32_bf16 v[24:27], a[208:211], a[128:131], v[24:27]\n"
    "s_waitcnt vmcnt(17)\n"
    "ds_read_b64_tr_b16 a[224:225], v252 offset:0\n"
    "ds_read_b64_tr_b16 a[226:227], v252 offset:512\n"
    "v_mfma_f32_16x16x32_bf16 v[24:27], a[212:215], a[160:163], v[24:27]\n"
    "ds_read_b64_tr_b16 a[228:229], v252 offset:4096\n"
    "ds_read_b64_tr_b16 a[230:231], v252 offset:4608\n"
    "v_mfma_f32_16x16x32_bf16 v[56:59], a[212:215], a[164:167], v[56:59]\n"
    "v_mfma_f32_16x16x32_bf16 v[56:59], a[208:211], a[132:135], v[56:59]\n"
    "ds_read_b64_tr_b16 a[232:233], v252 offset:8\n"
    "ds_read_b64_tr_b16 a[234:235], v252 offset:520\n"
    "v_mfma_f32_16x16x32_bf16 v[88:91], a[208:211], a[136:139], v[88:91]\n"
    "v_mfma_f32_16x16x32_bf16 v[88:91], a[212:215], a[168:171], v[88:91]\n"
    "ds_read_b64_tr_b16 a[236:237], v252 offset:4104\n"
    "ds_read_b64_tr_b16 a[238:239], v252 offset:4616\n"
    "v_mfma_f32_16x16x32_bf16 v[120:123], a[212:215], a[172:175], v[120:123]\n"
    "v_mfma_f32_16x16x32_bf16 v[120:123], a[208:211], a[140:143], v[120:123]\n"
    "ds_read_b64_tr_b16 a[192:193], v253 offset:0\n"
    "ds_read_b64_tr_b16 a[194:195], v253 offset:512\n"
    "v_mfma_f32_16x16x32_bf16 v[152:155], a[208:211], a[144:147], v[152:155]\n"
    "v_mfma_f32_16x16x32_bf16 v[152:155], a[212:215], a[176:179], v[152:155]\n"
    "ds_read_b64_tr_b16 a[196:197], v253 offset:4096\n"
    "ds_read_b64_tr_b16 a[198:199], v253 offset:4608\n"
    "v_mfma_f32_16x16x32_bf16 v[184:187], a[212:215], a[180:183], v[184:187]\n"
    "v_mfma_f32_16x16x32_bf16 v[184:187], a[208:211], a[148:151], v[184:187]\n"
    "ds_read_b64_tr_b16 a[200:201], v253 offset:8\n"
    "ds_read_b64_tr_b16 a[202:203], v253 offset:520\n"
    "v_mfma_f32_16x16x32_bf16 v[216:219], a[208:211], a[152:155], v[216:219]\n"
    "v_mfma_f32_16x16x32_bf16 v[216:219], a[212:215], a[184:187], v[216:219]\n"
    "s_barrier\n"
    "ds_read_b64_tr_b16 a[204:205], v253 offset:4104\n"
    "ds_read_b64_tr_b16 a[206:207], v253 offset:4616\n"
    "v_mfma_f32_16x16x32_bf16 a[248:251], a[212:215], a[188:191], a[248:251]\n"
    "v_mfma_f32_16x16x32_bf16 a[248:251], a[208:211], a[156:159], a[248:251]\n"
    "s_waitcnt lgkmcnt(15)\n"
    "v_mfma_f32_16x16x32_bf16 a[252:255], a[216:219], a[156:159], a[252:255]\n"
    "v_mfma_f32_16x16x32_bf16 a[252:255], a[220:223], a[188:191], a[252:255]\n"
    "v_mfma_f32_16x16x32_bf16 v[220:223], a[220:223], a[184:187], v[220:223]\n"
    "ds_read_b128 a[156:159], v241 offset:14336\n"
    "v_mfma_f32_16x16x32_bf16 v[220:223], a[216:219], a[152:155], v[220:223]\n"
    "ds_read_b128 a[188:191], v242 offset:14336\n"
    "v_mfma_f32_16x16x32_bf16 v[188:191], a[216:219], a[148:151], v[188:191]\n"
    "ds_read_b128 a[184:187], v242 offset:12288\n"
    "v_mfma_f32_16x16x32_bf16 v[188:191], a[220:223], a[180:183], v[188:191]\n"
    "ds_read_b128 a[152:155], v241 offset:12288\n"
    "v_mfma_f32_16x16x32_bf16 v[156:159], a[220:223], a[176:179], v[156:159]\n"
    "ds_read_b128 a[148:151], v241 offset:10240\n"
    "v_mfma_f32_16x16x32_bf16 v[156:159], a[216:219], a[144:147], v[156:159]\n"
    "ds_read_b128 a[180:183], v242 offset:10240\n"
    "v_mfma_f32_16x16x32_bf16 v[124:127], a[216:219], a[140:143], v[124:127]\n"
    "ds_read_b128 a[176:179], v242 offset:8192\n"
    "v_mfma_f32_16x16x32_bf16 v[124:127], a[220:223], a[172:175], v[124:127]\n"
    "ds_read_b128 a[144:147], v241 offset:8192\n"
    "v_mfma_f32_16x16x32_bf16 v[92:95], a[220:223], a[168:171], v[92:95]\n"
    "ds_read_b128 a[140:143], v241 offset:6144\n"
    "v_mfma_f32_16x16x32_bf16 v[92:95], a[216:219], a[136:139], v[92:95]\n"
    "s_add_u32 m0, s79, 131072\n"
    "s_lshl_b32 s54, s64, 17\n"
    "s_xor_b32 s54, s54, s57\n"
    "v_mfma_f32_16x16x32_bf16 v[60:63], a[216:219], a[132:135], v[60:63]\n"
    "s_add_u32 s92, s72, s54\n"
    "s_addc_u32 s93, s73, 0\n"
    "v_mfma_f32_16x16x32_bf16 v[60:63], a[220:223], a[164:167], v[60:63]\n"
    "buffer_load_dwordx4 v254, s[92:95], 0 offen lds\n"
    "ds_read_b128 a[172:175], v242 offset:6144\n"
    "v_mfma_f32_16x16x32_bf16 v[28:31], a[220:223], a[160:163], v[28:31]\n"
    "ds_read_b128 a[168:171], v242 offset:4096\n"
    "v_mfma_f32_16x16x32_bf16 v[28:31], a[216:219], a[128:131], v[28:31]\n"
    "s_waitcnt lgkmcnt(9)\n"
    "ds_read_b128 a[136:139], v241 offset:4096\n"
    "v_mfma_f32_16x16x32_bf16 v[224:227], a[224:227], a[156:159], v[224:227]\n"
    "ds_read_b128 a[132:135], v241 offset:2048\n"
    "s_addk_i32 m0, 1024\n"
    "v_mfma_f32_16x16x32_bf16 v[224:227], a[228:231], a[188:191], v[224:227]\n"
    "s_waitcnt lgkmcnt(9)\n"
    "s_add_u32 s92, s92, 16384\n"
    "s_addc_u32 s93, s93, 0\n"
    "v_mfma_f32_16x16x32_bf16 v[192:195], a[228:231], a[184:187], v[192:195]\n"
    "buffer_load_dwordx4 v254, s[92:95], 0 offen lds\n"
    "ds_read_b128 a[164:167], v242 offset:2048\n"
    "ds_read_b128 a[160:163], v242 offset:0\n"
    "ds_read_b128 a[128:131], v241 offset:0\n"
    "v_mfma_f32_16x16x32_bf16 v[192:195], a[224:227], a[152:155], v[192:195]\n"
    "s_waitcnt lgkmcnt(10)\n"
    "s_addk_i32 m0, 1024\n"
    "s_add_u32 s92, s92, 16384\n"
    "v_mfma_f32_16x16x32_bf16 v[160:163], a[224:227], a[148:151], v[160:163]\n"
    "s_addc_u32 s93, s93, 0\n"
    "buffer_load_dwordx4 v254, s[92:95], 0 offen lds\n"
    "v_mfma_f32_16x16x32_bf16 v[160:163], a[228:231], a[180:183], v[160:163]\n"
    "s_waitcnt lgkmcnt(8)\n"
    "v_mfma_f32_16x16x32_bf16 v[128:131], a[228:231], a[176:179], v[128:131]\n"
    "v_mfma_f32_16x16x32_bf16 v[128:131], a[224:227], a[144:147], v[128:131]\n"
    "s_waitcnt lgkmcnt(6)\n"
    "s_addk_i32 m0, 1024\n"
    "s_add_u32 s92, s92, 16384\n"
    "v_mfma_f32_16x16x32_bf16 v[96:99], a[224:227], a[140:143], v[96:99]\n"
    "s_addc_u32 s93, s93, 0\n"
    "buffer_load_dwordx4 v254, s[92:95], 0 offen lds\n"
    "v_mfma_f32_16x16x32_bf16 v[96:99], a[228:231], a[172:175], v[96:99]\n"
    "s_waitcnt lgkmcnt(4)\n"
    "v_mfma_f32_16x16x32_bf16 v[64:67], a[228:231], a[168:171], v[64:67]\n"
    "v_mfma_f32_16x16x32_bf16 v[64:67], a[224:227], a[136:139], v[64:67]\n"
    "s_waitcnt lgkmcnt(2)\n"
    "s_addk_i32 m0, 1024\n"
    "s_add_u32 s92, s92, 16384\n"
    "v_mfma_f32_16x16x32_bf16 v[32:35], a[224:227], a[132:135], v[32:35]\n"
    "s_addc_u32 s93, s93, 0\n"
    "buffer_load_dwordx4 v254, s[92:95], 0 offen lds\n"
    "v_mfma_f32_16x16x32_bf16 v[32:35], a[228:231], a[164:167], v[32:35]\n"
    "s_waitcnt lgkmcnt(0)\n"
    "v_mfma_f32_16x16x32_bf16 v[0:3], a[228:231], a[160:163], v[0:3]\n"
    "v_mfma_f32_16x16x32_bf16 v[0:3], a[224:227], a[128:131], v[0:3]\n"
    "s_addk_i32 m0, 1024\n"
    "s_add_u32 s92, s92, 16384\n"
    "s_addc_u32 s93, s93, 0\n"
    "v_mfma_f32_16x16x32_bf16 v[4:7], a[232:235], a[128:131], v[4:7]\n"
    "buffer_load_dwordx4 v254, s[92:95], 0 offen lds\n"
    "v_mfma_f32_16x16x32_bf16 v[4:7], a[236:239], a[160:163], v[4:7]\n"
    "v_mfma_f32_16x16x32_bf16 v[36:39], a[236:239], a[164:167], v[36:39]\n"
    "v_mfma_f32_16x16x32_bf16 v[36:39], a[232:235], a[132:135], v[36:39]\n"
    "s_addk_i32 m0, 1024\n"
    "s_add_u32 s92, s92, 16384\n"
    "s_addc_u32 s93, s93, 0\n"
    "v_mfma_f32_16x16x32_bf16 v[68:71], a[232:235], a[136:139], v[68:71]\n"
    "buffer_load_dwordx4 v254, s[92:95], 0 offen lds\n"
    "v_mfma_f32_16x16x32_bf16 v[68:71], a[236:239], a[168:171], v[68:71]\n"
    "v_mfma_f32_16x16x32_bf16 v[100:103], a[236:239], a[172:175], v[100:103]\n"
    "v_mfma_f32_16x16x32_bf16 v[100:103], a[232:235], a[140:143], v[100:103]\n"
    "s_addk_i32 m0, 1024\n"
    "s_add_u32 s92, s92, 16384\n"
    "s_addc_u32 s93, s93, 0\n"
    "v_mfma_f32_16x16x32_bf16 v[132:135], a[232:235], a[144:147], v[132:135]\n"
    "buffer_load_dwordx4 v254, s[92:95], 0 offen lds\n"
    "s_bitcmp1_b32 s85, 9\n"
    "s_cbranch_scc1 CP9_%=\n"
    "CR9_%=:\n"
    "v_mfma_f32_16x16x32_bf16 v[132:135], a[236:239], a[176:179], v[132:135]\n"
    "v_mfma_f32_16x16x32_bf16 v[164:167], a[236:239], a[180:183], v[164:167]\n"
    "v_mfma_f32_16x16x32_bf16 v[164:167], a[232:235], a[148:151], v[164:167]\n"
    "s_xor_b32 s59, s66, s67\n"
    "s_addk_i32 s67, 128\n"
    "s_cmpk_eq_u32 s67, 1536\n"
    "v_mfma_f32_16x16x32_bf16 v[196:199], a[232:235], a[152:155], v[196:199]\n"
    "s_cselect_b32 s67, 0, s67\n"
    "s_add_u32 m0, s68, 98304\n"
    "s_add_u32 s96, s24, s59\n"
    "v_mfma_f32_16x16x32_bf16 v[196:199], a[236:239], a[184:187], v[196:199]\n"
    "s_addc_u32 s97, s25, 0\n"
    "buffer_load_dwordx4 v255, s[96:99], 0 offen lds\n"
    "v_mfma_f32_16x16x32_bf16 v[228:231], a[236:239], a[188:191], v[228:231]\n"
    "v_mfma_f32_16x16x32_bf16 v[228:231], a[232:235], a[156:159], v[228:231]\n"
    "s_waitcnt vmcnt(17)\n"
    "ds_read_b64_tr_b16 a[208:209], v252 offset:32768\n"
    "ds_read_b64_tr_b16 a[210:211], v252 offset:33280\n"
    "v_mfma_f32_16x16x32_bf16 v[232:235], a[192:195], a[156:159], v[232:235]\n"
    "s_add_u32 m0, s68, 99328\n"
    "s_add_u32 s96, s96, 16384\n"
    "s_addc_u32 s97, s97, 0\n"
    "v_mfma_f32_16x16x32_bf16 v[232:235], a[196:199], a[188:191], v[232:235]\n"
    "buffer_load_dwordx4 v243, s[96:99], 0 offen lds\n"
    "ds_read_b64_tr_b16 a[212:213], v252 offset:36864\n"
    "v_mfma_f32_16x16x32_bf16 v[200:203], a[196:199], a[184:187], v[200:203]\n"
    "ds_read_b64_tr_b16 a[214:215], v252 offset:37376\n"
    "v_mfma_f32_16x16x32_bf16 v[200:203], a[192:195], a[152:155], v[200:203]\n"
    "ds_read_b64_tr_b16 a[216:217], v252 offset:32776\n"
    "ds_read_b64_tr_b16 a[218:219], v252 offset:33288\n"
    "v_mfma_f32_16x16x32_bf16 v[168:171], a[192:195], a[148:151], v[168:171]\n"
    "s_add_u32 m0, s68, 100352\n"
    "s_add_u32 s96, s96, 16384\n"
    "s_addc_u32 s97, s97, 0\n"
    "v_mfma_f32_16x16x32_bf16 v[168:171], a[196:199], a[180:183], v[168:171]\n"
    "buffer_load_dwordx4 v255, s[96:99], 0 offen lds\n"
    "ds_read_b64_tr_b16 a[220:221], v252 offset:36872\n"
    "v_mfma_f32_16x16x32_bf16 v[136:139], a[196:199], a[176:179], v[136:139]\n"
    "ds_read_b64_tr_b16 a[222:223], v252 offset:37384\n"
    "v_mfma_f32_16x16x32_bf16 v[136:139], a[192:195], a[144:147], v[136:139]\n"
    "ds_read_b64_tr_b16 a[224:225], v253 offset:32768\n"
    "ds_read_b64_tr_b16 a[226:227], v253 offset:33280\n"
    "v_mfma_f32_16x16x32_bf16 v[104:107], a[192:195], a[140:143], v[104:107]\n"
    "s_add_u32 m0, s68, 101376\n"
    "s_add_u32 s96, s96, 16384\n"
    "s_addc_u32 s97, s97, 0\n"
    "v_mfma_f32_16x16x32_bf16 v[104:107], a[196:199], a[172:175], v[104:107]\n"
    "buffer_load_dwordx4 v243, s[96:99], 0 offen lds\n"
    "ds_read_b64_tr_b16 a[228:229], v253 offset:36864\n"
    "v_mfma_f32_16x16x32_bf16 v[72:75], a[196:199], a[168:171], v[72:75]\n"
    "ds_read_b64_tr_b16 a[230:231], v253 offset:37376\n"
    "v_mfma_f32_16x16x32_bf16 v[72:75], a[192:195], a[136:139], v[72:75]\n"
    "ds_read_b64_tr_b16 a[232:233], v253 offset:32776\n"
    "ds_read_b64_tr_b16 a[234:235], v253 offset:33288\n"
    "v_mfma_f32_16x16x32_bf16 v[40:43], a[192:195], a[132:135], v[40:43]\n"
    "s_add_u32 m0, s79, 0\n"
    "s_lshl_b32 s54, s64, 17\n"
    "s_xor_b32 s54, s54, s57\n"
    "v_mfma_f32_16x16x32_bf16 v[40:43], a[196:199], a[164:167], v[40:43]\n"
    "s_addk_i32 s54, 512\n"
    "s_add_u32 s92, s72, s54\n"
    "s_addc_u32 s93, s73, 0\n"
    "v_mfma_f32_16x16x32_bf16 v[8:11], a[196:199], a[160:163], v[8:11]\n"
    "buffer_load_dwordx4 v254, s[92:95], 0 offen lds\n"
    "ds_read_b64_tr_b16 a[236:237], v253 offset:36872\n"
    "v_mfma_f32_16x16x32_bf16 v[8:11], a[192:195], a[128:131], v[8:11]\n"
    "ds_read_b64_tr_b16 a[238:239], v253 offset:37384\n"
    "s_addk_i32 m0, 1024\n"
    "v_mfma_f32_16x16x32_bf16 v[12:15], a[200:203], a[128:131], v[12:15]\n"
    "s_add_u32 s92, s92, 16384\n"
    "s_addc_u32 s93, s93, 0\n"
    "v_mfma_f32_16x16x32_bf16 v[12:15], a[204:207], a[160:163], v[12:15]\n"
    "buffer_load_dwordx4 v254, s[92:95], 0 offen lds\n"
    "v_mfma_f32_16x16x32_bf16 v[44:47], a[204:207], a[164:167], v[44:47]\n"
    "v_mfma_f32_16x16x32_bf16 v[44:47], a[200:203], a[132:135], v[44:47]\n"
    "s_addk_i32 m0, 1024\n"
    "s_add_u32 s92, s92, 16384\n"
    "s_addc_u32 s93, s93, 0\n"
    "v_mfma_f32_16x16x32_bf16 v[76:79], a[200:203], a[136:139], v[76:79]\n"
    "buffer_load_dwordx4 v254, s[92:95], 0 offen lds\n"
    "v_mfma_f32_16x16x32_bf16 v[76:79], a[204:207], a[168:171], v[76:79]\n"
    "v_mfma_f32_16x16x32_bf16 v[108:111], a[204:207], a[172:175], v[108:111]\n"
    "v_mfma_f32_16x16x32_bf16 v[108:111], a[200:203], a[140:143], v[108:111]\n"
    "s_addk_i32 m0, 1024\n"
    "s_add_u32 s92, s92, 16384\n"
    "s_addc_u32 s93, s93, 0\n"
    "v_mfma_f32_16x16x32_bf16 v[140:143], a[200:203], a[144:147], v[140:143]\n"
    "buffer_load_dwordx4 v254, s[92:95], 0 offen lds\n"
    "v_mfma_f32_16x16x32_bf16 v[140:143], a[204:207], a[176:179], v[140:143]\n"
    "v_mfma_f32_16x16x32_bf16 v[172:175], a[204:207], a[180:183], v[172:175]\n"
    "v_mfma_f32_16x16x32_bf16 v[172:175], a[200:203], a[148:151], v[172:175]\n"
    "s_addk_i32 m0, 1024\n"
    "s_add_u32 s92, s92, 16384\n"
    "s_addc_u32 s93, s93, 0\n"
    "v_mfma_f32_16x16x32_bf16 v[204:207], a[200:203], a[152:155], v[204:207]\n"
    "buffer_load_dwordx4 v254, s[92:95], 0 offen lds\n"
    "v_mfma_f32_16x16x32_bf16 v[204:207], a[204:207], a[184:187], v[204:207]\n"
    "v_mfma_f32_16x16x32_bf16 v[236:239], a[204:207], a[188:191], v[236:239]\n"
    "v_mfma_f32_16x16x32_bf16 v[236:239], a[200:203], a[156:159], v[236:239]\n"
    "s_waitcnt lgkmcnt(12)\n"
    "s_addk_i32 m0, 1024\n"
    "s_add_u32 s92, s92, 16384\n"
    "v_mfma_f32_16x16x32_bf16 a[240:243], a[208:211], a[156:159], a[240:243]\n"
    "s_addc_u32 s93, s93, 0\n"
    "buffer_load_dwordx4 v254, s[92:95], 0 offen lds\n"
    "v_mfma_f32_16x16x32_bf16 a[240:243], a[212:215], a[188:191], a[240:243]\n"
    "v_mfma_f32_16x16x32_bf16 v[208:211], a[212:215], a[184:187], v[208:211]\n"
    "v_add_u32 v246, s90, v252\n"
    "s_waitcnt vmcnt(18)\n"
    "v_mfma_f32_16x16x32_bf16 v[208:211], a[208:211], a[152:155], v[208:211]\n"
    "ds_read_b64_tr_b16 v[248:249], v246 offset:0\n"
    "ds_read_b64_tr_b16 v[250:251], v246 offset:512\n"
    "v_mfma_f32_16x16x32_bf16 v[176:179], a[208:211], a[148:151], v[176:179]\n"
    "s_addk_i32 m0, 1024\n"
    "s_add_u32 s92, s92, 16384\n"
    "s_addc_u32 s93, s93, 0\n"
    "v_mfma_f32_16x16x32_bf16 v[176:179], a[212:215], a[180:183], v[176:179]\n"
    "buffer_load_dwordx4 v254, s[92:95], 0 offen lds\n"
    "v_mfma_f32_16x16x32_bf16 v[144:147], a[212:215], a[176:179], v[144:147]\n"
    "v_mfma_f32_16x16x32_bf16 v[144:147], a[208:211], a[144:147], v[144:147]\n"
    "s_addk_i32 m0, 1024\n"
    "s_add_u32 s92, s92, 16384\n"
    "s_addc_u32 s93, s93, 0\n"
    "v_mfma_f32_16x16x32_bf16 v[112:115], a[208:211], a[140:143], v[112:115]\n"
    "buffer_load_dwordx4 v254, s[92:95], 0 offen lds\n"
    "s_add_u32 s57, s57, 131072\n"
    "v_mfma_f32_16x16x32_bf16 v[112:115], a[212:215], a[172:175], v[112:115]\n"
    "s_cmp_ge_u32 s57, 1572864\n"
    "s_cselect_b32 s57, s70, s57\n"
    "v_mfma_f32_16x16x32_bf16 v[80:83], a[212:215], a[168:171], v[80:83]\n"
    "v_mfma_f32_16x16x32_bf16 v[80:83], a[208:211], a[136:139], v[80:83]\n"
    "s_waitcnt lgkmcnt(2)\n"
    "s_add_u32 m0, s79, 32768\n"
    "s_lshl_b32 s54, s64, 17\n"
    "v_mfma_f32_16x16x32_bf16 v[48:51], a[208:211], a[132:135], v[48:51]\n"
    "s_xor_b32 s54, s54, s57\n"
    "s_add_u32 s92, s72, s54\n"
    "s_addc_u32 s93, s73, 0\n"
    "v_mfma_f32_16x16x32_bf16 v[48:51], a[212:215], a[164:167], v[48:51]\n"
    "buffer_load_dwordx4 v254, s[92:95], 0 offen lds\n"
    "s_add_u32 s62, s91, 1048576\n"
    "v_mfma_f32_16x16x32_bf16 v[16:19], a[212:215], a[160:163], v[16:19]\n"
    "s_waitcnt lgkmcnt(0)\n"
    "v_mfma_f32_16x16x32_bf16 v[16:19], a[208:211], a[128:131], v[16:19]\n"
    "buffer_store_dwordx4 v[248:251], v240, s[28:31], s62 offen offset:0 sc1\n"
    "s_nop 0\n"
    "s_addk_i32 m0, 1024\n"
    "s_add_u32 s92, s92, 16384\n"
    "v_mfma_f32_16x16x32_bf16 v[20:23], a[216:219], a[128:131], v[20:23]\n"
    "s_addc_u32 s93, s93, 0\n"
    "buffer_load_dwordx4 v254, s[92:95], 0 offen lds\n"
    "v_mfma_f32_16x16x32_bf16 v[20:23], a[220:223], a[160:163], v[20:23]\n"
    "v_mfma_f32_16x16x32_bf16 v[52:55], a[220:223], a[164:167], v[52:55]\n"
    "v_add_u32 v246, s90, v252\n"
    "ds_read_b64_tr_b16 v[248:249], v246 offset:8\n"
    "v_mfma_f32_16x16x32_bf16 v[52:55], a[216:219], a[132:135], v[52:55]\n"
    "ds_read_b64_tr_b16 v[250:251], v246 offset:520\n"
    "s_addk_i32 m0, 1024\n"
    "v_mfma_f32_16x16x32_bf16 v[84:87], a[216:219], a[136:139], v[84:87]\n"
    "s_add_u32 s92, s92, 16384\n"
    "s_addc_u32 s93, s93, 0\n"
    "v_mfma_f32_16x16x32_bf16 v[84:87], a[220:223], a[168:171], v[84:87]\n"
    "buffer_load_dwordx4 v254, s[92:95], 0 offen lds\n"
    "v_mfma_f32_16x16x32_bf16 v[116:119], a[220:223], a[172:175], v[116:119]\n"
    "v_mfma_f32_16x16x32_bf16 v[116:119], a[216:219], a[140:143], v[116:119]\n"
    "s_addk_i32 m0, 1024\n"
    "s_add_u32 s92, s92, 16384\n"
    "s_addc_u32 s93, s93, 0\n"
    "v_mfma_f32_16x16x32_bf16 v[148:151], a[216:219], a[144:147], v[148:151]\n"
    "buffer_load_dwordx4 v254, s[92:95], 0 offen lds\n"
    "v_mfma_f32_16x16x32_bf16 v[148:151], a[220:223], a[176:179], v[148:151]\n"
    "v_mfma_f32_16x16x32_bf16 v[180:183], a[220:223], a[180:183], v[180:183]\n"
    "v_mfma_f32_16x16x32_bf16 v[180:183], a[216:219], a[148:151], v[180:183]\n"
    "s_addk_i32 m0, 1024\n"
    "s_add_u32 s92, s92, 16384\n"
    "s_addc_u32 s93, s93, 0\n"
    "v_mfma_f32_16x16x32_bf16 v[212:215], a[216:219], a[152:155], v[212:215]\n"
    "buffer_load_dwordx4 v254, s[92:95], 0 offen lds\n"
    "s_add_u32 s62, s91, 1048576\n"
    "v_mfma_f32_16x16x32_bf16 v[212:215], a[220:223], a[184:187], v[212:215]\n"
    "s_waitcnt lgkmcnt(0)\n"
    "v_mfma_f32_16x16x32_bf16 a[244:247], a[220:223], a[188:191], a[244:247]\n"
    "buffer_store_dwordx4 v[248:251], v240, s[28:31], s62 offen offset:2048 sc1\n"
    "s_nop 0\n"
    "v_mfma_f32_16x16x32_bf16 a[244:247], a[216:219], a[156:159], a[244:247]\n"
    "v_add_u32 v252, 0x20000, v252\n"
    "v_add_u32 v253, 0x20000, v253\n"
    "v_mfma_f32_16x16x32_bf16 a[248:251], a[224:227], a[156:159], a[248:251]\n"
    "s_waitcnt vmcnt(19)\n"
    "ds_read_b64_tr_b16 a[192:193], v252 offset:0\n"
    "ds_read_b64_tr_b16 a[194:195], v252 offset:512\n"
    "v_mfma_f32_16x16x32_bf16 a[248:251], a[228:231], a[188:191], a[248:251]\n"
    "s_addk_i32 m0, 1024\n"
    "s_add_u32 s92, s92, 16384\n"
    "s_addc_u32 s93, s93, 0\n"
    "v_mfma_f32_16x16x32_bf16 v[216:219], a[228:231], a[184:187], v[216:219]\n"
    "buffer_load_dwordx4 v254, s[92:95], 0 offen lds\n"
    "ds_read_b64_tr_b16 a[196:197], v252 offset:4096\n"
    "v_mfma_f32_16x16x32_bf16 v[216:219], a[224:227], a[152:155], v[216:219]\n"
    "ds_read_b64_tr_b16 a[198:199], v252 offset:4608\n"
    "v_add_u32 v246, s90, v253\n"
    "v_mfma_f32_16x16x32_bf16 v[184:187], a[224:227], a[148:151], v[184:187]\n"
    "v_add_u32 v246, 0xfffe0000, v246\n"
    "ds_read_b64_tr_b16 v[248:249], v246 offset:0\n"
    "v_mfma_f32_16x16x32_bf16 v[184:187], a[228:231], a[180:183], v[184:187]\n"
    "ds_read_b64_tr_b16 v[250:251], v246 offset:512\n"
    "ds_read_b64_tr_b16 a[200:201], v252 offset:8\n"
    "v_mfma_f32_16x16x32_bf16 v[152:155], a[228:231], a[176:179], v[152:155]\n"
    "ds_read_b64_tr_b16 a[202:203], v252 offset:520\n"
    "s_addk_i32 m0, 1024\n"
    "v_mfma_f32_16x16x32_bf16 v[152:155], a[224:227], a[144:147], v[152:155]\n"
    "s_add_u32 s92, s92, 16384\n"
    "s_addc_u32 s93, s93, 0\n"
    "v_mfma_f32_16x16x32_bf16 v[120:123], a[224:227], a[140:143], v[120:123]\n"
    "buffer_load_dwordx4 v254, s[92:95], 0 offen lds\n"
    "ds_read_b64_tr_b16 a[204:205], v252 offset:4104\n"
    "ds_read_b64_tr_b16 a[206:207], v252 offset:4616\n"
    "v_mfma_f32_16x16x32_bf16 v[120:123], a[228:231], a[172:175], v[120:123]\n"
    "ds_read_b64_tr_b16 a[208:209], v253 offset:0\n"
    "ds_read_b64_tr_b16 a[210:211], v253 offset:512\n"
    "v_mfma_f32_16x16x32_bf16 v[88:91], a[228:231], a[168:171], v[88:91]\n"
    "s_addk_i32 m0, 1024\n"
    "s_add_u32 s92, s92, 16384\n"
    "s_addc_u32 s93, s93, 0\n"
    "v_mfma_f32_16x16x32_bf16 v[88:91], a[224:227], a[136:139], v[88:91]\n"
    "buffer_load_dwordx4 v254, s[92:95], 0 offen lds\n"
    "ds_read_b64_tr_b16 a[212:213], v253 offset:4096\n"
    "v_mfma_f32_16x16x32_bf16 v[56:59], a[224:227], a[132:135], v[56:59]\n"
    "ds_read_b64_tr_b16 a[214:215], v253 offset:4608\n"
    "ds_read_b64_tr_b16 a[216:217], v253 offset:8\n"
    "v_mfma_f32_16x16x32_bf16 v[56:59], a[228:231], a[164:167], v[56:59]\n"
    "ds_read_b64_tr_b16 a[218:219], v253 offset:520\n"
    "s_add_u32 s62, s91, 1052672\n"
    "s_waitcnt lgkmcnt(10)\n"
    "buffer_store_dwordx4 v[248:251], v240, s[28:31], s62 offen offset:0 sc1\n"
    "s_nop 0\n"
    "s_barrier\n"
    "ds_read_b64_tr_b16 a[220:221], v253 offset:4104\n"
    "v_mfma_f32_16x16x32_bf16 v[24:27], a[228:231], a[160:163], v[24:27]\n"
    "ds_read_b64_tr_b16 a[222:223], v253 offset:4616\n"
    "v_mfma_f32_16x16x32_bf16 v[24:27], a[224:227], a[128:131], v[24:27]\n"
    "s_add_u32 s82, s77, 3076\n"
    "s_and_b32 s82, s82, s86\n"
    "v_mfma_f32_16x16x32_bf16 v[28:31], a[232:235], a[128:131], v[28:31]\n"
    "buffer_load_dword v244, v240, s[80:83], s77 offen offset:3072 sc1\n"
    "v_mfma_f32_16x16x32_bf16 v[28:31], a[236:239], a[160:163], v[28:31]\n"
    "v_mfma_f32_16x16x32_bf16 v[60:63], a[236:239], a[164:167], v[60:63]\n"
    "v_add_u32 v246, s90, v253\n"
    "v_add_u32 v246, 0xfffe0000, v246\n"
    "v_mfma_f32_16x16x32_bf16 v[60:63], a[232:235], a[132:135], v[60:63]\n"
    "ds_read_b64_tr_b16 v[248:249], v246 offset:8\n"
    "ds_read_b64_tr_b16 v[250:251], v246 offset:520\n"
    "v_mfma_f32_16x16x32_bf16 v[92:95], a[232:235], a[136:139], v[92:95]\n"
    "ds_read_b128 a[128:131], v241 offset:16384\n"
    "s_waitcnt lgkmcnt(3)\n"
    "v_mfma_f32_16x16x32_bf16 v[92:95], a[236:239], a[168:171], v[92:95]\n"
    "s_add_u32 m0, s79, 131072\n"
    "s_lshl_b32 s54, s64, 17\n"
    "s_xor_b32 s54, s54, s57\n"
    "v_mfma_f32_16x16x32_bf16 v[124:127], a[236:239], a[172:175], v[124:127]\n"
    "s_addk_i32 s54, 512\n"
    "s_add_u32 s92, s72, s54\n"
    "s_addc_u32 s93, s73, 0\n"
    "v_mfma_f32_16x16x32_bf16 v[124:127], a[232:235], a[140:143], v[124:127]\n"
    "buffer_load_dwordx4 v254, s[92:95], 0 offen lds\n"
    "ds_read_b128 a[160:163], v242 offset:16384\n"
    "ds_read_b128 a[164:167], v242 offset:18432\n"
    "v_mfma_f32_16x16x32_bf16 v[156:159], a[232:235], a[144:147], v[156:159]\n"
    "ds_read_b128 a[132:135], v241 offset:18432\n"
    "v_mfma_f32_16x16x32_bf16 v[156:159], a[236:239], a[176:179], v[156:159]\n"
    "ds_read_b128 a[136:139], v241 offset:20480\n"
    "s_addk_i32 m0, 1024\n"
    "v_mfma_f32_16x16x32_bf16 v[188:191], a[236:239], a[180:183], v[188:191]\n"
    "s_add_u32 s92, s92, 16384\n"
    "s_addc_u32 s93, s93, 0\n"
    "v_mfma_f32_16x16x32_bf16 v[188:191], a[232:235], a[148:151], v[188:191]\n"
    "buffer_load_dwordx4 v254, s[92:95], 0 offen lds\n"
    "ds_read_b128 a[168:171], v242 offset:20480\n"
    "ds_read_b128 a[172:175], v242 offset:22528\n"
    "v_mfma_f32_16x16x32_bf16 v[220:223], a[232:235], a[152:155], v[220:223]\n"
    "ds_read_b128 a[140:143], v241 offset:22528\n"
    "v_mfma_f32_16x16x32_bf16 v[220:223], a[236:239], a[184:187], v[220:223]\n"
    "ds_read_b128 a[144:147], v241 offset:24576\n"
    "s_addk_i32 m0, 1024\n"
    "v_mfma_f32_16x16x32_bf16 a[252:255], a[236:239], a[188:191], a[252:255]\n"
    "s_add_u32 s92, s92, 16384\n"
    "s_addc_u32 s93, s93, 0\n"
    "v_mfma_f32_16x16x32_bf16 a[252:255], a[232:235], a[156:159], a[252:255]\n"
    "s_waitcnt lgkmcnt(7)\n"
    "buffer_load_dwordx4 v254, s[92:95], 0 offen lds\n"
    "ds_read_b128 a[176:179], v242 offset:24576\n"
    "v_mfma_f32_16x16x32_bf16 v[0:3], a[192:195], a[128:131], v[0:3]\n"
    "s_add_u32 s62, s91, 1052672\n"
    "s_waitcnt lgkmcnt(10)\n"
    "v_mfma_f32_16x16x32_bf16 v[0:3], a[196:199], a[160:163], v[0:3]\n"
    "s_waitcnt lgkmcnt(6)\n"
    "v_mfma_f32_16x16x32_bf16 v[32:35], a[196:199], a[164:167], v[32:35]\n"
    "buffer_store_dwordx4 v[248:251], v240, s[28:31], s62 offen offset:2048 sc1\n"
    "s_nop 0\n"
    "ds_read_b128 a[180:183], v242 offset:26624\n"
    "ds_read_b128 a[148:151], v241 offset:26624\n"
    "ds_read_b128 a[152:155], v241 offset:28672\n"
    "v_mfma_f32_16x16x32_bf16 v[32:35], a[192:195], a[132:135], v[32:35]\n"
    "s_waitcnt lgkmcnt(7)\n"
    "s_addk_i32 m0, 1024\n"
    "s_add_u32 s92, s92, 16384\n"
    "s_addc_u32 s93, s93, 0\n"
    "v_mfma_f32_16x16x32_bf16 v[64:67], a[192:195], a[136:139], v[64:67]\n"
    "buffer_load_dwordx4 v254, s[92:95], 0 offen lds\n"
    "ds_read_b128 a[184:187], v242 offset:28672\n"
    "ds_read_b128 a[188:191], v242 offset:30720\n"
    "ds_read_b128 a[156:159], v241 offset:30720\n"
    "v_mfma_f32_16x16x32_bf16 v[64:67], a[196:199], a[168:171], v[64:67]\n"
    "s_waitcnt lgkmcnt(8)\n"
    "s_addk_i32 m0, 1024\n"
    "s_add_u32 s92, s92, 16384\n"
    "v_mfma_f32_16x16x32_bf16 v[96:99], a[196:199], a[172:175], v[96:99]\n"
    "s_addc_u32 s93, s93, 0\n"
    "buffer_load_dwordx4 v254, s[92:95], 0 offen lds\n"
    "v_mfma_f32_16x16x32_bf16 v[96:99], a[192:195], a[140:143], v[96:99]\n"
    "s_waitcnt lgkmcnt(6)\n"
    "s_addk_i32 m0, 1024\n"
    "s_add_u32 s92, s92, 16384\n"
    "v_mfma_f32_16x16x32_bf16 v[128:131], a[192:195], a[144:147], v[128:131]\n"
    "s_addc_u32 s93, s93, 0\n"
    "buffer_load_dwordx4 v254, s[92:95], 0 offen lds\n"
    "v_mfma_f32_16x16x32_bf16 v[128:131], a[196:199], a[176:179], v[128:131]\n"
    "s_waitcnt lgkmcnt(4)\n"
    "v_mfma_f32_16x16x32_bf16 v[160:163], a[196:199], a[180:183], v[160:163]\n"
    "v_mfma_f32_16x16x32_bf16 v[160:163], a[192:195], a[148:151], v[160:163]\n"
    "s_waitcnt lgkmcnt(2)\n"
    "s_addk_i32 m0, 1024\n"
    "s_add_u32 s92, s92, 16384\n"
    "v_mfma_f32_16x16x32_bf16 v[192:195], a[192:195], a[152:155], v[192:195]\n"
    "s_addc_u32 s93, s93, 0\n"
    "buffer_load_dwordx4 v254, s[92:95], 0 offen lds\n"
    "v_mfma_f32_16x16x32_bf16 v[192:195], a[196:199], a[184:187], v[192:195]\n"
    "s_waitcnt lgkmcnt(0)\n"
    "v_mfma_f32_16x16x32_bf16 v[224:227], a[196:199], a[188:191], v[224:227]\n"
    "v_mfma_f32_16x16x32_bf16 v[224:227], a[192:195], a[156:159], v[224:227]\n"
    "s_addk_i32 m0, 1024\n"
    "s_add_u32 s92, s92, 16384\n"
    "s_addc_u32 s93, s93, 0\n"
    "v_mfma_f32_16x16x32_bf16 v[228:231], a[200:203], a[156:159], v[228:231]\n"
    "buffer_load_dwordx4 v254, s[92:95], 0 offen lds\n"
    "s_add_u32 s57, s57, 131072\n"
    "v_mfma_f32_16x16x32_bf16 v[228:231], a[204:207], a[188:191], v[228:231]\n"
    "s_cmp_ge_u32 s57, 1572864\n"
    "s_cselect_b32 s57, s70, s57\n"
    "v_mfma_f32_16x16x32_bf16 v[196:199], a[204:207], a[184:187], v[196:199]\n"
    "v_mfma_f32_16x16x32_bf16 v[196:199], a[200:203], a[152:155], v[196:199]\n"
    "v_mfma_f32_16x16x32_bf16 v[164:167], a[200:203], a[148:151], v[164:167]\n"
    "v_mfma_f32_16x16x32_bf16 v[164:167], a[204:207], a[180:183], v[164:167]\n"
    "v_mfma_f32_16x16x32_bf16 v[132:135], a[204:207], a[176:179], v[132:135]\n"
    "s_xor_b32 s59, s66, 1536\n"
    "s_add_u32 m0, s68, 65536\n"
    "s_add_u32 s96, s24, s59\n"
    "v_mfma_f32_16x16x32_bf16 v[132:135], a[200:203], a[144:147], v[132:135]\n"
    "s_addc_u32 s97, s25, 0\n"
    "buffer_load_dwordx4 v255, s[96:99], 0 offen lds\n"
    "v_mfma_f32_16x16x32_bf16 v[100:103], a[200:203], a[140:143], v[100:103]\n"
    "s_bitcmp1_b32 s85, 10\n"
    "s_cbranch_scc1 CP10_%=\n"
    "CR10_%=:\n"
    "v_mfma_f32_16x16x32_bf16 v[100:103], a[204:207], a[172:175], v[100:103]\n"
    "s_add_u32 m0, s68, 66560\n"
    "s_add_u32 s96, s96, 16384\n"
    "s_addc_u32 s97, s97, 0\n"
    "v_mfma_f32_16x16x32_bf16 v[68:71], a[204:207], a[168:171], v[68:71]\n"
    "buffer_load_dwordx4 v243, s[96:99], 0 offen lds\n"
    "v_mfma_f32_16x16x32_bf16 v[68:71], a[200:203], a[136:139], v[68:71]\n"
    "v_mfma_f32_16x16x32_bf16 v[36:39], a[200:203], a[132:135], v[36:39]\n"
    "s_add_u32 m0, s68, 67584\n"
    "s_add_u32 s96, s96, 16384\n"
    "s_addc_u32 s97, s97, 0\n"
    "v_mfma_f32_16x16x32_bf16 v[36:39], a[204:207], a[164:167], v[36:39]\n"
    "buffer_load_dwordx4 v255, s[96:99], 0 offen lds\n"
    "v_mfma_f32_16x16x32_bf16 v[4:7], a[204:207], a[160:163], v[4:7]\n"
    "v_mfma_f32_16x16x32_bf16 v[4:7], a[200:203], a[128:131], v[4:7]\n"
    "v_add_u32 v252, 0xfffe0000, v252\n"
    "v_add_u32 v253, 0xfffe0000, v253\n"
    "v_mfma_f32_16x16x32_bf16 v[8:11], a[208:211], a[128:131], v[8:11]\n"
    "s_waitcnt vmcnt(24)\n"
    "ds_read_b64_tr_b16 a[224:225], v252 offset:0\n"
    "ds_read_b64_tr_b16 a[226:227], v252 offset:512\n"
    "v_mfma_f32_16x16x32_bf16 v[8:11], a[212:215], a[160:163], v[8:11]\n"
    "s_add_u32 m0, s68, 68608\n"
    "s_add_u32 s96, s96, 16384\n"
    "s_addc_u32 s97, s97, 0\n"
    "v_mfma_f32_16x16x32_bf16 v[40:43], a[212:215], a[164:167], v[40:43]\n"
    "buffer_load_dwordx4 v243, s[96:99], 0 offen lds\n"
    "ds_read_b64_tr_b16 a[228:229], v252 offset:4096\n"
    "v_mfma_f32_16x16x32_bf16 v[40:43], a[208:211], a[132:135], v[40:43]\n"
    "ds_read_b64_tr_b16 a[230:231], v252 offset:4608\n"
    "ds_read_b64_tr_b16 a[232:233], v252 offset:8\n"
    "v_mfma_f32_16x16x32_bf16 v[72:75], a[208:211], a[136:139], v[72:75]\n"
    "ds_read_b64_tr_b16 a[234:235], v252 offset:520\n"
    "v_mfma_f32_16x16x32_bf16 v[72:75], a[212:215], a[168:171], v[72:75]\n"
    "ds_read_b64_tr_b16 a[236:237], v252 offset:4104\n"
    "ds_read_b64_tr_b16 a[238:239], v252 offset:4616\n"
    "v_mfma_f32_16x16x32_bf16 v[104:107], a[212:215], a[172:175], v[104:107]\n"
    "v_mfma_f32_16x16x32_bf16 v[104:107], a[208:211], a[140:143], v[104:107]\n"
    "ds_read_b64_tr_b16 a[192:193], v253 offset:0\n"
    "ds_read_b64_tr_b16 a[194:195], v253 offset:512\n"
    "v_mfma_f32_16x16x32_bf16 v[136:139], a[208:211], a[144:147], v[136:139]\n"
    "v_mfma_f32_16x16x32_bf16 v[136:139], a[212:215], a[176:179], v[136:139]\n"
    "ds_read_b64_tr_b16 a[196:197], v253 offset:4096\n"
    "ds_read_b64_tr_b16 a[198:199], v253 offset:4608\n"
    "v_mfma_f32_16x16x32_bf16 v[168:171], a[212:215], a[180:183], v[168:171]\n"
    "v_mfma_f32_16x16x32_bf16 v[168:171], a[208:211], a[148:151], v[168:171]\n"
    "ds_read_b64_tr_b16 a[200:201], v253 offset:8\n"
    "ds_read_b64_tr_b16 a[202:203], v253 offset:520\n"
    "v_mfma_f32_16x16x32_bf16 v[200:203], a[208:211], a[152:155], v[200:203]\n"
    "v_mfma_f32_16x16x32_bf16 v[200:203], a[212:215], a[184:187], v[200:203]\n"
    "ds_read_b64_tr_b16 a[204:205], v253 offset:4104\n"
    "ds_read_b64_tr_b16 a[206:207], v253 offset:4616\n"
    "v_mfma_f32_16x16x32_bf16 v[232:235], a[212:215], a[188:191], v[232:235]\n"
    "v_mfma_f32_16x16x32_bf16 v[232:235], a[208:211], a[156:159], v[232:235]\n"
    "v_mfma_f32_16x16x32_bf16 v[236:239], a[216:219], a[156:159], v[236:239]\n"
    "v_mfma_f32_16x16x32_bf16 v[236:239], a[220:223], a[188:191], v[236:239]\n"
    "v_mfma_f32_16x16x32_bf16 v[204:207], a[220:223], a[184:187], v[204:207]\n"
    "v_mfma_f32_16x16x32_bf16 v[204:207], a[216:219], a[152:155], v[204:207]\n"
    "v_mfma_f32_16x16x32_bf16 v[172:175], a[216:219], a[148:151], v[172:175]\n"
    "v_mfma_f32_16x16x32_bf16 v[172:175], a[220:223], a[180:183], v[172:175]\n"
    "v_mfma_f32_16x16x32_bf16 v[140:143], a[220:223], a[176:179], v[140:143]\n"
    "v_mfma_f32_16x16x32_bf16 v[140:143], a[216:219], a[144:147], v[140:143]\n"
    "v_mfma_f32_16x16x32_bf16 v[108:111], a[216:219], a[140:143], v[108:111]\n"
    "v_mfma_f32_16x16x32_bf16 v[108:111], a[220:223], a[172:175], v[108:111]\n"
    "v_mfma_f32_16x16x32_bf16 v[76:79], a[220:223], a[168:171], v[76:79]\n"
    "v_mfma_f32_16x16x32_bf16 v[76:79], a[216:219], a[136:139], v[76:79]\n"
    "v_mfma_f32_16x16x32_bf16 v[44:47], a[216:219], a[132:135], v[44:47]\n"
    "v_mfma_f32_16x16x32_bf16 v[44:47], a[220:223], a[164:167], v[44:47]\n"
    "v_mfma_f32_16x16x32_bf16 v[12:15], a[220:223], a[160:163], v[12:15]\n"
    "v_mfma_f32_16x16x32_bf16 v[12:15], a[216:219], a[128:131], v[12:15]\n"
    "s_waitcnt lgkmcnt(12)\n"
    "v_mfma_f32_16x16x32_bf16 v[16:19], a[224:227], a[128:131], v[16:19]\n"
    "v_mfma_f32_16x16x32_bf16 v[16:19], a[228:231], a[160:163], v[16:19]\n"
    "v_mfma_f32_16x16x32_bf16 v[48:51], a[228:231], a[164:167], v[48:51]\n"
    "v_mfma_f32_16x16x32_bf16 v[48:51], a[224:227], a[132:135], v[48:51]\n"
    "v_mfma_f32_16x16x32_bf16 v[80:83], a[224:227], a[136:139], v[80:83]\n"
    "v_mfma_f32_16x16x32_bf16 v[80:83], a[228:231], a[168:171], v[80:83]\n"
    "v_mfma_f32_16x16x32_bf16 v[112:115], a[228:231], a[172:175], v[112:115]\n"
    "v_mfma_f32_16x16x32_bf16 v[112:115], a[224:227], a[140:143], v[112:115]\n"
    "v_mfma_f32_16x16x32_bf16 v[144:147], a[224:227], a[144:147], v[144:147]\n"
    "v_mfma_f32_16x16x32_bf16 v[144:147], a[228:231], a[176:179], v[144:147]\n"
    "v_mfma_f32_16x16x32_bf16 v[176:179], a[228:231], a[180:183], v[176:179]\n"
    "v_mfma_f32_16x16x32_bf16 v[176:179], a[224:227], a[148:151], v[176:179]\n"
    "v_mfma_f32_16x16x32_bf16 v[208:211], a[224:227], a[152:155], v[208:211]\n"
    "v_mfma_f32_16x16x32_bf16 v[208:211], a[228:231], a[184:187], v[208:211]\n"
    "v_mfma_f32_16x16x32_bf16 a[240:243], a[228:231], a[188:191], a[240:243]\n"
    "v_mfma_f32_16x16x32_bf16 a[240:243], a[224:227], a[156:159], a[240:243]\n"
    "s_waitcnt lgkmcnt(8)\n"
    "v_mfma_f32_16x16x32_bf16 a[244:247], a[232:235], a[156:159], a[244:247]\n"
    "v_mfma_f32_16x16x32_bf16 a[244:247], a[236:239], a[188:191], a[244:247]\n"
    "v_mfma_f32_16x16x32_bf16 v[212:215], a[236:239], a[184:187], v[212:215]\n"
    "v_mfma_f32_16x16x32_bf16 v[212:215], a[232:235], a[152:155], v[212:215]\n"
    "v_mfma_f32_16x16x32_bf16 v[180:183], a[232:235], a[148:151], v[180:183]\n"
    "v_mfma_f32_16x16x32_bf16 v[180:183], a[236:239], a[180:183], v[180:183]\n"
    "v_mfma_f32_16x16x32_bf16 v[148:151], a[236:239], a[176:179], v[148:151]\n"
    "v_mfma_f32_16x16x32_bf16 v[148:151], a[232:235], a[144:147], v[148:151]\n"
    "v_mfma_f32_16x16x32_bf16 v[116:119], a[232:235], a[140:143], v[116:119]\n"
    "v_mfma_f32_16x16x32_bf16 v[116:119], a[236:239], a[172:175], v[116:119]\n"
    "v_mfma_f32_16x16x32_bf16 v[84:87], a[236:239], a[168:171], v[84:87]\n"
    "v_mfma_f32_16x16x32_bf16 v[84:87], a[232:235], a[136:139], v[84:87]\n"
    "v_mfma_f32_16x16x32_bf16 v[52:55], a[232:235], a[132:135], v[52:55]\n"
    "v_mfma_f32_16x16x32_bf16 v[52:55], a[236:239], a[164:167], v[52:55]\n"
    "v_mfma_f32_16x16x32_bf16 v[20:23], a[236:239], a[160:163], v[20:23]\n"
    "v_mfma_f32_16x16x32_bf16 v[20:23], a[232:235], a[128:131], v[20:23]\n"
    "s_waitcnt lgkmcnt(4)\n"
    "s_waitcnt vmcnt(15)\n"
    "ds_read_b64_tr_b16 a[208:209], v252 offset:32768\n"
    "v_mfma_f32_16x16x32_bf16 v[24:27], a[192:195], a[128:131], v[24:27]\n"
    "ds_read_b64_tr_b16 a[210:211], v252 offset:33280\n"
    "v_mfma_f32_16x16x32_bf16 v[24:27], a[196:199], a[160:163], v[24:27]\n"
    "ds_read_b64_tr_b16 a[212:213], v252 offset:36864\n"
    "ds_read_b64_tr_b16 a[214:215], v252 offset:37376\n"
    "v_mfma_f32_16x16x32_bf16 v[56:59], a[196:199], a[164:167], v[56:59]\n"
    "v_mfma_f32_16x16x32_bf16 v[56:59], a[192:195], a[132:135], v[56:59]\n"
    "ds_read_b64_tr_b16 a[216:217], v252 offset:32776\n"
    "ds_read_b64_tr_b16 a[218:219], v252 offset:33288\n"
    "v_mfma_f32_16x16x32_bf16 v[88:91], a[192:195], a[136:139], v[88:91]\n"
    "v_mfma_f32_16x16x32_bf16 v[88:91], a[196:199], a[168:171], v[88:91]\n"
    "ds_read_b64_tr_b16 a[220:221], v252 offset:36872\n"
    "ds_read_b64_tr_b16 a[222:223], v252 offset:37384\n"
    "v_mfma_f32_16x16x32_bf16 v[120:123], a[196:199], a[172:175], v[120:123]\n"
    "v_mfma_f32_16x16x32_bf16 v[120:123], a[192:195], a[140:143], v[120:123]\n"
    "ds_read_b64_tr_b16 a[224:225], v253 offset:32768\n"
    "ds_read_b64_tr_b16 a[226:227], v253 offset:33280\n"
    "v_mfma_f32_16x16x32_bf16 v[152:155], a[192:195], a[144:147], v[152:155]\n"
    "v_mfma_f32_16x16x32_bf16 v[152:155], a[196:199], a[176:179], v[152:155]\n"
    "ds_read_b64_tr_b16 a[228:229], v253 offset:36864\n"
    "ds_read_b64_tr_b16 a[230:231], v253 offset:37376\n"
    "v_mfma_f32_16x16x32_bf16 v[184:187], a[196:199], a[180:183], v[184:187]\n"
    "v_mfma_f32_16x16x32_bf16 v[184:187], a[192:195], a[148:151], v[184:187]\n"
    "ds_read_b64_tr_b16 a[232:233], v253 offset:32776\n"
    "ds_read_b64_tr_b16 a[234:235], v253 offset:33288\n"
    "v_mfma_f32_16x16x32_bf16 v[216:219], a[192:195], a[152:155], v[216:219]\n"
    "v_mfma_f32_16x16x32_bf16 v[216:219], a[196:199], a[184:187], v[216:219]\n"
    "s_waitcnt vmcnt(13)\n"
    "v_readfirstlane_b32 s62, v244\n"
    "s_cmp_eq_u32 s86, 0\n"
    "s_cbranch_scc1 LA3_%=\n"
    "s_cmp_eq_u32 %0, 0\n"
    "s_cbranch_scc1 LA3_%=\n"
    "s_cmp_eq_u32 s62, s88\n"
    "s_cbranch_scc1 LA3_%=\n"
    "s_mov_b32 s69, 0x400000\n"
    "LA4_%=:\n"
    "s_sleep 2\n"
    "s_add_u32 s82, s77, 2052\n"
    "s_and_b32 s82, s82, s86\n"
    "buffer_load_dword v244, v240, s[80:83], s77 offen offset:2048 sc1\n"
    "s_waitcnt vmcnt(0)\n"
    "s_add_u32 s82, s76, 8\n"
    "s_and_b32 s82, s82, s86\n"
    "buffer_store_dwordx2 v[244:245], v240, s[80:83], s76 offen sc1\n"
    "s_waitcnt vmcnt(0)\n"
    "s_add_u32 s82, s77, 3076\n"
    "s_and_b32 s82, s82, s86\n"
    "buffer_load_dword v244, v240, s[80:83], s77 offen offset:3072 sc1\n"
    "s_waitcnt vmcnt(0)\n"
    "v_readfirstlane_b32 s62, v244\n"
    "s_sub_u32 s69, s69, 1\n"
    "s_cbranch_scc1 LA3_%=\n"
    "s_cmp_eq_u32 s62, s88\n"
    "s_cbranch_scc0 LA4_%=\n"
    "LA3_%=:\n"
    "s_waitcnt vmcnt(9)\n"
    "s_barrier\n"
    "v_mfma_f32_16x16x32_bf16 a[248:251], a[196:199], a[188:191], a[248:251]\n"
    "ds_read_b64_tr_b16 a[236:237], v253 offset:36872\n"
    "ds_read_b64_tr_b16 a[238:239], v253 offset:37384\n"
    "v_mfma_f32_16x16x32_bf16 a[248:251], a[192:195], a[156:159], a[248:251]\n"
    "s_waitcnt lgkmcnt(15)\n"
    "v_mfma_f32_16x16x32_bf16 a[252:255], a[200:203], a[156:159], a[252:255]\n"
    "v_mfma_f32_16x16x32_bf16 a[252:255], a[204:207], a[188:191], a[252:255]\n"
    "v_mfma_f32_16x16x32_bf16 v[220:223], a[204:207], a[184:187], v[220:223]\n"
    "ds_read_b128 a[156:159], v241 offset:47104\n"
    "v_mfma_f32_16x16x32_bf16 v[220:223], a[200:203], a[152:155], v[220:223]\n"
    "s_xor_b32 s59, s66, 1664\n"
    "s_add_u32 m0, s68, 81920\n"
    "s_add_u32 s96, s24, s59\n"
    "v_mfma_f32_16x16x32_bf16 v[188:191], a[200:203], a[148:151], v[188:191]\n"
    "s_addc_u32 s97, s25, 0\n"
    "buffer_load_dwordx4 v255, s[96:99], 0 offen lds\n"
    "v_mfma_f32_16x16x32_bf16 v[188:191], a[204:207], a[180:183], v[188:191]\n"
    "ds_read_b128 a[188:191], v242 offset:47104\n"
    "v_mfma_f32_16x16x32_bf16 v[156:159], a[204:207], a[176:179], v[156:159]\n"
    "ds_read_b128 a[184:187], v242 offset:45056\n"
    "v_mfma_f32_16x16x32_bf16 v[156:159], a[200:203], a[144:147], v[156:159]\n"
    "ds_read_b128 a[152:155], v241 offset:45056\n"
    "v_mfma_f32_16x16x32_bf16 v[124:127], a[200:203], a[140:143], v[124:127]\n"
    "ds_read_b128 a[148:151], v241 offset:43008\n"
    "s_add_u32 m0, s68, 82944\n"
    "v_mfma_f32_16x16x32_bf16 v[124:127], a[204:207], a[172:175], v[124:127]\n"
    "s_add_u32 s96, s96, 16384\n"
    "s_addc_u32 s97, s97, 0\n"
    "v_mfma_f32_16x16x32_bf16 v[92:95], a[204:207], a[168:171], v[92:95]\n"
    "buffer_load_dwordx4 v243, s[96:99], 0 offen lds\n"
    "ds_read_b128 a[180:183], v242 offset:43008\n"
    "v_mfma_f32_16x16x32_bf16 v[92:95], a[200:203], a[136:139], v[92:95]\n"
    "ds_read_b128 a[176:179], v242 offset:40960\n"
    "v_mfma_f32_16x16x32_bf16 v[60:63], a[200:203], a[132:135], v[60:63]\n"
    "ds_read_b128 a[144:147], v241 offset:40960\n"
    "v_mfma_f32_16x16x32_bf16 v[60:63], a[204:207], a[164:167], v[60:63]\n"
    "ds_read_b128 a[140:143], v241 offset:38912\n"
    "s_add_u32 m0, s68, 83968\n"
    "v_mfma_f32_16x16x32_bf16 v[28:31], a[204:207], a[160:163], v[28:31]\n"
    "s_add_u32 s96, s96, 16384\n"
    "s_addc_u32 s97, s97, 0\n"
    "v_mfma_f32_16x16x32_bf16 v[28:31], a[200:203], a[128:131], v[28:31]\n"
    "s_waitcnt lgkmcnt(7)\n"
    "buffer_load_dwordx4 v255, s[96:99], 0 offen lds\n"
    "ds_read_b128 a[172:175], v242 offset:38912\n"
    "ds_read_b128 a[168:171], v242 offset:36864\n"
    "v_mfma_f32_16x16x32_bf16 v[224:227], a[208:211], a[156:159], v[224:227]\n"
    "ds_read_b128 a[136:139], v241 offset:36864\n"
    "v_mfma_f32_16x16x32_bf16 v[224:227], a[212:215], a[188:191], v[224:227]\n"
    "s_waitcnt lgkmcnt(8)\n"
    "ds_read_b128 a[132:135], v241 offset:34816\n"
    "s_add_u32 m0, s68, 84992\n"
    "v_mfma_f32_16x16x32_bf16 v[192:195], a[212:215], a[184:187], v[192:195]\n"
    "s_add_u32 s96, s96, 16384\n"
    "s_addc_u32 s97, s97, 0\n"
    "v_mfma_f32_16x16x32_bf16 v[192:195], a[208:211], a[152:155], v[192:195]\n"
    "s_waitcnt lgkmcnt(7)\n"
    "buffer_load_dwordx4 v243, s[96:99], 0 offen lds\n"
    "ds_read_b128 a[164:167], v242 offset:34816\n"
    "ds_read_b128 a[160:163], v242 offset:32768\n"
    "ds_read_b128 a[128:131], v241 offset:32768\n"
    "s_lshr_b32 s81, %3, 2\n"
    "s_lshl_b32 s81, s81, 21\n"
    "s_and_b32 s61, %3, 3\n"
    "s_lshl_b32 s61, s61, 15\n"
    "s_add_u32 s81, s81, s61\n"
    "s_mul_i32 s61, s65, 0x2400\n"
    "s_and_b32 s61, s61, 0x6400\n"
    "s_add_u32 s81, s81, s61\n"
    "s_mov_b32 s82, 0\n"
    "s_mov_b32 s83, 0\n"
    "v_mfma_f32_16x16x32_bf16 v[160:163], a[208:211], a[148:151], v[160:163]\n"
    "s_xor_b32 s59, s66, 1792\n"
    "s_add_u32 m0, s68, 114688\n"
    "s_add_u32 s96, s24, s59\n"
    "v_mfma_f32_16x16x32_bf16 v[160:163], a[212:215], a[180:183], v[160:163]\n"
    "s_waitcnt lgkmcnt(8)\n"
    "s_addc_u32 s97, s25, 0\n"
    "v_mfma_f32_16x16x32_bf16 v[128:131], a[212:215], a[176:179], v[128:131]\n"
    "buffer_load_dwordx4 v255, s[96:99], 0 offen lds\n"
    "v_mfma_f32_16x16x32_bf16 v[128:131], a[208:211], a[144:147], v[128:131]\n"
    "s_waitcnt lgkmcnt(6)\n"
    "s_add_u32 m0, s68, 115712\n"
    "s_add_u32 s96, s96, 16384\n"
    "v_mfma_f32_16x16x32_bf16 v[96:99], a[208:211], a[140:143], v[96:99]\n"
    "s_addc_u32 s97, s97, 0\n"
    "buffer_load_dwordx4 v243, s[96:99], 0 offen lds\n"
    "v_mfma_f32_16x16x32_bf16 v[96:99], a[212:215], a[172:175], v[96:99]\n"
    "s_waitcnt lgkmcnt(4)\n"
    "v_mfma_f32_16x16x32_bf16 v[64:67], a[212:215], a[168:171], v[64:67]\n"
    "v_mfma_f32_16x16x32_bf16 v[64:67], a[208:211], a[136:139], v[64:67]\n"
    "s_waitcnt lgkmcnt(2)\n"
    "s_add_u32 m0, s68, 116736\n"
    "s_add_u32 s96, s96, 16384\n"
    "v_mfma_f32_16x16x32_bf16 v[32:35], a[208:211], a[132:135], v[32:35]\n"
    "s_addc_u32 s97, s97, 0\n"
    "buffer_load_dwordx4 v255, s[96:99], 0 offen lds\n"
    "v_mfma_f32_16x16x32_bf16 v[32:35], a[212:215], a[164:167], v[32:35]\n"
    "s_waitcnt lgkmcnt(0)\n"
    "v_mfma_f32_16x16x32_bf16 v[0:3], a[212:215], a[160:163], v[0:3]\n"
    "v_mfma_f32_16x16x32_bf16 v[0:3], a[208:211], a[128:131], v[0:3]\n"
    "s_add_u32 m0, s68, 117760\n"
    "s_add_u32 s96, s96, 16384\n"
    "s_addc_u32 s97, s97, 0\n"
    "v_mfma_f32_16x16x32_bf16 v[4:7], a[216:219], a[128:131], v[4:7]\n"
    "buffer_load_dwordx4 v243, s[96:99], 0 offen lds\n"
    "v_mfma_f32_16x16x32_bf16 v[4:7], a[220:223], a[160:163], v[4:7]\n"
    "v_mfma_f32_16x16x32_bf16 v[36:39], a[220:223], a[164:167], v[36:39]\n"
    "v_mfma_f32_16x16x32_bf16 v[36:39], a[216:219], a[132:135], v[36:39]\n"
    "v_mfma_f32_16x16x32_bf16 v[68:71], a[216:219], a[136:139], v[68:71]\n"
    "s_load_dword s80, s[32:33], s81\n"
    "s_xor_b32 s81, s81, 8192\n"
    "s_load_dword s80, s[32:33], s81\n"
    "v_mfma_f32_16x16x32_bf16 v[68:71], a[220:223], a[168:171], v[68:71]\n"
    "s_xor_b32 s81, s81, 8192\n"
    "v_mfma_f32_16x16x32_bf16 v[100:103], a[220:223], a[172:175], v[100:103]\n"
    "v_mfma_f32_16x16x32_bf16 v[100:103], a[216:219], a[140:143], v[100:103]\n"
    "v_mfma_f32_16x16x32_bf16 v[132:135], a[216:219], a[144:147], v[132:135]\n"
    "s_bitcmp1_b32 s85, 11\n"
    "s_cbranch_scc1 CP11_%=\n"
    "CR11_%=:\n"
    "v_mfma_f32_16x16x32_bf16 v[132:135], a[220:223], a[176:179], v[132:135]\n"
    "v_mfma_f32_16x16x32_bf16 v[164:167], a[220:223], a[180:183], v[164:167]\n"
    "v_mfma_f32_16x16x32_bf16 v[164:167], a[216:219], a[148:151], v[164:167]\n"
    "v_mfma_f32_16x16x32_bf16 v[196:199], a[216:219], a[152:155], v[196:199]\n"
    "v_mfma_f32_16x16x32_bf16 v[196:199], a[220:223], a[184:187], v[196:199]\n"
    "v_mfma_f32_16x16x32_bf16 v[228:231], a[220:223], a[188:191], v[228:231]\n"
    "v_mfma_f32_16x16x32_bf16 v[228:231], a[216:219], a[156:159], v[228:231]\n"
    "v_add_u32 v252, 0x20000, v252\n"
    "v_add_u32 v253, 0x20000, v253\n"
    "v_mfma_f32_16x16x32_bf16 v[232:235], a[224:227], a[156:159], v[232:235]\n"
    "s_waitcnt vmcnt(12)\n"
    "ds_read_b64_tr_b16 a[192:193], v252 offset:0\n"
    "ds_read_b64_tr_b16 a[194:195], v252 offset:512\n"
    "v_mfma_f32_16x16x32_bf16 v[232:235], a[228:231], a[188:191], v[232:235]\n"
    "ds_read_b64_tr_b16 a[196:197], v252 offset:4096\n"
    "ds_read_b64_tr_b16 a[198:199], v252 offset:4608\n"
    "v_mfma_f32_16x16x32_bf16 v[200:203], a[228:231], a[184:187], v[200:203]\n"
    "v_mfma_f32_16x16x32_bf16 v[200:203], a[224:227], a[152:155], v[200:203]\n"
    "ds_read_b64_tr_b16 a[200:201], v252 offset:8\n"
    "ds_read_b64_tr_b16 a[202:203], v252 offset:520\n"
    "v_mfma_f32_16x16x32_bf16 v[168:171], a[224:227], a[148:151], v[168:171]\n"
    "v_mfma_f32_16x16x32_bf16 v[168:171], a[228:231], a[180:183], v[168:171]\n"
    "ds_read_b64_tr_b16 a[204:205], v252 offset:4104\n"
    "ds_read_b64_tr_b16 a[206:207], v252 offset:4616\n"
    "v_mfma_f32_16x16x32_bf16 v[136:139], a[228:231], a[176:179], v[136:139]\n"
    "v_mfma_f32_16x16x32_bf16 v[136:139], a[224:227], a[144:147], v[136:139]\n"
    "ds_read_b64_tr_b16 a[208:209], v253 offset:0\n"
    "ds_read_b64_tr_b16 a[210:211], v253 offset:512\n"
    "v_mfma_f32_16x16x32_bf16 v[104:107], a[224:227], a[140:143], v[104:107]\n"
    "v_mfma_f32_16x16x32_bf16 v[104:107], a[228:231], a[172:175], v[104:107]\n"
    "ds_read_b64_tr_b16 a[212:213], v253 offset:4096\n"
    "ds_read_b64_tr_b16 a[214:215], v253 offset:4608\n"
    "v_mfma_f32_16x16x32_bf16 v[72:75], a[228:231], a[168:171], v[72:75]\n"
    "v_mfma_f32_16x16x32_bf16 v[72:75], a[224:227], a[136:139], v[72:75]\n"
    "ds_read_b64_tr_b16 a[216:217], v253 offset:8\n"
    "ds_read_b64_tr_b16 a[218:219], v253 offset:520\n"
    "v_mfma_f32_16x16x32_bf16 v[40:43], a[224:227], a[132:135], v[40:43]\n"
    "v_mfma_f32_16x16x32_bf16 v[40:43], a[228:231], a[164:167], v[40:43]\n"
    "ds_read_b64_tr_b16 a[220:221], v253 offset:4104\n"
    "ds_read_b64_tr_b16 a[222:223], v253 offset:4616\n"
    "v_mfma_f32_16x16x32_bf16 v[8:11], a[228:231], a[160:163], v[8:11]\n"
    "v_mfma_f32_16x16x32_bf16 v[8:11], a[224:227], a[128:131], v[8:11]\n"
    "buffer_load_dwordx4 a[224:227], v240, s[20:23], 0 offen offset:0 sc1\n"
    "buffer_load_dwordx4 a[228:231], v240, s[20:23], 0 offen offset:1024 sc1\n"
    "s_lshr_b32 s60, %3, 3\n"
    "s_lshl_b32 s60, s60, 2\n"
    "s_add_u32 s60, s60, s65\n"
    "s_lshl_b32 s60, s60, 10\n"
    "s_min_u32 s60, s60, LAST_%= - LOOP_%= - 1024\n"
    "s_getpc_b64 s[62:63]\n"
    "LA5_%=:\n"
    "v_mfma_f32_16x16x32_bf16 v[12:15], a[232:235], a[128:131], v[12:15]\n"
    "s_add_u32 s62, s62, LOOP_%= - LA5_%=\n"
    "s_addc_u32 s63, s63, 0\n"
    "s_add_u32 s62, s62, s60\n"
    "v_mfma_f32_16x16x32_bf16 v[12:15], a[236:239], a[160:163], v[12:15]\n"
    "s_addc_u32 s63, s63, 0\n"
    "v_mfma_f32_16x16x32_bf16 v[44:47], a[236:239], a[164:167], v[44:47]\n"
    "global_load_dwordx4 v[248:251], v240, s[62:63]\n"
    "v_mfma_f32_16x16x32_bf16 v[44:47], a[232:235], a[132:135], v[44:47]\n"
    "v_mfma_f32_16x16x32_bf16 v[76:79], a[232:235], a[136:139], v[76:79]\n"
    "v_mfma_f32_16x16x32_bf16 v[76:79], a[236:239], a[168:171], v[76:79]\n"
    "v_mfma_f32_16x16x32_bf16 v[108:111], a[236:239], a[172:175], v[108:111]\n"
    "v_mfma_f32_16x16x32_bf16 v[108:111], a[232:235], a[140:143], v[108:111]\n"
    "v_mfma_f32_16x16x32_bf16 v[140:143], a[232:235], a[144:147], v[140:143]\n"
    "v_mfma_f32_16x16x32_bf16 v[140:143], a[236:239], a[176:179], v[140:143]\n"
    "v_mfma_f32_16x16x32_bf16 v[172:175], a[236:239], a[180:183], v[172:175]\n"
    "v_mfma_f32_16x16x32_bf16 v[172:175], a[232:235], a[148:151], v[172:175]\n"
    "v_mfma_f32_16x16x32_bf16 v[204:207], a[232:235], a[152:155], v[204:207]\n"
    "v_mfma_f32_16x16x32_bf16 v[204:207], a[236:239], a[184:187], v[204:207]\n"
    "v_mfma_f32_16x16x32_bf16 v[236:239], a[236:239], a[188:191], v[236:239]\n"
    "v_mfma_f32_16x16x32_bf16 v[236:239], a[232:235], a[156:159], v[236:239]\n"
    "s_waitcnt lgkmcnt(0)\n"
    "s_xor_b32 s81, s81, 8192\n"
    "s_load_dword s80, s[32:33], s81 offset:128\n"
    "s_load_dword s80, s[32:33], s81 offset:256\n"
    "v_mfma_f32_16x16x32_bf16 a[240:243], a[192:195], a[156:159], a[240:243]\n"
    "buffer_load_dwordx4 a[232:235], v240, s[20:23], 0 offen offset:2048 sc1\n"
    "v_mfma_f32_16x16x32_bf16 a[240:243], a[196:199], a[188:191], a[240:243]\n"
    "buffer_load_dwordx4 a[236:239], v240, s[20:23], 0 offen offset:3072 sc1\n"
    "s_addk_i32 s20, 4096\n"
    "v_mfma_f32_16x16x32_bf16 v[208:211], a[196:199], a[184:187], v[208:211]\n"
    "v_mfma_f32_16x16x32_bf16 v[208:211], a[192:195], a[152:155], v[208:211]\n"
    "v_mfma_f32_16x16x32_bf16 v[176:179], a[192:195], a[148:151], v[176:179]\n"
    "v_mfma_f32_16x16x32_bf16 v[176:179], a[196:199], a[180:183], v[176:179]\n"
    "v_mfma_f32_16x16x32_bf16 v[144:147], a[196:199], a[176:179], v[144:147]\n"
    "v_mfma_f32_16x16x32_bf16 v[144:147], a[192:195], a[144:147], v[144:147]\n"
    "v_mfma_f32_16x16x32_bf16 v[112:115], a[192:195], a[140:143], v[112:115]\n"
    "v_mfma_f32_16x16x32_bf16 v[112:115], a[196:199], a[172:175], v[112:115]\n"
    "v_mfma_f32_16x16x32_bf16 v[80:83], a[196:199], a[168:171], v[80:83]\n"
    "v_mfma_f32_16x16x32_bf16 v[80:83], a[192:195], a[136:139], v[80:83]\n"
    "v_mfma_f32_16x16x32_bf16 v[48:51], a[192:195], a[132:135], v[48:51]\n"
    "v_mfma_f32_16x16x32_bf16 v[48:51], a[196:199], a[164:167], v[48:51]\n"
    "v_mfma_f32_16x16x32_bf16 v[16:19], a[196:199], a[160:163], v[16:19]\n"
    "v_mfma_f32_16x16x32_bf16 v[16:19], a[192:195], a[128:131], v[16:19]\n"
    "s_getreg_b32 s73, hwreg(HW_REG_IB_STS, 8, 4)\n"
    "s_add_u32 s82, s82, s73\n"
    "v_mfma_f32_16x16x32_bf16 v[20:23], a[200:203], a[128:131], v[20:23]\n"
    "buffer_load_dwordx4 a[192:195], v240, s[20:23], 0 offen offset:0 sc1\n"
    "v_mfma_f32_16x16x32_bf16 v[20:23], a[204:207], a[160:163], v[20:23]\n"
    "buffer_load_dwordx4 a[196:199], v240, s[20:23], 0 offen offset:1024 sc1\n"
    "s_getreg_b32 s73, hwreg(HW_REG_IB_STS, 8, 4)\n"
    "v_mfma_f32_16x16x32_bf16 v[52:55], a[204:207], a[164:167], v[52:55]\n"
    "s_add_u32 s82, s82, s73\n"
    "v_mfma_f32_16x16x32_bf16 v[52:55], a[200:203], a[132:135], v[52:55]\n"
    "s_getreg_b32 s73, hwreg(HW_REG_IB_STS, 8, 4)\n"
    "s_add_u32 s82, s82, s73\n"
    "v_mfma_f32_16x16x32_bf16 v[84:87], a[200:203], a[136:139], v[84:87]\n"
    "v_mfma_f32_16x16x32_bf16 v[84:87], a[204:207], a[168:171], v[84:87]\n"
    "s_getreg_b32 s73, hwreg(HW_REG_IB_STS, 8, 4)\n"
    "s_add_u32 s82, s82, s73\n"
    "v_mfma_f32_16x16x32_bf16 v[116:119], a[204:207], a[172:175], v[116:119]\n"
    "v_mfma_f32_16x16x32_bf16 v[116:119], a[200:203], a[140:143], v[116:119]\n"
    "s_getreg_b32 s73, hwreg(HW_REG_IB_STS, 8, 4)\n"
    "s_add_u32 s82, s82, s73\n"
    "v_mfma_f32_16x16x32_bf16 v[148:151], a[200:203], a[144:147], v[148:151]\n"
    "v_mfma_f32_16x16x32_bf16 v[148:151], a[204:207], a[176:179], v[148:151]\n"
    "s_getreg_b32 s73, hwreg(HW_REG_IB_STS, 8, 4)\n"
    "s_add_u32 s82, s82, s73\n"
    "v_mfma_f32_16x16x32_bf16 v[180:183], a[204:207], a[180:183], v[180:183]\n"
    "v_mfma_f32_16x16x32_bf16 v[180:183], a[200:203], a[148:151], v[180:183]\n"
    "s_getreg_b32 s73, hwreg(HW_REG_IB_STS, 8, 4)\n"
    "s_add_u32 s82, s82, s73\n"
    "v_mfma_f32_16x16x32_bf16 v[212:215], a[200:203], a[152:155], v[212:215]\n"
    "v_mfma_f32_16x16x32_bf16 v[212:215], a[204:207], a[184:187], v[212:215]\n"
    "s_getreg_b32 s73, hwreg(HW_REG_IB_STS, 8, 4)\n"
    "s_add_u32 s82, s82, s73\n"
    "v_mfma_f32_16x16x32_bf16 a[244:247], a[204:207], a[188:191], a[244:247]\n"
    "v_mfma_f32_16x16x32_bf16 a[244:247], a[200:203], a[156:159], a[244:247]\n"
    "s_getreg_b32 s73, hwreg(HW_REG_IB_STS, 8, 4)\n"
    "s_add_u32 s82, s82, s73\n"
    "v_mfma_f32_16x16x32_bf16 a[248:251], a[208:211], a[156:159], a[248:251]\n"
    "buffer_load_dwordx4 a[200:203], v240, s[20:23], 0 offen offset:2048 sc1\n"
    "v_mfma_f32_16x16x32_bf16 a[248:251], a[212:215], a[188:191], a[248:251]\n"
    "buffer_load_dwordx4 a[204:207], v240, s[20:23], 0 offen offset:3072 sc1\n"
    "s_addk_i32 s20, 4096\n"
    "v_mfma_f32_16x16x32_bf16 v[216:219], a[212:215], a[184:187], v[216:219]\n"
    "s_getreg_b32 s73, hwreg(HW_REG_IB_STS, 8, 4)\n"
    "s_add_u32 s82, s82, s73\n"
    "v_mfma_f32_16x16x32_bf16 v[216:219], a[208:211], a[152:155], v[216:219]\n"
    "s_getreg_b32 s73, hwreg(HW_REG_IB_STS, 8, 4)\n"
    "s_add_u32 s82, s82, s73\n"
    "v_mfma_f32_16x16x32_bf16 v[184:187], a[208:211], a[148:151], v[184:187]\n"
    "v_mfma_f32_16x16x32_bf16 v[184:187], a[212:215], a[180:183], v[184:187]\n"
    "s_getreg_b32 s73, hwreg(HW_REG_IB_STS, 8, 4)\n"
    "s_add_u32 s82, s82, s73\n"
    "v_mfma_f32_16x16x32_bf16 v[152:155], a[212:215], a[176:179], v[152:155]\n"
    "v_mfma_f32_16x16x32_bf16 v[152:155], a[208:211], a[144:147], v[152:155]\n"
    "s_getreg_b32 s73, hwreg(HW_REG_IB_STS, 8, 4)\n"
    "s_add_u32 s82, s82, s73\n"
    "v_mfma_f32_16x16x32_bf16 v[120:123], a[208:211], a[140:143], v[120:123]\n"
    "v_mfma_f32_16x16x32_bf16 v[120:123], a[212:215], a[172:175], v[120:123]\n"
    "s_getreg_b32 s73, hwreg(HW_REG_IB_STS, 8, 4)\n"
    "s_add_u32 s82, s82, s73\n"
    "v_mfma_f32_16x16x32_bf16 v[88:91], a[212:215], a[168:171], v[88:91]\n"
    "v_mfma_f32_16x16x32_bf16 v[88:91], a[208:211], a[136:139], v[88:91]\n"
    "s_getreg_b32 s73, hwreg(HW_REG_IB_STS, 8, 4)\n"
    "s_add_u32 s82, s82, s73\n"
    "v_mfma_f32_16x16x32_bf16 v[56:59], a[208:211], a[132:135], v[56:59]\n"
    "v_mfma_f32_16x16x32_bf16 v[56:59], a[212:215], a[164:167], v[56:59]\n"
    "s_waitcnt vmcnt(13)\n"
    "s_barrier\n"
    "s_getreg_b32 s73, hwreg(HW_REG_IB_STS, 8, 4)\n"
    "v_mfma_f32_16x16x32_bf16 v[24:27], a[212:215], a[160:163], v[24:27]\n"
    "s_add_u32 s82, s82, s73\n"
    "v_mfma_f32_16x16x32_bf16 v[24:27], a[208:211], a[128:131], v[24:27]\n"
    "buffer_load_dwordx4 a[208:211], v240, s[20:23], 0 offen offset:0 sc1\n"
    "v_mfma_f32_16x16x32_bf16 v[28:31], a[216:219], a[128:131], v[28:31]\n"
    "buffer_load_dwordx4 a[212:215], v240, s[20:23], 0 offen offset:1024 sc1\n"
    "v_mfma_f32_16x16x32_bf16 v[28:31], a[220:223], a[160:163], v[28:31]\n"
    "v_mfma_f32_16x16x32_bf16 v[60:63], a[220:223], a[164:167], v[60:63]\n"
    "ds_read_b128 a[128:131], v241 offset:0\n"
    "v_mfma_f32_16x16x32_bf16 v[60:63], a[216:219], a[132:135], v[60:63]\n"
    "s_xor_b32 s59, s66, 1920\n"
    "s_add_u32 m0, s68, 98304\n"
    "s_add_u32 s96, s24, s59\n"
    "v_mfma_f32_16x16x32_bf16 v[92:95], a[216:219], a[136:139], v[92:95]\n"
    "s_addc_u32 s97, s25, 0\n"
    "buffer_load_dwordx4 v255, s[96:99], 0 offen lds\n"
    "v_mfma_f32_16x16x32_bf16 v[92:95], a[220:223], a[168:171], v[92:95]\n"
    "ds_read_b128 a[160:163], v242 offset:0\n"
    "v_mfma_f32_16x16x32_bf16 v[124:127], a[220:223], a[172:175], v[124:127]\n"
    "ds_read_b128 a[164:167], v242 offset:2048\n"
    "v_mfma_f32_16x16x32_bf16 v[124:127], a[216:219], a[140:143], v[124:127]\n"
    "ds_read_b128 a[132:135], v241 offset:2048\n"
    "v_mfma_f32_16x16x32_bf16 v[156:159], a[216:219], a[144:147], v[156:159]\n"
    "ds_read_b128 a[136:139], v241 offset:4096\n"
    "s_add_u32 m0, s68, 99328\n"
    "v_mfma_f32_16x16x32_bf16 v[156:159], a[220:223], a[176:179], v[156:159]\n"
    "s_add_u32 s96, s96, 16384\n"
    "s_addc_u32 s97, s97, 0\n"
    "v_mfma_f32_16x16x32_bf16 v[188:191], a[220:223], a[180:183], v[188:191]\n"
    "buffer_load_dwordx4 v243, s[96:99], 0 offen lds\n"
    "ds_read_b128 a[168:171], v242 offset:4096\n"
    "v_mfma_f32_16x16x32_bf16 v[188:191], a[216:219], a[148:151], v[188:191]\n"
    "ds_read_b128 a[172:175], v242 offset:6144\n"
    "v_mfma_f32_16x16x32_bf16 v[220:223], a[216:219], a[152:155], v[220:223]\n"
    "ds_read_b128 a[140:143], v241 offset:6144\n"
    "v_mfma_f32_16x16x32_bf16 v[220:223], a[220:223], a[184:187], v[220:223]\n"
    "ds_read_b128 a[144:147], v241 offset:8192\n"
    "s_add_u32 m0, s68, 100352\n"
    "v_mfma_f32_16x16x32_bf16 a[252:255], a[220:223], a[188:191], a[252:255]\n"
    "s_add_u32 s96, s96, 16384\n"
    "s_addc_u32 s97, s97, 0\n"
    "v_mfma_f32_16x16x32_bf16 a[252:255], a[216:219], a[156:159], a[252:255]\n"
    "s_waitcnt vmcnt(11) lgkmcnt(7)\n"
    "buffer_load_dwordx4 v255, s[96:99], 0 offen lds\n"
    "ds_read_b128 a[176:179], v242 offset:8192\n"
    "ds_read_b128 a[180:183], v242 offset:10240\n"
    "v_mfma_f32_16x16x32_bf16 v[0:3], a[224:227], a[128:131], v[0:3]\n"
    "ds_read_b128 a[148:151], v241 offset:10240\n"
    "v_mfma_f32_16x16x32_bf16 v[0:3], a[228:231], a[160:163], v[0:3]\n"
    "s_waitcnt lgkmcnt(8)\n"
    "ds_read_b128 a[152:155], v241 offset:12288\n"
    "s_add_u32 m0, s68, 101376\n"
    "v_mfma_f32_16x16x32_bf16 v[32:35], a[228:231], a[164:167], v[32:35]\n"
    "s_add_u32 s96, s96, 16384\n"
    "s_addc_u32 s97, s97, 0\n"
    "v_mfma_f32_16x16x32_bf16 v[32:35], a[224:227], a[132:135], v[32:35]\n"
    "s_waitcnt lgkmcnt(7)\n"
    "buffer_load_dwordx4 v243, s[96:99], 0 offen lds\n"
    "ds_read_b128 a[184:187], v242 offset:12288\n"
    "ds_read_b128 a[188:191], v242 offset:14336\n"
    "ds_read_b128 a[156:159], v241 offset:14336\n"
    "v_mfma_f32_16x16x32_bf16 v[64:67], a[224:227], a[136:139], v[64:67]\n"
    "buffer_load_dwordx4 a[216:219], v240, s[20:23], 0 offen offset:2048 sc1\n"
    "v_mfma_f32_16x16x32_bf16 v[64:67], a[228:231], a[168:171], v[64:67]\n"
    "s_waitcnt lgkmcnt(8)\n"
    "buffer_load_dwordx4 a[220:223], v240, s[20:23], 0 offen offset:3072 sc1\n"
    "s_addk_i32 s20, 4096\n"
    "v_mfma_f32_16x16x32_bf16 v[96:99], a[228:231], a[172:175], v[96:99]\n"
    "v_mfma_f32_16x16x32_bf16 v[96:99], a[224:227], a[140:143], v[96:99]\n"
    "s_waitcnt lgkmcnt(6)\n"
    "v_mfma_f32_16x16x32_bf16 v[128:131], a[224:227], a[144:147], v[128:131]\n"
    "v_mfma_f32_16x16x32_bf16 v[128:131], a[228:231], a[176:179], v[128:131]\n"
    "s_waitcnt lgkmcnt(4)\n"
    "v_mfma_f32_16x16x32_bf16 v[160:163], a[228:231], a[180:183], v[160:163]\n"
    "v_mfma_f32_16x16x32_bf16 v[160:163], a[224:227], a[148:151], v[160:163]\n"
    "s_waitcnt lgkmcnt(2)\n"
    "v_mfma_f32_16x16x32_bf16 v[192:195], a[224:227], a[152:155], v[192:195]\n"
    "v_mfma_f32_16x16x32_bf16 v[192:195], a[228:231], a[184:187], v[192:195]\n"
    "s_bitcmp1_b32 s85, 12\n"
    "s_cbranch_scc1 CP12_%=\n"
    "CR12_%=:\n"
    "s_waitcnt lgkmcnt(0)\n"
    "v_mfma_f32_16x16x32_bf16 v[224:227], a[228:231], a[188:191], v[224:227]\n"
    "v_mfma_f32_16x16x32_bf16 v[224:227], a[224:227], a[156:159], v[224:227]\n"
    "s_waitcnt lgkmcnt(0)\n"
    "s_xor_b32 s81, s81, 8192\n"
    "s_load_dword s80, s[32:33], s81 offset:128\n"
    "s_load_dword s80, s[32:33], s81 offset:256\n"
    "s_waitcnt vmcnt(12)\n"
    "v_mfma_f32_16x16x32_bf16 v[228:231], a[232:235], a[156:159], v[228:231]\n"
    "buffer_load_dwordx4 a[224:227], v240, s[20:23], 0 offen offset:0 sc1\n"
    "v_mfma_f32_16x16x32_bf16 v[228:231], a[236:239], a[188:191], v[228:231]\n"
    "buffer_load_dwordx4 a[228:231], v240, s[20:23], 0 offen offset:1024 sc1\n"
    "v_mfma_f32_16x16x32_bf16 v[196:199], a[236:239], a[184:187], v[196:199]\n"
    "v_mfma_f32_16x16x32_bf16 v[196:199], a[232:235], a[152:155], v[196:199]\n"
    "v_mfma_f32_16x16x32_bf16 v[164:167], a[232:235], a[148:151], v[164:167]\n"
    "v_mfma_f32_16x16x32_bf16 v[164:167], a[236:239], a[180:183], v[164:167]\n"
    "v_mfma_f32_16x16x32_bf16 v[132:135], a[236:239], a[176:179], v[132:135]\n"
    "v_mfma_f32_16x16x32_bf16 v[132:135], a[232:235], a[144:147], v[132:135]\n"
    "v_mfma_f32_16x16x32_bf16 v[100:103], a[232:235], a[140:143], v[100:103]\n"
    "v_mfma_f32_16x16x32_bf16 v[100:103], a[236:239], a[172:175], v[100:103]\n"
    "v_mfma_f32_16x16x32_bf16 v[68:71], a[236:239], a[168:171], v[68:71]\n"
    "v_mfma_f32_16x16x32_bf16 v[68:71], a[232:235], a[136:139], v[68:71]\n"
    "v_mfma_f32_16x16x32_bf16 v[36:39], a[232:235], a[132:135], v[36:39]\n"
    "v_mfma_f32_16x16x32_bf16 v[36:39], a[236:239], a[164:167], v[36:39]\n"
    "v_mfma_f32_16x16x32_bf16 v[4:7], a[236:239], a[160:163], v[4:7]\n"
    "v_mfma_f32_16x16x32_bf16 v[4:7], a[232:235], a[128:131], v[4:7]\n"
    "s_waitcnt vmcnt(12)\n"
    "s_getreg_b32 s73, hwreg(HW_REG_IB_STS, 8, 4)\n"
    "s_add_u32 s83, s83, s73\n"
    "v_mfma_f32_16x16x32_bf16 v[8:11], a[192:195], a[128:131], v[8:11]\n"
    "buffer_load_dwordx4 a[232:235], v240, s[20:23], 0 offen offset:2048 sc1\n"
    "v_mfma_f32_16x16x32_bf16 v[8:11], a[196:199], a[160:163], v[8:11]\n"
    "buffer_load_dwordx4 a[236:239], v240, s[20:23], 0 offen offset:3072 sc1\n"
    "s_addk_i32 s20, 4096\n"
    "v_mfma_f32_16x16x32_bf16 v[40:43], a[196:199], a[164:167], v[40:43]\n"
    "buffer_load_dword v253, v240, s[36:39], s55 offen offset:0 sc1\n"
    "v_mfma_f32_16x16x32_bf16 v[40:43], a[192:195], a[132:135], v[40:43]\n"
    "buffer_load_dword v252, v240, s[36:39], s55 offen offset:1024 sc1\n"
    "v_mfma_f32_16x16x32_bf16 v[72:75], a[192:195], a[136:139], v[72:75]\n"
    "s_getreg_b32 s73, hwreg(HW_REG_IB_STS, 8, 4)\n"
    "s_add_u32 s83, s83, s73\n"
    "s_getreg_b32 s73, hwreg(HW_REG_IB_STS, 8, 4)\n"
    "v_mfma_f32_16x16x32_bf16 v[72:75], a[196:199], a[168:171], v[72:75]\n"
    "s_add_u32 s83, s83, s73\n"
    "s_getreg_b32 s73, hwreg(HW_REG_IB_STS, 8, 4)\n"
    "s_add_u32 s83, s83, s73\n"
    "v_mfma_f32_16x16x32_bf16 v[104:107], a[196:199], a[172:175], v[104:107]\n"
    "v_mfma_f32_16x16x32_bf16 v[104:107], a[192:195], a[140:143], v[104:107]\n"
    "s_getreg_b32 s73, hwreg(HW_REG_IB_STS, 8, 4)\n"
    "s_add_u32 s83, s83, s73\n"
    "v_mfma_f32_16x16x32_bf16 v[136:139], a[192:195], a[144:147], v[136:139]\n"
    "v_mfma_f32_16x16x32_bf16 v[136:139], a[196:199], a[176:179], v[136:139]\n"
    "s_getreg_b32 s73, hwreg(HW_REG_IB_STS, 8, 4)\n"
    "s_add_u32 s83, s83, s73\n"
    "v_mfma_f32_16x16x32_bf16 v[168:171], a[196:199], a[180:183], v[168:171]\n"
    "v_mfma_f32_16x16x32_bf16 v[168:171], a[192:195], a[148:151], v[168:171]\n"
    "s_getreg_b32 s73, hwreg(HW_REG_IB_STS, 8, 4)\n"
    "s_add_u32 s83, s83, s73\n"
    "v_mfma_f32_16x16x32_bf16 v[200:203], a[192:195], a[152:155], v[200:203]\n"
    "v_mfma_f32_16x16x32_bf16 v[200:203], a[196:199], a[184:187], v[200:203]\n"
    "s_getreg_b32 s73, hwreg(HW_REG_IB_STS, 8, 4)\n"
    "s_add_u32 s83, s83, s73\n"
    "v_mfma_f32_16x16x32_bf16 v[232:235], a[196:199], a[188:191], v[232:235]\n"
    "v_mfma_f32_16x16x32_bf16 v[232:235], a[192:195], a[156:159], v[232:235]\n"
    "s_waitcnt vmcnt(14)\n"
    "s_getreg_b32 s73, hwreg(HW_REG_IB_STS, 8, 4)\n"
    "s_add_u32 s83, s83, s73\n"
    "v_mfma_f32_16x16x32_bf16 v[236:239], a[200:203], a[156:159], v[236:239]\n"
    "buffer_load_dwordx4 a[192:195], v240, s[20:23], 0 offen offset:0 sc1\n"
    "v_mfma_f32_16x16x32_bf16 v[236:239], a[204:207], a[188:191], v[236:239]\n"
    "buffer_load_dwordx4 a[196:199], v240, s[20:23], 0 offen offset:1024 sc1\n"
    "s_getreg_b32 s73, hwreg(HW_REG_IB_STS, 8, 4)\n"
    "v_mfma_f32_16x16x32_bf16 v[204:207], a[204:207], a[184:187], v[204:207]\n"
    "s_add_u32 s83, s83, s73\n"
    "ds_read_b128 a[156:159], v241 offset:30720\n"
    "v_mfma_f32_16x16x32_bf16 v[204:207], a[200:203], a[152:155], v[204:207]\n"
    "s_getreg_b32 s73, hwreg(HW_REG_IB_STS, 8, 4)\n"
    "s_add_u32 s83, s83, s73\n"
    "ds_read_b128 a[188:191], v242 offset:30720\n"
    "v_mfma_f32_16x16x32_bf16 v[172:175], a[200:203], a[148:151], v[172:175]\n"
    "ds_read_b128 a[184:187], v242 offset:28672\n"
    "v_mfma_f32_16x16x32_bf16 v[172:175], a[204:207], a[180:183], v[172:175]\n"
    "s_getreg_b32 s73, hwreg(HW_REG_IB_STS, 8, 4)\n"
    "s_add_u32 s83, s83, s73\n"
    "ds_read_b128 a[152:155], v241 offset:28672\n"
    "v_mfma_f32_16x16x32_bf16 v[140:143], a[204:207], a[176:179], v[140:143]\n"
    "ds_read_b128 a[148:151], v241 offset:26624\n"
    "v_mfma_f32_16x16x32_bf16 v[140:143], a[200:203], a[144:147], v[140:143]\n"
    "s_getreg_b32 s73, hwreg(HW_REG_IB_STS, 8, 4)\n"
    "s_add_u32 s83, s83, s73\n"
    "ds_read_b128 a[180:183], v242 offset:26624\n"
    "v_mfma_f32_16x16x32_bf16 v[108:111], a[200:203], a[140:143], v[108:111]\n"
    "ds_read_b128 a[176:179], v242 offset:24576\n"
    "v_mfma_f32_16x16x32_bf16 v[108:111], a[204:207], a[172:175], v[108:111]\n"
    "s_getreg_b32 s73, hwreg(HW_REG_IB_STS, 8, 4)\n"
    "s_add_u32 s83, s83, s73\n"
    "ds_read_b128 a[144:147], v241 offset:24576\n"
    "v_mfma_f32_16x16x32_bf16 v[76:79], a[204:207], a[168:171], v[76:79]\n"
    "ds_read_b128 a[140:143], v241 offset:22528\n"
    "v_mfma_f32_16x16x32_bf16 v[76:79], a[200:203], a[136:139], v[76:79]\n"
    "s_getreg_b32 s73, hwreg(HW_REG_IB_STS, 8, 4)\n"
    "s_add_u32 s83, s83, s73\n"
    "ds_read_b128 a[172:175], v242 offset:22528\n"
    "v_mfma_f32_16x16x32_bf16 v[44:47], a[200:203], a[132:135], v[44:47]\n"
    "ds_read_b128 a[168:171], v242 offset:20480\n"
    "v_mfma_f32_16x16x32_bf16 v[44:47], a[204:207], a[164:167], v[44:47]\n"
    "s_getreg_b32 s73, hwreg(HW_REG_IB_STS, 8, 4)\n"
    "s_add_u32 s83, s83, s73\n"
    "ds_read_b128 a[136:139], v241 offset:20480\n"
    "v_mfma_f32_16x16x32_bf16 v[12:15], a[204:207], a[160:163], v[12:15]\n"
    "ds_read_b128 a[132:135], v241 offset:18432\n"
    "v_mfma_f32_16x16x32_bf16 v[12:15], a[200:203], a[128:131], v[12:15]\n"
    "s_waitcnt vmcnt(14) lgkmcnt(11)\n"
    "ds_read_b128 a[164:167], v242 offset:18432\n"
    "v_mfma_f32_16x16x32_bf16 v[224:227], a[208:211], a[156:159], v[224:227]\n"
    "ds_read_b128 a[160:163], v242 offset:16384\n"
    "v_mfma_f32_16x16x32_bf16 v[224:227], a[212:215], a[188:191], v[224:227]\n"
    "s_waitcnt lgkmcnt(11)\n"
    "ds_read_b128 a[128:131], v241 offset:16384\n"
    "v_mfma_f32_16x16x32_bf16 v[192:195], a[212:215], a[184:187], v[192:195]\n"
    "buffer_load_dwordx4 a[200:203], v240, s[20:23], 0 offen offset:2048 sc1\n"
    "v_mfma_f32_16x16x32_bf16 v[192:195], a[208:211], a[152:155], v[192:195]\n"
    "s_waitcnt lgkmcnt(10)\n"
    "buffer_load_dwordx4 a[204:207], v240, s[20:23], 0 offen offset:3072 sc1\n"
    "s_addk_i32 s20, 4096\n"
    "v_mfma_f32_16x16x32_bf16 v[160:163], a[208:211], a[148:151], v[160:163]\n"
    "v_mfma_f32_16x16x32_bf16 v[160:163], a[212:215], a[180:183], v[160:163]\n"
    "s_waitcnt lgkmcnt(8)\n"
    "s_bitcmp1_b32 s65, 0\n"
    "s_cselect_b32 s60, s82, s83\n"
    "v_mfma_f32_16x16x32_bf16 v[128:131], a[212:215], a[176:179], v[128:131]\n"
    "s_cselect_b32 s61, s83, s82\n"
    "v_mov_b32 v246, s60\n"
    "v_mov_b32 v247, s61\n"
    "v_mfma_f32_16x16x32_bf16 v[128:131], a[208:211], a[144:147], v[128:131]\n"
    "s_waitcnt lgkmcnt(6)\n"
    "s_lshl_b32 s60, s65, 3\n"
    "s_add_u32 s60, s60, %2\n"
    "v_mfma_f32_16x16x32_bf16 v[96:99], a[208:211], a[140:143], v[96:99]\n"
    "s_add_u32 s60, s60, 131072\n"
    "v_mov_b32 v248, s60\n"
    "v_mfma_f32_16x16x32_bf16 v[96:99], a[212:215], a[172:175], v[96:99]\n"
    "s_waitcnt lgkmcnt(4)\n"
    "v_mfma_f32_16x16x32_bf16 v[64:67], a[212:215], a[168:171], v[64:67]\n"
    "ds_write_b64 v248, v[246:247]\n"
    "v_mfma_f32_16x16x32_bf16 v[64:67], a[208:211], a[136:139], v[64:67]\n"
    "s_waitcnt lgkmcnt(3)\n"
    "v_mfma_f32_16x16x32_bf16 v[32:35], a[208:211], a[132:135], v[32:35]\n"
    "v_mfma_f32_16x16x32_bf16 v[32:35], a[212:215], a[164:167], v[32:35]\n"
    "s_bitcmp1_b32 s85, 13\n"
    "s_cbranch_scc1 CP13_%=\n"
    "CR13_%=:\n"
    "s_waitcnt lgkmcnt(1)\n"
    "v_mfma_f32_16x16x32_bf16 v[0:3], a[212:215], a[160:163], v[0:3]\n"
    "v_mfma_f32_16x16x32_bf16 v[0:3], a[208:211], a[128:131], v[0:3]\n"
    "s_waitcnt vmcnt(10)\n"
    "buffer_load_dwordx4 a[208:211], v240, s[20:23], 0 offen offset:0 sc1\n"
    "v_mfma_f32_16x16x32_bf16 v[4:7], a[216:219], a[128:131], v[4:7]\n"
    "buffer_load_dwordx4 a[212:215], v240, s[20:23], 0 offen offset:1024 sc1\n"
    "s_lshr_b32 s60, s65, 1\n"
    "v_mfma_f32_16x16x32_bf16 v[4:7], a[220:223], a[160:163], v[4:7]\n"
    "s_lshl_b32 s60, s60, 14\n"
    "s_and_b32 s61, s65, 1\n"
    "s_lshl_b32 s61, s61, 10\n"
    "v_mfma_f32_16x16x32_bf16 v[36:39], a[220:223], a[164:167], v[36:39]\n"
    "s_add_u32 s62, s60, s61\n"
    "s_add_u32 s62, s62, %2\n"
    "v_add_u32 v243, s62, v240\n"
    "v_mfma_f32_16x16x32_bf16 v[36:39], a[216:219], a[132:135], v[36:39]\n"
    "s_mov_b32 s71, s62\n"
    "s_add_u32 s56, s62, 131072\n"
    "s_lshl_b32 s60, s60, 1\n"
    "s_add_u32 s60, s60, s61\n"
    "s_add_u32 s32, s32, s60\n"
    "s_mov_b32 s94, 65536\n"
    "s_mov_b32 s95, s35\n"
    "s_mov_b64 s[98:99], s[94:95]\n"
    "s_lshr_b32 s60, %3, 4\n"
    "s_lshl_b32 s60, s60, 22\n"
    "s_and_b32 s61, %3, 7\n"
    "s_lshl_b32 s61, s61, 19\n"
    "s_add_u32 s54, s60, s61\n"
    "s_add_u32 s54, s54, 134217728\n"
    "s_bitcmp1_b32 %3, 3\n"
    "s_cselect_b32 s60, 0x40000, 0\n"
    "s_xor_b32 s54, s54, s60\n"
    "s_xor_b32 s59, s54, 0x40000\n"
    "s_mov_b64 s[24:25], s[12:13]\n"
    "s_add_u32 s24, s24, 266240\n"
    "s_addc_u32 s25, s25, 0\n"
    "s_and_b32 s25, s25, 0xffff\n"
    "s_mov_b32 s26, 1024\n"
    "s_mov_b32 s66, %2\n"
    "s_add_u32 s57, %2, 65536\n"
    "s_add_u32 s67, %2, 131072\n"
    "v_mfma_f32_16x16x32_bf16 v[68:71], a[216:219], a[136:139], v[68:71]\n"
    "v_mfma_f32_16x16x32_bf16 v[68:71], a[220:223], a[168:171], v[68:71]\n"
    "v_mfma_f32_16x16x32_bf16 v[100:103], a[220:223], a[172:175], v[100:103]\n"
    "v_mfma_f32_16x16x32_bf16 v[100:103], a[216:219], a[140:143], v[100:103]\n"
    "v_mfma_f32_16x16x32_bf16 v[132:135], a[216:219], a[144:147], v[132:135]\n"
    "v_mfma_f32_16x16x32_bf16 v[132:135], a[220:223], a[176:179], v[132:135]\n"
    "v_mfma_f32_16x16x32_bf16 v[164:167], a[220:223], a[180:183], v[164:167]\n"
    "v_mfma_f32_16x16x32_bf16 v[164:167], a[216:219], a[148:151], v[164:167]\n"
    "v_mfma_f32_16x16x32_bf16 v[196:199], a[216:219], a[152:155], v[196:199]\n"
    "v_mfma_f32_16x16x32_bf16 v[196:199], a[220:223], a[184:187], v[196:199]\n"
    "v_mfma_f32_16x16x32_bf16 v[228:231], a[220:223], a[188:191], v[228:231]\n"
    "v_mfma_f32_16x16x32_bf16 v[228:231], a[216:219], a[156:159], v[228:231]\n"
    "s_waitcnt vmcnt(10)\n"
    "buffer_load_dwordx4 a[216:219], v240, s[20:23], 0 offen offset:2048 sc1\n"
    "v_mfma_f32_16x16x32_bf16 v[232:235], a[224:227], a[156:159], v[232:235]\n"
    "buffer_load_dwordx4 a[220:223], v240, s[20:23], 0 offen offset:3072 sc1\n"
    "s_addk_i32 s20, 4096\n"
    "v_mfma_f32_16x16x32_bf16 v[232:235], a[228:231], a[188:191], v[232:235]\n"
    "v_mfma_f32_16x16x32_bf16 v[200:203], a[228:231], a[184:187], v[200:203]\n"
    "v_mfma_f32_16x16x32_bf16 v[200:203], a[224:227], a[152:155], v[200:203]\n"
    "v_mfma_f32_16x16x32_bf16 v[168:171], a[224:227], a[148:151], v[168:171]\n"
    "v_mfma_f32_16x16x32_bf16 v[168:171], a[228:231], a[180:183], v[168:171]\n"
    "v_mfma_f32_16x16x32_bf16 v[136:139], a[228:231], a[176:179], v[136:139]\n"
    "v_mfma_f32_16x16x32_bf16 v[136:139], a[224:227], a[144:147], v[136:139]\n"
    "v_mfma_f32_16x16x32_bf16 v[104:107], a[224:227], a[140:143], v[104:107]\n"
    "v_mfma_f32_16x16x32_bf16 v[104:107], a[228:231], a[172:175], v[104:107]\n"
    "v_mfma_f32_16x16x32_bf16 v[72:75], a[228:231], a[168:171], v[72:75]\n"
    "v_mfma_f32_16x16x32_bf16 v[72:75], a[224:227], a[136:139], v[72:75]\n"
    "v_mfma_f32_16x16x32_bf16 v[40:43], a[224:227], a[132:135], v[40:43]\n"
    "v_mfma_f32_16x16x32_bf16 v[40:43], a[228:231], a[164:167], v[40:43]\n"
    "s_waitcnt lgkmcnt(0)\n"
    "s_barrier\n"
    "v_mfma_f32_16x16x32_bf16 v[8:11], a[228:231], a[160:163], v[8:11]\n"
    "v_mfma_f32_16x16x32_bf16 v[8:11], a[224:227], a[128:131], v[8:11]\n"
    "s_waitcnt vmcnt(10)\n"
    "buffer_load_dwordx4 a[224:227], v240, s[20:23], 0 offen offset:0 sc1\n"
    "v_mfma_f32_16x16x32_bf16 v[12:15], a[232:235], a[128:131], v[12:15]\n"
    "buffer_load_dwordx4 a[228:231], v240, s[20:23], 0 offen offset:1024 sc1\n"
    "v_mfma_f32_16x16x32_bf16 v[12:15], a[236:239], a[160:163], v[12:15]\n"
    "v_mfma_f32_16x16x32_bf16 v[44:47], a[236:239], a[164:167], v[44:47]\n"
    "ds_read_b128 a[128:131], v241 offset:49152\n"
    "v_mfma_f32_16x16x32_bf16 v[44:47], a[232:235], a[132:135], v[44:47]\n"
    "ds_read_b128 a[160:163], v242 offset:49152\n"
    "v_mfma_f32_16x16x32_bf16 v[76:79], a[232:235], a[136:139], v[76:79]\n"
    "ds_read_b128 a[164:167], v242 offset:51200\n"
    "v_mfma_f32_16x16x32_bf16 v[76:79], a[236:239], a[168:171], v[76:79]\n"
    "ds_read_b128 a[132:135], v241 offset:51200\n"
    "v_mfma_f32_16x16x32_bf16 v[108:111], a[236:239], a[172:175], v[108:111]\n"
    "ds_read_b128 a[136:139], v241 offset:53248\n"
    "v_mfma_f32_16x16x32_bf16 v[108:111], a[232:235], a[140:143], v[108:111]\n"
    "ds_read_b128 a[168:171], v242 offset:53248\n"
    "v_mfma_f32_16x16x32_bf16 v[140:143], a[232:235], a[144:147], v[140:143]\n"
    "ds_read_b128 a[172:175], v242 offset:55296\n"
    "v_mfma_f32_16x16x32_bf16 v[140:143], a[236:239], a[176:179], v[140:143]\n"
    "ds_read_b128 a[140:143], v241 offset:55296\n"
    "v_mfma_f32_16x16x32_bf16 v[172:175], a[236:239], a[180:183], v[172:175]\n"
    "ds_read_b128 a[144:147], v241 offset:57344\n"
    "v_mfma_f32_16x16x32_bf16 v[172:175], a[232:235], a[148:151], v[172:175]\n"
    "ds_read_b128 a[176:179], v242 offset:57344\n"
    "v_mfma_f32_16x16x32_bf16 v[204:207], a[232:235], a[152:155], v[204:207]\n"
    "ds_read_b128 a[180:183], v242 offset:59392\n"
    "v_mfma_f32_16x16x32_bf16 v[204:207], a[236:239], a[184:187], v[204:207]\n"
    "ds_read_b128 a[148:151], v241 offset:59392\n"
    "v_mfma_f32_16x16x32_bf16 v[236:239], a[236:239], a[188:191], v[236:239]\n"
    "ds_read_b128 a[152:155], v241 offset:61440\n"
    "v_mfma_f32_16x16x32_bf16 v[236:239], a[232:235], a[156:159], v[236:239]\n"
    "s_waitcnt vmcnt(8) lgkmcnt(11)\n"
    "ds_read_b128 a[184:187], v242 offset:61440\n"
    "v_mfma_f32_16x16x32_bf16 v[0:3], a[192:195], a[128:131], v[0:3]\n"
    "ds_read_b128 a[188:191], v242 offset:63488\n"
    "v_mfma_f32_16x16x32_bf16 v[0:3], a[196:199], a[160:163], v[0:3]\n"
    "s_waitcnt lgkmcnt(11)\n"
    "ds_read_b128 a[156:159], v241 offset:63488\n"
    "v_mfma_f32_16x16x32_bf16 v[32:35], a[196:199], a[164:167], v[32:35]\n"
    "buffer_load_dwordx4 a[232:235], v240, s[20:23], 0 offen offset:2048 sc1\n"
    "v_mfma_f32_16x16x32_bf16 v[32:35], a[192:195], a[132:135], v[32:35]\n"
    "s_waitcnt lgkmcnt(10)\n"
    "buffer_load_dwordx4 a[236:239], v240, s[20:23], 0 offen offset:3072 sc1\n"
    "s_addk_i32 s20, 4096\n"
    "v_mfma_f32_16x16x32_bf16 v[64:67], a[192:195], a[136:139], v[64:67]\n"
    "v_mfma_f32_16x16x32_bf16 v[64:67], a[196:199], a[168:171], v[64:67]\n"
    "s_waitcnt lgkmcnt(8)\n"
    "v_mfma_f32_16x16x32_bf16 v[96:99], a[196:199], a[172:175], v[96:99]\n"
    "v_mfma_f32_16x16x32_bf16 v[96:99], a[192:195], a[140:143], v[96:99]\n"
    "s_waitcnt lgkmcnt(6)\n"
    "v_mfma_f32_16x16x32_bf16 v[128:131], a[192:195], a[144:147], v[128:131]\n"
    "v_mfma_f32_16x16x32_bf16 v[128:131], a[196:199], a[176:179], v[128:131]\n"
    "s_waitcnt lgkmcnt(4)\n"
    "v_mfma_f32_16x16x32_bf16 v[160:163], a[196:199], a[180:183], v[160:163]\n"
    "v_mfma_f32_16x16x32_bf16 v[160:163], a[192:195], a[148:151], v[160:163]\n"
    "s_waitcnt lgkmcnt(2)\n"
    "v_mfma_f32_16x16x32_bf16 v[192:195], a[192:195], a[152:155], v[192:195]\n"
    "v_mfma_f32_16x16x32_bf16 v[192:195], a[196:199], a[184:187], v[192:195]\n"
    "s_bitcmp1_b32 s85, 14\n"
    "s_cbranch_scc1 CP14_%=\n"
    "CR14_%=:\n"
    "s_waitcnt lgkmcnt(0)\n"
    "v_mfma_f32_16x16x32_bf16 v[224:227], a[196:199], a[188:191], v[224:227]\n"
    "v_mfma_f32_16x16x32_bf16 v[224:227], a[192:195], a[156:159], v[224:227]\n"
    "s_waitcnt vmcnt(8)\n"
    "buffer_load_dwordx4 a[192:195], v240, s[20:23], 0 offen offset:0 sc1\n"
    "v_mfma_f32_16x16x32_bf16 v[228:231], a[200:203], a[156:159], v[228:231]\n"
    "buffer_load_dwordx4 a[196:199], v240, s[20:23], 0 offen offset:1024 sc1\n"
    "s_add_u32 s60, %2, 131072\n"
    "v_mfma_f32_16x16x32_bf16 v[228:231], a[204:207], a[188:191], v[228:231]\n"
    "v_mov_b32 v244, s60\n"
    "v_mfma_f32_16x16x32_bf16 v[196:199], a[204:207], a[184:187], v[196:199]\n"
    "ds_read_b128 v[248:251], v244\n"
    "ds_read_b128 v[244:247], v244 offset:16\n"
    "v_mfma_f32_16x16x32_bf16 v[196:199], a[200:203], a[152:155], v[196:199]\n"
    "v_mfma_f32_16x16x32_bf16 v[164:167], a[200:203], a[148:151], v[164:167]\n"
    "v_mfma_f32_16x16x32_bf16 v[164:167], a[204:207], a[180:183], v[164:167]\n"
    "v_mfma_f32_16x16x32_bf16 v[132:135], a[204:207], a[176:179], v[132:135]\n"
    "v_mfma_f32_16x16x32_bf16 v[132:135], a[200:203], a[144:147], v[132:135]\n"
    "v_mfma_f32_16x16x32_bf16 v[100:103], a[200:203], a[140:143], v[100:103]\n"
    "v_mfma_f32_16x16x32_bf16 v[100:103], a[204:207], a[172:175], v[100:103]\n"
    "v_mfma_f32_16x16x32_bf16 v[68:71], a[204:207], a[168:171], v[68:71]\n"
    "v_mfma_f32_16x16x32_bf16 v[68:71], a[200:203], a[136:139], v[68:71]\n"
    "v_mfma_f32_16x16x32_bf16 v[36:39], a[200:203], a[132:135], v[36:39]\n"
    "v_mfma_f32_16x16x32_bf16 v[36:39], a[204:207], a[164:167], v[36:39]\n"
    "v_mfma_f32_16x16x32_bf16 v[4:7], a[204:207], a[160:163], v[4:7]\n"
    "v_mfma_f32_16x16x32_bf16 v[4:7], a[200:203], a[128:131], v[4:7]\n"
    "s_waitcnt vmcnt(8)\n"
    "buffer_load_dwordx4 a[200:203], v240, s[20:23], 0 offen offset:2048 sc1\n"
    "v_mfma_f32_16x16x32_bf16 v[8:11], a[208:211], a[128:131], v[8:11]\n"
    "buffer_load_dwordx4 a[204:207], v240, s[20:23], 0 offen offset:3072 sc1\n"
    "s_addk_i32 s20, 4096\n"
    "v_mfma_f32_16x16x32_bf16 v[8:11], a[212:215], a[160:163], v[8:11]\n"
    "s_waitcnt lgkmcnt(0)\n"
    "v_add_u32 v248, v248, v250\n"
    "v_add_u32 v249, v249, v251\n"
    "v_mfma_f32_16x16x32_bf16 v[40:43], a[212:215], a[164:167], v[40:43]\n"
    "v_add_u32 v244, v244, v246\n"
    "v_add_u32 v245, v245, v247\n"
    "v_readfirstlane_b32 s72, v248\n"
    "v_readfirstlane_b32 s73, v249\n"
    "v_readfirstlane_b32 s74, v244\n"
    "v_readfirstlane_b32 s75, v245\n"
    "v_mfma_f32_16x16x32_bf16 v[40:43], a[208:211], a[132:135], v[40:43]\n"
    "s_add_u32 s60, s72, s74\n"
    "s_add_u32 s61, s73, s75\n"
    "s_cmp_lt_u32 s61, s60\n"
    "s_cselect_b32 s64, 1, 0\n"
    "s_cselect_b32 s72, s73, s72\n"
    "s_cselect_b32 s74, s75, s74\n"
    "s_lshl_b32 s62, s64, 13\n"
    "s_xor_b32 s54, s54, s62\n"
    "s_sub_i32 s73, s72, s74\n"
    "s_sub_i32 s75, s74, s72\n"
    "s_bitcmp1_b32 %3, 0\n"
    "s_cselect_b32 s73, s75, s73\n"
    "s_lshl_b32 s75, s73, 1\n"
    "s_addk_i32 s75, 4097\n"
    "s_sub_i32 s72, s60, s61\n"
    "s_min_i32 s72, s72, 255\n"
    "s_max_i32 s72, s72, -255\n"
    "s_addk_i32 s72, 256\n"
    "s_lshl_b32 s72, s72, 20\n"
    "s_or_b32 s75, s75, s72\n"
    "v_mov_b32 v248, s75\n"
    "s_add_u32 s26, s77, 4\n"
    "s_and_b32 s26, s26, s86\n"
    "v_mfma_f32_16x16x32_bf16 v[72:75], a[208:211], a[136:139], v[72:75]\n"
    "buffer_store_dword v248, v240, s[24:27], s77 offen sc1\n"
    "s_movk_i32 s26, 1024\n"
    "v_mfma_f32_16x16x32_bf16 v[72:75], a[212:215], a[168:171], v[72:75]\n"
    "v_mfma_f32_16x16x32_bf16 v[104:107], a[212:215], a[172:175], v[104:107]\n"
    "v_mfma_f32_16x16x32_bf16 v[104:107], a[208:211], a[140:143], v[104:107]\n"
    "v_mfma_f32_16x16x32_bf16 v[136:139], a[208:211], a[144:147], v[136:139]\n"
    "v_mfma_f32_16x16x32_bf16 v[136:139], a[212:215], a[176:179], v[136:139]\n"
    "v_mfma_f32_16x16x32_bf16 v[168:171], a[212:215], a[180:183], v[168:171]\n"
    "v_mfma_f32_16x16x32_bf16 v[168:171], a[208:211], a[148:151], v[168:171]\n"
    "v_mfma_f32_16x16x32_bf16 v[200:203], a[208:211], a[152:155], v[200:203]\n"
    "v_mfma_f32_16x16x32_bf16 v[200:203], a[212:215], a[184:187], v[200:203]\n"
    "s_barrier\n"
    "v_mfma_f32_16x16x32_bf16 v[232:235], a[212:215], a[188:191], v[232:235]\n"
    "v_mfma_f32_16x16x32_bf16 v[232:235], a[208:211], a[156:159], v[232:235]\n"
    "s_waitcnt vmcnt(9)\n"
    "buffer_load_dwordx4 a[208:211], v240, s[20:23], 0 offen offset:0 sc1\n"
    "v_mfma_f32_16x16x32_bf16 v[236:239], a[216:219], a[156:159], v[236:239]\n"
    "buffer_load_dwordx4 a[212:215], v240, s[20:23], 0 offen offset:1024 sc1\n"
    "v_mfma_f32_16x16x32_bf16 v[236:239], a[220:223], a[188:191], v[236:239]\n"
    "v_mfma_f32_16x16x32_bf16 v[204:207], a[220:223], a[184:187], v[204:207]\n"
    "ds_read_b128 a[156:159], v241 offset:47104\n"
    "v_mfma_f32_16x16x32_bf16 v[204:207], a[216:219], a[152:155], v[204:207]\n"
    "ds_read_b128 a[188:191], v242 offset:47104\n"
    "v_mfma_f32_16x16x32_bf16 v[172:175], a[216:219], a[148:151], v[172:175]\n"
    "ds_read_b128 a[184:187], v242 offset:45056\n"
    "v_mfma_f32_16x16x32_bf16 v[172:175], a[220:223], a[180:183], v[172:175]\n"
    "ds_read_b128 a[152:155], v241 offset:45056\n"
    "v_mfma_f32_16x16x32_bf16 v[140:143], a[220:223], a[176:179], v[140:143]\n"
    "ds_read_b128 a[148:151], v241 offset:43008\n"
    "v_mfma_f32_16x16x32_bf16 v[140:143], a[216:219], a[144:147], v[140:143]\n"
    "ds_read_b128 a[180:183], v242 offset:43008\n"
    "v_mfma_f32_16x16x32_bf16 v[108:111], a[216:219], a[140:143], v[108:111]\n"
    "ds_read_b128 a[176:179], v242 offset:40960\n"
    "v_mfma_f32_16x16x32_bf16 v[108:111], a[220:223], a[172:175], v[108:111]\n"
    "ds_read_b128 a[144:147], v241 offset:40960\n"
    "v_mfma_f32_16x16x32_bf16 v[76:79], a[220:223], a[168:171], v[76:79]\n"
    "ds_read_b128 a[140:143], v241 offset:38912\n"
    "v_mfma_f32_16x16x32_bf16 v[76:79], a[216:219], a[136:139], v[76:79]\n"
    "ds_read_b128 a[172:175], v242 offset:38912\n"
    "v_mfma_f32_16x16x32_bf16 v[44:47], a[216:219], a[132:135], v[44:47]\n"
    "ds_read_b128 a[168:171], v242 offset:36864\n"
    "v_mfma_f32_16x16x32_bf16 v[44:47], a[220:223], a[164:167], v[44:47]\n"
    "ds_read_b128 a[136:139], v241 offset:36864\n"
    "v_mfma_f32_16x16x32_bf16 v[12:15], a[220:223], a[160:163], v[12:15]\n"
    "ds_read_b128 a[132:135], v241 offset:34816\n"
    "v_mfma_f32_16x16x32_bf16 v[12:15], a[216:219], a[128:131], v[12:15]\n"
    "s_waitcnt vmcnt(9) lgkmcnt(11)\n"
    "ds_read_b128 a[164:167], v242 offset:34816\n"
    "v_mfma_f32_16x16x32_bf16 v[224:227], a[224:227], a[156:159], v[224:227]\n"
    "ds_read_b128 a[160:163], v242 offset:32768\n"
    "v_mfma_f32_16x16x32_bf16 v[224:227], a[228:231], a[188:191], v[224:227]\n"
    "s_waitcnt lgkmcnt(11)\n"
    "ds_read_b128 a[128:131], v241 offset:32768\n"
    "v_mfma_f32_16x16x32_bf16 v[192:195], a[228:231], a[184:187], v[192:195]\n"
    "buffer_load_dwordx4 a[216:219], v240, s[20:23], 0 offen offset:2048 sc1\n"
    "v_mfma_f32_16x16x32_bf16 v[192:195], a[224:227], a[152:155], v[192:195]\n"
    "s_waitcnt lgkmcnt(10)\n"
    "buffer_load_dwordx4 a[220:223], v240, s[20:23], 0 offen offset:3072 sc1\n"
    "s_addk_i32 s20, 4096\n"
    "v_mfma_f32_16x16x32_bf16 v[160:163], a[224:227], a[148:151], v[160:163]\n"
    "v_mfma_f32_16x16x32_bf16 v[160:163], a[228:231], a[180:183], v[160:163]\n"
    "s_waitcnt lgkmcnt(8)\n"
    "v_mfma_f32_16x16x32_bf16 v[128:131], a[228:231], a[176:179], v[128:131]\n"
    "v_mfma_f32_16x16x32_bf16 v[128:131], a[224:227], a[144:147], v[128:131]\n"
    "s_waitcnt lgkmcnt(6)\n"
    "v_mfma_f32_16x16x32_bf16 v[96:99], a[224:227], a[140:143], v[96:99]\n"
    "v_mfma_f32_16x16x32_bf16 v[96:99], a[228:231], a[172:175], v[96:99]\n"
    "s_waitcnt lgkmcnt(4)\n"
    "v_mfma_f32_16x16x32_bf16 v[64:67], a[228:231], a[168:171], v[64:67]\n"
    "v_mfma_f32_16x16x32_bf16 v[64:67], a[224:227], a[136:139], v[64:67]\n"
    "s_waitcnt lgkmcnt(2)\n"
    "v_mfma_f32_16x16x32_bf16 v[32:35], a[224:227], a[132:135], v[32:35]\n"
    "v_mfma_f32_16x16x32_bf16 v[32:35], a[228:231], a[164:167], v[32:35]\n"
    "s_bitcmp1_b32 s85, 15\n"
    "s_cbranch_scc1 CP15_%=\n"
    "CR15_%=:\n"
    "s_waitcnt lgkmcnt(0)\n"
    "v_mfma_f32_16x16x32_bf16 v[0:3], a[228:231], a[160:163], v[0:3]\n"
    "v_mfma_f32_16x16x32_bf16 v[0:3], a[224:227], a[128:131], v[0:3]\n"
    "s_waitcnt vmcnt(9)\n"
    "buffer_load_dwordx4 a[224:227], v240, s[20:23], 0 offen offset:0 sc1\n"
    "v_mfma_f32_16x16x32_bf16 v[4:7], a[232:235], a[128:131], v[4:7]\n"
    "buffer_load_dwordx4 a[228:231], v240, s[20:23], 0 offen offset:1024 sc1\n"
    "v_mfma_f32_16x16x32_bf16 v[4:7], a[236:239], a[160:163], v[4:7]\n"
    "v_mfma_f32_16x16x32_bf16 v[36:39], a[236:239], a[164:167], v[36:39]\n"
    "v_mfma_f32_16x16x32_bf16 v[36:39], a[232:235], a[132:135], v[36:39]\n"
    "v_mfma_f32_16x16x32_bf16 v[68:71], a[232:235], a[136:139], v[68:71]\n"
    "v_mfma_f32_16x16x32_bf16 v[68:71], a[236:239], a[168:171], v[68:71]\n"
    "v_mfma_f32_16x16x32_bf16 v[100:103], a[236:239], a[172:175], v[100:103]\n"
    "v_mfma_f32_16x16x32_bf16 v[100:103], a[232:235], a[140:143], v[100:103]\n"
    "v_mfma_f32_16x16x32_bf16 v[132:135], a[232:235], a[144:147], v[132:135]\n"
    "v_mfma_f32_16x16x32_bf16 v[132:135], a[236:239], a[176:179], v[132:135]\n"
    "v_mfma_f32_16x16x32_bf16 v[164:167], a[236:239], a[180:183], v[164:167]\n"
    "v_accvgpr_read_b32 v245, a0\n"
    "v_accvgpr_read_b32 v247, a1\n"
    "v_mfma_f32_16x16x32_bf16 v[164:167], a[232:235], a[148:151], v[164:167]\n"
    "v_lshlrev_b32 v244, 16, v245\n"
    "v_lshlrev_b32 v246, 16, v247\n"
    "v_mfma_f32_16x16x32_bf16 v[196:199], a[232:235], a[152:155], v[196:199]\n"
    "v_and_b32 v245, s58, v245\n"
    "v_and_b32 v247, s58, v247\n"
    "v_mfma_f32_16x16x32_bf16 v[196:199], a[236:239], a[184:187], v[196:199]\n"
    "v_add_f32_dpp v254, v253, v0 row_newbcast:0 row_mask:0xf bank_mask:0xf\n"
    "v_add_f32_dpp v255, v253, v2 row_newbcast:2 row_mask:0xf bank_mask:0xf\n"
    "v_mfma_f32_16x16x32_bf16 v[228:231], a[236:239], a[188:191], v[228:231]\n"
    "v_mul_f32 v244, v244, v254\n"
    "v_mul_f32 v246, v246, v255\n"
    "v_mfma_f32_16x16x32_bf16 v[228:231], a[232:235], a[156:159], v[228:231]\n"
    "s_waitcnt vmcnt(9)\n"
    "buffer_load_dwordx4 a[232:235], v240, s[20:23], 0 offen offset:2048 sc1\n"
    "v_mfma_f32_16x16x32_bf16 v[232:235], a[192:195], a[156:159], v[232:235]\n"
    "buffer_load_dwordx4 a[236:239], v240, s[20:23], 0 offen offset:3072 sc1\n"
    "s_addk_i32 s20, 4096\n"
    "v_mfma_f32_16x16x32_bf16 v[232:235], a[196:199], a[188:191], v[232:235]\n"
    "v_add_f32_dpp v254, v253, v1 row_newbcast:1 row_mask:0xf bank_mask:0xf\n"
    "v_add_f32_dpp v255, v253, v3 row_newbcast:3 row_mask:0xf bank_mask:0xf\n"
    "v_mfma_f32_16x16x32_bf16 v[200:203], a[196:199], a[184:187], v[200:203]\n"
    "v_mul_f32 v245, v245, v254\n"
    "v_mul_f32 v247, v247, v255\n"
    "v_mfma_f32_16x16x32_bf16 v[200:203], a[192:195], a[152:155], v[200:203]\n"
    "v_cvt_pk_bf16_f32 v248, v244, v245\n"
    "v_cvt_pk_bf16_f32 v249, v246, v247\n"
    "v_mfma_f32_16x16x32_bf16 v[168:171], a[192:195], a[148:151], v[168:171]\n"
    "v_accvgpr_read_b32 v245, a2\n"
    "v_accvgpr_read_b32 v247, a3\n"
    "v_mfma_f32_16x16x32_bf16 v[168:171], a[196:199], a[180:183], v[168:171]\n"
    "v_lshlrev_b32 v244, 16, v245\n"
    "v_lshlrev_b32 v246, 16, v247\n"
    "v_mfma_f32_16x16x32_bf16 v[136:139], a[196:199], a[176:179], v[136:139]\n"
    "v_and_b32 v245, s58, v245\n"
    "v_and_b32 v247, s58, v247\n"
    "v_mfma_f32_16x16x32_bf16 v[136:139], a[192:195], a[144:147], v[136:139]\n"
    "s_xor_b32 s96, s54, 0x40000\n"
    "s_add_u32 s96, s96, s32\n"
    "s_addc_u32 s97, s33, 0\n"
    "v_mfma_f32_16x16x32_bf16 v[104:107], a[192:195], a[140:143], v[104:107]\n"
    "v_add_f32_dpp v254, v253, v4 row_newbcast:4 row_mask:0xf bank_mask:0xf\n"
    "v_add_f32_dpp v255, v253, v6 row_newbcast:6 row_mask:0xf bank_mask:0xf\n"
    "v_mfma_f32_16x16x32_bf16 v[104:107], a[196:199], a[172:175], v[104:107]\n"
    "v_mul_f32 v244, v244, v254\n"
    "v_mul_f32 v246, v246, v255\n"
    "v_mfma_f32_16x16x32_bf16 v[72:75], a[196:199], a[168:171], v[72:75]\n"
    "v_add_f32_dpp v254, v253, v5 row_newbcast:5 row_mask:0xf bank_mask:0xf\n"
    "v_add_f32_dpp v255, v253, v7 row_newbcast:7 row_mask:0xf bank_mask:0xf\n"
    "v_mfma_f32_16x16x32_bf16 v[72:75], a[192:195], a[136:139], v[72:75]\n"
    "v_mul_f32 v245, v245, v254\n"
    "v_mul_f32 v247, v247, v255\n"
    "v_mfma_f32_16x16x32_bf16 v[40:43], a[192:195], a[132:135], v[40:43]\n"
    "v_cvt_pk_bf16_f32 v250, v244, v245\n"
    "v_cvt_pk_bf16_f32 v251, v246, v247\n"
    "v_mfma_f32_16x16x32_bf16 v[40:43], a[196:199], a[164:167], v[40:43]\n"
    "v_accvgpr_read_b32 v245, a4\n"
    "v_accvgpr_read_b32 v247, a5\n"
    "v_mfma_f32_16x16x32_bf16 v[8:11], a[196:199], a[160:163], v[8:11]\n"
    "v_lshlrev_b32 v244, 16, v245\n"
    "v_lshlrev_b32 v246, 16, v247\n"
    "v_mfma_f32_16x16x32_bf16 v[8:11], a[192:195], a[128:131], v[8:11]\n"
    "s_waitcnt vmcnt(9)\n"
    "buffer_load_dwordx4 a[192:195], v240, s[20:23], 0 offen offset:0 sc1\n"
    "v_mfma_f32_16x16x32_bf16 v[12:15], a[200:203], a[128:131], v[12:15]\n"
    "buffer_load_dwordx4 a[196:199], v240, s[20:23], 0 offen offset:1024 sc1\n"
    "v_and_b32 v245, s58, v245\n"
    "v_mfma_f32_16x16x32_bf16 v[12:15], a[204:207], a[160:163], v[12:15]\n"
    "v_and_b32 v247, s58, v247\n"
    "v_add_f32_dpp v254, v253, v32 row_newbcast:0 row_mask:0xf bank_mask:0xf\n"
    "v_mfma_f32_16x16x32_bf16 v[44:47], a[204:207], a[164:167], v[44:47]\n"
    "v_add_f32_dpp v255, v253, v34 row_newbcast:2 row_mask:0xf bank_mask:0xf\n"
    "v_mul_f32 v244, v244, v254\n"
    "v_mfma_f32_16x16x32_bf16 v[44:47], a[200:203], a[132:135], v[44:47]\n"
    "v_mul_f32 v246, v246, v255\n"
    "v_add_f32_dpp v254, v253, v33 row_newbcast:1 row_mask:0xf bank_mask:0xf\n"
    "v_mfma_f32_16x16x32_bf16 v[76:79], a[200:203], a[136:139], v[76:79]\n"
    "v_add_f32_dpp v255, v253, v35 row_newbcast:3 row_mask:0xf bank_mask:0xf\n"
    "v_mul_f32 v245, v245, v254\n"
    "v_mfma_f32_16x16x32_bf16 v[76:79], a[204:207], a[168:171], v[76:79]\n"
    "v_mul_f32 v247, v247, v255\n"
    "v_mfma_f32_16x16x32_bf16 v[108:111], a[204:207], a[172:175], v[108:111]\n"
    "v_mfma_f32_16x16x32_bf16 v[108:111], a[200:203], a[140:143], v[108:111]\n"
    "buffer_store_dwordx4 v[248:251], v240, s[96:99], 0 offen sc1\n"
    "ds_write_b128 v243, v[248:251] offset:0\n"
    "v_mfma_f32_16x16x32_bf16 v[140:143], a[200:203], a[144:147], v[140:143]\n"
    "v_cvt_pk_bf16_f32 v248, v244, v245\n"
    "v_cvt_pk_bf16_f32 v249, v246, v247\n"
    "v_mfma_f32_16x16x32_bf16 v[140:143], a[204:207], a[176:179], v[140:143]\n"
    "v_accvgpr_read_b32 v245, a6\n"
    "v_accvgpr_read_b32 v247, a7\n"
    "v_mfma_f32_16x16x32_bf16 v[172:175], a[204:207], a[180:183], v[172:175]\n"
    "v_lshlrev_b32 v244, 16, v245\n"
    "v_lshlrev_b32 v246, 16, v247\n"
    "v_mfma_f32_16x16x32_bf16 v[172:175], a[200:203], a[148:151], v[172:175]\n"
    "v_and_b32 v245, s58, v245\n"
    "v_and_b32 v247, s58, v247\n"
    "v_mfma_f32_16x16x32_bf16 v[204:207], a[200:203], a[152:155], v[204:207]\n"
    "s_xor_b32 s96, s96, 0x800\n"
    "v_add_f32_dpp v254, v253, v36 row_newbcast:4 row_mask:0xf bank_mask:0xf\n"
    "v_add_f32_dpp v255, v253, v38 row_newbcast:6 row_mask:0xf bank_mask:0xf\n"
    "v_mfma_f32_16x16x32_bf16 v[204:207], a[204:207], a[184:187], v[204:207]\n"
    "v_mul_f32 v244, v244, v254\n"
    "v_mul_f32 v246, v246, v255\n"
    "v_mfma_f32_16x16x32_bf16 v[236:239], a[204:207], a[188:191], v[236:239]\n"
    "v_add_f32_dpp v254, v253, v37 row_newbcast:5 row_mask:0xf bank_mask:0xf\n"
    "v_add_f32_dpp v255, v253, v39 row_newbcast:7 row_mask:0xf bank_mask:0xf\n"
    "v_mfma_f32_16x16x32_bf16 v[236:239], a[200:203], a[156:159], v[236:239]\n"
    "s_waitcnt vmcnt(9)\n"
    "buffer_load_dwordx4 a[200:203], v240, s[20:23], 0 offen offset:2048 sc1\n"
    "v_mfma_f32_16x16x32_bf16 a[240:243], a[208:211], a[156:159], a[240:243]\n"
    "buffer_load_dwordx4 a[204:207], v240, s[20:23], 0 offen offset:3072 sc1\n"
    "s_addk_i32 s20, 4096\n"
    "v_mfma_f32_16x16x32_bf16 a[240:243], a[212:215], a[188:191], a[240:243]\n"
    "v_mul_f32 v245, v245, v254\n"
    "v_mul_f32 v247, v247, v255\n"
    "v_mfma_f32_16x16x32_bf16 v[208:211], a[212:215], a[184:187], v[208:211]\n"
    "v_cvt_pk_bf16_f32 v250, v244, v245\n"
    "v_cvt_pk_bf16_f32 v251, v246, v247\n"
    "v_mfma_f32_16x16x32_bf16 v[208:211], a[208:211], a[152:155], v[208:211]\n"
    "v_accvgpr_read_b32 v245, a8\n"
    "v_accvgpr_read_b32 v247, a9\n"
    "v_mfma_f32_16x16x32_bf16 v[176:179], a[208:211], a[148:151], v[176:179]\n"
    "v_lshlrev_b32 v244, 16, v245\n"
    "v_lshlrev_b32 v246, 16, v247\n"
    "v_mfma_f32_16x16x32_bf16 v[176:179], a[212:215], a[180:183], v[176:179]\n"
    "v_and_b32 v245, s58, v245\n"
    "v_and_b32 v247, s58, v247\n"
    "v_mfma_f32_16x16x32_bf16 v[144:147], a[212:215], a[176:179], v[144:147]\n"
    "v_add_f32_dpp v254, v253, v64 row_newbcast:0 row_mask:0xf bank_mask:0xf\n"
    "v_add_f32_dpp v255, v253, v66 row_newbcast:2 row_mask:0xf bank_mask:0xf\n"
    "v_mfma_f32_16x16x32_bf16 v[144:147], a[208:211], a[144:147], v[144:147]\n"
    "v_mul_f32 v244, v244, v254\n"
    "v_mul_f32 v246, v246, v255\n"
    "v_mfma_f32_16x16x32_bf16 v[112:115], a[208:211], a[140:143], v[112:115]\n"
    "v_add_f32_dpp v254, v253, v65 row_newbcast:1 row_mask:0xf bank_mask:0xf\n"
    "v_add_f32_dpp v255, v253, v67 row_newbcast:3 row_mask:0xf bank_mask:0xf\n"
    "v_mfma_f32_16x16x32_bf16 v[112:115], a[212:215], a[172:175], v[112:115]\n"
    "v_mul_f32 v245, v245, v254\n"
    "v_mul_f32 v247, v247, v255\n"
    "v_mfma_f32_16x16x32_bf16 v[80:83], a[212:215], a[168:171], v[80:83]\n"
    "v_mfma_f32_16x16x32_bf16 v[80:83], a[208:211], a[136:139], v[80:83]\n"
    "buffer_store_dwordx4 v[248:251], v240, s[96:99], 0 offen sc1\n"
    "ds_write_b128 v243, v[248:251] offset:2048\n"
    "v_mfma_f32_16x16x32_bf16 v[48:51], a[208:211], a[132:135], v[48:51]\n"
    "v_cvt_pk_bf16_f32 v248, v244, v245\n"
    "v_cvt_pk_bf16_f32 v249, v246, v247\n"
    "v_mfma_f32_16x16x32_bf16 v[48:51], a[212:215], a[164:167], v[48:51]\n"
    "v_accvgpr_read_b32 v245, a10\n"
    "v_accvgpr_read_b32 v247, a11\n"
    "v_mfma_f32_16x16x32_bf16 v[16:19], a[212:215], a[160:163], v[16:19]\n"
    "v_lshlrev_b32 v244, 16, v245\n"
    "v_lshlrev_b32 v246, 16, v247\n"
    "v_mfma_f32_16x16x32_bf16 v[16:19], a[208:211], a[128:131], v[16:19]\n"
    "s_waitcnt vmcnt(10)\n"
    "buffer_load_dwordx4 a[208:211], v240, s[20:23], 0 offen offset:0 sc1\n"
    "v_mfma_f32_16x16x32_bf16 v[20:23], a[216:219], a[128:131], v[20:23]\n"
    "buffer_load_dwordx4 a[212:215], v240, s[20:23], 0 offen offset:1024 sc1\n"
    "v_and_b32 v245, s58, v245\n"
    "v_mfma_f32_16x16x32_bf16 v[20:23], a[220:223], a[160:163], v[20:23]\n"
    "v_and_b32 v247, s58, v247\n"
    "v_add_f32_dpp v254, v253, v68 row_newbcast:4 row_mask:0xf bank_mask:0xf\n"
    "v_mfma_f32_16x16x32_bf16 v[52:55], a[220:223], a[164:167], v[52:55]\n"
    "s_xor_b32 s96, s96, 0x101800\n"
    "v_add_f32_dpp v255, v253, v70 row_newbcast:6 row_mask:0xf bank_mask:0xf\n"
    "v_mul_f32 v244, v244, v254\n"
    "v_mfma_f32_16x16x32_bf16 v[52:55], a[216:219], a[132:135], v[52:55]\n"
    "v_mul_f32 v246, v246, v255\n"
    "v_add_f32_dpp v254, v253, v69 row_newbcast:5 row_mask:0xf bank_mask:0xf\n"
    "v_mfma_f32_16x16x32_bf16 v[84:87], a[216:219], a[136:139], v[84:87]\n"
    "v_add_f32_dpp v255, v253, v71 row_newbcast:7 row_mask:0xf bank_mask:0xf\n"
    "v_mul_f32 v245, v245, v254\n"
    "v_mfma_f32_16x16x32_bf16 v[84:87], a[220:223], a[168:171], v[84:87]\n"
    "v_mul_f32 v247, v247, v255\n"
    "v_cvt_pk_bf16_f32 v250, v244, v245\n"
    "v_mfma_f32_16x16x32_bf16 v[116:119], a[220:223], a[172:175], v[116:119]\n"
    "v_cvt_pk_bf16_f32 v251, v246, v247\n"
    "v_accvgpr_read_b32 v245, a12\n"
    "v_mfma_f32_16x16x32_bf16 v[116:119], a[216:219], a[140:143], v[116:119]\n"
    "v_accvgpr_read_b32 v247, a13\n"
    "v_lshlrev_b32 v244, 16, v245\n"
    "v_mfma_f32_16x16x32_bf16 v[148:151], a[216:219], a[144:147], v[148:151]\n"
    "v_lshlrev_b32 v246, 16, v247\n"
    "v_and_b32 v245, s58, v245\n"
    "v_mfma_f32_16x16x32_bf16 v[148:151], a[220:223], a[176:179], v[148:151]\n"
    "v_and_b32 v247, s58, v247\n"
    "v_add_f32_dpp v254, v253, v96 row_newbcast:0 row_mask:0xf bank_mask:0xf\n"
    "v_mfma_f32_16x16x32_bf16 v[180:183], a[220:223], a[180:183], v[180:183]\n"
    "v_add_f32_dpp v255, v253, v98 row_newbcast:2 row_mask:0xf bank_mask:0xf\n"
    "v_mul_f32 v244, v244, v254\n"
    "v_mfma_f32_16x16x32_bf16 v[180:183], a[216:219], a[148:151], v[180:183]\n"
    "v_mul_f32 v246, v246, v255\n"
    "v_add_f32_dpp v254, v253, v97 row_newbcast:1 row_mask:0xf bank_mask:0xf\n"
    "v_mfma_f32_16x16x32_bf16 v[212:215], a[216:219], a[152:155], v[212:215]\n"
    "v_add_f32_dpp v255, v253, v99 row_newbcast:3 row_mask:0xf bank_mask:0xf\n"
    "v_mul_f32 v245, v245, v254\n"
    "v_mfma_f32_16x16x32_bf16 v[212:215], a[220:223], a[184:187], v[212:215]\n"
    "v_mul_f32 v247, v247, v255\n"
    "v_mfma_f32_16x16x32_bf16 a[244:247], a[220:223], a[188:191], a[244:247]\n"
    "v_mfma_f32_16x16x32_bf16 a[244:247], a[216:219], a[156:159], a[244:247]\n"
    "s_waitcnt vmcnt(10)\n"
    "v_mfma_f32_16x16x32_bf16 a[248:251], a[224:227], a[156:159], a[248:251]\n"
    "buffer_store_dwordx4 v[248:251], v240, s[96:99], 0 offen sc1\n"
    "ds_write_b128 v243, v[248:251] offset:4096\n"
    "v_mfma_f32_16x16x32_bf16 a[248:251], a[228:231], a[188:191], a[248:251]\n"
    "buffer_load_dwordx4 a[216:219], v240, s[20:23], 0 offen offset:2048 sc1\n"
    "v_cvt_pk_bf16_f32 v248, v244, v245\n"
    "v_mfma_f32_16x16x32_bf16 v[216:219], a[228:231], a[184:187], v[216:219]\n"
    "buffer_load_dwordx4 a[220:223], v240, s[20:23], 0 offen offset:3072 sc1\n"
    "s_addk_i32 s20, 4096\n"
    "v_mfma_f32_16x16x32_bf16 v[216:219], a[224:227], a[152:155], v[216:219]\n"
    "v_cvt_pk_bf16_f32 v249, v246, v247\n"
    "v_accvgpr_read_b32 v245, a14\n"
    "v_mfma_f32_16x16x32_bf16 v[184:187], a[224:227], a[148:151], v[184:187]\n"
    "v_accvgpr_read_b32 v247, a15\n"
    "v_lshlrev_b32 v244, 16, v245\n"
    "v_mfma_f32_16x16x32_bf16 v[184:187], a[228:231], a[180:183], v[184:187]\n"
    "v_lshlrev_b32 v246, 16, v247\n"
    "v_and_b32 v245, s58, v245\n"
    "v_mfma_f32_16x16x32_bf16 v[152:155], a[228:231], a[176:179], v[152:155]\n"
    "v_and_b32 v247, s58, v247\n"
    "v_add_f32_dpp v254, v253, v100 row_newbcast:4 row_mask:0xf bank_mask:0xf\n"
    "v_mfma_f32_16x16x32_bf16 v[152:155], a[224:227], a[144:147], v[152:155]\n"
    "s_xor_b32 s96, s96, 0x800\n"
    "v_add_f32_dpp v255, v253, v102 row_newbcast:6 row_mask:0xf bank_mask:0xf\n"
    "v_mul_f32 v244, v244, v254\n"
    "v_mfma_f32_16x16x32_bf16 v[120:123], a[224:227], a[140:143], v[120:123]\n"
    "v_mul_f32 v246, v246, v255\n"
    "v_add_f32_dpp v254, v253, v101 row_newbcast:5 row_mask:0xf bank_mask:0xf\n"
    "v_mfma_f32_16x16x32_bf16 v[120:123], a[228:231], a[172:175], v[120:123]\n"
    "v_add_f32_dpp v255, v253, v103 row_newbcast:7 row_mask:0xf bank_mask:0xf\n"
    "v_mul_f32 v245, v245, v254\n"
    "v_mfma_f32_16x16x32_bf16 v[88:91], a[228:231], a[168:171], v[88:91]\n"
    "v_mul_f32 v247, v247, v255\n"
    "v_cvt_pk_bf16_f32 v250, v244, v245\n"
    "v_mfma_f32_16x16x32_bf16 v[88:91], a[224:227], a[136:139], v[88:91]\n"
    "v_cvt_pk_bf16_f32 v251, v246, v247\n"
    "v_accvgpr_read_b32 v245, a16\n"
    "v_mfma_f32_16x16x32_bf16 v[56:59], a[224:227], a[132:135], v[56:59]\n"
    "v_accvgpr_read_b32 v247, a17\n"
    "v_lshlrev_b32 v244, 16, v245\n"
    "v_mfma_f32_16x16x32_bf16 v[56:59], a[228:231], a[164:167], v[56:59]\n"
    "v_lshlrev_b32 v246, 16, v247\n"
    "v_and_b32 v245, s58, v245\n"
    "v_mfma_f32_16x16x32_bf16 v[24:27], a[228:231], a[160:163], v[24:27]\n"
    "v_and_b32 v247, s58, v247\n"
    "v_add_f32_dpp v254, v253, v128 row_newbcast:0 row_mask:0xf bank_mask:0xf\n"
    "v_mfma_f32_16x16x32_bf16 v[24:27], a[224:227], a[128:131], v[24:27]\n"
    "s_waitcnt vmcnt(11)\n"
    "buffer_load_dwordx4 a[224:227], v240, s[20:23], 0 offen offset:0 sc1\n"
    "v_mfma_f32_16x16x32_bf16 v[28:31], a[232:235], a[128:131], v[28:31]\n"
    "buffer_load_dwordx4 a[228:231], v240, s[20:23], 0 offen offset:1024 sc1\n"
    "v_add_f32_dpp v255, v253, v130 row_newbcast:2 row_mask:0xf bank_mask:0xf\n"
    "v_mfma_f32_16x16x32_bf16 v[28:31], a[236:239], a[160:163], v[28:31]\n"
    "v_mul_f32 v244, v244, v254\n"
    "v_mul_f32 v246, v246, v255\n"
    "v_mfma_f32_16x16x32_bf16 v[60:63], a[236:239], a[164:167], v[60:63]\n"
    "ds_read_b128 a[128:131], v241 offset:49152\n"
    "v_add_f32_dpp v254, v253, v129 row_newbcast:1 row_mask:0xf bank_mask:0xf\n"
    "v_mfma_f32_16x16x32_bf16 v[60:63], a[232:235], a[132:135], v[60:63]\n"
    "ds_read_b128 a[160:163], v242 offset:49152\n"
    "v_add_f32_dpp v255, v253, v131 row_newbcast:3 row_mask:0xf bank_mask:0xf\n"
    "v_mfma_f32_16x16x32_bf16 v[92:95], a[232:235], a[136:139], v[92:95]\n"
    "ds_read_b128 a[164:167], v242 offset:51200\n"
    "v_mul_f32 v245, v245, v254\n"
    "v_mfma_f32_16x16x32_bf16 v[92:95], a[236:239], a[168:171], v[92:95]\n"
    "ds_read_b128 a[132:135], v241 offset:51200\n"
    "v_mul_f32 v247, v247, v255\n"
    "v_mfma_f32_16x16x32_bf16 v[124:127], a[236:239], a[172:175], v[124:127]\n"
    "ds_read_b128 a[136:139], v241 offset:53248\n"
    "v_mfma_f32_16x16x32_bf16 v[124:127], a[232:235], a[140:143], v[124:127]\n"
    "buffer_store_dwordx4 v[248:251], v240, s[96:99], 0 offen sc1\n"
    "ds_write_b128 v243, v[248:251] offset:6144\n"
    "v_mfma_f32_16x16x32_bf16 v[156:159], a[232:235], a[144:147], v[156:159]\n"
    "ds_read_b128 a[168:171], v242 offset:53248\n"
    "v_cvt_pk_bf16_f32 v248, v244, v245\n"
    "v_mfma_f32_16x16x32_bf16 v[156:159], a[236:239], a[176:179], v[156:159]\n"
    "ds_read_b128 a[172:175], v242 offset:55296\n"
    "v_cvt_pk_bf16_f32 v249, v246, v247\n"
    "v_mfma_f32_16x16x32_bf16 v[188:191], a[236:239], a[180:183], v[188:191]\n"
    "ds_read_b128 a[140:143], v241 offset:55296\n"
    "v_accvgpr_read_b32 v245, a18\n"
    "v_mfma_f32_16x16x32_bf16 v[188:191], a[232:235], a[148:151], v[188:191]\n"
    "ds_read_b128 a[144:147], v241 offset:57344\n"
    "v_accvgpr_read_b32 v247, a19\n"
    "v_mfma_f32_16x16x32_bf16 v[220:223], a[232:235], a[152:155], v[220:223]\n"
    "ds_read_b128 a[176:179], v242 offset:57344\n"
    "v_lshlrev_b32 v244, 16, v245\n"
    "v_mfma_f32_16x16x32_bf16 v[220:223], a[236:239], a[184:187], v[220:223]\n"
    "ds_read_b128 a[180:183], v242 offset:59392\n"
    "v_lshlrev_b32 v246, 16, v247\n"
    "v_mfma_f32_16x16x32_bf16 a[252:255], a[236:239], a[188:191], a[252:255]\n"
    "ds_read_b128 a[148:151], v241 offset:59392\n"
    "v_and_b32 v245, s58, v245\n"
    "v_mfma_f32_16x16x32_bf16 a[252:255], a[232:235], a[156:159], a[252:255]\n"
    "s_waitcnt vmcnt(12) lgkmcnt(11)\n"
    "ds_read_b128 a[152:155], v241 offset:61440\n"
    "v_and_b32 v247, s58, v247\n"
    "v_mfma_f32_16x16x32_bf16 v[16:19], a[192:195], a[128:131], v[16:19]\n"
    "ds_read_b128 a[184:187], v242 offset:61440\n"
    "v_add_f32_dpp v254, v253, v132 row_newbcast:4 row_mask:0xf bank_mask:0xf\n"
    "v_mfma_f32_16x16x32_bf16 v[16:19], a[196:199], a[160:163], v[16:19]\n"
    "s_waitcnt lgkmcnt(11)\n"
    "ds_read_b128 a[188:191], v242 offset:63488\n"
    "v_add_f32_dpp v255, v253, v134 row_newbcast:6 row_mask:0xf bank_mask:0xf\n"
    "v_mfma_f32_16x16x32_bf16 v[48:51], a[196:199], a[164:167], v[48:51]\n"
    "ds_read_b128 a[156:159], v241 offset:63488\n"
    "v_mul_f32 v244, v244, v254\n"
    "v_mfma_f32_16x16x32_bf16 v[48:51], a[192:195], a[132:135], v[48:51]\n"
    "s_waitcnt lgkmcnt(10)\n"
    "buffer_load_dwordx4 a[232:235], v240, s[20:23], 0 offen offset:2048 sc1\n"
    "v_mfma_f32_16x16x32_bf16 v[80:83], a[192:195], a[136:139], v[80:83]\n"
    "buffer_load_dwordx4 a[236:239], v240, s[20:23], 0 offen offset:3072 sc1\n"
    "s_addk_i32 s20, 4096\n"
    "v_mfma_f32_16x16x32_bf16 v[80:83], a[196:199], a[168:171], v[80:83]\n"
    "s_waitcnt lgkmcnt(8)\n"
    "s_xor_b32 s96, s96, 0x185800\n"
    "v_mul_f32 v246, v246, v255\n"
    "v_mfma_f32_16x16x32_bf16 v[112:115], a[196:199], a[172:175], v[112:115]\n"
    "v_add_f32_dpp v254, v253, v133 row_newbcast:5 row_mask:0xf bank_mask:0xf\n"
    "v_add_f32_dpp v255, v253, v135 row_newbcast:7 row_mask:0xf bank_mask:0xf\n"
    "v_mfma_f32_16x16x32_bf16 v[112:115], a[192:195], a[140:143], v[112:115]\n"
    "s_waitcnt lgkmcnt(6)\n"
    "v_mul_f32 v245, v245, v254\n"
    "v_mul_f32 v247, v247, v255\n"
    "v_mfma_f32_16x16x32_bf16 v[144:147], a[192:195], a[144:147], v[144:147]\n"
    "v_cvt_pk_bf16_f32 v250, v244, v245\n"
    "v_cvt_pk_bf16_f32 v251, v246, v247\n"
    "v_mfma_f32_16x16x32_bf16 v[144:147], a[196:199], a[176:179], v[144:147]\n"
    "s_waitcnt lgkmcnt(4)\n"
    "v_accvgpr_read_b32 v245, a20\n"
    "v_accvgpr_read_b32 v247, a21\n"
    "v_mfma_f32_16x16x32_bf16 v[176:179], a[196:199], a[180:183], v[176:179]\n"
    "v_lshlrev_b32 v244, 16, v245\n"
    "v_lshlrev_b32 v246, 16, v247\n"
    "v_mfma_f32_16x16x32_bf16 v[176:179], a[192:195], a[148:151], v[176:179]\n"
    "s_waitcnt lgkmcnt(2)\n"
    "v_and_b32 v245, s58, v245\n"
    "v_and_b32 v247, s58, v247\n"
    "v_mfma_f32_16x16x32_bf16 v[208:211], a[192:195], a[152:155], v[208:211]\n"
    "v_add_f32_dpp v254, v253, v160 row_newbcast:0 row_mask:0xf bank_mask:0xf\n"
    "v_add_f32_dpp v255, v253, v162 row_newbcast:2 row_mask:0xf bank_mask:0xf\n"
    "v_mfma_f32_16x16x32_bf16 v[208:211], a[196:199], a[184:187], v[208:211]\n"
    "s_waitcnt lgkmcnt(0)\n"
    "v_mul_f32 v244, v244, v254\n"
    "v_mul_f32 v246, v246, v255\n"
    "v_mfma_f32_16x16x32_bf16 a[240:243], a[196:199], a[188:191], a[240:243]\n"
    "v_add_f32_dpp v254, v253, v161 row_newbcast:1 row_mask:0xf bank_mask:0xf\n"
    "v_add_f32_dpp v255, v253, v163 row_newbcast:3 row_mask:0xf bank_mask:0xf\n"
    "v_mfma_f32_16x16x32_bf16 a[240:243], a[192:195], a[156:159], a[240:243]\n"
    "s_waitcnt vmcnt(11)\n"
    "buffer_load_dwordx4 a[192:195], v240, s[20:23], 0 offen offset:0 sc1\n"
    "v_mfma_f32_16x16x32_bf16 a[244:247], a[200:203], a[156:159], a[244:247]\n"
    "buffer_load_dwordx4 a[196:199], v240, s[20:23], 0 offen offset:1024 sc1\n"
    "v_mul_f32 v245, v245, v254\n"
    "v_mfma_f32_16x16x32_bf16 a[244:247], a[204:207], a[188:191], a[244:247]\n"
    "v_mul_f32 v247, v247, v255\n"
    "v_mfma_f32_16x16x32_bf16 v[212:215], a[204:207], a[184:187], v[212:215]\n"
    "v_mfma_f32_16x16x32_bf16 v[212:215], a[200:203], a[152:155], v[212:215]\n"
    "buffer_store_dwordx4 v[248:251], v240, s[96:99], 0 offen sc1\n"
    "ds_write_b128 v243, v[248:251] offset:8192\n"
    "v_mfma_f32_16x16x32_bf16 v[180:183], a[200:203], a[148:151], v[180:183]\n"
    "v_cvt_pk_bf16_f32 v248, v244, v245\n"
    "v_cvt_pk_bf16_f32 v249, v246, v247\n"
    "v_mfma_f32_16x16x32_bf16 v[180:183], a[204:207], a[180:183], v[180:183]\n"
    "v_accvgpr_read_b32 v245, a22\n"
    "v_accvgpr_read_b32 v247, a23\n"
    "v_mfma_f32_16x16x32_bf16 v[148:151], a[204:207], a[176:179], v[148:151]\n"
    "v_lshlrev_b32 v244, 16, v245\n"
    "v_lshlrev_b32 v246, 16, v247\n"
    "v_mfma_f32_16x16x32_bf16 v[148:151], a[200:203], a[144:147], v[148:151]\n"
    "v_and_b32 v245, s58, v245\n"
    "v_and_b32 v247, s58, v247\n"
    "v_mfma_f32_16x16x32_bf16 v[116:119], a[200:203], a[140:143], v[116:119]\n"
    "s_xor_b32 s96, s96, 0x800\n"
    "v_add_f32_dpp v254, v253, v164 row_newbcast:4 row_mask:0xf bank_mask:0xf\n"
    "v_add_f32_dpp v255, v253, v166 row_newbcast:6 row_mask:0xf bank_mask:0xf\n"
    "v_mfma_f32_16x16x32_bf16 v[116:119], a[204:207], a[172:175], v[116:119]\n"
    "v_mul_f32 v244, v244, v254\n"
    "v_mul_f32 v246, v246, v255\n"
    "v_mfma_f32_16x16x32_bf16 v[84:87], a[204:207], a[168:171], v[84:87]\n"
    "v_add_f32_dpp v254, v253, v165 row_newbcast:5 row_mask:0xf bank_mask:0xf\n"
    "v_add_f32_dpp v255, v253, v167 row_newbcast:7 row_mask:0xf bank_mask:0xf\n"
    "v_mfma_f32_16x16x32_bf16 v[84:87], a[200:203], a[136:139], v[84:87]\n"
    "v_mul_f32 v245, v245, v254\n"
    "v_mul_f32 v247, v247, v255\n"
    "v_mfma_f32_16x16x32_bf16 v[52:55], a[200:203], a[132:135], v[52:55]\n"
    "v_cvt_pk_bf16_f32 v250, v244, v245\n"
    "v_cvt_pk_bf16_f32 v251, v246, v247\n"
    "v_mfma_f32_16x16x32_bf16 v[52:55], a[204:207], a[164:167], v[52:55]\n"
    "v_accvgpr_read_b32 v245, a24\n"
    "v_accvgpr_read_b32 v247, a25\n"
    "v_mfma_f32_16x16x32_bf16 v[20:23], a[204:207], a[160:163], v[20:23]\n"
    "v_lshlrev_b32 v244, 16, v245\n"
    "v_lshlrev_b32 v246, 16, v247\n"
    "v_mfma_f32_16x16x32_bf16 v[20:23], a[200:203], a[128:131], v[20:23]\n"
    "s_waitcnt vmcnt(11)\n"
    "buffer_load_dwordx4 a[200:203], v240, s[20:23], 0 offen offset:2048 sc1\n"
    "v_mfma_f32_16x16x32_bf16 v[24:27], a[208:211], a[128:131], v[24:27]\n"
    "buffer_load_dwordx4 a[204:207], v240, s[20:23], 0 offen offset:3072 sc1\n"
    "s_addk_i32 s20, 4096\n"
    "v_mfma_f32_16x16x32_bf16 v[24:27], a[212:215], a[160:163], v[24:27]\n"
    "v_and_b32 v245, s58, v245\n"
    "v_and_b32 v247, s58, v247\n"
    "v_mfma_f32_16x16x32_bf16 v[56:59], a[212:215], a[164:167], v[56:59]\n"
    "v_add_f32_dpp v254, v253, v192 row_newbcast:0 row_mask:0xf bank_mask:0xf\n"
    "v_add_f32_dpp v255, v253, v194 row_newbcast:2 row_mask:0xf bank_mask:0xf\n"
    "v_mfma_f32_16x16x32_bf16 v[56:59], a[208:211], a[132:135], v[56:59]\n"
    "v_mul_f32 v244, v244, v254\n"
    "v_mul_f32 v246, v246, v255\n"
    "v_mfma_f32_16x16x32_bf16 v[88:91], a[208:211], a[136:139], v[88:91]\n"
    "buffer_store_dwordx4 v[248:251], v240, s[96:99], 0 offen sc1\n"
    "ds_write_b128 v243, v[248:251] offset:10240\n"
    "v_mfma_f32_16x16x32_bf16 v[88:91], a[212:215], a[168:171], v[88:91]\n"
    "v_add_f32_dpp v254, v253, v193 row_newbcast:1 row_mask:0xf bank_mask:0xf\n"
    "v_add_f32_dpp v255, v253, v195 row_newbcast:3 row_mask:0xf bank_mask:0xf\n"
    "v_mfma_f32_16x16x32_bf16 v[120:123], a[212:215], a[172:175], v[120:123]\n"
    "v_mul_f32 v245, v245, v254\n"
    "v_mul_f32 v247, v247, v255\n"
    "v_mfma_f32_16x16x32_bf16 v[120:123], a[208:211], a[140:143], v[120:123]\n"
    "s_xor_b32 s96, s96, 0x101800\n"
    "v_cvt_pk_bf16_f32 v248, v244, v245\n"
    "v_cvt_pk_bf16_f32 v249, v246, v247\n"
    "v_mfma_f32_16x16x32_bf16 v[152:155], a[208:211], a[144:147], v[152:155]\n"
    "v_accvgpr_read_b32 v245, a26\n"
    "v_accvgpr_read_b32 v247, a27\n"
    "v_mfma_f32_16x16x32_bf16 v[152:155], a[212:215], a[176:179], v[152:155]\n"
    "v_lshlrev_b32 v244, 16, v245\n"
    "v_lshlrev_b32 v246, 16, v247\n"
    "v_mfma_f32_16x16x32_bf16 v[184:187], a[212:215], a[180:183], v[184:187]\n"
    "v_and_b32 v245, s58, v245\n"
    "v_and_b32 v247, s58, v247\n"
    "v_mfma_f32_16x16x32_bf16 v[184:187], a[208:211], a[148:151], v[184:187]\n"
    "v_add_f32_dpp v254, v253, v196 row_newbcast:4 row_mask:0xf bank_mask:0xf\n"
    "v_add_f32_dpp v255, v253, v198 row_newbcast:6 row_mask:0xf bank_mask:0xf\n"
    "v_mfma_f32_16x16x32_bf16 v[216:219], a[208:211], a[152:155], v[216:219]\n"
    "v_mul_f32 v244, v244, v254\n"
    "v_mul_f32 v246, v246, v255\n"
    "v_mfma_f32_16x16x32_bf16 v[216:219], a[212:215], a[184:187], v[216:219]\n"
    "v_add_f32_dpp v254, v253, v197 row_newbcast:5 row_mask:0xf bank_mask:0xf\n"
    "v_add_f32_dpp v255, v253, v199 row_newbcast:7 row_mask:0xf bank_mask:0xf\n"
    "v_mfma_f32_16x16x32_bf16 a[248:251], a[212:215], a[188:191], a[248:251]\n"
    "v_mul_f32 v245, v245, v254\n"
    "v_mul_f32 v247, v247, v255\n"
    "v_mfma_f32_16x16x32_bf16 a[248:251], a[208:211], a[156:159], a[248:251]\n"
    "s_waitcnt vmcnt(11)\n"
    "v_cvt_pk_bf16_f32 v250, v244, v245\n"
    "v_cvt_pk_bf16_f32 v251, v246, v247\n"
    "v_mfma_f32_16x16x32_bf16 a[252:255], a[216:219], a[156:159], a[252:255]\n"
    "buffer_store_dwordx4 v[248:251], v240, s[96:99], 0 offen sc1\n"
    "ds_write_b128 v243, v[248:251] offset:12288\n"
    "v_mfma_f32_16x16x32_bf16 a[252:255], a[220:223], a[188:191], a[252:255]\n"
    "buffer_load_dwordx4 a[208:211], v240, s[20:23], 0 offen offset:0 sc1\n"
    "v_accvgpr_read_b32 v245, a28\n"
    "v_mfma_f32_16x16x32_bf16 v[220:223], a[220:223], a[184:187], v[220:223]\n"
    "buffer_load_dwordx4 a[212:215], v240, s[20:23], 0 offen offset:1024 sc1\n"
    "ds_read_b128 a[156:159], v241 offset:30720\n"
    "v_accvgpr_read_b32 v247, a29\n"
    "v_lshlrev_b32 v244, 16, v245\n"
    "v_lshlrev_b32 v246, 16, v247\n"
    "v_mfma_f32_16x16x32_bf16 v[220:223], a[216:219], a[152:155], v[220:223]\n"
    "ds_read_b128 a[188:191], v242 offset:30720\n"
    "v_and_b32 v245, s58, v245\n"
    "v_and_b32 v247, s58, v247\n"
    "v_add_f32_dpp v254, v253, v224 row_newbcast:0 row_mask:0xf bank_mask:0xf\n"
    "v_add_f32_dpp v255, v253, v226 row_newbcast:2 row_mask:0xf bank_mask:0xf\n"
    "v_mfma_f32_16x16x32_bf16 v[188:191], a[216:219], a[148:151], v[188:191]\n"
    "ds_read_b128 a[184:187], v242 offset:28672\n"
    "v_mul_f32 v244, v244, v254\n"
    "v_mul_f32 v246, v246, v255\n"
    "v_add_f32_dpp v254, v253, v225 row_newbcast:1 row_mask:0xf bank_mask:0xf\n"
    "v_add_f32_dpp v255, v253, v227 row_newbcast:3 row_mask:0xf bank_mask:0xf\n"
    "v_mfma_f32_16x16x32_bf16 v[188:191], a[220:223], a[180:183], v[188:191]\n"
    "ds_read_b128 a[152:155], v241 offset:28672\n"
    "v_mul_f32 v245, v245, v254\n"
    "v_mul_f32 v247, v247, v255\n"
    "v_cvt_pk_bf16_f32 v248, v244, v245\n"
    "v_mfma_f32_16x16x32_bf16 v[156:159], a[220:223], a[176:179], v[156:159]\n"
    "ds_read_b128 a[148:151], v241 offset:26624\n"
    "v_cvt_pk_bf16_f32 v249, v246, v247\n"
    "v_accvgpr_read_b32 v245, a30\n"
    "v_accvgpr_read_b32 v247, a31\n"
    "v_lshlrev_b32 v244, 16, v245\n"
    "v_mfma_f32_16x16x32_bf16 v[156:159], a[216:219], a[144:147], v[156:159]\n"
    "ds_read_b128 a[180:183], v242 offset:26624\n"
    "v_lshlrev_b32 v246, 16, v247\n"
    "v_and_b32 v245, s58, v245\n"
    "v_and_b32 v247, s58, v247\n"
    "v_mfma_f32_16x16x32_bf16 v[124:127], a[216:219], a[140:143], v[124:127]\n"
    "ds_read_b128 a[176:179], v242 offset:24576\n"
    "v_add_f32_dpp v254, v253, v228 row_newbcast:4 row_mask:0xf bank_mask:0xf\n"
    "v_mfma_f32_16x16x32_bf16 v[124:127], a[220:223], a[172:175], v[124:127]\n"
    "ds_read_b128 a[144:147], v241 offset:24576\n"
    "s_xor_b32 s96, s96, 0x800\n"
    "v_mfma_f32_16x16x32_bf16 v[92:95], a[220:223], a[168:171], v[92:95]\n"
    "ds_read_b128 a[140:143], v241 offset:22528\n"
    "v_add_f32_dpp v255, v253, v230 row_newbcast:6 row_mask:0xf bank_mask:0xf\n"
    "v_mfma_f32_16x16x32_bf16 v[92:95], a[216:219], a[136:139], v[92:95]\n"
    "ds_read_b128 a[172:175], v242 offset:22528\n"
    "v_mul_f32 v244, v244, v254\n"
    "v_mfma_f32_16x16x32_bf16 v[60:63], a[216:219], a[132:135], v[60:63]\n"
    "ds_read_b128 a[168:171], v242 offset:20480\n"
    "v_mul_f32 v246, v246, v255\n"
    "v_mfma_f32_16x16x32_bf16 v[60:63], a[220:223], a[164:167], v[60:63]\n"
    "ds_read_b128 a[136:139], v241 offset:20480\n"
    "v_add_f32_dpp v254, v253, v229 row_newbcast:5 row_mask:0xf bank_mask:0xf\n"
    "v_mfma_f32_16x16x32_bf16 v[28:31], a[220:223], a[160:163], v[28:31]\n"
    "ds_read_b128 a[132:135], v241 offset:18432\n"
    "v_add_f32_dpp v255, v253, v231 row_newbcast:7 row_mask:0xf bank_mask:0xf\n"
    "v_mfma_f32_16x16x32_bf16 v[28:31], a[216:219], a[128:131], v[28:31]\n"
    "s_waitcnt vmcnt(12) lgkmcnt(11)\n"
    "ds_read_b128 a[164:167], v242 offset:18432\n"
    "v_mul_f32 v245, v245, v254\n"
    "v_mfma_f32_16x16x32_bf16 a[240:243], a[224:227], a[156:159], a[240:243]\n"
    "ds_read_b128 a[160:163], v242 offset:16384\n"
    "v_mul_f32 v247, v247, v255\n"
    "v_mfma_f32_16x16x32_bf16 a[240:243], a[228:231], a[188:191], a[240:243]\n"
    "s_waitcnt lgkmcnt(11)\n"
    "ds_read_b128 a[128:131], v241 offset:16384\n"
    "v_cvt_pk_bf16_f32 v250, v244, v245\n"
    "v_mfma_f32_16x16x32_bf16 v[208:211], a[228:231], a[184:187], v[208:211]\n"
    "buffer_load_dwordx4 a[216:219], v240, s[20:23], 0 offen offset:2048 sc1\n"
    "v_cvt_pk_bf16_f32 v251, v246, v247\n"
    "v_mfma_f32_16x16x32_bf16 v[208:211], a[224:227], a[152:155], v[208:211]\n"
    "s_waitcnt lgkmcnt(10)\n"
    "buffer_load_dwordx4 a[220:223], v240, s[20:23], 0 offen offset:3072 sc1\n"
    "s_addk_i32 s20, 4096\n"
    "v_mfma_f32_16x16x32_bf16 v[176:179], a[224:227], a[148:151], v[176:179]\n"
    "buffer_store_dwordx4 v[248:251], v240, s[96:99], 0 offen sc1\n"
    "ds_write_b128 v243, v[248:251] offset:14336\n"
    "v_mfma_f32_16x16x32_bf16 v[176:179], a[228:231], a[180:183], v[176:179]\n"
    "s_waitcnt lgkmcnt(9)\n"
    "v_accvgpr_read_b32 v245, a32\n"
    "v_accvgpr_read_b32 v247, a33\n"
    "v_mfma_f32_16x16x32_bf16 v[144:147], a[228:231], a[176:179], v[144:147]\n"
    "v_lshlrev_b32 v244, 16, v245\n"
    "v_lshlrev_b32 v246, 16, v247\n"
    "v_mfma_f32_16x16x32_bf16 v[144:147], a[224:227], a[144:147], v[144:147]\n"
    "s_waitcnt lgkmcnt(7)\n"
    "v_and_b32 v245, s58, v245\n"
    "v_and_b32 v247, s58, v247\n"
    "v_mfma_f32_16x16x32_bf16 v[112:115], a[224:227], a[140:143], v[112:115]\n"
    "v_add_f32_dpp v254, v253, v8 row_newbcast:8 row_mask:0xf bank_mask:0xf\n"
    "v_add_f32_dpp v255, v253, v10 row_newbcast:10 row_mask:0xf bank_mask:0xf\n"
    "v_mfma_f32_16x16x32_bf16 v[112:115], a[228:231], a[172:175], v[112:115]\n"
    "s_waitcnt lgkmcnt(5)\n"
    "v_mul_f32 v244, v244, v254\n"
    "v_mul_f32 v246, v246, v255\n"
    "v_mfma_f32_16x16x32_bf16 v[80:83], a[228:231], a[168:171], v[80:83]\n"
    "v_add_f32_dpp v254, v253, v9 row_newbcast:9 row_mask:0xf bank_mask:0xf\n"
    "v_add_f32_dpp v255, v253, v11 row_newbcast:11 row_mask:0xf bank_mask:0xf\n"
    "v_mfma_f32_16x16x32_bf16 v[80:83], a[224:227], a[136:139], v[80:83]\n"
    "s_waitcnt lgkmcnt(3)\n"
    "v_mul_f32 v245, v245, v254\n"
    "v_mul_f32 v247, v247, v255\n"
    "v_mfma_f32_16x16x32_bf16 v[48:51], a[224:227], a[132:135], v[48:51]\n"
    "v_cvt_pk_bf16_f32 v248, v244, v245\n"
    "v_cvt_pk_bf16_f32 v249, v246, v247\n"
    "v_mfma_f32_16x16x32_bf16 v[48:51], a[228:231], a[164:167], v[48:51]\n"
    "s_waitcnt lgkmcnt(1)\n"
    "s_xor_b32 s96, s96, 0x195800\n"
    "v_accvgpr_read_b32 v245, a34\n"
    "v_mfma_f32_16x16x32_bf16 v[16:19], a[228:231], a[160:163], v[16:19]\n"
    "v_accvgpr_read_b32 v247, a35\n"
    "v_lshlrev_b32 v244, 16, v245\n"
    "v_mfma_f32_16x16x32_bf16 v[16:19], a[224:227], a[128:131], v[16:19]\n"
    "s_waitcnt vmcnt(12)\n"
    "buffer_load_dwordx4 a[224:227], v240, s[20:23], 0 offen offset:0 sc1\n"
    "v_mfma_f32_16x16x32_bf16 v[20:23], a[232:235], a[128:131], v[20:23]\n"
    "buffer_load_dwordx4 a[228:231], v240, s[20:23], 0 offen offset:1024 sc1\n"
    "v_lshlrev_b32 v246, 16, v247\n"
    "v_mfma_f32_16x16x32_bf16 v[20:23], a[236:239], a[160:163], v[20:23]\n"
    "v_and_b32 v245, s58, v245\n"
    "v_and_b32 v247, s58, v247\n"
    "v_mfma_f32_16x16x32_bf16 v[52:55], a[236:239], a[164:167], v[52:55]\n"
    "v_add_f32_dpp v254, v253, v12 row_newbcast:12 row_mask:0xf bank_mask:0xf\n"
    "v_add_f32_dpp v255, v253, v14 row_newbcast:14 row_mask:0xf bank_mask:0xf\n"
    "v_mfma_f32_16x16x32_bf16 v[52:55], a[232:235], a[132:135], v[52:55]\n"
    "v_mul_f32 v244, v244, v254\n"
    "v_mul_f32 v246, v246, v255\n"
    "v_mfma_f32_16x16x32_bf16 v[84:87], a[232:235], a[136:139], v[84:87]\n"
    "v_add_f32_dpp v254, v253, v13 row_newbcast:13 row_mask:0xf bank_mask:0xf\n"
    "v_add_f32_dpp v255, v253, v15 row_newbcast:15 row_mask:0xf bank_mask:0xf\n"
    "v_mfma_f32_16x16x32_bf16 v[84:87], a[236:239], a[168:171], v[84:87]\n"
    "v_mul_f32 v245, v245, v254\n"
    "v_mul_f32 v247, v247, v255\n"
    "v_mfma_f32_16x16x32_bf16 v[116:119], a[236:239], a[172:175], v[116:119]\n"
    "v_cvt_pk_bf16_f32 v250, v244, v245\n"
    "v_cvt_pk_bf16_f32 v251, v246, v247\n"
    "v_mfma_f32_16x16x32_bf16 v[116:119], a[232:235], a[140:143], v[116:119]\n"
    "buffer_store_dwordx4 v[248:251], v240, s[96:99], 0 offen sc1\n"
    "ds_write_b128 v243, v[248:251] offset:32768\n"
    "v_mfma_f32_16x16x32_bf16 v[148:151], a[232:235], a[144:147], v[148:151]\n"
    "s_xor_b32 s96, s96, 0x800\n"
    "v_accvgpr_read_b32 v245, a36\n"
    "v_accvgpr_read_b32 v247, a37\n"
    "v_mfma_f32_16x16x32_bf16 v[148:151], a[236:239], a[176:179], v[148:151]\n"
    "v_lshlrev_b32 v244, 16, v245\n"
    "v_lshlrev_b32 v246, 16, v247\n"
    "v_and_b32 v245, s58, v245\n"
    "v_mfma_f32_16x16x32_bf16 v[180:183], a[236:239], a[180:183], v[180:183]\n"
    "v_and_b32 v247, s58, v247\n"
    "v_add_f32_dpp v254, v253, v40 row_newbcast:8 row_mask:0xf bank_mask:0xf\n"
    "v_add_f32_dpp v255, v253, v42 row_newbcast:10 row_mask:0xf bank_mask:0xf\n"
    "v_mul_f32 v244, v244, v254\n"
    "v_mfma_f32_16x16x32_bf16 v[180:183], a[232:235], a[148:151], v[180:183]\n"
    "v_mul_f32 v246, v246, v255\n"
    "v_add_f32_dpp v254, v253, v41 row_newbcast:9 row_mask:0xf bank_mask:0xf\n"
    "v_add_f32_dpp v255, v253, v43 row_newbcast:11 row_mask:0xf bank_mask:0xf\n"
    "v_mul_f32 v245, v245, v254\n"
    "v_mfma_f32_16x16x32_bf16 v[212:215], a[232:235], a[152:155], v[212:215]\n"
    "v_mul_f32 v247, v247, v255\n"
    "v_cvt_pk_bf16_f32 v248, v244, v245\n"
    "v_mfma_f32_16x16x32_bf16 v[212:215], a[236:239], a[184:187], v[212:215]\n"
    "v_cvt_pk_bf16_f32 v249, v246, v247\n"
    "v_accvgpr_read_b32 v245, a38\n"
    "v_accvgpr_read_b32 v247, a39\n"
    "v_lshlrev_b32 v244, 16, v245\n"
    "v_mfma_f32_16x16x32_bf16 a[244:247], a[236:239], a[188:191], a[244:247]\n"
    "v_lshlrev_b32 v246, 16, v247\n"
    "v_and_b32 v245, s58, v245\n"
    "v_and_b32 v247, s58, v247\n"
    "v_add_f32_dpp v254, v253, v44 row_newbcast:12 row_mask:0xf bank_mask:0xf\n"
    "v_mfma_f32_16x16x32_bf16 a[244:247], a[232:235], a[156:159], a[244:247]\n"
    "s_waitcnt vmcnt(13)\n"
    "v_add_f32_dpp v255, v253, v46 row_newbcast:14 row_mask:0xf bank_mask:0xf\n"
    "v_mul_f32 v244, v244, v254\n"
    "v_mul_f32 v246, v246, v255\n"
    "v_mfma_f32_16x16x32_bf16 a[248:251], a[192:195], a[156:159], a[248:251]\n"
    "v_add_f32_dpp v254, v253, v45 row_newbcast:13 row_mask:0xf bank_mask:0xf\n"
    "v_add_f32_dpp v255, v253, v47 row_newbcast:15 row_mask:0xf bank_mask:0xf\n"
    "v_mul_f32 v245, v245, v254\n"
    "v_mfma_f32_16x16x32_bf16 a[248:251], a[196:199], a[188:191], a[248:251]\n"
    "v_mul_f32 v247, v247, v255\n"
    "v_cvt_pk_bf16_f32 v250, v244, v245\n"
    "v_cvt_pk_bf16_f32 v251, v246, v247\n"
    "v_accvgpr_read_b32 v245, a40\n"
    "v_mfma_f32_16x16x32_bf16 v[216:219], a[196:199], a[184:187], v[216:219]\n"
    "buffer_store_dwordx4 v[248:251], v240, s[96:99], 0 offen sc1\n"
    "ds_write_b128 v243, v[248:251] offset:34816\n"
    "v_accvgpr_read_b32 v247, a41\n"
    "v_lshlrev_b32 v244, 16, v245\n"
    "v_lshlrev_b32 v246, 16, v247\n"
    "v_and_b32 v245, s58, v245\n"
    "v_mfma_f32_16x16x32_bf16 v[216:219], a[192:195], a[152:155], v[216:219]\n"
    "buffer_load_dwordx4 a[232:235], v240, s[20:23], 0 offen offset:2048 sc1\n"
    "v_and_b32 v247, s58, v247\n"
    "v_add_f32_dpp v254, v253, v72 row_newbcast:8 row_mask:0xf bank_mask:0xf\n"
    "v_add_f32_dpp v255, v253, v74 row_newbcast:10 row_mask:0xf bank_mask:0xf\n"
    "v_mul_f32 v244, v244, v254\n"
    "v_mfma_f32_16x16x32_bf16 v[184:187], a[192:195], a[148:151], v[184:187]\n"
    "buffer_load_dwordx4 a[236:239], v240, s[20:23], 0 offen offset:3072 sc1\n"
    "s_addk_i32 s20, 4096\n"
    "s_xor_b32 s96, s96, 0x101800\n"
    "v_mul_f32 v246, v246, v255\n"
    "v_add_f32_dpp v254, v253, v73 row_newbcast:9 row_mask:0xf bank_mask:0xf\n"
    "v_add_f32_dpp v255, v253, v75 row_newbcast:11 row_mask:0xf bank_mask:0xf\n"
    "v_mul_f32 v245, v245, v254\n"
    "v_mfma_f32_16x16x32_bf16 v[184:187], a[196:199], a[180:183], v[184:187]\n"
    "v_mul_f32 v247, v247, v255\n"
    "v_cvt_pk_bf16_f32 v248, v244, v245\n"
    "v_mfma_f32_16x16x32_bf16 v[152:155], a[196:199], a[176:179], v[152:155]\n"
    "v_cvt_pk_bf16_f32 v249, v246, v247\n"
    "v_accvgpr_read_b32 v245, a42\n"
    "v_accvgpr_read_b32 v247, a43\n"
    "v_lshlrev_b32 v244, 16, v245\n"
    "v_mfma_f32_16x16x32_bf16 v[152:155], a[192:195], a[144:147], v[152:155]\n"
    "v_lshlrev_b32 v246, 16, v247\n"
    "v_and_b32 v245, s58, v245\n"
    "v_and_b32 v247, s58, v247\n"
    "v_add_f32_dpp v254, v253, v76 row_newbcast:12 row_mask:0xf bank_mask:0xf\n"
    "v_mfma_f32_16x16x32_bf16 v[120:123], a[192:195], a[140:143], v[120:123]\n"
    "v_add_f32_dpp v255, v253, v78 row_newbcast:14 row_mask:0xf bank_mask:0xf\n"
    "v_mul_f32 v244, v244, v254\n"
    "v_mul_f32 v246, v246, v255\n"
    "v_mfma_f32_16x16x32_bf16 v[120:123], a[196:199], a[172:175], v[120:123]\n"
    "v_add_f32_dpp v254, v253, v77 row_newbcast:13 row_mask:0xf bank_mask:0xf\n"
    "v_add_f32_dpp v255, v253, v79 row_newbcast:15 row_mask:0xf bank_mask:0xf\n"
    "v_mul_f32 v245, v245, v254\n"
    "v_mfma_f32_16x16x32_bf16 v[88:91], a[196:199], a[168:171], v[88:91]\n"
    "v_mul_f32 v247, v247, v255\n"
    "v_cvt_pk_bf16_f32 v250, v244, v245\n"
    "v_cvt_pk_bf16_f32 v251, v246, v247\n"
    "v_accvgpr_read_b32 v245, a44\n"
    "v_mfma_f32_16x16x32_bf16 v[88:91], a[192:195], a[136:139], v[88:91]\n"
    "buffer_store_dwordx4 v[248:251], v240, s[96:99], 0 offen sc1\n"
    "ds_write_b128 v243, v[248:251] offset:36864\n"
    "v_accvgpr_read_b32 v247, a45\n"
    "v_lshlrev_b32 v244, 16, v245\n"
    "v_lshlrev_b32 v246, 16, v247\n"
    "v_and_b32 v245, s58, v245\n"
    "v_mfma_f32_16x16x32_bf16 v[56:59], a[192:195], a[132:135], v[56:59]\n"
    "s_xor_b32 s96, s96, 0x800\n"
    "v_and_b32 v247, s58, v247\n"
    "v_add_f32_dpp v254, v253, v104 row_newbcast:8 row_mask:0xf bank_mask:0xf\n"
    "v_add_f32_dpp v255, v253, v106 row_newbcast:10 row_mask:0xf bank_mask:0xf\n"
    "v_mul_f32 v244, v244, v254\n"
    "v_mfma_f32_16x16x32_bf16 v[56:59], a[196:199], a[164:167], v[56:59]\n"
    "v_mul_f32 v246, v246, v255\n"
    "v_add_f32_dpp v254, v253, v105 row_newbcast:9 row_mask:0xf bank_mask:0xf\n"
    "v_add_f32_dpp v255, v253, v107 row_newbcast:11 row_mask:0xf bank_mask:0xf\n"
    "v_mfma_f32_16x16x32_bf16 v[24:27], a[196:199], a[160:163], v[24:27]\n"
    "v_mul_f32 v245, v245, v254\n"
    "v_mul_f32 v247, v247, v255\n"
    "v_cvt_pk_bf16_f32 v248, v244, v245\n"
    "v_mfma_f32_16x16x32_bf16 v[24:27], a[192:195], a[128:131], v[24:27]\n"
    "s_waitcnt vmcnt(14)\n"
    "v_cvt_pk_bf16_f32 v249, v246, v247\n"
    "v_accvgpr_read_b32 v245, a46\n"
    "v_accvgpr_read_b32 v247, a47\n"
    "v_lshlrev_b32 v244, 16, v245\n"
    "v_mfma_f32_16x16x32_bf16 v[28:31], a[200:203], a[128:131], v[28:31]\n"
    "v_lshlrev_b32 v246, 16, v247\n"
    "v_and_b32 v245, s58, v245\n"
    "v_and_b32 v247, s58, v247\n"
    "v_add_f32_dpp v254, v253, v108 row_newbcast:12 row_mask:0xf bank_mask:0xf\n"
    "v_mfma_f32_16x16x32_bf16 v[28:31], a[204:207], a[160:163], v[28:31]\n"
    "v_add_f32_dpp v255, v253, v110 row_newbcast:14 row_mask:0xf bank_mask:0xf\n"
    "v_mul_f32 v244, v244, v254\n"
    "v_mul_f32 v246, v246, v255\n"
    "v_mfma_f32_16x16x32_bf16 v[60:63], a[204:207], a[164:167], v[60:63]\n"
    "v_add_f32_dpp v254, v253, v109 row_newbcast:13 row_mask:0xf bank_mask:0xf\n"
    "v_add_f32_dpp v255, v253, v111 row_newbcast:15 row_mask:0xf bank_mask:0xf\n"
    "v_mul_f32 v245, v245, v254\n"
    "v_mfma_f32_16x16x32_bf16 v[60:63], a[200:203], a[132:135], v[60:63]\n"
    "v_mul_f32 v247, v247, v255\n"
    "v_cvt_pk_bf16_f32 v250, v244, v245\n"
    "v_cvt_pk_bf16_f32 v251, v246, v247\n"
    "v_accvgpr_read_b32 v245, a48\n"
    "v_mfma_f32_16x16x32_bf16 v[92:95], a[200:203], a[136:139], v[92:95]\n"
    "buffer_store_dwordx4 v[248:251], v240, s[96:99], 0 offen sc1\n"
    "ds_write_b128 v243, v[248:251] offset:38912\n"
    "v_accvgpr_read_b32 v247, a49\n"
    "v_lshlrev_b32 v244, 16, v245\n"
    "v_lshlrev_b32 v246, 16, v247\n"
    "v_and_b32 v245, s58, v245\n"
    "v_mfma_f32_16x16x32_bf16 v[92:95], a[204:207], a[168:171], v[92:95]\n"
    "buffer_load_dwordx4 a[192:195], v240, s[20:23], 0 offen offset:0 sc1\n"
    "v_and_b32 v247, s58, v247\n"
    "v_add_f32_dpp v254, v253, v136 row_newbcast:8 row_mask:0xf bank_mask:0xf\n"
    "v_add_f32_dpp v255, v253, v138 row_newbcast:10 row_mask:0xf bank_mask:0xf\n"
    "v_mul_f32 v244, v244, v254\n"
    "v_mfma_f32_16x16x32_bf16 v[124:127], a[204:207], a[172:175], v[124:127]\n"
    "buffer_load_dwordx4 a[196:199], v240, s[20:23], 0 offen offset:1024 sc1\n"
    "ds_read_b128 a[128:131], v241 offset:0\n"
    "ds_read_b128 a[160:163], v242 offset:0\n"
    "v_mul_f32 v246, v246, v255\n"
    "v_add_f32_dpp v254, v253, v137 row_newbcast:9 row_mask:0xf bank_mask:0xf\n"
    "v_add_f32_dpp v255, v253, v139 row_newbcast:11 row_mask:0xf bank_mask:0xf\n"
    "v_mfma_f32_16x16x32_bf16 v[124:127], a[200:203], a[140:143], v[124:127]\n"
    "ds_read_b128 a[164:167], v242 offset:2048\n"
    "v_mul_f32 v245, v245, v254\n"
    "v_mul_f32 v247, v247, v255\n"
    "v_cvt_pk_bf16_f32 v248, v244, v245\n"
    "v_mfma_f32_16x16x32_bf16 v[156:159], a[200:203], a[144:147], v[156:159]\n"
    "ds_read_b128 a[132:135], v241 offset:2048\n"
    "v_cvt_pk_bf16_f32 v249, v246, v247\n"
    "v_mfma_f32_16x16x32_bf16 v[156:159], a[204:207], a[176:179], v[156:159]\n"
    "ds_read_b128 a[136:139], v241 offset:4096\n"
    "v_accvgpr_read_b32 v245, a50\n"
    "v_mfma_f32_16x16x32_bf16 v[188:191], a[204:207], a[180:183], v[188:191]\n"
    "ds_read_b128 a[168:171], v242 offset:4096\n"
    "v_accvgpr_read_b32 v247, a51\n"
    "v_mfma_f32_16x16x32_bf16 v[188:191], a[200:203], a[148:151], v[188:191]\n"
    "ds_read_b128 a[172:175], v242 offset:6144\n"
    "v_lshlrev_b32 v244, 16, v245\n"
    "v_mfma_f32_16x16x32_bf16 v[220:223], a[200:203], a[152:155], v[220:223]\n"
    "ds_read_b128 a[140:143], v241 offset:6144\n"
    "v_lshlrev_b32 v246, 16, v247\n"
    "v_mfma_f32_16x16x32_bf16 v[220:223], a[204:207], a[184:187], v[220:223]\n"
    "ds_read_b128 a[144:147], v241 offset:8192\n"
    "v_and_b32 v245, s58, v245\n"
    "v_mfma_f32_16x16x32_bf16 a[252:255], a[204:207], a[188:191], a[252:255]\n"
    "ds_read_b128 a[176:179], v242 offset:8192\n"
    "v_and_b32 v247, s58, v247\n"
    "v_mfma_f32_16x16x32_bf16 a[252:255], a[200:203], a[156:159], a[252:255]\n"
    "s_waitcnt vmcnt(13) lgkmcnt(8)\n"
    "ds_read_b128 a[180:183], v242 offset:10240\n"
    "v_add_f32_dpp v254, v253, v140 row_newbcast:12 row_mask:0xf bank_mask:0xf\n"
    "v_mfma_f32_16x16x32_bf16 v[16:19], a[208:211], a[128:131], v[16:19]\n"
    "ds_read_b128 a[148:151], v241 offset:10240\n"
    "v_add_f32_dpp v255, v253, v142 row_newbcast:14 row_mask:0xf bank_mask:0xf\n"
    "v_mfma_f32_16x16x32_bf16 v[16:19], a[212:215], a[160:163], v[16:19]\n"
    "s_waitcnt lgkmcnt(8)\n"
    "ds_read_b128 a[152:155], v241 offset:12288\n"
    "v_mul_f32 v244, v244, v254\n"
    "v_mfma_f32_16x16x32_bf16 v[48:51], a[212:215], a[164:167], v[48:51]\n"
    "ds_read_b128 a[184:187], v242 offset:12288\n"
    "ds_read_b128 a[188:191], v242 offset:14336\n"
    "ds_read_b128 a[156:159], v241 offset:14336\n"
    "v_mfma_f32_16x16x32_bf16 v[48:51], a[208:211], a[132:135], v[48:51]\n"
    "s_waitcnt lgkmcnt(10)\n"
    "buffer_load_dwordx4 a[200:203], v240, s[20:23], 0 offen offset:2048 sc1\n"
    "v_mfma_f32_16x16x32_bf16 v[80:83], a[208:211], a[136:139], v[80:83]\n"
    "buffer_load_dwordx4 a[204:207], v240, s[20:23], 0 offen offset:3072 sc1\n"
    "s_addk_i32 s20, 4096\n"
    "s_xor_b32 s96, s96, 0x185800\n"
    "v_mfma_f32_16x16x32_bf16 v[80:83], a[212:215], a[168:171], v[80:83]\n"
    "s_waitcnt lgkmcnt(8)\n"
    "v_mul_f32 v246, v246, v255\n"
    "v_add_f32_dpp v254, v253, v141 row_newbcast:13 row_mask:0xf bank_mask:0xf\n"
    "v_mfma_f32_16x16x32_bf16 v[112:115], a[212:215], a[172:175], v[112:115]\n"
    "v_add_f32_dpp v255, v253, v143 row_newbcast:15 row_mask:0xf bank_mask:0xf\n"
    "v_mul_f32 v245, v245, v254\n"
    "v_mfma_f32_16x16x32_bf16 v[112:115], a[208:211], a[140:143], v[112:115]\n"
    "s_waitcnt lgkmcnt(6)\n"
    "v_mul_f32 v247, v247, v255\n"
    "v_cvt_pk_bf16_f32 v250, v244, v245\n"
    "v_mfma_f32_16x16x32_bf16 v[144:147], a[208:211], a[144:147], v[144:147]\n"
    "v_cvt_pk_bf16_f32 v251, v246, v247\n"
    "v_accvgpr_read_b32 v245, a52\n"
    "v_mfma_f32_16x16x32_bf16 v[144:147], a[212:215], a[176:179], v[144:147]\n"
    "s_waitcnt lgkmcnt(4)\n"
    "v_accvgpr_read_b32 v247, a53\n"
    "v_lshlrev_b32 v244, 16, v245\n"
    "v_mfma_f32_16x16x32_bf16 v[176:179], a[212:215], a[180:183], v[176:179]\n"
    "v_lshlrev_b32 v246, 16, v247\n"
    "v_and_b32 v245, s58, v245\n"
    "v_mfma_f32_16x16x32_bf16 v[176:179], a[208:211], a[148:151], v[176:179]\n"
    "s_waitcnt lgkmcnt(2)\n"
    "v_and_b32 v247, s58, v247\n"
    "v_add_f32_dpp v254, v253, v168 row_newbcast:8 row_mask:0xf bank_mask:0xf\n"
    "v_mfma_f32_16x16x32_bf16 v[208:211], a[208:211], a[152:155], v[208:211]\n"
    "buffer_store_dwordx4 v[248:251], v240, s[96:99], 0 offen sc1\n"
    "ds_write_b128 v243, v[248:251] offset:40960\n"
    "v_mfma_f32_16x16x32_bf16 v[208:211], a[212:215], a[184:187], v[208:211]\n"
    "s_waitcnt lgkmcnt(1)\n"
    "v_add_f32_dpp v255, v253, v170 row_newbcast:10 row_mask:0xf bank_mask:0xf\n"
    "v_mul_f32 v244, v244, v254\n"
    "v_mfma_f32_16x16x32_bf16 a[240:243], a[212:215], a[188:191], a[240:243]\n"
    "v_mul_f32 v246, v246, v255\n"
    "v_add_f32_dpp v254, v253, v169 row_newbcast:9 row_mask:0xf bank_mask:0xf\n"
    "v_mfma_f32_16x16x32_bf16 a[240:243], a[208:211], a[156:159], a[240:243]\n"
    "s_waitcnt vmcnt(14)\n"
    "buffer_load_dwordx4 a[208:211], v240, s[20:23], 0 offen offset:0 sc1\n"
    "v_mfma_f32_16x16x32_bf16 a[244:247], a[216:219], a[156:159], a[244:247]\n"
    "buffer_load_dwordx4 a[212:215], v240, s[20:23], 0 offen offset:1024 sc1\n"
    "s_xor_b32 s96, s96, 0x800\n"
    "v_mfma_f32_16x16x32_bf16 a[244:247], a[220:223], a[188:191], a[244:247]\n"
    "v_add_f32_dpp v255, v253, v171 row_newbcast:11 row_mask:0xf bank_mask:0xf\n"
    "v_mul_f32 v245, v245, v254\n"
    "v_mfma_f32_16x16x32_bf16 v[212:215], a[220:223], a[184:187], v[212:215]\n"
    "v_mul_f32 v247, v247, v255\n"
    "v_cvt_pk_bf16_f32 v248, v244, v245\n"
    "v_mfma_f32_16x16x32_bf16 v[212:215], a[216:219], a[152:155], v[212:215]\n"
    "v_cvt_pk_bf16_f32 v249, v246, v247\n"
    "v_accvgpr_read_b32 v245, a54\n"
    "v_mfma_f32_16x16x32_bf16 v[180:183], a[216:219], a[148:151], v[180:183]\n"
    "v_accvgpr_read_b32 v247, a55\n"
    "v_lshlrev_b32 v244, 16, v245\n"
    "v_mfma_f32_16x16x32_bf16 v[180:183], a[220:223], a[180:183], v[180:183]\n"
    "v_lshlrev_b32 v246, 16, v247\n"
    "v_and_b32 v245, s58, v245\n"
    "v_mfma_f32_16x16x32_bf16 v[148:151], a[220:223], a[176:179], v[148:151]\n"
    "v_and_b32 v247, s58, v247\n"
    "v_add_f32_dpp v254, v253, v172 row_newbcast:12 row_mask:0xf bank_mask:0xf\n"
    "v_mfma_f32_16x16x32_bf16 v[148:151], a[216:219], a[144:147], v[148:151]\n"
    "v_add_f32_dpp v255, v253, v174 row_newbcast:14 row_mask:0xf bank_mask:0xf\n"
    "v_mul_f32 v244, v244, v254\n"
    "v_mfma_f32_16x16x32_bf16 v[116:119], a[216:219], a[140:143], v[116:119]\n"
    "v_mul_f32 v246, v246, v255\n"
    "v_add_f32_dpp v254, v253, v173 row_newbcast:13 row_mask:0xf bank_mask:0xf\n"
    "v_mfma_f32_16x16x32_bf16 v[116:119], a[220:223], a[172:175], v[116:119]\n"
    "v_add_f32_dpp v255, v253, v175 row_newbcast:15 row_mask:0xf bank_mask:0xf\n"
    "v_mul_f32 v245, v245, v254\n"
    "v_mul_f32 v247, v247, v255\n"
    "v_mfma_f32_16x16x32_bf16 v[84:87], a[220:223], a[168:171], v[84:87]\n"
    "v_cvt_pk_bf16_f32 v250, v244, v245\n"
    "v_cvt_pk_bf16_f32 v251, v246, v247\n"
    "v_accvgpr_read_b32 v245, a56\n"
    "v_accvgpr_read_b32 v247, a57\n"
    "v_mfma_f32_16x16x32_bf16 v[84:87], a[216:219], a[136:139], v[84:87]\n"
    "buffer_store_dwordx4 v[248:251], v240, s[96:99], 0 offen sc1\n"
    "ds_write_b128 v243, v[248:251] offset:43008\n"
    "v_lshlrev_b32 v244, 16, v245\n"
    "v_lshlrev_b32 v246, 16, v247\n"
    "v_and_b32 v245, s58, v245\n"
    "v_mfma_f32_16x16x32_bf16 v[52:55], a[216:219], a[132:135], v[52:55]\n"
    "s_xor_b32 s96, s96, 0x101800\n"
    "v_and_b32 v247, s58, v247\n"
    "v_add_f32_dpp v254, v253, v200 row_newbcast:8 row_mask:0xf bank_mask:0xf\n"
    "v_add_f32_dpp v255, v253, v202 row_newbcast:10 row_mask:0xf bank_mask:0xf\n"
    "v_mul_f32 v244, v244, v254\n"
    "v_mfma_f32_16x16x32_bf16 v[52:55], a[220:223], a[164:167], v[52:55]\n"
    "v_mul_f32 v246, v246, v255\n"
    "v_add_f32_dpp v254, v253, v201 row_newbcast:9 row_mask:0xf bank_mask:0xf\n"
    "v_add_f32_dpp v255, v253, v203 row_newbcast:11 row_mask:0xf bank_mask:0xf\n"
    "v_mul_f32 v245, v245, v254\n"
    "v_mfma_f32_16x16x32_bf16 v[20:23], a[220:223], a[160:163], v[20:23]\n"
    "v_mul_f32 v247, v247, v255\n"
    "v_cvt_pk_bf16_f32 v248, v244, v245\n"
    "v_cvt_pk_bf16_f32 v249, v246, v247\n"
    "v_mfma_f32_16x16x32_bf16 v[20:23], a[216:219], a[128:131], v[20:23]\n"
    "s_waitcnt vmcnt(14)\n"
    "v_accvgpr_read_b32 v245, a58\n"
    "v_accvgpr_read_b32 v247, a59\n"
    "v_lshlrev_b32 v244, 16, v245\n"
    "v_lshlrev_b32 v246, 16, v247\n"
    "v_mfma_f32_16x16x32_bf16 v[24:27], a[224:227], a[128:131], v[24:27]\n"
    "v_and_b32 v245, s58, v245\n"
    "v_and_b32 v247, s58, v247\n"
    "v_add_f32_dpp v254, v253, v204 row_newbcast:12 row_mask:0xf bank_mask:0xf\n"
    "v_mfma_f32_16x16x32_bf16 v[24:27], a[228:231], a[160:163], v[24:27]\n"
    "v_add_f32_dpp v255, v253, v206 row_newbcast:14 row_mask:0xf bank_mask:0xf\n"
    "v_mul_f32 v244, v244, v254\n"
    "v_mul_f32 v246, v246, v255\n"
    "v_mfma_f32_16x16x32_bf16 v[56:59], a[228:231], a[164:167], v[56:59]\n"
    "v_add_f32_dpp v254, v253, v205 row_newbcast:13 row_mask:0xf bank_mask:0xf\n"
    "v_add_f32_dpp v255, v253, v207 row_newbcast:15 row_mask:0xf bank_mask:0xf\n"
    "v_mul_f32 v245, v245, v254\n"
    "v_mul_f32 v247, v247, v255\n"
    "v_mfma_f32_16x16x32_bf16 v[56:59], a[224:227], a[132:135], v[56:59]\n"
    "v_cvt_pk_bf16_f32 v250, v244, v245\n"
    "v_cvt_pk_bf16_f32 v251, v246, v247\n"
    "v_accvgpr_read_b32 v245, a60\n"
    "v_accvgpr_read_b32 v247, a61\n"
    "v_mfma_f32_16x16x32_bf16 v[88:91], a[224:227], a[136:139], v[88:91]\n"
    "buffer_store_dwordx4 v[248:251], v240, s[96:99], 0 offen sc1\n"
    "ds_write_b128 v243, v[248:251] offset:45056\n"
    "v_lshlrev_b32 v244, 16, v245\n"
    "v_lshlrev_b32 v246, 16, v247\n"
    "v_and_b32 v245, s58, v245\n"
    "v_mfma_f32_16x16x32_bf16 v[88:91], a[228:231], a[168:171], v[88:91]\n"
    "buffer_load_dwordx4 a[216:219], v240, s[20:23], 0 offen offset:2048 sc1\n"
    "v_and_b32 v247, s58, v247\n"
    "v_add_f32_dpp v254, v253, v232 row_newbcast:8 row_mask:0xf bank_mask:0xf\n"
    "v_add_f32_dpp v255, v253, v234 row_newbcast:10 row_mask:0xf bank_mask:0xf\n"
    "v_mul_f32 v244, v244, v254\n"
    "v_mfma_f32_16x16x32_bf16 v[120:123], a[228:231], a[172:175], v[120:123]\n"
    "buffer_load_dwordx4 a[220:223], v240, s[20:23], 0 offen offset:3072 sc1\n"
    "s_addk_i32 s20, 4096\n"
    "s_xor_b32 s96, s96, 0x800\n"
    "v_mul_f32 v246, v246, v255\n"
    "v_add_f32_dpp v254, v253, v233 row_newbcast:9 row_mask:0xf bank_mask:0xf\n"
    "v_add_f32_dpp v255, v253, v235 row_newbcast:11 row_mask:0xf bank_mask:0xf\n"
    "v_mul_f32 v245, v245, v254\n"
    "v_mfma_f32_16x16x32_bf16 v[120:123], a[224:227], a[140:143], v[120:123]\n"
    "v_mul_f32 v247, v247, v255\n"
    "v_cvt_pk_bf16_f32 v248, v244, v245\n"
    "v_cvt_pk_bf16_f32 v249, v246, v247\n"
    "v_mfma_f32_16x16x32_bf16 v[152:155], a[224:227], a[144:147], v[152:155]\n"
    "v_accvgpr_read_b32 v245, a62\n"
    "v_accvgpr_read_b32 v247, a63\n"
    "v_lshlrev_b32 v244, 16, v245\n"
    "v_mfma_f32_16x16x32_bf16 v[152:155], a[228:231], a[176:179], v[152:155]\n"
    "v_lshlrev_b32 v246, 16, v247\n"
    "v_and_b32 v245, s58, v245\n"
    "v_and_b32 v247, s58, v247\n"
    "v_add_f32_dpp v254, v253, v236 row_newbcast:12 row_mask:0xf bank_mask:0xf\n"
    "v_mfma_f32_16x16x32_bf16 v[184:187], a[228:231], a[180:183], v[184:187]\n"
    "v_add_f32_dpp v255, v253, v238 row_newbcast:14 row_mask:0xf bank_mask:0xf\n"
    "v_mul_f32 v244, v244, v254\n"
    "v_mul_f32 v246, v246, v255\n"
    "v_mfma_f32_16x16x32_bf16 v[184:187], a[224:227], a[148:151], v[184:187]\n"
    "v_add_f32_dpp v254, v253, v237 row_newbcast:13 row_mask:0xf bank_mask:0xf\n"
    "v_add_f32_dpp v255, v253, v239 row_newbcast:15 row_mask:0xf bank_mask:0xf\n"
    "v_mul_f32 v245, v245, v254\n"
    "v_mul_f32 v247, v247, v255\n"
    "v_mfma_f32_16x16x32_bf16 v[216:219], a[224:227], a[152:155], v[216:219]\n"
    "v_cvt_pk_bf16_f32 v250, v244, v245\n"
    "v_cvt_pk_bf16_f32 v251, v246, v247\n"
    "v_mfma_f32_16x16x32_bf16 v[216:219], a[228:231], a[184:187], v[216:219]\n"
    "buffer_store_dwordx4 v[248:251], v240, s[96:99], 0 offen sc1\n"
    "ds_write_b128 v243, v[248:251] offset:47104\n"
    "s_waitcnt lgkmcnt(0)\n"
    "s_barrier\n"
    "v_mfma_f32_16x16x32_bf16 a[248:251], a[228:231], a[188:191], a[248:251]\n"
    "v_mfma_f32_16x16x32_bf16 a[248:251], a[224:227], a[156:159], a[248:251]\n"
    "s_waitcnt vmcnt(14)\n"
    "buffer_load_dwordx4 a[224:227], v240, s[20:23], 0 offen offset:0 sc1\n"
    "v_mfma_f32_16x16x32_bf16 a[252:255], a[232:235], a[156:159], a[252:255]\n"
    "buffer_load_dwordx4 a[228:231], v240, s[20:23], 0 offen offset:1024 sc1\n"
    "v_mfma_f32_16x16x32_bf16 a[252:255], a[236:239], a[188:191], a[252:255]\n"
    "v_mfma_f32_16x16x32_bf16 v[220:223], a[236:239], a[184:187], v[220:223]\n"
    "v_add_u32 v241, s66, v240\n"
    "v_mfma_f32_16x16x32_bf16 v[220:223], a[232:235], a[152:155], v[220:223]\n"
    "ds_read_b128 a[156:159], v241 offset:14336\n"
    "v_mfma_f32_16x16x32_bf16 v[188:191], a[232:235], a[148:151], v[188:191]\n"
    "ds_read_b128 a[188:191], v241 offset:15360\n"
    "v_mfma_f32_16x16x32_bf16 v[188:191], a[236:239], a[180:183], v[188:191]\n"
    "ds_read_b128 a[184:187], v241 offset:13312\n"
    "v_mfma_f32_16x16x32_bf16 v[156:159], a[236:239], a[176:179], v[156:159]\n"
    "ds_read_b128 a[152:155], v241 offset:12288\n"
    "v_mfma_f32_16x16x32_bf16 v[156:159], a[232:235], a[144:147], v[156:159]\n"
    "ds_read_b128 a[148:151], v241 offset:10240\n"
    "v_mfma_f32_16x16x32_bf16 v[124:127], a[232:235], a[140:143], v[124:127]\n"
    "ds_read_b128 a[180:183], v241 offset:11264\n"
    "v_mfma_f32_16x16x32_bf16 v[124:127], a[236:239], a[172:175], v[124:127]\n"
    "ds_read_b128 a[176:179], v241 offset:9216\n"
    "v_mfma_f32_16x16x32_bf16 v[92:95], a[236:239], a[168:171], v[92:95]\n"
    "ds_read_b128 a[144:147], v241 offset:8192\n"
    "v_mfma_f32_16x16x32_bf16 v[92:95], a[232:235], a[136:139], v[92:95]\n"
    "ds_read_b128 a[140:143], v241 offset:6144\n"
    "v_mfma_f32_16x16x32_bf16 v[60:63], a[232:235], a[132:135], v[60:63]\n"
    "ds_read_b128 a[172:175], v241 offset:7168\n"
    "v_mfma_f32_16x16x32_bf16 v[60:63], a[236:239], a[164:167], v[60:63]\n"
    "ds_read_b128 a[168:171], v241 offset:5120\n"
    "v_mfma_f32_16x16x32_bf16 v[28:31], a[236:239], a[160:163], v[28:31]\n"
    "ds_read_b128 a[136:139], v241 offset:4096\n"
    "v_mfma_f32_16x16x32_bf16 v[28:31], a[232:235], a[128:131], v[28:31]\n"
    "ds_read_b128 a[132:135], v241 offset:2048\n"
    "ds_read_b128 a[164:167], v241 offset:3072\n"
    "ds_read_b128 a[160:163], v241 offset:1024\n"
    "ds_read_b128 a[128:131], v241 offset:0\n"
    "buffer_load_dwordx4 a[232:235], v240, s[20:23], 0 offen offset:2048 sc1\n"
    "buffer_load_dwordx4 a[236:239], v240, s[20:23], 0 offen offset:3072 sc1\n"
    "s_addk_i32 s20, 4096\n"
    "LOOP_%=:\n"
    "s_waitcnt vmcnt(10) lgkmcnt(14)\n"
    "v_mfma_f32_16x16x32_bf16 v[224:227], a[192:195], a[156:159], v[224:227]\n"
    "s_add_u32 s52, s52, 2\n"
    "s_xor_b32 s54, s54, 0x40000\n"
    "s_xor_b32 s59, s59, 0x40000\n"
    "v_mfma_f32_16x16x32_bf16 v[224:227], a[196:199], a[188:191], v[224:227]\n"
    "s_waitcnt lgkmcnt(12)\n"
    "s_addk_i32 s55, 2048\n"
    "s_xor_b32 s57, s57, 65536\n"
    "v_mfma_f32_16x16x32_bf16 v[192:195], a[196:199], a[184:187], v[192:195]\n"
    "s_xor_b32 s66, s66, 65536\n"
    "s_xor_b32 s71, s71, 65536\n"
    "v_xor_b32 v243, 0x10000, v243\n"
    "v_mfma_f32_16x16x32_bf16 v[192:195], a[192:195], a[152:155], v[192:195]\n"
    "s_waitcnt lgkmcnt(10)\n"
    "v_accvgpr_read_b32 v245, a64\n"
    "v_accvgpr_read_b32 v247, a65\n"
    "v_mfma_f32_16x16x32_bf16 v[160:163], a[192:195], a[148:151], v[160:163]\n"
    "v_lshlrev_b32 v244, 16, v245\n"
    "v_lshlrev_b32 v246, 16, v247\n"
    "v_mfma_f32_16x16x32_bf16 v[160:163], a[196:199], a[180:183], v[160:163]\n"
    "s_waitcnt lgkmcnt(8)\n"
    "v_and_b32 v245, s58, v245\n"
    "v_and_b32 v247, s58, v247\n"
    "v_mfma_f32_16x16x32_bf16 v[128:131], a[196:199], a[176:179], v[128:131]\n"
    "v_add_f32_dpp v254, v252, v16 row_newbcast:0 row_mask:0xf bank_mask:0xf\n"
    "v_add_f32_dpp v255, v252, v18 row_newbcast:2 row_mask:0xf bank_mask:0xf\n"
    "v_mfma_f32_16x16x32_bf16 v[128:131], a[192:195], a[144:147], v[128:131]\n"
    "s_waitcnt lgkmcnt(6)\n"
    "v_mul_f32 v244, v244, v254\n"
    "v_mul_f32 v246, v246, v255\n"
    "v_mfma_f32_16x16x32_bf16 v[96:99], a[192:195], a[140:143], v[96:99]\n"
    "v_add_f32_dpp v254, v252, v17 row_newbcast:1 row_mask:0xf bank_mask:0xf\n"
    "v_add_f32_dpp v255, v252, v19 row_newbcast:3 row_mask:0xf bank_mask:0xf\n"
    "v_mfma_f32_16x16x32_bf16 v[96:99], a[196:199], a[172:175], v[96:99]\n"
    "s_waitcnt lgkmcnt(4)\n"
    "v_mul_f32 v245, v245, v254\n"
    "v_mul_f32 v247, v247, v255\n"
    "v_mfma_f32_16x16x32_bf16 v[64:67], a[196:199], a[168:171], v[64:67]\n"
    "v_cvt_pk_bf16_f32 v248, v244, v245\n"
    "v_cvt_pk_bf16_f32 v249, v246, v247\n"
    "v_mfma_f32_16x16x32_bf16 v[64:67], a[192:195], a[136:139], v[64:67]\n"
    "s_waitcnt lgkmcnt(2)\n"
    "v_accvgpr_read_b32 v245, a66\n"
    "v_accvgpr_read_b32 v247, a67\n"
    "v_mfma_f32_16x16x32_bf16 v[32:35], a[192:195], a[132:135], v[32:35]\n"
    "v_lshlrev_b32 v244, 16, v245\n"
    "v_lshlrev_b32 v246, 16, v247\n"
    "v_mfma_f32_16x16x32_bf16 v[32:35], a[196:199], a[164:167], v[32:35]\n"
    "s_waitcnt lgkmcnt(0)\n"
    "v_and_b32 v245, s58, v245\n"
    "v_and_b32 v247, s58, v247\n"
    "v_mfma_f32_16x16x32_bf16 v[0:3], a[196:199], a[160:163], v[0:3]\n"
    "s_xor_b32 s96, s54, 0x20000\n"
    "s_add_u32 s96, s96, s32\n"
    "s_addc_u32 s97, s33, 0\n"
    "v_mfma_f32_16x16x32_bf16 v[0:3], a[192:195], a[128:131], v[0:3]\n"
    "s_waitcnt vmcnt(8)\n"
    "buffer_load_dwordx4 a[192:195], v240, s[20:23], 0 offen offset:0 sc1\n"
    "v_mfma_f32_16x16x32_bf16 v[4:7], a[200:203], a[128:131], v[4:7]\n"
    "buffer_load_dwordx4 a[196:199], v240, s[20:23], 0 offen offset:1024 sc1\n"
    "v_add_f32_dpp v254, v252, v20 row_newbcast:4 row_mask:0xf bank_mask:0xf\n"
    "v_mfma_f32_16x16x32_bf16 v[4:7], a[204:207], a[160:163], v[4:7]\n"
    "v_add_f32_dpp v255, v252, v22 row_newbcast:6 row_mask:0xf bank_mask:0xf\n"
    "v_mul_f32 v244, v244, v254\n"
    "v_mfma_f32_16x16x32_bf16 v[36:39], a[204:207], a[164:167], v[36:39]\n"
    "v_mul_f32 v246, v246, v255\n"
    "v_add_f32_dpp v254, v252, v21 row_newbcast:5 row_mask:0xf bank_mask:0xf\n"
    "v_mfma_f32_16x16x32_bf16 v[36:39], a[200:203], a[132:135], v[36:39]\n"
    "v_add_f32_dpp v255, v252, v23 row_newbcast:7 row_mask:0xf bank_mask:0xf\n"
    "v_mul_f32 v245, v245, v254\n"
    "v_mfma_f32_16x16x32_bf16 v[68:71], a[200:203], a[136:139], v[68:71]\n"
    "v_mul_f32 v247, v247, v255\n"
    "v_cvt_pk_bf16_f32 v250, v244, v245\n"
    "v_mfma_f32_16x16x32_bf16 v[68:71], a[204:207], a[168:171], v[68:71]\n"
    "v_cvt_pk_bf16_f32 v251, v246, v247\n"
    "v_accvgpr_read_b32 v245, a68\n"
    "v_mfma_f32_16x16x32_bf16 v[100:103], a[204:207], a[172:175], v[100:103]\n"
    "v_accvgpr_read_b32 v247, a69\n"
    "v_lshlrev_b32 v244, 16, v245\n"
    "v_mfma_f32_16x16x32_bf16 v[100:103], a[200:203], a[140:143], v[100:103]\n"
    "v_lshlrev_b32 v246, 16, v247\n"
    "v_and_b32 v245, s58, v245\n"
    "v_mfma_f32_16x16x32_bf16 v[132:135], a[200:203], a[144:147], v[132:135]\n"
    "v_and_b32 v247, s58, v247\n"
    "v_add_f32_dpp v254, v252, v48 row_newbcast:0 row_mask:0xf bank_mask:0xf\n"
    "v_mfma_f32_16x16x32_bf16 v[132:135], a[204:207], a[176:179], v[132:135]\n"
    "v_add_f32_dpp v255, v252, v50 row_newbcast:2 row_mask:0xf bank_mask:0xf\n"
    "v_mul_f32 v244, v244, v254\n"
    "v_mfma_f32_16x16x32_bf16 v[164:167], a[204:207], a[180:183], v[164:167]\n"
    "v_mul_f32 v246, v246, v255\n"
    "v_add_f32_dpp v254, v252, v49 row_newbcast:1 row_mask:0xf bank_mask:0xf\n"
    "v_mfma_f32_16x16x32_bf16 v[164:167], a[200:203], a[148:151], v[164:167]\n"
    "v_add_f32_dpp v255, v252, v51 row_newbcast:3 row_mask:0xf bank_mask:0xf\n"
    "v_mul_f32 v245, v245, v254\n"
    "v_mfma_f32_16x16x32_bf16 v[196:199], a[200:203], a[152:155], v[196:199]\n"
    "v_mul_f32 v247, v247, v255\n"
    "v_mfma_f32_16x16x32_bf16 v[196:199], a[204:207], a[184:187], v[196:199]\n"
    "buffer_store_dwordx4 v[248:251], v240, s[96:99], 0 offen sc1\n"
    "ds_write_b128 v243, v[248:251] offset:0\n"
    "v_mfma_f32_16x16x32_bf16 v[228:231], a[204:207], a[188:191], v[228:231]\n"
    "v_cvt_pk_bf16_f32 v248, v244, v245\n"
    "v_cvt_pk_bf16_f32 v249, v246, v247\n"
    "v_mfma_f32_16x16x32_bf16 v[228:231], a[200:203], a[156:159], v[228:231]\n"
    "s_waitcnt vmcnt(9)\n"
    "buffer_load_dwordx4 a[200:203], v240, s[20:23], 0 offen offset:2048 sc1\n"
    "v_mfma_f32_16x16x32_bf16 v[232:235], a[208:211], a[156:159], v[232:235]\n"
    "buffer_load_dwordx4 a[204:207], v240, s[20:23], 0 offen offset:3072 sc1\n"
    "s_addk_i32 s20, 4096\n"
    "v_mfma_f32_16x16x32_bf16 v[232:235], a[212:215], a[188:191], v[232:235]\n"
    "v_accvgpr_read_b32 v245, a70\n"
    "v_accvgpr_read_b32 v247, a71\n"
    "v_mfma_f32_16x16x32_bf16 v[200:203], a[212:215], a[184:187], v[200:203]\n"
    "v_lshlrev_b32 v244, 16, v245\n"
    "v_lshlrev_b32 v246, 16, v247\n"
    "v_mfma_f32_16x16x32_bf16 v[200:203], a[208:211], a[152:155], v[200:203]\n"
    "v_and_b32 v245, s58, v245\n"
    "v_and_b32 v247, s58, v247\n"
    "v_mfma_f32_16x16x32_bf16 v[168:171], a[208:211], a[148:151], v[168:171]\n"
    "s_xor_b32 s96, s96, 0x800\n"
    "v_add_f32_dpp v254, v252, v52 row_newbcast:4 row_mask:0xf bank_mask:0xf\n"
    "v_add_f32_dpp v255, v252, v54 row_newbcast:6 row_mask:0xf bank_mask:0xf\n"
    "v_mfma_f32_16x16x32_bf16 v[168:171], a[212:215], a[180:183], v[168:171]\n"
    "v_mul_f32 v244, v244, v254\n"
    "v_mul_f32 v246, v246, v255\n"
    "v_mfma_f32_16x16x32_bf16 v[136:139], a[212:215], a[176:179], v[136:139]\n"
    "v_add_f32_dpp v254, v252, v53 row_newbcast:5 row_mask:0xf bank_mask:0xf\n"
    "v_add_f32_dpp v255, v252, v55 row_newbcast:7 row_mask:0xf bank_mask:0xf\n"
    "v_mfma_f32_16x16x32_bf16 v[136:139], a[208:211], a[144:147], v[136:139]\n"
    "v_mul_f32 v245, v245, v254\n"
    "v_mul_f32 v247, v247, v255\n"
    "v_mfma_f32_16x16x32_bf16 v[104:107], a[208:211], a[140:143], v[104:107]\n"
    "v_cvt_pk_bf16_f32 v250, v244, v245\n"
    "v_cvt_pk_bf16_f32 v251, v246, v247\n"
    "v_mfma_f32_16x16x32_bf16 v[104:107], a[212:215], a[172:175], v[104:107]\n"
    "v_accvgpr_read_b32 v245, a72\n"
    "v_accvgpr_read_b32 v247, a73\n"
    "v_mfma_f32_16x16x32_bf16 v[72:75], a[212:215], a[168:171], v[72:75]\n"
    "v_lshlrev_b32 v244, 16, v245\n"
    "v_lshlrev_b32 v246, 16, v247\n"
    "v_mfma_f32_16x16x32_bf16 v[72:75], a[208:211], a[136:139], v[72:75]\n"
    "v_and_b32 v245, s58, v245\n"
    "v_and_b32 v247, s58, v247\n"
    "v_mfma_f32_16x16x32_bf16 v[40:43], a[208:211], a[132:135], v[40:43]\n"
    "v_add_f32_dpp v254, v252, v80 row_newbcast:0 row_mask:0xf bank_mask:0xf\n"
    "v_add_f32_dpp v255, v252, v82 row_newbcast:2 row_mask:0xf bank_mask:0xf\n"
    "v_mfma_f32_16x16x32_bf16 v[40:43], a[212:215], a[164:167], v[40:43]\n"
    "v_mul_f32 v244, v244, v254\n"
    "v_mul_f32 v246, v246, v255\n"
    "v_mfma_f32_16x16x32_bf16 v[8:11], a[212:215], a[160:163], v[8:11]\n"
    "v_add_f32_dpp v254, v252, v81 row_newbcast:1 row_mask:0xf bank_mask:0xf\n"
    "v_add_f32_dpp v255, v252, v83 row_newbcast:3 row_mask:0xf bank_mask:0xf\n"
    "v_mfma_f32_16x16x32_bf16 v[8:11], a[208:211], a[128:131], v[8:11]\n"
    "s_waitcnt vmcnt(9)\n"
    "buffer_load_dwordx4 a[208:211], v240, s[20:23], 0 offen offset:0 sc1\n"
    "v_mfma_f32_16x16x32_bf16 v[12:15], a[216:219], a[128:131], v[12:15]\n"
    "buffer_load_dwordx4 a[212:215], v240, s[20:23], 0 offen offset:1024 sc1\n"
    "v_mul_f32 v245, v245, v254\n"
    "v_mfma_f32_16x16x32_bf16 v[12:15], a[220:223], a[160:163], v[12:15]\n"
    "v_mul_f32 v247, v247, v255\n"
    "v_mfma_f32_16x16x32_bf16 v[44:47], a[220:223], a[164:167], v[44:47]\n"
    "ds_read_b128 a[128:131], v241 offset:16384\n"
    "v_mfma_f32_16x16x32_bf16 v[44:47], a[216:219], a[132:135], v[44:47]\n"
    "buffer_store_dwordx4 v[248:251], v240, s[96:99], 0 offen sc1\n"
    "ds_write_b128 v243, v[248:251] offset:2048\n"
    "v_mfma_f32_16x16x32_bf16 v[76:79], a[216:219], a[136:139], v[76:79]\n"
    "ds_read_b128 a[160:163], v241 offset:17408\n"
    "v_cvt_pk_bf16_f32 v248, v244, v245\n"
    "v_mfma_f32_16x16x32_bf16 v[76:79], a[220:223], a[168:171], v[76:79]\n"
    "ds_read_b128 a[164:167], v241 offset:19456\n"
    "v_cvt_pk_bf16_f32 v249, v246, v247\n"
    "v_mfma_f32_16x16x32_bf16 v[108:111], a[220:223], a[172:175], v[108:111]\n"
    "ds_read_b128 a[132:135], v241 offset:18432\n"
    "v_accvgpr_read_b32 v245, a74\n"
    "v_mfma_f32_16x16x32_bf16 v[108:111], a[216:219], a[140:143], v[108:111]\n"
    "ds_read_b128 a[136:139], v241 offset:20480\n"
    "v_accvgpr_read_b32 v247, a75\n"
    "v_mfma_f32_16x16x32_bf16 v[140:143], a[216:219], a[144:147], v[140:143]\n"
    "ds_read_b128 a[168:171], v241 offset:21504\n"
    "v_lshlrev_b32 v244, 16, v245\n"
    "v_mfma_f32_16x16x32_bf16 v[140:143], a[220:223], a[176:179], v[140:143]\n"
    "ds_read_b128 a[172:175], v241 offset:23552\n"
    "v_lshlrev_b32 v246, 16, v247\n"
    "v_mfma_f32_16x16x32_bf16 v[172:175], a[220:223], a[180:183], v[172:175]\n"
    "ds_read_b128 a[140:143], v241 offset:22528\n"
    "v_and_b32 v245, s58, v245\n"
    "v_mfma_f32_16x16x32_bf16 v[172:175], a[216:219], a[148:151], v[172:175]\n"
    "ds_read_b128 a[144:147], v241 offset:24576\n"
    "v_and_b32 v247, s58, v247\n"
    "v_mfma_f32_16x16x32_bf16 v[204:207], a[216:219], a[152:155], v[204:207]\n"
    "ds_read_b128 a[176:179], v241 offset:25600\n"
    "v_add_f32_dpp v254, v252, v84 row_newbcast:4 row_mask:0xf bank_mask:0xf\n"
    "v_mfma_f32_16x16x32_bf16 v[204:207], a[220:223], a[184:187], v[204:207]\n"
    "ds_read_b128 a[180:183], v241 offset:27648\n"
    "v_add_f32_dpp v255, v252, v86 row_newbcast:6 row_mask:0xf bank_mask:0xf\n"
    "v_mfma_f32_16x16x32_bf16 v[236:239], a[220:223], a[188:191], v[236:239]\n"
    "ds_read_b128 a[148:151], v241 offset:26624\n"
    "s_xor_b32 s96, s96, 0x101800\n"
    "v_mfma_f32_16x16x32_bf16 v[236:239], a[216:219], a[156:159], v[236:239]\n"
    "s_waitcnt vmcnt(10) lgkmcnt(10)\n"
    "ds_read_b128 a[152:155], v241 offset:28672\n"
    "v_mul_f32 v244, v244, v254\n"
    "v_mfma_f32_16x16x32_bf16 v[0:3], a[224:227], a[128:131], v[0:3]\n"
    "ds_read_b128 a[184:187], v241 offset:29696\n"
    "v_mul_f32 v246, v246, v255\n"
    "v_mfma_f32_16x16x32_bf16 v[0:3], a[228:231], a[160:163], v[0:3]\n"
    "s_waitcnt lgkmcnt(10)\n"
    "buffer_load_dwordx4 a[216:219], v240, s[20:23], 0 offen offset:2048 sc1\n"
    "v_mfma_f32_16x16x32_bf16 v[32:35], a[228:231], a[164:167], v[32:35]\n"
    "buffer_load_dwordx4 a[220:223], v240, s[20:23], 0 offen offset:3072 sc1\n"
    "s_addk_i32 s20, 4096\n"
    "v_mfma_f32_16x16x32_bf16 v[32:35], a[224:227], a[132:135], v[32:35]\n"
    "s_waitcnt lgkmcnt(8)\n"
    "ds_read_b128 a[188:191], v241 offset:31744\n"
    "v_add_f32_dpp v254, v252, v85 row_newbcast:5 row_mask:0xf bank_mask:0xf\n"
    "v_mfma_f32_16x16x32_bf16 v[64:67], a[224:227], a[136:139], v[64:67]\n"
    "ds_read_b128 a[156:159], v241 offset:30720\n"
    "v_add_f32_dpp v255, v252, v87 row_newbcast:7 row_mask:0xf bank_mask:0xf\n"
    "v_mfma_f32_16x16x32_bf16 v[64:67], a[228:231], a[168:171], v[64:67]\n"
    "s_waitcnt lgkmcnt(8)\n"
    "v_mul_f32 v245, v245, v254\n"
    "v_mul_f32 v247, v247, v255\n"
    "v_mfma_f32_16x16x32_bf16 v[96:99], a[228:231], a[172:175], v[96:99]\n"
    "v_cvt_pk_bf16_f32 v250, v244, v245\n"
    "v_cvt_pk_bf16_f32 v251, v246, v247\n"
    "v_mfma_f32_16x16x32_bf16 v[96:99], a[224:227], a[140:143], v[96:99]\n"
    "s_waitcnt lgkmcnt(6)\n"
    "v_accvgpr_read_b32 v245, a76\n"
    "v_accvgpr_read_b32 v247, a77\n"
    "v_mfma_f32_16x16x32_bf16 v[128:131], a[224:227], a[144:147], v[128:131]\n"
    "v_lshlrev_b32 v244, 16, v245\n"
    "v_lshlrev_b32 v246, 16, v247\n"
    "v_mfma_f32_16x16x32_bf16 v[128:131], a[228:231], a[176:179], v[128:131]\n"
    "s_waitcnt lgkmcnt(4)\n"
    "v_and_b32 v245, s58, v245\n"
    "v_and_b32 v247, s58, v247\n"
    "v_mfma_f32_16x16x32_bf16 v[160:163], a[228:231], a[180:183], v[160:163]\n"
    "v_add_f32_dpp v254, v252, v112 row_newbcast:0 row_mask:0xf bank_mask:0xf\n"
    "v_add_f32_dpp v255, v252, v114 row_newbcast:2 row_mask:0xf bank_mask:0xf\n"
    "v_mfma_f32_16x16x32_bf16 v[160:163], a[224:227], a[148:151], v[160:163]\n"
    "s_waitcnt lgkmcnt(2)\n"
    "v_mul_f32 v244, v244, v254\n"
    "v_mul_f32 v246, v246, v255\n"
    "v_mfma_f32_16x16x32_bf16 v[192:195], a[224:227], a[152:155], v[192:195]\n"
    "v_add_f32_dpp v254, v252, v113 row_newbcast:1 row_mask:0xf bank_mask:0xf\n"
    "v_add_f32_dpp v255, v252, v115 row_newbcast:3 row_mask:0xf bank_mask:0xf\n"
    "v_mfma_f32_16x16x32_bf16 v[192:195], a[228:231], a[184:187], v[192:195]\n"
    "s_waitcnt lgkmcnt(0)\n"
    "v_mul_f32 v245, v245, v254\n"
    "v_mul_f32 v247, v247, v255\n"
    "v_mfma_f32_16x16x32_bf16 v[224:227], a[228:231], a[188:191], v[224:227]\n"
    "v_mfma_f32_16x16x32_bf16 v[224:227], a[224:227], a[156:159], v[224:227]\n"
    "s_waitcnt vmcnt(10)\n"
    "buffer_load_dwordx4 a[224:227], v240, s[20:23], 0 offen offset:0 sc1\n"
    "v_mfma_f32_16x16x32_bf16 v[228:231], a[232:235], a[156:159], v[228:231]\n"
    "buffer_load_dwordx4 a[228:231], v240, s[20:23], 0 offen offset:1024 sc1\n"
    "v_mfma_f32_16x16x32_bf16 v[228:231], a[236:239], a[188:191], v[228:231]\n"
    "buffer_store_dwordx4 v[248:251], v240, s[96:99], 0 offen sc1\n"
    "ds_write_b128 v243, v[248:251] offset:4096\n"
    "v_mfma_f32_16x16x32_bf16 v[196:199], a[236:239], a[184:187], v[196:199]\n"
    "v_cvt_pk_bf16_f32 v248, v244, v245\n"
    "v_cvt_pk_bf16_f32 v249, v246, v247\n"
    "v_mfma_f32_16x16x32_bf16 v[196:199], a[232:235], a[152:155], v[196:199]\n"
    "v_accvgpr_read_b32 v245, a78\n"
    "v_accvgpr_read_b32 v247, a79\n"
    "v_mfma_f32_16x16x32_bf16 v[164:167], a[232:235], a[148:151], v[164:167]\n"
    "v_lshlrev_b32 v244, 16, v245\n"
    "v_lshlrev_b32 v246, 16, v247\n"
    "v_mfma_f32_16x16x32_bf16 v[164:167], a[236:239], a[180:183], v[164:167]\n"
    "v_and_b32 v245, s58, v245\n"
    "v_and_b32 v247, s58, v247\n"
    "v_mfma_f32_16x16x32_bf16 v[132:135], a[236:239], a[176:179], v[132:135]\n"
    "s_xor_b32 s96, s96, 0x800\n"
    "v_add_f32_dpp v254, v252, v116 row_newbcast:4 row_mask:0xf bank_mask:0xf\n"
    "v_add_f32_dpp v255, v252, v118 row_newbcast:6 row_mask:0xf bank_mask:0xf\n"
    "v_mfma_f32_16x16x32_bf16 v[132:135], a[232:235], a[144:147], v[132:135]\n"
    "v_mul_f32 v244, v244, v254\n"
    "v_mul_f32 v246, v246, v255\n"
    "v_mfma_f32_16x16x32_bf16 v[100:103], a[232:235], a[140:143], v[100:103]\n"
    "v_add_f32_dpp v254, v252, v117 row_newbcast:5 row_mask:0xf bank_mask:0xf\n"
    "v_add_f32_dpp v255, v252, v119 row_newbcast:7 row_mask:0xf bank_mask:0xf\n"
    "v_mfma_f32_16x16x32_bf16 v[100:103], a[236:239], a[172:175], v[100:103]\n"
    "v_mul_f32 v245, v245, v254\n"
    "v_mul_f32 v247, v247, v255\n"
    "v_mfma_f32_16x16x32_bf16 v[68:71], a[236:239], a[168:171], v[68:71]\n"
    "v_cvt_pk_bf16_f32 v250, v244, v245\n"
    "v_cvt_pk_bf16_f32 v251, v246, v247\n"
    "v_mfma_f32_16x16x32_bf16 v[68:71], a[232:235], a[136:139], v[68:71]\n"
    "v_accvgpr_read_b32 v245, a80\n"
    "v_accvgpr_read_b32 v247, a81\n"
    "v_mfma_f32_16x16x32_bf16 v[36:39], a[232:235], a[132:135], v[36:39]\n"
    "v_lshlrev_b32 v244, 16, v245\n"
    "v_lshlrev_b32 v246, 16, v247\n"
    "v_mfma_f32_16x16x32_bf16 v[36:39], a[236:239], a[164:167], v[36:39]\n"
    "v_and_b32 v245, s58, v245\n"
    "v_and_b32 v247, s58, v247\n"
    "v_mfma_f32_16x16x32_bf16 v[4:7], a[236:239], a[160:163], v[4:7]\n"
    "v_add_f32_dpp v254, v252, v144 row_newbcast:0 row_mask:0xf bank_mask:0xf\n"
    "v_add_f32_dpp v255, v252, v146 row_newbcast:2 row_mask:0xf bank_mask:0xf\n"
    "v_mfma_f32_16x16x32_bf16 v[4:7], a[232:235], a[128:131], v[4:7]\n"
    "s_waitcnt vmcnt(11)\n"
    "buffer_load_dwordx4 a[232:235], v240, s[20:23], 0 offen offset:2048 sc1\n"
    "v_mfma_f32_16x16x32_bf16 v[8:11], a[192:195], a[128:131], v[8:11]\n"
    "buffer_load_dwordx4 a[236:239], v240, s[20:23], 0 offen offset:3072 sc1\n"
    "s_addk_i32 s20, 4096\n"
    "v_mfma_f32_16x16x32_bf16 v[8:11], a[196:199], a[160:163], v[8:11]\n"
    "v_mul_f32 v244, v244, v254\n"
    "v_mul_f32 v246, v246, v255\n"
    "v_mfma_f32_16x16x32_bf16 v[40:43], a[196:199], a[164:167], v[40:43]\n"
    "v_add_f32_dpp v254, v252, v145 row_newbcast:1 row_mask:0xf bank_mask:0xf\n"
    "v_add_f32_dpp v255, v252, v147 row_newbcast:3 row_mask:0xf bank_mask:0xf\n"
    "v_mfma_f32_16x16x32_bf16 v[40:43], a[192:195], a[132:135], v[40:43]\n"
    "v_mul_f32 v245, v245, v254\n"
    "v_mul_f32 v247, v247, v255\n"
    "v_mfma_f32_16x16x32_bf16 v[72:75], a[192:195], a[136:139], v[72:75]\n"
    "v_mfma_f32_16x16x32_bf16 v[72:75], a[196:199], a[168:171], v[72:75]\n"
    "buffer_store_dwordx4 v[248:251], v240, s[96:99], 0 offen sc1\n"
    "ds_write_b128 v243, v[248:251] offset:6144\n"
    "v_mfma_f32_16x16x32_bf16 v[104:107], a[196:199], a[172:175], v[104:107]\n"
    "v_cvt_pk_bf16_f32 v248, v244, v245\n"
    "v_cvt_pk_bf16_f32 v249, v246, v247\n"
    "v_mfma_f32_16x16x32_bf16 v[104:107], a[192:195], a[140:143], v[104:107]\n"
    "v_accvgpr_read_b32 v245, a82\n"
    "v_accvgpr_read_b32 v247, a83\n"
    "v_mfma_f32_16x16x32_bf16 v[136:139], a[192:195], a[144:147], v[136:139]\n"
    "v_lshlrev_b32 v244, 16, v245\n"
    "v_lshlrev_b32 v246, 16, v247\n"
    "v_mfma_f32_16x16x32_bf16 v[136:139], a[196:199], a[176:179], v[136:139]\n"
    "v_and_b32 v245, s58, v245\n"
    "v_and_b32 v247, s58, v247\n"
    "v_mfma_f32_16x16x32_bf16 v[168:171], a[196:199], a[180:183], v[168:171]\n"
    "s_xor_b32 s96, s96, 0x185800\n"
    "v_add_f32_dpp v254, v252, v148 row_newbcast:4 row_mask:0xf bank_mask:0xf\n"
    "v_add_f32_dpp v255, v252, v150 row_newbcast:6 row_mask:0xf bank_mask:0xf\n"
    "v_mfma_f32_16x16x32_bf16 v[168:171], a[192:195], a[148:151], v[168:171]\n"
    "v_mul_f32 v244, v244, v254\n"
    "v_mul_f32 v246, v246, v255\n"
    "v_mfma_f32_16x16x32_bf16 v[200:203], a[192:195], a[152:155], v[200:203]\n"
    "v_add_f32_dpp v254, v252, v149 row_newbcast:5 row_mask:0xf bank_mask:0xf\n"
    "v_add_f32_dpp v255, v252, v151 row_newbcast:7 row_mask:0xf bank_mask:0xf\n"
    "v_mfma_f32_16x16x32_bf16 v[200:203], a[196:199], a[184:187], v[200:203]\n"
    "v_mul_f32 v245, v245, v254\n"
    "v_mul_f32 v247, v247, v255\n"
    "v_mfma_f32_16x16x32_bf16 v[232:235], a[196:199], a[188:191], v[232:235]\n"
    "v_cvt_pk_bf16_f32 v250, v244, v245\n"
    "v_cvt_pk_bf16_f32 v251, v246, v247\n"
    "v_mfma_f32_16x16x32_bf16 v[232:235], a[192:195], a[156:159], v[232:235]\n"
    "s_waitcnt vmcnt(11)\n"
    "buffer_load_dwordx4 a[192:195], v240, s[20:23], 0 offen offset:0 sc1\n"
    "v_mfma_f32_16x16x32_bf16 v[236:239], a[200:203], a[156:159], v[236:239]\n"
    "buffer_load_dwordx4 a[196:199], v240, s[20:23], 0 offen offset:1024 sc1\n"
    "v_accvgpr_read_b32 v245, a84\n"
    "v_mfma_f32_16x16x32_bf16 v[236:239], a[204:207], a[188:191], v[236:239]\n"
    "v_accvgpr_read_b32 v247, a85\n"
    "v_lshlrev_b32 v244, 16, v245\n"
    "v_mfma_f32_16x16x32_bf16 v[204:207], a[204:207], a[184:187], v[204:207]\n"
    "ds_read_b128 a[156:159], v241 offset:47104\n"
    "v_lshlrev_b32 v246, 16, v247\n"
    "v_mfma_f32_16x16x32_bf16 v[204:207], a[200:203], a[152:155], v[204:207]\n"
    "ds_read_b128 a[188:191], v241 offset:48128\n"
    "v_and_b32 v245, s58, v245\n"
    "v_mfma_f32_16x16x32_bf16 v[172:175], a[200:203], a[148:151], v[172:175]\n"
    "ds_read_b128 a[184:187], v241 offset:46080\n"
    "v_and_b32 v247, s58, v247\n"
    "v_mfma_f32_16x16x32_bf16 v[172:175], a[204:207], a[180:183], v[172:175]\n"
    "ds_read_b128 a[152:155], v241 offset:45056\n"
    "v_add_f32_dpp v254, v252, v176 row_newbcast:0 row_mask:0xf bank_mask:0xf\n"
    "v_mfma_f32_16x16x32_bf16 v[140:143], a[204:207], a[176:179], v[140:143]\n"
    "ds_read_b128 a[148:151], v241 offset:43008\n"
    "v_add_f32_dpp v255, v252, v178 row_newbcast:2 row_mask:0xf bank_mask:0xf\n"
    "v_mfma_f32_16x16x32_bf16 v[140:143], a[200:203], a[144:147], v[140:143]\n"
    "ds_read_b128 a[180:183], v241 offset:44032\n"
    "v_mul_f32 v244, v244, v254\n"
    "v_mfma_f32_16x16x32_bf16 v[108:111], a[200:203], a[140:143], v[108:111]\n"
    "s_add_u32 s62, s52, s64\n"
    "v_mov_b32 v242, s62\n"
    "s_mov_b32 s46, 4\n"
    "v_mfma_f32_16x16x32_bf16 v[108:111], a[204:207], a[172:175], v[108:111]\n"
    "buffer_store_dword v242, v240, s[44:47], 0 offen offset:0 sc1\n"
    "v_mfma_f32_16x16x32_bf16 v[76:79], a[204:207], a[168:171], v[76:79]\n"
    "ds_read_b128 a[176:179], v241 offset:41984\n"
    "v_mul_f32 v246, v246, v255\n"
    "v_mfma_f32_16x16x32_bf16 v[76:79], a[200:203], a[136:139], v[76:79]\n"
    "ds_read_b128 a[144:147], v241 offset:40960\n"
    "v_add_f32_dpp v254, v252, v177 row_newbcast:1 row_mask:0xf bank_mask:0xf\n"
    "v_mfma_f32_16x16x32_bf16 v[44:47], a[200:203], a[132:135], v[44:47]\n"
    "ds_read_b128 a[140:143], v241 offset:38912\n"
    "v_add_f32_dpp v255, v252, v179 row_newbcast:3 row_mask:0xf bank_mask:0xf\n"
    "v_mfma_f32_16x16x32_bf16 v[44:47], a[204:207], a[164:167], v[44:47]\n"
    "ds_read_b128 a[172:175], v241 offset:39936\n"
    "v_mul_f32 v245, v245, v254\n"
    "v_mfma_f32_16x16x32_bf16 v[12:15], a[204:207], a[160:163], v[12:15]\n"
    "ds_read_b128 a[168:171], v241 offset:37888\n"
    "v_mul_f32 v247, v247, v255\n"
    "v_mfma_f32_16x16x32_bf16 v[12:15], a[200:203], a[128:131], v[12:15]\n"
    "s_waitcnt vmcnt(12) lgkmcnt(9)\n"
    "ds_read_b128 a[136:139], v241 offset:36864\n"
    "v_mfma_f32_16x16x32_bf16 v[224:227], a[208:211], a[156:159], v[224:227]\n"
    "ds_read_b128 a[132:135], v241 offset:34816\n"
    "v_mfma_f32_16x16x32_bf16 v[224:227], a[212:215], a[188:191], v[224:227]\n"
    "s_waitcnt lgkmcnt(9)\n"
    "ds_read_b128 a[164:167], v241 offset:35840\n"
    "v_mfma_f32_16x16x32_bf16 v[192:195], a[212:215], a[184:187], v[192:195]\n"
    "buffer_load_dwordx4 a[200:203], v240, s[20:23], 0 offen offset:2048 sc1\n"
    "v_mfma_f32_16x16x32_bf16 v[192:195], a[208:211], a[152:155], v[192:195]\n"
    "s_waitcnt lgkmcnt(8)\n"
    "buffer_load_dwordx4 a[204:207], v240, s[20:23], 0 offen offset:3072 sc1\n"
    "s_addk_i32 s20, 4096\n"
    "ds_read_b128 a[160:163], v241 offset:33792\n"
    "v_mfma_f32_16x16x32_bf16 v[160:163], a[208:211], a[148:151], v[160:163]\n"
    "ds_read_b128 a[128:131], v241 offset:32768\n"
    "v_mfma_f32_16x16x32_bf16 v[160:163], a[212:215], a[180:183], v[160:163]\n"
    "s_waitcnt lgkmcnt(8)\n"
    "v_mfma_f32_16x16x32_bf16 v[128:131], a[212:215], a[176:179], v[128:131]\n"
    "buffer_store_dwordx4 v[248:251], v240, s[96:99], 0 offen sc1\n"
    "ds_write_b128 v243, v[248:251] offset:8192\n"
    "v_mfma_f32_16x16x32_bf16 v[128:131], a[208:211], a[144:147], v[128:131]\n"
    "s_waitcnt lgkmcnt(7)\n"
    "v_cvt_pk_bf16_f32 v248, v244, v245\n"
    "v_cvt_pk_bf16_f32 v249, v246, v247\n"
    "v_mfma_f32_16x16x32_bf16 v[96:99], a[208:211], a[140:143], v[96:99]\n"
    "v_accvgpr_read_b32 v245, a86\n"
    "v_accvgpr_read_b32 v247, a87\n"
    "v_mfma_f32_16x16x32_bf16 v[96:99], a[212:215], a[172:175], v[96:99]\n"
    "s_waitcnt lgkmcnt(5)\n"
    "v_lshlrev_b32 v244, 16, v245\n"
    "v_lshlrev_b32 v246, 16, v247\n"
    "v_mfma_f32_16x16x32_bf16 v[64:67], a[212:215], a[168:171], v[64:67]\n"
    "v_and_b32 v245, s58, v245\n"
    "v_and_b32 v247, s58, v247\n"
    "v_mfma_f32_16x16x32_bf16 v[64:67], a[208:211], a[136:139], v[64:67]\n"
    "s_waitcnt lgkmcnt(3)\n"
    "s_xor_b32 s96, s96, 0x800\n"
    "v_add_f32_dpp v254, v252, v180 row_newbcast:4 row_mask:0xf bank_mask:0xf\n"
    "v_mfma_f32_16x16x32_bf16 v[32:35], a[208:211], a[132:135], v[32:35]\n"
    "v_add_f32_dpp v255, v252, v182 row_newbcast:6 row_mask:0xf bank_mask:0xf\n"
    "v_mul_f32 v244, v244, v254\n"
    "v_mfma_f32_16x16x32_bf16 v[32:35], a[212:215], a[164:167], v[32:35]\n"
    "s_waitcnt lgkmcnt(1)\n"
    "v_mul_f32 v246, v246, v255\n"
    "v_add_f32_dpp v254, v252, v181 row_newbcast:5 row_mask:0xf bank_mask:0xf\n"
    "v_mfma_f32_16x16x32_bf16 v[0:3], a[212:215], a[160:163], v[0:3]\n"
    "v_add_f32_dpp v255, v252, v183 row_newbcast:7 row_mask:0xf bank_mask:0xf\n"
    "v_mul_f32 v245, v245, v254\n"
    "v_mfma_f32_16x16x32_bf16 v[0:3], a[208:211], a[128:131], v[0:3]\n"
    "s_waitcnt vmcnt(12)\n"
    "buffer_load_dwordx4 a[212:215], v240, s[20:23], 0 offen offset:0 sc1\n"
    "v_mfma_f32_16x16x32_bf16 v[4:7], a[216:219], a[128:131], v[4:7]\n"
    "v_mul_f32 v247, v247, v255\n"
    "v_cvt_pk_bf16_f32 v250, v244, v245\n"
    "v_mfma_f32_16x16x32_bf16 v[4:7], a[220:223], a[160:163], v[4:7]\n"
    "v_cvt_pk_bf16_f32 v251, v246, v247\n"
    "v_accvgpr_read_b32 v245, a88\n"
    "v_mfma_f32_16x16x32_bf16 v[36:39], a[220:223], a[164:167], v[36:39]\n"
    "v_accvgpr_read_b32 v247, a89\n"
    "v_lshlrev_b32 v244, 16, v245\n"
    "v_mfma_f32_16x16x32_bf16 v[36:39], a[216:219], a[132:135], v[36:39]\n"
    "v_lshlrev_b32 v246, 16, v247\n"
    "v_and_b32 v245, s58, v245\n"
    "v_mfma_f32_16x16x32_bf16 v[68:71], a[216:219], a[136:139], v[68:71]\n"
    "v_and_b32 v247, s58, v247\n"
    "v_add_f32_dpp v254, v252, v208 row_newbcast:0 row_mask:0xf bank_mask:0xf\n"
    "v_mfma_f32_16x16x32_bf16 v[68:71], a[220:223], a[168:171], v[68:71]\n"
    "v_add_f32_dpp v255, v252, v210 row_newbcast:2 row_mask:0xf bank_mask:0xf\n"
    "v_mul_f32 v244, v244, v254\n"
    "v_mfma_f32_16x16x32_bf16 v[100:103], a[220:223], a[172:175], v[100:103]\n"
    "v_mul_f32 v246, v246, v255\n"
    "v_add_f32_dpp v254, v252, v209 row_newbcast:1 row_mask:0xf bank_mask:0xf\n"
    "v_mfma_f32_16x16x32_bf16 v[100:103], a[216:219], a[140:143], v[100:103]\n"
    "v_add_f32_dpp v255, v252, v211 row_newbcast:3 row_mask:0xf bank_mask:0xf\n"
    "v_mul_f32 v245, v245, v254\n"
    "v_mfma_f32_16x16x32_bf16 v[132:135], a[216:219], a[144:147], v[132:135]\n"
    "v_mul_f32 v247, v247, v255\n"
    "v_mfma_f32_16x16x32_bf16 v[132:135], a[220:223], a[176:179], v[132:135]\n"
    "buffer_store_dwordx4 v[248:251], v240, s[96:99], 0 offen sc1\n"
    "ds_write_b128 v243, v[248:251] offset:10240\n"
    "v_mfma_f32_16x16x32_bf16 v[164:167], a[220:223], a[180:183], v[164:167]\n"
    "v_cvt_pk_bf16_f32 v248, v244, v245\n"
    "v_cvt_pk_bf16_f32 v249, v246, v247\n"
    "v_mfma_f32_16x16x32_bf16 v[164:167], a[216:219], a[148:151], v[164:167]\n"
    "v_accvgpr_read_b32 v245, a90\n"
    "v_accvgpr_read_b32 v247, a91\n"
    "v_mfma_f32_16x16x32_bf16 v[196:199], a[216:219], a[152:155], v[196:199]\n"
    "v_lshlrev_b32 v244, 16, v245\n"
    "v_lshlrev_b32 v246, 16, v247\n"
    "v_mfma_f32_16x16x32_bf16 v[196:199], a[220:223], a[184:187], v[196:199]\n"
    "v_and_b32 v245, s58, v245\n"
    "v_and_b32 v247, s58, v247\n"
    "v_mfma_f32_16x16x32_bf16 v[228:231], a[220:223], a[188:191], v[228:231]\n"
    "s_xor_b32 s96, s96, 0x101800\n"
    "v_add_f32_dpp v254, v252, v212 row_newbcast:4 row_mask:0xf bank_mask:0xf\n"
    "v_add_f32_dpp v255, v252, v214 row_newbcast:6 row_mask:0xf bank_mask:0xf\n"
    "v_mfma_f32_16x16x32_bf16 v[228:231], a[216:219], a[156:159], v[228:231]\n"
    "s_waitcnt vmcnt(12)\n"
    "buffer_load_dwordx4 a[216:219], v240, s[20:23], 0 offen offset:1024 sc1\n"
    "v_mfma_f32_16x16x32_bf16 v[232:235], a[224:227], a[156:159], v[232:235]\n"
    "buffer_load_dwordx4 a[220:223], v240, s[20:23], 0 offen offset:2048 sc1\n"
    "v_mul_f32 v244, v244, v254\n"
    "v_mfma_f32_16x16x32_bf16 v[232:235], a[228:231], a[188:191], v[232:235]\n"
    "v_mul_f32 v246, v246, v255\n"
    "v_add_f32_dpp v254, v252, v213 row_newbcast:5 row_mask:0xf bank_mask:0xf\n"
    "v_mfma_f32_16x16x32_bf16 v[200:203], a[228:231], a[184:187], v[200:203]\n"
    "v_add_f32_dpp v255, v252, v215 row_newbcast:7 row_mask:0xf bank_mask:0xf\n"
    "v_mul_f32 v245, v245, v254\n"
    "v_mfma_f32_16x16x32_bf16 v[200:203], a[224:227], a[152:155], v[200:203]\n"
    "v_mul_f32 v247, v247, v255\n"
    "v_cvt_pk_bf16_f32 v250, v244, v245\n"
    "v_mfma_f32_16x16x32_bf16 v[168:171], a[224:227], a[148:151], v[168:171]\n"
    "v_cvt_pk_bf16_f32 v251, v246, v247\n"
    "v_accvgpr_read_b32 v245, a92\n"
    "v_mfma_f32_16x16x32_bf16 v[168:171], a[228:231], a[180:183], v[168:171]\n"
    "v_accvgpr_read_b32 v247, a93\n"
    "v_lshlrev_b32 v244, 16, v245\n"
    "v_mfma_f32_16x16x32_bf16 v[136:139], a[228:231], a[176:179], v[136:139]\n"
    "v_lshlrev_b32 v246, 16, v247\n"
    "v_and_b32 v245, s58, v245\n"
    "v_mfma_f32_16x16x32_bf16 v[136:139], a[224:227], a[144:147], v[136:139]\n"
    "v_and_b32 v247, s58, v247\n"
    "v_accvgpr_read_b32 v254, a240\n"
    "v_mfma_f32_16x16x32_bf16 v[104:107], a[224:227], a[140:143], v[104:107]\n"
    "v_accvgpr_read_b32 v255, a242\n"
    "v_add_f32_dpp v254, v252, v254 row_newbcast:0 row_mask:0xf bank_mask:0xf\n"
    "v_mfma_f32_16x16x32_bf16 v[104:107], a[228:231], a[172:175], v[104:107]\n"
    "v_add_f32_dpp v255, v252, v255 row_newbcast:2 row_mask:0xf bank_mask:0xf\n"
    "v_mul_f32 v244, v244, v254\n"
    "v_mfma_f32_16x16x32_bf16 v[72:75], a[228:231], a[168:171], v[72:75]\n"
    "v_mul_f32 v246, v246, v255\n"
    "v_accvgpr_read_b32 v254, a241\n"
    "v_mfma_f32_16x16x32_bf16 v[72:75], a[224:227], a[136:139], v[72:75]\n"
    "v_accvgpr_read_b32 v255, a243\n"
    "v_add_f32_dpp v254, v252, v254 row_newbcast:1 row_mask:0xf bank_mask:0xf\n"
    "v_mfma_f32_16x16x32_bf16 v[40:43], a[224:227], a[132:135], v[40:43]\n"
    "v_add_f32_dpp v255, v252, v255 row_newbcast:3 row_mask:0xf bank_mask:0xf\n"
    "v_mul_f32 v245, v245, v254\n"
    "v_mfma_f32_16x16x32_bf16 v[40:43], a[228:231], a[164:167], v[40:43]\n"
    "v_mul_f32 v247, v247, v255\n"
    "v_mfma_f32_16x16x32_bf16 v[8:11], a[228:231], a[160:163], v[8:11]\n"
    "v_mfma_f32_16x16x32_bf16 v[8:11], a[224:227], a[128:131], v[8:11]\n"
    "s_waitcnt vmcnt(11)\n"
    "buffer_load_dwordx4 a[224:227], v240, s[20:23], 0 offen offset:3072 sc1\n"
    "v_mfma_f32_16x16x32_bf16 v[12:15], a[232:235], a[128:131], v[12:15]\n"
    "s_addk_i32 s20, 4096\n"
    "buffer_load_dwordx4 a[228:231], v240, s[20:23], 0 offen offset:0 sc1\n"
    "v_mfma_f32_16x16x32_bf16 v[12:15], a[236:239], a[160:163], v[12:15]\n"
    "buffer_store_dwordx4 v[248:251], v240, s[96:99], 0 offen sc1\n"
    "ds_write_b128 v243, v[248:251] offset:12288\n"
    "v_mfma_f32_16x16x32_bf16 v[44:47], a[236:239], a[164:167], v[44:47]\n"
    "ds_read_b128 a[128:131], v241 offset:49152\n"
    "v_cvt_pk_bf16_f32 v248, v244, v245\n"
    "v_mfma_f32_16x16x32_bf16 v[44:47], a[232:235], a[132:135], v[44:47]\n"
    "ds_read_b128 a[160:163], v241 offset:50176\n"
    "v_cvt_pk_bf16_f32 v249, v246, v247\n"
    "v_mfma_f32_16x16x32_bf16 v[76:79], a[232:235], a[136:139], v[76:79]\n"
    "ds_read_b128 a[164:167], v241 offset:52224\n"
    "v_accvgpr_read_b32 v245, a94\n"
    "v_mfma_f32_16x16x32_bf16 v[76:79], a[236:239], a[168:171], v[76:79]\n"
    "ds_read_b128 a[132:135], v241 offset:51200\n"
    "v_accvgpr_read_b32 v247, a95\n"
    "v_mfma_f32_16x16x32_bf16 v[108:111], a[236:239], a[172:175], v[108:111]\n"
    "ds_read_b128 a[136:139], v241 offset:53248\n"
    "v_lshlrev_b32 v244, 16, v245\n"
    "v_mfma_f32_16x16x32_bf16 v[108:111], a[232:235], a[140:143], v[108:111]\n"
    "ds_read_b128 a[168:171], v241 offset:54272\n"
    "v_lshlrev_b32 v246, 16, v247\n"
    "v_mfma_f32_16x16x32_bf16 v[140:143], a[232:235], a[144:147], v[140:143]\n"
    "ds_read_b128 a[172:175], v241 offset:56320\n"
    "v_and_b32 v245, s58, v245\n"
    "v_mfma_f32_16x16x32_bf16 v[140:143], a[236:239], a[176:179], v[140:143]\n"
    "ds_read_b128 a[140:143], v241 offset:55296\n"
    "v_and_b32 v247, s58, v247\n"
    "v_mfma_f32_16x16x32_bf16 v[172:175], a[236:239], a[180:183], v[172:175]\n"
    "ds_read_b128 a[144:147], v241 offset:57344\n"
    "v_accvgpr_read_b32 v254, a244\n"
    "v_mfma_f32_16x16x32_bf16 v[172:175], a[232:235], a[148:151], v[172:175]\n"
    "ds_read_b128 a[176:179], v241 offset:58368\n"
    "v_accvgpr_read_b32 v255, a246\n"
    "v_mfma_f32_16x16x32_bf16 v[204:207], a[232:235], a[152:155], v[204:207]\n"
    "ds_read_b128 a[180:183], v241 offset:60416\n"
    "v_add_f32_dpp v254, v252, v254 row_newbcast:4 row_mask:0xf bank_mask:0xf\n"
    "v_mfma_f32_16x16x32_bf16 v[204:207], a[236:239], a[184:187], v[204:207]\n"
    "ds_read_b128 a[148:151], v241 offset:59392\n"
    "v_add_f32_dpp v255, v252, v255 row_newbcast:6 row_mask:0xf bank_mask:0xf\n"
    "v_mfma_f32_16x16x32_bf16 v[236:239], a[236:239], a[188:191], v[236:239]\n"
    "ds_read_b128 a[152:155], v241 offset:61440\n"
    "v_mul_f32 v244, v244, v254\n"
    "v_mfma_f32_16x16x32_bf16 v[236:239], a[232:235], a[156:159], v[236:239]\n"
    "s_waitcnt vmcnt(11) lgkmcnt(11)\n"
    "ds_read_b128 a[184:187], v241 offset:62464\n"
    "v_mul_f32 v246, v246, v255\n"
    "v_mfma_f32_16x16x32_bf16 v[0:3], a[192:195], a[128:131], v[0:3]\n"
    "buffer_load_dwordx4 a[232:235], v240, s[20:23], 0 offen offset:1024 sc1\n"
    "s_xor_b32 s96, s96, 0x800\n"
    "v_mfma_f32_16x16x32_bf16 v[0:3], a[196:199], a[160:163], v[0:3]\n"
    "s_waitcnt lgkmcnt(10)\n"
    "ds_read_b128 a[188:191], v241 offset:64512\n"
    "v_accvgpr_read_b32 v254, a245\n"
    "v_mfma_f32_16x16x32_bf16 v[32:35], a[196:199], a[164:167], v[32:35]\n"
    "ds_read_b128 a[156:159], v241 offset:63488\n"
    "v_accvgpr_read_b32 v255, a247\n"
    "v_mfma_f32_16x16x32_bf16 v[32:35], a[192:195], a[132:135], v[32:35]\n"
    "s_waitcnt lgkmcnt(10)\n"
    "v_add_f32_dpp v254, v252, v254 row_newbcast:5 row_mask:0xf bank_mask:0xf\n"
    "v_add_f32_dpp v255, v252, v255 row_newbcast:7 row_mask:0xf bank_mask:0xf\n"
    "v_mfma_f32_16x16x32_bf16 v[64:67], a[192:195], a[136:139], v[64:67]\n"
    "v_mul_f32 v245, v245, v254\n"
    "v_mul_f32 v247, v247, v255\n"
    "v_mfma_f32_16x16x32_bf16 v[64:67], a[196:199], a[168:171], v[64:67]\n"
    "s_waitcnt lgkmcnt(8)\n"
    "v_cvt_pk_bf16_f32 v250, v244, v245\n"
    "v_cvt_pk_bf16_f32 v251, v246, v247\n"
    "v_mfma_f32_16x16x32_bf16 v[96:99], a[196:199], a[172:175], v[96:99]\n"
    "v_mfma_f32_16x16x32_bf16 v[96:99], a[192:195], a[140:143], v[96:99]\n"
    "s_waitcnt lgkmcnt(6)\n"
    "v_mfma_f32_16x16x32_bf16 v[128:131], a[192:195], a[144:147], v[128:131]\n"
    "buffer_store_dwordx4 v[248:251], v240, s[96:99], 0 offen sc1\n"
    "ds_write_b128 v243, v[248:251] offset:14336\n"
    "v_mfma_f32_16x16x32_bf16 v[128:131], a[196:199], a[176:179], v[128:131]\n"
    "s_waitcnt lgkmcnt(5)\n"
    "v_accvgpr_read_b32 v245, a96\n"
    "v_accvgpr_read_b32 v247, a97\n"
    "v_mfma_f32_16x16x32_bf16 v[160:163], a[196:199], a[180:183], v[160:163]\n"
    "v_lshlrev_b32 v244, 16, v245\n"
    "v_lshlrev_b32 v246, 16, v247\n"
    "v_mfma_f32_16x16x32_bf16 v[160:163], a[192:195], a[148:151], v[160:163]\n"
    "s_waitcnt lgkmcnt(3)\n"
    "v_and_b32 v245, s58, v245\n"
    "v_and_b32 v247, s58, v247\n"
    "v_mfma_f32_16x16x32_bf16 v[192:195], a[192:195], a[152:155], v[192:195]\n"
    "v_add_f32_dpp v254, v252, v24 row_newbcast:8 row_mask:0xf bank_mask:0xf\n"
    "v_add_f32_dpp v255, v252, v26 row_newbcast:10 row_mask:0xf bank_mask:0xf\n"
    "v_mfma_f32_16x16x32_bf16 v[192:195], a[196:199], a[184:187], v[192:195]\n"
    "s_waitcnt lgkmcnt(1)\n"
    "v_mul_f32 v244, v244, v254\n"
    "v_mul_f32 v246, v246, v255\n"
    "v_mfma_f32_16x16x32_bf16 v[224:227], a[196:199], a[188:191], v[224:227]\n"
    "v_add_f32_dpp v254, v252, v25 row_newbcast:9 row_mask:0xf bank_mask:0xf\n"
    "v_add_f32_dpp v255, v252, v27 row_newbcast:11 row_mask:0xf bank_mask:0xf\n"
    "v_mfma_f32_16x16x32_bf16 v[224:227], a[192:195], a[156:159], v[224:227]\n"
    "s_waitcnt vmcnt(10)\n"
    "buffer_load_dwordx4 a[192:195], v240, s[20:23], 0 offen offset:2048 sc1\n"
    "v_mfma_f32_16x16x32_bf16 v[228:231], a[200:203], a[156:159], v[228:231]\n"
    "buffer_load_dwordx4 a[196:199], v240, s[20:23], 0 offen offset:3072 sc1\n"
    "s_addk_i32 s20, 4096\n"
    "v_mfma_f32_16x16x32_bf16 v[228:231], a[204:207], a[188:191], v[228:231]\n"
    "v_mul_f32 v245, v245, v254\n"
    "v_mul_f32 v247, v247, v255\n"
    "v_mfma_f32_16x16x32_bf16 v[196:199], a[204:207], a[184:187], v[196:199]\n"
    "v_cvt_pk_bf16_f32 v248, v244, v245\n"
    "v_cvt_pk_bf16_f32 v249, v246, v247\n"
    "v_mfma_f32_16x16x32_bf16 v[196:199], a[200:203], a[152:155], v[196:199]\n"
    "v_accvgpr_read_b32 v245, a98\n"
    "v_accvgpr_read_b32 v247, a99\n"
    "v_mfma_f32_16x16x32_bf16 v[164:167], a[200:203], a[148:151], v[164:167]\n"
    "v_lshlrev_b32 v244, 16, v245\n"
    "v_lshlrev_b32 v246, 16, v247\n"
    "v_mfma_f32_16x16x32_bf16 v[164:167], a[204:207], a[180:183], v[164:167]\n"
    "v_and_b32 v245, s58, v245\n"
    "v_and_b32 v247, s58, v247\n"
    "v_mfma_f32_16x16x32_bf16 v[132:135], a[204:207], a[176:179], v[132:135]\n"
    "s_xor_b32 s96, s96, 0x195800\n"
    "v_add_f32_dpp v254, v252, v28 row_newbcast:12 row_mask:0xf bank_mask:0xf\n"
    "v_add_f32_dpp v255, v252, v30 row_newbcast:14 row_mask:0xf bank_mask:0xf\n"
    "v_mfma_f32_16x16x32_bf16 v[132:135], a[200:203], a[144:147], v[132:135]\n"
    "v_mul_f32 v244, v244, v254\n"
    "v_mul_f32 v246, v246, v255\n"
    "v_mfma_f32_16x16x32_bf16 v[100:103], a[200:203], a[140:143], v[100:103]\n"
    "v_add_f32_dpp v254, v252, v29 row_newbcast:13 row_mask:0xf bank_mask:0xf\n"
    "v_add_f32_dpp v255, v252, v31 row_newbcast:15 row_mask:0xf bank_mask:0xf\n"
    "v_mfma_f32_16x16x32_bf16 v[100:103], a[204:207], a[172:175], v[100:103]\n"
    "v_mul_f32 v245, v245, v254\n"
    "v_mul_f32 v247, v247, v255\n"
    "v_mfma_f32_16x16x32_bf16 v[68:71], a[204:207], a[168:171], v[68:71]\n"
    "v_cvt_pk_bf16_f32 v250, v244, v245\n"
    "v_cvt_pk_bf16_f32 v251, v246, v247\n"
    "v_mfma_f32_16x16x32_bf16 v[68:71], a[200:203], a[136:139], v[68:71]\n"
    "v_accvgpr_read_b32 v245, a100\n"
    "v_accvgpr_read_b32 v247, a101\n"
    "v_mfma_f32_16x16x32_bf16 v[36:39], a[200:203], a[132:135], v[36:39]\n"
    "v_lshlrev_b32 v244, 16, v245\n"
    "v_lshlrev_b32 v246, 16, v247\n"
    "v_mfma_f32_16x16x32_bf16 v[36:39], a[204:207], a[164:167], v[36:39]\n"
    "v_and_b32 v245, s58, v245\n"
    "v_and_b32 v247, s58, v247\n"
    "v_mfma_f32_16x16x32_bf16 v[4:7], a[204:207], a[160:163], v[4:7]\n"
    "v_add_f32_dpp v254, v252, v56 row_newbcast:8 row_mask:0xf bank_mask:0xf\n"
    "v_add_f32_dpp v255, v252, v58 row_newbcast:10 row_mask:0xf bank_mask:0xf\n"
    "v_mfma_f32_16x16x32_bf16 v[4:7], a[200:203], a[128:131], v[4:7]\n"
    "s_waitcnt vmcnt(8)\n"
    "buffer_load_dwordx4 a[200:203], v240, s[20:23], 0 offen offset:0 sc1\n"
    "v_mfma_f32_16x16x32_bf16 v[8:11], a[212:215], a[128:131], v[8:11]\n"
    "buffer_load_dwordx4 a[204:207], v240, s[20:23], 0 offen offset:1024 sc1\n"
    "v_mul_f32 v244, v244, v254\n"
    "v_mfma_f32_16x16x32_bf16 v[8:11], a[216:219], a[160:163], v[8:11]\n"
    "buffer_load_dwordx4 a[208:211], v240, s[20:23], 0 offen offset:2048 sc1\n"
    "v_mul_f32 v246, v246, v255\n"
    "v_mfma_f32_16x16x32_bf16 v[40:43], a[216:219], a[164:167], v[40:43]\n"
    "v_add_f32_dpp v254, v252, v57 row_newbcast:9 row_mask:0xf bank_mask:0xf\n"
    "v_add_f32_dpp v255, v252, v59 row_newbcast:11 row_mask:0xf bank_mask:0xf\n"
    "v_mfma_f32_16x16x32_bf16 v[40:43], a[212:215], a[132:135], v[40:43]\n"
    "v_mul_f32 v245, v245, v254\n"
    "v_mul_f32 v247, v247, v255\n"
    "v_mfma_f32_16x16x32_bf16 v[72:75], a[212:215], a[136:139], v[72:75]\n"
    "v_mfma_f32_16x16x32_bf16 v[72:75], a[216:219], a[168:171], v[72:75]\n"
    "buffer_store_dwordx4 v[248:251], v240, s[96:99], 0 offen sc1\n"
    "ds_write_b128 v243, v[248:251] offset:32768\n"
    "v_mfma_f32_16x16x32_bf16 v[104:107], a[216:219], a[172:175], v[104:107]\n"
    "v_cvt_pk_bf16_f32 v248, v244, v245\n"
    "v_cvt_pk_bf16_f32 v249, v246, v247\n"
    "v_mfma_f32_16x16x32_bf16 v[104:107], a[212:215], a[140:143], v[104:107]\n"
    "v_accvgpr_read_b32 v245, a102\n"
    "v_accvgpr_read_b32 v247, a103\n"
    "v_mfma_f32_16x16x32_bf16 v[136:139], a[212:215], a[144:147], v[136:139]\n"
    "v_lshlrev_b32 v244, 16, v245\n"
    "v_lshlrev_b32 v246, 16, v247\n"
    "v_mfma_f32_16x16x32_bf16 v[136:139], a[216:219], a[176:179], v[136:139]\n"
    "v_and_b32 v245, s58, v245\n"
    "v_and_b32 v247, s58, v247\n"
    "v_mfma_f32_16x16x32_bf16 v[168:171], a[216:219], a[180:183], v[168:171]\n"
    "s_xor_b32 s96, s96, 0x800\n"
    "v_add_f32_dpp v254, v252, v60 row_newbcast:12 row_mask:0xf bank_mask:0xf\n"
    "v_add_f32_dpp v255, v252, v62 row_newbcast:14 row_mask:0xf bank_mask:0xf\n"
    "v_mfma_f32_16x16x32_bf16 v[168:171], a[212:215], a[148:151], v[168:171]\n"
    "v_mul_f32 v244, v244, v254\n"
    "v_mul_f32 v246, v246, v255\n"
    "v_mfma_f32_16x16x32_bf16 v[200:203], a[212:215], a[152:155], v[200:203]\n"
    "v_add_f32_dpp v254, v252, v61 row_newbcast:13 row_mask:0xf bank_mask:0xf\n"
    "v_add_f32_dpp v255, v252, v63 row_newbcast:15 row_mask:0xf bank_mask:0xf\n"
    "v_mfma_f32_16x16x32_bf16 v[200:203], a[216:219], a[184:187], v[200:203]\n"
    "v_mul_f32 v245, v245, v254\n"
    "s_waitcnt lgkmcnt(0)\n"
    "s_barrier\n"
    "v_mfma_f32_16x16x32_bf16 v[232:235], a[216:219], a[188:191], v[232:235]\n"
    "v_mul_f32 v247, v247, v255\n"
    "v_cvt_pk_bf16_f32 v250, v244, v245\n"
    "v_mfma_f32_16x16x32_bf16 v[232:235], a[212:215], a[156:159], v[232:235]\n"
    "s_waitcnt vmcnt(10)\n"
    "buffer_load_dwordx4 a[212:215], v240, s[20:23], 0 offen offset:3072 sc1\n"
    "v_mfma_f32_16x16x32_bf16 v[236:239], a[220:223], a[156:159], v[236:239]\n"
    "s_addk_i32 s20, 4096\n"
    "v_cvt_pk_bf16_f32 v251, v246, v247\n"
    "v_accvgpr_read_b32 v245, a104\n"
    "v_mfma_f32_16x16x32_bf16 v[236:239], a[224:227], a[188:191], v[236:239]\n"
    "v_accvgpr_read_b32 v247, a105\n"
    "v_lshlrev_b32 v244, 16, v245\n"
    "v_mfma_f32_16x16x32_bf16 v[204:207], a[224:227], a[184:187], v[204:207]\n"
    "v_add_u32 v241, s66, v240\n"
    "v_lshlrev_b32 v246, 16, v247\n"
    "v_mfma_f32_16x16x32_bf16 v[204:207], a[220:223], a[152:155], v[204:207]\n"
    "ds_read_b128 a[156:159], v241 offset:14336\n"
    "v_and_b32 v245, s58, v245\n"
    "v_mfma_f32_16x16x32_bf16 v[172:175], a[220:223], a[148:151], v[172:175]\n"
    "ds_read_b128 a[188:191], v241 offset:15360\n"
    "v_and_b32 v247, s58, v247\n"
    "v_mfma_f32_16x16x32_bf16 v[172:175], a[224:227], a[180:183], v[172:175]\n"
    "ds_read_b128 a[184:187], v241 offset:13312\n"
    "v_add_f32_dpp v254, v252, v88 row_newbcast:8 row_mask:0xf bank_mask:0xf\n"
    "v_mfma_f32_16x16x32_bf16 v[140:143], a[224:227], a[176:179], v[140:143]\n"
    "ds_read_b128 a[152:155], v241 offset:12288\n"
    "v_add_f32_dpp v255, v252, v90 row_newbcast:10 row_mask:0xf bank_mask:0xf\n"
    "v_mfma_f32_16x16x32_bf16 v[140:143], a[220:223], a[144:147], v[140:143]\n"
    "ds_read_b128 a[148:151], v241 offset:10240\n"
    "v_mul_f32 v244, v244, v254\n"
    "v_mfma_f32_16x16x32_bf16 v[108:111], a[220:223], a[140:143], v[108:111]\n"
    "ds_read_b128 a[180:183], v241 offset:11264\n"
    "v_mul_f32 v246, v246, v255\n"
    "v_mfma_f32_16x16x32_bf16 v[108:111], a[224:227], a[172:175], v[108:111]\n"
    "ds_read_b128 a[176:179], v241 offset:9216\n"
    "v_add_f32_dpp v254, v252, v89 row_newbcast:9 row_mask:0xf bank_mask:0xf\n"
    "v_mfma_f32_16x16x32_bf16 v[76:79], a[224:227], a[168:171], v[76:79]\n"
    "ds_read_b128 a[144:147], v241 offset:8192\n"
    "v_add_f32_dpp v255, v252, v91 row_newbcast:11 row_mask:0xf bank_mask:0xf\n"
    "v_mfma_f32_16x16x32_bf16 v[76:79], a[220:223], a[136:139], v[76:79]\n"
    "ds_read_b128 a[140:143], v241 offset:6144\n"
    "v_mul_f32 v245, v245, v254\n"
    "v_mfma_f32_16x16x32_bf16 v[44:47], a[220:223], a[132:135], v[44:47]\n"
    "ds_read_b128 a[172:175], v241 offset:7168\n"
    "v_mul_f32 v247, v247, v255\n"
    "v_mfma_f32_16x16x32_bf16 v[44:47], a[224:227], a[164:167], v[44:47]\n"
    "ds_read_b128 a[168:171], v241 offset:5120\n"
    "v_mfma_f32_16x16x32_bf16 v[12:15], a[224:227], a[160:163], v[12:15]\n"
    "ds_read_b128 a[136:139], v241 offset:4096\n"
    "v_mfma_f32_16x16x32_bf16 v[12:15], a[220:223], a[128:131], v[12:15]\n"
    "s_waitcnt vmcnt(8) lgkmcnt(10)\n"
    "ds_read_b128 a[132:135], v241 offset:2048\n"
    "v_mfma_f32_16x16x32_bf16 v[224:227], a[228:231], a[156:159], v[224:227]\n"
    "ds_read_b128 a[164:167], v241 offset:3072\n"
    "v_mfma_f32_16x16x32_bf16 v[224:227], a[232:235], a[188:191], v[224:227]\n"
    "s_waitcnt lgkmcnt(10)\n"
    "buffer_load_dwordx4 a[220:223], v240, s[20:23], 0 offen offset:0 sc1\n"
    "v_mfma_f32_16x16x32_bf16 v[192:195], a[232:235], a[184:187], v[192:195]\n"
    "buffer_load_dwordx4 a[224:227], v240, s[20:23], 0 offen offset:1024 sc1\n"
    "ds_read_b128 a[160:163], v241 offset:1024\n"
    "v_mfma_f32_16x16x32_bf16 v[192:195], a[228:231], a[152:155], v[192:195]\n"
    "s_waitcnt lgkmcnt(9)\n"
    "ds_read_b128 a[128:131], v241 offset:0\n"
    "v_mfma_f32_16x16x32_bf16 v[160:163], a[228:231], a[148:151], v[160:163]\n"
    "v_mfma_f32_16x16x32_bf16 v[160:163], a[232:235], a[180:183], v[160:163]\n"
    "s_waitcnt lgkmcnt(8)\n"
    "v_mfma_f32_16x16x32_bf16 v[128:131], a[232:235], a[176:179], v[128:131]\n"
    "buffer_store_dwordx4 v[248:251], v240, s[96:99], 0 offen sc1\n"
    "ds_write_b128 v243, v[248:251] offset:34816\n"
    "v_mfma_f32_16x16x32_bf16 v[128:131], a[228:231], a[144:147], v[128:131]\n"
    "s_waitcnt lgkmcnt(7)\n"
    "v_cvt_pk_bf16_f32 v248, v244, v245\n"
    "v_cvt_pk_bf16_f32 v249, v246, v247\n"
    "v_mfma_f32_16x16x32_bf16 v[96:99], a[228:231], a[140:143], v[96:99]\n"
    "v_accvgpr_read_b32 v245, a106\n"
    "v_accvgpr_read_b32 v247, a107\n"
    "v_mfma_f32_16x16x32_bf16 v[96:99], a[232:235], a[172:175], v[96:99]\n"
    "s_waitcnt lgkmcnt(5)\n"
    "v_lshlrev_b32 v244, 16, v245\n"
    "v_lshlrev_b32 v246, 16, v247\n"
    "v_mfma_f32_16x16x32_bf16 v[64:67], a[232:235], a[168:171], v[64:67]\n"
    "v_and_b32 v245, s58, v245\n"
    "v_and_b32 v247, s58, v247\n"
    "v_mfma_f32_16x16x32_bf16 v[64:67], a[228:231], a[136:139], v[64:67]\n"
    "s_waitcnt lgkmcnt(3)\n"
    "s_xor_b32 s96, s96, 0x101800\n"
    "v_add_f32_dpp v254, v252, v92 row_newbcast:12 row_mask:0xf bank_mask:0xf\n"
    "v_mfma_f32_16x16x32_bf16 v[32:35], a[228:231], a[132:135], v[32:35]\n"
    "v_add_f32_dpp v255, v252, v94 row_newbcast:14 row_mask:0xf bank_mask:0xf\n"
    "v_mul_f32 v244, v244, v254\n"
    "v_mfma_f32_16x16x32_bf16 v[32:35], a[232:235], a[164:167], v[32:35]\n"
    "s_waitcnt lgkmcnt(1)\n"
    "v_mul_f32 v246, v246, v255\n"
    "v_add_f32_dpp v254, v252, v93 row_newbcast:13 row_mask:0xf bank_mask:0xf\n"
    "v_mfma_f32_16x16x32_bf16 v[0:3], a[232:235], a[160:163], v[0:3]\n"
    "v_add_f32_dpp v255, v252, v95 row_newbcast:15 row_mask:0xf bank_mask:0xf\n"
    "v_mul_f32 v245, v245, v254\n"
    "v_mfma_f32_16x16x32_bf16 v[0:3], a[228:231], a[128:131], v[0:3]\n"
    "s_waitcnt vmcnt(8)\n"
    "buffer_load_dwordx4 a[228:231], v240, s[20:23], 0 offen offset:2048 sc1\n"
    "v_mfma_f32_16x16x32_bf16 v[4:7], a[192:195], a[128:131], v[4:7]\n"
    "buffer_load_dwordx4 a[232:235], v240, s[20:23], 0 offen offset:3072 sc1\n"
    "s_addk_i32 s20, 4096\n"
    "v_mfma_f32_16x16x32_bf16 v[4:7], a[196:199], a[160:163], v[4:7]\n"
    "buffer_load_dwordx4 a[236:239], v240, s[20:23], 0 offen offset:0 sc1\n"
    "v_mul_f32 v247, v247, v255\n"
    "v_mfma_f32_16x16x32_bf16 v[36:39], a[196:199], a[164:167], v[36:39]\n"
    "v_cvt_pk_bf16_f32 v250, v244, v245\n"
    "v_cvt_pk_bf16_f32 v251, v246, v247\n"
    "v_mfma_f32_16x16x32_bf16 v[36:39], a[192:195], a[132:135], v[36:39]\n"
    "v_accvgpr_read_b32 v245, a108\n"
    "v_accvgpr_read_b32 v247, a109\n"
    "v_mfma_f32_16x16x32_bf16 v[68:71], a[192:195], a[136:139], v[68:71]\n"
    "v_lshlrev_b32 v244, 16, v245\n"
    "v_lshlrev_b32 v246, 16, v247\n"
    "v_mfma_f32_16x16x32_bf16 v[68:71], a[196:199], a[168:171], v[68:71]\n"
    "v_and_b32 v245, s58, v245\n"
    "v_and_b32 v247, s58, v247\n"
    "v_mfma_f32_16x16x32_bf16 v[100:103], a[196:199], a[172:175], v[100:103]\n"
    "v_add_f32_dpp v254, v252, v120 row_newbcast:8 row_mask:0xf bank_mask:0xf\n"
    "v_add_f32_dpp v255, v252, v122 row_newbcast:10 row_mask:0xf bank_mask:0xf\n"
    "v_mfma_f32_16x16x32_bf16 v[100:103], a[192:195], a[140:143], v[100:103]\n"
    "v_mul_f32 v244, v244, v254\n"
    "v_mul_f32 v246, v246, v255\n"
    "v_mfma_f32_16x16x32_bf16 v[132:135], a[192:195], a[144:147], v[132:135]\n"
    "v_add_f32_dpp v254, v252, v121 row_newbcast:9 row_mask:0xf bank_mask:0xf\n"
    "v_add_f32_dpp v255, v252, v123 row_newbcast:11 row_mask:0xf bank_mask:0xf\n"
    "v_mfma_f32_16x16x32_bf16 v[132:135], a[196:199], a[176:179], v[132:135]\n"
    "v_mul_f32 v245, v245, v254\n"
    "v_mul_f32 v247, v247, v255\n"
    "v_mfma_f32_16x16x32_bf16 v[164:167], a[196:199], a[180:183], v[164:167]\n"
    "v_mfma_f32_16x16x32_bf16 v[164:167], a[192:195], a[148:151], v[164:167]\n"
    "buffer_store_dwordx4 v[248:251], v240, s[96:99], 0 offen sc1\n"
    "ds_write_b128 v243, v[248:251] offset:36864\n"
    "v_mfma_f32_16x16x32_bf16 v[196:199], a[192:195], a[152:155], v[196:199]\n"
    "v_cvt_pk_bf16_f32 v248, v244, v245\n"
    "v_cvt_pk_bf16_f32 v249, v246, v247\n"
    "v_mfma_f32_16x16x32_bf16 v[196:199], a[196:199], a[184:187], v[196:199]\n"
    "v_accvgpr_read_b32 v245, a110\n"
    "v_accvgpr_read_b32 v247, a111\n"
    "v_mfma_f32_16x16x32_bf16 v[228:231], a[196:199], a[188:191], v[228:231]\n"
    "v_lshlrev_b32 v244, 16, v245\n"
    "v_lshlrev_b32 v246, 16, v247\n"
    "v_mfma_f32_16x16x32_bf16 v[228:231], a[192:195], a[156:159], v[228:231]\n"
    "s_waitcnt vmcnt(10)\n"
    "buffer_load_dwordx4 a[192:195], v240, s[20:23], 0 offen offset:1024 sc1\n"
    "v_mfma_f32_16x16x32_bf16 v[232:235], a[200:203], a[156:159], v[232:235]\n"
    "v_and_b32 v245, s58, v245\n"
    "v_and_b32 v247, s58, v247\n"
    "v_mfma_f32_16x16x32_bf16 v[232:235], a[204:207], a[188:191], v[232:235]\n"
    "s_xor_b32 s96, s96, 0x800\n"
    "v_add_f32_dpp v254, v252, v124 row_newbcast:12 row_mask:0xf bank_mask:0xf\n"
    "v_add_f32_dpp v255, v252, v126 row_newbcast:14 row_mask:0xf bank_mask:0xf\n"
    "v_mfma_f32_16x16x32_bf16 v[200:203], a[204:207], a[184:187], v[200:203]\n"
    "v_mul_f32 v244, v244, v254\n"
    "v_mul_f32 v246, v246, v255\n"
    "v_mfma_f32_16x16x32_bf16 v[200:203], a[200:203], a[152:155], v[200:203]\n"
    "v_add_f32_dpp v254, v252, v125 row_newbcast:13 row_mask:0xf bank_mask:0xf\n"
    "v_add_f32_dpp v255, v252, v127 row_newbcast:15 row_mask:0xf bank_mask:0xf\n"
    "v_mfma_f32_16x16x32_bf16 v[168:171], a[200:203], a[148:151], v[168:171]\n"
    "v_mul_f32 v245, v245, v254\n"
    "v_mul_f32 v247, v247, v255\n"
    "v_mfma_f32_16x16x32_bf16 v[168:171], a[204:207], a[180:183], v[168:171]\n"
    "v_cvt_pk_bf16_f32 v250, v244, v245\n"
    "v_cvt_pk_bf16_f32 v251, v246, v247\n"
    "v_mfma_f32_16x16x32_bf16 v[136:139], a[204:207], a[176:179], v[136:139]\n"
    "v_accvgpr_read_b32 v245, a112\n"
    "v_accvgpr_read_b32 v247, a113\n"
    "v_mfma_f32_16x16x32_bf16 v[136:139], a[200:203], a[144:147], v[136:139]\n"
    "v_lshlrev_b32 v244, 16, v245\n"
    "v_lshlrev_b32 v246, 16, v247\n"
    "v_mfma_f32_16x16x32_bf16 v[104:107], a[200:203], a[140:143], v[104:107]\n"
    "v_and_b32 v245, s58, v245\n"
    "v_and_b32 v247, s58, v247\n"
    "v_mfma_f32_16x16x32_bf16 v[104:107], a[204:207], a[172:175], v[104:107]\n"
    "v_add_f32_dpp v254, v252, v152 row_newbcast:8 row_mask:0xf bank_mask:0xf\n"
    "v_add_f32_dpp v255, v252, v154 row_newbcast:10 row_mask:0xf bank_mask:0xf\n"
    "v_mfma_f32_16x16x32_bf16 v[72:75], a[204:207], a[168:171], v[72:75]\n"
    "v_mul_f32 v244, v244, v254\n"
    "v_mul_f32 v246, v246, v255\n"
    "v_mfma_f32_16x16x32_bf16 v[72:75], a[200:203], a[136:139], v[72:75]\n"
    "v_add_f32_dpp v254, v252, v153 row_newbcast:9 row_mask:0xf bank_mask:0xf\n"
    "v_add_f32_dpp v255, v252, v155 row_newbcast:11 row_mask:0xf bank_mask:0xf\n"
    "v_mfma_f32_16x16x32_bf16 v[40:43], a[200:203], a[132:135], v[40:43]\n"
    "v_mul_f32 v245, v245, v254\n"
    "v_mul_f32 v247, v247, v255\n"
    "v_mfma_f32_16x16x32_bf16 v[40:43], a[204:207], a[164:167], v[40:43]\n"
    "buffer_store_dwordx4 v[248:251], v240, s[96:99], 0 offen sc1\n"
    "ds_write_b128 v243, v[248:251] offset:38912\n"
    "v_mfma_f32_16x16x32_bf16 v[8:11], a[204:207], a[160:163], v[8:11]\n"
    "v_cvt_pk_bf16_f32 v248, v244, v245\n"
    "v_cvt_pk_bf16_f32 v249, v246, v247\n"
    "v_mfma_f32_16x16x32_bf16 v[8:11], a[200:203], a[128:131], v[8:11]\n"
    "s_waitcnt vmcnt(9)\n"
    "buffer_load_dwordx4 a[200:203], v240, s[20:23], 0 offen offset:2048 sc1\n"
    "v_mfma_f32_16x16x32_bf16 v[12:15], a[208:211], a[128:131], v[12:15]\n"
    "buffer_load_dwordx4 a[204:207], v240, s[20:23], 0 offen offset:3072 sc1\n"
    "s_addk_i32 s20, 4096\n"
    "v_mfma_f32_16x16x32_bf16 v[12:15], a[212:215], a[160:163], v[12:15]\n"
    "v_accvgpr_read_b32 v245, a114\n"
    "v_accvgpr_read_b32 v247, a115\n"
    "v_mfma_f32_16x16x32_bf16 v[44:47], a[212:215], a[164:167], v[44:47]\n"
    "v_lshlrev_b32 v244, 16, v245\n"
    "v_lshlrev_b32 v246, 16, v247\n"
    "v_mfma_f32_16x16x32_bf16 v[44:47], a[208:211], a[132:135], v[44:47]\n"
    "v_and_b32 v245, s58, v245\n"
    "v_and_b32 v247, s58, v247\n"
    "v_mfma_f32_16x16x32_bf16 v[76:79], a[208:211], a[136:139], v[76:79]\n"
    "s_xor_b32 s96, s96, 0x185800\n"
    "v_add_f32_dpp v254, v252, v156 row_newbcast:12 row_mask:0xf bank_mask:0xf\n"
    "v_add_f32_dpp v255, v252, v158 row_newbcast:14 row_mask:0xf bank_mask:0xf\n"
    "v_mfma_f32_16x16x32_bf16 v[76:79], a[212:215], a[168:171], v[76:79]\n"
    "s_mov_b32 s50, 64\n"
    "buffer_load_dword v242, v240, s[48:51], 0 offen offset:0 sc1\n"
    "v_mfma_f32_16x16x32_bf16 v[108:111], a[212:215], a[172:175], v[108:111]\n"
    "v_mul_f32 v244, v244, v254\n"
    "v_mul_f32 v246, v246, v255\n"
    "v_mfma_f32_16x16x32_bf16 v[108:111], a[208:211], a[140:143], v[108:111]\n"
    "v_add_f32_dpp v254, v252, v157 row_newbcast:13 row_mask:0xf bank_mask:0xf\n"
    "v_add_f32_dpp v255, v252, v159 row_newbcast:15 row_mask:0xf bank_mask:0xf\n"
    "v_mfma_f32_16x16x32_bf16 v[140:143], a[208:211], a[144:147], v[140:143]\n"
    "v_mul_f32 v245, v245, v254\n"
    "v_mul_f32 v247, v247, v255\n"
    "v_mfma_f32_16x16x32_bf16 v[140:143], a[212:215], a[176:179], v[140:143]\n"
    "v_cvt_pk_bf16_f32 v250, v244, v245\n"
    "v_cvt_pk_bf16_f32 v251, v246, v247\n"
    "v_mfma_f32_16x16x32_bf16 v[172:175], a[212:215], a[180:183], v[172:175]\n"
    "v_accvgpr_read_b32 v245, a116\n"
    "v_accvgpr_read_b32 v247, a117\n"
    "v_mfma_f32_16x16x32_bf16 v[172:175], a[208:211], a[148:151], v[172:175]\n"
    "v_lshlrev_b32 v244, 16, v245\n"
    "v_lshlrev_b32 v246, 16, v247\n"
    "v_mfma_f32_16x16x32_bf16 v[204:207], a[208:211], a[152:155], v[204:207]\n"
    "v_and_b32 v245, s58, v245\n"
    "v_and_b32 v247, s58, v247\n"
    "v_mfma_f32_16x16x32_bf16 v[204:207], a[212:215], a[184:187], v[204:207]\n"
    "v_add_f32_dpp v254, v252, v184 row_newbcast:8 row_mask:0xf bank_mask:0xf\n"
    "v_add_f32_dpp v255, v252, v186 row_newbcast:10 row_mask:0xf bank_mask:0xf\n"
    "v_mfma_f32_16x16x32_bf16 v[236:239], a[212:215], a[188:191], v[236:239]\n"
    "v_mul_f32 v244, v244, v254\n"
    "v_mul_f32 v246, v246, v255\n"
    "v_mfma_f32_16x16x32_bf16 v[236:239], a[208:211], a[156:159], v[236:239]\n"
    "s_waitcnt vmcnt(10)\n"
    "buffer_load_dwordx4 a[208:211], v240, s[20:23], 0 offen offset:0 sc1\n"
    "v_mfma_f32_16x16x32_bf16 a[240:243], a[220:223], a[156:159], a[240:243]\n"
    "buffer_load_dwordx4 a[212:215], v240, s[20:23], 0 offen offset:1024 sc1\n"
    "v_add_f32_dpp v254, v252, v185 row_newbcast:9 row_mask:0xf bank_mask:0xf\n"
    "v_mfma_f32_16x16x32_bf16 a[240:243], a[224:227], a[188:191], a[240:243]\n"
    "buffer_load_dwordx4 a[216:219], v240, s[20:23], 0 offen offset:2048 sc1\n"
    "v_add_f32_dpp v255, v252, v187 row_newbcast:11 row_mask:0xf bank_mask:0xf\n"
    "v_mfma_f32_16x16x32_bf16 v[208:211], a[224:227], a[184:187], v[208:211]\n"
    "buffer_store_dwordx4 v[248:251], v240, s[96:99], 0 offen sc1\n"
    "ds_write_b128 v243, v[248:251] offset:40960\n"
    "v_mfma_f32_16x16x32_bf16 v[208:211], a[220:223], a[152:155], v[208:211]\n"
    "v_mul_f32 v245, v245, v254\n"
    "v_mul_f32 v247, v247, v255\n"
    "v_mfma_f32_16x16x32_bf16 v[176:179], a[220:223], a[148:151], v[176:179]\n"
    "v_cvt_pk_bf16_f32 v248, v244, v245\n"
    "v_cvt_pk_bf16_f32 v249, v246, v247\n"
    "v_mfma_f32_16x16x32_bf16 v[176:179], a[224:227], a[180:183], v[176:179]\n"
    "v_accvgpr_read_b32 v245, a118\n"
    "v_accvgpr_read_b32 v247, a119\n"
    "v_mfma_f32_16x16x32_bf16 v[144:147], a[224:227], a[176:179], v[144:147]\n"
    "s_xor_b32 s96, s96, 0x800\n"
    "v_lshlrev_b32 v244, 16, v245\n"
    "v_lshlrev_b32 v246, 16, v247\n"
    "v_mfma_f32_16x16x32_bf16 v[144:147], a[220:223], a[144:147], v[144:147]\n"
    "v_and_b32 v245, s58, v245\n"
    "v_and_b32 v247, s58, v247\n"
    "v_mfma_f32_16x16x32_bf16 v[112:115], a[220:223], a[140:143], v[112:115]\n"
    "v_add_f32_dpp v254, v252, v188 row_newbcast:12 row_mask:0xf bank_mask:0xf\n"
    "v_add_f32_dpp v255, v252, v190 row_newbcast:14 row_mask:0xf bank_mask:0xf\n"
    "v_mfma_f32_16x16x32_bf16 v[112:115], a[224:227], a[172:175], v[112:115]\n"
    "v_mul_f32 v244, v244, v254\n"
    "v_mul_f32 v246, v246, v255\n"
    "v_mfma_f32_16x16x32_bf16 v[80:83], a[224:227], a[168:171], v[80:83]\n"
    "v_add_f32_dpp v254, v252, v189 row_newbcast:13 row_mask:0xf bank_mask:0xf\n"
    "v_add_f32_dpp v255, v252, v191 row_newbcast:15 row_mask:0xf bank_mask:0xf\n"
    "v_mfma_f32_16x16x32_bf16 v[80:83], a[220:223], a[136:139], v[80:83]\n"
    "v_mul_f32 v245, v245, v254\n"
    "v_mul_f32 v247, v247, v255\n"
    "v_mfma_f32_16x16x32_bf16 v[48:51], a[220:223], a[132:135], v[48:51]\n"
    "v_cvt_pk_bf16_f32 v250, v244, v245\n"
    "v_cvt_pk_bf16_f32 v251, v246, v247\n"
    "v_mfma_f32_16x16x32_bf16 v[48:51], a[224:227], a[164:167], v[48:51]\n"
    "v_accvgpr_read_b32 v245, a120\n"
    "v_accvgpr_read_b32 v247, a121\n"
    "v_mfma_f32_16x16x32_bf16 v[16:19], a[224:227], a[160:163], v[16:19]\n"
    "v_lshlrev_b32 v244, 16, v245\n"
    "v_lshlrev_b32 v246, 16, v247\n"
    "v_mfma_f32_16x16x32_bf16 v[16:19], a[220:223], a[128:131], v[16:19]\n"
    "s_waitcnt vmcnt(11)\n"
    "buffer_load_dwordx4 a[220:223], v240, s[20:23], 0 offen offset:3072 sc1\n"
    "v_mfma_f32_16x16x32_bf16 v[20:23], a[228:231], a[128:131], v[20:23]\n"
    "s_addk_i32 s20, 4096\n"
    "v_and_b32 v245, s58, v245\n"
    "v_and_b32 v247, s58, v247\n"
    "v_mfma_f32_16x16x32_bf16 v[20:23], a[232:235], a[160:163], v[20:23]\n"
    "buffer_store_dwordx4 v[248:251], v240, s[96:99], 0 offen sc1\n"
    "ds_write_b128 v243, v[248:251] offset:43008\n"
    "v_mfma_f32_16x16x32_bf16 v[52:55], a[232:235], a[164:167], v[52:55]\n"
    "v_add_f32_dpp v254, v252, v216 row_newbcast:8 row_mask:0xf bank_mask:0xf\n"
    "v_add_f32_dpp v255, v252, v218 row_newbcast:10 row_mask:0xf bank_mask:0xf\n"
    "v_mfma_f32_16x16x32_bf16 v[52:55], a[228:231], a[132:135], v[52:55]\n"
    "v_mul_f32 v244, v244, v254\n"
    "v_mul_f32 v246, v246, v255\n"
    "v_mfma_f32_16x16x32_bf16 v[84:87], a[228:231], a[136:139], v[84:87]\n"
    "s_xor_b32 s96, s96, 0x101800\n"
    "v_add_f32_dpp v254, v252, v217 row_newbcast:9 row_mask:0xf bank_mask:0xf\n"
    "v_add_f32_dpp v255, v252, v219 row_newbcast:11 row_mask:0xf bank_mask:0xf\n"
    "v_mfma_f32_16x16x32_bf16 v[84:87], a[232:235], a[168:171], v[84:87]\n"
    "v_mul_f32 v245, v245, v254\n"
    "v_mul_f32 v247, v247, v255\n"
    "v_mfma_f32_16x16x32_bf16 v[116:119], a[232:235], a[172:175], v[116:119]\n"
    "v_cvt_pk_bf16_f32 v248, v244, v245\n"
    "v_cvt_pk_bf16_f32 v249, v246, v247\n"
    "v_mfma_f32_16x16x32_bf16 v[116:119], a[228:231], a[140:143], v[116:119]\n"
    "v_accvgpr_read_b32 v245, a122\n"
    "v_accvgpr_read_b32 v247, a123\n"
    "v_mfma_f32_16x16x32_bf16 v[148:151], a[228:231], a[144:147], v[148:151]\n"
    "v_lshlrev_b32 v244, 16, v245\n"
    "v_lshlrev_b32 v246, 16, v247\n"
    "v_mfma_f32_16x16x32_bf16 v[148:151], a[232:235], a[176:179], v[148:151]\n"
    "v_and_b32 v245, s58, v245\n"
    "v_and_b32 v247, s58, v247\n"
    "v_mfma_f32_16x16x32_bf16 v[180:183], a[232:235], a[180:183], v[180:183]\n"
    "v_add_f32_dpp v254, v252, v220 row_newbcast:12 row_mask:0xf bank_mask:0xf\n"
    "v_add_f32_dpp v255, v252, v222 row_newbcast:14 row_mask:0xf bank_mask:0xf\n"
    "v_mfma_f32_16x16x32_bf16 v[180:183], a[228:231], a[148:151], v[180:183]\n"
    "v_mul_f32 v244, v244, v254\n"
    "v_mul_f32 v246, v246, v255\n"
    "v_mfma_f32_16x16x32_bf16 v[212:215], a[228:231], a[152:155], v[212:215]\n"
    "v_add_f32_dpp v254, v252, v221 row_newbcast:13 row_mask:0xf bank_mask:0xf\n"
    "v_add_f32_dpp v255, v252, v223 row_newbcast:15 row_mask:0xf bank_mask:0xf\n"
    "v_mfma_f32_16x16x32_bf16 v[212:215], a[232:235], a[184:187], v[212:215]\n"
    "v_mul_f32 v245, v245, v254\n"
    "v_mul_f32 v247, v247, v255\n"
    "v_mfma_f32_16x16x32_bf16 a[244:247], a[232:235], a[188:191], a[244:247]\n"
    "v_cvt_pk_bf16_f32 v250, v244, v245\n"
    "v_cvt_pk_bf16_f32 v251, v246, v247\n"
    "v_mfma_f32_16x16x32_bf16 a[244:247], a[228:231], a[156:159], a[244:247]\n"
    "v_accvgpr_read_b32 v245, a124\n"
    "v_accvgpr_read_b32 v247, a125\n"
    "v_lshlrev_b32 v244, 16, v245\n"
    "v_lshlrev_b32 v246, 16, v247\n"
    "v_and_b32 v245, s58, v245\n"
    "v_and_b32 v247, s58, v247\n"
    "v_accvgpr_read_b32 v254, a248\n"
    "v_accvgpr_read_b32 v255, a250\n"
    "s_nop 0\n"
    "v_add_f32_dpp v254, v252, v254 row_newbcast:8 row_mask:0xf bank_mask:0xf\n"
    "v_add_f32_dpp v255, v252, v255 row_newbcast:10 row_mask:0xf bank_mask:0xf\n"
    "v_mul_f32 v244, v244, v254\n"
    "v_mul_f32 v246, v246, v255\n"
    "v_accvgpr_read_b32 v254, a249\n"
    "v_accvgpr_read_b32 v255, a251\n"
    "s_waitcnt vmcnt(10)\n"
    "v_mfma_f32_16x16x32_bf16 a[248:251], a[236:239], a[156:159], a[248:251]\n"
    "buffer_store_dwordx4 v[248:251], v240, s[96:99], 0 offen sc1\n"
    "ds_write_b128 v243, v[248:251] offset:45056\n"
    "v_mfma_f32_16x16x32_bf16 a[248:251], a[192:195], a[188:191], a[248:251]\n"
    "buffer_load_dwordx4 a[228:231], v240, s[20:23], 0 offen offset:0 sc1\n"
    "v_add_f32_dpp v254, v252, v254 row_newbcast:9 row_mask:0xf bank_mask:0xf\n"
    "v_mfma_f32_16x16x32_bf16 v[216:219], a[192:195], a[184:187], v[216:219]\n"
    "buffer_load_dwordx4 a[232:235], v240, s[20:23], 0 offen offset:1024 sc1\n"
    "v_add_f32_dpp v255, v252, v255 row_newbcast:11 row_mask:0xf bank_mask:0xf\n"
    "v_mfma_f32_16x16x32_bf16 v[216:219], a[236:239], a[152:155], v[216:219]\n"
    "v_mul_f32 v245, v245, v254\n"
    "v_mul_f32 v247, v247, v255\n"
    "v_mfma_f32_16x16x32_bf16 v[184:187], a[236:239], a[148:151], v[184:187]\n"
    "v_cvt_pk_bf16_f32 v248, v244, v245\n"
    "v_cvt_pk_bf16_f32 v249, v246, v247\n"
    "v_mfma_f32_16x16x32_bf16 v[184:187], a[192:195], a[180:183], v[184:187]\n"
    "s_xor_b32 s96, s96, 0x800\n"
    "v_accvgpr_read_b32 v245, a126\n"
    "v_accvgpr_read_b32 v247, a127\n"
    "v_mfma_f32_16x16x32_bf16 v[152:155], a[192:195], a[176:179], v[152:155]\n"
    "v_lshlrev_b32 v244, 16, v245\n"
    "v_lshlrev_b32 v246, 16, v247\n"
    "v_mfma_f32_16x16x32_bf16 v[152:155], a[236:239], a[144:147], v[152:155]\n"
    "v_and_b32 v245, s58, v245\n"
    "v_and_b32 v247, s58, v247\n"
    "v_mfma_f32_16x16x32_bf16 v[120:123], a[236:239], a[140:143], v[120:123]\n"
    "v_accvgpr_read_b32 v254, a252\n"
    "v_accvgpr_read_b32 v255, a254\n"
    "v_mfma_f32_16x16x32_bf16 v[120:123], a[192:195], a[172:175], v[120:123]\n"
    "v_add_f32_dpp v254, v252, v254 row_newbcast:12 row_mask:0xf bank_mask:0xf\n"
    "v_add_f32_dpp v255, v252, v255 row_newbcast:14 row_mask:0xf bank_mask:0xf\n"
    "v_mfma_f32_16x16x32_bf16 v[88:91], a[192:195], a[168:171], v[88:91]\n"
    "v_mul_f32 v244, v244, v254\n"
    "v_mul_f32 v246, v246, v255\n"
    "v_mfma_f32_16x16x32_bf16 v[88:91], a[236:239], a[136:139], v[88:91]\n"
    "v_accvgpr_read_b32 v254, a253\n"
    "v_accvgpr_read_b32 v255, a255\n"
    "v_mfma_f32_16x16x32_bf16 v[56:59], a[236:239], a[132:135], v[56:59]\n"
    "v_add_f32_dpp v254, v252, v254 row_newbcast:13 row_mask:0xf bank_mask:0xf\n"
    "v_add_f32_dpp v255, v252, v255 row_newbcast:15 row_mask:0xf bank_mask:0xf\n"
    "v_mfma_f32_16x16x32_bf16 v[56:59], a[192:195], a[164:167], v[56:59]\n"
    "v_mul_f32 v245, v245, v254\n"
    "v_mul_f32 v247, v247, v255\n"
    "v_mfma_f32_16x16x32_bf16 v[24:27], a[192:195], a[160:163], v[24:27]\n"
    "v_cvt_pk_bf16_f32 v250, v244, v245\n"
    "v_cvt_pk_bf16_f32 v251, v246, v247\n"
    "v_mfma_f32_16x16x32_bf16 v[24:27], a[236:239], a[128:131], v[24:27]\n"
    "s_waitcnt vmcnt(10)\n"
    "v_mfma_f32_16x16x32_bf16 v[28:31], a[200:203], a[128:131], v[28:31]\n"
    "buffer_store_dwordx4 v[248:251], v240, s[96:99], 0 offen sc1\n"
    "ds_write_b128 v243, v[248:251] offset:47104\n"
    "v_mfma_f32_16x16x32_bf16 v[28:31], a[204:207], a[160:163], v[28:31]\n"
    "buffer_load_dwordx4 a[236:239], v240, s[20:23], 0 offen offset:2048 sc1\n"
    "v_mfma_f32_16x16x32_bf16 v[60:63], a[204:207], a[164:167], v[60:63]\n"
    "buffer_load_dwordx4 a[192:195], v240, s[20:23], 0 offen offset:3072 sc1\n"
    "s_addk_i32 s20, 4096\n"
    "v_mfma_f32_16x16x32_bf16 v[60:63], a[200:203], a[132:135], v[60:63]\n"
    "buffer_load_dwordx4 a[196:199], v240, s[20:23], 0 offen offset:0 sc1\n"
    "ds_read_b128 a[128:131], v241 offset:16384\n"
    "v_mfma_f32_16x16x32_bf16 v[92:95], a[200:203], a[136:139], v[92:95]\n"
    "ds_read_b128 a[160:163], v241 offset:17408\n"
    "v_mfma_f32_16x16x32_bf16 v[92:95], a[204:207], a[168:171], v[92:95]\n"
    "ds_read_b128 a[164:167], v241 offset:19456\n"
    "v_mfma_f32_16x16x32_bf16 v[124:127], a[204:207], a[172:175], v[124:127]\n"
    "ds_read_b128 a[132:135], v241 offset:18432\n"
    "v_mfma_f32_16x16x32_bf16 v[124:127], a[200:203], a[140:143], v[124:127]\n"
    "ds_read_b128 a[136:139], v241 offset:20480\n"
    "v_mfma_f32_16x16x32_bf16 v[156:159], a[200:203], a[144:147], v[156:159]\n"
    "ds_read_b128 a[168:171], v241 offset:21504\n"
    "v_mfma_f32_16x16x32_bf16 v[156:159], a[204:207], a[176:179], v[156:159]\n"
    "ds_read_b128 a[172:175], v241 offset:23552\n"
    "v_mfma_f32_16x16x32_bf16 v[188:191], a[204:207], a[180:183], v[188:191]\n"
    "ds_read_b128 a[140:143], v241 offset:22528\n"
    "v_mfma_f32_16x16x32_bf16 v[188:191], a[200:203], a[148:151], v[188:191]\n"
    "ds_read_b128 a[144:147], v241 offset:24576\n"
    "v_mfma_f32_16x16x32_bf16 v[220:223], a[200:203], a[152:155], v[220:223]\n"
    "ds_read_b128 a[176:179], v241 offset:25600\n"
    "v_mfma_f32_16x16x32_bf16 v[220:223], a[204:207], a[184:187], v[220:223]\n"
    "ds_read_b128 a[180:183], v241 offset:27648\n"
    "v_mfma_f32_16x16x32_bf16 a[252:255], a[204:207], a[188:191], a[252:255]\n"
    "ds_read_b128 a[148:151], v241 offset:26624\n"
    "v_mfma_f32_16x16x32_bf16 a[252:255], a[200:203], a[156:159], a[252:255]\n"
    "s_waitcnt vmcnt(11) lgkmcnt(10)\n"
    "ds_read_b128 a[152:155], v241 offset:28672\n"
    "v_mfma_f32_16x16x32_bf16 v[0:3], a[208:211], a[128:131], v[0:3]\n"
    "ds_read_b128 a[184:187], v241 offset:29696\n"
    "v_mfma_f32_16x16x32_bf16 v[0:3], a[212:215], a[160:163], v[0:3]\n"
    "s_waitcnt lgkmcnt(10)\n"
    "buffer_load_dwordx4 a[200:203], v240, s[20:23], 0 offen offset:1024 sc1\n"
    "v_mfma_f32_16x16x32_bf16 v[32:35], a[212:215], a[164:167], v[32:35]\n"
    "ds_read_b128 a[188:191], v241 offset:31744\n"
    "v_mfma_f32_16x16x32_bf16 v[32:35], a[208:211], a[132:135], v[32:35]\n"
    "s_waitcnt lgkmcnt(9)\n"
    "ds_read_b128 a[156:159], v241 offset:30720\n"
    "v_mfma_f32_16x16x32_bf16 v[64:67], a[208:211], a[136:139], v[64:67]\n"
    "v_mfma_f32_16x16x32_bf16 v[64:67], a[212:215], a[168:171], v[64:67]\n"
    "s_waitcnt lgkmcnt(8)\n"
    "v_mfma_f32_16x16x32_bf16 v[96:99], a[212:215], a[172:175], v[96:99]\n"
    "v_mfma_f32_16x16x32_bf16 v[96:99], a[208:211], a[140:143], v[96:99]\n"
    "s_waitcnt lgkmcnt(6)\n"
    "v_mfma_f32_16x16x32_bf16 v[128:131], a[208:211], a[144:147], v[128:131]\n"
    "v_mfma_f32_16x16x32_bf16 v[128:131], a[212:215], a[176:179], v[128:131]\n"
    "s_waitcnt lgkmcnt(4)\n"
    "v_mfma_f32_16x16x32_bf16 v[160:163], a[212:215], a[180:183], v[160:163]\n"
    "v_mfma_f32_16x16x32_bf16 v[160:163], a[208:211], a[148:151], v[160:163]\n"
    "s_waitcnt lgkmcnt(2)\n"
    "v_mfma_f32_16x16x32_bf16 v[192:195], a[208:211], a[152:155], v[192:195]\n"
    "v_mfma_f32_16x16x32_bf16 v[192:195], a[212:215], a[184:187], v[192:195]\n"
    "s_waitcnt lgkmcnt(0)\n"
    "v_mfma_f32_16x16x32_bf16 v[224:227], a[212:215], a[188:191], v[224:227]\n"
    "v_mfma_f32_16x16x32_bf16 v[224:227], a[208:211], a[156:159], v[224:227]\n"
    "v_cmp_le_u32 vcc, s52, v242\n"
    "s_nop 1\n"
    "s_and_b32 s63, vcc_lo, 15\n"
    "s_cmp_eq_u32 s63, 15\n"
    "s_cbranch_scc1 LA6_%=\n"
    "s_mov_b32 s69, 0x400000\n"
    "LA7_%=:\n"
    "s_sleep 1\n"
    "buffer_load_dword v242, v240, s[48:51], 0 offen offset:0 sc1\n"
    "s_waitcnt vmcnt(0)\n"
    "v_cmp_le_u32 vcc, s52, v242\n"
    "s_sub_u32 s69, s69, 1\n"
    "s_cbranch_scc1 LA6_%=\n"
    "s_and_b32 s63, vcc_lo, 15\n"
    "s_cmp_eq_u32 s63, 15\n"
    "s_cbranch_scc0 LA7_%=\n"
    "LA6_%=:\n"
    "s_waitcnt vmcnt(9)\n"
    "v_mfma_f32_16x16x32_bf16 v[228:231], a[216:219], a[156:159], v[228:231]\n"
    "v_readfirstlane_b32 s90, v242\n"
    "s_and_b32 s90, s90, 1\n"
    "v_mfma_f32_16x16x32_bf16 v[228:231], a[220:223], a[188:191], v[228:231]\n"
    "s_lshl_b32 s90, s90, 13\n"
    "buffer_load_dwordx4 a[208:211], v240, s[20:23], 0 offen offset:2048 sc1\n"
    "v_mfma_f32_16x16x32_bf16 v[196:199], a[220:223], a[184:187], v[196:199]\n"
    "buffer_load_dwordx4 a[212:215], v240, s[20:23], 0 offen offset:3072 sc1\n"
    "s_addk_i32 s20, 4096\n"
    "v_mfma_f32_16x16x32_bf16 v[196:199], a[216:219], a[152:155], v[196:199]\n"
    "s_mov_b32 m0, s56\n"
    "s_xor_b32 s92, s90, s59\n"
    "s_add_u32 s92, s92, s32\n"
    "v_mfma_f32_16x16x32_bf16 v[164:167], a[216:219], a[148:151], v[164:167]\n"
    "s_addc_u32 s93, s33, 0\n"
    "buffer_load_dwordx4 v240, s[92:95], 0 offen sc1 lds\n"
    "v_mfma_f32_16x16x32_bf16 v[164:167], a[220:223], a[180:183], v[164:167]\n"
    "v_mfma_f32_16x16x32_bf16 v[132:135], a[220:223], a[176:179], v[132:135]\n"
    "v_mfma_f32_16x16x32_bf16 v[132:135], a[216:219], a[144:147], v[132:135]\n"
    "v_mfma_f32_16x16x32_bf16 v[100:103], a[216:219], a[140:143], v[100:103]\n"
    "v_mfma_f32_16x16x32_bf16 v[100:103], a[220:223], a[172:175], v[100:103]\n"
    "v_mfma_f32_16x16x32_bf16 v[68:71], a[220:223], a[168:171], v[68:71]\n"
    "v_mfma_f32_16x16x32_bf16 v[68:71], a[216:219], a[136:139], v[68:71]\n"
    "v_mfma_f32_16x16x32_bf16 v[36:39], a[216:219], a[132:135], v[36:39]\n"
    "v_mfma_f32_16x16x32_bf16 v[36:39], a[220:223], a[164:167], v[36:39]\n"
    "v_mfma_f32_16x16x32_bf16 v[4:7], a[220:223], a[160:163], v[4:7]\n"
    "v_mfma_f32_16x16x32_bf16 v[4:7], a[216:219], a[128:131], v[4:7]\n"
    "s_waitcnt vmcnt(8)\n"
    "buffer_load_dwordx4 a[216:219], v240, s[20:23], 0 offen offset:0 sc1\n"
    "v_mfma_f32_16x16x32_bf16 v[8:11], a[228:231], a[128:131], v[8:11]\n"
    "buffer_load_dwordx4 a[220:223], v240, s[20:23], 0 offen offset:1024 sc1\n"
    "v_mfma_f32_16x16x32_bf16 v[8:11], a[232:235], a[160:163], v[8:11]\n"
    "buffer_load_dwordx4 a[224:227], v240, s[20:23], 0 offen offset:2048 sc1\n"
    "v_mfma_f32_16x16x32_bf16 v[40:43], a[232:235], a[164:167], v[40:43]\n"
    "v_mfma_f32_16x16x32_bf16 v[40:43], a[228:231], a[132:135], v[40:43]\n"
    "v_mfma_f32_16x16x32_bf16 v[72:75], a[228:231], a[136:139], v[72:75]\n"
    "v_mfma_f32_16x16x32_bf16 v[72:75], a[232:235], a[168:171], v[72:75]\n"
    "v_mfma_f32_16x16x32_bf16 v[104:107], a[232:235], a[172:175], v[104:107]\n"
    "v_mfma_f32_16x16x32_bf16 v[104:107], a[228:231], a[140:143], v[104:107]\n"
    "v_mfma_f32_16x16x32_bf16 v[136:139], a[228:231], a[144:147], v[136:139]\n"
    "s_addk_i32 m0, 2048\n"
    "s_xor_b32 s92, s92, 0x800\n"
    "v_mfma_f32_16x16x32_bf16 v[136:139], a[232:235], a[176:179], v[136:139]\n"
    "buffer_load_dwordx4 v240, s[92:95], 0 offen sc1 lds\n"
    "v_mfma_f32_16x16x32_bf16 v[168:171], a[232:235], a[180:183], v[168:171]\n"
    "v_mfma_f32_16x16x32_bf16 v[168:171], a[228:231], a[148:151], v[168:171]\n"
    "v_mfma_f32_16x16x32_bf16 v[200:203], a[228:231], a[152:155], v[200:203]\n"
    "v_mfma_f32_16x16x32_bf16 v[200:203], a[232:235], a[184:187], v[200:203]\n"
    "v_mfma_f32_16x16x32_bf16 v[232:235], a[232:235], a[188:191], v[232:235]\n"
    "v_mfma_f32_16x16x32_bf16 v[232:235], a[228:231], a[156:159], v[232:235]\n"
    "s_waitcnt vmcnt(9)\n"
    "buffer_load_dwordx4 a[228:231], v240, s[20:23], 0 offen offset:3072 sc1\n"
    "v_mfma_f32_16x16x32_bf16 v[236:239], a[236:239], a[156:159], v[236:239]\n"
    "s_addk_i32 s20, 4096\n"
    "v_mfma_f32_16x16x32_bf16 v[236:239], a[192:195], a[188:191], v[236:239]\n"
    "v_mfma_f32_16x16x32_bf16 v[204:207], a[192:195], a[184:187], v[204:207]\n"
    "v_mfma_f32_16x16x32_bf16 v[204:207], a[236:239], a[152:155], v[204:207]\n"
    "v_mfma_f32_16x16x32_bf16 v[172:175], a[236:239], a[148:151], v[172:175]\n"
    "v_mfma_f32_16x16x32_bf16 v[172:175], a[192:195], a[180:183], v[172:175]\n"
    "v_mfma_f32_16x16x32_bf16 v[140:143], a[192:195], a[176:179], v[140:143]\n"
    "v_mfma_f32_16x16x32_bf16 v[140:143], a[236:239], a[144:147], v[140:143]\n"
    "v_mfma_f32_16x16x32_bf16 v[108:111], a[236:239], a[140:143], v[108:111]\n"
    "v_mfma_f32_16x16x32_bf16 v[108:111], a[192:195], a[172:175], v[108:111]\n"
    "v_mfma_f32_16x16x32_bf16 v[76:79], a[192:195], a[168:171], v[76:79]\n"
    "v_mfma_f32_16x16x32_bf16 v[76:79], a[236:239], a[136:139], v[76:79]\n"
    "v_mfma_f32_16x16x32_bf16 v[44:47], a[236:239], a[132:135], v[44:47]\n"
    "v_mfma_f32_16x16x32_bf16 v[44:47], a[192:195], a[164:167], v[44:47]\n"
    "v_mfma_f32_16x16x32_bf16 v[12:15], a[192:195], a[160:163], v[12:15]\n"
    "v_mfma_f32_16x16x32_bf16 v[12:15], a[236:239], a[128:131], v[12:15]\n"
    "s_waitcnt vmcnt(8)\n"
    "buffer_load_dwordx4 a[236:239], v240, s[20:23], 0 offen offset:0 sc1\n"
    "v_mfma_f32_16x16x32_bf16 v[16:19], a[196:199], a[128:131], v[16:19]\n"
    "buffer_load_dwordx4 a[192:195], v240, s[20:23], 0 offen offset:1024 sc1\n"
    "s_addk_i32 m0, 2048\n"
    "v_mfma_f32_16x16x32_bf16 v[16:19], a[200:203], a[160:163], v[16:19]\n"
    "s_xor_b32 s92, s92, 0x101800\n"
    "buffer_load_dwordx4 v240, s[92:95], 0 offen sc1 lds\n"
    "v_mfma_f32_16x16x32_bf16 v[48:51], a[200:203], a[164:167], v[48:51]\n"
    "v_mfma_f32_16x16x32_bf16 v[48:51], a[196:199], a[132:135], v[48:51]\n"
    "v_mfma_f32_16x16x32_bf16 v[80:83], a[196:199], a[136:139], v[80:83]\n"
    "v_mfma_f32_16x16x32_bf16 v[80:83], a[200:203], a[168:171], v[80:83]\n"
    "v_mfma_f32_16x16x32_bf16 v[112:115], a[200:203], a[172:175], v[112:115]\n"
    "v_mfma_f32_16x16x32_bf16 v[112:115], a[196:199], a[140:143], v[112:115]\n"
    "v_mfma_f32_16x16x32_bf16 v[144:147], a[196:199], a[144:147], v[144:147]\n"
    "v_mfma_f32_16x16x32_bf16 v[144:147], a[200:203], a[176:179], v[144:147]\n"
    "v_mfma_f32_16x16x32_bf16 v[176:179], a[200:203], a[180:183], v[176:179]\n"
    "v_mfma_f32_16x16x32_bf16 v[176:179], a[196:199], a[148:151], v[176:179]\n"
    "v_mfma_f32_16x16x32_bf16 v[208:211], a[196:199], a[152:155], v[208:211]\n"
    "v_mfma_f32_16x16x32_bf16 v[208:211], a[200:203], a[184:187], v[208:211]\n"
    "v_mfma_f32_16x16x32_bf16 a[240:243], a[200:203], a[188:191], a[240:243]\n"
    "s_add_u32 s62, s52, s64\n"
    "v_mov_b32 v242, s62\n"
    "s_mov_b32 s46, 260\n"
    "v_mfma_f32_16x16x32_bf16 a[240:243], a[196:199], a[156:159], a[240:243]\n"
    "s_waitcnt vmcnt(9)\n"
    "v_mfma_f32_16x16x32_bf16 a[244:247], a[208:211], a[156:159], a[244:247]\n"
    "buffer_store_dword v242, v240, s[44:47], 0 offen offset:256 sc1\n"
    "v_mfma_f32_16x16x32_bf16 a[244:247], a[212:215], a[188:191], a[244:247]\n"
    "buffer_load_dwordx4 a[196:199], v240, s[20:23], 0 offen offset:2048 sc1\n"
    "v_mfma_f32_16x16x32_bf16 v[212:215], a[212:215], a[184:187], v[212:215]\n"
    "buffer_load_dwordx4 a[200:203], v240, s[20:23], 0 offen offset:3072 sc1\n"
    "s_addk_i32 s20, 4096\n"
    "v_mfma_f32_16x16x32_bf16 v[212:215], a[208:211], a[152:155], v[212:215]\n"
    "buffer_load_dwordx4 a[204:207], v240, s[20:23], 0 offen offset:0 sc1\n"
    "v_mfma_f32_16x16x32_bf16 v[180:183], a[208:211], a[148:151], v[180:183]\n"
    "v_mfma_f32_16x16x32_bf16 v[180:183], a[212:215], a[180:183], v[180:183]\n"
    "v_mfma_f32_16x16x32_bf16 v[148:151], a[212:215], a[176:179], v[148:151]\n"
    "v_mfma_f32_16x16x32_bf16 v[148:151], a[208:211], a[144:147], v[148:151]\n"
    "v_mfma_f32_16x16x32_bf16 v[116:119], a[208:211], a[140:143], v[116:119]\n"
    "s_addk_i32 m0, 2048\n"
    "s_xor_b32 s92, s92, 0x800\n"
    "v_mfma_f32_16x16x32_bf16 v[116:119], a[212:215], a[172:175], v[116:119]\n"
    "buffer_load_dwordx4 v240, s[92:95], 0 offen sc1 lds\n"
    "v_mfma_f32_16x16x32_bf16 v[84:87], a[212:215], a[168:171], v[84:87]\n"
    "v_mfma_f32_16x16x32_bf16 v[84:87], a[208:211], a[136:139], v[84:87]\n"
    "v_mfma_f32_16x16x32_bf16 v[52:55], a[208:211], a[132:135], v[52:55]\n"
    "v_mfma_f32_16x16x32_bf16 v[52:55], a[212:215], a[164:167], v[52:55]\n"
    "v_mfma_f32_16x16x32_bf16 v[20:23], a[212:215], a[160:163], v[20:23]\n"
    "v_mfma_f32_16x16x32_bf16 v[20:23], a[208:211], a[128:131], v[20:23]\n"
    "s_waitcnt vmcnt(11)\n"
    "buffer_load_dwordx4 a[208:211], v240, s[20:23], 0 offen offset:1024 sc1\n"
    "v_mfma_f32_16x16x32_bf16 v[24:27], a[216:219], a[128:131], v[24:27]\n"
    "v_mfma_f32_16x16x32_bf16 v[24:27], a[220:223], a[160:163], v[24:27]\n"
    "v_mfma_f32_16x16x32_bf16 v[56:59], a[220:223], a[164:167], v[56:59]\n"
    "v_mfma_f32_16x16x32_bf16 v[56:59], a[216:219], a[132:135], v[56:59]\n"
    "v_mfma_f32_16x16x32_bf16 v[88:91], a[216:219], a[136:139], v[88:91]\n"
    "v_mfma_f32_16x16x32_bf16 v[88:91], a[220:223], a[168:171], v[88:91]\n"
    "v_mfma_f32_16x16x32_bf16 v[120:123], a[220:223], a[172:175], v[120:123]\n"
    "v_mfma_f32_16x16x32_bf16 v[120:123], a[216:219], a[140:143], v[120:123]\n"
    "v_mfma_f32_16x16x32_bf16 v[152:155], a[216:219], a[144:147], v[152:155]\n"
    "v_mfma_f32_16x16x32_bf16 v[152:155], a[220:223], a[176:179], v[152:155]\n"
    "v_mfma_f32_16x16x32_bf16 v[184:187], a[220:223], a[180:183], v[184:187]\n"
    "v_mfma_f32_16x16x32_bf16 v[184:187], a[216:219], a[148:151], v[184:187]\n"
    "v_mfma_f32_16x16x32_bf16 v[216:219], a[216:219], a[152:155], v[216:219]\n"
    "v_mfma_f32_16x16x32_bf16 v[216:219], a[220:223], a[184:187], v[216:219]\n"
    "s_barrier\n"
    "v_mfma_f32_16x16x32_bf16 a[248:251], a[220:223], a[188:191], a[248:251]\n"
    "v_mfma_f32_16x16x32_bf16 a[248:251], a[216:219], a[156:159], a[248:251]\n"
    "s_waitcnt vmcnt(9)\n"
    "buffer_load_dwordx4 a[216:219], v240, s[20:23], 0 offen offset:2048 sc1\n"
    "v_mfma_f32_16x16x32_bf16 a[252:255], a[224:227], a[156:159], a[252:255]\n"
    "buffer_load_dwordx4 a[220:223], v240, s[20:23], 0 offen offset:3072 sc1\n"
    "s_addk_i32 s20, 4096\n"
    "v_mfma_f32_16x16x32_bf16 a[252:255], a[228:231], a[188:191], a[252:255]\n"
    "v_mfma_f32_16x16x32_bf16 v[220:223], a[228:231], a[184:187], v[220:223]\n"
    "ds_read_b128 a[156:159], v241 offset:47104\n"
    "v_mfma_f32_16x16x32_bf16 v[220:223], a[224:227], a[152:155], v[220:223]\n"
    "ds_read_b128 a[188:191], v241 offset:48128\n"
    "v_mfma_f32_16x16x32_bf16 v[188:191], a[224:227], a[148:151], v[188:191]\n"
    "s_addk_i32 m0, 2048\n"
    "s_xor_b32 s92, s92, 0x185800\n"
    "v_mfma_f32_16x16x32_bf16 v[188:191], a[228:231], a[180:183], v[188:191]\n"
    "buffer_load_dwordx4 v240, s[92:95], 0 offen sc1 lds\n"
    "ds_read_b128 a[184:187], v241 offset:46080\n"
    "v_mfma_f32_16x16x32_bf16 v[156:159], a[228:231], a[176:179], v[156:159]\n"
    "ds_read_b128 a[152:155], v241 offset:45056\n"
    "v_mfma_f32_16x16x32_bf16 v[156:159], a[224:227], a[144:147], v[156:159]\n"
    "ds_read_b128 a[148:151], v241 offset:43008\n"
    "v_mfma_f32_16x16x32_bf16 v[124:127], a[224:227], a[140:143], v[124:127]\n"
    "ds_read_b128 a[180:183], v241 offset:44032\n"
    "v_mfma_f32_16x16x32_bf16 v[124:127], a[228:231], a[172:175], v[124:127]\n"
    "ds_read_b128 a[176:179], v241 offset:41984\n"
    "v_mfma_f32_16x16x32_bf16 v[92:95], a[228:231], a[168:171], v[92:95]\n"
    "ds_read_b128 a[144:147], v241 offset:40960\n"
    "v_mfma_f32_16x16x32_bf16 v[92:95], a[224:227], a[136:139], v[92:95]\n"
    "ds_read_b128 a[140:143], v241 offset:38912\n"
    "v_mfma_f32_16x16x32_bf16 v[60:63], a[224:227], a[132:135], v[60:63]\n"
    "ds_read_b128 a[172:175], v241 offset:39936\n"
    "v_mfma_f32_16x16x32_bf16 v[60:63], a[228:231], a[164:167], v[60:63]\n"
    "ds_read_b128 a[168:171], v241 offset:37888\n"
    "v_mfma_f32_16x16x32_bf16 v[28:31], a[228:231], a[160:163], v[28:31]\n"
    "ds_read_b128 a[136:139], v241 offset:36864\n"
    "v_mfma_f32_16x16x32_bf16 v[28:31], a[224:227], a[128:131], v[28:31]\n"
    "s_waitcnt vmcnt(10) lgkmcnt(10)\n"
    "ds_read_b128 a[132:135], v241 offset:34816\n"
    "v_mfma_f32_16x16x32_bf16 v[224:227], a[236:239], a[156:159], v[224:227]\n"
    "ds_read_b128 a[164:167], v241 offset:35840\n"
    "v_mfma_f32_16x16x32_bf16 v[224:227], a[192:195], a[188:191], v[224:227]\n"
    "s_waitcnt lgkmcnt(10)\n"
    "buffer_load_dwordx4 a[224:227], v240, s[20:23], 0 offen offset:0 sc1\n"
    "v_mfma_f32_16x16x32_bf16 v[192:195], a[192:195], a[184:187], v[192:195]\n"
    "buffer_load_dwordx4 a[228:231], v240, s[20:23], 0 offen offset:1024 sc1\n"
    "v_mfma_f32_16x16x32_bf16 v[192:195], a[236:239], a[152:155], v[192:195]\n"
    "s_waitcnt lgkmcnt(8)\n"
    "buffer_load_dwordx4 a[232:235], v240, s[20:23], 0 offen offset:2048 sc1\n"
    "ds_read_b128 a[160:163], v241 offset:33792\n"
    "v_mfma_f32_16x16x32_bf16 v[160:163], a[236:239], a[148:151], v[160:163]\n"
    "ds_read_b128 a[128:131], v241 offset:32768\n"
    "v_mfma_f32_16x16x32_bf16 v[160:163], a[192:195], a[180:183], v[160:163]\n"
    "s_waitcnt lgkmcnt(8)\n"
    "v_mfma_f32_16x16x32_bf16 v[128:131], a[192:195], a[176:179], v[128:131]\n"
    "v_mfma_f32_16x16x32_bf16 v[128:131], a[236:239], a[144:147], v[128:131]\n"
    "s_waitcnt lgkmcnt(6)\n"
    "v_mfma_f32_16x16x32_bf16 v[96:99], a[236:239], a[140:143], v[96:99]\n"
    "v_mfma_f32_16x16x32_bf16 v[96:99], a[192:195], a[172:175], v[96:99]\n"
    "s_waitcnt lgkmcnt(4)\n"
    "v_mfma_f32_16x16x32_bf16 v[64:67], a[192:195], a[168:171], v[64:67]\n"
    "v_mfma_f32_16x16x32_bf16 v[64:67], a[236:239], a[136:139], v[64:67]\n"
    "s_waitcnt lgkmcnt(2)\n"
    "v_mfma_f32_16x16x32_bf16 v[32:35], a[236:239], a[132:135], v[32:35]\n"
    "s_addk_i32 m0, 2048\n"
    "s_xor_b32 s92, s92, 0x800\n"
    "v_mfma_f32_16x16x32_bf16 v[32:35], a[192:195], a[164:167], v[32:35]\n"
    "s_waitcnt lgkmcnt(0)\n"
    "buffer_load_dwordx4 v240, s[92:95], 0 offen sc1 lds\n"
    "v_mfma_f32_16x16x32_bf16 v[0:3], a[192:195], a[160:163], v[0:3]\n"
    "v_mfma_f32_16x16x32_bf16 v[0:3], a[236:239], a[128:131], v[0:3]\n"
    "s_waitcnt vmcnt(10)\n"
    "buffer_load_dwordx4 a[236:239], v240, s[20:23], 0 offen offset:3072 sc1\n"
    "v_mfma_f32_16x16x32_bf16 v[4:7], a[196:199], a[128:131], v[4:7]\n"
    "s_addk_i32 s20, 4096\n"
    "buffer_load_dwordx4 a[192:195], v240, s[20:23], 0 offen offset:0 sc1\n"
    "v_mfma_f32_16x16x32_bf16 v[4:7], a[200:203], a[160:163], v[4:7]\n"
    "v_mfma_f32_16x16x32_bf16 v[36:39], a[200:203], a[164:167], v[36:39]\n"
    "v_mfma_f32_16x16x32_bf16 v[36:39], a[196:199], a[132:135], v[36:39]\n"
    "v_mfma_f32_16x16x32_bf16 v[68:71], a[196:199], a[136:139], v[68:71]\n"
    "v_mfma_f32_16x16x32_bf16 v[68:71], a[200:203], a[168:171], v[68:71]\n"
    "v_mfma_f32_16x16x32_bf16 v[100:103], a[200:203], a[172:175], v[100:103]\n"
    "v_mfma_f32_16x16x32_bf16 v[100:103], a[196:199], a[140:143], v[100:103]\n"
    "v_mfma_f32_16x16x32_bf16 v[132:135], a[196:199], a[144:147], v[132:135]\n"
    "v_mfma_f32_16x16x32_bf16 v[132:135], a[200:203], a[176:179], v[132:135]\n"
    "v_mfma_f32_16x16x32_bf16 v[164:167], a[200:203], a[180:183], v[164:167]\n"
    "v_mfma_f32_16x16x32_bf16 v[164:167], a[196:199], a[148:151], v[164:167]\n"
    "v_mfma_f32_16x16x32_bf16 v[196:199], a[196:199], a[152:155], v[196:199]\n"
    "v_mfma_f32_16x16x32_bf16 v[196:199], a[200:203], a[184:187], v[196:199]\n"
    "v_mfma_f32_16x16x32_bf16 v[228:231], a[200:203], a[188:191], v[228:231]\n"
    "v_mfma_f32_16x16x32_bf16 v[228:231], a[196:199], a[156:159], v[228:231]\n"
    "s_waitcnt vmcnt(9)\n"
    "buffer_load_dwordx4 a[196:199], v240, s[20:23], 0 offen offset:1024 sc1\n"
    "v_mfma_f32_16x16x32_bf16 v[232:235], a[204:207], a[156:159], v[232:235]\n"
    "buffer_load_dwordx4 a[200:203], v240, s[20:23], 0 offen offset:2048 sc1\n"
    "v_mfma_f32_16x16x32_bf16 v[232:235], a[208:211], a[188:191], v[232:235]\n"
    "v_mfma_f32_16x16x32_bf16 v[200:203], a[208:211], a[184:187], v[200:203]\n"
    "v_mfma_f32_16x16x32_bf16 v[200:203], a[204:207], a[152:155], v[200:203]\n"
    "v_mfma_f32_16x16x32_bf16 v[168:171], a[204:207], a[148:151], v[168:171]\n"
    "s_addk_i32 m0, 2048\n"
    "s_xor_b32 s92, s92, 0x101800\n"
    "v_mfma_f32_16x16x32_bf16 v[168:171], a[208:211], a[180:183], v[168:171]\n"
    "buffer_load_dwordx4 v240, s[92:95], 0 offen sc1 lds\n"
    "v_mfma_f32_16x16x32_bf16 v[136:139], a[208:211], a[176:179], v[136:139]\n"
    "v_mfma_f32_16x16x32_bf16 v[136:139], a[204:207], a[144:147], v[136:139]\n"
    "v_mfma_f32_16x16x32_bf16 v[104:107], a[204:207], a[140:143], v[104:107]\n"
    "v_mfma_f32_16x16x32_bf16 v[104:107], a[208:211], a[172:175], v[104:107]\n"
    "v_mfma_f32_16x16x32_bf16 v[72:75], a[208:211], a[168:171], v[72:75]\n"
    "v_mfma_f32_16x16x32_bf16 v[72:75], a[204:207], a[136:139], v[72:75]\n"
    "v_mfma_f32_16x16x32_bf16 v[40:43], a[204:207], a[132:135], v[40:43]\n"
    "v_mfma_f32_16x16x32_bf16 v[40:43], a[208:211], a[164:167], v[40:43]\n"
    "v_mfma_f32_16x16x32_bf16 v[8:11], a[208:211], a[160:163], v[8:11]\n"
    "v_mfma_f32_16x16x32_bf16 v[8:11], a[204:207], a[128:131], v[8:11]\n"
    "s_waitcnt vmcnt(10)\n"
    "buffer_load_dwordx4 a[204:207], v240, s[20:23], 0 offen offset:3072 sc1\n"
    "v_mfma_f32_16x16x32_bf16 v[12:15], a[216:219], a[128:131], v[12:15]\n"
    "s_addk_i32 s20, 4096\n"
    "buffer_load_dwordx4 a[208:211], v240, s[20:23], 0 offen offset:0 sc1\n"
    "v_mfma_f32_16x16x32_bf16 v[12:15], a[220:223], a[160:163], v[12:15]\n"
    "buffer_load_dwordx4 a[212:215], v240, s[20:23], 0 offen offset:1024 sc1\n"
    "v_mfma_f32_16x16x32_bf16 v[44:47], a[220:223], a[164:167], v[44:47]\n"
    "v_mfma_f32_16x16x32_bf16 v[44:47], a[216:219], a[132:135], v[44:47]\n"
    "v_mfma_f32_16x16x32_bf16 v[76:79], a[216:219], a[136:139], v[76:79]\n"
    "v_mfma_f32_16x16x32_bf16 v[76:79], a[220:223], a[168:171], v[76:79]\n"
    "v_mfma_f32_16x16x32_bf16 v[108:111], a[220:223], a[172:175], v[108:111]\n"
    "v_mfma_f32_16x16x32_bf16 v[108:111], a[216:219], a[140:143], v[108:111]\n"
    "v_mfma_f32_16x16x32_bf16 v[140:143], a[216:219], a[144:147], v[140:143]\n"
    "v_mfma_f32_16x16x32_bf16 v[140:143], a[220:223], a[176:179], v[140:143]\n"
    "v_mfma_f32_16x16x32_bf16 v[172:175], a[220:223], a[180:183], v[172:175]\n"
    "v_mfma_f32_16x16x32_bf16 v[172:175], a[216:219], a[148:151], v[172:175]\n"
    "v_mfma_f32_16x16x32_bf16 v[204:207], a[216:219], a[152:155], v[204:207]\n"
    "s_addk_i32 m0, 2048\n"
    "s_xor_b32 s92, s92, 0x800\n"
    "v_mfma_f32_16x16x32_bf16 v[204:207], a[220:223], a[184:187], v[204:207]\n"
    "buffer_load_dwordx4 v240, s[92:95], 0 offen sc1 lds\n"
    "v_mfma_f32_16x16x32_bf16 v[236:239], a[220:223], a[188:191], v[236:239]\n"
    "v_mfma_f32_16x16x32_bf16 v[236:239], a[216:219], a[156:159], v[236:239]\n"
    "s_waitcnt vmcnt(11)\n"
    "buffer_load_dwordx4 a[216:219], v240, s[20:23], 0 offen offset:2048 sc1\n"
    "v_mfma_f32_16x16x32_bf16 a[240:243], a[224:227], a[156:159], a[240:243]\n"
    "buffer_load_dwordx4 a[220:223], v240, s[20:23], 0 offen offset:3072 sc1\n"
    "s_addk_i32 s20, 4096\n"
    "v_mfma_f32_16x16x32_bf16 a[240:243], a[228:231], a[188:191], a[240:243]\n"
    "v_mfma_f32_16x16x32_bf16 v[208:211], a[228:231], a[184:187], v[208:211]\n"
    "v_mfma_f32_16x16x32_bf16 v[208:211], a[224:227], a[152:155], v[208:211]\n"
    "v_mfma_f32_16x16x32_bf16 v[176:179], a[224:227], a[148:151], v[176:179]\n"
    "v_mfma_f32_16x16x32_bf16 v[176:179], a[228:231], a[180:183], v[176:179]\n"
    "v_mfma_f32_16x16x32_bf16 v[144:147], a[228:231], a[176:179], v[144:147]\n"
    "v_mfma_f32_16x16x32_bf16 v[144:147], a[224:227], a[144:147], v[144:147]\n"
    "v_mfma_f32_16x16x32_bf16 v[112:115], a[224:227], a[140:143], v[112:115]\n"
    "v_mfma_f32_16x16x32_bf16 v[112:115], a[228:231], a[172:175], v[112:115]\n"
    "s_bitcmp1_b32 0x404, s52\n"
    "s_cbranch_scc1 OA_%=\n"
    "OB_%=:\n"
    "v_mfma_f32_16x16x32_bf16 v[80:83], a[228:231], a[168:171], v[80:83]\n"
    "v_mfma_f32_16x16x32_bf16 v[80:83], a[224:227], a[136:139], v[80:83]\n"
    "v_mfma_f32_16x16x32_bf16 v[48:51], a[224:227], a[132:135], v[48:51]\n"
    "v_mfma_f32_16x16x32_bf16 v[48:51], a[228:231], a[164:167], v[48:51]\n"
    "v_mfma_f32_16x16x32_bf16 v[16:19], a[228:231], a[160:163], v[16:19]\n"
    "v_mfma_f32_16x16x32_bf16 v[16:19], a[224:227], a[128:131], v[16:19]\n"
    "s_waitcnt vmcnt(10)\n"
    "buffer_load_dwordx4 a[224:227], v240, s[20:23], 0 offen offset:0 sc1\n"
    "v_mfma_f32_16x16x32_bf16 v[20:23], a[232:235], a[128:131], v[20:23]\n"
    "buffer_load_dwordx4 a[228:231], v240, s[20:23], 0 offen offset:1024 sc1\n"
    "v_mfma_f32_16x16x32_bf16 v[20:23], a[236:239], a[160:163], v[20:23]\n"
    "v_mfma_f32_16x16x32_bf16 v[52:55], a[236:239], a[164:167], v[52:55]\n"
    "v_mfma_f32_16x16x32_bf16 v[52:55], a[232:235], a[132:135], v[52:55]\n"
    "v_mfma_f32_16x16x32_bf16 v[84:87], a[232:235], a[136:139], v[84:87]\n"
    "v_mfma_f32_16x16x32_bf16 v[84:87], a[236:239], a[168:171], v[84:87]\n"
    "v_mfma_f32_16x16x32_bf16 v[116:119], a[236:239], a[172:175], v[116:119]\n"
    "v_mfma_f32_16x16x32_bf16 v[116:119], a[232:235], a[140:143], v[116:119]\n"
    "v_mfma_f32_16x16x32_bf16 v[148:151], a[232:235], a[144:147], v[148:151]\n"
    "s_mov_b32 m0, s71\n"
    "s_xor_b32 s92, s92, 0x195800\n"
    "v_mfma_f32_16x16x32_bf16 v[148:151], a[236:239], a[176:179], v[148:151]\n"
    "buffer_load_dwordx4 v240, s[92:95], 0 offen sc1 lds\n"
    "v_mfma_f32_16x16x32_bf16 v[180:183], a[236:239], a[180:183], v[180:183]\n"
    "v_mfma_f32_16x16x32_bf16 v[180:183], a[232:235], a[148:151], v[180:183]\n"
    "v_mfma_f32_16x16x32_bf16 v[212:215], a[232:235], a[152:155], v[212:215]\n"
    "v_mfma_f32_16x16x32_bf16 v[212:215], a[236:239], a[184:187], v[212:215]\n"
    "v_mfma_f32_16x16x32_bf16 a[244:247], a[236:239], a[188:191], a[244:247]\n"
    "v_mfma_f32_16x16x32_bf16 a[244:247], a[232:235], a[156:159], a[244:247]\n"
    "s_waitcnt vmcnt(11)\n"
    "buffer_load_dwordx4 a[232:235], v240, s[20:23], 0 offen offset:2048 sc1\n"
    "v_mfma_f32_16x16x32_bf16 a[248:251], a[192:195], a[156:159], a[248:251]\n"
    "buffer_load_dwordx4 a[236:239], v240, s[20:23], 0 offen offset:3072 sc1\n"
    "s_addk_i32 s20, 4096\n"
    "v_mfma_f32_16x16x32_bf16 a[248:251], a[196:199], a[188:191], a[248:251]\n"
    "v_mfma_f32_16x16x32_bf16 v[216:219], a[196:199], a[184:187], v[216:219]\n"
    "v_mfma_f32_16x16x32_bf16 v[216:219], a[192:195], a[152:155], v[216:219]\n"
    "v_mfma_f32_16x16x32_bf16 v[184:187], a[192:195], a[148:151], v[184:187]\n"
    "v_mfma_f32_16x16x32_bf16 v[184:187], a[196:199], a[180:183], v[184:187]\n"
    "v_mfma_f32_16x16x32_bf16 v[152:155], a[196:199], a[176:179], v[152:155]\n"
    "v_mfma_f32_16x16x32_bf16 v[152:155], a[192:195], a[144:147], v[152:155]\n"
    "v_mfma_f32_16x16x32_bf16 v[120:123], a[192:195], a[140:143], v[120:123]\n"
    "v_mfma_f32_16x16x32_bf16 v[120:123], a[196:199], a[172:175], v[120:123]\n"
    "v_mfma_f32_16x16x32_bf16 v[88:91], a[196:199], a[168:171], v[88:91]\n"
    "v_mfma_f32_16x16x32_bf16 v[88:91], a[192:195], a[136:139], v[88:91]\n"
    "s_addk_i32 m0, 2048\n"
    "s_xor_b32 s92, s92, 0x800\n"
    "v_mfma_f32_16x16x32_bf16 v[56:59], a[192:195], a[132:135], v[56:59]\n"
    "buffer_load_dwordx4 v240, s[92:95], 0 offen sc1 lds\n"
    "v_mfma_f32_16x16x32_bf16 v[56:59], a[196:199], a[164:167], v[56:59]\n"
    "v_mfma_f32_16x16x32_bf16 v[24:27], a[196:199], a[160:163], v[24:27]\n"
    "v_mfma_f32_16x16x32_bf16 v[24:27], a[192:195], a[128:131], v[24:27]\n"
    "s_waitcnt vmcnt(11)\n"
    "buffer_load_dwordx4 a[192:195], v240, s[20:23], 0 offen offset:0 sc1\n"
    "v_mfma_f32_16x16x32_bf16 v[28:31], a[200:203], a[128:131], v[28:31]\n"
    "buffer_load_dwordx4 a[196:199], v240, s[20:23], 0 offen offset:1024 sc1\n"
    "v_mfma_f32_16x16x32_bf16 v[28:31], a[204:207], a[160:163], v[28:31]\n"
    "v_mfma_f32_16x16x32_bf16 v[60:63], a[204:207], a[164:167], v[60:63]\n"
    "ds_read_b128 a[128:131], v241 offset:49152\n"
    "v_mfma_f32_16x16x32_bf16 v[60:63], a[200:203], a[132:135], v[60:63]\n"
    "ds_read_b128 a[160:163], v241 offset:50176\n"
    "v_mfma_f32_16x16x32_bf16 v[92:95], a[200:203], a[136:139], v[92:95]\n"
    "ds_read_b128 a[164:167], v241 offset:52224\n"
    "v_mfma_f32_16x16x32_bf16 v[92:95], a[204:207], a[168:171], v[92:95]\n"
    "ds_read_b128 a[132:135], v241 offset:51200\n"
    "v_mfma_f32_16x16x32_bf16 v[124:127], a[204:207], a[172:175], v[124:127]\n"
    "ds_read_b128 a[136:139], v241 offset:53248\n"
    "v_mfma_f32_16x16x32_bf16 v[124:127], a[200:203], a[140:143], v[124:127]\n"
    "ds_read_b128 a[168:171], v241 offset:54272\n"
    "v_mfma_f32_16x16x32_bf16 v[156:159], a[200:203], a[144:147], v[156:159]\n"
    "ds_read_b128 a[172:175], v241 offset:56320\n"
    "v_mfma_f32_16x16x32_bf16 v[156:159], a[204:207], a[176:179], v[156:159]\n"
    "ds_read_b128 a[140:143], v241 offset:55296\n"
    "v_mfma_f32_16x16x32_bf16 v[188:191], a[204:207], a[180:183], v[188:191]\n"
    "ds_read_b128 a[144:147], v241 offset:57344\n"
    "v_mfma_f32_16x16x32_bf16 v[188:191], a[200:203], a[148:151], v[188:191]\n"
    "ds_read_b128 a[176:179], v241 offset:58368\n"
    "v_mfma_f32_16x16x32_bf16 v[220:223], a[200:203], a[152:155], v[220:223]\n"
    "ds_read_b128 a[180:183], v241 offset:60416\n"
    "v_mfma_f32_16x16x32_bf16 v[220:223], a[204:207], a[184:187], v[220:223]\n"
    "ds_read_b128 a[148:151], v241 offset:59392\n"
    "v_mfma_f32_16x16x32_bf16 a[252:255], a[204:207], a[188:191], a[252:255]\n"
    "s_addk_i32 m0, 2048\n"
    "s_xor_b32 s92, s92, 0x101800\n"
    "v_mfma_f32_16x16x32_bf16 a[252:255], a[200:203], a[156:159], a[252:255]\n"
    "s_waitcnt vmcnt(11) lgkmcnt(10)\n"
    "buffer_load_dwordx4 v240, s[92:95], 0 offen sc1 lds\n"
    "v_mfma_f32_16x16x32_bf16 v[0:3], a[208:211], a[128:131], v[0:3]\n"
    "ds_read_b128 a[152:155], v241 offset:61440\n"
    "v_mfma_f32_16x16x32_bf16 v[0:3], a[212:215], a[160:163], v[0:3]\n"
    "s_waitcnt lgkmcnt(9)\n"
    "ds_read_b128 a[184:187], v241 offset:62464\n"
    "v_mfma_f32_16x16x32_bf16 v[32:35], a[212:215], a[164:167], v[32:35]\n"
    "buffer_load_dwordx4 a[200:203], v240, s[20:23], 0 offen offset:2048 sc1\n"
    "v_mfma_f32_16x16x32_bf16 v[32:35], a[208:211], a[132:135], v[32:35]\n"
    "s_waitcnt lgkmcnt(8)\n"
    "buffer_load_dwordx4 a[204:207], v240, s[20:23], 0 offen offset:3072 sc1\n"
    "s_addk_i32 s20, 4096\n"
    "ds_read_b128 a[188:191], v241 offset:64512\n"
    "v_mfma_f32_16x16x32_bf16 v[64:67], a[208:211], a[136:139], v[64:67]\n"
    "ds_read_b128 a[156:159], v241 offset:63488\n"
    "v_mfma_f32_16x16x32_bf16 v[64:67], a[212:215], a[168:171], v[64:67]\n"
    "s_waitcnt lgkmcnt(8)\n"
    "v_mfma_f32_16x16x32_bf16 v[96:99], a[212:215], a[172:175], v[96:99]\n"
    "v_mfma_f32_16x16x32_bf16 v[96:99], a[208:211], a[140:143], v[96:99]\n"
    "s_waitcnt lgkmcnt(6)\n"
    "v_mfma_f32_16x16x32_bf16 v[128:131], a[208:211], a[144:147], v[128:131]\n"
    "v_mfma_f32_16x16x32_bf16 v[128:131], a[212:215], a[176:179], v[128:131]\n"
    "s_waitcnt lgkmcnt(4)\n"
    "v_mfma_f32_16x16x32_bf16 v[160:163], a[212:215], a[180:183], v[160:163]\n"
    "v_mfma_f32_16x16x32_bf16 v[160:163], a[208:211], a[148:151], v[160:163]\n"
    "s_waitcnt lgkmcnt(2)\n"
    "v_mfma_f32_16x16x32_bf16 v[192:195], a[208:211], a[152:155], v[192:195]\n"
    "v_mfma_f32_16x16x32_bf16 v[192:195], a[212:215], a[184:187], v[192:195]\n"
    "s_waitcnt lgkmcnt(0)\n"
    "v_mfma_f32_16x16x32_bf16 v[224:227], a[212:215], a[188:191], v[224:227]\n"
    "v_mfma_f32_16x16x32_bf16 v[224:227], a[208:211], a[156:159], v[224:227]\n"
    "s_waitcnt vmcnt(11)\n"
    "buffer_load_dwordx4 a[208:211], v240, s[20:23], 0 offen offset:0 sc1\n"
    "v_mfma_f32_16x16x32_bf16 v[228:231], a[216:219], a[156:159], v[228:231]\n"
    "buffer_load_dwordx4 a[212:215], v240, s[20:23], 0 offen offset:1024 sc1\n"
    "v_mfma_f32_16x16x32_bf16 v[228:231], a[220:223], a[188:191], v[228:231]\n"
    "s_addk_i32 m0, 2048\n"
    "s_xor_b32 s92, s92, 0x800\n"
    "v_mfma_f32_16x16x32_bf16 v[196:199], a[220:223], a[184:187], v[196:199]\n"
    "buffer_load_dwordx4 v240, s[92:95], 0 offen sc1 lds\n"
    "v_mfma_f32_16x16x32_bf16 v[196:199], a[216:219], a[152:155], v[196:199]\n"
    "v_mfma_f32_16x16x32_bf16 v[164:167], a[216:219], a[148:151], v[164:167]\n"
    "v_mfma_f32_16x16x32_bf16 v[164:167], a[220:223], a[180:183], v[164:167]\n"
    "v_mfma_f32_16x16x32_bf16 v[132:135], a[220:223], a[176:179], v[132:135]\n"
    "v_mfma_f32_16x16x32_bf16 v[132:135], a[216:219], a[144:147], v[132:135]\n"
    "v_mfma_f32_16x16x32_bf16 v[100:103], a[216:219], a[140:143], v[100:103]\n"
    "v_mfma_f32_16x16x32_bf16 v[100:103], a[220:223], a[172:175], v[100:103]\n"
    "v_mfma_f32_16x16x32_bf16 v[68:71], a[220:223], a[168:171], v[68:71]\n"
    "v_mfma_f32_16x16x32_bf16 v[68:71], a[216:219], a[136:139], v[68:71]\n"
    "v_mfma_f32_16x16x32_bf16 v[36:39], a[216:219], a[132:135], v[36:39]\n"
    "v_mfma_f32_16x16x32_bf16 v[36:39], a[220:223], a[164:167], v[36:39]\n"
    "s_mov_b32 s50, 320\n"
    "buffer_load_dword v242, v240, s[48:51], 0 offen offset:256 sc1\n"
    "v_mfma_f32_16x16x32_bf16 v[4:7], a[220:223], a[160:163], v[4:7]\n"
    "v_mfma_f32_16x16x32_bf16 v[4:7], a[216:219], a[128:131], v[4:7]\n"
    "s_waitcnt vmcnt(13)\n"
    "buffer_load_dwordx4 a[216:219], v240, s[20:23], 0 offen offset:2048 sc1\n"
    "v_mfma_f32_16x16x32_bf16 v[8:11], a[224:227], a[128:131], v[8:11]\n"
    "buffer_load_dwordx4 a[220:223], v240, s[20:23], 0 offen offset:3072 sc1\n"
    "s_addk_i32 s20, 4096\n"
    "v_mfma_f32_16x16x32_bf16 v[8:11], a[228:231], a[160:163], v[8:11]\n"
    "v_mfma_f32_16x16x32_bf16 v[40:43], a[228:231], a[164:167], v[40:43]\n"
    "v_mfma_f32_16x16x32_bf16 v[40:43], a[224:227], a[132:135], v[40:43]\n"
    "v_mfma_f32_16x16x32_bf16 v[72:75], a[224:227], a[136:139], v[72:75]\n"
    "v_mfma_f32_16x16x32_bf16 v[72:75], a[228:231], a[168:171], v[72:75]\n"
    "v_mfma_f32_16x16x32_bf16 v[104:107], a[228:231], a[172:175], v[104:107]\n"
    "v_mfma_f32_16x16x32_bf16 v[104:107], a[224:227], a[140:143], v[104:107]\n"
    "v_mfma_f32_16x16x32_bf16 v[136:139], a[224:227], a[144:147], v[136:139]\n"
    "s_addk_i32 m0, 2048\n"
    "s_xor_b32 s92, s92, 0x185800\n"
    "v_mfma_f32_16x16x32_bf16 v[136:139], a[228:231], a[176:179], v[136:139]\n"
    "buffer_load_dwordx4 v240, s[92:95], 0 offen sc1 lds\n"
    "v_mfma_f32_16x16x32_bf16 v[168:171], a[228:231], a[180:183], v[168:171]\n"
    "v_mfma_f32_16x16x32_bf16 v[168:171], a[224:227], a[148:151], v[168:171]\n"
    "v_mfma_f32_16x16x32_bf16 v[200:203], a[224:227], a[152:155], v[200:203]\n"
    "v_mfma_f32_16x16x32_bf16 v[200:203], a[228:231], a[184:187], v[200:203]\n"
    "v_mfma_f32_16x16x32_bf16 v[232:235], a[228:231], a[188:191], v[232:235]\n"
    "v_mfma_f32_16x16x32_bf16 v[232:235], a[224:227], a[156:159], v[232:235]\n"
    "s_waitcnt vmcnt(13)\n"
    "buffer_load_dwordx4 a[224:227], v240, s[20:23], 0 offen offset:0 sc1\n"
    "v_mfma_f32_16x16x32_bf16 v[236:239], a[232:235], a[156:159], v[236:239]\n"
    "buffer_load_dwordx4 a[228:231], v240, s[20:23], 0 offen offset:1024 sc1\n"
    "v_mfma_f32_16x16x32_bf16 v[236:239], a[236:239], a[188:191], v[236:239]\n"
    "v_mfma_f32_16x16x32_bf16 v[204:207], a[236:239], a[184:187], v[204:207]\n"
    "v_mfma_f32_16x16x32_bf16 v[204:207], a[232:235], a[152:155], v[204:207]\n"
    "v_mfma_f32_16x16x32_bf16 v[172:175], a[232:235], a[148:151], v[172:175]\n"
    "v_mfma_f32_16x16x32_bf16 v[172:175], a[236:239], a[180:183], v[172:175]\n"
    "v_mfma_f32_16x16x32_bf16 v[140:143], a[236:239], a[176:179], v[140:143]\n"
    "v_mfma_f32_16x16x32_bf16 v[140:143], a[232:235], a[144:147], v[140:143]\n"
    "v_mfma_f32_16x16x32_bf16 v[108:111], a[232:235], a[140:143], v[108:111]\n"
    "v_mfma_f32_16x16x32_bf16 v[108:111], a[236:239], a[172:175], v[108:111]\n"
    "v_mfma_f32_16x16x32_bf16 v[76:79], a[236:239], a[168:171], v[76:79]\n"
    "v_mfma_f32_16x16x32_bf16 v[76:79], a[232:235], a[136:139], v[76:79]\n"
    "s_addk_i32 m0, 2048\n"
    "s_xor_b32 s92, s92, 0x800\n"
    "v_mfma_f32_16x16x32_bf16 v[44:47], a[232:235], a[132:135], v[44:47]\n"
    "buffer_load_dwordx4 v240, s[92:95], 0 offen sc1 lds\n"
    "v_mfma_f32_16x16x32_bf16 v[44:47], a[236:239], a[164:167], v[44:47]\n"
    "v_mfma_f32_16x16x32_bf16 v[12:15], a[236:239], a[160:163], v[12:15]\n"
    "v_mfma_f32_16x16x32_bf16 v[12:15], a[232:235], a[128:131], v[12:15]\n"
    "s_waitcnt vmcnt(13)\n"
    "buffer_load_dwordx4 a[232:235], v240, s[20:23], 0 offen offset:2048 sc1\n"
    "v_mfma_f32_16x16x32_bf16 v[16:19], a[192:195], a[128:131], v[16:19]\n"
    "buffer_load_dwordx4 a[236:239], v240, s[20:23], 0 offen offset:3072 sc1\n"
    "s_addk_i32 s20, 4096\n"
    "v_mfma_f32_16x16x32_bf16 v[16:19], a[196:199], a[160:163], v[16:19]\n"
    "v_mfma_f32_16x16x32_bf16 v[48:51], a[196:199], a[164:167], v[48:51]\n"
    "v_mfma_f32_16x16x32_bf16 v[48:51], a[192:195], a[132:135], v[48:51]\n"
    "v_mfma_f32_16x16x32_bf16 v[80:83], a[192:195], a[136:139], v[80:83]\n"
    "v_mfma_f32_16x16x32_bf16 v[80:83], a[196:199], a[168:171], v[80:83]\n"
    "v_mfma_f32_16x16x32_bf16 v[112:115], a[196:199], a[172:175], v[112:115]\n"
    "v_mfma_f32_16x16x32_bf16 v[112:115], a[192:195], a[140:143], v[112:115]\n"
    "v_mfma_f32_16x16x32_bf16 v[144:147], a[192:195], a[144:147], v[144:147]\n"
    "v_mfma_f32_16x16x32_bf16 v[144:147], a[196:199], a[176:179], v[144:147]\n"
    "v_mfma_f32_16x16x32_bf16 v[176:179], a[196:199], a[180:183], v[176:179]\n"
    "v_mfma_f32_16x16x32_bf16 v[176:179], a[192:195], a[148:151], v[176:179]\n"
    "v_mfma_f32_16x16x32_bf16 v[208:211], a[192:195], a[152:155], v[208:211]\n"
    "v_mfma_f32_16x16x32_bf16 v[208:211], a[196:199], a[184:187], v[208:211]\n"
    "v_mfma_f32_16x16x32_bf16 a[240:243], a[196:199], a[188:191], a[240:243]\n"
    "s_addk_i32 m0, 2048\n"
    "s_xor_b32 s92, s92, 0x101800\n"
    "v_mfma_f32_16x16x32_bf16 a[240:243], a[192:195], a[156:159], a[240:243]\n"
    "s_waitcnt vmcnt(12)\n"
    "buffer_load_dwordx4 v240, s[92:95], 0 offen sc1 lds\n"
    "v_mfma_f32_16x16x32_bf16 a[244:247], a[200:203], a[156:159], a[244:247]\n"
    "buffer_load_dwordx4 a[192:195], v240, s[20:23], 0 offen offset:0 sc1\n"
    "v_mfma_f32_16x16x32_bf16 a[244:247], a[204:207], a[188:191], a[244:247]\n"
    "buffer_load_dwordx4 a[196:199], v240, s[20:23], 0 offen offset:1024 sc1\n"
    "v_mfma_f32_16x16x32_bf16 v[212:215], a[204:207], a[184:187], v[212:215]\n"
    "v_mfma_f32_16x16x32_bf16 v[212:215], a[200:203], a[152:155], v[212:215]\n"
    "v_mfma_f32_16x16x32_bf16 v[180:183], a[200:203], a[148:151], v[180:183]\n"
    "v_mfma_f32_16x16x32_bf16 v[180:183], a[204:207], a[180:183], v[180:183]\n"
    "v_mfma_f32_16x16x32_bf16 v[148:151], a[204:207], a[176:179], v[148:151]\n"
    "v_mfma_f32_16x16x32_bf16 v[148:151], a[200:203], a[144:147], v[148:151]\n"
    "v_mfma_f32_16x16x32_bf16 v[116:119], a[200:203], a[140:143], v[116:119]\n"
    "v_mfma_f32_16x16x32_bf16 v[116:119], a[204:207], a[172:175], v[116:119]\n"
    "v_mfma_f32_16x16x32_bf16 v[84:87], a[204:207], a[168:171], v[84:87]\n"
    "v_mfma_f32_16x16x32_bf16 v[84:87], a[200:203], a[136:139], v[84:87]\n"
    "v_mfma_f32_16x16x32_bf16 v[52:55], a[200:203], a[132:135], v[52:55]\n"
    "v_mfma_f32_16x16x32_bf16 v[52:55], a[204:207], a[164:167], v[52:55]\n"
    "v_mfma_f32_16x16x32_bf16 v[20:23], a[204:207], a[160:163], v[20:23]\n"
    "v_mfma_f32_16x16x32_bf16 v[20:23], a[200:203], a[128:131], v[20:23]\n"
    "s_waitcnt vmcnt(13)\n"
    "buffer_load_dwordx4 a[200:203], v240, s[20:23], 0 offen offset:2048 sc1\n"
    "v_mfma_f32_16x16x32_bf16 v[24:27], a[208:211], a[128:131], v[24:27]\n"
    "buffer_load_dwordx4 a[204:207], v240, s[20:23], 0 offen offset:3072 sc1\n"
    "s_addk_i32 s20, 4096\n"
    "v_mfma_f32_16x16x32_bf16 v[24:27], a[212:215], a[160:163], v[24:27]\n"
    "s_addk_i32 m0, 2048\n"
    "s_xor_b32 s92, s92, 0x800\n"
    "v_mfma_f32_16x16x32_bf16 v[56:59], a[212:215], a[164:167], v[56:59]\n"
    "buffer_load_dwordx4 v240, s[92:95], 0 offen sc1 lds\n"
    "v_mfma_f32_16x16x32_bf16 v[56:59], a[208:211], a[132:135], v[56:59]\n"
    "v_mfma_f32_16x16x32_bf16 v[88:91], a[208:211], a[136:139], v[88:91]\n"
    "v_mfma_f32_16x16x32_bf16 v[88:91], a[212:215], a[168:171], v[88:91]\n"
    "v_mfma_f32_16x16x32_bf16 v[120:123], a[212:215], a[172:175], v[120:123]\n"
    "v_mfma_f32_16x16x32_bf16 v[120:123], a[208:211], a[140:143], v[120:123]\n"
    "v_mfma_f32_16x16x32_bf16 v[152:155], a[208:211], a[144:147], v[152:155]\n"
    "v_mfma_f32_16x16x32_bf16 v[152:155], a[212:215], a[176:179], v[152:155]\n"
    "v_mfma_f32_16x16x32_bf16 v[184:187], a[212:215], a[180:183], v[184:187]\n"
    "v_mfma_f32_16x16x32_bf16 v[184:187], a[208:211], a[148:151], v[184:187]\n"
    "v_mfma_f32_16x16x32_bf16 v[216:219], a[208:211], a[152:155], v[216:219]\n"
    "v_mfma_f32_16x16x32_bf16 v[216:219], a[212:215], a[184:187], v[216:219]\n"
    "s_barrier\n"
    "v_mfma_f32_16x16x32_bf16 a[248:251], a[212:215], a[188:191], a[248:251]\n"
    "v_mfma_f32_16x16x32_bf16 a[248:251], a[208:211], a[156:159], a[248:251]\n"
    "s_waitcnt vmcnt(12)\n"
    "buffer_load_dwordx4 a[208:211], v240, s[20:23], 0 offen offset:0 sc1\n"
    "v_mfma_f32_16x16x32_bf16 a[252:255], a[216:219], a[156:159], a[252:255]\n"
    "buffer_load_dwordx4 a[212:215], v240, s[20:23], 0 offen offset:1024 sc1\n"
    "v_mfma_f32_16x16x32_bf16 a[252:255], a[220:223], a[188:191], a[252:255]\n"
    "v_mfma_f32_16x16x32_bf16 v[220:223], a[220:223], a[184:187], v[220:223]\n"
    "v_add_u32 v241, s67, v240\n"
    "v_mfma_f32_16x16x32_bf16 v[220:223], a[216:219], a[152:155], v[220:223]\n"
    "ds_read_b128 a[156:159], v241 offset:14336\n"
    "v_mfma_f32_16x16x32_bf16 v[188:191], a[216:219], a[148:151], v[188:191]\n"
    "ds_read_b128 a[188:191], v241 offset:15360\n"
    "v_mfma_f32_16x16x32_bf16 v[188:191], a[220:223], a[180:183], v[188:191]\n"
    "ds_read_b128 a[184:187], v241 offset:13312\n"
    "v_mfma_f32_16x16x32_bf16 v[156:159], a[220:223], a[176:179], v[156:159]\n"
    "ds_read_b128 a[152:155], v241 offset:12288\n"
    "v_mfma_f32_16x16x32_bf16 v[156:159], a[216:219], a[144:147], v[156:159]\n"
    "ds_read_b128 a[148:151], v241 offset:10240\n"
    "v_cmp_le_u32 vcc, s52, v242\n"
    "s_nop 1\n"
    "s_and_b32 s63, vcc_lo, 15\n"
    "s_cmp_eq_u32 s63, 15\n"
    "s_cbranch_scc1 LA8_%=\n"
    "s_mov_b32 s69, 0x400000\n"
    "LA9_%=:\n"
    "s_sleep 1\n"
    "buffer_load_dword v242, v240, s[48:51], 0 offen offset:256 sc1\n"
    "s_waitcnt vmcnt(0)\n"
    "v_cmp_le_u32 vcc, s52, v242\n"
    "s_sub_u32 s69, s69, 1\n"
    "s_cbranch_scc1 LA8_%=\n"
    "s_and_b32 s63, vcc_lo, 15\n"
    "s_cmp_eq_u32 s63, 15\n"
    "s_cbranch_scc0 LA9_%=\n"
    "LA8_%=:\n"
    "v_mfma_f32_16x16x32_bf16 v[124:127], a[216:219], a[140:143], v[124:127]\n"
    "ds_read_b128 a[180:183], v241 offset:11264\n"
    "s_add_u32 m0, s71, 32768\n"
    "v_mfma_f32_16x16x32_bf16 v[124:127], a[220:223], a[172:175], v[124:127]\n"
    "s_xor_b32 s92, s92, 0x1b5800\n"
    "buffer_load_dwordx4 v240, s[92:95], 0 offen sc1 lds\n"
    "v_mfma_f32_16x16x32_bf16 v[92:95], a[220:223], a[168:171], v[92:95]\n"
    "ds_read_b128 a[176:179], v241 offset:9216\n"
    "v_mfma_f32_16x16x32_bf16 v[92:95], a[216:219], a[136:139], v[92:95]\n"
    "ds_read_b128 a[144:147], v241 offset:8192\n"
    "v_mfma_f32_16x16x32_bf16 v[60:63], a[216:219], a[132:135], v[60:63]\n"
    "ds_read_b128 a[140:143], v241 offset:6144\n"
    "v_mfma_f32_16x16x32_bf16 v[60:63], a[220:223], a[164:167], v[60:63]\n"
    "ds_read_b128 a[172:175], v241 offset:7168\n"
    "v_mfma_f32_16x16x32_bf16 v[28:31], a[220:223], a[160:163], v[28:31]\n"
    "ds_read_b128 a[168:171], v241 offset:5120\n"
    "v_mfma_f32_16x16x32_bf16 v[28:31], a[216:219], a[128:131], v[28:31]\n"
    "s_waitcnt vmcnt(12) lgkmcnt(9)\n"
    "ds_read_b128 a[136:139], v241 offset:4096\n"
    "v_mfma_f32_16x16x32_bf16 v[224:227], a[224:227], a[156:159], v[224:227]\n"
    "ds_read_b128 a[132:135], v241 offset:2048\n"
    "v_mfma_f32_16x16x32_bf16 v[224:227], a[228:231], a[188:191], v[224:227]\n"
    "s_waitcnt lgkmcnt(9)\n"
    "ds_read_b128 a[164:167], v241 offset:3072\n"
    "v_mfma_f32_16x16x32_bf16 v[192:195], a[228:231], a[184:187], v[192:195]\n"
    "buffer_load_dwordx4 a[216:219], v240, s[20:23], 0 offen offset:2048 sc1\n"
    "v_mfma_f32_16x16x32_bf16 v[192:195], a[224:227], a[152:155], v[192:195]\n"
    "s_waitcnt lgkmcnt(8)\n"
    "buffer_load_dwordx4 a[220:223], v240, s[20:23], 0 offen offset:3072 sc1\n"
    "s_addk_i32 s20, 4096\n"
    "ds_read_b128 a[160:163], v241 offset:1024\n"
    "v_mfma_f32_16x16x32_bf16 v[160:163], a[224:227], a[148:151], v[160:163]\n"
    "ds_read_b128 a[128:131], v241 offset:0\n"
    "v_mfma_f32_16x16x32_bf16 v[160:163], a[228:231], a[180:183], v[160:163]\n"
    "s_waitcnt lgkmcnt(8)\n"
    "v_mfma_f32_16x16x32_bf16 v[128:131], a[228:231], a[176:179], v[128:131]\n"
    "v_mfma_f32_16x16x32_bf16 v[128:131], a[224:227], a[144:147], v[128:131]\n"
    "s_waitcnt lgkmcnt(6)\n"
    "v_mfma_f32_16x16x32_bf16 v[96:99], a[224:227], a[140:143], v[96:99]\n"
    "s_addk_i32 m0, 2048\n"
    "s_xor_b32 s92, s92, 0x800\n"
    "v_mfma_f32_16x16x32_bf16 v[96:99], a[228:231], a[172:175], v[96:99]\n"
    "s_waitcnt lgkmcnt(4)\n"
    "buffer_load_dwordx4 v240, s[92:95], 0 offen sc1 lds\n"
    "v_mfma_f32_16x16x32_bf16 v[64:67], a[228:231], a[168:171], v[64:67]\n"
    "v_mfma_f32_16x16x32_bf16 v[64:67], a[224:227], a[136:139], v[64:67]\n"
    "s_waitcnt lgkmcnt(2)\n"
    "v_mfma_f32_16x16x32_bf16 v[32:35], a[224:227], a[132:135], v[32:35]\n"
    "v_mfma_f32_16x16x32_bf16 v[32:35], a[228:231], a[164:167], v[32:35]\n"
    "s_waitcnt lgkmcnt(0)\n"
    "v_mfma_f32_16x16x32_bf16 v[0:3], a[228:231], a[160:163], v[0:3]\n"
    "v_mfma_f32_16x16x32_bf16 v[0:3], a[224:227], a[128:131], v[0:3]\n"
    "s_waitcnt vmcnt(12)\n"
    "buffer_load_dwordx4 a[224:227], v240, s[20:23], 0 offen offset:0 sc1\n"
    "v_mfma_f32_16x16x32_bf16 v[4:7], a[232:235], a[128:131], v[4:7]\n"
    "buffer_load_dwordx4 a[228:231], v240, s[20:23], 0 offen offset:1024 sc1\n"
    "v_mfma_f32_16x16x32_bf16 v[4:7], a[236:239], a[160:163], v[4:7]\n"
    "v_mfma_f32_16x16x32_bf16 v[36:39], a[236:239], a[164:167], v[36:39]\n"
    "v_mfma_f32_16x16x32_bf16 v[36:39], a[232:235], a[132:135], v[36:39]\n"
    "v_mfma_f32_16x16x32_bf16 v[68:71], a[232:235], a[136:139], v[68:71]\n"
    "v_mfma_f32_16x16x32_bf16 v[68:71], a[236:239], a[168:171], v[68:71]\n"
    "v_mfma_f32_16x16x32_bf16 v[100:103], a[236:239], a[172:175], v[100:103]\n"
    "v_mfma_f32_16x16x32_bf16 v[100:103], a[232:235], a[140:143], v[100:103]\n"
    "v_mfma_f32_16x16x32_bf16 v[132:135], a[232:235], a[144:147], v[132:135]\n"
    "s_addk_i32 m0, 2048\n"
    "s_xor_b32 s92, s92, 0x101800\n"
    "v_mfma_f32_16x16x32_bf16 v[132:135], a[236:239], a[176:179], v[132:135]\n"
    "buffer_load_dwordx4 v240, s[92:95], 0 offen sc1 lds\n"
    "v_mfma_f32_16x16x32_bf16 v[164:167], a[236:239], a[180:183], v[164:167]\n"
    "v_mfma_f32_16x16x32_bf16 v[164:167], a[232:235], a[148:151], v[164:167]\n"
    "v_mfma_f32_16x16x32_bf16 v[196:199], a[232:235], a[152:155], v[196:199]\n"
    "v_mfma_f32_16x16x32_bf16 v[196:199], a[236:239], a[184:187], v[196:199]\n"
    "v_mfma_f32_16x16x32_bf16 v[228:231], a[236:239], a[188:191], v[228:231]\n"
    "v_mfma_f32_16x16x32_bf16 v[228:231], a[232:235], a[156:159], v[228:231]\n"
    "s_waitcnt vmcnt(12)\n"
    "buffer_load_dwordx4 a[232:235], v240, s[20:23], 0 offen offset:2048 sc1\n"
    "v_mfma_f32_16x16x32_bf16 v[232:235], a[192:195], a[156:159], v[232:235]\n"
    "buffer_load_dwordx4 a[236:239], v240, s[20:23], 0 offen offset:3072 sc1\n"
    "s_addk_i32 s20, 4096\n"
    "v_mfma_f32_16x16x32_bf16 v[232:235], a[196:199], a[188:191], v[232:235]\n"
    "v_mfma_f32_16x16x32_bf16 v[200:203], a[196:199], a[184:187], v[200:203]\n"
    "v_mfma_f32_16x16x32_bf16 v[200:203], a[192:195], a[152:155], v[200:203]\n"
    "v_mfma_f32_16x16x32_bf16 v[168:171], a[192:195], a[148:151], v[168:171]\n"
    "v_mfma_f32_16x16x32_bf16 v[168:171], a[196:199], a[180:183], v[168:171]\n"
    "v_mfma_f32_16x16x32_bf16 v[136:139], a[196:199], a[176:179], v[136:139]\n"
    "v_mfma_f32_16x16x32_bf16 v[136:139], a[192:195], a[144:147], v[136:139]\n"
    "v_mfma_f32_16x16x32_bf16 v[104:107], a[192:195], a[140:143], v[104:107]\n"
    "s_addk_i32 m0, 2048\n"
    "s_xor_b32 s92, s92, 0x800\n"
    "v_mfma_f32_16x16x32_bf16 v[104:107], a[196:199], a[172:175], v[104:107]\n"
    "buffer_load_dwordx4 v240, s[92:95], 0 offen sc1 lds\n"
    "v_mfma_f32_16x16x32_bf16 v[72:75], a[196:199], a[168:171], v[72:75]\n"
    "v_mfma_f32_16x16x32_bf16 v[72:75], a[192:195], a[136:139], v[72:75]\n"
    "v_mfma_f32_16x16x32_bf16 v[40:43], a[192:195], a[132:135], v[40:43]\n"
    "v_mfma_f32_16x16x32_bf16 v[40:43], a[196:199], a[164:167], v[40:43]\n"
    "v_mfma_f32_16x16x32_bf16 v[8:11], a[196:199], a[160:163], v[8:11]\n"
    "v_mfma_f32_16x16x32_bf16 v[8:11], a[192:195], a[128:131], v[8:11]\n"
    "s_waitcnt vmcnt(13)\n"
    "buffer_load_dwordx4 a[192:195], v240, s[20:23], 0 offen offset:0 sc1\n"
    "v_mfma_f32_16x16x32_bf16 v[12:15], a[200:203], a[128:131], v[12:15]\n"
    "buffer_load_dwordx4 a[196:199], v240, s[20:23], 0 offen offset:1024 sc1\n"
    "v_mfma_f32_16x16x32_bf16 v[12:15], a[204:207], a[160:163], v[12:15]\n"
    "v_mfma_f32_16x16x32_bf16 v[44:47], a[204:207], a[164:167], v[44:47]\n"
    "v_mfma_f32_16x16x32_bf16 v[44:47], a[200:203], a[132:135], v[44:47]\n"
    "v_mfma_f32_16x16x32_bf16 v[76:79], a[200:203], a[136:139], v[76:79]\n"
    "v_mfma_f32_16x16x32_bf16 v[76:79], a[204:207], a[168:171], v[76:79]\n"
    "v_mfma_f32_16x16x32_bf16 v[108:111], a[204:207], a[172:175], v[108:111]\n"
    "v_mfma_f32_16x16x32_bf16 v[108:111], a[200:203], a[140:143], v[108:111]\n"
    "v_mfma_f32_16x16x32_bf16 v[140:143], a[200:203], a[144:147], v[140:143]\n"
    "v_mfma_f32_16x16x32_bf16 v[140:143], a[204:207], a[176:179], v[140:143]\n"
    "v_mfma_f32_16x16x32_bf16 v[172:175], a[204:207], a[180:183], v[172:175]\n"
    "v_mfma_f32_16x16x32_bf16 v[172:175], a[200:203], a[148:151], v[172:175]\n"
    "v_mfma_f32_16x16x32_bf16 v[204:207], a[200:203], a[152:155], v[204:207]\n"
    "v_mfma_f32_16x16x32_bf16 v[204:207], a[204:207], a[184:187], v[204:207]\n"
    "v_mfma_f32_16x16x32_bf16 v[236:239], a[204:207], a[188:191], v[236:239]\n"
    "s_addk_i32 m0, 2048\n"
    "s_xor_b32 s92, s92, 0x185800\n"
    "v_mfma_f32_16x16x32_bf16 v[236:239], a[200:203], a[156:159], v[236:239]\n"
    "s_waitcnt vmcnt(12)\n"
    "buffer_load_dwordx4 v240, s[92:95], 0 offen sc1 lds\n"
    "v_mfma_f32_16x16x32_bf16 a[240:243], a[208:211], a[156:159], a[240:243]\n"
    "buffer_load_dwordx4 a[200:203], v240, s[20:23], 0 offen offset:2048 sc1\n"
    "v_mfma_f32_16x16x32_bf16 a[240:243], a[212:215], a[188:191], a[240:243]\n"
    "buffer_load_dwordx4 a[204:207], v240, s[20:23], 0 offen offset:3072 sc1\n"
    "s_addk_i32 s20, 4096\n"
    "v_mfma_f32_16x16x32_bf16 v[208:211], a[212:215], a[184:187], v[208:211]\n"
    "v_mfma_f32_16x16x32_bf16 v[208:211], a[208:211], a[152:155], v[208:211]\n"
    "v_mfma_f32_16x16x32_bf16 v[176:179], a[208:211], a[148:151], v[176:179]\n"
    "v_mfma_f32_16x16x32_bf16 v[176:179], a[212:215], a[180:183], v[176:179]\n"
    "v_mfma_f32_16x16x32_bf16 v[144:147], a[212:215], a[176:179], v[144:147]\n"
    "v_mfma_f32_16x16x32_bf16 v[144:147], a[208:211], a[144:147], v[144:147]\n"
    "v_mfma_f32_16x16x32_bf16 v[112:115], a[208:211], a[140:143], v[112:115]\n"
    "v_mfma_f32_16x16x32_bf16 v[112:115], a[212:215], a[172:175], v[112:115]\n"
    "v_mfma_f32_16x16x32_bf16 v[80:83], a[212:215], a[168:171], v[80:83]\n"
    "v_mfma_f32_16x16x32_bf16 v[80:83], a[208:211], a[136:139], v[80:83]\n"
    "v_mfma_f32_16x16x32_bf16 v[48:51], a[208:211], a[132:135], v[48:51]\n"
    "v_mfma_f32_16x16x32_bf16 v[48:51], a[212:215], a[164:167], v[48:51]\n"
    "v_mfma_f32_16x16x32_bf16 v[16:19], a[212:215], a[160:163], v[16:19]\n"
    "s_addk_i32 m0, 2048\n"
    "s_xor_b32 s92, s92, 0x800\n"
    "v_mfma_f32_16x16x32_bf16 v[16:19], a[208:211], a[128:131], v[16:19]\n"
    "s_waitcnt vmcnt(12)\n"
    "buffer_load_dwordx4 v240, s[92:95], 0 offen sc1 lds\n"
    "v_mfma_f32_16x16x32_bf16 v[20:23], a[216:219], a[128:131], v[20:23]\n"
    "buffer_load_dwordx4 a[208:211], v240, s[20:23], 0 offen offset:0 sc1\n"
    "v_mfma_f32_16x16x32_bf16 v[20:23], a[220:223], a[160:163], v[20:23]\n"
    "buffer_load_dwordx4 a[212:215], v240, s[20:23], 0 offen offset:1024 sc1\n"
    "v_mfma_f32_16x16x32_bf16 v[52:55], a[220:223], a[164:167], v[52:55]\n"
    "v_mfma_f32_16x16x32_bf16 v[52:55], a[216:219], a[132:135], v[52:55]\n"
    "v_mfma_f32_16x16x32_bf16 v[84:87], a[216:219], a[136:139], v[84:87]\n"
    "v_mfma_f32_16x16x32_bf16 v[84:87], a[220:223], a[168:171], v[84:87]\n"
    "v_mfma_f32_16x16x32_bf16 v[116:119], a[220:223], a[172:175], v[116:119]\n"
    "v_mfma_f32_16x16x32_bf16 v[116:119], a[216:219], a[140:143], v[116:119]\n"
    "v_mfma_f32_16x16x32_bf16 v[148:151], a[216:219], a[144:147], v[148:151]\n"
    "v_mfma_f32_16x16x32_bf16 v[148:151], a[220:223], a[176:179], v[148:151]\n"
    "v_mfma_f32_16x16x32_bf16 v[180:183], a[220:223], a[180:183], v[180:183]\n"
    "v_mfma_f32_16x16x32_bf16 v[180:183], a[216:219], a[148:151], v[180:183]\n"
    "v_mfma_f32_16x16x32_bf16 v[212:215], a[216:219], a[152:155], v[212:215]\n"
    "v_mfma_f32_16x16x32_bf16 v[212:215], a[220:223], a[184:187], v[212:215]\n"
    "v_mfma_f32_16x16x32_bf16 a[244:247], a[220:223], a[188:191], a[244:247]\n"
    "s_addk_i32 m0, 2048\n"
    "s_xor_b32 s92, s92, 0x101800\n"
    "v_mfma_f32_16x16x32_bf16 a[244:247], a[216:219], a[156:159], a[244:247]\n"
    "s_waitcnt vmcnt(12)\n"
    "buffer_load_dwordx4 v240, s[92:95], 0 offen sc1 lds\n"
    "v_mfma_f32_16x16x32_bf16 a[248:251], a[224:227], a[156:159], a[248:251]\n"
    "buffer_load_dwordx4 a[216:219], v240, s[20:23], 0 offen offset:2048 sc1\n"
    "v_mfma_f32_16x16x32_bf16 a[248:251], a[228:231], a[188:191], a[248:251]\n"
    "buffer_load_dwordx4 a[220:223], v240, s[20:23], 0 offen offset:3072 sc1\n"
    "s_addk_i32 s20, 4096\n"
    "v_mfma_f32_16x16x32_bf16 v[216:219], a[228:231], a[184:187], v[216:219]\n"
    "v_mfma_f32_16x16x32_bf16 v[216:219], a[224:227], a[152:155], v[216:219]\n"
    "v_mfma_f32_16x16x32_bf16 v[184:187], a[224:227], a[148:151], v[184:187]\n"
    "v_mfma_f32_16x16x32_bf16 v[184:187], a[228:231], a[180:183], v[184:187]\n"
    "v_mfma_f32_16x16x32_bf16 v[152:155], a[228:231], a[176:179], v[152:155]\n"
    "v_mfma_f32_16x16x32_bf16 v[152:155], a[224:227], a[144:147], v[152:155]\n"
    "v_mfma_f32_16x16x32_bf16 v[120:123], a[224:227], a[140:143], v[120:123]\n"
    "v_mfma_f32_16x16x32_bf16 v[120:123], a[228:231], a[172:175], v[120:123]\n"
    "v_mfma_f32_16x16x32_bf16 v[88:91], a[228:231], a[168:171], v[88:91]\n"
    "v_mfma_f32_16x16x32_bf16 v[88:91], a[224:227], a[136:139], v[88:91]\n"
    "v_mfma_f32_16x16x32_bf16 v[56:59], a[224:227], a[132:135], v[56:59]\n"
    "v_mfma_f32_16x16x32_bf16 v[56:59], a[228:231], a[164:167], v[56:59]\n"
    "v_mfma_f32_16x16x32_bf16 v[24:27], a[228:231], a[160:163], v[24:27]\n"
    "s_addk_i32 m0, 2048\n"
    "s_xor_b32 s92, s92, 0x800\n"
    "v_mfma_f32_16x16x32_bf16 v[24:27], a[224:227], a[128:131], v[24:27]\n"
    "s_waitcnt vmcnt(12)\n"
    "buffer_load_dwordx4 v240, s[92:95], 0 offen sc1 lds\n"
    "v_mfma_f32_16x16x32_bf16 v[28:31], a[232:235], a[128:131], v[28:31]\n"
    "buffer_load_dwordx4 a[224:227], v240, s[20:23], 0 offen offset:0 sc1\n"
    "v_mfma_f32_16x16x32_bf16 v[28:31], a[236:239], a[160:163], v[28:31]\n"
    "buffer_load_dwordx4 a[228:231], v240, s[20:23], 0 offen offset:1024 sc1\n"
    "v_mfma_f32_16x16x32_bf16 v[60:63], a[236:239], a[164:167], v[60:63]\n"
    "ds_read_b128 a[128:131], v241 offset:16384\n"
    "v_mfma_f32_16x16x32_bf16 v[60:63], a[232:235], a[132:135], v[60:63]\n"
    "ds_read_b128 a[160:163], v241 offset:17408\n"
    "v_mfma_f32_16x16x32_bf16 v[92:95], a[232:235], a[136:139], v[92:95]\n"
    "ds_read_b128 a[164:167], v241 offset:19456\n"
    "v_mfma_f32_16x16x32_bf16 v[92:95], a[236:239], a[168:171], v[92:95]\n"
    "ds_read_b128 a[132:135], v241 offset:18432\n"
    "v_mfma_f32_16x16x32_bf16 v[124:127], a[236:239], a[172:175], v[124:127]\n"
    "ds_read_b128 a[136:139], v241 offset:20480\n"
    "v_mfma_f32_16x16x32_bf16 v[124:127], a[232:235], a[140:143], v[124:127]\n"
    "ds_read_b128 a[168:171], v241 offset:21504\n"
    "v_mfma_f32_16x16x32_bf16 v[156:159], a[232:235], a[144:147], v[156:159]\n"
    "ds_read_b128 a[172:175], v241 offset:23552\n"
    "v_mfma_f32_16x16x32_bf16 v[156:159], a[236:239], a[176:179], v[156:159]\n"
    "ds_read_b128 a[140:143], v241 offset:22528\n"
    "v_mfma_f32_16x16x32_bf16 v[188:191], a[236:239], a[180:183], v[188:191]\n"
    "ds_read_b128 a[144:147], v241 offset:24576\n"
    "v_mfma_f32_16x16x32_bf16 v[188:191], a[232:235], a[148:151], v[188:191]\n"
    "ds_read_b128 a[176:179], v241 offset:25600\n"
    "v_mfma_f32_16x16x32_bf16 v[220:223], a[232:235], a[152:155], v[220:223]\n"
    "ds_read_b128 a[180:183], v241 offset:27648\n"
    "v_mfma_f32_16x16x32_bf16 v[220:223], a[236:239], a[184:187], v[220:223]\n"
    "ds_read_b128 a[148:151], v241 offset:26624\n"
    "v_mfma_f32_16x16x32_bf16 a[252:255], a[236:239], a[188:191], a[252:255]\n"
    "ds_read_b128 a[152:155], v241 offset:28672\n"
    "v_mfma_f32_16x16x32_bf16 a[252:255], a[232:235], a[156:159], a[252:255]\n"
    "s_waitcnt vmcnt(12) lgkmcnt(11)\n"
    "ds_read_b128 a[184:187], v241 offset:29696\n"
    "v_mfma_f32_16x16x32_bf16 v[0:3], a[192:195], a[128:131], v[0:3]\n"
    "buffer_load_dwordx4 a[232:235], v240, s[20:23], 0 offen offset:2048 sc1\n"
    "v_mfma_f32_16x16x32_bf16 v[0:3], a[196:199], a[160:163], v[0:3]\n"
    "s_waitcnt lgkmcnt(10)\n"
    "buffer_load_dwordx4 a[236:239], v240, s[20:23], 0 offen offset:3072 sc1\n"
    "v_mfma_f32_16x16x32_bf16 v[32:35], a[196:199], a[164:167], v[32:35]\n"
    "s_addk_i32 s20, 4096\n"
    "ds_read_b128 a[188:191], v241 offset:31744\n"
    "v_mfma_f32_16x16x32_bf16 v[32:35], a[192:195], a[132:135], v[32:35]\n"
    "s_waitcnt lgkmcnt(9)\n"
    "ds_read_b128 a[156:159], v241 offset:30720\n"
    "v_mfma_f32_16x16x32_bf16 v[64:67], a[192:195], a[136:139], v[64:67]\n"
    "v_mfma_f32_16x16x32_bf16 v[64:67], a[196:199], a[168:171], v[64:67]\n"
    "s_waitcnt lgkmcnt(8)\n"
    "v_mfma_f32_16x16x32_bf16 v[96:99], a[196:199], a[172:175], v[96:99]\n"
    "v_mfma_f32_16x16x32_bf16 v[96:99], a[192:195], a[140:143], v[96:99]\n"
    "s_waitcnt lgkmcnt(6)\n"
    "v_mfma_f32_16x16x32_bf16 v[128:131], a[192:195], a[144:147], v[128:131]\n"
    "v_mfma_f32_16x16x32_bf16 v[128:131], a[196:199], a[176:179], v[128:131]\n"
    "s_waitcnt lgkmcnt(4)\n"
    "v_mfma_f32_16x16x32_bf16 v[160:163], a[196:199], a[180:183], v[160:163]\n"
    "v_mfma_f32_16x16x32_bf16 v[160:163], a[192:195], a[148:151], v[160:163]\n"
    "s_waitcnt lgkmcnt(2)\n"
    "v_mfma_f32_16x16x32_bf16 v[192:195], a[192:195], a[152:155], v[192:195]\n"
    "v_mfma_f32_16x16x32_bf16 v[192:195], a[196:199], a[184:187], v[192:195]\n"
    "s_waitcnt lgkmcnt(0)\n"
    "v_mfma_f32_16x16x32_bf16 v[224:227], a[196:199], a[188:191], v[224:227]\n"
    "v_mfma_f32_16x16x32_bf16 v[224:227], a[192:195], a[156:159], v[224:227]\n"
    "s_waitcnt vmcnt(11)\n"
    "buffer_load_dwordx4 a[192:195], v240, s[20:23], 0 offen offset:0 sc1\n"
    "v_mfma_f32_16x16x32_bf16 v[228:231], a[200:203], a[156:159], v[228:231]\n"
    "buffer_load_dwordx4 a[196:199], v240, s[20:23], 0 offen offset:1024 sc1\n"
    "v_mfma_f32_16x16x32_bf16 v[228:231], a[204:207], a[188:191], v[228:231]\n"
    "v_mfma_f32_16x16x32_bf16 v[196:199], a[204:207], a[184:187], v[196:199]\n"
    "v_mfma_f32_16x16x32_bf16 v[196:199], a[200:203], a[152:155], v[196:199]\n"
    "v_mfma_f32_16x16x32_bf16 v[164:167], a[200:203], a[148:151], v[164:167]\n"
    "v_mfma_f32_16x16x32_bf16 v[164:167], a[204:207], a[180:183], v[164:167]\n"
    "v_mfma_f32_16x16x32_bf16 v[132:135], a[204:207], a[176:179], v[132:135]\n"
    "v_mfma_f32_16x16x32_bf16 v[132:135], a[200:203], a[144:147], v[132:135]\n"
    "v_mfma_f32_16x16x32_bf16 v[100:103], a[200:203], a[140:143], v[100:103]\n"
    "buffer_load_dword v253, v240, s[36:39], s55 offen offset:0 sc1\n"
    "v_mfma_f32_16x16x32_bf16 v[100:103], a[204:207], a[172:175], v[100:103]\n"
    "buffer_load_dword v252, v240, s[36:39], s55 offen offset:1024 sc1\n"
    "v_mfma_f32_16x16x32_bf16 v[68:71], a[204:207], a[168:171], v[68:71]\n"
    "v_mfma_f32_16x16x32_bf16 v[68:71], a[200:203], a[136:139], v[68:71]\n"
    "v_mfma_f32_16x16x32_bf16 v[36:39], a[200:203], a[132:135], v[36:39]\n"
    "v_mfma_f32_16x16x32_bf16 v[36:39], a[204:207], a[164:167], v[36:39]\n"
    "v_mfma_f32_16x16x32_bf16 v[4:7], a[204:207], a[160:163], v[4:7]\n"
    "v_mfma_f32_16x16x32_bf16 v[4:7], a[200:203], a[128:131], v[4:7]\n"
    "s_waitcnt vmcnt(12)\n"
    "buffer_load_dwordx4 a[200:203], v240, s[20:23], 0 offen offset:2048 sc1\n"
    "v_mfma_f32_16x16x32_bf16 v[8:11], a[208:211], a[128:131], v[8:11]\n"
    "buffer_load_dwordx4 a[204:207], v240, s[20:23], 0 offen offset:3072 sc1\n"
    "s_addk_i32 s20, 4096\n"
    "v_mfma_f32_16x16x32_bf16 v[8:11], a[212:215], a[160:163], v[8:11]\n"
    "v_mfma_f32_16x16x32_bf16 v[40:43], a[212:215], a[164:167], v[40:43]\n"
    "v_mfma_f32_16x16x32_bf16 v[40:43], a[208:211], a[132:135], v[40:43]\n"
    "v_mfma_f32_16x16x32_bf16 v[72:75], a[208:211], a[136:139], v[72:75]\n"
    "v_mfma_f32_16x16x32_bf16 v[72:75], a[212:215], a[168:171], v[72:75]\n"
    "v_mfma_f32_16x16x32_bf16 v[104:107], a[212:215], a[172:175], v[104:107]\n"
    "v_mfma_f32_16x16x32_bf16 v[104:107], a[208:211], a[140:143], v[104:107]\n"
    "v_mfma_f32_16x16x32_bf16 v[136:139], a[208:211], a[144:147], v[136:139]\n"
    "v_mfma_f32_16x16x32_bf16 v[136:139], a[212:215], a[176:179], v[136:139]\n"
    "v_mfma_f32_16x16x32_bf16 v[168:171], a[212:215], a[180:183], v[168:171]\n"
    "v_mfma_f32_16x16x32_bf16 v[168:171], a[208:211], a[148:151], v[168:171]\n"
    "v_mfma_f32_16x16x32_bf16 v[200:203], a[208:211], a[152:155], v[200:203]\n"
    "v_mfma_f32_16x16x32_bf16 v[200:203], a[212:215], a[184:187], v[200:203]\n"
    "v_mfma_f32_16x16x32_bf16 v[232:235], a[212:215], a[188:191], v[232:235]\n"
    "v_mfma_f32_16x16x32_bf16 v[232:235], a[208:211], a[156:159], v[232:235]\n"
    "s_waitcnt vmcnt(11)\n"
    "buffer_load_dwordx4 a[208:211], v240, s[20:23], 0 offen offset:0 sc1\n"
    "v_mfma_f32_16x16x32_bf16 v[236:239], a[216:219], a[156:159], v[236:239]\n"
    "buffer_load_dwordx4 a[212:215], v240, s[20:23], 0 offen offset:1024 sc1\n"
    "v_mfma_f32_16x16x32_bf16 v[236:239], a[220:223], a[188:191], v[236:239]\n"
    "v_mfma_f32_16x16x32_bf16 v[204:207], a[220:223], a[184:187], v[204:207]\n"
    "v_mfma_f32_16x16x32_bf16 v[204:207], a[216:219], a[152:155], v[204:207]\n"
    "v_mfma_f32_16x16x32_bf16 v[172:175], a[216:219], a[148:151], v[172:175]\n"
    "v_mfma_f32_16x16x32_bf16 v[172:175], a[220:223], a[180:183], v[172:175]\n"
    "v_mfma_f32_16x16x32_bf16 v[140:143], a[220:223], a[176:179], v[140:143]\n"
    "v_mfma_f32_16x16x32_bf16 v[140:143], a[216:219], a[144:147], v[140:143]\n"
    "v_mfma_f32_16x16x32_bf16 v[108:111], a[216:219], a[140:143], v[108:111]\n"
    "v_mfma_f32_16x16x32_bf16 v[108:111], a[220:223], a[172:175], v[108:111]\n"
    "v_mfma_f32_16x16x32_bf16 v[76:79], a[220:223], a[168:171], v[76:79]\n"
    "v_mfma_f32_16x16x32_bf16 v[76:79], a[216:219], a[136:139], v[76:79]\n"
    "v_mfma_f32_16x16x32_bf16 v[44:47], a[216:219], a[132:135], v[44:47]\n"
    "v_mfma_f32_16x16x32_bf16 v[44:47], a[220:223], a[164:167], v[44:47]\n"
    "v_mfma_f32_16x16x32_bf16 v[12:15], a[220:223], a[160:163], v[12:15]\n"
    "v_mfma_f32_16x16x32_bf16 v[12:15], a[216:219], a[128:131], v[12:15]\n"
    "s_waitcnt vmcnt(10)\n"
    "buffer_load_dwordx4 a[216:219], v240, s[20:23], 0 offen offset:2048 sc1\n"
    "v_mfma_f32_16x16x32_bf16 v[16:19], a[224:227], a[128:131], v[16:19]\n"
    "buffer_load_dwordx4 a[220:223], v240, s[20:23], 0 offen offset:3072 sc1\n"
    "s_addk_i32 s20, 4096\n"
    "v_mfma_f32_16x16x32_bf16 v[16:19], a[228:231], a[160:163], v[16:19]\n"
    "v_mfma_f32_16x16x32_bf16 v[48:51], a[228:231], a[164:167], v[48:51]\n"
    "v_mfma_f32_16x16x32_bf16 v[48:51], a[224:227], a[132:135], v[48:51]\n"
    "v_mfma_f32_16x16x32_bf16 v[80:83], a[224:227], a[136:139], v[80:83]\n"
    "v_mfma_f32_16x16x32_bf16 v[80:83], a[228:231], a[168:171], v[80:83]\n"
    "v_mfma_f32_16x16x32_bf16 v[112:115], a[228:231], a[172:175], v[112:115]\n"
    "v_mfma_f32_16x16x32_bf16 v[112:115], a[224:227], a[140:143], v[112:115]\n"
    "v_mfma_f32_16x16x32_bf16 v[144:147], a[224:227], a[144:147], v[144:147]\n"
    "v_mfma_f32_16x16x32_bf16 v[144:147], a[228:231], a[176:179], v[144:147]\n"
    "v_mfma_f32_16x16x32_bf16 v[176:179], a[228:231], a[180:183], v[176:179]\n"
    "v_mfma_f32_16x16x32_bf16 v[176:179], a[224:227], a[148:151], v[176:179]\n"
    "v_mfma_f32_16x16x32_bf16 v[208:211], a[224:227], a[152:155], v[208:211]\n"
    "v_mfma_f32_16x16x32_bf16 v[208:211], a[228:231], a[184:187], v[208:211]\n"
    "v_mfma_f32_16x16x32_bf16 a[240:243], a[228:231], a[188:191], a[240:243]\n"
    "v_mfma_f32_16x16x32_bf16 a[240:243], a[224:227], a[156:159], a[240:243]\n"
    "s_waitcnt vmcnt(10)\n"
    "buffer_load_dwordx4 a[224:227], v240, s[20:23], 0 offen offset:0 sc1\n"
    "v_mfma_f32_16x16x32_bf16 a[244:247], a[232:235], a[156:159], a[244:247]\n"
    "buffer_load_dwordx4 a[228:231], v240, s[20:23], 0 offen offset:1024 sc1\n"
    "v_mfma_f32_16x16x32_bf16 a[244:247], a[236:239], a[188:191], a[244:247]\n"
    "v_mfma_f32_16x16x32_bf16 v[212:215], a[236:239], a[184:187], v[212:215]\n"
    "v_mfma_f32_16x16x32_bf16 v[212:215], a[232:235], a[152:155], v[212:215]\n"
    "v_mfma_f32_16x16x32_bf16 v[180:183], a[232:235], a[148:151], v[180:183]\n"
    "v_mfma_f32_16x16x32_bf16 v[180:183], a[236:239], a[180:183], v[180:183]\n"
    "v_mfma_f32_16x16x32_bf16 v[148:151], a[236:239], a[176:179], v[148:151]\n"
    "v_mfma_f32_16x16x32_bf16 v[148:151], a[232:235], a[144:147], v[148:151]\n"
    "v_mfma_f32_16x16x32_bf16 v[116:119], a[232:235], a[140:143], v[116:119]\n"
    "v_mfma_f32_16x16x32_bf16 v[116:119], a[236:239], a[172:175], v[116:119]\n"
    "v_mfma_f32_16x16x32_bf16 v[84:87], a[236:239], a[168:171], v[84:87]\n"
    "v_mfma_f32_16x16x32_bf16 v[84:87], a[232:235], a[136:139], v[84:87]\n"
    "v_mfma_f32_16x16x32_bf16 v[52:55], a[232:235], a[132:135], v[52:55]\n"
    "v_mfma_f32_16x16x32_bf16 v[52:55], a[236:239], a[164:167], v[52:55]\n"
    "v_mfma_f32_16x16x32_bf16 v[20:23], a[236:239], a[160:163], v[20:23]\n"
    "v_mfma_f32_16x16x32_bf16 v[20:23], a[232:235], a[128:131], v[20:23]\n"
    "s_waitcnt vmcnt(10)\n"
    "buffer_load_dwordx4 a[232:235], v240, s[20:23], 0 offen offset:2048 sc1\n"
    "v_mfma_f32_16x16x32_bf16 v[24:27], a[192:195], a[128:131], v[24:27]\n"
    "buffer_load_dwordx4 a[236:239], v240, s[20:23], 0 offen offset:3072 sc1\n"
    "s_addk_i32 s20, 4096\n"
    "v_mfma_f32_16x16x32_bf16 v[24:27], a[196:199], a[160:163], v[24:27]\n"
    "v_mfma_f32_16x16x32_bf16 v[56:59], a[196:199], a[164:167], v[56:59]\n"
    "v_mfma_f32_16x16x32_bf16 v[56:59], a[192:195], a[132:135], v[56:59]\n"
    "v_mfma_f32_16x16x32_bf16 v[88:91], a[192:195], a[136:139], v[88:91]\n"
    "v_mfma_f32_16x16x32_bf16 v[88:91], a[196:199], a[168:171], v[88:91]\n"
    "v_mfma_f32_16x16x32_bf16 v[120:123], a[196:199], a[172:175], v[120:123]\n"
    "v_mfma_f32_16x16x32_bf16 v[120:123], a[192:195], a[140:143], v[120:123]\n"
    "v_mfma_f32_16x16x32_bf16 v[152:155], a[192:195], a[144:147], v[152:155]\n"
    "v_mfma_f32_16x16x32_bf16 v[152:155], a[196:199], a[176:179], v[152:155]\n"
    "v_mfma_f32_16x16x32_bf16 v[184:187], a[196:199], a[180:183], v[184:187]\n"
    "v_mfma_f32_16x16x32_bf16 v[184:187], a[192:195], a[148:151], v[184:187]\n"
    "v_mfma_f32_16x16x32_bf16 v[216:219], a[192:195], a[152:155], v[216:219]\n"
    "v_mfma_f32_16x16x32_bf16 v[216:219], a[196:199], a[184:187], v[216:219]\n"
    "s_barrier\n"
    "v_mfma_f32_16x16x32_bf16 a[248:251], a[196:199], a[188:191], a[248:251]\n"
    "v_mfma_f32_16x16x32_bf16 a[248:251], a[192:195], a[156:159], a[248:251]\n"
    "s_waitcnt vmcnt(8)\n"
    "buffer_load_dwordx4 a[192:195], v240, s[20:23], 0 offen offset:0 sc1\n"
    "v_mfma_f32_16x16x32_bf16 a[252:255], a[200:203], a[156:159], a[252:255]\n"
    "buffer_load_dwordx4 a[196:199], v240, s[20:23], 0 offen offset:1024 sc1\n"
    "v_mfma_f32_16x16x32_bf16 a[252:255], a[204:207], a[188:191], a[252:255]\n"
    "v_mfma_f32_16x16x32_bf16 v[220:223], a[204:207], a[184:187], v[220:223]\n"
    "v_add_u32 v241, s66, v240\n"
    "v_mfma_f32_16x16x32_bf16 v[220:223], a[200:203], a[152:155], v[220:223]\n"
    "ds_read_b128 a[156:159], v241 offset:14336\n"
    "v_mfma_f32_16x16x32_bf16 v[188:191], a[200:203], a[148:151], v[188:191]\n"
    "ds_read_b128 a[188:191], v241 offset:15360\n"
    "v_mfma_f32_16x16x32_bf16 v[188:191], a[204:207], a[180:183], v[188:191]\n"
    "ds_read_b128 a[184:187], v241 offset:13312\n"
    "v_mfma_f32_16x16x32_bf16 v[156:159], a[204:207], a[176:179], v[156:159]\n"
    "ds_read_b128 a[152:155], v241 offset:12288\n"
    "v_mfma_f32_16x16x32_bf16 v[156:159], a[200:203], a[144:147], v[156:159]\n"
    "ds_read_b128 a[148:151], v241 offset:10240\n"
    "v_mfma_f32_16x16x32_bf16 v[124:127], a[200:203], a[140:143], v[124:127]\n"
    "ds_read_b128 a[180:183], v241 offset:11264\n"
    "v_mfma_f32_16x16x32_bf16 v[124:127], a[204:207], a[172:175], v[124:127]\n"
    "ds_read_b128 a[176:179], v241 offset:9216\n"
    "v_mfma_f32_16x16x32_bf16 v[92:95], a[204:207], a[168:171], v[92:95]\n"
    "ds_read_b128 a[144:147], v241 offset:8192\n"
    "v_mfma_f32_16x16x32_bf16 v[92:95], a[200:203], a[136:139], v[92:95]\n"
    "ds_read_b128 a[140:143], v241 offset:6144\n"
    "v_mfma_f32_16x16x32_bf16 v[60:63], a[200:203], a[132:135], v[60:63]\n"
    "ds_read_b128 a[172:175], v241 offset:7168\n"
    "v_mfma_f32_16x16x32_bf16 v[60:63], a[204:207], a[164:167], v[60:63]\n"
    "ds_read_b128 a[168:171], v241 offset:5120\n"
    "v_mfma_f32_16x16x32_bf16 v[28:31], a[204:207], a[160:163], v[28:31]\n"
    "ds_read_b128 a[136:139], v241 offset:4096\n"
    "v_mfma_f32_16x16x32_bf16 v[28:31], a[200:203], a[128:131], v[28:31]\n"
    "ds_read_b128 a[132:135], v241 offset:2048\n"
    "ds_read_b128 a[164:167], v241 offset:3072\n"
    "ds_read_b128 a[160:163], v241 offset:1024\n"
    "ds_read_b128 a[128:131], v241 offset:0\n"
    "buffer_load_dwordx4 a[200:203], v240, s[20:23], 0 offen offset:2048 sc1\n"
    "buffer_load_dwordx4 a[204:207], v240, s[20:23], 0 offen offset:3072 sc1\n"
    "s_addk_i32 s20, 4096\n"
    "s_bitcmp1_b32 0x404, s52\n"
    "s_cbranch_scc1 OD_%=\n"
    "OE_%=:\n"
    "s_waitcnt vmcnt(10) lgkmcnt(14)\n"
    "v_mfma_f32_16x16x32_bf16 v[224:227], a[208:211], a[156:159], v[224:227]\n"
    "v_mfma_f32_16x16x32_bf16 v[224:227], a[212:215], a[188:191], v[224:227]\n"
    "s_waitcnt lgkmcnt(12)\n"
    "v_mfma_f32_16x16x32_bf16 v[192:195], a[212:215], a[184:187], v[192:195]\n"
    "v_mfma_f32_16x16x32_bf16 v[192:195], a[208:211], a[152:155], v[192:195]\n"
    "s_waitcnt lgkmcnt(10)\n"
    "v_mfma_f32_16x16x32_bf16 v[160:163], a[208:211], a[148:151], v[160:163]\n"
    "v_mfma_f32_16x16x32_bf16 v[160:163], a[212:215], a[180:183], v[160:163]\n"
    "s_waitcnt lgkmcnt(8)\n"
    "v_mfma_f32_16x16x32_bf16 v[128:131], a[212:215], a[176:179], v[128:131]\n"
    "s_mov_b32 m0, s56\n"
    "s_xor_b32 s92, s92, 0x195800\n"
    "v_mfma_f32_16x16x32_bf16 v[128:131], a[208:211], a[144:147], v[128:131]\n"
    "s_waitcnt lgkmcnt(6)\n"
    "buffer_load_dwordx4 v240, s[92:95], 0 offen sc1 lds\n"
    "v_mfma_f32_16x16x32_bf16 v[96:99], a[208:211], a[140:143], v[96:99]\n"
    "v_mfma_f32_16x16x32_bf16 v[96:99], a[212:215], a[172:175], v[96:99]\n"
    "s_waitcnt lgkmcnt(4)\n"
    "v_mfma_f32_16x16x32_bf16 v[64:67], a[212:215], a[168:171], v[64:67]\n"
    "v_mfma_f32_16x16x32_bf16 v[64:67], a[208:211], a[136:139], v[64:67]\n"
    "s_waitcnt lgkmcnt(2)\n"
    "v_mfma_f32_16x16x32_bf16 v[32:35], a[208:211], a[132:135], v[32:35]\n"
    "v_mfma_f32_16x16x32_bf16 v[32:35], a[212:215], a[164:167], v[32:35]\n"
    "s_waitcnt lgkmcnt(0)\n"
    "v_mfma_f32_16x16x32_bf16 v[0:3], a[212:215], a[160:163], v[0:3]\n"
    "v_mfma_f32_16x16x32_bf16 v[0:3], a[208:211], a[128:131], v[0:3]\n"
    "s_waitcnt vmcnt(9)\n"
    "buffer_load_dwordx4 a[208:211], v240, s[20:23], 0 offen offset:0 sc1\n"
    "v_mfma_f32_16x16x32_bf16 v[4:7], a[216:219], a[128:131], v[4:7]\n"
    "buffer_load_dwordx4 a[212:215], v240, s[20:23], 0 offen offset:1024 sc1\n"
    "v_mfma_f32_16x16x32_bf16 v[4:7], a[220:223], a[160:163], v[4:7]\n"
    "v_mfma_f32_16x16x32_bf16 v[36:39], a[220:223], a[164:167], v[36:39]\n"
    "v_mfma_f32_16x16x32_bf16 v[36:39], a[216:219], a[132:135], v[36:39]\n"
    "v_mfma_f32_16x16x32_bf16 v[68:71], a[216:219], a[136:139], v[68:71]\n"
    "v_mfma_f32_16x16x32_bf16 v[68:71], a[220:223], a[168:171], v[68:71]\n"
    "v_mfma_f32_16x16x32_bf16 v[100:103], a[220:223], a[172:175], v[100:103]\n"
    "v_mfma_f32_16x16x32_bf16 v[100:103], a[216:219], a[140:143], v[100:103]\n"
    "v_mfma_f32_16x16x32_bf16 v[132:135], a[216:219], a[144:147], v[132:135]\n"
    "v_mfma_f32_16x16x32_bf16 v[132:135], a[220:223], a[176:179], v[132:135]\n"
    "v_mfma_f32_16x16x32_bf16 v[164:167], a[220:223], a[180:183], v[164:167]\n"
    "v_mfma_f32_16x16x32_bf16 v[164:167], a[216:219], a[148:151], v[164:167]\n"
    "v_mfma_f32_16x16x32_bf16 v[196:199], a[216:219], a[152:155], v[196:199]\n"
    "v_mfma_f32_16x16x32_bf16 v[196:199], a[220:223], a[184:187], v[196:199]\n"
    "v_mfma_f32_16x16x32_bf16 v[228:231], a[220:223], a[188:191], v[228:231]\n"
    "s_addk_i32 m0, 2048\n"
    "s_xor_b32 s92, s92, 0x800\n"
    "v_mfma_f32_16x16x32_bf16 v[228:231], a[216:219], a[156:159], v[228:231]\n"
    "s_waitcnt vmcnt(9)\n"
    "buffer_load_dwordx4 v240, s[92:95], 0 offen sc1 lds\n"
    "v_mfma_f32_16x16x32_bf16 v[232:235], a[224:227], a[156:159], v[232:235]\n"
    "buffer_load_dwordx4 a[216:219], v240, s[20:23], 0 offen offset:2048 sc1\n"
    "v_mfma_f32_16x16x32_bf16 v[232:235], a[228:231], a[188:191], v[232:235]\n"
    "buffer_load_dwordx4 a[220:223], v240, s[20:23], 0 offen offset:3072 sc1\n"
    "s_addk_i32 s20, 4096\n"
    "v_mfma_f32_16x16x32_bf16 v[200:203], a[228:231], a[184:187], v[200:203]\n"
    "v_mfma_f32_16x16x32_bf16 v[200:203], a[224:227], a[152:155], v[200:203]\n"
    "v_mfma_f32_16x16x32_bf16 v[168:171], a[224:227], a[148:151], v[168:171]\n"
    "v_mfma_f32_16x16x32_bf16 v[168:171], a[228:231], a[180:183], v[168:171]\n"
    "v_mfma_f32_16x16x32_bf16 v[136:139], a[228:231], a[176:179], v[136:139]\n"
    "v_mfma_f32_16x16x32_bf16 v[136:139], a[224:227], a[144:147], v[136:139]\n"
    "v_mfma_f32_16x16x32_bf16 v[104:107], a[224:227], a[140:143], v[104:107]\n"
    "v_mfma_f32_16x16x32_bf16 v[104:107], a[228:231], a[172:175], v[104:107]\n"
    "v_mfma_f32_16x16x32_bf16 v[72:75], a[228:231], a[168:171], v[72:75]\n"
    "v_mfma_f32_16x16x32_bf16 v[72:75], a[224:227], a[136:139], v[72:75]\n"
    "v_mfma_f32_16x16x32_bf16 v[40:43], a[224:227], a[132:135], v[40:43]\n"
    "v_mfma_f32_16x16x32_bf16 v[40:43], a[228:231], a[164:167], v[40:43]\n"
    "v_mfma_f32_16x16x32_bf16 v[8:11], a[228:231], a[160:163], v[8:11]\n"
    "v_mfma_f32_16x16x32_bf16 v[8:11], a[224:227], a[128:131], v[8:11]\n"
    "s_waitcnt vmcnt(10)\n"
    "buffer_load_dwordx4 a[224:227], v240, s[20:23], 0 offen offset:0 sc1\n"
    "v_mfma_f32_16x16x32_bf16 v[12:15], a[232:235], a[128:131], v[12:15]\n"
    "buffer_load_dwordx4 a[228:231], v240, s[20:23], 0 offen offset:1024 sc1\n"
    "v_mfma_f32_16x16x32_bf16 v[12:15], a[236:239], a[160:163], v[12:15]\n"
    "v_mfma_f32_16x16x32_bf16 v[44:47], a[236:239], a[164:167], v[44:47]\n"
    "v_mfma_f32_16x16x32_bf16 v[44:47], a[232:235], a[132:135], v[44:47]\n"
    "v_mfma_f32_16x16x32_bf16 v[76:79], a[232:235], a[136:139], v[76:79]\n"
    "v_mfma_f32_16x16x32_bf16 v[76:79], a[236:239], a[168:171], v[76:79]\n"
    "v_mfma_f32_16x16x32_bf16 v[108:111], a[236:239], a[172:175], v[108:111]\n"
    "s_addk_i32 m0, 2048\n"
    "s_xor_b32 s92, s92, 0x101800\n"
    "v_mfma_f32_16x16x32_bf16 v[108:111], a[232:235], a[140:143], v[108:111]\n"
    "buffer_load_dwordx4 v240, s[92:95], 0 offen sc1 lds\n"
    "v_mfma_f32_16x16x32_bf16 v[140:143], a[232:235], a[144:147], v[140:143]\n"
    "v_mfma_f32_16x16x32_bf16 v[140:143], a[236:239], a[176:179], v[140:143]\n"
    "v_mfma_f32_16x16x32_bf16 v[172:175], a[236:239], a[180:183], v[172:175]\n"
    "v_mfma_f32_16x16x32_bf16 v[172:175], a[232:235], a[148:151], v[172:175]\n"
    "v_mfma_f32_16x16x32_bf16 v[204:207], a[232:235], a[152:155], v[204:207]\n"
    "v_mfma_f32_16x16x32_bf16 v[204:207], a[236:239], a[184:187], v[204:207]\n"
    "v_mfma_f32_16x16x32_bf16 v[236:239], a[236:239], a[188:191], v[236:239]\n"
    "v_mfma_f32_16x16x32_bf16 v[236:239], a[232:235], a[156:159], v[236:239]\n"
    "s_waitcnt vmcnt(11)\n"
    "buffer_load_dwordx4 a[232:235], v240, s[20:23], 0 offen offset:2048 sc1\n"
    "v_mfma_f32_16x16x32_bf16 a[240:243], a[192:195], a[156:159], a[240:243]\n"
    "buffer_load_dwordx4 a[236:239], v240, s[20:23], 0 offen offset:3072 sc1\n"
    "s_addk_i32 s20, 4096\n"
    "v_mfma_f32_16x16x32_bf16 a[240:243], a[196:199], a[188:191], a[240:243]\n"
    "v_mfma_f32_16x16x32_bf16 v[208:211], a[196:199], a[184:187], v[208:211]\n"
    "v_mfma_f32_16x16x32_bf16 v[208:211], a[192:195], a[152:155], v[208:211]\n"
    "v_mfma_f32_16x16x32_bf16 v[176:179], a[192:195], a[148:151], v[176:179]\n"
    "v_mfma_f32_16x16x32_bf16 v[176:179], a[196:199], a[180:183], v[176:179]\n"
    "v_mfma_f32_16x16x32_bf16 v[144:147], a[196:199], a[176:179], v[144:147]\n"
    "v_mfma_f32_16x16x32_bf16 v[144:147], a[192:195], a[144:147], v[144:147]\n"
    "v_mfma_f32_16x16x32_bf16 v[112:115], a[192:195], a[140:143], v[112:115]\n"
    "v_mfma_f32_16x16x32_bf16 v[112:115], a[196:199], a[172:175], v[112:115]\n"
    "v_mfma_f32_16x16x32_bf16 v[80:83], a[196:199], a[168:171], v[80:83]\n"
    "v_mfma_f32_16x16x32_bf16 v[80:83], a[192:195], a[136:139], v[80:83]\n"
    "v_mfma_f32_16x16x32_bf16 v[48:51], a[192:195], a[132:135], v[48:51]\n"
    "v_mfma_f32_16x16x32_bf16 v[48:51], a[196:199], a[164:167], v[48:51]\n"
    "v_mfma_f32_16x16x32_bf16 v[16:19], a[196:199], a[160:163], v[16:19]\n"
    "s_addk_i32 m0, 2048\n"
    "s_xor_b32 s92, s92, 0x800\n"
    "v_mfma_f32_16x16x32_bf16 v[16:19], a[192:195], a[128:131], v[16:19]\n"
    "s_waitcnt vmcnt(11)\n"
    "buffer_load_dwordx4 v240, s[92:95], 0 offen sc1 lds\n"
    "v_mfma_f32_16x16x32_bf16 v[20:23], a[200:203], a[128:131], v[20:23]\n"
    "buffer_load_dwordx4 a[192:195], v240, s[20:23], 0 offen offset:0 sc1\n"
    "v_mfma_f32_16x16x32_bf16 v[20:23], a[204:207], a[160:163], v[20:23]\n"
    "buffer_load_dwordx4 a[196:199], v240, s[20:23], 0 offen offset:1024 sc1\n"
    "v_mfma_f32_16x16x32_bf16 v[52:55], a[204:207], a[164:167], v[52:55]\n"
    "v_mfma_f32_16x16x32_bf16 v[52:55], a[200:203], a[132:135], v[52:55]\n"
    "v_mfma_f32_16x16x32_bf16 v[84:87], a[200:203], a[136:139], v[84:87]\n"
    "v_mfma_f32_16x16x32_bf16 v[84:87], a[204:207], a[168:171], v[84:87]\n"
    "v_mfma_f32_16x16x32_bf16 v[116:119], a[204:207], a[172:175], v[116:119]\n"
    "v_mfma_f32_16x16x32_bf16 v[116:119], a[200:203], a[140:143], v[116:119]\n"
    "v_mfma_f32_16x16x32_bf16 v[148:151], a[200:203], a[144:147], v[148:151]\n"
    "v_mfma_f32_16x16x32_bf16 v[148:151], a[204:207], a[176:179], v[148:151]\n"
    "v_mfma_f32_16x16x32_bf16 v[180:183], a[204:207], a[180:183], v[180:183]\n"
    "v_mfma_f32_16x16x32_bf16 v[180:183], a[200:203], a[148:151], v[180:183]\n"
    "v_mfma_f32_16x16x32_bf16 v[212:215], a[200:203], a[152:155], v[212:215]\n"
    "v_mfma_f32_16x16x32_bf16 v[212:215], a[204:207], a[184:187], v[212:215]\n"
    "v_mfma_f32_16x16x32_bf16 a[244:247], a[204:207], a[188:191], a[244:247]\n"
    "v_mfma_f32_16x16x32_bf16 a[244:247], a[200:203], a[156:159], a[244:247]\n"
    "s_waitcnt vmcnt(11)\n"
    "buffer_load_dwordx4 a[200:203], v240, s[20:23], 0 offen offset:2048 sc1\n"
    "v_mfma_f32_16x16x32_bf16 a[248:251], a[208:211], a[156:159], a[248:251]\n"
    "buffer_load_dwordx4 a[204:207], v240, s[20:23], 0 offen offset:3072 sc1\n"
    "s_addk_i32 s20, 4096\n"
    "v_mfma_f32_16x16x32_bf16 a[248:251], a[212:215], a[188:191], a[248:251]\n"
    "v_mfma_f32_16x16x32_bf16 v[216:219], a[212:215], a[184:187], v[216:219]\n"
    "v_mfma_f32_16x16x32_bf16 v[216:219], a[208:211], a[152:155], v[216:219]\n"
    "v_mfma_f32_16x16x32_bf16 v[184:187], a[208:211], a[148:151], v[184:187]\n"
    "v_mfma_f32_16x16x32_bf16 v[184:187], a[212:215], a[180:183], v[184:187]\n"
    "v_mfma_f32_16x16x32_bf16 v[152:155], a[212:215], a[176:179], v[152:155]\n"
    "v_mfma_f32_16x16x32_bf16 v[152:155], a[208:211], a[144:147], v[152:155]\n"
    "v_mfma_f32_16x16x32_bf16 v[120:123], a[208:211], a[140:143], v[120:123]\n"
    "v_mfma_f32_16x16x32_bf16 v[120:123], a[212:215], a[172:175], v[120:123]\n"
    "v_mfma_f32_16x16x32_bf16 v[88:91], a[212:215], a[168:171], v[88:91]\n"
    "s_addk_i32 m0, 2048\n"
    "s_xor_b32 s92, s92, 0x185800\n"
    "v_mfma_f32_16x16x32_bf16 v[88:91], a[208:211], a[136:139], v[88:91]\n"
    "buffer_load_dwordx4 v240, s[92:95], 0 offen sc1 lds\n"
    "v_mfma_f32_16x16x32_bf16 v[56:59], a[208:211], a[132:135], v[56:59]\n"
    "v_mfma_f32_16x16x32_bf16 v[56:59], a[212:215], a[164:167], v[56:59]\n"
    "v_mfma_f32_16x16x32_bf16 v[24:27], a[212:215], a[160:163], v[24:27]\n"
    "v_mfma_f32_16x16x32_bf16 v[24:27], a[208:211], a[128:131], v[24:27]\n"
    "s_waitcnt vmcnt(11)\n"
    "buffer_load_dwordx4 a[208:211], v240, s[20:23], 0 offen offset:0 sc1\n"
    "v_mfma_f32_16x16x32_bf16 v[28:31], a[216:219], a[128:131], v[28:31]\n"
    "buffer_load_dwordx4 a[212:215], v240, s[20:23], 0 offen offset:1024 sc1\n"
    "v_mfma_f32_16x16x32_bf16 v[28:31], a[220:223], a[160:163], v[28:31]\n"
    "v_mfma_f32_16x16x32_bf16 v[60:63], a[220:223], a[164:167], v[60:63]\n"
    "ds_read_b128 a[128:131], v241 offset:16384\n"
    "v_mfma_f32_16x16x32_bf16 v[60:63], a[216:219], a[132:135], v[60:63]\n"
    "ds_read_b128 a[160:163], v241 offset:17408\n"
    "v_mfma_f32_16x16x32_bf16 v[92:95], a[216:219], a[136:139], v[92:95]\n"
    "ds_read_b128 a[164:167], v241 offset:19456\n"
    "v_mfma_f32_16x16x32_bf16 v[92:95], a[220:223], a[168:171], v[92:95]\n"
    "ds_read_b128 a[132:135], v241 offset:18432\n"
    "v_mfma_f32_16x16x32_bf16 v[124:127], a[220:223], a[172:175], v[124:127]\n"
    "ds_read_b128 a[136:139], v241 offset:20480\n"
    "v_mfma_f32_16x16x32_bf16 v[124:127], a[216:219], a[140:143], v[124:127]\n"
    "ds_read_b128 a[168:171], v241 offset:21504\n"
    "v_mfma_f32_16x16x32_bf16 v[156:159], a[216:219], a[144:147], v[156:159]\n"
    "ds_read_b128 a[172:175], v241 offset:23552\n"
    "v_mfma_f32_16x16x32_bf16 v[156:159], a[220:223], a[176:179], v[156:159]\n"
    "ds_read_b128 a[140:143], v241 offset:22528\n"
    "v_mfma_f32_16x16x32_bf16 v[188:191], a[220:223], a[180:183], v[188:191]\n"
    "ds_read_b128 a[144:147], v241 offset:24576\n"
    "v_mfma_f32_16x16x32_bf16 v[188:191], a[216:219], a[148:151], v[188:191]\n"
    "ds_read_b128 a[176:179], v241 offset:25600\n"
    "v_mfma_f32_16x16x32_bf16 v[220:223], a[216:219], a[152:155], v[220:223]\n"
    "ds_read_b128 a[180:183], v241 offset:27648\n"
    "v_mfma_f32_16x16x32_bf16 v[220:223], a[220:223], a[184:187], v[220:223]\n"
    "ds_read_b128 a[148:151], v241 offset:26624\n"
    "v_mfma_f32_16x16x32_bf16 a[252:255], a[220:223], a[188:191], a[252:255]\n"
    "ds_read_b128 a[152:155], v241 offset:28672\n"
    "v_mfma_f32_16x16x32_bf16 a[252:255], a[216:219], a[156:159], a[252:255]\n"
    "s_waitcnt vmcnt(11) lgkmcnt(11)\n"
    "ds_read_b128 a[184:187], v241 offset:29696\n"
    "v_mfma_f32_16x16x32_bf16 v[0:3], a[224:227], a[128:131], v[0:3]\n"
    "buffer_load_dwordx4 a[216:219], v240, s[20:23], 0 offen offset:2048 sc1\n"
    "v_mfma_f32_16x16x32_bf16 v[0:3], a[228:231], a[160:163], v[0:3]\n"
    "s_waitcnt lgkmcnt(10)\n"
    "buffer_load_dwordx4 a[220:223], v240, s[20:23], 0 offen offset:3072 sc1\n"
    "v_mfma_f32_16x16x32_bf16 v[32:35], a[228:231], a[164:167], v[32:35]\n"
    "s_addk_i32 s20, 4096\n"
    "ds_read_b128 a[188:191], v241 offset:31744\n"
    "v_mfma_f32_16x16x32_bf16 v[32:35], a[224:227], a[132:135], v[32:35]\n"
    "s_waitcnt lgkmcnt(9)\n"
    "ds_read_b128 a[156:159], v241 offset:30720\n"
    "s_addk_i32 m0, 2048\n"
    "v_mfma_f32_16x16x32_bf16 v[64:67], a[224:227], a[136:139], v[64:67]\n"
    "s_xor_b32 s92, s92, 0x800\n"
    "buffer_load_dwordx4 v240, s[92:95], 0 offen sc1 lds\n"
    "v_mfma_f32_16x16x32_bf16 v[64:67], a[228:231], a[168:171], v[64:67]\n"
    "s_waitcnt lgkmcnt(8)\n"
    "v_mfma_f32_16x16x32_bf16 v[96:99], a[228:231], a[172:175], v[96:99]\n"
    "v_mfma_f32_16x16x32_bf16 v[96:99], a[224:227], a[140:143], v[96:99]\n"
    "s_waitcnt lgkmcnt(6)\n"
    "v_mfma_f32_16x16x32_bf16 v[128:131], a[224:227], a[144:147], v[128:131]\n"
    "v_mfma_f32_16x16x32_bf16 v[128:131], a[228:231], a[176:179], v[128:131]\n"
    "s_waitcnt lgkmcnt(4)\n"
    "v_mfma_f32_16x16x32_bf16 v[160:163], a[228:231], a[180:183], v[160:163]\n"
    "v_mfma_f32_16x16x32_bf16 v[160:163], a[224:227], a[148:151], v[160:163]\n"
    "s_waitcnt lgkmcnt(2)\n"
    "v_mfma_f32_16x16x32_bf16 v[192:195], a[224:227], a[152:155], v[192:195]\n"
    "v_mfma_f32_16x16x32_bf16 v[192:195], a[228:231], a[184:187], v[192:195]\n"
    "s_waitcnt lgkmcnt(0)\n"
    "v_mfma_f32_16x16x32_bf16 v[224:227], a[228:231], a[188:191], v[224:227]\n"
    "v_mfma_f32_16x16x32_bf16 v[224:227], a[224:227], a[156:159], v[224:227]\n"
    "s_waitcnt vmcnt(11)\n"
    "buffer_load_dwordx4 a[224:227], v240, s[20:23], 0 offen offset:0 sc1\n"
    "v_mfma_f32_16x16x32_bf16 v[228:231], a[232:235], a[156:159], v[228:231]\n"
    "buffer_load_dwordx4 a[228:231], v240, s[20:23], 0 offen offset:1024 sc1\n"
    "v_mfma_f32_16x16x32_bf16 v[228:231], a[236:239], a[188:191], v[228:231]\n"
    "v_mfma_f32_16x16x32_bf16 v[196:199], a[236:239], a[184:187], v[196:199]\n"
    "v_mfma_f32_16x16x32_bf16 v[196:199], a[232:235], a[152:155], v[196:199]\n"
    "v_mfma_f32_16x16x32_bf16 v[164:167], a[232:235], a[148:151], v[164:167]\n"
    "v_mfma_f32_16x16x32_bf16 v[164:167], a[236:239], a[180:183], v[164:167]\n"
    "v_mfma_f32_16x16x32_bf16 v[132:135], a[236:239], a[176:179], v[132:135]\n"
    "v_mfma_f32_16x16x32_bf16 v[132:135], a[232:235], a[144:147], v[132:135]\n"
    "v_mfma_f32_16x16x32_bf16 v[100:103], a[232:235], a[140:143], v[100:103]\n"
    "v_mfma_f32_16x16x32_bf16 v[100:103], a[236:239], a[172:175], v[100:103]\n"
    "v_mfma_f32_16x16x32_bf16 v[68:71], a[236:239], a[168:171], v[68:71]\n"
    "s_addk_i32 m0, 2048\n"
    "s_xor_b32 s92, s92, 0x101800\n"
    "v_mfma_f32_16x16x32_bf16 v[68:71], a[232:235], a[136:139], v[68:71]\n"
    "buffer_load_dwordx4 v240, s[92:95], 0 offen sc1 lds\n"
    "v_mfma_f32_16x16x32_bf16 v[36:39], a[232:235], a[132:135], v[36:39]\n"
    "v_mfma_f32_16x16x32_bf16 v[36:39], a[236:239], a[164:167], v[36:39]\n"
    "v_mfma_f32_16x16x32_bf16 v[4:7], a[236:239], a[160:163], v[4:7]\n"
    "v_mfma_f32_16x16x32_bf16 v[4:7], a[232:235], a[128:131], v[4:7]\n"
    "s_waitcnt vmcnt(11)\n"
    "buffer_load_dwordx4 a[232:235], v240, s[20:23], 0 offen offset:2048 sc1\n"
    "v_mfma_f32_16x16x32_bf16 v[8:11], a[192:195], a[128:131], v[8:11]\n"
    "buffer_load_dwordx4 a[236:239], v240, s[20:23], 0 offen offset:3072 sc1\n"
    "s_addk_i32 s20, 4096\n"
    "v_mfma_f32_16x16x32_bf16 v[8:11], a[196:199], a[160:163], v[8:11]\n"
    "v_mfma_f32_16x16x32_bf16 v[40:43], a[196:199], a[164:167], v[40:43]\n"
    "v_mfma_f32_16x16x32_bf16 v[40:43], a[192:195], a[132:135], v[40:43]\n"
    "v_mfma_f32_16x16x32_bf16 v[72:75], a[192:195], a[136:139], v[72:75]\n"
    "v_mfma_f32_16x16x32_bf16 v[72:75], a[196:199], a[168:171], v[72:75]\n"
    "v_mfma_f32_16x16x32_bf16 v[104:107], a[196:199], a[172:175], v[104:107]\n"
    "v_mfma_f32_16x16x32_bf16 v[104:107], a[192:195], a[140:143], v[104:107]\n"
    "v_mfma_f32_16x16x32_bf16 v[136:139], a[192:195], a[144:147], v[136:139]\n"
    "v_mfma_f32_16x16x32_bf16 v[136:139], a[196:199], a[176:179], v[136:139]\n"
    "v_mfma_f32_16x16x32_bf16 v[168:171], a[196:199], a[180:183], v[168:171]\n"
    "v_mfma_f32_16x16x32_bf16 v[168:171], a[192:195], a[148:151], v[168:171]\n"
    "v_mfma_f32_16x16x32_bf16 v[200:203], a[192:195], a[152:155], v[200:203]\n"
    "v_mfma_f32_16x16x32_bf16 v[200:203], a[196:199], a[184:187], v[200:203]\n"
    "v_mfma_f32_16x16x32_bf16 v[232:235], a[196:199], a[188:191], v[232:235]\n"
    "v_mfma_f32_16x16x32_bf16 v[232:235], a[192:195], a[156:159], v[232:235]\n"
    "s_waitcnt vmcnt(11)\n"
    "buffer_load_dwordx4 a[192:195], v240, s[20:23], 0 offen offset:0 sc1\n"
    "v_mfma_f32_16x16x32_bf16 v[236:239], a[200:203], a[156:159], v[236:239]\n"
    "buffer_load_dwordx4 a[196:199], v240, s[20:23], 0 offen offset:1024 sc1\n"
    "v_mfma_f32_16x16x32_bf16 v[236:239], a[204:207], a[188:191], v[236:239]\n"
    "v_mfma_f32_16x16x32_bf16 v[204:207], a[204:207], a[184:187], v[204:207]\n"
    "s_addk_i32 m0, 2048\n"
    "s_xor_b32 s92, s92, 0x800\n"
    "v_mfma_f32_16x16x32_bf16 v[204:207], a[200:203], a[152:155], v[204:207]\n"
    "buffer_load_dwordx4 v240, s[92:95], 0 offen sc1 lds\n"
    "v_mfma_f32_16x16x32_bf16 v[172:175], a[200:203], a[148:151], v[172:175]\n"
    "v_mfma_f32_16x16x32_bf16 v[172:175], a[204:207], a[180:183], v[172:175]\n"
    "v_mfma_f32_16x16x32_bf16 v[140:143], a[204:207], a[176:179], v[140:143]\n"
    "v_mfma_f32_16x16x32_bf16 v[140:143], a[200:203], a[144:147], v[140:143]\n"
    "v_mfma_f32_16x16x32_bf16 v[108:111], a[200:203], a[140:143], v[108:111]\n"
    "v_mfma_f32_16x16x32_bf16 v[108:111], a[204:207], a[172:175], v[108:111]\n"
    "v_mfma_f32_16x16x32_bf16 v[76:79], a[204:207], a[168:171], v[76:79]\n"
    "v_mfma_f32_16x16x32_bf16 v[76:79], a[200:203], a[136:139], v[76:79]\n"
    "v_mfma_f32_16x16x32_bf16 v[44:47], a[200:203], a[132:135], v[44:47]\n"
    "v_mfma_f32_16x16x32_bf16 v[44:47], a[204:207], a[164:167], v[44:47]\n"
    "v_mfma_f32_16x16x32_bf16 v[12:15], a[204:207], a[160:163], v[12:15]\n"
    "v_mfma_f32_16x16x32_bf16 v[12:15], a[200:203], a[128:131], v[12:15]\n"
    "s_waitcnt vmcnt(11)\n"
    "buffer_load_dwordx4 a[200:203], v240, s[20:23], 0 offen offset:2048 sc1\n"
    "v_mfma_f32_16x16x32_bf16 v[16:19], a[208:211], a[128:131], v[16:19]\n"
    "buffer_load_dwordx4 a[204:207], v240, s[20:23], 0 offen offset:3072 sc1\n"
    "s_addk_i32 s20, 4096\n"
    "v_mfma_f32_16x16x32_bf16 v[16:19], a[212:215], a[160:163], v[16:19]\n"
    "v_mfma_f32_16x16x32_bf16 v[48:51], a[212:215], a[164:167], v[48:51]\n"
    "v_mfma_f32_16x16x32_bf16 v[48:51], a[208:211], a[132:135], v[48:51]\n"
    "v_mfma_f32_16x16x32_bf16 v[80:83], a[208:211], a[136:139], v[80:83]\n"
    "v_mfma_f32_16x16x32_bf16 v[80:83], a[212:215], a[168:171], v[80:83]\n"
    "v_mfma_f32_16x16x32_bf16 v[112:115], a[212:215], a[172:175], v[112:115]\n"
    "v_mfma_f32_16x16x32_bf16 v[112:115], a[208:211], a[140:143], v[112:115]\n"
    "v_mfma_f32_16x16x32_bf16 v[144:147], a[208:211], a[144:147], v[144:147]\n"
    "v_mfma_f32_16x16x32_bf16 v[144:147], a[212:215], a[176:179], v[144:147]\n"
    "v_mfma_f32_16x16x32_bf16 v[176:179], a[212:215], a[180:183], v[176:179]\n"
    "v_mfma_f32_16x16x32_bf16 v[176:179], a[208:211], a[148:151], v[176:179]\n"
    "v_mfma_f32_16x16x32_bf16 v[208:211], a[208:211], a[152:155], v[208:211]\n"
    "v_mfma_f32_16x16x32_bf16 v[208:211], a[212:215], a[184:187], v[208:211]\n"
    "v_mfma_f32_16x16x32_bf16 a[240:243], a[212:215], a[188:191], a[240:243]\n"
    "v_mfma_f32_16x16x32_bf16 a[240:243], a[208:211], a[156:159], a[240:243]\n"
    "s_waitcnt vmcnt(11)\n"
    "buffer_load_dwordx4 a[208:211], v240, s[20:23], 0 offen offset:0 sc1\n"
    "v_mfma_f32_16x16x32_bf16 a[244:247], a[216:219], a[156:159], a[244:247]\n"
    "buffer_load_dwordx4 a[212:215], v240, s[20:23], 0 offen offset:1024 sc1\n"
    "v_mfma_f32_16x16x32_bf16 a[244:247], a[220:223], a[188:191], a[244:247]\n"
    "v_mfma_f32_16x16x32_bf16 v[212:215], a[220:223], a[184:187], v[212:215]\n"
    "v_mfma_f32_16x16x32_bf16 v[212:215], a[216:219], a[152:155], v[212:215]\n"
    "v_mfma_f32_16x16x32_bf16 v[180:183], a[216:219], a[148:151], v[180:183]\n"
    "v_mfma_f32_16x16x32_bf16 v[180:183], a[220:223], a[180:183], v[180:183]\n"
    "v_mfma_f32_16x16x32_bf16 v[148:151], a[220:223], a[176:179], v[148:151]\n"
    "v_mfma_f32_16x16x32_bf16 v[148:151], a[216:219], a[144:147], v[148:151]\n"
    "v_mfma_f32_16x16x32_bf16 v[116:119], a[216:219], a[140:143], v[116:119]\n"
    "v_mfma_f32_16x16x32_bf16 v[116:119], a[220:223], a[172:175], v[116:119]\n"
    "v_mfma_f32_16x16x32_bf16 v[84:87], a[220:223], a[168:171], v[84:87]\n"
    "v_mfma_f32_16x16x32_bf16 v[84:87], a[216:219], a[136:139], v[84:87]\n"
    "v_mfma_f32_16x16x32_bf16 v[52:55], a[216:219], a[132:135], v[52:55]\n"
    "v_mfma_f32_16x16x32_bf16 v[52:55], a[220:223], a[164:167], v[52:55]\n"
    "v_mfma_f32_16x16x32_bf16 v[20:23], a[220:223], a[160:163], v[20:23]\n"
    "v_mfma_f32_16x16x32_bf16 v[20:23], a[216:219], a[128:131], v[20:23]\n"
    "s_waitcnt vmcnt(10)\n"
    "buffer_load_dwordx4 a[216:219], v240, s[20:23], 0 offen offset:2048 sc1\n"
    "v_mfma_f32_16x16x32_bf16 v[24:27], a[224:227], a[128:131], v[24:27]\n"
    "buffer_load_dwordx4 a[220:223], v240, s[20:23], 0 offen offset:3072 sc1\n"
    "s_addk_i32 s20, 4096\n"
    "v_mfma_f32_16x16x32_bf16 v[24:27], a[228:231], a[160:163], v[24:27]\n"
    "v_mfma_f32_16x16x32_bf16 v[56:59], a[228:231], a[164:167], v[56:59]\n"
    "v_mfma_f32_16x16x32_bf16 v[56:59], a[224:227], a[132:135], v[56:59]\n"
    "v_mfma_f32_16x16x32_bf16 v[88:91], a[224:227], a[136:139], v[88:91]\n"
    "v_mfma_f32_16x16x32_bf16 v[88:91], a[228:231], a[168:171], v[88:91]\n"
    "v_mfma_f32_16x16x32_bf16 v[120:123], a[228:231], a[172:175], v[120:123]\n"
    "v_mfma_f32_16x16x32_bf16 v[120:123], a[224:227], a[140:143], v[120:123]\n"
    "v_mfma_f32_16x16x32_bf16 v[152:155], a[224:227], a[144:147], v[152:155]\n"
    "v_mfma_f32_16x16x32_bf16 v[152:155], a[228:231], a[176:179], v[152:155]\n"
    "v_mfma_f32_16x16x32_bf16 v[184:187], a[228:231], a[180:183], v[184:187]\n"
    "v_mfma_f32_16x16x32_bf16 v[184:187], a[224:227], a[148:151], v[184:187]\n"
    "v_mfma_f32_16x16x32_bf16 v[216:219], a[224:227], a[152:155], v[216:219]\n"
    "v_mfma_f32_16x16x32_bf16 v[216:219], a[228:231], a[184:187], v[216:219]\n"
    "v_mfma_f32_16x16x32_bf16 a[248:251], a[228:231], a[188:191], a[248:251]\n"
    "v_mfma_f32_16x16x32_bf16 a[248:251], a[224:227], a[156:159], a[248:251]\n"
    "s_waitcnt vmcnt(9)\n"
    "buffer_load_dwordx4 a[224:227], v240, s[20:23], 0 offen offset:0 sc1\n"
    "v_mfma_f32_16x16x32_bf16 a[252:255], a[232:235], a[156:159], a[252:255]\n"
    "buffer_load_dwordx4 a[228:231], v240, s[20:23], 0 offen offset:1024 sc1\n"
    "v_mfma_f32_16x16x32_bf16 a[252:255], a[236:239], a[188:191], a[252:255]\n"
    "v_mfma_f32_16x16x32_bf16 v[220:223], a[236:239], a[184:187], v[220:223]\n"
    "ds_read_b128 a[156:159], v241 offset:47104\n"
    "v_mfma_f32_16x16x32_bf16 v[220:223], a[232:235], a[152:155], v[220:223]\n"
    "ds_read_b128 a[188:191], v241 offset:48128\n"
    "v_mfma_f32_16x16x32_bf16 v[188:191], a[232:235], a[148:151], v[188:191]\n"
    "ds_read_b128 a[184:187], v241 offset:46080\n"
    "v_mfma_f32_16x16x32_bf16 v[188:191], a[236:239], a[180:183], v[188:191]\n"
    "ds_read_b128 a[152:155], v241 offset:45056\n"
    "v_mfma_f32_16x16x32_bf16 v[156:159], a[236:239], a[176:179], v[156:159]\n"
    "ds_read_b128 a[148:151], v241 offset:43008\n"
    "v_mfma_f32_16x16x32_bf16 v[156:159], a[232:235], a[144:147], v[156:159]\n"
    "ds_read_b128 a[180:183], v241 offset:44032\n"
    "v_mfma_f32_16x16x32_bf16 v[124:127], a[232:235], a[140:143], v[124:127]\n"
    "ds_read_b128 a[176:179], v241 offset:41984\n"
    "v_mfma_f32_16x16x32_bf16 v[124:127], a[236:239], a[172:175], v[124:127]\n"
    "ds_read_b128 a[144:147], v241 offset:40960\n"
    "v_mfma_f32_16x16x32_bf16 v[92:95], a[236:239], a[168:171], v[92:95]\n"
    "ds_read_b128 a[140:143], v241 offset:38912\n"
    "v_mfma_f32_16x16x32_bf16 v[92:95], a[232:235], a[136:139], v[92:95]\n"
    "ds_read_b128 a[172:175], v241 offset:39936\n"
    "v_mfma_f32_16x16x32_bf16 v[60:63], a[232:235], a[132:135], v[60:63]\n"
    "ds_read_b128 a[168:171], v241 offset:37888\n"
    "v_mfma_f32_16x16x32_bf16 v[60:63], a[236:239], a[164:167], v[60:63]\n"
    "ds_read_b128 a[136:139], v241 offset:36864\n"
    "v_mfma_f32_16x16x32_bf16 v[28:31], a[236:239], a[160:163], v[28:31]\n"
    "ds_read_b128 a[132:135], v241 offset:34816\n"
    "v_mfma_f32_16x16x32_bf16 v[28:31], a[232:235], a[128:131], v[28:31]\n"
    "s_waitcnt vmcnt(9) lgkmcnt(11)\n"
    "ds_read_b128 a[164:167], v241 offset:35840\n"
    "v_mfma_f32_16x16x32_bf16 v[224:227], a[192:195], a[156:159], v[224:227]\n"
    "buffer_load_dwordx4 a[232:235], v240, s[20:23], 0 offen offset:2048 sc1\n"
    "v_mfma_f32_16x16x32_bf16 v[224:227], a[196:199], a[188:191], v[224:227]\n"
    "s_waitcnt lgkmcnt(10)\n"
    "buffer_load_dwordx4 a[236:239], v240, s[20:23], 0 offen offset:3072 sc1\n"
    "v_mfma_f32_16x16x32_bf16 v[192:195], a[196:199], a[184:187], v[192:195]\n"
    "s_addk_i32 s20, 4096\n"
    "ds_read_b128 a[160:163], v241 offset:33792\n"
    "v_mfma_f32_16x16x32_bf16 v[192:195], a[192:195], a[152:155], v[192:195]\n"
    "s_waitcnt lgkmcnt(9)\n"
    "ds_read_b128 a[128:131], v241 offset:32768\n"
    "v_mfma_f32_16x16x32_bf16 v[160:163], a[192:195], a[148:151], v[160:163]\n"
    "v_mfma_f32_16x16x32_bf16 v[160:163], a[196:199], a[180:183], v[160:163]\n"
    "s_waitcnt lgkmcnt(8)\n"
    "v_mfma_f32_16x16x32_bf16 v[128:131], a[196:199], a[176:179], v[128:131]\n"
    "v_mfma_f32_16x16x32_bf16 v[128:131], a[192:195], a[144:147], v[128:131]\n"
    "s_waitcnt lgkmcnt(6)\n"
    "v_mfma_f32_16x16x32_bf16 v[96:99], a[192:195], a[140:143], v[96:99]\n"
    "v_mfma_f32_16x16x32_bf16 v[96:99], a[196:199], a[172:175], v[96:99]\n"
    "s_waitcnt lgkmcnt(4)\n"
    "v_mfma_f32_16x16x32_bf16 v[64:67], a[196:199], a[168:171], v[64:67]\n"
    "v_mfma_f32_16x16x32_bf16 v[64:67], a[192:195], a[136:139], v[64:67]\n"
    "s_waitcnt lgkmcnt(2)\n"
    "v_mfma_f32_16x16x32_bf16 v[32:35], a[192:195], a[132:135], v[32:35]\n"
    "v_mfma_f32_16x16x32_bf16 v[32:35], a[196:199], a[164:167], v[32:35]\n"
    "s_waitcnt lgkmcnt(0)\n"
    "v_mfma_f32_16x16x32_bf16 v[0:3], a[196:199], a[160:163], v[0:3]\n"
    "v_mfma_f32_16x16x32_bf16 v[0:3], a[192:195], a[128:131], v[0:3]\n"
    "s_waitcnt vmcnt(8)\n"
    "buffer_load_dwordx4 a[192:195], v240, s[20:23], 0 offen offset:0 sc1\n"
    "v_mfma_f32_16x16x32_bf16 v[4:7], a[200:203], a[128:131], v[4:7]\n"
    "buffer_load_dwordx4 a[196:199], v240, s[20:23], 0 offen offset:1024 sc1\n"
    "v_mfma_f32_16x16x32_bf16 v[4:7], a[204:207], a[160:163], v[4:7]\n"
    "v_mfma_f32_16x16x32_bf16 v[36:39], a[204:207], a[164:167], v[36:39]\n"
    "v_mfma_f32_16x16x32_bf16 v[36:39], a[200:203], a[132:135], v[36:39]\n"
    "v_mfma_f32_16x16x32_bf16 v[68:71], a[200:203], a[136:139], v[68:71]\n"
    "v_mfma_f32_16x16x32_bf16 v[68:71], a[204:207], a[168:171], v[68:71]\n"
    "v_mfma_f32_16x16x32_bf16 v[100:103], a[204:207], a[172:175], v[100:103]\n"
    "v_mfma_f32_16x16x32_bf16 v[100:103], a[200:203], a[140:143], v[100:103]\n"
    "v_mfma_f32_16x16x32_bf16 v[132:135], a[200:203], a[144:147], v[132:135]\n"
    "v_mfma_f32_16x16x32_bf16 v[132:135], a[204:207], a[176:179], v[132:135]\n"
    "v_mfma_f32_16x16x32_bf16 v[164:167], a[204:207], a[180:183], v[164:167]\n"
    "v_mfma_f32_16x16x32_bf16 v[164:167], a[200:203], a[148:151], v[164:167]\n"
    "v_mfma_f32_16x16x32_bf16 v[196:199], a[200:203], a[152:155], v[196:199]\n"
    "v_mfma_f32_16x16x32_bf16 v[196:199], a[204:207], a[184:187], v[196:199]\n"
    "v_mfma_f32_16x16x32_bf16 v[228:231], a[204:207], a[188:191], v[228:231]\n"
    "v_mfma_f32_16x16x32_bf16 v[228:231], a[200:203], a[156:159], v[228:231]\n"
    "s_waitcnt vmcnt(8)\n"
    "buffer_load_dwordx4 a[200:203], v240, s[20:23], 0 offen offset:2048 sc1\n"
    "v_mfma_f32_16x16x32_bf16 v[232:235], a[208:211], a[156:159], v[232:235]\n"
    "buffer_load_dwordx4 a[204:207], v240, s[20:23], 0 offen offset:3072 sc1\n"
    "s_addk_i32 s20, 4096\n"
    "v_mfma_f32_16x16x32_bf16 v[232:235], a[212:215], a[188:191], v[232:235]\n"
    "v_mfma_f32_16x16x32_bf16 v[200:203], a[212:215], a[184:187], v[200:203]\n"
    "v_mfma_f32_16x16x32_bf16 v[200:203], a[208:211], a[152:155], v[200:203]\n"
    "v_mfma_f32_16x16x32_bf16 v[168:171], a[208:211], a[148:151], v[168:171]\n"
    "v_mfma_f32_16x16x32_bf16 v[168:171], a[212:215], a[180:183], v[168:171]\n"
    "v_mfma_f32_16x16x32_bf16 v[136:139], a[212:215], a[176:179], v[136:139]\n"
    "v_mfma_f32_16x16x32_bf16 v[136:139], a[208:211], a[144:147], v[136:139]\n"
    "v_mfma_f32_16x16x32_bf16 v[104:107], a[208:211], a[140:143], v[104:107]\n"
    "v_mfma_f32_16x16x32_bf16 v[104:107], a[212:215], a[172:175], v[104:107]\n"
    "v_mfma_f32_16x16x32_bf16 v[72:75], a[212:215], a[168:171], v[72:75]\n"
    "v_mfma_f32_16x16x32_bf16 v[72:75], a[208:211], a[136:139], v[72:75]\n"
    "v_mfma_f32_16x16x32_bf16 v[40:43], a[208:211], a[132:135], v[40:43]\n"
    "v_mfma_f32_16x16x32_bf16 v[40:43], a[212:215], a[164:167], v[40:43]\n"
    "v_mfma_f32_16x16x32_bf16 v[8:11], a[212:215], a[160:163], v[8:11]\n"
    "v_mfma_f32_16x16x32_bf16 v[8:11], a[208:211], a[128:131], v[8:11]\n"
    "s_waitcnt vmcnt(8)\n"
    "buffer_load_dwordx4 a[208:211], v240, s[20:23], 0 offen offset:0 sc1\n"
    "v_mfma_f32_16x16x32_bf16 v[12:15], a[216:219], a[128:131], v[12:15]\n"
    "buffer_load_dwordx4 a[212:215], v240, s[20:23], 0 offen offset:1024 sc1\n"
    "v_mfma_f32_16x16x32_bf16 v[12:15], a[220:223], a[160:163], v[12:15]\n"
    "v_mfma_f32_16x16x32_bf16 v[44:47], a[220:223], a[164:167], v[44:47]\n"
    "v_mfma_f32_16x16x32_bf16 v[44:47], a[216:219], a[132:135], v[44:47]\n"
    "v_mfma_f32_16x16x32_bf16 v[76:79], a[216:219], a[136:139], v[76:79]\n"
    "v_mfma_f32_16x16x32_bf16 v[76:79], a[220:223], a[168:171], v[76:79]\n"
    "v_mfma_f32_16x16x32_bf16 v[108:111], a[220:223], a[172:175], v[108:111]\n"
    "v_mfma_f32_16x16x32_bf16 v[108:111], a[216:219], a[140:143], v[108:111]\n"
    "v_mfma_f32_16x16x32_bf16 v[140:143], a[216:219], a[144:147], v[140:143]\n"
    "v_mfma_f32_16x16x32_bf16 v[140:143], a[220:223], a[176:179], v[140:143]\n"
    "v_mfma_f32_16x16x32_bf16 v[172:175], a[220:223], a[180:183], v[172:175]\n"
    "v_mfma_f32_16x16x32_bf16 v[172:175], a[216:219], a[148:151], v[172:175]\n"
    "v_mfma_f32_16x16x32_bf16 v[204:207], a[216:219], a[152:155], v[204:207]\n"
    "v_mfma_f32_16x16x32_bf16 v[204:207], a[220:223], a[184:187], v[204:207]\n"
    "v_mfma_f32_16x16x32_bf16 v[236:239], a[220:223], a[188:191], v[236:239]\n"
    "v_mfma_f32_16x16x32_bf16 v[236:239], a[216:219], a[156:159], v[236:239]\n"
    "s_waitcnt vmcnt(8)\n"
    "buffer_load_dwordx4 a[216:219], v240, s[20:23], 0 offen offset:2048 sc1\n"
    "v_mfma_f32_16x16x32_bf16 a[240:243], a[224:227], a[156:159], a[240:243]\n"
    "buffer_load_dwordx4 a[220:223], v240, s[20:23], 0 offen offset:3072 sc1\n"
    "s_addk_i32 s20, 4096\n"
    "v_mfma_f32_16x16x32_bf16 a[240:243], a[228:231], a[188:191], a[240:243]\n"
    "v_mfma_f32_16x16x32_bf16 v[208:211], a[228:231], a[184:187], v[208:211]\n"
    "v_mfma_f32_16x16x32_bf16 v[208:211], a[224:227], a[152:155], v[208:211]\n"
    "v_mfma_f32_16x16x32_bf16 v[176:179], a[224:227], a[148:151], v[176:179]\n"
    "v_mfma_f32_16x16x32_bf16 v[176:179], a[228:231], a[180:183], v[176:179]\n"
    "v_mfma_f32_16x16x32_bf16 v[144:147], a[228:231], a[176:179], v[144:147]\n"
    "v_mfma_f32_16x16x32_bf16 v[144:147], a[224:227], a[144:147], v[144:147]\n"
    "v_mfma_f32_16x16x32_bf16 v[112:115], a[224:227], a[140:143], v[112:115]\n"
    "v_mfma_f32_16x16x32_bf16 v[112:115], a[228:231], a[172:175], v[112:115]\n"
    "v_mfma_f32_16x16x32_bf16 v[80:83], a[228:231], a[168:171], v[80:83]\n"
    "v_mfma_f32_16x16x32_bf16 v[80:83], a[224:227], a[136:139], v[80:83]\n"
    "v_mfma_f32_16x16x32_bf16 v[48:51], a[224:227], a[132:135], v[48:51]\n"
    "v_mfma_f32_16x16x32_bf16 v[48:51], a[228:231], a[164:167], v[48:51]\n"
    "v_mfma_f32_16x16x32_bf16 v[16:19], a[228:231], a[160:163], v[16:19]\n"
    "v_mfma_f32_16x16x32_bf16 v[16:19], a[224:227], a[128:131], v[16:19]\n"
    "s_waitcnt vmcnt(8)\n"
    "buffer_load_dwordx4 a[224:227], v240, s[20:23], 0 offen offset:0 sc1\n"
    "v_mfma_f32_16x16x32_bf16 v[20:23], a[232:235], a[128:131], v[20:23]\n"
    "buffer_load_dwordx4 a[228:231], v240, s[20:23], 0 offen offset:1024 sc1\n"
    "v_mfma_f32_16x16x32_bf16 v[20:23], a[236:239], a[160:163], v[20:23]\n"
    "v_mfma_f32_16x16x32_bf16 v[52:55], a[236:239], a[164:167], v[52:55]\n"
    "v_mfma_f32_16x16x32_bf16 v[52:55], a[232:235], a[132:135], v[52:55]\n"
    "v_mfma_f32_16x16x32_bf16 v[84:87], a[232:235], a[136:139], v[84:87]\n"
    "v_mfma_f32_16x16x32_bf16 v[84:87], a[236:239], a[168:171], v[84:87]\n"
    "v_mfma_f32_16x16x32_bf16 v[116:119], a[236:239], a[172:175], v[116:119]\n"
    "v_mfma_f32_16x16x32_bf16 v[116:119], a[232:235], a[140:143], v[116:119]\n"
    "v_mfma_f32_16x16x32_bf16 v[148:151], a[232:235], a[144:147], v[148:151]\n"
    "v_mfma_f32_16x16x32_bf16 v[148:151], a[236:239], a[176:179], v[148:151]\n"
    "v_mfma_f32_16x16x32_bf16 v[180:183], a[236:239], a[180:183], v[180:183]\n"
    "v_mfma_f32_16x16x32_bf16 v[180:183], a[232:235], a[148:151], v[180:183]\n"
    "v_mfma_f32_16x16x32_bf16 v[212:215], a[232:235], a[152:155], v[212:215]\n"
    "v_mfma_f32_16x16x32_bf16 v[212:215], a[236:239], a[184:187], v[212:215]\n"
    "v_mfma_f32_16x16x32_bf16 a[244:247], a[236:239], a[188:191], a[244:247]\n"
    "v_mfma_f32_16x16x32_bf16 a[244:247], a[232:235], a[156:159], a[244:247]\n"
    "s_waitcnt vmcnt(8)\n"
    "buffer_load_dwordx4 a[232:235], v240, s[20:23], 0 offen offset:2048 sc1\n"
    "v_mfma_f32_16x16x32_bf16 a[248:251], a[192:195], a[156:159], a[248:251]\n"
    "buffer_load_dwordx4 a[236:239], v240, s[20:23], 0 offen offset:3072 sc1\n"
    "s_addk_i32 s20, 4096\n"
    "v_mfma_f32_16x16x32_bf16 a[248:251], a[196:199], a[188:191], a[248:251]\n"
    "v_mfma_f32_16x16x32_bf16 v[216:219], a[196:199], a[184:187], v[216:219]\n"
    "v_mfma_f32_16x16x32_bf16 v[216:219], a[192:195], a[152:155], v[216:219]\n"
    "v_mfma_f32_16x16x32_bf16 v[184:187], a[192:195], a[148:151], v[184:187]\n"
    "v_mfma_f32_16x16x32_bf16 v[184:187], a[196:199], a[180:183], v[184:187]\n"
    "v_mfma_f32_16x16x32_bf16 v[152:155], a[196:199], a[176:179], v[152:155]\n"
    "v_mfma_f32_16x16x32_bf16 v[152:155], a[192:195], a[144:147], v[152:155]\n"
    "v_mfma_f32_16x16x32_bf16 v[120:123], a[192:195], a[140:143], v[120:123]\n"
    "v_mfma_f32_16x16x32_bf16 v[120:123], a[196:199], a[172:175], v[120:123]\n"
    "v_mfma_f32_16x16x32_bf16 v[88:91], a[196:199], a[168:171], v[88:91]\n"
    "v_mfma_f32_16x16x32_bf16 v[88:91], a[192:195], a[136:139], v[88:91]\n"
    "v_mfma_f32_16x16x32_bf16 v[56:59], a[192:195], a[132:135], v[56:59]\n"
    "v_mfma_f32_16x16x32_bf16 v[56:59], a[196:199], a[164:167], v[56:59]\n"
    "v_mfma_f32_16x16x32_bf16 v[24:27], a[196:199], a[160:163], v[24:27]\n"
    "v_mfma_f32_16x16x32_bf16 v[24:27], a[192:195], a[128:131], v[24:27]\n"
    "s_waitcnt vmcnt(8)\n"
    "buffer_load_dwordx4 a[192:195], v240, s[20:23], 0 offen offset:0 sc1\n"
    "v_mfma_f32_16x16x32_bf16 v[28:31], a[200:203], a[128:131], v[28:31]\n"
    "buffer_load_dwordx4 a[196:199], v240, s[20:23], 0 offen offset:1024 sc1\n"
    "v_mfma_f32_16x16x32_bf16 v[28:31], a[204:207], a[160:163], v[28:31]\n"
    "v_mfma_f32_16x16x32_bf16 v[60:63], a[204:207], a[164:167], v[60:63]\n"
    "ds_read_b128 a[128:131], v241 offset:49152\n"
    "v_mfma_f32_16x16x32_bf16 v[60:63], a[200:203], a[132:135], v[60:63]\n"
    "ds_read_b128 a[160:163], v241 offset:50176\n"
    "v_mfma_f32_16x16x32_bf16 v[92:95], a[200:203], a[136:139], v[92:95]\n"
    "ds_read_b128 a[164:167], v241 offset:52224\n"
    "v_mfma_f32_16x16x32_bf16 v[92:95], a[204:207], a[168:171], v[92:95]\n"
    "ds_read_b128 a[132:135], v241 offset:51200\n"
    "v_mfma_f32_16x16x32_bf16 v[124:127], a[204:207], a[172:175], v[124:127]\n"
    "ds_read_b128 a[136:139], v241 offset:53248\n"
    "v_mfma_f32_16x16x32_bf16 v[124:127], a[200:203], a[140:143], v[124:127]\n"
    "ds_read_b128 a[168:171], v241 offset:54272\n"
    "v_mfma_f32_16x16x32_bf16 v[156:159], a[200:203], a[144:147], v[156:159]\n"
    "ds_read_b128 a[172:175], v241 offset:56320\n"
    "v_mfma_f32_16x16x32_bf16 v[156:159], a[204:207], a[176:179], v[156:159]\n"
    "ds_read_b128 a[140:143], v241 offset:55296\n"
    "v_mfma_f32_16x16x32_bf16 v[188:191], a[204:207], a[180:183], v[188:191]\n"
    "ds_read_b128 a[144:147], v241 offset:57344\n"
    "v_mfma_f32_16x16x32_bf16 v[188:191], a[200:203], a[148:151], v[188:191]\n"
    "ds_read_b128 a[176:179], v241 offset:58368\n"
    "v_mfma_f32_16x16x32_bf16 v[220:223], a[200:203], a[152:155], v[220:223]\n"
    "ds_read_b128 a[180:183], v241 offset:60416\n"
    "v_mfma_f32_16x16x32_bf16 v[220:223], a[204:207], a[184:187], v[220:223]\n"
    "ds_read_b128 a[148:151], v241 offset:59392\n"
    "v_mfma_f32_16x16x32_bf16 a[252:255], a[204:207], a[188:191], a[252:255]\n"
    "ds_read_b128 a[152:155], v241 offset:61440\n"
    "v_mfma_f32_16x16x32_bf16 a[252:255], a[200:203], a[156:159], a[252:255]\n"
    "s_waitcnt vmcnt(8) lgkmcnt(11)\n"
    "ds_read_b128 a[184:187], v241 offset:62464\n"
    "v_mfma_f32_16x16x32_bf16 v[0:3], a[208:211], a[128:131], v[0:3]\n"
    "buffer_load_dwordx4 a[200:203], v240, s[20:23], 0 offen offset:2048 sc1\n"
    "v_mfma_f32_16x16x32_bf16 v[0:3], a[212:215], a[160:163], v[0:3]\n"
    "s_waitcnt lgkmcnt(10)\n"
    "buffer_load_dwordx4 a[204:207], v240, s[20:23], 0 offen offset:3072 sc1\n"
    "v_mfma_f32_16x16x32_bf16 v[32:35], a[212:215], a[164:167], v[32:35]\n"
    "s_addk_i32 s20, 4096\n"
    "ds_read_b128 a[188:191], v241 offset:64512\n"
    "v_mfma_f32_16x16x32_bf16 v[32:35], a[208:211], a[132:135], v[32:35]\n"
    "s_waitcnt lgkmcnt(9)\n"
    "ds_read_b128 a[156:159], v241 offset:63488\n"
    "v_mfma_f32_16x16x32_bf16 v[64:67], a[208:211], a[136:139], v[64:67]\n"
    "v_mfma_f32_16x16x32_bf16 v[64:67], a[212:215], a[168:171], v[64:67]\n"
    "s_waitcnt lgkmcnt(8)\n"
    "v_mfma_f32_16x16x32_bf16 v[96:99], a[212:215], a[172:175], v[96:99]\n"
    "v_mfma_f32_16x16x32_bf16 v[96:99], a[208:211], a[140:143], v[96:99]\n"
    "s_waitcnt lgkmcnt(6)\n"
    "v_mfma_f32_16x16x32_bf16 v[128:131], a[208:211], a[144:147], v[128:131]\n"
    "v_mfma_f32_16x16x32_bf16 v[128:131], a[212:215], a[176:179], v[128:131]\n"
    "s_waitcnt lgkmcnt(4)\n"
    "v_mfma_f32_16x16x32_bf16 v[160:163], a[212:215], a[180:183], v[160:163]\n"
    "v_mfma_f32_16x16x32_bf16 v[160:163], a[208:211], a[148:151], v[160:163]\n"
    "s_waitcnt lgkmcnt(2)\n"
    "v_mfma_f32_16x16x32_bf16 v[192:195], a[208:211], a[152:155], v[192:195]\n"
    "v_mfma_f32_16x16x32_bf16 v[192:195], a[212:215], a[184:187], v[192:195]\n"
    "s_waitcnt lgkmcnt(0)\n"
    "v_mfma_f32_16x16x32_bf16 v[224:227], a[212:215], a[188:191], v[224:227]\n"
    "v_mfma_f32_16x16x32_bf16 v[224:227], a[208:211], a[156:159], v[224:227]\n"
    "s_waitcnt vmcnt(8)\n"
    "buffer_load_dwordx4 a[208:211], v240, s[20:23], 0 offen offset:0 sc1\n"
    "v_mfma_f32_16x16x32_bf16 v[228:231], a[216:219], a[156:159], v[228:231]\n"
    "buffer_load_dwordx4 a[212:215], v240, s[20:23], 0 offen offset:1024 sc1\n"
    "v_mfma_f32_16x16x32_bf16 v[228:231], a[220:223], a[188:191], v[228:231]\n"
    "v_mfma_f32_16x16x32_bf16 v[196:199], a[220:223], a[184:187], v[196:199]\n"
    "v_mfma_f32_16x16x32_bf16 v[196:199], a[216:219], a[152:155], v[196:199]\n"
    "v_mfma_f32_16x16x32_bf16 v[164:167], a[216:219], a[148:151], v[164:167]\n"
    "v_mfma_f32_16x16x32_bf16 v[164:167], a[220:223], a[180:183], v[164:167]\n"
    "v_mfma_f32_16x16x32_bf16 v[132:135], a[220:223], a[176:179], v[132:135]\n"
    "v_mfma_f32_16x16x32_bf16 v[132:135], a[216:219], a[144:147], v[132:135]\n"
    "v_mfma_f32_16x16x32_bf16 v[100:103], a[216:219], a[140:143], v[100:103]\n"
    "v_mfma_f32_16x16x32_bf16 v[100:103], a[220:223], a[172:175], v[100:103]\n"
    "v_mfma_f32_16x16x32_bf16 v[68:71], a[220:223], a[168:171], v[68:71]\n"
    "v_mfma_f32_16x16x32_bf16 v[68:71], a[216:219], a[136:139], v[68:71]\n"
    "v_mfma_f32_16x16x32_bf16 v[36:39], a[216:219], a[132:135], v[36:39]\n"
    "v_mfma_f32_16x16x32_bf16 v[36:39], a[220:223], a[164:167], v[36:39]\n"
    "v_mfma_f32_16x16x32_bf16 v[4:7], a[220:223], a[160:163], v[4:7]\n"
    "v_mfma_f32_16x16x32_bf16 v[4:7], a[216:219], a[128:131], v[4:7]\n"
    "s_waitcnt vmcnt(8)\n"
    "buffer_load_dwordx4 a[216:219], v240, s[20:23], 0 offen offset:2048 sc1\n"
    "v_mfma_f32_16x16x32_bf16 v[8:11], a[224:227], a[128:131], v[8:11]\n"
    "buffer_load_dwordx4 a[220:223], v240, s[20:23], 0 offen offset:3072 sc1\n"
    "s_addk_i32 s20, 4096\n"
    "v_mfma_f32_16x16x32_bf16 v[8:11], a[228:231], a[160:163], v[8:11]\n"
    "v_mfma_f32_16x16x32_bf16 v[40:43], a[228:231], a[164:167], v[40:43]\n"
    "v_mfma_f32_16x16x32_bf16 v[40:43], a[224:227], a[132:135], v[40:43]\n"
    "v_mfma_f32_16x16x32_bf16 v[72:75], a[224:227], a[136:139], v[72:75]\n"
    "v_mfma_f32_16x16x32_bf16 v[72:75], a[228:231], a[168:171], v[72:75]\n"
    "v_mfma_f32_16x16x32_bf16 v[104:107], a[228:231], a[172:175], v[104:107]\n"
    "v_mfma_f32_16x16x32_bf16 v[104:107], a[224:227], a[140:143], v[104:107]\n"
    "v_mfma_f32_16x16x32_bf16 v[136:139], a[224:227], a[144:147], v[136:139]\n"
    "v_mfma_f32_16x16x32_bf16 v[136:139], a[228:231], a[176:179], v[136:139]\n"
    "v_mfma_f32_16x16x32_bf16 v[168:171], a[228:231], a[180:183], v[168:171]\n"
    "v_mfma_f32_16x16x32_bf16 v[168:171], a[224:227], a[148:151], v[168:171]\n"
    "v_mfma_f32_16x16x32_bf16 v[200:203], a[224:227], a[152:155], v[200:203]\n"
    "v_mfma_f32_16x16x32_bf16 v[200:203], a[228:231], a[184:187], v[200:203]\n"
    "v_mfma_f32_16x16x32_bf16 v[232:235], a[228:231], a[188:191], v[232:235]\n"
    "v_mfma_f32_16x16x32_bf16 v[232:235], a[224:227], a[156:159], v[232:235]\n"
    "s_waitcnt vmcnt(8)\n"
    "buffer_load_dwordx4 a[224:227], v240, s[20:23], 0 offen offset:0 sc1\n"
    "v_mfma_f32_16x16x32_bf16 v[236:239], a[232:235], a[156:159], v[236:239]\n"
    "buffer_load_dwordx4 a[228:231], v240, s[20:23], 0 offen offset:1024 sc1\n"
    "v_mfma_f32_16x16x32_bf16 v[236:239], a[236:239], a[188:191], v[236:239]\n"
    "v_mfma_f32_16x16x32_bf16 v[204:207], a[236:239], a[184:187], v[204:207]\n"
    "v_mfma_f32_16x16x32_bf16 v[204:207], a[232:235], a[152:155], v[204:207]\n"
    "v_mfma_f32_16x16x32_bf16 v[172:175], a[232:235], a[148:151], v[172:175]\n"
    "v_mfma_f32_16x16x32_bf16 v[172:175], a[236:239], a[180:183], v[172:175]\n"
    "v_mfma_f32_16x16x32_bf16 v[140:143], a[236:239], a[176:179], v[140:143]\n"
    "v_mfma_f32_16x16x32_bf16 v[140:143], a[232:235], a[144:147], v[140:143]\n"
    "v_mfma_f32_16x16x32_bf16 v[108:111], a[232:235], a[140:143], v[108:111]\n"
    "v_mfma_f32_16x16x32_bf16 v[108:111], a[236:239], a[172:175], v[108:111]\n"
    "v_mfma_f32_16x16x32_bf16 v[76:79], a[236:239], a[168:171], v[76:79]\n"
    "v_mfma_f32_16x16x32_bf16 v[76:79], a[232:235], a[136:139], v[76:79]\n"
    "v_mfma_f32_16x16x32_bf16 v[44:47], a[232:235], a[132:135], v[44:47]\n"
    "v_mfma_f32_16x16x32_bf16 v[44:47], a[236:239], a[164:167], v[44:47]\n"
    "v_mfma_f32_16x16x32_bf16 v[12:15], a[236:239], a[160:163], v[12:15]\n"
    "v_mfma_f32_16x16x32_bf16 v[12:15], a[232:235], a[128:131], v[12:15]\n"
    "s_waitcnt vmcnt(8)\n"
    "buffer_load_dwordx4 a[232:235], v240, s[20:23], 0 offen offset:2048 sc1\n"
    "v_mfma_f32_16x16x32_bf16 v[16:19], a[192:195], a[128:131], v[16:19]\n"
    "buffer_load_dwordx4 a[236:239], v240, s[20:23], 0 offen offset:3072 sc1\n"
    "s_addk_i32 s20, 4096\n"
    "v_mfma_f32_16x16x32_bf16 v[16:19], a[196:199], a[160:163], v[16:19]\n"
    "v_mfma_f32_16x16x32_bf16 v[48:51], a[196:199], a[164:167], v[48:51]\n"
    "v_mfma_f32_16x16x32_bf16 v[48:51], a[192:195], a[132:135], v[48:51]\n"
    "v_mfma_f32_16x16x32_bf16 v[80:83], a[192:195], a[136:139], v[80:83]\n"
    "v_mfma_f32_16x16x32_bf16 v[80:83], a[196:199], a[168:171], v[80:83]\n"
    "v_mfma_f32_16x16x32_bf16 v[112:115], a[196:199], a[172:175], v[112:115]\n"
    "v_mfma_f32_16x16x32_bf16 v[112:115], a[192:195], a[140:143], v[112:115]\n"
    "v_mfma_f32_16x16x32_bf16 v[144:147], a[192:195], a[144:147], v[144:147]\n"
    "v_mfma_f32_16x16x32_bf16 v[144:147], a[196:199], a[176:179], v[144:147]\n"
    "v_mfma_f32_16x16x32_bf16 v[176:179], a[196:199], a[180:183], v[176:179]\n"
    "v_mfma_f32_16x16x32_bf16 v[176:179], a[192:195], a[148:151], v[176:179]\n"
    "v_mfma_f32_16x16x32_bf16 v[208:211], a[192:195], a[152:155], v[208:211]\n"
    "v_mfma_f32_16x16x32_bf16 v[208:211], a[196:199], a[184:187], v[208:211]\n"
    "v_mfma_f32_16x16x32_bf16 a[240:243], a[196:199], a[188:191], a[240:243]\n"
    "v_mfma_f32_16x16x32_bf16 a[240:243], a[192:195], a[156:159], a[240:243]\n"
    "s_waitcnt vmcnt(8)\n"
    "buffer_load_dwordx4 a[192:195], v240, s[20:23], 0 offen offset:0 sc1\n"
    "v_mfma_f32_16x16x32_bf16 a[244:247], a[200:203], a[156:159], a[244:247]\n"
    "buffer_load_dwordx4 a[196:199], v240, s[20:23], 0 offen offset:1024 sc1\n"
    "v_mfma_f32_16x16x32_bf16 a[244:247], a[204:207], a[188:191], a[244:247]\n"
    "v_mfma_f32_16x16x32_bf16 v[212:215], a[204:207], a[184:187], v[212:215]\n"
    "v_mfma_f32_16x16x32_bf16 v[212:215], a[200:203], a[152:155], v[212:215]\n"
    "v_mfma_f32_16x16x32_bf16 v[180:183], a[200:203], a[148:151], v[180:183]\n"
    "v_mfma_f32_16x16x32_bf16 v[180:183], a[204:207], a[180:183], v[180:183]\n"
    "v_mfma_f32_16x16x32_bf16 v[148:151], a[204:207], a[176:179], v[148:151]\n"
    "v_mfma_f32_16x16x32_bf16 v[148:151], a[200:203], a[144:147], v[148:151]\n"
    "v_mfma_f32_16x16x32_bf16 v[116:119], a[200:203], a[140:143], v[116:119]\n"
    "v_mfma_f32_16x16x32_bf16 v[116:119], a[204:207], a[172:175], v[116:119]\n"
    "v_mfma_f32_16x16x32_bf16 v[84:87], a[204:207], a[168:171], v[84:87]\n"
    "v_mfma_f32_16x16x32_bf16 v[84:87], a[200:203], a[136:139], v[84:87]\n"
    "v_mfma_f32_16x16x32_bf16 v[52:55], a[200:203], a[132:135], v[52:55]\n"
    "v_mfma_f32_16x16x32_bf16 v[52:55], a[204:207], a[164:167], v[52:55]\n"
    "v_mfma_f32_16x16x32_bf16 v[20:23], a[204:207], a[160:163], v[20:23]\n"
    "v_mfma_f32_16x16x32_bf16 v[20:23], a[200:203], a[128:131], v[20:23]\n"
    "s_waitcnt vmcnt(8)\n"
    "buffer_load_dwordx4 a[200:203], v240, s[20:23], 0 offen offset:2048 sc1\n"
    "v_mfma_f32_16x16x32_bf16 v[24:27], a[208:211], a[128:131], v[24:27]\n"
    "buffer_load_dwordx4 a[204:207], v240, s[20:23], 0 offen offset:3072 sc1\n"
    "s_addk_i32 s20, 4096\n"
    "v_mfma_f32_16x16x32_bf16 v[24:27], a[212:215], a[160:163], v[24:27]\n"
    "v_mfma_f32_16x16x32_bf16 v[56:59], a[212:215], a[164:167], v[56:59]\n"
    "v_mfma_f32_16x16x32_bf16 v[56:59], a[208:211], a[132:135], v[56:59]\n"
    "v_mfma_f32_16x16x32_bf16 v[88:91], a[208:211], a[136:139], v[88:91]\n"
    "v_mfma_f32_16x16x32_bf16 v[88:91], a[212:215], a[168:171], v[88:91]\n"
    "v_mfma_f32_16x16x32_bf16 v[120:123], a[212:215], a[172:175], v[120:123]\n"
    "v_mfma_f32_16x16x32_bf16 v[120:123], a[208:211], a[140:143], v[120:123]\n"
    "v_mfma_f32_16x16x32_bf16 v[152:155], a[208:211], a[144:147], v[152:155]\n"
    "v_mfma_f32_16x16x32_bf16 v[152:155], a[212:215], a[176:179], v[152:155]\n"
    "v_mfma_f32_16x16x32_bf16 v[184:187], a[212:215], a[180:183], v[184:187]\n"
    "v_mfma_f32_16x16x32_bf16 v[184:187], a[208:211], a[148:151], v[184:187]\n"
    "v_mfma_f32_16x16x32_bf16 v[216:219], a[208:211], a[152:155], v[216:219]\n"
    "v_mfma_f32_16x16x32_bf16 v[216:219], a[212:215], a[184:187], v[216:219]\n"
    "s_barrier\n"
    "v_mfma_f32_16x16x32_bf16 a[248:251], a[212:215], a[188:191], a[248:251]\n"
    "v_mfma_f32_16x16x32_bf16 a[248:251], a[208:211], a[156:159], a[248:251]\n"
    "s_waitcnt vmcnt(8)\n"
    "buffer_load_dwordx4 a[208:211], v240, s[20:23], 0 offen offset:0 sc1\n"
    "v_mfma_f32_16x16x32_bf16 a[252:255], a[216:219], a[156:159], a[252:255]\n"
    "buffer_load_dwordx4 a[212:215], v240, s[20:23], 0 offen offset:1024 sc1\n"
    "v_mfma_f32_16x16x32_bf16 a[252:255], a[220:223], a[188:191], a[252:255]\n"
    "v_mfma_f32_16x16x32_bf16 v[220:223], a[220:223], a[184:187], v[220:223]\n"
    "v_add_u32 v241, s67, v240\n"
    "v_mfma_f32_16x16x32_bf16 v[220:223], a[216:219], a[152:155], v[220:223]\n"
    "ds_read_b128 a[156:159], v241 offset:14336\n"
    "v_mfma_f32_16x16x32_bf16 v[188:191], a[216:219], a[148:151], v[188:191]\n"
    "ds_read_b128 a[188:191], v241 offset:15360\n"
    "v_mfma_f32_16x16x32_bf16 v[188:191], a[220:223], a[180:183], v[188:191]\n"
    "ds_read_b128 a[184:187], v241 offset:13312\n"
    "v_mfma_f32_16x16x32_bf16 v[156:159], a[220:223], a[176:179], v[156:159]\n"
    "ds_read_b128 a[152:155], v241 offset:12288\n"
    "v_mfma_f32_16x16x32_bf16 v[156:159], a[216:219], a[144:147], v[156:159]\n"
    "ds_read_b128 a[148:151], v241 offset:10240\n"
    "v_mfma_f32_16x16x32_bf16 v[124:127], a[216:219], a[140:143], v[124:127]\n"
    "ds_read_b128 a[180:183], v241 offset:11264\n"
    "v_mfma_f32_16x16x32_bf16 v[124:127], a[220:223], a[172:175], v[124:127]\n"
    "ds_read_b128 a[176:179], v241 offset:9216\n"
    "v_mfma_f32_16x16x32_bf16 v[92:95], a[220:223], a[168:171], v[92:95]\n"
    "ds_read_b128 a[144:147], v241 offset:8192\n"
    "v_mfma_f32_16x16x32_bf16 v[92:95], a[216:219], a[136:139], v[92:95]\n"
    "ds_read_b128 a[140:143], v241 offset:6144\n"
    "v_mfma_f32_16x16x32_bf16 v[60:63], a[216:219], a[132:135], v[60:63]\n"
    "ds_read_b128 a[172:175], v241 offset:7168\n"
    "v_mfma_f32_16x16x32_bf16 v[60:63], a[220:223], a[164:167], v[60:63]\n"
    "ds_read_b128 a[168:171], v241 offset:5120\n"
    "v_mfma_f32_16x16x32_bf16 v[28:31], a[220:223], a[160:163], v[28:31]\n"
    "ds_read_b128 a[136:139], v241 offset:4096\n"
    "v_mfma_f32_16x16x32_bf16 v[28:31], a[216:219], a[128:131], v[28:31]\n"
    "s_waitcnt vmcnt(8) lgkmcnt(10)\n"
    "ds_read_b128 a[132:135], v241 offset:2048\n"
    "v_mfma_f32_16x16x32_bf16 v[224:227], a[224:227], a[156:159], v[224:227]\n"
    "ds_read_b128 a[164:167], v241 offset:3072\n"
    "v_mfma_f32_16x16x32_bf16 v[224:227], a[228:231], a[188:191], v[224:227]\n"
    "s_waitcnt lgkmcnt(10)\n"
    "buffer_load_dwordx4 a[216:219], v240, s[20:23], 0 offen offset:2048 sc1\n"
    "v_mfma_f32_16x16x32_bf16 v[192:195], a[228:231], a[184:187], v[192:195]\n"
    "buffer_load_dwordx4 a[220:223], v240, s[20:23], 0 offen offset:3072 sc1\n"
    "s_addk_i32 s20, 4096\n"
    "v_mfma_f32_16x16x32_bf16 v[192:195], a[224:227], a[152:155], v[192:195]\n"
    "s_waitcnt lgkmcnt(8)\n"
    "ds_read_b128 a[160:163], v241 offset:1024\n"
    "v_mfma_f32_16x16x32_bf16 v[160:163], a[224:227], a[148:151], v[160:163]\n"
    "ds_read_b128 a[128:131], v241 offset:0\n"
    "v_mfma_f32_16x16x32_bf16 v[160:163], a[228:231], a[180:183], v[160:163]\n"
    "s_waitcnt lgkmcnt(8)\n"
    "v_mfma_f32_16x16x32_bf16 v[128:131], a[228:231], a[176:179], v[128:131]\n"
    "v_mfma_f32_16x16x32_bf16 v[128:131], a[224:227], a[144:147], v[128:131]\n"
    "s_waitcnt lgkmcnt(6)\n"
    "v_mfma_f32_16x16x32_bf16 v[96:99], a[224:227], a[140:143], v[96:99]\n"
    "v_mfma_f32_16x16x32_bf16 v[96:99], a[228:231], a[172:175], v[96:99]\n"
    "s_waitcnt lgkmcnt(4)\n"
    "v_mfma_f32_16x16x32_bf16 v[64:67], a[228:231], a[168:171], v[64:67]\n"
    "v_mfma_f32_16x16x32_bf16 v[64:67], a[224:227], a[136:139], v[64:67]\n"
    "s_waitcnt lgkmcnt(2)\n"
    "v_mfma_f32_16x16x32_bf16 v[32:35], a[224:227], a[132:135], v[32:35]\n"
    "v_mfma_f32_16x16x32_bf16 v[32:35], a[228:231], a[164:167], v[32:35]\n"
    "s_waitcnt lgkmcnt(0)\n"
    "v_mfma_f32_16x16x32_bf16 v[0:3], a[228:231], a[160:163], v[0:3]\n"
    "v_mfma_f32_16x16x32_bf16 v[0:3], a[224:227], a[128:131], v[0:3]\n"
    "s_waitcnt vmcnt(8)\n"
    "buffer_load_dwordx4 a[224:227], v240, s[20:23], 0 offen offset:0 sc1\n"
    "v_mfma_f32_16x16x32_bf16 v[4:7], a[232:235], a[128:131], v[4:7]\n"
    "buffer_load_dwordx4 a[228:231], v240, s[20:23], 0 offen offset:1024 sc1\n"
    "v_mfma_f32_16x16x32_bf16 v[4:7], a[236:239], a[160:163], v[4:7]\n"
    "v_mfma_f32_16x16x32_bf16 v[36:39], a[236:239], a[164:167], v[36:39]\n"
    "v_mfma_f32_16x16x32_bf16 v[36:39], a[232:235], a[132:135], v[36:39]\n"
    "v_mfma_f32_16x16x32_bf16 v[68:71], a[232:235], a[136:139], v[68:71]\n"
    "v_mfma_f32_16x16x32_bf16 v[68:71], a[236:239], a[168:171], v[68:71]\n"
    "v_mfma_f32_16x16x32_bf16 v[100:103], a[236:239], a[172:175], v[100:103]\n"
    "v_mfma_f32_16x16x32_bf16 v[100:103], a[232:235], a[140:143], v[100:103]\n"
    "v_mfma_f32_16x16x32_bf16 v[132:135], a[232:235], a[144:147], v[132:135]\n"
    "v_mfma_f32_16x16x32_bf16 v[132:135], a[236:239], a[176:179], v[132:135]\n"
    "v_mfma_f32_16x16x32_bf16 v[164:167], a[236:239], a[180:183], v[164:167]\n"
    "v_mfma_f32_16x16x32_bf16 v[164:167], a[232:235], a[148:151], v[164:167]\n"
    "v_mfma_f32_16x16x32_bf16 v[196:199], a[232:235], a[152:155], v[196:199]\n"
    "v_mfma_f32_16x16x32_bf16 v[196:199], a[236:239], a[184:187], v[196:199]\n"
    "v_mfma_f32_16x16x32_bf16 v[228:231], a[236:239], a[188:191], v[228:231]\n"
    "v_mfma_f32_16x16x32_bf16 v[228:231], a[232:235], a[156:159], v[228:231]\n"
    "s_waitcnt vmcnt(8)\n"
    "buffer_load_dwordx4 a[232:235], v240, s[20:23], 0 offen offset:2048 sc1\n"
    "v_mfma_f32_16x16x32_bf16 v[232:235], a[192:195], a[156:159], v[232:235]\n"
    "buffer_load_dwordx4 a[236:239], v240, s[20:23], 0 offen offset:3072 sc1\n"
    "s_addk_i32 s20, 4096\n"
    "v_mfma_f32_16x16x32_bf16 v[232:235], a[196:199], a[188:191], v[232:235]\n"
    "v_mfma_f32_16x16x32_bf16 v[200:203], a[196:199], a[184:187], v[200:203]\n"
    "v_mfma_f32_16x16x32_bf16 v[200:203], a[192:195], a[152:155], v[200:203]\n"
    "v_mfma_f32_16x16x32_bf16 v[168:171], a[192:195], a[148:151], v[168:171]\n"
    "v_mfma_f32_16x16x32_bf16 v[168:171], a[196:199], a[180:183], v[168:171]\n"
    "v_mfma_f32_16x16x32_bf16 v[136:139], a[196:199], a[176:179], v[136:139]\n"
    "v_mfma_f32_16x16x32_bf16 v[136:139], a[192:195], a[144:147], v[136:139]\n"
    "v_mfma_f32_16x16x32_bf16 v[104:107], a[192:195], a[140:143], v[104:107]\n"
    "v_mfma_f32_16x16x32_bf16 v[104:107], a[196:199], a[172:175], v[104:107]\n"
    "v_mfma_f32_16x16x32_bf16 v[72:75], a[196:199], a[168:171], v[72:75]\n"
    "v_mfma_f32_16x16x32_bf16 v[72:75], a[192:195], a[136:139], v[72:75]\n"
    "v_mfma_f32_16x16x32_bf16 v[40:43], a[192:195], a[132:135], v[40:43]\n"
    "v_mfma_f32_16x16x32_bf16 v[40:43], a[196:199], a[164:167], v[40:43]\n"
    "v_mfma_f32_16x16x32_bf16 v[8:11], a[196:199], a[160:163], v[8:11]\n"
    "v_mfma_f32_16x16x32_bf16 v[8:11], a[192:195], a[128:131], v[8:11]\n"
    "s_waitcnt vmcnt(8)\n"
    "buffer_load_dwordx4 a[192:195], v240, s[20:23], 0 offen offset:0 sc1\n"
    "v_mfma_f32_16x16x32_bf16 v[12:15], a[200:203], a[128:131], v[12:15]\n"
    "buffer_load_dwordx4 a[196:199], v240, s[20:23], 0 offen offset:1024 sc1\n"
    "v_mfma_f32_16x16x32_bf16 v[12:15], a[204:207], a[160:163], v[12:15]\n"
    "v_mfma_f32_16x16x32_bf16 v[44:47], a[204:207], a[164:167], v[44:47]\n"
    "v_mfma_f32_16x16x32_bf16 v[44:47], a[200:203], a[132:135], v[44:47]\n"
    "v_mfma_f32_16x16x32_bf16 v[76:79], a[200:203], a[136:139], v[76:79]\n"
    "v_mfma_f32_16x16x32_bf16 v[76:79], a[204:207], a[168:171], v[76:79]\n"
    "v_mfma_f32_16x16x32_bf16 v[108:111], a[204:207], a[172:175], v[108:111]\n"
    "v_mfma_f32_16x16x32_bf16 v[108:111], a[200:203], a[140:143], v[108:111]\n"
    "v_mfma_f32_16x16x32_bf16 v[140:143], a[200:203], a[144:147], v[140:143]\n"
    "v_mfma_f32_16x16x32_bf16 v[140:143], a[204:207], a[176:179], v[140:143]\n"
    "v_mfma_f32_16x16x32_bf16 v[172:175], a[204:207], a[180:183], v[172:175]\n"
    "v_mfma_f32_16x16x32_bf16 v[172:175], a[200:203], a[148:151], v[172:175]\n"
    "v_mfma_f32_16x16x32_bf16 v[204:207], a[200:203], a[152:155], v[204:207]\n"
    "v_mfma_f32_16x16x32_bf16 v[204:207], a[204:207], a[184:187], v[204:207]\n"
    "v_mfma_f32_16x16x32_bf16 v[236:239], a[204:207], a[188:191], v[236:239]\n"
    "v_mfma_f32_16x16x32_bf16 v[236:239], a[200:203], a[156:159], v[236:239]\n"
    "s_waitcnt vmcnt(8)\n"
    "buffer_load_dwordx4 a[200:203], v240, s[20:23], 0 offen offset:2048 sc1\n"
    "v_mfma_f32_16x16x32_bf16 a[240:243], a[208:211], a[156:159], a[240:243]\n"
    "buffer_load_dwordx4 a[204:207], v240, s[20:23], 0 offen offset:3072 sc1\n"
    "s_addk_i32 s20, 4096\n"
    "v_mfma_f32_16x16x32_bf16 a[240:243], a[212:215], a[188:191], a[240:243]\n"
    "v_mfma_f32_16x16x32_bf16 v[208:211], a[212:215], a[184:187], v[208:211]\n"
    "v_mfma_f32_16x16x32_bf16 v[208:211], a[208:211], a[152:155], v[208:211]\n"
    "v_mfma_f32_16x16x32_bf16 v[176:179], a[208:211], a[148:151], v[176:179]\n"
    "v_mfma_f32_16x16x32_bf16 v[176:179], a[212:215], a[180:183], v[176:179]\n"
    "v_mfma_f32_16x16x32_bf16 v[144:147], a[212:215], a[176:179], v[144:147]\n"
    "v_mfma_f32_16x16x32_bf16 v[144:147], a[208:211], a[144:147], v[144:147]\n"
    "v_mfma_f32_16x16x32_bf16 v[112:115], a[208:211], a[140:143], v[112:115]\n"
    "v_mfma_f32_16x16x32_bf16 v[112:115], a[212:215], a[172:175], v[112:115]\n"
    "v_mfma_f32_16x16x32_bf16 v[80:83], a[212:215], a[168:171], v[80:83]\n"
    "v_mfma_f32_16x16x32_bf16 v[80:83], a[208:211], a[136:139], v[80:83]\n"
    "v_mfma_f32_16x16x32_bf16 v[48:51], a[208:211], a[132:135], v[48:51]\n"
    "v_mfma_f32_16x16x32_bf16 v[48:51], a[212:215], a[164:167], v[48:51]\n"
    "v_mfma_f32_16x16x32_bf16 v[16:19], a[212:215], a[160:163], v[16:19]\n"
    "v_mfma_f32_16x16x32_bf16 v[16:19], a[208:211], a[128:131], v[16:19]\n"
    "s_waitcnt vmcnt(8)\n"
    "buffer_load_dwordx4 a[208:211], v240, s[20:23], 0 offen offset:0 sc1\n"
    "v_mfma_f32_16x16x32_bf16 v[20:23], a[216:219], a[128:131], v[20:23]\n"
    "buffer_load_dwordx4 a[212:215], v240, s[20:23], 0 offen offset:1024 sc1\n"
    "v_mfma_f32_16x16x32_bf16 v[20:23], a[220:223], a[160:163], v[20:23]\n"
    "v_mfma_f32_16x16x32_bf16 v[52:55], a[220:223], a[164:167], v[52:55]\n"
    "v_mfma_f32_16x16x32_bf16 v[52:55], a[216:219], a[132:135], v[52:55]\n"
    "v_mfma_f32_16x16x32_bf16 v[84:87], a[216:219], a[136:139], v[84:87]\n"
    "v_mfma_f32_16x16x32_bf16 v[84:87], a[220:223], a[168:171], v[84:87]\n"
    "v_mfma_f32_16x16x32_bf16 v[116:119], a[220:223], a[172:175], v[116:119]\n"
    "v_mfma_f32_16x16x32_bf16 v[116:119], a[216:219], a[140:143], v[116:119]\n"
    "v_mfma_f32_16x16x32_bf16 v[148:151], a[216:219], a[144:147], v[148:151]\n"
    "v_mfma_f32_16x16x32_bf16 v[148:151], a[220:223], a[176:179], v[148:151]\n"
    "v_mfma_f32_16x16x32_bf16 v[180:183], a[220:223], a[180:183], v[180:183]\n"
    "v_mfma_f32_16x16x32_bf16 v[180:183], a[216:219], a[148:151], v[180:183]\n"
    "v_mfma_f32_16x16x32_bf16 v[212:215], a[216:219], a[152:155], v[212:215]\n"
    "v_mfma_f32_16x16x32_bf16 v[212:215], a[220:223], a[184:187], v[212:215]\n"
    "v_mfma_f32_16x16x32_bf16 a[244:247], a[220:223], a[188:191], a[244:247]\n"
    "v_mfma_f32_16x16x32_bf16 a[244:247], a[216:219], a[156:159], a[244:247]\n"
    "s_waitcnt vmcnt(8)\n"
    "buffer_load_dwordx4 a[216:219], v240, s[20:23], 0 offen offset:2048 sc1\n"
    "v_mfma_f32_16x16x32_bf16 a[248:251], a[224:227], a[156:159], a[248:251]\n"
    "buffer_load_dwordx4 a[220:223], v240, s[20:23], 0 offen offset:3072 sc1\n"
    "s_addk_i32 s20, 4096\n"
    "v_mfma_f32_16x16x32_bf16 a[248:251], a[228:231], a[188:191], a[248:251]\n"
    "v_mfma_f32_16x16x32_bf16 v[216:219], a[228:231], a[184:187], v[216:219]\n"
    "v_mfma_f32_16x16x32_bf16 v[216:219], a[224:227], a[152:155], v[216:219]\n"
    "v_mfma_f32_16x16x32_bf16 v[184:187], a[224:227], a[148:151], v[184:187]\n"
    "v_mfma_f32_16x16x32_bf16 v[184:187], a[228:231], a[180:183], v[184:187]\n"
    "v_mfma_f32_16x16x32_bf16 v[152:155], a[228:231], a[176:179], v[152:155]\n"
    "v_mfma_f32_16x16x32_bf16 v[152:155], a[224:227], a[144:147], v[152:155]\n"
    "v_mfma_f32_16x16x32_bf16 v[120:123], a[224:227], a[140:143], v[120:123]\n"
    "v_mfma_f32_16x16x32_bf16 v[120:123], a[228:231], a[172:175], v[120:123]\n"
    "v_mfma_f32_16x16x32_bf16 v[88:91], a[228:231], a[168:171], v[88:91]\n"
    "v_mfma_f32_16x16x32_bf16 v[88:91], a[224:227], a[136:139], v[88:91]\n"
    "v_mfma_f32_16x16x32_bf16 v[56:59], a[224:227], a[132:135], v[56:59]\n"
    "v_mfma_f32_16x16x32_bf16 v[56:59], a[228:231], a[164:167], v[56:59]\n"
    "v_mfma_f32_16x16x32_bf16 v[24:27], a[228:231], a[160:163], v[24:27]\n"
    "v_mfma_f32_16x16x32_bf16 v[24:27], a[224:227], a[128:131], v[24:27]\n"
    "s_waitcnt vmcnt(8)\n"
    "buffer_load_dwordx4 a[224:227], v240, s[20:23], 0 offen offset:0 sc1\n"
    "v_mfma_f32_16x16x32_bf16 v[28:31], a[232:235], a[128:131], v[28:31]\n"
    "buffer_load_dwordx4 a[228:231], v240, s[20:23], 0 offen offset:1024 sc1\n"
    "v_mfma_f32_16x16x32_bf16 v[28:31], a[236:239], a[160:163], v[28:31]\n"
    "v_mfma_f32_16x16x32_bf16 v[60:63], a[236:239], a[164:167], v[60:63]\n"
    "ds_read_b128 a[128:131], v241 offset:16384\n"
    "v_mfma_f32_16x16x32_bf16 v[60:63], a[232:235], a[132:135], v[60:63]\n"
    "ds_read_b128 a[160:163], v241 offset:17408\n"
    "v_mfma_f32_16x16x32_bf16 v[92:95], a[232:235], a[136:139], v[92:95]\n"
    "ds_read_b128 a[164:167], v241 offset:19456\n"
    "v_mfma_f32_16x16x32_bf16 v[92:95], a[236:239], a[168:171], v[92:95]\n"
    "ds_read_b128 a[132:135], v241 offset:18432\n"
    "v_mfma_f32_16x16x32_bf16 v[124:127], a[236:239], a[172:175], v[124:127]\n"
    "ds_read_b128 a[136:139], v241 offset:20480\n"
    "v_mfma_f32_16x16x32_bf16 v[124:127], a[232:235], a[140:143], v[124:127]\n"
    "ds_read_b128 a[168:171], v241 offset:21504\n"
    "v_mfma_f32_16x16x32_bf16 v[156:159], a[232:235], a[144:147], v[156:159]\n"
    "ds_read_b128 a[172:175], v241 offset:23552\n"
    "v_mfma_f32_16x16x32_bf16 v[156:159], a[236:239], a[176:179], v[156:159]\n"
    "ds_read_b128 a[140:143], v241 offset:22528\n"
    "v_mfma_f32_16x16x32_bf16 v[188:191], a[236:239], a[180:183], v[188:191]\n"
    "ds_read_b128 a[144:147], v241 offset:24576\n"
    "v_mfma_f32_16x16x32_bf16 v[188:191], a[232:235], a[148:151], v[188:191]\n"
    "ds_read_b128 a[176:179], v241 offset:25600\n"
    "v_mfma_f32_16x16x32_bf16 v[220:223], a[232:235], a[152:155], v[220:223]\n"
    "ds_read_b128 a[180:183], v241 offset:27648\n"
    "v_mfma_f32_16x16x32_bf16 v[220:223], a[236:239], a[184:187], v[220:223]\n"
    "ds_read_b128 a[148:151], v241 offset:26624\n"
    "v_mfma_f32_16x16x32_bf16 a[252:255], a[236:239], a[188:191], a[252:255]\n"
    "ds_read_b128 a[152:155], v241 offset:28672\n"
    "v_mfma_f32_16x16x32_bf16 a[252:255], a[232:235], a[156:159], a[252:255]\n"
    "s_waitcnt vmcnt(8) lgkmcnt(11)\n"
    "ds_read_b128 a[184:187], v241 offset:29696\n"
    "v_mfma_f32_16x16x32_bf16 v[0:3], a[192:195], a[128:131], v[0:3]\n"
    "buffer_load_dwordx4 a[232:235], v240, s[20:23], 0 offen offset:2048 sc1\n"
    "v_accvgpr_read_b32 v245, a0\n"
    "v_mfma_f32_16x16x32_bf16 v[0:3], a[196:199], a[160:163], v[0:3]\n"
    "s_waitcnt lgkmcnt(10)\n"
    "buffer_load_dwordx4 a[236:239], v240, s[20:23], 0 offen offset:3072 sc1\n"
    "v_mfma_f32_16x16x32_bf16 v[32:35], a[196:199], a[164:167], v[32:35]\n"
    "s_addk_i32 s20, 4096\n"
    "ds_read_b128 a[188:191], v241 offset:31744\n"
    "v_accvgpr_read_b32 v247, a1\n"
    "v_mfma_f32_16x16x32_bf16 v[32:35], a[192:195], a[132:135], v[32:35]\n"
    "s_waitcnt lgkmcnt(9)\n"
    "ds_read_b128 a[156:159], v241 offset:30720\n"
    "v_lshlrev_b32 v244, 16, v245\n"
    "v_mfma_f32_16x16x32_bf16 v[64:67], a[192:195], a[136:139], v[64:67]\n"
    "v_lshlrev_b32 v246, 16, v247\n"
    "v_and_b32 v245, s58, v245\n"
    "v_mfma_f32_16x16x32_bf16 v[64:67], a[196:199], a[168:171], v[64:67]\n"
    "s_waitcnt lgkmcnt(8)\n"
    "v_and_b32 v247, s58, v247\n"
    "v_add_f32_dpp v254, v253, v0 row_newbcast:0 row_mask:0xf bank_mask:0xf\n"
    "v_mfma_f32_16x16x32_bf16 v[96:99], a[196:199], a[172:175], v[96:99]\n"
    "v_add_f32_dpp v255, v253, v2 row_newbcast:2 row_mask:0xf bank_mask:0xf\n"
    "v_mul_f32 v244, v244, v254\n"
    "v_mfma_f32_16x16x32_bf16 v[96:99], a[192:195], a[140:143], v[96:99]\n"
    "s_waitcnt lgkmcnt(6)\n"
    "v_mul_f32 v246, v246, v255\n"
    "v_add_f32_dpp v254, v253, v1 row_newbcast:1 row_mask:0xf bank_mask:0xf\n"
    "v_mfma_f32_16x16x32_bf16 v[128:131], a[192:195], a[144:147], v[128:131]\n"
    "v_add_f32_dpp v255, v253, v3 row_newbcast:3 row_mask:0xf bank_mask:0xf\n"
    "v_mul_f32 v245, v245, v254\n"
    "v_mfma_f32_16x16x32_bf16 v[128:131], a[196:199], a[176:179], v[128:131]\n"
    "s_waitcnt lgkmcnt(4)\n"
    "v_mul_f32 v247, v247, v255\n"
    "v_cvt_pk_bf16_f32 v248, v244, v245\n"
    "v_mfma_f32_16x16x32_bf16 v[160:163], a[196:199], a[180:183], v[160:163]\n"
    "v_cvt_pk_bf16_f32 v249, v246, v247\n"
    "v_accvgpr_read_b32 v245, a2\n"
    "v_mfma_f32_16x16x32_bf16 v[160:163], a[192:195], a[148:151], v[160:163]\n"
    "s_waitcnt lgkmcnt(2)\n"
    "v_accvgpr_read_b32 v247, a3\n"
    "v_lshlrev_b32 v244, 16, v245\n"
    "v_mfma_f32_16x16x32_bf16 v[192:195], a[192:195], a[152:155], v[192:195]\n"
    "v_lshlrev_b32 v246, 16, v247\n"
    "v_and_b32 v245, s58, v245\n"
    "v_mfma_f32_16x16x32_bf16 v[192:195], a[196:199], a[184:187], v[192:195]\n"
    "s_waitcnt lgkmcnt(0)\n"
    "v_and_b32 v247, s58, v247\n"
    "v_mfma_f32_16x16x32_bf16 v[224:227], a[196:199], a[188:191], v[224:227]\n"
    "v_mfma_f32_16x16x32_bf16 v[224:227], a[192:195], a[156:159], v[224:227]\n"
    "s_waitcnt vmcnt(8)\n"
    "buffer_load_dwordx4 a[192:195], v240, s[20:23], 0 offen offset:0 sc1\n"
    "v_mfma_f32_16x16x32_bf16 v[228:231], a[200:203], a[156:159], v[228:231]\n"
    "buffer_load_dwordx4 a[196:199], v240, s[20:23], 0 offen offset:1024 sc1\n"
    "v_mfma_f32_16x16x32_bf16 v[228:231], a[204:207], a[188:191], v[228:231]\n"
    "v_mfma_f32_16x16x32_bf16 v[196:199], a[204:207], a[184:187], v[196:199]\n"
    "v_mfma_f32_16x16x32_bf16 v[196:199], a[200:203], a[152:155], v[196:199]\n"
    "v_mfma_f32_16x16x32_bf16 v[164:167], a[200:203], a[148:151], v[164:167]\n"
    "v_mfma_f32_16x16x32_bf16 v[164:167], a[204:207], a[180:183], v[164:167]\n"
    "v_mfma_f32_16x16x32_bf16 v[132:135], a[204:207], a[176:179], v[132:135]\n"
    "v_mfma_f32_16x16x32_bf16 v[132:135], a[200:203], a[144:147], v[132:135]\n"
    "v_mfma_f32_16x16x32_bf16 v[100:103], a[200:203], a[140:143], v[100:103]\n"
    "v_mfma_f32_16x16x32_bf16 v[100:103], a[204:207], a[172:175], v[100:103]\n"
    "v_mfma_f32_16x16x32_bf16 v[68:71], a[204:207], a[168:171], v[68:71]\n"
    "v_mfma_f32_16x16x32_bf16 v[68:71], a[200:203], a[136:139], v[68:71]\n"
    "v_mfma_f32_16x16x32_bf16 v[36:39], a[200:203], a[132:135], v[36:39]\n"
    "v_mfma_f32_16x16x32_bf16 v[36:39], a[204:207], a[164:167], v[36:39]\n"
    "v_mfma_f32_16x16x32_bf16 v[4:7], a[204:207], a[160:163], v[4:7]\n"
    "v_mfma_f32_16x16x32_bf16 v[4:7], a[200:203], a[128:131], v[4:7]\n"
    "s_waitcnt vmcnt(8)\n"
    "buffer_load_dwordx4 a[200:203], v240, s[20:23], 0 offen offset:2048 sc1\n"
    "v_mfma_f32_16x16x32_bf16 v[8:11], a[208:211], a[128:131], v[8:11]\n"
    "buffer_load_dwordx4 a[204:207], v240, s[20:23], 0 offen offset:3072 sc1\n"
    "s_addk_i32 s20, 4096\n"
    "v_mfma_f32_16x16x32_bf16 v[8:11], a[212:215], a[160:163], v[8:11]\n"
    "v_mfma_f32_16x16x32_bf16 v[40:43], a[212:215], a[164:167], v[40:43]\n"
    "s_xor_b32 s96, s54, 0x40000\n"
    "s_add_u32 s96, s96, s32\n"
    "s_addc_u32 s97, s33, 0\n"
    "v_mfma_f32_16x16x32_bf16 v[40:43], a[208:211], a[132:135], v[40:43]\n"
    "v_add_f32_dpp v254, v253, v4 row_newbcast:4 row_mask:0xf bank_mask:0xf\n"
    "v_add_f32_dpp v255, v253, v6 row_newbcast:6 row_mask:0xf bank_mask:0xf\n"
    "v_mfma_f32_16x16x32_bf16 v[72:75], a[208:211], a[136:139], v[72:75]\n"
    "v_mul_f32 v244, v244, v254\n"
    "v_mul_f32 v246, v246, v255\n"
    "v_mfma_f32_16x16x32_bf16 v[72:75], a[212:215], a[168:171], v[72:75]\n"
    "v_add_f32_dpp v254, v253, v5 row_newbcast:5 row_mask:0xf bank_mask:0xf\n"
    "v_add_f32_dpp v255, v253, v7 row_newbcast:7 row_mask:0xf bank_mask:0xf\n"
    "v_mfma_f32_16x16x32_bf16 v[104:107], a[212:215], a[172:175], v[104:107]\n"
    "v_mul_f32 v245, v245, v254\n"
    "v_mul_f32 v247, v247, v255\n"
    "v_mfma_f32_16x16x32_bf16 v[104:107], a[208:211], a[140:143], v[104:107]\n"
    "v_cvt_pk_bf16_f32 v250, v244, v245\n"
    "v_cvt_pk_bf16_f32 v251, v246, v247\n"
    "v_mfma_f32_16x16x32_bf16 v[136:139], a[208:211], a[144:147], v[136:139]\n"
    "v_accvgpr_read_b32 v245, a4\n"
    "v_accvgpr_read_b32 v247, a5\n"
    "v_mfma_f32_16x16x32_bf16 v[136:139], a[212:215], a[176:179], v[136:139]\n"
    "v_lshlrev_b32 v244, 16, v245\n"
    "v_lshlrev_b32 v246, 16, v247\n"
    "v_mfma_f32_16x16x32_bf16 v[168:171], a[212:215], a[180:183], v[168:171]\n"
    "v_and_b32 v245, s58, v245\n"
    "v_and_b32 v247, s58, v247\n"
    "v_mfma_f32_16x16x32_bf16 v[168:171], a[208:211], a[148:151], v[168:171]\n"
    "v_add_f32_dpp v254, v253, v32 row_newbcast:0 row_mask:0xf bank_mask:0xf\n"
    "v_add_f32_dpp v255, v253, v34 row_newbcast:2 row_mask:0xf bank_mask:0xf\n"
    "v_mfma_f32_16x16x32_bf16 v[200:203], a[208:211], a[152:155], v[200:203]\n"
    "v_mul_f32 v244, v244, v254\n"
    "v_mul_f32 v246, v246, v255\n"
    "v_mfma_f32_16x16x32_bf16 v[200:203], a[212:215], a[184:187], v[200:203]\n"
    "v_add_f32_dpp v254, v253, v33 row_newbcast:1 row_mask:0xf bank_mask:0xf\n"
    "v_add_f32_dpp v255, v253, v35 row_newbcast:3 row_mask:0xf bank_mask:0xf\n"
    "v_mfma_f32_16x16x32_bf16 v[232:235], a[212:215], a[188:191], v[232:235]\n"
    "v_mul_f32 v245, v245, v254\n"
    "v_mul_f32 v247, v247, v255\n"
    "v_mfma_f32_16x16x32_bf16 v[232:235], a[208:211], a[156:159], v[232:235]\n"
    "s_waitcnt vmcnt(8)\n"
    "buffer_load_dwordx4 a[208:211], v240, s[20:23], 0 offen offset:0 sc1\n"
    "v_mfma_f32_16x16x32_bf16 v[236:239], a[216:219], a[156:159], v[236:239]\n"
    "buffer_load_dwordx4 a[212:215], v240, s[20:23], 0 offen offset:1024 sc1\n"
    "v_mfma_f32_16x16x32_bf16 v[236:239], a[220:223], a[188:191], v[236:239]\n"
    "buffer_store_dwordx4 v[248:251], v240, s[96:99], 0 offen sc1\n"
    "ds_write_b128 v243, v[248:251] offset:0\n"
    "v_mfma_f32_16x16x32_bf16 v[204:207], a[220:223], a[184:187], v[204:207]\n"
    "v_cvt_pk_bf16_f32 v248, v244, v245\n"
    "v_cvt_pk_bf16_f32 v249, v246, v247\n"
    "v_mfma_f32_16x16x32_bf16 v[204:207], a[216:219], a[152:155], v[204:207]\n"
    "v_accvgpr_read_b32 v245, a6\n"
    "v_accvgpr_read_b32 v247, a7\n"
    "v_mfma_f32_16x16x32_bf16 v[172:175], a[216:219], a[148:151], v[172:175]\n"
    "v_lshlrev_b32 v244, 16, v245\n"
    "v_lshlrev_b32 v246, 16, v247\n"
    "v_mfma_f32_16x16x32_bf16 v[172:175], a[220:223], a[180:183], v[172:175]\n"
    "v_and_b32 v245, s58, v245\n"
    "v_and_b32 v247, s58, v247\n"
    "v_mfma_f32_16x16x32_bf16 v[140:143], a[220:223], a[176:179], v[140:143]\n"
    "s_xor_b32 s96, s96, 0x800\n"
    "v_add_f32_dpp v254, v253, v36 row_newbcast:4 row_mask:0xf bank_mask:0xf\n"
    "v_add_f32_dpp v255, v253, v38 row_newbcast:6 row_mask:0xf bank_mask:0xf\n"
    "v_mfma_f32_16x16x32_bf16 v[140:143], a[216:219], a[144:147], v[140:143]\n"
    "v_mul_f32 v244, v244, v254\n"
    "v_mul_f32 v246, v246, v255\n"
    "v_mfma_f32_16x16x32_bf16 v[108:111], a[216:219], a[140:143], v[108:111]\n"
    "v_add_f32_dpp v254, v253, v37 row_newbcast:5 row_mask:0xf bank_mask:0xf\n"
    "v_add_f32_dpp v255, v253, v39 row_newbcast:7 row_mask:0xf bank_mask:0xf\n"
    "v_mfma_f32_16x16x32_bf16 v[108:111], a[220:223], a[172:175], v[108:111]\n"
    "v_mul_f32 v245, v245, v254\n"
    "v_mul_f32 v247, v247, v255\n"
    "v_mfma_f32_16x16x32_bf16 v[76:79], a[220:223], a[168:171], v[76:79]\n"
    "v_cvt_pk_bf16_f32 v250, v244, v245\n"
    "v_cvt_pk_bf16_f32 v251, v246, v247\n"
    "v_mfma_f32_16x16x32_bf16 v[76:79], a[216:219], a[136:139], v[76:79]\n"
    "v_accvgpr_read_b32 v245, a8\n"
    "v_accvgpr_read_b32 v247, a9\n"
    "v_mfma_f32_16x16x32_bf16 v[44:47], a[216:219], a[132:135], v[44:47]\n"
    "v_lshlrev_b32 v244, 16, v245\n"
    "v_lshlrev_b32 v246, 16, v247\n"
    "v_mfma_f32_16x16x32_bf16 v[44:47], a[220:223], a[164:167], v[44:47]\n"
    "v_and_b32 v245, s58, v245\n"
    "v_and_b32 v247, s58, v247\n"
    "v_mfma_f32_16x16x32_bf16 v[12:15], a[220:223], a[160:163], v[12:15]\n"
    "v_add_f32_dpp v254, v253, v64 row_newbcast:0 row_mask:0xf bank_mask:0xf\n"
    "v_add_f32_dpp v255, v253, v66 row_newbcast:2 row_mask:0xf bank_mask:0xf\n"
    "v_mfma_f32_16x16x32_bf16 v[12:15], a[216:219], a[128:131], v[12:15]\n"
    "s_waitcnt vmcnt(9)\n"
    "buffer_load_dwordx4 a[216:219], v240, s[20:23], 0 offen offset:2048 sc1\n"
    "v_mfma_f32_16x16x32_bf16 v[16:19], a[224:227], a[128:131], v[16:19]\n"
    "buffer_load_dwordx4 a[220:223], v240, s[20:23], 0 offen offset:3072 sc1\n"
    "s_addk_i32 s20, 4096\n"
    "v_mfma_f32_16x16x32_bf16 v[16:19], a[228:231], a[160:163], v[16:19]\n"
    "v_mul_f32 v244, v244, v254\n"
    "v_mul_f32 v246, v246, v255\n"
    "v_mfma_f32_16x16x32_bf16 v[48:51], a[228:231], a[164:167], v[48:51]\n"
    "v_add_f32_dpp v254, v253, v65 row_newbcast:1 row_mask:0xf bank_mask:0xf\n"
    "v_add_f32_dpp v255, v253, v67 row_newbcast:3 row_mask:0xf bank_mask:0xf\n"
    "v_mfma_f32_16x16x32_bf16 v[48:51], a[224:227], a[132:135], v[48:51]\n"
    "v_mul_f32 v245, v245, v254\n"
    "v_mul_f32 v247, v247, v255\n"
    "v_mfma_f32_16x16x32_bf16 v[80:83], a[224:227], a[136:139], v[80:83]\n"
    "v_mfma_f32_16x16x32_bf16 v[80:83], a[228:231], a[168:171], v[80:83]\n"
    "buffer_store_dwordx4 v[248:251], v240, s[96:99], 0 offen sc1\n"
    "ds_write_b128 v243, v[248:251] offset:2048\n"
    "v_mfma_f32_16x16x32_bf16 v[112:115], a[228:231], a[172:175], v[112:115]\n"
    "v_cvt_pk_bf16_f32 v248, v244, v245\n"
    "v_cvt_pk_bf16_f32 v249, v246, v247\n"
    "v_mfma_f32_16x16x32_bf16 v[112:115], a[224:227], a[140:143], v[112:115]\n"
    "v_accvgpr_read_b32 v245, a10\n"
    "v_accvgpr_read_b32 v247, a11\n"
    "v_mfma_f32_16x16x32_bf16 v[144:147], a[224:227], a[144:147], v[144:147]\n"
    "v_lshlrev_b32 v244, 16, v245\n"
    "v_lshlrev_b32 v246, 16, v247\n"
    "v_mfma_f32_16x16x32_bf16 v[144:147], a[228:231], a[176:179], v[144:147]\n"
    "v_and_b32 v245, s58, v245\n"
    "v_and_b32 v247, s58, v247\n"
    "v_mfma_f32_16x16x32_bf16 v[176:179], a[228:231], a[180:183], v[176:179]\n"
    "s_xor_b32 s96, s96, 0x101800\n"
    "v_add_f32_dpp v254, v253, v68 row_newbcast:4 row_mask:0xf bank_mask:0xf\n"
    "v_add_f32_dpp v255, v253, v70 row_newbcast:6 row_mask:0xf bank_mask:0xf\n"
    "v_mfma_f32_16x16x32_bf16 v[176:179], a[224:227], a[148:151], v[176:179]\n"
    "v_mul_f32 v244, v244, v254\n"
    "v_mul_f32 v246, v246, v255\n"
    "v_mfma_f32_16x16x32_bf16 v[208:211], a[224:227], a[152:155], v[208:211]\n"
    "v_add_f32_dpp v254, v253, v69 row_newbcast:5 row_mask:0xf bank_mask:0xf\n"
    "v_add_f32_dpp v255, v253, v71 row_newbcast:7 row_mask:0xf bank_mask:0xf\n"
    "v_mfma_f32_16x16x32_bf16 v[208:211], a[228:231], a[184:187], v[208:211]\n"
    "v_mul_f32 v245, v245, v254\n"
    "v_mul_f32 v247, v247, v255\n"
    "v_mfma_f32_16x16x32_bf16 a[240:243], a[228:231], a[188:191], a[240:243]\n"
    "v_cvt_pk_bf16_f32 v250, v244, v245\n"
    "v_cvt_pk_bf16_f32 v251, v246, v247\n"
    "v_mfma_f32_16x16x32_bf16 a[240:243], a[224:227], a[156:159], a[240:243]\n"
    "s_waitcnt vmcnt(10)\n"
    "buffer_load_dwordx4 a[224:227], v240, s[20:23], 0 offen offset:0 sc1\n"
    "v_mfma_f32_16x16x32_bf16 a[244:247], a[232:235], a[156:159], a[244:247]\n"
    "buffer_load_dwordx4 a[228:231], v240, s[20:23], 0 offen offset:1024 sc1\n"
    "v_accvgpr_read_b32 v245, a12\n"
    "v_mfma_f32_16x16x32_bf16 a[244:247], a[236:239], a[188:191], a[244:247]\n"
    "v_accvgpr_read_b32 v247, a13\n"
    "v_lshlrev_b32 v244, 16, v245\n"
    "v_mfma_f32_16x16x32_bf16 v[212:215], a[236:239], a[184:187], v[212:215]\n"
    "v_lshlrev_b32 v246, 16, v247\n"
    "v_and_b32 v245, s58, v245\n"
    "v_mfma_f32_16x16x32_bf16 v[212:215], a[232:235], a[152:155], v[212:215]\n"
    "v_and_b32 v247, s58, v247\n"
    "v_add_f32_dpp v254, v253, v96 row_newbcast:0 row_mask:0xf bank_mask:0xf\n"
    "v_mfma_f32_16x16x32_bf16 v[180:183], a[232:235], a[148:151], v[180:183]\n"
    "v_add_f32_dpp v255, v253, v98 row_newbcast:2 row_mask:0xf bank_mask:0xf\n"
    "v_mul_f32 v244, v244, v254\n"
    "v_mfma_f32_16x16x32_bf16 v[180:183], a[236:239], a[180:183], v[180:183]\n"
    "v_mul_f32 v246, v246, v255\n"
    "v_add_f32_dpp v254, v253, v97 row_newbcast:1 row_mask:0xf bank_mask:0xf\n"
    "v_mfma_f32_16x16x32_bf16 v[148:151], a[236:239], a[176:179], v[148:151]\n"
    "v_add_f32_dpp v255, v253, v99 row_newbcast:3 row_mask:0xf bank_mask:0xf\n"
    "v_mul_f32 v245, v245, v254\n"
    "v_mfma_f32_16x16x32_bf16 v[148:151], a[232:235], a[144:147], v[148:151]\n"
    "v_mul_f32 v247, v247, v255\n"
    "v_mfma_f32_16x16x32_bf16 v[116:119], a[232:235], a[140:143], v[116:119]\n"
    "v_mfma_f32_16x16x32_bf16 v[116:119], a[236:239], a[172:175], v[116:119]\n"
    "buffer_store_dwordx4 v[248:251], v240, s[96:99], 0 offen sc1\n"
    "ds_write_b128 v243, v[248:251] offset:4096\n"
    "v_mfma_f32_16x16x32_bf16 v[84:87], a[236:239], a[168:171], v[84:87]\n"
    "v_cvt_pk_bf16_f32 v248, v244, v245\n"
    "v_cvt_pk_bf16_f32 v249, v246, v247\n"
    "v_mfma_f32_16x16x32_bf16 v[84:87], a[232:235], a[136:139], v[84:87]\n"
    "v_accvgpr_read_b32 v245, a14\n"
    "v_accvgpr_read_b32 v247, a15\n"
    "v_mfma_f32_16x16x32_bf16 v[52:55], a[232:235], a[132:135], v[52:55]\n"
    "v_lshlrev_b32 v244, 16, v245\n"
    "v_lshlrev_b32 v246, 16, v247\n"
    "v_mfma_f32_16x16x32_bf16 v[52:55], a[236:239], a[164:167], v[52:55]\n"
    "v_and_b32 v245, s58, v245\n"
    "v_and_b32 v247, s58, v247\n"
    "v_mfma_f32_16x16x32_bf16 v[20:23], a[236:239], a[160:163], v[20:23]\n"
    "s_xor_b32 s96, s96, 0x800\n"
    "v_add_f32_dpp v254, v253, v100 row_newbcast:4 row_mask:0xf bank_mask:0xf\n"
    "v_add_f32_dpp v255, v253, v102 row_newbcast:6 row_mask:0xf bank_mask:0xf\n"
    "v_mfma_f32_16x16x32_bf16 v[20:23], a[232:235], a[128:131], v[20:23]\n"
    "s_waitcnt vmcnt(11)\n"
    "buffer_load_dwordx4 a[232:235], v240, s[20:23], 0 offen offset:2048 sc1\n"
    "v_mfma_f32_16x16x32_bf16 v[24:27], a[192:195], a[128:131], v[24:27]\n"
    "buffer_load_dwordx4 a[236:239], v240, s[20:23], 0 offen offset:3072 sc1\n"
    "s_addk_i32 s20, 4096\n"
    "v_mfma_f32_16x16x32_bf16 v[24:27], a[196:199], a[160:163], v[24:27]\n"
    "v_mul_f32 v244, v244, v254\n"
    "v_mul_f32 v246, v246, v255\n"
    "v_mfma_f32_16x16x32_bf16 v[56:59], a[196:199], a[164:167], v[56:59]\n"
    "v_add_f32_dpp v254, v253, v101 row_newbcast:5 row_mask:0xf bank_mask:0xf\n"
    "v_add_f32_dpp v255, v253, v103 row_newbcast:7 row_mask:0xf bank_mask:0xf\n"
    "v_mfma_f32_16x16x32_bf16 v[56:59], a[192:195], a[132:135], v[56:59]\n"
    "v_mul_f32 v245, v245, v254\n"
    "v_mul_f32 v247, v247, v255\n"
    "v_mfma_f32_16x16x32_bf16 v[88:91], a[192:195], a[136:139], v[88:91]\n"
    "v_cvt_pk_bf16_f32 v250, v244, v245\n"
    "v_cvt_pk_bf16_f32 v251, v246, v247\n"
    "v_mfma_f32_16x16x32_bf16 v[88:91], a[196:199], a[168:171], v[88:91]\n"
    "v_accvgpr_read_b32 v245, a16\n"
    "v_accvgpr_read_b32 v247, a17\n"
    "v_mfma_f32_16x16x32_bf16 v[120:123], a[196:199], a[172:175], v[120:123]\n"
    "v_lshlrev_b32 v244, 16, v245\n"
    "v_lshlrev_b32 v246, 16, v247\n"
    "v_mfma_f32_16x16x32_bf16 v[120:123], a[192:195], a[140:143], v[120:123]\n"
    "v_and_b32 v245, s58, v245\n"
    "v_and_b32 v247, s58, v247\n"
    "v_mfma_f32_16x16x32_bf16 v[152:155], a[192:195], a[144:147], v[152:155]\n"
    "v_add_f32_dpp v254, v253, v128 row_newbcast:0 row_mask:0xf bank_mask:0xf\n"
    "v_add_f32_dpp v255, v253, v130 row_newbcast:2 row_mask:0xf bank_mask:0xf\n"
    "v_mfma_f32_16x16x32_bf16 v[152:155], a[196:199], a[176:179], v[152:155]\n"
    "v_mul_f32 v244, v244, v254\n"
    "v_mul_f32 v246, v246, v255\n"
    "v_mfma_f32_16x16x32_bf16 v[184:187], a[196:199], a[180:183], v[184:187]\n"
    "v_add_f32_dpp v254, v253, v129 row_newbcast:1 row_mask:0xf bank_mask:0xf\n"
    "v_add_f32_dpp v255, v253, v131 row_newbcast:3 row_mask:0xf bank_mask:0xf\n"
    "v_mfma_f32_16x16x32_bf16 v[184:187], a[192:195], a[148:151], v[184:187]\n"
    "v_mul_f32 v245, v245, v254\n"
    "v_mul_f32 v247, v247, v255\n"
    "v_mfma_f32_16x16x32_bf16 v[216:219], a[192:195], a[152:155], v[216:219]\n"
    "v_mfma_f32_16x16x32_bf16 v[216:219], a[196:199], a[184:187], v[216:219]\n"
    "buffer_store_dwordx4 v[248:251], v240, s[96:99], 0 offen sc1\n"
    "ds_write_b128 v243, v[248:251] offset:6144\n"
    "v_mfma_f32_16x16x32_bf16 a[248:251], a[196:199], a[188:191], a[248:251]\n"
    "v_cvt_pk_bf16_f32 v248, v244, v245\n"
    "v_cvt_pk_bf16_f32 v249, v246, v247\n"
    "v_mfma_f32_16x16x32_bf16 a[248:251], a[192:195], a[156:159], a[248:251]\n"
    "s_waitcnt vmcnt(12)\n"
    "buffer_load_dwordx4 a[192:195], v240, s[20:23], 0 offen offset:0 sc1\n"
    "v_mfma_f32_16x16x32_bf16 a[252:255], a[200:203], a[156:159], a[252:255]\n"
    "buffer_load_dwordx4 a[196:199], v240, s[20:23], 0 offen offset:1024 sc1\n"
    "v_accvgpr_read_b32 v245, a18\n"
    "v_mfma_f32_16x16x32_bf16 a[252:255], a[204:207], a[188:191], a[252:255]\n"
    "v_accvgpr_read_b32 v247, a19\n"
    "v_lshlrev_b32 v244, 16, v245\n"
    "v_mfma_f32_16x16x32_bf16 v[220:223], a[204:207], a[184:187], v[220:223]\n"
    "v_add_u32 v241, s57, v240\n"
    "v_lshlrev_b32 v246, 16, v247\n"
    "v_mfma_f32_16x16x32_bf16 v[220:223], a[200:203], a[152:155], v[220:223]\n"
    "ds_read_b128 a[156:159], v241 offset:14336\n"
    "v_and_b32 v245, s58, v245\n"
    "v_mfma_f32_16x16x32_bf16 v[188:191], a[200:203], a[148:151], v[188:191]\n"
    "ds_read_b128 a[188:191], v241 offset:15360\n"
    "v_and_b32 v247, s58, v247\n"
    "v_mfma_f32_16x16x32_bf16 v[188:191], a[204:207], a[180:183], v[188:191]\n"
    "ds_read_b128 a[184:187], v241 offset:13312\n"
    "v_add_f32_dpp v254, v253, v132 row_newbcast:4 row_mask:0xf bank_mask:0xf\n"
    "v_mfma_f32_16x16x32_bf16 v[156:159], a[204:207], a[176:179], v[156:159]\n"
    "ds_read_b128 a[152:155], v241 offset:12288\n"
    "v_add_f32_dpp v255, v253, v134 row_newbcast:6 row_mask:0xf bank_mask:0xf\n"
    "v_mfma_f32_16x16x32_bf16 v[156:159], a[200:203], a[144:147], v[156:159]\n"
    "ds_read_b128 a[148:151], v241 offset:10240\n"
    "s_xor_b32 s96, s96, 0x185800\n"
    "v_mfma_f32_16x16x32_bf16 v[124:127], a[200:203], a[140:143], v[124:127]\n"
    "ds_read_b128 a[180:183], v241 offset:11264\n"
    "v_mul_f32 v244, v244, v254\n"
    "v_mfma_f32_16x16x32_bf16 v[124:127], a[204:207], a[172:175], v[124:127]\n"
    "ds_read_b128 a[176:179], v241 offset:9216\n"
    "v_mul_f32 v246, v246, v255\n"
    "v_mfma_f32_16x16x32_bf16 v[92:95], a[204:207], a[168:171], v[92:95]\n"
    "ds_read_b128 a[144:147], v241 offset:8192\n"
    "v_add_f32_dpp v254, v253, v133 row_newbcast:5 row_mask:0xf bank_mask:0xf\n"
    "v_mfma_f32_16x16x32_bf16 v[92:95], a[200:203], a[136:139], v[92:95]\n"
    "ds_read_b128 a[140:143], v241 offset:6144\n"
    "v_add_f32_dpp v255, v253, v135 row_newbcast:7 row_mask:0xf bank_mask:0xf\n"
    "v_mfma_f32_16x16x32_bf16 v[60:63], a[200:203], a[132:135], v[60:63]\n"
    "ds_read_b128 a[172:175], v241 offset:7168\n"
    "v_mul_f32 v245, v245, v254\n"
    "v_mfma_f32_16x16x32_bf16 v[60:63], a[204:207], a[164:167], v[60:63]\n"
    "ds_read_b128 a[168:171], v241 offset:5120\n"
    "v_mul_f32 v247, v247, v255\n"
    "v_mfma_f32_16x16x32_bf16 v[28:31], a[204:207], a[160:163], v[28:31]\n"
    "ds_read_b128 a[136:139], v241 offset:4096\n"
    "v_cvt_pk_bf16_f32 v250, v244, v245\n"
    "v_mfma_f32_16x16x32_bf16 v[28:31], a[200:203], a[128:131], v[28:31]\n"
    "s_waitcnt vmcnt(12) lgkmcnt(10)\n"
    "ds_read_b128 a[132:135], v241 offset:2048\n"
    "v_cvt_pk_bf16_f32 v251, v246, v247\n"
    "v_mfma_f32_16x16x32_bf16 a[240:243], a[208:211], a[156:159], a[240:243]\n"
    "ds_read_b128 a[164:167], v241 offset:3072\n"
    "v_accvgpr_read_b32 v245, a20\n"
    "v_mfma_f32_16x16x32_bf16 a[240:243], a[212:215], a[188:191], a[240:243]\n"
    "s_waitcnt lgkmcnt(10)\n"
    "buffer_load_dwordx4 a[200:203], v240, s[20:23], 0 offen offset:2048 sc1\n"
    "v_mfma_f32_16x16x32_bf16 v[208:211], a[212:215], a[184:187], v[208:211]\n"
    "buffer_load_dwordx4 a[204:207], v240, s[20:23], 0 offen offset:3072 sc1\n"
    "s_addk_i32 s20, 4096\n"
    "v_mfma_f32_16x16x32_bf16 v[208:211], a[208:211], a[152:155], v[208:211]\n"
    "s_waitcnt lgkmcnt(8)\n"
    "ds_read_b128 a[160:163], v241 offset:1024\n"
    "v_accvgpr_read_b32 v247, a21\n"
    "v_mfma_f32_16x16x32_bf16 v[176:179], a[208:211], a[148:151], v[176:179]\n"
    "ds_read_b128 a[128:131], v241 offset:0\n"
    "v_lshlrev_b32 v244, 16, v245\n"
    "v_mfma_f32_16x16x32_bf16 v[176:179], a[212:215], a[180:183], v[176:179]\n"
    "s_waitcnt lgkmcnt(8)\n"
    "v_lshlrev_b32 v246, 16, v247\n"
    "v_and_b32 v245, s58, v245\n"
    "v_mfma_f32_16x16x32_bf16 v[144:147], a[212:215], a[176:179], v[144:147]\n"
    "v_and_b32 v247, s58, v247\n"
    "v_add_f32_dpp v254, v253, v160 row_newbcast:0 row_mask:0xf bank_mask:0xf\n"
    "v_mfma_f32_16x16x32_bf16 v[144:147], a[208:211], a[144:147], v[144:147]\n"
    "s_waitcnt lgkmcnt(6)\n"
    "v_add_f32_dpp v255, v253, v162 row_newbcast:2 row_mask:0xf bank_mask:0xf\n"
    "v_mul_f32 v244, v244, v254\n"
    "v_mfma_f32_16x16x32_bf16 v[112:115], a[208:211], a[140:143], v[112:115]\n"
    "v_mul_f32 v246, v246, v255\n"
    "v_add_f32_dpp v254, v253, v161 row_newbcast:1 row_mask:0xf bank_mask:0xf\n"
    "v_mfma_f32_16x16x32_bf16 v[112:115], a[212:215], a[172:175], v[112:115]\n"
    "s_waitcnt lgkmcnt(4)\n"
    "v_add_f32_dpp v255, v253, v163 row_newbcast:3 row_mask:0xf bank_mask:0xf\n"
    "v_mul_f32 v245, v245, v254\n"
    "v_mfma_f32_16x16x32_bf16 v[80:83], a[212:215], a[168:171], v[80:83]\n"
    "v_mul_f32 v247, v247, v255\n"
    "v_mfma_f32_16x16x32_bf16 v[80:83], a[208:211], a[136:139], v[80:83]\n"
    "s_waitcnt lgkmcnt(2)\n"
    "v_mfma_f32_16x16x32_bf16 v[48:51], a[208:211], a[132:135], v[48:51]\n"
    "buffer_store_dwordx4 v[248:251], v240, s[96:99], 0 offen sc1\n"
    "ds_write_b128 v243, v[248:251] offset:8192\n"
    "v_mfma_f32_16x16x32_bf16 v[48:51], a[212:215], a[164:167], v[48:51]\n"
    "s_waitcnt lgkmcnt(1)\n"
    "v_cvt_pk_bf16_f32 v248, v244, v245\n"
    "v_cvt_pk_bf16_f32 v249, v246, v247\n"
    "v_mfma_f32_16x16x32_bf16 v[16:19], a[212:215], a[160:163], v[16:19]\n"
    "v_accvgpr_read_b32 v245, a22\n"
    "v_accvgpr_read_b32 v247, a23\n"
    "v_mfma_f32_16x16x32_bf16 v[16:19], a[208:211], a[128:131], v[16:19]\n"
    "s_waitcnt vmcnt(12)\n"
    "buffer_load_dwordx4 a[208:211], v240, s[20:23], 0 offen offset:0 sc1\n"
    "v_mfma_f32_16x16x32_bf16 v[20:23], a[216:219], a[128:131], v[20:23]\n"
    "buffer_load_dwordx4 a[212:215], v240, s[20:23], 0 offen offset:1024 sc1\n"
    "v_lshlrev_b32 v244, 16, v245\n"
    "v_mfma_f32_16x16x32_bf16 v[20:23], a[220:223], a[160:163], v[20:23]\n"
    "v_lshlrev_b32 v246, 16, v247\n"
    "v_and_b32 v245, s58, v245\n"
    "v_mfma_f32_16x16x32_bf16 v[52:55], a[220:223], a[164:167], v[52:55]\n"
    "v_and_b32 v247, s58, v247\n"
    "v_add_f32_dpp v254, v253, v164 row_newbcast:4 row_mask:0xf bank_mask:0xf\n"
    "v_mfma_f32_16x16x32_bf16 v[52:55], a[216:219], a[132:135], v[52:55]\n"
    "s_xor_b32 s96, s96, 0x800\n"
    "v_add_f32_dpp v255, v253, v166 row_newbcast:6 row_mask:0xf bank_mask:0xf\n"
    "v_mul_f32 v244, v244, v254\n"
    "v_mfma_f32_16x16x32_bf16 v[84:87], a[216:219], a[136:139], v[84:87]\n"
    "v_mul_f32 v246, v246, v255\n"
    "v_add_f32_dpp v254, v253, v165 row_newbcast:5 row_mask:0xf bank_mask:0xf\n"
    "v_mfma_f32_16x16x32_bf16 v[84:87], a[220:223], a[168:171], v[84:87]\n"
    "v_add_f32_dpp v255, v253, v167 row_newbcast:7 row_mask:0xf bank_mask:0xf\n"
    "v_mul_f32 v245, v245, v254\n"
    "v_mfma_f32_16x16x32_bf16 v[116:119], a[220:223], a[172:175], v[116:119]\n"
    "v_mul_f32 v247, v247, v255\n"
    "v_cvt_pk_bf16_f32 v250, v244, v245\n"
    "v_mfma_f32_16x16x32_bf16 v[116:119], a[216:219], a[140:143], v[116:119]\n"
    "v_cvt_pk_bf16_f32 v251, v246, v247\n"
    "v_accvgpr_read_b32 v245, a24\n"
    "v_mfma_f32_16x16x32_bf16 v[148:151], a[216:219], a[144:147], v[148:151]\n"
    "v_accvgpr_read_b32 v247, a25\n"
    "v_lshlrev_b32 v244, 16, v245\n"
    "v_mfma_f32_16x16x32_bf16 v[148:151], a[220:223], a[176:179], v[148:151]\n"
    "v_lshlrev_b32 v246, 16, v247\n"
    "v_and_b32 v245, s58, v245\n"
    "v_mfma_f32_16x16x32_bf16 v[180:183], a[220:223], a[180:183], v[180:183]\n"
    "v_and_b32 v247, s58, v247\n"
    "v_add_f32_dpp v254, v253, v192 row_newbcast:0 row_mask:0xf bank_mask:0xf\n"
    "v_mfma_f32_16x16x32_bf16 v[180:183], a[216:219], a[148:151], v[180:183]\n"
    "v_add_f32_dpp v255, v253, v194 row_newbcast:2 row_mask:0xf bank_mask:0xf\n"
    "v_mul_f32 v244, v244, v254\n"
    "v_mfma_f32_16x16x32_bf16 v[212:215], a[216:219], a[152:155], v[212:215]\n"
    "v_mul_f32 v246, v246, v255\n"
    "v_add_f32_dpp v254, v253, v193 row_newbcast:1 row_mask:0xf bank_mask:0xf\n"
    "v_mfma_f32_16x16x32_bf16 v[212:215], a[220:223], a[184:187], v[212:215]\n"
    "v_add_f32_dpp v255, v253, v195 row_newbcast:3 row_mask:0xf bank_mask:0xf\n"
    "v_mul_f32 v245, v245, v254\n"
    "v_mfma_f32_16x16x32_bf16 a[244:247], a[220:223], a[188:191], a[244:247]\n"
    "v_mul_f32 v247, v247, v255\n"
    "v_mfma_f32_16x16x32_bf16 a[244:247], a[216:219], a[156:159], a[244:247]\n"
    "s_waitcnt vmcnt(11)\n"
    "buffer_load_dwordx4 a[216:219], v240, s[20:23], 0 offen offset:2048 sc1\n"
    "v_mfma_f32_16x16x32_bf16 a[248:251], a[224:227], a[156:159], a[248:251]\n"
    "buffer_load_dwordx4 a[220:223], v240, s[20:23], 0 offen offset:3072 sc1\n"
    "s_addk_i32 s20, 4096\n"
    "v_mfma_f32_16x16x32_bf16 a[248:251], a[228:231], a[188:191], a[248:251]\n"
    "buffer_store_dwordx4 v[248:251], v240, s[96:99], 0 offen sc1\n"
    "ds_write_b128 v243, v[248:251] offset:10240\n"
    "v_mfma_f32_16x16x32_bf16 v[216:219], a[228:231], a[184:187], v[216:219]\n"
    "v_cvt_pk_bf16_f32 v248, v244, v245\n"
    "v_cvt_pk_bf16_f32 v249, v246, v247\n"
    "v_mfma_f32_16x16x32_bf16 v[216:219], a[224:227], a[152:155], v[216:219]\n"
    "v_accvgpr_read_b32 v245, a26\n"
    "v_accvgpr_read_b32 v247, a27\n"
    "v_mfma_f32_16x16x32_bf16 v[184:187], a[224:227], a[148:151], v[184:187]\n"
    "v_lshlrev_b32 v244, 16, v245\n"
    "v_lshlrev_b32 v246, 16, v247\n"
    "v_mfma_f32_16x16x32_bf16 v[184:187], a[228:231], a[180:183], v[184:187]\n"
    "v_and_b32 v245, s58, v245\n"
    "v_and_b32 v247, s58, v247\n"
    "v_mfma_f32_16x16x32_bf16 v[152:155], a[228:231], a[176:179], v[152:155]\n"
    "s_xor_b32 s96, s96, 0x101800\n"
    "v_add_f32_dpp v254, v253, v196 row_newbcast:4 row_mask:0xf bank_mask:0xf\n"
    "v_add_f32_dpp v255, v253, v198 row_newbcast:6 row_mask:0xf bank_mask:0xf\n"
    "v_mfma_f32_16x16x32_bf16 v[152:155], a[224:227], a[144:147], v[152:155]\n"
    "v_mul_f32 v244, v244, v254\n"
    "v_mul_f32 v246, v246, v255\n"
    "v_mfma_f32_16x16x32_bf16 v[120:123], a[224:227], a[140:143], v[120:123]\n"
    "v_add_f32_dpp v254, v253, v197 row_newbcast:5 row_mask:0xf bank_mask:0xf\n"
    "v_add_f32_dpp v255, v253, v199 row_newbcast:7 row_mask:0xf bank_mask:0xf\n"
    "v_mfma_f32_16x16x32_bf16 v[120:123], a[228:231], a[172:175], v[120:123]\n"
    "v_mul_f32 v245, v245, v254\n"
    "v_mul_f32 v247, v247, v255\n"
    "v_mfma_f32_16x16x32_bf16 v[88:91], a[228:231], a[168:171], v[88:91]\n"
    "v_cvt_pk_bf16_f32 v250, v244, v245\n"
    "v_cvt_pk_bf16_f32 v251, v246, v247\n"
    "v_mfma_f32_16x16x32_bf16 v[88:91], a[224:227], a[136:139], v[88:91]\n"
    "v_accvgpr_read_b32 v245, a28\n"
    "v_accvgpr_read_b32 v247, a29\n"
    "v_mfma_f32_16x16x32_bf16 v[56:59], a[224:227], a[132:135], v[56:59]\n"
    "v_lshlrev_b32 v244, 16, v245\n"
    "v_lshlrev_b32 v246, 16, v247\n"
    "v_mfma_f32_16x16x32_bf16 v[56:59], a[228:231], a[164:167], v[56:59]\n"
    "v_and_b32 v245, s58, v245\n"
    "v_and_b32 v247, s58, v247\n"
    "v_mfma_f32_16x16x32_bf16 v[24:27], a[228:231], a[160:163], v[24:27]\n"
    "v_add_f32_dpp v254, v253, v224 row_newbcast:0 row_mask:0xf bank_mask:0xf\n"
    "v_add_f32_dpp v255, v253, v226 row_newbcast:2 row_mask:0xf bank_mask:0xf\n"
    "v_mfma_f32_16x16x32_bf16 v[24:27], a[224:227], a[128:131], v[24:27]\n"
    "s_waitcnt vmcnt(11)\n"
    "buffer_load_dwordx4 a[224:227], v240, s[20:23], 0 offen offset:0 sc1\n"
    "v_mfma_f32_16x16x32_bf16 v[28:31], a[232:235], a[128:131], v[28:31]\n"
    "buffer_load_dwordx4 a[228:231], v240, s[20:23], 0 offen offset:1024 sc1\n"
    "v_mul_f32 v244, v244, v254\n"
    "v_mfma_f32_16x16x32_bf16 v[28:31], a[236:239], a[160:163], v[28:31]\n"
    "v_mul_f32 v246, v246, v255\n"
    "v_add_f32_dpp v254, v253, v225 row_newbcast:1 row_mask:0xf bank_mask:0xf\n"
    "v_mfma_f32_16x16x32_bf16 v[60:63], a[236:239], a[164:167], v[60:63]\n"
    "ds_read_b128 a[128:131], v241 offset:16384\n"
    "v_add_f32_dpp v255, v253, v227 row_newbcast:3 row_mask:0xf bank_mask:0xf\n"
    "v_mfma_f32_16x16x32_bf16 v[60:63], a[232:235], a[132:135], v[60:63]\n"
    "ds_read_b128 a[160:163], v241 offset:17408\n"
    "v_mul_f32 v245, v245, v254\n"
    "v_mfma_f32_16x16x32_bf16 v[92:95], a[232:235], a[136:139], v[92:95]\n"
    "ds_read_b128 a[164:167], v241 offset:19456\n"
    "v_mul_f32 v247, v247, v255\n"
    "v_mfma_f32_16x16x32_bf16 v[92:95], a[236:239], a[168:171], v[92:95]\n"
    "buffer_store_dwordx4 v[248:251], v240, s[96:99], 0 offen sc1\n"
    "ds_write_b128 v243, v[248:251] offset:12288\n"
    "v_mfma_f32_16x16x32_bf16 v[124:127], a[236:239], a[172:175], v[124:127]\n"
    "ds_read_b128 a[132:135], v241 offset:18432\n"
    "v_cvt_pk_bf16_f32 v248, v244, v245\n"
    "v_mfma_f32_16x16x32_bf16 v[124:127], a[232:235], a[140:143], v[124:127]\n"
    "ds_read_b128 a[136:139], v241 offset:20480\n"
    "v_cvt_pk_bf16_f32 v249, v246, v247\n"
    "v_mfma_f32_16x16x32_bf16 v[156:159], a[232:235], a[144:147], v[156:159]\n"
    "ds_read_b128 a[168:171], v241 offset:21504\n"
    "v_accvgpr_read_b32 v245, a30\n"
    "v_mfma_f32_16x16x32_bf16 v[156:159], a[236:239], a[176:179], v[156:159]\n"
    "ds_read_b128 a[172:175], v241 offset:23552\n"
    "v_accvgpr_read_b32 v247, a31\n"
    "v_mfma_f32_16x16x32_bf16 v[188:191], a[236:239], a[180:183], v[188:191]\n"
    "ds_read_b128 a[140:143], v241 offset:22528\n"
    "v_lshlrev_b32 v244, 16, v245\n"
    "v_mfma_f32_16x16x32_bf16 v[188:191], a[232:235], a[148:151], v[188:191]\n"
    "ds_read_b128 a[144:147], v241 offset:24576\n"
    "v_lshlrev_b32 v246, 16, v247\n"
    "v_mfma_f32_16x16x32_bf16 v[220:223], a[232:235], a[152:155], v[220:223]\n"
    "ds_read_b128 a[176:179], v241 offset:25600\n"
    "v_and_b32 v245, s58, v245\n"
    "v_mfma_f32_16x16x32_bf16 v[220:223], a[236:239], a[184:187], v[220:223]\n"
    "ds_read_b128 a[180:183], v241 offset:27648\n"
    "v_and_b32 v247, s58, v247\n"
    "v_mfma_f32_16x16x32_bf16 a[252:255], a[236:239], a[188:191], a[252:255]\n"
    "ds_read_b128 a[148:151], v241 offset:26624\n"
    "v_add_f32_dpp v254, v253, v228 row_newbcast:4 row_mask:0xf bank_mask:0xf\n"
    "v_mfma_f32_16x16x32_bf16 a[252:255], a[232:235], a[156:159], a[252:255]\n"
    "s_waitcnt vmcnt(11) lgkmcnt(11)\n"
    "ds_read_b128 a[152:155], v241 offset:28672\n"
    "v_add_f32_dpp v255, v253, v230 row_newbcast:6 row_mask:0xf bank_mask:0xf\n"
    "v_mfma_f32_16x16x32_bf16 v[16:19], a[192:195], a[128:131], v[16:19]\n"
    "ds_read_b128 a[184:187], v241 offset:29696\n"
    "v_mul_f32 v244, v244, v254\n"
    "v_mfma_f32_16x16x32_bf16 v[16:19], a[196:199], a[160:163], v[16:19]\n"
    "s_waitcnt lgkmcnt(10)\n"
    "buffer_load_dwordx4 a[232:235], v240, s[20:23], 0 offen offset:2048 sc1\n"
    "v_mfma_f32_16x16x32_bf16 v[48:51], a[196:199], a[164:167], v[48:51]\n"
    "buffer_load_dwordx4 a[236:239], v240, s[20:23], 0 offen offset:3072 sc1\n"
    "s_addk_i32 s20, 4096\n"
    "v_mfma_f32_16x16x32_bf16 v[48:51], a[192:195], a[132:135], v[48:51]\n"
    "s_waitcnt lgkmcnt(8)\n"
    "s_xor_b32 s96, s96, 0x800\n"
    "ds_read_b128 a[188:191], v241 offset:31744\n"
    "v_mfma_f32_16x16x32_bf16 v[80:83], a[192:195], a[136:139], v[80:83]\n"
    "ds_read_b128 a[156:159], v241 offset:30720\n"
    "v_mul_f32 v246, v246, v255\n"
    "v_mfma_f32_16x16x32_bf16 v[80:83], a[196:199], a[168:171], v[80:83]\n"
    "s_waitcnt lgkmcnt(8)\n"
    "v_add_f32_dpp v254, v253, v229 row_newbcast:5 row_mask:0xf bank_mask:0xf\n"
    "v_add_f32_dpp v255, v253, v231 row_newbcast:7 row_mask:0xf bank_mask:0xf\n"
    "v_mfma_f32_16x16x32_bf16 v[112:115], a[196:199], a[172:175], v[112:115]\n"
    "v_mul_f32 v245, v245, v254\n"
    "v_mul_f32 v247, v247, v255\n"
    "v_mfma_f32_16x16x32_bf16 v[112:115], a[192:195], a[140:143], v[112:115]\n"
    "s_waitcnt lgkmcnt(6)\n"
    "v_cvt_pk_bf16_f32 v250, v244, v245\n"
    "v_cvt_pk_bf16_f32 v251, v246, v247\n"
    "v_mfma_f32_16x16x32_bf16 v[144:147], a[192:195], a[144:147], v[144:147]\n"
    "v_mfma_f32_16x16x32_bf16 v[144:147], a[196:199], a[176:179], v[144:147]\n"
    "s_waitcnt lgkmcnt(4)\n"
    "v_mfma_f32_16x16x32_bf16 v[176:179], a[196:199], a[180:183], v[176:179]\n"
    "buffer_store_dwordx4 v[248:251], v240, s[96:99], 0 offen sc1\n"
    "ds_write_b128 v243, v[248:251] offset:14336\n"
    "v_mfma_f32_16x16x32_bf16 v[176:179], a[192:195], a[148:151], v[176:179]\n"
    "s_waitcnt lgkmcnt(3)\n"
    "v_accvgpr_read_b32 v245, a32\n"
    "v_accvgpr_read_b32 v247, a33\n"
    "v_mfma_f32_16x16x32_bf16 v[208:211], a[192:195], a[152:155], v[208:211]\n"
    "v_lshlrev_b32 v244, 16, v245\n"
    "v_lshlrev_b32 v246, 16, v247\n"
    "v_mfma_f32_16x16x32_bf16 v[208:211], a[196:199], a[184:187], v[208:211]\n"
    "s_waitcnt lgkmcnt(1)\n"
    "v_and_b32 v245, s58, v245\n"
    "v_and_b32 v247, s58, v247\n"
    "v_mfma_f32_16x16x32_bf16 a[240:243], a[196:199], a[188:191], a[240:243]\n"
    "v_add_f32_dpp v254, v253, v8 row_newbcast:8 row_mask:0xf bank_mask:0xf\n"
    "v_add_f32_dpp v255, v253, v10 row_newbcast:10 row_mask:0xf bank_mask:0xf\n"
    "v_mfma_f32_16x16x32_bf16 a[240:243], a[192:195], a[156:159], a[240:243]\n"
    "s_waitcnt vmcnt(12)\n"
    "buffer_load_dwordx4 a[192:195], v240, s[20:23], 0 offen offset:0 sc1\n"
    "v_mfma_f32_16x16x32_bf16 a[244:247], a[200:203], a[156:159], a[244:247]\n"
    "buffer_load_dwordx4 a[196:199], v240, s[20:23], 0 offen offset:1024 sc1\n"
    "v_mul_f32 v244, v244, v254\n"
    "v_mfma_f32_16x16x32_bf16 a[244:247], a[204:207], a[188:191], a[244:247]\n"
    "v_mul_f32 v246, v246, v255\n"
    "v_add_f32_dpp v254, v253, v9 row_newbcast:9 row_mask:0xf bank_mask:0xf\n"
    "v_mfma_f32_16x16x32_bf16 v[212:215], a[204:207], a[184:187], v[212:215]\n"
    "v_add_f32_dpp v255, v253, v11 row_newbcast:11 row_mask:0xf bank_mask:0xf\n"
    "v_mul_f32 v245, v245, v254\n"
    "v_mfma_f32_16x16x32_bf16 v[212:215], a[200:203], a[152:155], v[212:215]\n"
    "v_mul_f32 v247, v247, v255\n"
    "v_cvt_pk_bf16_f32 v248, v244, v245\n"
    "v_mfma_f32_16x16x32_bf16 v[180:183], a[200:203], a[148:151], v[180:183]\n"
    "v_cvt_pk_bf16_f32 v249, v246, v247\n"
    "v_accvgpr_read_b32 v245, a34\n"
    "v_mfma_f32_16x16x32_bf16 v[180:183], a[204:207], a[180:183], v[180:183]\n"
    "v_accvgpr_read_b32 v247, a35\n"
    "v_lshlrev_b32 v244, 16, v245\n"
    "v_mfma_f32_16x16x32_bf16 v[148:151], a[204:207], a[176:179], v[148:151]\n"
    "v_lshlrev_b32 v246, 16, v247\n"
    "v_and_b32 v245, s58, v245\n"
    "v_mfma_f32_16x16x32_bf16 v[148:151], a[200:203], a[144:147], v[148:151]\n"
    "v_and_b32 v247, s58, v247\n"
    "v_add_f32_dpp v254, v253, v12 row_newbcast:12 row_mask:0xf bank_mask:0xf\n"
    "v_mfma_f32_16x16x32_bf16 v[116:119], a[200:203], a[140:143], v[116:119]\n"
    "s_xor_b32 s96, s96, 0x195800\n"
    "v_add_f32_dpp v255, v253, v14 row_newbcast:14 row_mask:0xf bank_mask:0xf\n"
    "v_mul_f32 v244, v244, v254\n"
    "v_mfma_f32_16x16x32_bf16 v[116:119], a[204:207], a[172:175], v[116:119]\n"
    "v_mul_f32 v246, v246, v255\n"
    "v_add_f32_dpp v254, v253, v13 row_newbcast:13 row_mask:0xf bank_mask:0xf\n"
    "v_mfma_f32_16x16x32_bf16 v[84:87], a[204:207], a[168:171], v[84:87]\n"
    "v_add_f32_dpp v255, v253, v15 row_newbcast:15 row_mask:0xf bank_mask:0xf\n"
    "v_mul_f32 v245, v245, v254\n"
    "v_mfma_f32_16x16x32_bf16 v[84:87], a[200:203], a[136:139], v[84:87]\n"
    "v_mul_f32 v247, v247, v255\n"
    "v_cvt_pk_bf16_f32 v250, v244, v245\n"
    "v_mfma_f32_16x16x32_bf16 v[52:55], a[200:203], a[132:135], v[52:55]\n"
    "v_cvt_pk_bf16_f32 v251, v246, v247\n"
    "v_accvgpr_read_b32 v245, a36\n"
    "v_mfma_f32_16x16x32_bf16 v[52:55], a[204:207], a[164:167], v[52:55]\n"
    "v_accvgpr_read_b32 v247, a37\n"
    "v_lshlrev_b32 v244, 16, v245\n"
    "v_mfma_f32_16x16x32_bf16 v[20:23], a[204:207], a[160:163], v[20:23]\n"
    "v_lshlrev_b32 v246, 16, v247\n"
    "v_and_b32 v245, s58, v245\n"
    "v_mfma_f32_16x16x32_bf16 v[20:23], a[200:203], a[128:131], v[20:23]\n"
    "s_waitcnt vmcnt(11)\n"
    "buffer_load_dwordx4 a[200:203], v240, s[20:23], 0 offen offset:2048 sc1\n"
    "v_mfma_f32_16x16x32_bf16 v[24:27], a[208:211], a[128:131], v[24:27]\n"
    "buffer_load_dwordx4 a[204:207], v240, s[20:23], 0 offen offset:3072 sc1\n"
    "s_addk_i32 s20, 4096\n"
    "v_mfma_f32_16x16x32_bf16 v[24:27], a[212:215], a[160:163], v[24:27]\n"
    "v_and_b32 v247, s58, v247\n"
    "v_add_f32_dpp v254, v253, v40 row_newbcast:8 row_mask:0xf bank_mask:0xf\n"
    "v_mfma_f32_16x16x32_bf16 v[56:59], a[212:215], a[164:167], v[56:59]\n"
    "v_add_f32_dpp v255, v253, v42 row_newbcast:10 row_mask:0xf bank_mask:0xf\n"
    "v_mul_f32 v244, v244, v254\n"
    "v_mfma_f32_16x16x32_bf16 v[56:59], a[208:211], a[132:135], v[56:59]\n"
    "buffer_store_dwordx4 v[248:251], v240, s[96:99], 0 offen sc1\n"
    "ds_write_b128 v243, v[248:251] offset:32768\n"
    "v_mfma_f32_16x16x32_bf16 v[88:91], a[208:211], a[136:139], v[88:91]\n"
    "v_mul_f32 v246, v246, v255\n"
    "v_add_f32_dpp v254, v253, v41 row_newbcast:9 row_mask:0xf bank_mask:0xf\n"
    "v_mfma_f32_16x16x32_bf16 v[88:91], a[212:215], a[168:171], v[88:91]\n"
    "v_add_f32_dpp v255, v253, v43 row_newbcast:11 row_mask:0xf bank_mask:0xf\n"
    "v_mul_f32 v245, v245, v254\n"
    "v_mfma_f32_16x16x32_bf16 v[120:123], a[212:215], a[172:175], v[120:123]\n"
    "v_mul_f32 v247, v247, v255\n"
    "v_cvt_pk_bf16_f32 v248, v244, v245\n"
    "v_mfma_f32_16x16x32_bf16 v[120:123], a[208:211], a[140:143], v[120:123]\n"
    "s_xor_b32 s96, s96, 0x800\n"
    "v_cvt_pk_bf16_f32 v249, v246, v247\n"
    "v_accvgpr_read_b32 v245, a38\n"
    "v_mfma_f32_16x16x32_bf16 v[152:155], a[208:211], a[144:147], v[152:155]\n"
    "v_accvgpr_read_b32 v247, a39\n"
    "v_lshlrev_b32 v244, 16, v245\n"
    "v_mfma_f32_16x16x32_bf16 v[152:155], a[212:215], a[176:179], v[152:155]\n"
    "v_lshlrev_b32 v246, 16, v247\n"
    "v_and_b32 v245, s58, v245\n"
    "v_mfma_f32_16x16x32_bf16 v[184:187], a[212:215], a[180:183], v[184:187]\n"
    "v_and_b32 v247, s58, v247\n"
    "v_add_f32_dpp v254, v253, v44 row_newbcast:12 row_mask:0xf bank_mask:0xf\n"
    "v_mfma_f32_16x16x32_bf16 v[184:187], a[208:211], a[148:151], v[184:187]\n"
    "v_add_f32_dpp v255, v253, v46 row_newbcast:14 row_mask:0xf bank_mask:0xf\n"
    "v_mul_f32 v244, v244, v254\n"
    "v_mfma_f32_16x16x32_bf16 v[216:219], a[208:211], a[152:155], v[216:219]\n"
    "v_mul_f32 v246, v246, v255\n"
    "v_add_f32_dpp v254, v253, v45 row_newbcast:13 row_mask:0xf bank_mask:0xf\n"
    "v_mfma_f32_16x16x32_bf16 v[216:219], a[212:215], a[184:187], v[216:219]\n"
    "v_add_f32_dpp v255, v253, v47 row_newbcast:15 row_mask:0xf bank_mask:0xf\n"
    "v_mul_f32 v245, v245, v254\n"
    "v_mfma_f32_16x16x32_bf16 a[248:251], a[212:215], a[188:191], a[248:251]\n"
    "v_mul_f32 v247, v247, v255\n"
    "v_cvt_pk_bf16_f32 v250, v244, v245\n"
    "v_mfma_f32_16x16x32_bf16 a[248:251], a[208:211], a[156:159], a[248:251]\n"
    "s_waitcnt vmcnt(12)\n"
    "buffer_load_dwordx4 a[208:211], v240, s[20:23], 0 offen offset:0 sc1\n"
    "v_mfma_f32_16x16x32_bf16 a[252:255], a[216:219], a[156:159], a[252:255]\n"
    "buffer_load_dwordx4 a[212:215], v240, s[20:23], 0 offen offset:1024 sc1\n"
    "v_cvt_pk_bf16_f32 v251, v246, v247\n"
    "v_mfma_f32_16x16x32_bf16 a[252:255], a[220:223], a[188:191], a[252:255]\n"
    "v_accvgpr_read_b32 v245, a40\n"
    "v_accvgpr_read_b32 v247, a41\n"
    "v_mfma_f32_16x16x32_bf16 v[220:223], a[220:223], a[184:187], v[220:223]\n"
    "ds_read_b128 a[156:159], v241 offset:47104\n"
    "v_lshlrev_b32 v244, 16, v245\n"
    "v_mfma_f32_16x16x32_bf16 v[220:223], a[216:219], a[152:155], v[220:223]\n"
    "ds_read_b128 a[188:191], v241 offset:48128\n"
    "v_lshlrev_b32 v246, 16, v247\n"
    "v_mfma_f32_16x16x32_bf16 v[188:191], a[216:219], a[148:151], v[188:191]\n"
    "ds_read_b128 a[184:187], v241 offset:46080\n"
    "v_and_b32 v245, s58, v245\n"
    "v_and_b32 v247, s58, v247\n"
    "v_mfma_f32_16x16x32_bf16 v[188:191], a[220:223], a[180:183], v[188:191]\n"
    "ds_read_b128 a[152:155], v241 offset:45056\n"
    "v_add_f32_dpp v254, v253, v72 row_newbcast:8 row_mask:0xf bank_mask:0xf\n"
    "v_add_f32_dpp v255, v253, v74 row_newbcast:10 row_mask:0xf bank_mask:0xf\n"
    "v_mfma_f32_16x16x32_bf16 v[156:159], a[220:223], a[176:179], v[156:159]\n"
    "ds_read_b128 a[148:151], v241 offset:43008\n"
    "v_mul_f32 v244, v244, v254\n"
    "v_mul_f32 v246, v246, v255\n"
    "v_add_f32_dpp v254, v253, v73 row_newbcast:9 row_mask:0xf bank_mask:0xf\n"
    "v_mfma_f32_16x16x32_bf16 v[156:159], a[216:219], a[144:147], v[156:159]\n"
    "ds_read_b128 a[180:183], v241 offset:44032\n"
    "v_add_f32_dpp v255, v253, v75 row_newbcast:11 row_mask:0xf bank_mask:0xf\n"
    "v_mul_f32 v245, v245, v254\n"
    "v_mfma_f32_16x16x32_bf16 v[124:127], a[216:219], a[140:143], v[124:127]\n"
    "ds_read_b128 a[176:179], v241 offset:41984\n"
    "v_mul_f32 v247, v247, v255\n"
    "v_mfma_f32_16x16x32_bf16 v[124:127], a[220:223], a[172:175], v[124:127]\n"
    "buffer_store_dwordx4 v[248:251], v240, s[96:99], 0 offen sc1\n"
    "ds_write_b128 v243, v[248:251] offset:34816\n"
    "v_mfma_f32_16x16x32_bf16 v[92:95], a[220:223], a[168:171], v[92:95]\n"
    "ds_read_b128 a[144:147], v241 offset:40960\n"
    "v_cvt_pk_bf16_f32 v248, v244, v245\n"
    "v_mfma_f32_16x16x32_bf16 v[92:95], a[216:219], a[136:139], v[92:95]\n"
    "ds_read_b128 a[140:143], v241 offset:38912\n"
    "v_cvt_pk_bf16_f32 v249, v246, v247\n"
    "v_mfma_f32_16x16x32_bf16 v[60:63], a[216:219], a[132:135], v[60:63]\n"
    "ds_read_b128 a[172:175], v241 offset:39936\n"
    "v_accvgpr_read_b32 v245, a42\n"
    "v_mfma_f32_16x16x32_bf16 v[60:63], a[220:223], a[164:167], v[60:63]\n"
    "ds_read_b128 a[168:171], v241 offset:37888\n"
    "v_accvgpr_read_b32 v247, a43\n"
    "v_mfma_f32_16x16x32_bf16 v[28:31], a[220:223], a[160:163], v[28:31]\n"
    "ds_read_b128 a[136:139], v241 offset:36864\n"
    "v_lshlrev_b32 v244, 16, v245\n"
    "v_mfma_f32_16x16x32_bf16 v[28:31], a[216:219], a[128:131], v[28:31]\n"
    "s_waitcnt vmcnt(12) lgkmcnt(11)\n"
    "ds_read_b128 a[132:135], v241 offset:34816\n"
    "v_lshlrev_b32 v246, 16, v247\n"
    "v_mfma_f32_16x16x32_bf16 a[240:243], a[224:227], a[156:159], a[240:243]\n"
    "ds_read_b128 a[164:167], v241 offset:35840\n"
    "v_and_b32 v245, s58, v245\n"
    "v_mfma_f32_16x16x32_bf16 a[240:243], a[228:231], a[188:191], a[240:243]\n"
    "s_waitcnt lgkmcnt(11)\n"
    "buffer_load_dwordx4 a[216:219], v240, s[20:23], 0 offen offset:2048 sc1\n"
    "v_mfma_f32_16x16x32_bf16 v[208:211], a[228:231], a[184:187], v[208:211]\n"
    "buffer_load_dwordx4 a[220:223], v240, s[20:23], 0 offen offset:3072 sc1\n"
    "s_addk_i32 s20, 4096\n"
    "v_mfma_f32_16x16x32_bf16 v[208:211], a[224:227], a[152:155], v[208:211]\n"
    "s_waitcnt lgkmcnt(9)\n"
    "ds_read_b128 a[160:163], v241 offset:33792\n"
    "v_and_b32 v247, s58, v247\n"
    "v_mfma_f32_16x16x32_bf16 v[176:179], a[224:227], a[148:151], v[176:179]\n"
    "ds_read_b128 a[128:131], v241 offset:32768\n"
    "v_add_f32_dpp v254, v253, v76 row_newbcast:12 row_mask:0xf bank_mask:0xf\n"
    "v_mfma_f32_16x16x32_bf16 v[176:179], a[228:231], a[180:183], v[176:179]\n"
    "s_waitcnt lgkmcnt(8)\n"
    "s_xor_b32 s96, s96, 0x101800\n"
    "v_add_f32_dpp v255, v253, v78 row_newbcast:14 row_mask:0xf bank_mask:0xf\n"
    "v_mfma_f32_16x16x32_bf16 v[144:147], a[228:231], a[176:179], v[144:147]\n"
    "v_mul_f32 v244, v244, v254\n"
    "v_mul_f32 v246, v246, v255\n"
    "v_mfma_f32_16x16x32_bf16 v[144:147], a[224:227], a[144:147], v[144:147]\n"
    "s_waitcnt lgkmcnt(6)\n"
    "v_add_f32_dpp v254, v253, v77 row_newbcast:13 row_mask:0xf bank_mask:0xf\n"
    "v_add_f32_dpp v255, v253, v79 row_newbcast:15 row_mask:0xf bank_mask:0xf\n"
    "v_mfma_f32_16x16x32_bf16 v[112:115], a[224:227], a[140:143], v[112:115]\n"
    "v_mul_f32 v245, v245, v254\n"
    "v_mul_f32 v247, v247, v255\n"
    "v_mfma_f32_16x16x32_bf16 v[112:115], a[228:231], a[172:175], v[112:115]\n"
    "s_waitcnt lgkmcnt(4)\n"
    "v_cvt_pk_bf16_f32 v250, v244, v245\n"
    "v_cvt_pk_bf16_f32 v251, v246, v247\n"
    "v_mfma_f32_16x16x32_bf16 v[80:83], a[228:231], a[168:171], v[80:83]\n"
    "v_accvgpr_read_b32 v245, a44\n"
    "v_accvgpr_read_b32 v247, a45\n"
    "v_mfma_f32_16x16x32_bf16 v[80:83], a[224:227], a[136:139], v[80:83]\n"
    "s_waitcnt lgkmcnt(2)\n"
    "v_lshlrev_b32 v244, 16, v245\n"
    "v_lshlrev_b32 v246, 16, v247\n"
    "v_mfma_f32_16x16x32_bf16 v[48:51], a[224:227], a[132:135], v[48:51]\n"
    "v_and_b32 v245, s58, v245\n"
    "v_and_b32 v247, s58, v247\n"
    "v_mfma_f32_16x16x32_bf16 v[48:51], a[228:231], a[164:167], v[48:51]\n"
    "s_waitcnt lgkmcnt(0)\n"
    "v_add_f32_dpp v254, v253, v104 row_newbcast:8 row_mask:0xf bank_mask:0xf\n"
    "v_add_f32_dpp v255, v253, v106 row_newbcast:10 row_mask:0xf bank_mask:0xf\n"
    "v_mfma_f32_16x16x32_bf16 v[16:19], a[228:231], a[160:163], v[16:19]\n"
    "v_mul_f32 v244, v244, v254\n"
    "v_mul_f32 v246, v246, v255\n"
    "v_mfma_f32_16x16x32_bf16 v[16:19], a[224:227], a[128:131], v[16:19]\n"
    "s_waitcnt vmcnt(11)\n"
    "buffer_load_dwordx4 a[224:227], v240, s[20:23], 0 offen offset:0 sc1\n"
    "v_mfma_f32_16x16x32_bf16 v[20:23], a[232:235], a[128:131], v[20:23]\n"
    "buffer_load_dwordx4 a[228:231], v240, s[20:23], 0 offen offset:1024 sc1\n"
    "v_add_f32_dpp v254, v253, v105 row_newbcast:9 row_mask:0xf bank_mask:0xf\n"
    "v_mfma_f32_16x16x32_bf16 v[20:23], a[236:239], a[160:163], v[20:23]\n"
    "v_add_f32_dpp v255, v253, v107 row_newbcast:11 row_mask:0xf bank_mask:0xf\n"
    "v_mul_f32 v245, v245, v254\n"
    "v_mfma_f32_16x16x32_bf16 v[52:55], a[236:239], a[164:167], v[52:55]\n"
    "buffer_store_dwordx4 v[248:251], v240, s[96:99], 0 offen sc1\n"
    "ds_write_b128 v243, v[248:251] offset:36864\n"
    "v_mfma_f32_16x16x32_bf16 v[52:55], a[232:235], a[132:135], v[52:55]\n"
    "v_mul_f32 v247, v247, v255\n"
    "v_cvt_pk_bf16_f32 v248, v244, v245\n"
    "v_mfma_f32_16x16x32_bf16 v[84:87], a[232:235], a[136:139], v[84:87]\n"
    "v_cvt_pk_bf16_f32 v249, v246, v247\n"
    "v_accvgpr_read_b32 v245, a46\n"
    "v_mfma_f32_16x16x32_bf16 v[84:87], a[236:239], a[168:171], v[84:87]\n"
    "v_accvgpr_read_b32 v247, a47\n"
    "v_lshlrev_b32 v244, 16, v245\n"
    "v_mfma_f32_16x16x32_bf16 v[116:119], a[236:239], a[172:175], v[116:119]\n"
    "s_xor_b32 s96, s96, 0x800\n"
    "v_lshlrev_b32 v246, 16, v247\n"
    "v_and_b32 v245, s58, v245\n"
    "v_mfma_f32_16x16x32_bf16 v[116:119], a[232:235], a[140:143], v[116:119]\n"
    "v_and_b32 v247, s58, v247\n"
    "v_add_f32_dpp v254, v253, v108 row_newbcast:12 row_mask:0xf bank_mask:0xf\n"
    "v_mfma_f32_16x16x32_bf16 v[148:151], a[232:235], a[144:147], v[148:151]\n"
    "v_add_f32_dpp v255, v253, v110 row_newbcast:14 row_mask:0xf bank_mask:0xf\n"
    "v_mul_f32 v244, v244, v254\n"
    "v_mfma_f32_16x16x32_bf16 v[148:151], a[236:239], a[176:179], v[148:151]\n"
    "v_mul_f32 v246, v246, v255\n"
    "v_add_f32_dpp v254, v253, v109 row_newbcast:13 row_mask:0xf bank_mask:0xf\n"
    "v_mfma_f32_16x16x32_bf16 v[180:183], a[236:239], a[180:183], v[180:183]\n"
    "v_add_f32_dpp v255, v253, v111 row_newbcast:15 row_mask:0xf bank_mask:0xf\n"
    "v_mul_f32 v245, v245, v254\n"
    "v_mfma_f32_16x16x32_bf16 v[180:183], a[232:235], a[148:151], v[180:183]\n"
    "v_mul_f32 v247, v247, v255\n"
    "v_cvt_pk_bf16_f32 v250, v244, v245\n"
    "v_mfma_f32_16x16x32_bf16 v[212:215], a[232:235], a[152:155], v[212:215]\n"
    "v_cvt_pk_bf16_f32 v251, v246, v247\n"
    "v_accvgpr_read_b32 v245, a48\n"
    "v_mfma_f32_16x16x32_bf16 v[212:215], a[236:239], a[184:187], v[212:215]\n"
    "v_accvgpr_read_b32 v247, a49\n"
    "v_lshlrev_b32 v244, 16, v245\n"
    "v_mfma_f32_16x16x32_bf16 a[244:247], a[236:239], a[188:191], a[244:247]\n"
    "v_lshlrev_b32 v246, 16, v247\n"
    "v_and_b32 v245, s58, v245\n"
    "v_mfma_f32_16x16x32_bf16 a[244:247], a[232:235], a[156:159], a[244:247]\n"
    "s_waitcnt vmcnt(11)\n"
    "v_and_b32 v247, s58, v247\n"
    "v_add_f32_dpp v254, v253, v136 row_newbcast:8 row_mask:0xf bank_mask:0xf\n"
    "v_mfma_f32_16x16x32_bf16 a[248:251], a[192:195], a[156:159], a[248:251]\n"
    "buffer_store_dwordx4 v[248:251], v240, s[96:99], 0 offen sc1\n"
    "ds_write_b128 v243, v[248:251] offset:38912\n"
    "v_mfma_f32_16x16x32_bf16 a[248:251], a[196:199], a[188:191], a[248:251]\n"
    "buffer_load_dwordx4 a[232:235], v240, s[20:23], 0 offen offset:2048 sc1\n"
    "v_add_f32_dpp v255, v253, v138 row_newbcast:10 row_mask:0xf bank_mask:0xf\n"
    "v_mfma_f32_16x16x32_bf16 v[216:219], a[196:199], a[184:187], v[216:219]\n"
    "buffer_load_dwordx4 a[236:239], v240, s[20:23], 0 offen offset:3072 sc1\n"
    "s_addk_i32 s20, 4096\n"
    "v_mfma_f32_16x16x32_bf16 v[216:219], a[192:195], a[152:155], v[216:219]\n"
    "v_mul_f32 v244, v244, v254\n"
    "v_mul_f32 v246, v246, v255\n"
    "v_mfma_f32_16x16x32_bf16 v[184:187], a[192:195], a[148:151], v[184:187]\n"
    "s_xor_b32 s96, s96, 0x185800\n"
    "v_add_f32_dpp v254, v253, v137 row_newbcast:9 row_mask:0xf bank_mask:0xf\n"
    "v_add_f32_dpp v255, v253, v139 row_newbcast:11 row_mask:0xf bank_mask:0xf\n"
    "v_mfma_f32_16x16x32_bf16 v[184:187], a[196:199], a[180:183], v[184:187]\n"
    "v_mul_f32 v245, v245, v254\n"
    "v_mul_f32 v247, v247, v255\n"
    "v_mfma_f32_16x16x32_bf16 v[152:155], a[196:199], a[176:179], v[152:155]\n"
    "v_cvt_pk_bf16_f32 v248, v244, v245\n"
    "v_cvt_pk_bf16_f32 v249, v246, v247\n"
    "v_mfma_f32_16x16x32_bf16 v[152:155], a[192:195], a[144:147], v[152:155]\n"
    "v_accvgpr_read_b32 v245, a50\n"
    "v_accvgpr_read_b32 v247, a51\n"
    "v_mfma_f32_16x16x32_bf16 v[120:123], a[192:195], a[140:143], v[120:123]\n"
    "v_lshlrev_b32 v244, 16, v245\n"
    "v_lshlrev_b32 v246, 16, v247\n"
    "v_mfma_f32_16x16x32_bf16 v[120:123], a[196:199], a[172:175], v[120:123]\n"
    "v_and_b32 v245, s58, v245\n"
    "v_and_b32 v247, s58, v247\n"
    "v_mfma_f32_16x16x32_bf16 v[88:91], a[196:199], a[168:171], v[88:91]\n"
    "v_add_f32_dpp v254, v253, v140 row_newbcast:12 row_mask:0xf bank_mask:0xf\n"
    "v_add_f32_dpp v255, v253, v142 row_newbcast:14 row_mask:0xf bank_mask:0xf\n"
    "v_mfma_f32_16x16x32_bf16 v[88:91], a[192:195], a[136:139], v[88:91]\n"
    "v_mul_f32 v244, v244, v254\n"
    "v_mul_f32 v246, v246, v255\n"
    "v_mfma_f32_16x16x32_bf16 v[56:59], a[192:195], a[132:135], v[56:59]\n"
    "v_add_f32_dpp v254, v253, v141 row_newbcast:13 row_mask:0xf bank_mask:0xf\n"
    "v_add_f32_dpp v255, v253, v143 row_newbcast:15 row_mask:0xf bank_mask:0xf\n"
    "v_mfma_f32_16x16x32_bf16 v[56:59], a[196:199], a[164:167], v[56:59]\n"
    "v_mul_f32 v245, v245, v254\n"
    "v_mul_f32 v247, v247, v255\n"
    "v_mfma_f32_16x16x32_bf16 v[24:27], a[196:199], a[160:163], v[24:27]\n"
    "v_cvt_pk_bf16_f32 v250, v244, v245\n"
    "v_cvt_pk_bf16_f32 v251, v246, v247\n"
    "v_mfma_f32_16x16x32_bf16 v[24:27], a[192:195], a[128:131], v[24:27]\n"
    "s_waitcnt vmcnt(12)\n"
    "v_accvgpr_read_b32 v245, a52\n"
    "v_accvgpr_read_b32 v247, a53\n"
    "v_mfma_f32_16x16x32_bf16 v[28:31], a[200:203], a[128:131], v[28:31]\n"
    "buffer_store_dwordx4 v[248:251], v240, s[96:99], 0 offen sc1\n"
    "ds_write_b128 v243, v[248:251] offset:40960\n"
    "v_lshlrev_b32 v244, 16, v245\n"
    "v_lshlrev_b32 v246, 16, v247\n"
    "v_mfma_f32_16x16x32_bf16 v[28:31], a[204:207], a[160:163], v[28:31]\n"
    "buffer_load_dwordx4 a[192:195], v240, s[20:23], 0 offen offset:0 sc1\n"
    "v_and_b32 v245, s58, v245\n"
    "v_and_b32 v247, s58, v247\n"
    "v_mfma_f32_16x16x32_bf16 v[60:63], a[204:207], a[164:167], v[60:63]\n"
    "buffer_load_dwordx4 a[196:199], v240, s[20:23], 0 offen offset:1024 sc1\n"
    "ds_read_b128 a[128:131], v241 offset:49152\n"
    "v_add_f32_dpp v254, v253, v168 row_newbcast:8 row_mask:0xf bank_mask:0xf\n"
    "v_add_f32_dpp v255, v253, v170 row_newbcast:10 row_mask:0xf bank_mask:0xf\n"
    "v_mul_f32 v244, v244, v254\n"
    "v_mfma_f32_16x16x32_bf16 v[60:63], a[200:203], a[132:135], v[60:63]\n"
    "ds_read_b128 a[160:163], v241 offset:50176\n"
    "v_mul_f32 v246, v246, v255\n"
    "v_add_f32_dpp v254, v253, v169 row_newbcast:9 row_mask:0xf bank_mask:0xf\n"
    "v_mfma_f32_16x16x32_bf16 v[92:95], a[200:203], a[136:139], v[92:95]\n"
    "ds_read_b128 a[164:167], v241 offset:52224\n"
    "v_add_f32_dpp v255, v253, v171 row_newbcast:11 row_mask:0xf bank_mask:0xf\n"
    "v_mul_f32 v245, v245, v254\n"
    "v_mul_f32 v247, v247, v255\n"
    "v_mfma_f32_16x16x32_bf16 v[92:95], a[204:207], a[168:171], v[92:95]\n"
    "ds_read_b128 a[132:135], v241 offset:51200\n"
    "v_cvt_pk_bf16_f32 v248, v244, v245\n"
    "v_mfma_f32_16x16x32_bf16 v[124:127], a[204:207], a[172:175], v[124:127]\n"
    "ds_read_b128 a[136:139], v241 offset:53248\n"
    "v_cvt_pk_bf16_f32 v249, v246, v247\n"
    "v_accvgpr_read_b32 v245, a54\n"
    "v_accvgpr_read_b32 v247, a55\n"
    "v_mfma_f32_16x16x32_bf16 v[124:127], a[200:203], a[140:143], v[124:127]\n"
    "ds_read_b128 a[168:171], v241 offset:54272\n"
    "v_lshlrev_b32 v244, 16, v245\n"
    "v_lshlrev_b32 v246, 16, v247\n"
    "v_mfma_f32_16x16x32_bf16 v[156:159], a[200:203], a[144:147], v[156:159]\n"
    "ds_read_b128 a[172:175], v241 offset:56320\n"
    "v_and_b32 v245, s58, v245\n"
    "v_mfma_f32_16x16x32_bf16 v[156:159], a[204:207], a[176:179], v[156:159]\n"
    "ds_read_b128 a[140:143], v241 offset:55296\n"
    "v_and_b32 v247, s58, v247\n"
    "v_mfma_f32_16x16x32_bf16 v[188:191], a[204:207], a[180:183], v[188:191]\n"
    "ds_read_b128 a[144:147], v241 offset:57344\n"
    "v_add_f32_dpp v254, v253, v172 row_newbcast:12 row_mask:0xf bank_mask:0xf\n"
    "v_mfma_f32_16x16x32_bf16 v[188:191], a[200:203], a[148:151], v[188:191]\n"
    "ds_read_b128 a[176:179], v241 offset:58368\n"
    "s_xor_b32 s96, s96, 0x800\n"
    "v_mfma_f32_16x16x32_bf16 v[220:223], a[200:203], a[152:155], v[220:223]\n"
    "ds_read_b128 a[180:183], v241 offset:60416\n"
    "v_add_f32_dpp v255, v253, v174 row_newbcast:14 row_mask:0xf bank_mask:0xf\n"
    "v_mfma_f32_16x16x32_bf16 v[220:223], a[204:207], a[184:187], v[220:223]\n"
    "ds_read_b128 a[148:151], v241 offset:59392\n"
    "v_mul_f32 v244, v244, v254\n"
    "v_mfma_f32_16x16x32_bf16 a[252:255], a[204:207], a[188:191], a[252:255]\n"
    "ds_read_b128 a[152:155], v241 offset:61440\n"
    "v_mul_f32 v246, v246, v255\n"
    "v_mfma_f32_16x16x32_bf16 a[252:255], a[200:203], a[156:159], a[252:255]\n"
    "s_waitcnt vmcnt(12) lgkmcnt(11)\n"
    "ds_read_b128 a[184:187], v241 offset:62464\n"
    "v_add_f32_dpp v254, v253, v173 row_newbcast:13 row_mask:0xf bank_mask:0xf\n"
    "v_mfma_f32_16x16x32_bf16 v[16:19], a[208:211], a[128:131], v[16:19]\n"
    "buffer_load_dwordx4 a[200:203], v240, s[20:23], 0 offen offset:2048 sc1\n"
    "v_add_f32_dpp v255, v253, v175 row_newbcast:15 row_mask:0xf bank_mask:0xf\n"
    "v_mfma_f32_16x16x32_bf16 v[16:19], a[212:215], a[160:163], v[16:19]\n"
    "s_waitcnt lgkmcnt(10)\n"
    "buffer_load_dwordx4 a[204:207], v240, s[20:23], 0 offen offset:3072 sc1\n"
    "v_mfma_f32_16x16x32_bf16 v[48:51], a[212:215], a[164:167], v[48:51]\n"
    "s_addk_i32 s20, 4096\n"
    "ds_read_b128 a[188:191], v241 offset:64512\n"
    "v_mul_f32 v245, v245, v254\n"
    "v_mfma_f32_16x16x32_bf16 v[48:51], a[208:211], a[132:135], v[48:51]\n"
    "s_waitcnt lgkmcnt(9)\n"
    "ds_read_b128 a[156:159], v241 offset:63488\n"
    "v_mul_f32 v247, v247, v255\n"
    "v_mfma_f32_16x16x32_bf16 v[80:83], a[208:211], a[136:139], v[80:83]\n"
    "v_cvt_pk_bf16_f32 v250, v244, v245\n"
    "v_cvt_pk_bf16_f32 v251, v246, v247\n"
    "v_mfma_f32_16x16x32_bf16 v[80:83], a[212:215], a[168:171], v[80:83]\n"
    "s_waitcnt lgkmcnt(8)\n"
    "v_accvgpr_read_b32 v245, a56\n"
    "v_accvgpr_read_b32 v247, a57\n"
    "v_mfma_f32_16x16x32_bf16 v[112:115], a[212:215], a[172:175], v[112:115]\n"
    "v_lshlrev_b32 v244, 16, v245\n"
    "v_lshlrev_b32 v246, 16, v247\n"
    "v_mfma_f32_16x16x32_bf16 v[112:115], a[208:211], a[140:143], v[112:115]\n"
    "s_waitcnt lgkmcnt(6)\n"
    "v_and_b32 v245, s58, v245\n"
    "v_and_b32 v247, s58, v247\n"
    "v_mfma_f32_16x16x32_bf16 v[144:147], a[208:211], a[144:147], v[144:147]\n"
    "v_add_f32_dpp v254, v253, v200 row_newbcast:8 row_mask:0xf bank_mask:0xf\n"
    "v_add_f32_dpp v255, v253, v202 row_newbcast:10 row_mask:0xf bank_mask:0xf\n"
    "v_mfma_f32_16x16x32_bf16 v[144:147], a[212:215], a[176:179], v[144:147]\n"
    "s_waitcnt lgkmcnt(4)\n"
    "v_mul_f32 v244, v244, v254\n"
    "v_mul_f32 v246, v246, v255\n"
    "v_mfma_f32_16x16x32_bf16 v[176:179], a[212:215], a[180:183], v[176:179]\n"
    "v_add_f32_dpp v254, v253, v201 row_newbcast:9 row_mask:0xf bank_mask:0xf\n"
    "v_add_f32_dpp v255, v253, v203 row_newbcast:11 row_mask:0xf bank_mask:0xf\n"
    "v_mfma_f32_16x16x32_bf16 v[176:179], a[208:211], a[148:151], v[176:179]\n"
    "s_waitcnt lgkmcnt(2)\n"
    "v_mul_f32 v245, v245, v254\n"
    "v_mul_f32 v247, v247, v255\n"
    "v_mfma_f32_16x16x32_bf16 v[208:211], a[208:211], a[152:155], v[208:211]\n"
    "v_mfma_f32_16x16x32_bf16 v[208:211], a[212:215], a[184:187], v[208:211]\n"
    "s_waitcnt lgkmcnt(0)\n"
    "v_mfma_f32_16x16x32_bf16 a[240:243], a[212:215], a[188:191], a[240:243]\n"
    "buffer_store_dwordx4 v[248:251], v240, s[96:99], 0 offen sc1\n"
    "ds_write_b128 v243, v[248:251] offset:43008\n"
    "v_mfma_f32_16x16x32_bf16 a[240:243], a[208:211], a[156:159], a[240:243]\n"
    "s_waitcnt vmcnt(12)\n"
    "buffer_load_dwordx4 a[208:211], v240, s[20:23], 0 offen offset:0 sc1\n"
    "v_mfma_f32_16x16x32_bf16 a[244:247], a[216:219], a[156:159], a[244:247]\n"
    "buffer_load_dwordx4 a[212:215], v240, s[20:23], 0 offen offset:1024 sc1\n"
    "v_cvt_pk_bf16_f32 v248, v244, v245\n"
    "v_mfma_f32_16x16x32_bf16 a[244:247], a[220:223], a[188:191], a[244:247]\n"
    "v_cvt_pk_bf16_f32 v249, v246, v247\n"
    "v_accvgpr_read_b32 v245, a58\n"
    "v_mfma_f32_16x16x32_bf16 v[212:215], a[220:223], a[184:187], v[212:215]\n"
    "v_accvgpr_read_b32 v247, a59\n"
    "v_lshlrev_b32 v244, 16, v245\n"
    "v_mfma_f32_16x16x32_bf16 v[212:215], a[216:219], a[152:155], v[212:215]\n"
    "s_xor_b32 s96, s96, 0x101800\n"
    "v_lshlrev_b32 v246, 16, v247\n"
    "v_and_b32 v245, s58, v245\n"
    "v_mfma_f32_16x16x32_bf16 v[180:183], a[216:219], a[148:151], v[180:183]\n"
    "v_and_b32 v247, s58, v247\n"
    "v_add_f32_dpp v254, v253, v204 row_newbcast:12 row_mask:0xf bank_mask:0xf\n"
    "v_mfma_f32_16x16x32_bf16 v[180:183], a[220:223], a[180:183], v[180:183]\n"
    "v_add_f32_dpp v255, v253, v206 row_newbcast:14 row_mask:0xf bank_mask:0xf\n"
    "v_mul_f32 v244, v244, v254\n"
    "v_mfma_f32_16x16x32_bf16 v[148:151], a[220:223], a[176:179], v[148:151]\n"
    "v_mul_f32 v246, v246, v255\n"
    "v_add_f32_dpp v254, v253, v205 row_newbcast:13 row_mask:0xf bank_mask:0xf\n"
    "v_mfma_f32_16x16x32_bf16 v[148:151], a[216:219], a[144:147], v[148:151]\n"
    "v_add_f32_dpp v255, v253, v207 row_newbcast:15 row_mask:0xf bank_mask:0xf\n"
    "v_mul_f32 v245, v245, v254\n"
    "v_mfma_f32_16x16x32_bf16 v[116:119], a[216:219], a[140:143], v[116:119]\n"
    "v_mul_f32 v247, v247, v255\n"
    "v_cvt_pk_bf16_f32 v250, v244, v245\n"
    "v_mfma_f32_16x16x32_bf16 v[116:119], a[220:223], a[172:175], v[116:119]\n"
    "v_cvt_pk_bf16_f32 v251, v246, v247\n"
    "v_accvgpr_read_b32 v245, a60\n"
    "v_mfma_f32_16x16x32_bf16 v[84:87], a[220:223], a[168:171], v[84:87]\n"
    "v_accvgpr_read_b32 v247, a61\n"
    "v_lshlrev_b32 v244, 16, v245\n"
    "v_mfma_f32_16x16x32_bf16 v[84:87], a[216:219], a[136:139], v[84:87]\n"
    "v_lshlrev_b32 v246, 16, v247\n"
    "v_and_b32 v245, s58, v245\n"
    "v_mfma_f32_16x16x32_bf16 v[52:55], a[216:219], a[132:135], v[52:55]\n"
    "buffer_store_dwordx4 v[248:251], v240, s[96:99], 0 offen sc1\n"
    "ds_write_b128 v243, v[248:251] offset:45056\n"
    "v_mfma_f32_16x16x32_bf16 v[52:55], a[220:223], a[164:167], v[52:55]\n"
    "v_and_b32 v247, s58, v247\n"
    "v_add_f32_dpp v254, v253, v232 row_newbcast:8 row_mask:0xf bank_mask:0xf\n"
    "v_mfma_f32_16x16x32_bf16 v[20:23], a[220:223], a[160:163], v[20:23]\n"
    "v_add_f32_dpp v255, v253, v234 row_newbcast:10 row_mask:0xf bank_mask:0xf\n"
    "v_mul_f32 v244, v244, v254\n"
    "v_mfma_f32_16x16x32_bf16 v[20:23], a[216:219], a[128:131], v[20:23]\n"
    "s_waitcnt vmcnt(13)\n"
    "buffer_load_dwordx4 a[216:219], v240, s[20:23], 0 offen offset:2048 sc1\n"
    "v_mfma_f32_16x16x32_bf16 v[24:27], a[224:227], a[128:131], v[24:27]\n"
    "buffer_load_dwordx4 a[220:223], v240, s[20:23], 0 offen offset:3072 sc1\n"
    "s_addk_i32 s20, 4096\n"
    "v_mfma_f32_16x16x32_bf16 v[24:27], a[228:231], a[160:163], v[24:27]\n"
    "s_xor_b32 s96, s96, 0x800\n"
    "v_mul_f32 v246, v246, v255\n"
    "v_add_f32_dpp v254, v253, v233 row_newbcast:9 row_mask:0xf bank_mask:0xf\n"
    "v_mfma_f32_16x16x32_bf16 v[56:59], a[228:231], a[164:167], v[56:59]\n"
    "v_add_f32_dpp v255, v253, v235 row_newbcast:11 row_mask:0xf bank_mask:0xf\n"
    "v_mul_f32 v245, v245, v254\n"
    "v_mfma_f32_16x16x32_bf16 v[56:59], a[224:227], a[132:135], v[56:59]\n"
    "v_mul_f32 v247, v247, v255\n"
    "v_cvt_pk_bf16_f32 v248, v244, v245\n"
    "v_mfma_f32_16x16x32_bf16 v[88:91], a[224:227], a[136:139], v[88:91]\n"
    "v_cvt_pk_bf16_f32 v249, v246, v247\n"
    "v_accvgpr_read_b32 v245, a62\n"
    "v_mfma_f32_16x16x32_bf16 v[88:91], a[228:231], a[168:171], v[88:91]\n"
    "v_accvgpr_read_b32 v247, a63\n"
    "v_lshlrev_b32 v244, 16, v245\n"
    "v_mfma_f32_16x16x32_bf16 v[120:123], a[228:231], a[172:175], v[120:123]\n"
    "v_lshlrev_b32 v246, 16, v247\n"
    "v_and_b32 v245, s58, v245\n"
    "v_mfma_f32_16x16x32_bf16 v[120:123], a[224:227], a[140:143], v[120:123]\n"
    "v_and_b32 v247, s58, v247\n"
    "v_add_f32_dpp v254, v253, v236 row_newbcast:12 row_mask:0xf bank_mask:0xf\n"
    "v_mfma_f32_16x16x32_bf16 v[152:155], a[224:227], a[144:147], v[152:155]\n"
    "v_add_f32_dpp v255, v253, v238 row_newbcast:14 row_mask:0xf bank_mask:0xf\n"
    "v_mul_f32 v244, v244, v254\n"
    "v_mfma_f32_16x16x32_bf16 v[152:155], a[228:231], a[176:179], v[152:155]\n"
    "v_mul_f32 v246, v246, v255\n"
    "v_add_f32_dpp v254, v253, v237 row_newbcast:13 row_mask:0xf bank_mask:0xf\n"
    "v_mfma_f32_16x16x32_bf16 v[184:187], a[228:231], a[180:183], v[184:187]\n"
    "v_add_f32_dpp v255, v253, v239 row_newbcast:15 row_mask:0xf bank_mask:0xf\n"
    "v_mul_f32 v245, v245, v254\n"
    "v_mfma_f32_16x16x32_bf16 v[184:187], a[224:227], a[148:151], v[184:187]\n"
    "v_mul_f32 v247, v247, v255\n"
    "v_cvt_pk_bf16_f32 v250, v244, v245\n"
    "v_mfma_f32_16x16x32_bf16 v[216:219], a[224:227], a[152:155], v[216:219]\n"
    "v_cvt_pk_bf16_f32 v251, v246, v247\n"
    "v_mfma_f32_16x16x32_bf16 v[216:219], a[228:231], a[184:187], v[216:219]\n"
    "buffer_store_dwordx4 v[248:251], v240, s[96:99], 0 offen sc1\n"
    "ds_write_b128 v243, v[248:251] offset:47104\n"
    "s_waitcnt lgkmcnt(0)\n"
    "s_barrier\n"
    "v_mfma_f32_16x16x32_bf16 a[248:251], a[228:231], a[188:191], a[248:251]\n"
    "v_mfma_f32_16x16x32_bf16 a[248:251], a[224:227], a[156:159], a[248:251]\n"
    "s_waitcnt vmcnt(12)\n"
    "buffer_load_dwordx4 a[224:227], v240, s[20:23], 0 offen offset:0 sc1\n"
    "v_mfma_f32_16x16x32_bf16 a[252:255], a[232:235], a[156:159], a[252:255]\n"
    "buffer_load_dwordx4 a[228:231], v240, s[20:23], 0 offen offset:1024 sc1\n"
    "v_mfma_f32_16x16x32_bf16 a[252:255], a[236:239], a[188:191], a[252:255]\n"
    "v_mfma_f32_16x16x32_bf16 v[220:223], a[236:239], a[184:187], v[220:223]\n"
    "v_add_u32 v241, s66, v240\n"
    "v_mfma_f32_16x16x32_bf16 v[220:223], a[232:235], a[152:155], v[220:223]\n"
    "ds_read_b128 a[156:159], v241 offset:14336\n"
    "v_mfma_f32_16x16x32_bf16 v[188:191], a[232:235], a[148:151], v[188:191]\n"
    "ds_read_b128 a[188:191], v241 offset:15360\n"
    "v_mfma_f32_16x16x32_bf16 v[188:191], a[236:239], a[180:183], v[188:191]\n"
    "ds_read_b128 a[184:187], v241 offset:13312\n"
    "v_mfma_f32_16x16x32_bf16 v[156:159], a[236:239], a[176:179], v[156:159]\n"
    "ds_read_b128 a[152:155], v241 offset:12288\n"
    "v_mfma_f32_16x16x32_bf16 v[156:159], a[232:235], a[144:147], v[156:159]\n"
    "ds_read_b128 a[148:151], v241 offset:10240\n"
    "v_mfma_f32_16x16x32_bf16 v[124:127], a[232:235], a[140:143], v[124:127]\n"
    "ds_read_b128 a[180:183], v241 offset:11264\n"
    "v_mfma_f32_16x16x32_bf16 v[124:127], a[236:239], a[172:175], v[124:127]\n"
    "ds_read_b128 a[176:179], v241 offset:9216\n"
    "v_mfma_f32_16x16x32_bf16 v[92:95], a[236:239], a[168:171], v[92:95]\n"
    "ds_read_b128 a[144:147], v241 offset:8192\n"
    "v_mfma_f32_16x16x32_bf16 v[92:95], a[232:235], a[136:139], v[92:95]\n"
    "ds_read_b128 a[140:143], v241 offset:6144\n"
    "v_mfma_f32_16x16x32_bf16 v[60:63], a[232:235], a[132:135], v[60:63]\n"
    "ds_read_b128 a[172:175], v241 offset:7168\n"
    "v_mfma_f32_16x16x32_bf16 v[60:63], a[236:239], a[164:167], v[60:63]\n"
    "ds_read_b128 a[168:171], v241 offset:5120\n"
    "v_mfma_f32_16x16x32_bf16 v[28:31], a[236:239], a[160:163], v[28:31]\n"
    "ds_read_b128 a[136:139], v241 offset:4096\n"
    "v_mfma_f32_16x16x32_bf16 v[28:31], a[232:235], a[128:131], v[28:31]\n"
    "ds_read_b128 a[132:135], v241 offset:2048\n"
    "ds_read_b128 a[164:167], v241 offset:3072\n"
    "ds_read_b128 a[160:163], v241 offset:1024\n"
    "ds_read_b128 a[128:131], v241 offset:0\n"
    "buffer_load_dwordx4 a[232:235], v240, s[20:23], 0 offen offset:2048 sc1\n"
    "buffer_load_dwordx4 a[236:239], v240, s[20:23], 0 offen offset:3072 sc1\n"
    "s_addk_i32 s20, 4096\n"
    "s_branch LOOP_%=\n"
    "LOOPE_%=:\n"
    "CP0_%=:\n"
    "s_cmp_lg_u32 s84, 0\n"
    "s_cbranch_scc1 CQ0_%=\n"
    "ds_read_b128 a[0:3], v241 offset:0\n"
    "ds_read_b128 a[4:7], v241 offset:2048\n"
    "ds_read_b128 a[8:11], v241 offset:4096\n"
    "ds_read_b128 a[12:15], v241 offset:6144\n"
    "ds_read_b128 a[16:19], v241 offset:8192\n"
    "ds_read_b128 a[20:23], v241 offset:10240\n"
    "ds_read_b128 a[24:27], v241 offset:12288\n"
    "ds_read_b128 a[28:31], v241 offset:14336\n"
    "ds_read_b128 a[32:35], v242 offset:0\n"
    "ds_read_b128 a[36:39], v242 offset:2048\n"
    "ds_read_b128 a[40:43], v242 offset:4096\n"
    "ds_read_b128 a[44:47], v242 offset:6144\n"
    "ds_read_b128 a[48:51], v242 offset:8192\n"
    "ds_read_b128 a[52:55], v242 offset:10240\n"
    "ds_read_b128 a[56:59], v242 offset:12288\n"
    "ds_read_b128 a[60:63], v242 offset:14336\n"
    "s_branch CR0_%=\n"
    "CQ0_%=:\n"
    "ds_read_b128 a[64:67], v241 offset:0\n"
    "ds_read_b128 a[68:71], v241 offset:2048\n"
    "ds_read_b128 a[72:75], v241 offset:4096\n"
    "ds_read_b128 a[76:79], v241 offset:6144\n"
    "ds_read_b128 a[80:83], v241 offset:8192\n"
    "ds_read_b128 a[84:87], v241 offset:10240\n"
    "ds_read_b128 a[88:91], v241 offset:12288\n"
    "ds_read_b128 a[92:95], v241 offset:14336\n"
    "ds_read_b128 a[96:99], v242 offset:0\n"
    "ds_read_b128 a[100:103], v242 offset:2048\n"
    "ds_read_b128 a[104:107], v242 offset:4096\n"
    "ds_read_b128 a[108:111], v242 offset:6144\n"
    "ds_read_b128 a[112:115], v242 offset:8192\n"
    "ds_read_b128 a[116:119], v242 offset:10240\n"
    "ds_read_b128 a[120:123], v242 offset:12288\n"
    "ds_read_b128 a[124:127], v242 offset:14336\n"
    "s_branch CR0_%=\n"
    "CP1_%=:\n"
    "s_cmp_lg_u32 s84, 1\n"
    "s_cbranch_scc1 CQ1_%=\n"
    "ds_read_b128 a[0:3], v241 offset:16384\n"
    "ds_read_b128 a[4:7], v241 offset:18432\n"
    "ds_read_b128 a[8:11], v241 offset:20480\n"
    "ds_read_b128 a[12:15], v241 offset:22528\n"
    "ds_read_b128 a[16:19], v241 offset:24576\n"
    "ds_read_b128 a[20:23], v241 offset:26624\n"
    "ds_read_b128 a[24:27], v241 offset:28672\n"
    "ds_read_b128 a[28:31], v241 offset:30720\n"
    "ds_read_b128 a[32:35], v242 offset:16384\n"
    "ds_read_b128 a[36:39], v242 offset:18432\n"
    "ds_read_b128 a[40:43], v242 offset:20480\n"
    "ds_read_b128 a[44:47], v242 offset:22528\n"
    "ds_read_b128 a[48:51], v242 offset:24576\n"
    "ds_read_b128 a[52:55], v242 offset:26624\n"
    "ds_read_b128 a[56:59], v242 offset:28672\n"
    "ds_read_b128 a[60:63], v242 offset:30720\n"
    "s_branch CR1_%=\n"
    "CQ1_%=:\n"
    "ds_read_b128 a[64:67], v241 offset:16384\n"
    "ds_read_b128 a[68:71], v241 offset:18432\n"
    "ds_read_b128 a[72:75], v241 offset:20480\n"
    "ds_read_b128 a[76:79], v241 offset:22528\n"
    "ds_read_b128 a[80:83], v241 offset:24576\n"
    "ds_read_b128 a[84:87], v241 offset:26624\n"
    "ds_read_b128 a[88:91], v241 offset:28672\n"
    "ds_read_b128 a[92:95], v241 offset:30720\n"
    "ds_read_b128 a[96:99], v242 offset:16384\n"
    "ds_read_b128 a[100:103], v242 offset:18432\n"
    "ds_read_b128 a[104:107], v242 offset:20480\n"
    "ds_read_b128 a[108:111], v242 offset:22528\n"
    "ds_read_b128 a[112:115], v242 offset:24576\n"
    "ds_read_b128 a[116:119], v242 offset:26624\n"
    "ds_read_b128 a[120:123], v242 offset:28672\n"
    "ds_read_b128 a[124:127], v242 offset:30720\n"
    "s_branch CR1_%=\n"
    "CP2_%=:\n"
    "s_cmp_lg_u32 s84, 2\n"
    "s_cbranch_scc1 CQ2_%=\n"
    "ds_read_b128 a[0:3], v241 offset:32768\n"
    "ds_read_b128 a[4:7], v241 offset:34816\n"
    "ds_read_b128 a[8:11], v241 offset:36864\n"
    "ds_read_b128 a[12:15], v241 offset:38912\n"
    "ds_read_b128 a[16:19], v241 offset:40960\n"
    "ds_read_b128 a[20:23], v241 offset:43008\n"
    "ds_read_b128 a[24:27], v241 offset:45056\n"
    "ds_read_b128 a[28:31], v241 offset:47104\n"
    "ds_read_b128 a[32:35], v242 offset:32768\n"
    "ds_read_b128 a[36:39], v242 offset:34816\n"
    "ds_read_b128 a[40:43], v242 offset:36864\n"
    "ds_read_b128 a[44:47], v242 offset:38912\n"
    "ds_read_b128 a[48:51], v242 offset:40960\n"
    "ds_read_b128 a[52:55], v242 offset:43008\n"
    "ds_read_b128 a[56:59], v242 offset:45056\n"
    "ds_read_b128 a[60:63], v242 offset:47104\n"
    "s_branch CR2_%=\n"
    "CQ2_%=:\n"
    "ds_read_b128 a[64:67], v241 offset:32768\n"
    "ds_read_b128 a[68:71], v241 offset:34816\n"
    "ds_read_b128 a[72:75], v241 offset:36864\n"
    "ds_read_b128 a[76:79], v241 offset:38912\n"
    "ds_read_b128 a[80:83], v241 offset:40960\n"
    "ds_read_b128 a[84:87], v241 offset:43008\n"
    "ds_read_b128 a[88:91], v241 offset:45056\n"
    "ds_read_b128 a[92:95], v241 offset:47104\n"
    "ds_read_b128 a[96:99], v242 offset:32768\n"
    "ds_read_b128 a[100:103], v242 offset:34816\n"
    "ds_read_b128 a[104:107], v242 offset:36864\n"
    "ds_read_b128 a[108:111], v242 offset:38912\n"
    "ds_read_b128 a[112:115], v242 offset:40960\n"
    "ds_read_b128 a[116:119], v242 offset:43008\n"
    "ds_read_b128 a[120:123], v242 offset:45056\n"
    "ds_read_b128 a[124:127], v242 offset:47104\n"
    "s_branch CR2_%=\n"
    "CP3_%=:\n"
    "s_cmp_lg_u32 s84, 3\n"
    "s_cbranch_scc1 CQ3_%=\n"
    "ds_read_b128 a[0:3], v241 offset:0\n"
    "ds_read_b128 a[4:7], v241 offset:2048\n"
    "ds_read_b128 a[8:11], v241 offset:4096\n"
    "ds_read_b128 a[12:15], v241 offset:6144\n"
    "ds_read_b128 a[16:19], v241 offset:8192\n"
    "ds_read_b128 a[20:23], v241 offset:10240\n"
    "ds_read_b128 a[24:27], v241 offset:12288\n"
    "ds_read_b128 a[28:31], v241 offset:14336\n"
    "ds_read_b128 a[32:35], v242 offset:0\n"
    "ds_read_b128 a[36:39], v242 offset:2048\n"
    "ds_read_b128 a[40:43], v242 offset:4096\n"
    "ds_read_b128 a[44:47], v242 offset:6144\n"
    "ds_read_b128 a[48:51], v242 offset:8192\n"
    "ds_read_b128 a[52:55], v242 offset:10240\n"
    "ds_read_b128 a[56:59], v242 offset:12288\n"
    "ds_read_b128 a[60:63], v242 offset:14336\n"
    "s_branch CR3_%=\n"
    "CQ3_%=:\n"
    "ds_read_b128 a[64:67], v241 offset:0\n"
    "ds_read_b128 a[68:71], v241 offset:2048\n"
    "ds_read_b128 a[72:75], v241 offset:4096\n"
    "ds_read_b128 a[76:79], v241 offset:6144\n"
    "ds_read_b128 a[80:83], v241 offset:8192\n"
    "ds_read_b128 a[84:87], v241 offset:10240\n"
    "ds_read_b128 a[88:91], v241 offset:12288\n"
    "ds_read_b128 a[92:95], v241 offset:14336\n"
    "ds_read_b128 a[96:99], v242 offset:0\n"
    "ds_read_b128 a[100:103], v242 offset:2048\n"
    "ds_read_b128 a[104:107], v242 offset:4096\n"
    "ds_read_b128 a[108:111], v242 offset:6144\n"
    "ds_read_b128 a[112:115], v242 offset:8192\n"
    "ds_read_b128 a[116:119], v242 offset:10240\n"
    "ds_read_b128 a[120:123], v242 offset:12288\n"
    "ds_read_b128 a[124:127], v242 offset:14336\n"
    "s_branch CR3_%=\n"
    "CP4_%=:\n"
    "s_cmp_lg_u32 s84, 4\n"
    "s_cbranch_scc1 CQ4_%=\n"
    "ds_read_b128 a[0:3], v241 offset:16384\n"
    "ds_read_b128 a[4:7], v241 offset:18432\n"
    "ds_read_b128 a[8:11], v241 offset:20480\n"
    "ds_read_b128 a[12:15], v241 offset:22528\n"
    "ds_read_b128 a[16:19], v241 offset:24576\n"
    "ds_read_b128 a[20:23], v241 offset:26624\n"
    "ds_read_b128 a[24:27], v241 offset:28672\n"
    "ds_read_b128 a[28:31], v241 offset:30720\n"
    "ds_read_b128 a[32:35], v242 offset:16384\n"
    "ds_read_b128 a[36:39], v242 offset:18432\n"
    "ds_read_b128 a[40:43], v242 offset:20480\n"
    "ds_read_b128 a[44:47], v242 offset:22528\n"
    "ds_read_b128 a[48:51], v242 offset:24576\n"
    "ds_read_b128 a[52:55], v242 offset:26624\n"
    "ds_read_b128 a[56:59], v242 offset:28672\n"
    "ds_read_b128 a[60:63], v242 offset:30720\n"
    "s_branch CR4_%=\n"
    "CQ4_%=:\n"
    "ds_read_b128 a[64:67], v241 offset:16384\n"
    "ds_read_b128 a[68:71], v241 offset:18432\n"
    "ds_read_b128 a[72:75], v241 offset:20480\n"
    "ds_read_b128 a[76:79], v241 offset:22528\n"
    "ds_read_b128 a[80:83], v241 offset:24576\n"
    "ds_read_b128 a[84:87], v241 offset:26624\n"
    "ds_read_b128 a[88:91], v241 offset:28672\n"
    "ds_read_b128 a[92:95], v241 offset:30720\n"
    "ds_read_b128 a[96:99], v242 offset:16384\n"
    "ds_read_b128 a[100:103], v242 offset:18432\n"
    "ds_read_b128 a[104:107], v242 offset:20480\n"
    "ds_read_b128 a[108:111], v242 offset:22528\n"
    "ds_read_b128 a[112:115], v242 offset:24576\n"
    "ds_read_b128 a[116:119], v242 offset:26624\n"
    "ds_read_b128 a[120:123], v242 offset:28672\n"
    "ds_read_b128 a[124:127], v242 offset:30720\n"
    "s_branch CR4_%=\n"
    "CP5_%=:\n"
    "s_cmp_lg_u32 s84, 5\n"
    "s_cbranch_scc1 CQ5_%=\n"
    "ds_read_b128 a[0:3], v241 offset:32768\n"
    "ds_read_b128 a[4:7], v241 offset:34816\n"
    "ds_read_b128 a[8:11], v241 offset:36864\n"
    "ds_read_b128 a[12:15], v241 offset:38912\n"
    "ds_read_b128 a[16:19], v241 offset:40960\n"
    "ds_read_b128 a[20:23], v241 offset:43008\n"
    "ds_read_b128 a[24:27], v241 offset:45056\n"
    "ds_read_b128 a[28:31], v241 offset:47104\n"
    "ds_read_b128 a[32:35], v242 offset:32768\n"
    "ds_read_b128 a[36:39], v242 offset:34816\n"
    "ds_read_b128 a[40:43], v242 offset:36864\n"
    "ds_read_b128 a[44:47], v242 offset:38912\n"
    "ds_read_b128 a[48:51], v242 offset:40960\n"
    "ds_read_b128 a[52:55], v242 offset:43008\n"
    "ds_read_b128 a[56:59], v242 offset:45056\n"
    "ds_read_b128 a[60:63], v242 offset:47104\n"
    "s_branch CR5_%=\n"
    "CQ5_%=:\n"
    "ds_read_b128 a[64:67], v241 offset:32768\n"
    "ds_read_b128 a[68:71], v241 offset:34816\n"
    "ds_read_b128 a[72:75], v241 offset:36864\n"
    "ds_read_b128 a[76:79], v241 offset:38912\n"
    "ds_read_b128 a[80:83], v241 offset:40960\n"
    "ds_read_b128 a[84:87], v241 offset:43008\n"
    "ds_read_b128 a[88:91], v241 offset:45056\n"
    "ds_read_b128 a[92:95], v241 offset:47104\n"
    "ds_read_b128 a[96:99], v242 offset:32768\n"
    "ds_read_b128 a[100:103], v242 offset:34816\n"
    "ds_read_b128 a[104:107], v242 offset:36864\n"
    "ds_read_b128 a[108:111], v242 offset:38912\n"
    "ds_read_b128 a[112:115], v242 offset:40960\n"
    "ds_read_b128 a[116:119], v242 offset:43008\n"
    "ds_read_b128 a[120:123], v242 offset:45056\n"
    "ds_read_b128 a[124:127], v242 offset:47104\n"
    "s_branch CR5_%=\n"
    "CP6_%=:\n"
    "s_cmp_lg_u32 s84, 6\n"
    "s_cbranch_scc1 CQ6_%=\n"
    "ds_read_b128 a[0:3], v241 offset:0\n"
    "ds_read_b128 a[4:7], v241 offset:2048\n"
    "ds_read_b128 a[8:11], v241 offset:4096\n"
    "ds_read_b128 a[12:15], v241 offset:6144\n"
    "ds_read_b128 a[16:19], v241 offset:8192\n"
    "ds_read_b128 a[20:23], v241 offset:10240\n"
    "ds_read_b128 a[24:27], v241 offset:12288\n"
    "ds_read_b128 a[28:31], v241 offset:14336\n"
    "ds_read_b128 a[32:35], v242 offset:0\n"
    "ds_read_b128 a[36:39], v242 offset:2048\n"
    "ds_read_b128 a[40:43], v242 offset:4096\n"
    "ds_read_b128 a[44:47], v242 offset:6144\n"
    "ds_read_b128 a[48:51], v242 offset:8192\n"
    "ds_read_b128 a[52:55], v242 offset:10240\n"
    "ds_read_b128 a[56:59], v242 offset:12288\n"
    "ds_read_b128 a[60:63], v242 offset:14336\n"
    "s_branch CR6_%=\n"
    "CQ6_%=:\n"
    "ds_read_b128 a[64:67], v241 offset:0\n"
    "ds_read_b128 a[68:71], v241 offset:2048\n"
    "ds_read_b128 a[72:75], v241 offset:4096\n"
    "ds_read_b128 a[76:79], v241 offset:6144\n"
    "ds_read_b128 a[80:83], v241 offset:8192\n"
    "ds_read_b128 a[84:87], v241 offset:10240\n"
    "ds_read_b128 a[88:91], v241 offset:12288\n"
    "ds_read_b128 a[92:95], v241 offset:14336\n"
    "ds_read_b128 a[96:99], v242 offset:0\n"
    "ds_read_b128 a[100:103], v242 offset:2048\n"
    "ds_read_b128 a[104:107], v242 offset:4096\n"
    "ds_read_b128 a[108:111], v242 offset:6144\n"
    "ds_read_b128 a[112:115], v242 offset:8192\n"
    "ds_read_b128 a[116:119], v242 offset:10240\n"
    "ds_read_b128 a[120:123], v242 offset:12288\n"
    "ds_read_b128 a[124:127], v242 offset:14336\n"
    "s_branch CR6_%=\n"
    "CP7_%=:\n"
    "s_cmp_lg_u32 s84, 7\n"
    "s_cbranch_scc1 CQ7_%=\n"
    "ds_read_b128 a[0:3], v241 offset:16384\n"
    "ds_read_b128 a[4:7], v241 offset:18432\n"
    "ds_read_b128 a[8:11], v241 offset:20480\n"
    "ds_read_b128 a[12:15], v241 offset:22528\n"
    "ds_read_b128 a[16:19], v241 offset:24576\n"
    "ds_read_b128 a[20:23], v241 offset:26624\n"
    "ds_read_b128 a[24:27], v241 offset:28672\n"
    "ds_read_b128 a[28:31], v241 offset:30720\n"
    "ds_read_b128 a[32:35], v242 offset:16384\n"
    "ds_read_b128 a[36:39], v242 offset:18432\n"
    "ds_read_b128 a[40:43], v242 offset:20480\n"
    "ds_read_b128 a[44:47], v242 offset:22528\n"
    "ds_read_b128 a[48:51], v242 offset:24576\n"
    "ds_read_b128 a[52:55], v242 offset:26624\n"
    "ds_read_b128 a[56:59], v242 offset:28672\n"
    "ds_read_b128 a[60:63], v242 offset:30720\n"
    "s_branch CR7_%=\n"
    "CQ7_%=:\n"
    "ds_read_b128 a[64:67], v241 offset:16384\n"
    "ds_read_b128 a[68:71], v241 offset:18432\n"
    "ds_read_b128 a[72:75], v241 offset:20480\n"
    "ds_read_b128 a[76:79], v241 offset:22528\n"
    "ds_read_b128 a[80:83], v241 offset:24576\n"
    "ds_read_b128 a[84:87], v241 offset:26624\n"
    "ds_read_b128 a[88:91], v241 offset:28672\n"
    "ds_read_b128 a[92:95], v241 offset:30720\n"
    "ds_read_b128 a[96:99], v242 offset:16384\n"
    "ds_read_b128 a[100:103], v242 offset:18432\n"
    "ds_read_b128 a[104:107], v242 offset:20480\n"
    "ds_read_b128 a[108:111], v242 offset:22528\n"
    "ds_read_b128 a[112:115], v242 offset:24576\n"
    "ds_read_b128 a[116:119], v242 offset:26624\n"
    "ds_read_b128 a[120:123], v242 offset:28672\n"
    "ds_read_b128 a[124:127], v242 offset:30720\n"
    "s_branch CR7_%=\n"
    "CP8_%=:\n"
    "s_cmp_lg_u32 s84, 8\n"
    "s_cbranch_scc1 CQ8_%=\n"
    "ds_read_b128 a[0:3], v241 offset:32768\n"
    "ds_read_b128 a[4:7], v241 offset:34816\n"
    "ds_read_b128 a[8:11], v241 offset:36864\n"
    "ds_read_b128 a[12:15], v241 offset:38912\n"
    "ds_read_b128 a[16:19], v241 offset:40960\n"
    "ds_read_b128 a[20:23], v241 offset:43008\n"
    "ds_read_b128 a[24:27], v241 offset:45056\n"
    "ds_read_b128 a[28:31], v241 offset:47104\n"
    "ds_read_b128 a[32:35], v242 offset:32768\n"
    "ds_read_b128 a[36:39], v242 offset:34816\n"
    "ds_read_b128 a[40:43], v242 offset:36864\n"
    "ds_read_b128 a[44:47], v242 offset:38912\n"
    "ds_read_b128 a[48:51], v242 offset:40960\n"
    "ds_read_b128 a[52:55], v242 offset:43008\n"
    "ds_read_b128 a[56:59], v242 offset:45056\n"
    "ds_read_b128 a[60:63], v242 offset:47104\n"
    "s_branch CR8_%=\n"
    "CQ8_%=:\n"
    "ds_read_b128 a[64:67], v241 offset:32768\n"
    "ds_read_b128 a[68:71], v241 offset:34816\n"
    "ds_read_b128 a[72:75], v241 offset:36864\n"
    "ds_read_b128 a[76:79], v241 offset:38912\n"
    "ds_read_b128 a[80:83], v241 offset:40960\n"
    "ds_read_b128 a[84:87], v241 offset:43008\n"
    "ds_read_b128 a[88:91], v241 offset:45056\n"
    "ds_read_b128 a[92:95], v241 offset:47104\n"
    "ds_read_b128 a[96:99], v242 offset:32768\n"
    "ds_read_b128 a[100:103], v242 offset:34816\n"
    "ds_read_b128 a[104:107], v242 offset:36864\n"
    "ds_read_b128 a[108:111], v242 offset:38912\n"
    "ds_read_b128 a[112:115], v242 offset:40960\n"
    "ds_read_b128 a[116:119], v242 offset:43008\n"
    "ds_read_b128 a[120:123], v242 offset:45056\n"
    "ds_read_b128 a[124:127], v242 offset:47104\n"
    "s_branch CR8_%=\n"
    "CP9_%=:\n"
    "s_cmp_lg_u32 s84, 9\n"
    "s_cbranch_scc1 CQ9_%=\n"
    "ds_read_b128 a[0:3], v241 offset:0\n"
    "ds_read_b128 a[4:7], v241 offset:2048\n"
    "ds_read_b128 a[8:11], v241 offset:4096\n"
    "ds_read_b128 a[12:15], v241 offset:6144\n"
    "ds_read_b128 a[16:19], v241 offset:8192\n"
    "ds_read_b128 a[20:23], v241 offset:10240\n"
    "ds_read_b128 a[24:27], v241 offset:12288\n"
    "ds_read_b128 a[28:31], v241 offset:14336\n"
    "ds_read_b128 a[32:35], v242 offset:0\n"
    "ds_read_b128 a[36:39], v242 offset:2048\n"
    "ds_read_b128 a[40:43], v242 offset:4096\n"
    "ds_read_b128 a[44:47], v242 offset:6144\n"
    "ds_read_b128 a[48:51], v242 offset:8192\n"
    "ds_read_b128 a[52:55], v242 offset:10240\n"
    "ds_read_b128 a[56:59], v242 offset:12288\n"
    "ds_read_b128 a[60:63], v242 offset:14336\n"
    "s_branch CR9_%=\n"
    "CQ9_%=:\n"
    "ds_read_b128 a[64:67], v241 offset:0\n"
    "ds_read_b128 a[68:71], v241 offset:2048\n"
    "ds_read_b128 a[72:75], v241 offset:4096\n"
    "ds_read_b128 a[76:79], v241 offset:6144\n"
    "ds_read_b128 a[80:83], v241 offset:8192\n"
    "ds_read_b128 a[84:87], v241 offset:10240\n"
    "ds_read_b128 a[88:91], v241 offset:12288\n"
    "ds_read_b128 a[92:95], v241 offset:14336\n"
    "ds_read_b128 a[96:99], v242 offset:0\n"
    "ds_read_b128 a[100:103], v242 offset:2048\n"
    "ds_read_b128 a[104:107], v242 offset:4096\n"
    "ds_read_b128 a[108:111], v242 offset:6144\n"
    "ds_read_b128 a[112:115], v242 offset:8192\n"
    "ds_read_b128 a[116:119], v242 offset:10240\n"
    "ds_read_b128 a[120:123], v242 offset:12288\n"
    "ds_read_b128 a[124:127], v242 offset:14336\n"
    "s_branch CR9_%=\n"
    "CP10_%=:\n"
    "s_cmp_lg_u32 s84, 10\n"
    "s_cbranch_scc1 CQ10_%=\n"
    "ds_read_b128 a[0:3], v241 offset:16384\n"
    "ds_read_b128 a[4:7], v241 offset:18432\n"
    "ds_read_b128 a[8:11], v241 offset:20480\n"
    "ds_read_b128 a[12:15], v241 offset:22528\n"
    "ds_read_b128 a[16:19], v241 offset:24576\n"
    "ds_read_b128 a[20:23], v241 offset:26624\n"
    "ds_read_b128 a[24:27], v241 offset:28672\n"
    "ds_read_b128 a[28:31], v241 offset:30720\n"
    "ds_read_b128 a[32:35], v242 offset:16384\n"
    "ds_read_b128 a[36:39], v242 offset:18432\n"
    "ds_read_b128 a[40:43], v242 offset:20480\n"
    "ds_read_b128 a[44:47], v242 offset:22528\n"
    "ds_read_b128 a[48:51], v242 offset:24576\n"
    "ds_read_b128 a[52:55], v242 offset:26624\n"
    "ds_read_b128 a[56:59], v242 offset:28672\n"
    "ds_read_b128 a[60:63], v242 offset:30720\n"
    "s_branch CR10_%=\n"
    "CQ10_%=:\n"
    "ds_read_b128 a[64:67], v241 offset:16384\n"
    "ds_read_b128 a[68:71], v241 offset:18432\n"
    "ds_read_b128 a[72:75], v241 offset:20480\n"
    "ds_read_b128 a[76:79], v241 offset:22528\n"
    "ds_read_b128 a[80:83], v241 offset:24576\n"
    "ds_read_b128 a[84:87], v241 offset:26624\n"
    "ds_read_b128 a[88:91], v241 offset:28672\n"
    "ds_read_b128 a[92:95], v241 offset:30720\n"
    "ds_read_b128 a[96:99], v242 offset:16384\n"
    "ds_read_b128 a[100:103], v242 offset:18432\n"
    "ds_read_b128 a[104:107], v242 offset:20480\n"
    "ds_read_b128 a[108:111], v242 offset:22528\n"
    "ds_read_b128 a[112:115], v242 offset:24576\n"
    "ds_read_b128 a[116:119], v242 offset:26624\n"
    "ds_read_b128 a[120:123], v242 offset:28672\n"
    "ds_read_b128 a[124:127], v242 offset:30720\n"
    "s_branch CR10_%=\n"
    "CP11_%=:\n"
    "s_cmp_lg_u32 s84, 11\n"
    "s_cbranch_scc1 CQ11_%=\n"
    "ds_read_b128 a[0:3], v241 offset:32768\n"
    "ds_read_b128 a[4:7], v241 offset:34816\n"
    "ds_read_b128 a[8:11], v241 offset:36864\n"
    "ds_read_b128 a[12:15], v241 offset:38912\n"
    "ds_read_b128 a[16:19], v241 offset:40960\n"
    "ds_read_b128 a[20:23], v241 offset:43008\n"
    "ds_read_b128 a[24:27], v241 offset:45056\n"
    "ds_read_b128 a[28:31], v241 offset:47104\n"
    "ds_read_b128 a[32:35], v242 offset:32768\n"
    "ds_read_b128 a[36:39], v242 offset:34816\n"
    "ds_read_b128 a[40:43], v242 offset:36864\n"
    "ds_read_b128 a[44:47], v242 offset:38912\n"
    "ds_read_b128 a[48:51], v242 offset:40960\n"
    "ds_read_b128 a[52:55], v242 offset:43008\n"
    "ds_read_b128 a[56:59], v242 offset:45056\n"
    "ds_read_b128 a[60:63], v242 offset:47104\n"
    "s_branch CR11_%=\n"
    "CQ11_%=:\n"
    "ds_read_b128 a[64:67], v241 offset:32768\n"
    "ds_read_b128 a[68:71], v241 offset:34816\n"
    "ds_read_b128 a[72:75], v241 offset:36864\n"
    "ds_read_b128 a[76:79], v241 offset:38912\n"
    "ds_read_b128 a[80:83], v241 offset:40960\n"
    "ds_read_b128 a[84:87], v241 offset:43008\n"
    "ds_read_b128 a[88:91], v241 offset:45056\n"
    "ds_read_b128 a[92:95], v241 offset:47104\n"
    "ds_read_b128 a[96:99], v242 offset:32768\n"
    "ds_read_b128 a[100:103], v242 offset:34816\n"
    "ds_read_b128 a[104:107], v242 offset:36864\n"
    "ds_read_b128 a[108:111], v242 offset:38912\n"
    "ds_read_b128 a[112:115], v242 offset:40960\n"
    "ds_read_b128 a[116:119], v242 offset:43008\n"
    "ds_read_b128 a[120:123], v242 offset:45056\n"
    "ds_read_b128 a[124:127], v242 offset:47104\n"
    "s_branch CR11_%=\n"
    "CP12_%=:\n"
    "s_cmp_lg_u32 s84, 12\n"
    "s_cbranch_scc1 CQ12_%=\n"
    "ds_read_b128 a[0:3], v241 offset:0\n"
    "ds_read_b128 a[4:7], v241 offset:2048\n"
    "ds_read_b128 a[8:11], v241 offset:4096\n"
    "ds_read_b128 a[12:15], v241 offset:6144\n"
    "ds_read_b128 a[16:19], v241 offset:8192\n"
    "ds_read_b128 a[20:23], v241 offset:10240\n"
    "ds_read_b128 a[24:27], v241 offset:12288\n"
    "ds_read_b128 a[28:31], v241 offset:14336\n"
    "ds_read_b128 a[32:35], v242 offset:0\n"
    "ds_read_b128 a[36:39], v242 offset:2048\n"
    "ds_read_b128 a[40:43], v242 offset:4096\n"
    "ds_read_b128 a[44:47], v242 offset:6144\n"
    "ds_read_b128 a[48:51], v242 offset:8192\n"
    "ds_read_b128 a[52:55], v242 offset:10240\n"
    "ds_read_b128 a[56:59], v242 offset:12288\n"
    "ds_read_b128 a[60:63], v242 offset:14336\n"
    "s_branch CR12_%=\n"
    "CQ12_%=:\n"
    "ds_read_b128 a[64:67], v241 offset:0\n"
    "ds_read_b128 a[68:71], v241 offset:2048\n"
    "ds_read_b128 a[72:75], v241 offset:4096\n"
    "ds_read_b128 a[76:79], v241 offset:6144\n"
    "ds_read_b128 a[80:83], v241 offset:8192\n"
    "ds_read_b128 a[84:87], v241 offset:10240\n"
    "ds_read_b128 a[88:91], v241 offset:12288\n"
    "ds_read_b128 a[92:95], v241 offset:14336\n"
    "ds_read_b128 a[96:99], v242 offset:0\n"
    "ds_read_b128 a[100:103], v242 offset:2048\n"
    "ds_read_b128 a[104:107], v242 offset:4096\n"
    "ds_read_b128 a[108:111], v242 offset:6144\n"
    "ds_read_b128 a[112:115], v242 offset:8192\n"
    "ds_read_b128 a[116:119], v242 offset:10240\n"
    "ds_read_b128 a[120:123], v242 offset:12288\n"
    "ds_read_b128 a[124:127], v242 offset:14336\n"
    "s_branch CR12_%=\n"
    "CP13_%=:\n"
    "s_cmp_lg_u32 s84, 13\n"
    "s_cbranch_scc1 CQ13_%=\n"
    "ds_read_b128 a[0:3], v241 offset:16384\n"
    "ds_read_b128 a[4:7], v241 offset:18432\n"
    "ds_read_b128 a[8:11], v241 offset:20480\n"
    "ds_read_b128 a[12:15], v241 offset:22528\n"
    "ds_read_b128 a[16:19], v241 offset:24576\n"
    "ds_read_b128 a[20:23], v241 offset:26624\n"
    "ds_read_b128 a[24:27], v241 offset:28672\n"
    "ds_read_b128 a[28:31], v241 offset:30720\n"
    "ds_read_b128 a[32:35], v242 offset:16384\n"
    "ds_read_b128 a[36:39], v242 offset:18432\n"
    "ds_read_b128 a[40:43], v242 offset:20480\n"
    "ds_read_b128 a[44:47], v242 offset:22528\n"
    "ds_read_b128 a[48:51], v242 offset:24576\n"
    "ds_read_b128 a[52:55], v242 offset:26624\n"
    "ds_read_b128 a[56:59], v242 offset:28672\n"
    "ds_read_b128 a[60:63], v242 offset:30720\n"
    "s_branch CR13_%=\n"
    "CQ13_%=:\n"
    "ds_read_b128 a[64:67], v241 offset:16384\n"
    "ds_read_b128 a[68:71], v241 offset:18432\n"
    "ds_read_b128 a[72:75], v241 offset:20480\n"
    "ds_read_b128 a[76:79], v241 offset:22528\n"
    "ds_read_b128 a[80:83], v241 offset:24576\n"
    "ds_read_b128 a[84:87], v241 offset:26624\n"
    "ds_read_b128 a[88:91], v241 offset:28672\n"
    "ds_read_b128 a[92:95], v241 offset:30720\n"
    "ds_read_b128 a[96:99], v242 offset:16384\n"
    "ds_read_b128 a[100:103], v242 offset:18432\n"
    "ds_read_b128 a[104:107], v242 offset:20480\n"
    "ds_read_b128 a[108:111], v242 offset:22528\n"
    "ds_read_b128 a[112:115], v242 offset:24576\n"
    "ds_read_b128 a[116:119], v242 offset:26624\n"
    "ds_read_b128 a[120:123], v242 offset:28672\n"
    "ds_read_b128 a[124:127], v242 offset:30720\n"
    "s_branch CR13_%=\n"
    "CP14_%=:\n"
    "s_cmp_lg_u32 s84, 14\n"
    "s_cbranch_scc1 CQ14_%=\n"
    "ds_read_b128 a[0:3], v241 offset:49152\n"
    "ds_read_b128 a[4:7], v241 offset:51200\n"
    "ds_read_b128 a[8:11], v241 offset:53248\n"
    "ds_read_b128 a[12:15], v241 offset:55296\n"
    "ds_read_b128 a[16:19], v241 offset:57344\n"
    "ds_read_b128 a[20:23], v241 offset:59392\n"
    "ds_read_b128 a[24:27], v241 offset:61440\n"
    "ds_read_b128 a[28:31], v241 offset:63488\n"
    "ds_read_b128 a[32:35], v242 offset:49152\n"
    "ds_read_b128 a[36:39], v242 offset:51200\n"
    "ds_read_b128 a[40:43], v242 offset:53248\n"
    "ds_read_b128 a[44:47], v242 offset:55296\n"
    "ds_read_b128 a[48:51], v242 offset:57344\n"
    "ds_read_b128 a[52:55], v242 offset:59392\n"
    "ds_read_b128 a[56:59], v242 offset:61440\n"
    "ds_read_b128 a[60:63], v242 offset:63488\n"
    "s_branch CR14_%=\n"
    "CQ14_%=:\n"
    "ds_read_b128 a[64:67], v241 offset:49152\n"
    "ds_read_b128 a[68:71], v241 offset:51200\n"
    "ds_read_b128 a[72:75], v241 offset:53248\n"
    "ds_read_b128 a[76:79], v241 offset:55296\n"
    "ds_read_b128 a[80:83], v241 offset:57344\n"
    "ds_read_b128 a[84:87], v241 offset:59392\n"
    "ds_read_b128 a[88:91], v241 offset:61440\n"
    "ds_read_b128 a[92:95], v241 offset:63488\n"
    "ds_read_b128 a[96:99], v242 offset:49152\n"
    "ds_read_b128 a[100:103], v242 offset:51200\n"
    "ds_read_b128 a[104:107], v242 offset:53248\n"
    "ds_read_b128 a[108:111], v242 offset:55296\n"
    "ds_read_b128 a[112:115], v242 offset:57344\n"
    "ds_read_b128 a[116:119], v242 offset:59392\n"
    "ds_read_b128 a[120:123], v242 offset:61440\n"
    "ds_read_b128 a[124:127], v242 offset:63488\n"
    "s_branch CR14_%=\n"
    "CP15_%=:\n"
    "s_cmp_lg_u32 s84, 15\n"
    "s_cbranch_scc1 CQ15_%=\n"
    "ds_read_b128 a[0:3], v241 offset:32768\n"
    "ds_read_b128 a[4:7], v241 offset:34816\n"
    "ds_read_b128 a[8:11], v241 offset:36864\n"
    "ds_read_b128 a[12:15], v241 offset:38912\n"
    "ds_read_b128 a[16:19], v241 offset:40960\n"
    "ds_read_b128 a[20:23], v241 offset:43008\n"
    "ds_read_b128 a[24:27], v241 offset:45056\n"
    "ds_read_b128 a[28:31], v241 offset:47104\n"
    "ds_read_b128 a[32:35], v242 offset:32768\n"
    "ds_read_b128 a[36:39], v242 offset:34816\n"
    "ds_read_b128 a[40:43], v242 offset:36864\n"
    "ds_read_b128 a[44:47], v242 offset:38912\n"
    "ds_read_b128 a[48:51], v242 offset:40960\n"
    "ds_read_b128 a[52:55], v242 offset:43008\n"
    "ds_read_b128 a[56:59], v242 offset:45056\n"
    "ds_read_b128 a[60:63], v242 offset:47104\n"
    "s_branch CR15_%=\n"
    "CQ15_%=:\n"
    "ds_read_b128 a[64:67], v241 offset:32768\n"
    "ds_read_b128 a[68:71], v241 offset:34816\n"
    "ds_read_b128 a[72:75], v241 offset:36864\n"
    "ds_read_b128 a[76:79], v241 offset:38912\n"
    "ds_read_b128 a[80:83], v241 offset:40960\n"
    "ds_read_b128 a[84:87], v241 offset:43008\n"
    "ds_read_b128 a[88:91], v241 offset:45056\n"
    "ds_read_b128 a[92:95], v241 offset:47104\n"
    "ds_read_b128 a[96:99], v242 offset:32768\n"
    "ds_read_b128 a[100:103], v242 offset:34816\n"
    "ds_read_b128 a[104:107], v242 offset:36864\n"
    "ds_read_b128 a[108:111], v242 offset:38912\n"
    "ds_read_b128 a[112:115], v242 offset:40960\n"
    "ds_read_b128 a[116:119], v242 offset:43008\n"
    "ds_read_b128 a[120:123], v242 offset:45056\n"
    "ds_read_b128 a[124:127], v242 offset:47104\n"
    "s_branch CR15_%=\n"
    "OA_%=:\n"
    "s_cmp_eq_u32 s52, 2\n"
    "s_cbranch_scc1 OC_%=\n"
    "s_lshr_b32 s60, %3, 3\n"
    "s_lshl_b32 s60, s60, 2\n"
    "s_add_u32 s60, s60, s65\n"
    "s_lshl_b32 s60, s60, 10\n"
    "s_min_u32 s60, s60, CODE_END_%= - LAST_%= - 1024\n"
    "s_getpc_b64 s[62:63]\n"
    "LA9001_%=:\n"
    "s_add_u32 s62, s62, LAST_%= - LA9001_%=\n"
    "s_addc_u32 s63, s63, 0\n"
    "s_add_u32 s62, s62, s60\n"
    "s_addc_u32 s63, s63, 0\n"
    "global_load_dwordx4 v[244:247], v240, s[62:63]\n"
    "s_branch OB_%=\n"
    "OC_%=:\n"
    "buffer_load_dwordx4 v[248:251], v240, s[24:27], 0 offen sc1\n"
    "s_branch OB_%=\n"
    "OD_%=:\n"
    "s_cmp_eq_u32 s52, 10\n"
    "s_cbranch_scc1 LAST_%=\n"
    "v_and_b32 v244, v248, v249\n"
    "v_and_b32 v245, v250, v251\n"
    "v_and_b32 v244, v244, v245\n"
    "v_and_b32 v244, 1, v244\n"
    "v_cmp_ne_u32 vcc, 1, v244\n"
    "s_nop 1\n"
    "s_cmp_eq_u64 vcc, 0\n"
    "s_cbranch_scc1 LA9002_%=\n"
    "s_mov_b32 s69, 0x400000\n"
    "LA9003_%=:\n"
    "s_sleep 2\n"
    "buffer_load_dwordx4 v[248:251], v240, s[24:27], 0 offen sc1\n"
    "s_waitcnt vmcnt(0)\n"
    "s_sub_u32 s69, s69, 1\n"
    "s_cbranch_scc1 LA9002_%=\n"
    "v_and_b32 v244, v248, v249\n"
    "v_and_b32 v245, v250, v251\n"
    "v_and_b32 v244, v244, v245\n"
    "v_and_b32 v244, 1, v244\n"
    "v_cmp_ne_u32 vcc, 1, v244\n"
    "s_nop 1\n"
    "s_cmp_eq_u64 vcc, 0\n"
    "s_cbranch_scc0 LA9003_%=\n"
    "LA9002_%=:\n"
    "v_add_u32 v248, v248, v249\n"
    "v_add3_u32 v248, v248, v250, v251\n"
    "s_lshr_b32 s60, %0, 2\n"
    "s_xor_b32 s61, s60, 2\n"
    "v_and_b32 v249, 0xfffff, v248\n"
    "v_readlane_b32 s62, v248, s60\n"
    "v_readlane_b32 s63, v248, s61\n"
    "v_add_u32_dpp v249, v249, v249 row_ror:8 row_mask:0xf bank_mask:0xf\n"
    "s_nop 1\n"
    "v_add_u32_dpp v249, v249, v249 row_ror:4 row_mask:0xf bank_mask:0xf\n"
    "s_nop 1\n"
    "v_add_u32_dpp v249, v249, v249 row_ror:2 row_mask:0xf bank_mask:0xf\n"
    "s_nop 1\n"
    "v_add_u32_dpp v249, v249, v249 row_ror:1 row_mask:0xf bank_mask:0xf\n"
    "s_nop 1\n"
    "s_add_u32 s62, s62, s63\n"
    "s_lshr_b32 s62, s62, 20\n"
    "s_cmp_gt_u32 s62, 2048\n"
    "s_cselect_b32 s64, 1, 0\n"
    "v_readlane_b32 s62, v249, s60\n"
    "s_cmp_gt_u32 s62, 262208\n"
    "s_cselect_b32 s62, 524288, 0\n"
    "s_lshl_b32 s54, %3, 19\n"
    "s_xor_b32 s54, s54, s62\n"
    "s_xor_b32 s59, s54, 4194304\n"
    "s_lshl_b32 s62, s64, 13\n"
    "s_xor_b32 s54, s54, s62\n"
    "s_branch OE_%=\n"
    "LAST_%=:\n"
    "s_waitcnt vmcnt(10) lgkmcnt(14)\n"
    "v_mfma_f32_16x16x32_bf16 v[224:227], a[208:211], a[156:159], v[224:227]\n"
    "v_mfma_f32_16x16x32_bf16 v[224:227], a[212:215], a[188:191], v[224:227]\n"
    "s_waitcnt lgkmcnt(12)\n"
    "v_mfma_f32_16x16x32_bf16 v[192:195], a[212:215], a[184:187], v[192:195]\n"
    "v_mfma_f32_16x16x32_bf16 v[192:195], a[208:211], a[152:155], v[192:195]\n"
    "s_waitcnt lgkmcnt(10)\n"
    "v_mfma_f32_16x16x32_bf16 v[160:163], a[208:211], a[148:151], v[160:163]\n"
    "v_mfma_f32_16x16x32_bf16 v[160:163], a[212:215], a[180:183], v[160:163]\n"
    "s_waitcnt lgkmcnt(8)\n"
    "v_mfma_f32_16x16x32_bf16 v[128:131], a[212:215], a[176:179], v[128:131]\n"
    "s_mov_b32 m0, s56\n"
    "s_xor_b32 s92, s92, 0x195800\n"
    "v_mfma_f32_16x16x32_bf16 v[128:131], a[208:211], a[144:147], v[128:131]\n"
    "s_waitcnt lgkmcnt(6)\n"
    "buffer_load_dwordx4 v240, s[92:95], 0 offen sc1 lds\n"
    "v_mfma_f32_16x16x32_bf16 v[96:99], a[208:211], a[140:143], v[96:99]\n"
    "v_mfma_f32_16x16x32_bf16 v[96:99], a[212:215], a[172:175], v[96:99]\n"
    "s_waitcnt lgkmcnt(4)\n"
    "v_mfma_f32_16x16x32_bf16 v[64:67], a[212:215], a[168:171], v[64:67]\n"
    "v_mfma_f32_16x16x32_bf16 v[64:67], a[208:211], a[136:139], v[64:67]\n"
    "s_waitcnt lgkmcnt(2)\n"
    "v_mfma_f32_16x16x32_bf16 v[32:35], a[208:211], a[132:135], v[32:35]\n"
    "v_mfma_f32_16x16x32_bf16 v[32:35], a[212:215], a[164:167], v[32:35]\n"
    "s_waitcnt lgkmcnt(0)\n"
    "v_mfma_f32_16x16x32_bf16 v[0:3], a[212:215], a[160:163], v[0:3]\n"
    "v_mfma_f32_16x16x32_bf16 v[0:3], a[208:211], a[128:131], v[0:3]\n"
    "s_waitcnt vmcnt(9)\n"
    "buffer_load_dwordx4 a[208:211], v240, s[20:23], 0 offen offset:0 sc1\n"
    "v_mfma_f32_16x16x32_bf16 v[4:7], a[216:219], a[128:131], v[4:7]\n"
    "buffer_load_dwordx4 a[212:215], v240, s[20:23], 0 offen offset:1024 sc1\n"
    "v_mfma_f32_16x16x32_bf16 v[4:7], a[220:223], a[160:163], v[4:7]\n"
    "v_mfma_f32_16x16x32_bf16 v[36:39], a[220:223], a[164:167], v[36:39]\n"
    "v_mfma_f32_16x16x32_bf16 v[36:39], a[216:219], a[132:135], v[36:39]\n"
    "v_mfma_f32_16x16x32_bf16 v[68:71], a[216:219], a[136:139], v[68:71]\n"
    "v_mfma_f32_16x16x32_bf16 v[68:71], a[220:223], a[168:171], v[68:71]\n"
    "v_mfma_f32_16x16x32_bf16 v[100:103], a[220:223], a[172:175], v[100:103]\n"
    "s_addk_i32 m0, 2048\n"
    "s_xor_b32 s92, s92, 0x800\n"
    "v_mfma_f32_16x16x32_bf16 v[100:103], a[216:219], a[140:143], v[100:103]\n"
    "buffer_load_dwordx4 v240, s[92:95], 0 offen sc1 lds\n"
    "v_mfma_f32_16x16x32_bf16 v[132:135], a[216:219], a[144:147], v[132:135]\n"
    "v_mfma_f32_16x16x32_bf16 v[132:135], a[220:223], a[176:179], v[132:135]\n"
    "v_mfma_f32_16x16x32_bf16 v[164:167], a[220:223], a[180:183], v[164:167]\n"
    "v_mfma_f32_16x16x32_bf16 v[164:167], a[216:219], a[148:151], v[164:167]\n"
    "v_mfma_f32_16x16x32_bf16 v[196:199], a[216:219], a[152:155], v[196:199]\n"
    "v_mfma_f32_16x16x32_bf16 v[196:199], a[220:223], a[184:187], v[196:199]\n"
    "v_mfma_f32_16x16x32_bf16 v[228:231], a[220:223], a[188:191], v[228:231]\n"
    "v_mfma_f32_16x16x32_bf16 v[228:231], a[216:219], a[156:159], v[228:231]\n"
    "s_waitcnt vmcnt(10)\n"
    "buffer_load_dwordx4 a[216:219], v240, s[20:23], 0 offen offset:2048 sc1\n"
    "v_mfma_f32_16x16x32_bf16 v[232:235], a[224:227], a[156:159], v[232:235]\n"
    "buffer_load_dwordx4 a[220:223], v240, s[20:23], 0 offen offset:3072 sc1\n"
    "v_mfma_f32_16x16x32_bf16 v[232:235], a[228:231], a[188:191], v[232:235]\n"
    "v_mfma_f32_16x16x32_bf16 v[200:203], a[228:231], a[184:187], v[200:203]\n"
    "v_mfma_f32_16x16x32_bf16 v[200:203], a[224:227], a[152:155], v[200:203]\n"
    "v_mfma_f32_16x16x32_bf16 v[168:171], a[224:227], a[148:151], v[168:171]\n"
    "v_mfma_f32_16x16x32_bf16 v[168:171], a[228:231], a[180:183], v[168:171]\n"
    "v_mfma_f32_16x16x32_bf16 v[136:139], a[228:231], a[176:179], v[136:139]\n"
    "s_addk_i32 m0, 2048\n"
    "s_xor_b32 s92, s92, 0x101800\n"
    "v_mfma_f32_16x16x32_bf16 v[136:139], a[224:227], a[144:147], v[136:139]\n"
    "buffer_load_dwordx4 v240, s[92:95], 0 offen sc1 lds\n"
    "v_mfma_f32_16x16x32_bf16 v[104:107], a[224:227], a[140:143], v[104:107]\n"
    "v_mfma_f32_16x16x32_bf16 v[104:107], a[228:231], a[172:175], v[104:107]\n"
    "v_mfma_f32_16x16x32_bf16 v[72:75], a[228:231], a[168:171], v[72:75]\n"
    "v_mfma_f32_16x16x32_bf16 v[72:75], a[224:227], a[136:139], v[72:75]\n"
    "v_mfma_f32_16x16x32_bf16 v[40:43], a[224:227], a[132:135], v[40:43]\n"
    "v_mfma_f32_16x16x32_bf16 v[40:43], a[228:231], a[164:167], v[40:43]\n"
    "v_mfma_f32_16x16x32_bf16 v[8:11], a[228:231], a[160:163], v[8:11]\n"
    "v_mfma_f32_16x16x32_bf16 v[8:11], a[224:227], a[128:131], v[8:11]\n"
    "s_waitcnt vmcnt(11)\n"
    "s_addk_i32 s20, 4096\n"
    "v_mfma_f32_16x16x32_bf16 v[12:15], a[232:235], a[128:131], v[12:15]\n"
    "buffer_load_dwordx4 a[224:227], v240, s[20:23], 0 offen offset:0 sc1\n"
    "v_mfma_f32_16x16x32_bf16 v[12:15], a[236:239], a[160:163], v[12:15]\n"
    "buffer_load_dwordx4 a[228:231], v240, s[20:23], 0 offen offset:1024 sc1\n"
    "v_mfma_f32_16x16x32_bf16 v[44:47], a[236:239], a[164:167], v[44:47]\n"
    "v_mfma_f32_16x16x32_bf16 v[44:47], a[232:235], a[132:135], v[44:47]\n"
    "v_mfma_f32_16x16x32_bf16 v[76:79], a[232:235], a[136:139], v[76:79]\n"
    "v_mfma_f32_16x16x32_bf16 v[76:79], a[236:239], a[168:171], v[76:79]\n"
    "v_mfma_f32_16x16x32_bf16 v[108:111], a[236:239], a[172:175], v[108:111]\n"
    "s_addk_i32 m0, 2048\n"
    "s_xor_b32 s92, s92, 0x800\n"
    "v_mfma_f32_16x16x32_bf16 v[108:111], a[232:235], a[140:143], v[108:111]\n"
    "buffer_load_dwordx4 v240, s[92:95], 0 offen sc1 lds\n"
    "v_mfma_f32_16x16x32_bf16 v[140:143], a[232:235], a[144:147], v[140:143]\n"
    "v_mfma_f32_16x16x32_bf16 v[140:143], a[236:239], a[176:179], v[140:143]\n"
    "v_mfma_f32_16x16x32_bf16 v[172:175], a[236:239], a[180:183], v[172:175]\n"
    "v_mfma_f32_16x16x32_bf16 v[172:175], a[232:235], a[148:151], v[172:175]\n"
    "v_mfma_f32_16x16x32_bf16 v[204:207], a[232:235], a[152:155], v[204:207]\n"
    "v_mfma_f32_16x16x32_bf16 v[204:207], a[236:239], a[184:187], v[204:207]\n"
    "v_mfma_f32_16x16x32_bf16 v[236:239], a[236:239], a[188:191], v[236:239]\n"
    "v_mfma_f32_16x16x32_bf16 v[236:239], a[232:235], a[156:159], v[236:239]\n"
    "s_waitcnt vmcnt(12)\n"
    "buffer_load_dwordx4 a[232:235], v240, s[20:23], 0 offen offset:2048 sc1\n"
    "v_mfma_f32_16x16x32_bf16 a[240:243], a[192:195], a[156:159], a[240:243]\n"
    "buffer_load_dwordx4 a[236:239], v240, s[20:23], 0 offen offset:3072 sc1\n"
    "v_mfma_f32_16x16x32_bf16 a[240:243], a[196:199], a[188:191], a[240:243]\n"
    "v_mfma_f32_16x16x32_bf16 v[208:211], a[196:199], a[184:187], v[208:211]\n"
    "v_mfma_f32_16x16x32_bf16 v[208:211], a[192:195], a[152:155], v[208:211]\n"
    "v_mfma_f32_16x16x32_bf16 v[176:179], a[192:195], a[148:151], v[176:179]\n"
    "v_mfma_f32_16x16x32_bf16 v[176:179], a[196:199], a[180:183], v[176:179]\n"
    "v_mfma_f32_16x16x32_bf16 v[144:147], a[196:199], a[176:179], v[144:147]\n"
    "v_mfma_f32_16x16x32_bf16 v[144:147], a[192:195], a[144:147], v[144:147]\n"
    "v_mfma_f32_16x16x32_bf16 v[112:115], a[192:195], a[140:143], v[112:115]\n"
    "v_mfma_f32_16x16x32_bf16 v[112:115], a[196:199], a[172:175], v[112:115]\n"
    "v_mfma_f32_16x16x32_bf16 v[80:83], a[196:199], a[168:171], v[80:83]\n"
    "v_mfma_f32_16x16x32_bf16 v[80:83], a[192:195], a[136:139], v[80:83]\n"
    "v_mfma_f32_16x16x32_bf16 v[48:51], a[192:195], a[132:135], v[48:51]\n"
    "s_addk_i32 m0, 2048\n"
    "s_xor_b32 s92, s92, 0x185800\n"
    "v_mfma_f32_16x16x32_bf16 v[48:51], a[196:199], a[164:167], v[48:51]\n"
    "buffer_load_dwordx4 v240, s[92:95], 0 offen sc1 lds\n"
    "v_mfma_f32_16x16x32_bf16 v[16:19], a[196:199], a[160:163], v[16:19]\n"
    "v_mfma_f32_16x16x32_bf16 v[16:19], a[192:195], a[128:131], v[16:19]\n"
    "s_waitcnt vmcnt(13)\n"
    "s_addk_i32 s20, 4096\n"
    "v_mfma_f32_16x16x32_bf16 v[20:23], a[200:203], a[128:131], v[20:23]\n"
    "buffer_load_dwordx4 a[192:195], v240, s[20:23], 0 offen offset:0 sc1\n"
    "v_mfma_f32_16x16x32_bf16 v[20:23], a[204:207], a[160:163], v[20:23]\n"
    "buffer_load_dwordx4 a[196:199], v240, s[20:23], 0 offen offset:1024 sc1\n"
    "v_mfma_f32_16x16x32_bf16 v[52:55], a[204:207], a[164:167], v[52:55]\n"
    "v_mfma_f32_16x16x32_bf16 v[52:55], a[200:203], a[132:135], v[52:55]\n"
    "v_mfma_f32_16x16x32_bf16 v[84:87], a[200:203], a[136:139], v[84:87]\n"
    "v_mfma_f32_16x16x32_bf16 v[84:87], a[204:207], a[168:171], v[84:87]\n"
    "v_mfma_f32_16x16x32_bf16 v[116:119], a[204:207], a[172:175], v[116:119]\n"
    "v_mfma_f32_16x16x32_bf16 v[116:119], a[200:203], a[140:143], v[116:119]\n"
    "v_mfma_f32_16x16x32_bf16 v[148:151], a[200:203], a[144:147], v[148:151]\n"
    "v_mfma_f32_16x16x32_bf16 v[148:151], a[204:207], a[176:179], v[148:151]\n"
    "v_mfma_f32_16x16x32_bf16 v[180:183], a[204:207], a[180:183], v[180:183]\n"
    "v_mfma_f32_16x16x32_bf16 v[180:183], a[200:203], a[148:151], v[180:183]\n"
    "v_mfma_f32_16x16x32_bf16 v[212:215], a[200:203], a[152:155], v[212:215]\n"
    "s_addk_i32 m0, 2048\n"
    "s_xor_b32 s92, s92, 0x800\n"
    "v_mfma_f32_16x16x32_bf16 v[212:215], a[204:207], a[184:187], v[212:215]\n"
    "buffer_load_dwordx4 v240, s[92:95], 0 offen sc1 lds\n"
    "v_mfma_f32_16x16x32_bf16 a[244:247], a[204:207], a[188:191], a[244:247]\n"
    "v_mfma_f32_16x16x32_bf16 a[244:247], a[200:203], a[156:159], a[244:247]\n"
    "s_waitcnt vmcnt(13)\n"
    "buffer_load_dwordx4 a[200:203], v240, s[20:23], 0 offen offset:2048 sc1\n"
    "v_mfma_f32_16x16x32_bf16 a[248:251], a[208:211], a[156:159], a[248:251]\n"
    "buffer_load_dwordx4 a[204:207], v240, s[20:23], 0 offen offset:3072 sc1\n"
    "v_mfma_f32_16x16x32_bf16 a[248:251], a[212:215], a[188:191], a[248:251]\n"
    "v_mfma_f32_16x16x32_bf16 v[216:219], a[212:215], a[184:187], v[216:219]\n"
    "v_mfma_f32_16x16x32_bf16 v[216:219], a[208:211], a[152:155], v[216:219]\n"
    "v_mfma_f32_16x16x32_bf16 v[184:187], a[208:211], a[148:151], v[184:187]\n"
    "v_mfma_f32_16x16x32_bf16 v[184:187], a[212:215], a[180:183], v[184:187]\n"
    "v_mfma_f32_16x16x32_bf16 v[152:155], a[212:215], a[176:179], v[152:155]\n"
    "v_mfma_f32_16x16x32_bf16 v[152:155], a[208:211], a[144:147], v[152:155]\n"
    "v_mfma_f32_16x16x32_bf16 v[120:123], a[208:211], a[140:143], v[120:123]\n"
    "v_mfma_f32_16x16x32_bf16 v[120:123], a[212:215], a[172:175], v[120:123]\n"
    "v_mfma_f32_16x16x32_bf16 v[88:91], a[212:215], a[168:171], v[88:91]\n"
    "v_mfma_f32_16x16x32_bf16 v[88:91], a[208:211], a[136:139], v[88:91]\n"
    "v_mfma_f32_16x16x32_bf16 v[56:59], a[208:211], a[132:135], v[56:59]\n"
    "s_addk_i32 m0, 2048\n"
    "s_xor_b32 s92, s92, 0x101800\n"
    "v_mfma_f32_16x16x32_bf16 v[56:59], a[212:215], a[164:167], v[56:59]\n"
    "buffer_load_dwordx4 v240, s[92:95], 0 offen sc1 lds\n"
    "v_mfma_f32_16x16x32_bf16 v[24:27], a[212:215], a[160:163], v[24:27]\n"
    "v_mfma_f32_16x16x32_bf16 v[24:27], a[208:211], a[128:131], v[24:27]\n"
    "s_waitcnt vmcnt(13)\n"
    "s_addk_i32 s20, 12288\n"
    "v_mfma_f32_16x16x32_bf16 v[28:31], a[216:219], a[128:131], v[28:31]\n"
    "buffer_load_dwordx4 a[208:211], v240, s[20:23], 0 offen offset:0 sc1\n"
    "v_mfma_f32_16x16x32_bf16 v[28:31], a[220:223], a[160:163], v[28:31]\n"
    "buffer_load_dwordx4 a[212:215], v240, s[20:23], 0 offen offset:1024 sc1\n"
    "v_mfma_f32_16x16x32_bf16 v[60:63], a[220:223], a[164:167], v[60:63]\n"
    "ds_read_b128 a[128:131], v241 offset:16384\n"
    "v_mfma_f32_16x16x32_bf16 v[60:63], a[216:219], a[132:135], v[60:63]\n"
    "ds_read_b128 a[160:163], v241 offset:17408\n"
    "v_mfma_f32_16x16x32_bf16 v[92:95], a[216:219], a[136:139], v[92:95]\n"
    "ds_read_b128 a[164:167], v241 offset:19456\n"
    "v_mfma_f32_16x16x32_bf16 v[92:95], a[220:223], a[168:171], v[92:95]\n"
    "ds_read_b128 a[132:135], v241 offset:18432\n"
    "v_mfma_f32_16x16x32_bf16 v[124:127], a[220:223], a[172:175], v[124:127]\n"
    "ds_read_b128 a[136:139], v241 offset:20480\n"
    "v_mfma_f32_16x16x32_bf16 v[124:127], a[216:219], a[140:143], v[124:127]\n"
    "ds_read_b128 a[168:171], v241 offset:21504\n"
    "v_mfma_f32_16x16x32_bf16 v[156:159], a[216:219], a[144:147], v[156:159]\n"
    "ds_read_b128 a[172:175], v241 offset:23552\n"
    "v_mfma_f32_16x16x32_bf16 v[156:159], a[220:223], a[176:179], v[156:159]\n"
    "ds_read_b128 a[140:143], v241 offset:22528\n"
    "v_mfma_f32_16x16x32_bf16 v[188:191], a[220:223], a[180:183], v[188:191]\n"
    "ds_read_b128 a[144:147], v241 offset:24576\n"
    "v_mfma_f32_16x16x32_bf16 v[188:191], a[216:219], a[148:151], v[188:191]\n"
    "ds_read_b128 a[176:179], v241 offset:25600\n"
    "v_mfma_f32_16x16x32_bf16 v[220:223], a[216:219], a[152:155], v[220:223]\n"
    "s_addk_i32 m0, 2048\n"
    "s_xor_b32 s92, s92, 0x800\n"
    "v_mfma_f32_16x16x32_bf16 v[220:223], a[220:223], a[184:187], v[220:223]\n"
    "buffer_load_dwordx4 v240, s[92:95], 0 offen sc1 lds\n"
    "ds_read_b128 a[180:183], v241 offset:27648\n"
    "v_mfma_f32_16x16x32_bf16 a[252:255], a[220:223], a[188:191], a[252:255]\n"
    "ds_read_b128 a[148:151], v241 offset:26624\n"
    "v_mfma_f32_16x16x32_bf16 a[252:255], a[216:219], a[156:159], a[252:255]\n"
    "s_waitcnt vmcnt(13) lgkmcnt(10)\n"
    "ds_read_b128 a[152:155], v241 offset:28672\n"
    "v_mfma_f32_16x16x32_bf16 v[0:3], a[224:227], a[128:131], v[0:3]\n"
    "ds_read_b128 a[184:187], v241 offset:29696\n"
    "v_mfma_f32_16x16x32_bf16 v[0:3], a[228:231], a[160:163], v[0:3]\n"
    "s_waitcnt lgkmcnt(10)\n"
    "buffer_load_dwordx4 a[216:219], v240, s[20:23], 0 offen offset:2048 sc1\n"
    "v_mfma_f32_16x16x32_bf16 v[32:35], a[228:231], a[164:167], v[32:35]\n"
    "buffer_load_dwordx4 a[220:223], v240, s[20:23], 0 offen offset:3072 sc1\n"
    "ds_read_b128 a[188:191], v241 offset:31744\n"
    "v_mfma_f32_16x16x32_bf16 v[32:35], a[224:227], a[132:135], v[32:35]\n"
    "s_waitcnt lgkmcnt(9)\n"
    "ds_read_b128 a[156:159], v241 offset:30720\n"
    "v_mfma_f32_16x16x32_bf16 v[64:67], a[224:227], a[136:139], v[64:67]\n"
    "v_add_f32_dpp v16, v252, v16 row_newbcast:0 row_mask:0xf bank_mask:0xf\n"
    "v_add_f32_dpp v17, v252, v17 row_newbcast:1 row_mask:0xf bank_mask:0xf\n"
    "v_mfma_f32_16x16x32_bf16 v[64:67], a[228:231], a[168:171], v[64:67]\n"
    "s_waitcnt lgkmcnt(8)\n"
    "v_add_f32_dpp v18, v252, v18 row_newbcast:2 row_mask:0xf bank_mask:0xf\n"
    "v_mfma_f32_16x16x32_bf16 v[96:99], a[228:231], a[172:175], v[96:99]\n"
    "v_add_f32_dpp v19, v252, v19 row_newbcast:3 row_mask:0xf bank_mask:0xf\n"
    "v_mfma_f32_16x16x32_bf16 v[96:99], a[224:227], a[140:143], v[96:99]\n"
    "s_waitcnt lgkmcnt(6)\n"
    "v_add_f32_dpp v20, v252, v20 row_newbcast:4 row_mask:0xf bank_mask:0xf\n"
    "v_add_f32_dpp v21, v252, v21 row_newbcast:5 row_mask:0xf bank_mask:0xf\n"
    "v_mfma_f32_16x16x32_bf16 v[128:131], a[224:227], a[144:147], v[128:131]\n"
    "v_add_f32_dpp v22, v252, v22 row_newbcast:6 row_mask:0xf bank_mask:0xf\n"
    "v_mfma_f32_16x16x32_bf16 v[128:131], a[228:231], a[176:179], v[128:131]\n"
    "s_waitcnt lgkmcnt(4)\n"
    "v_add_f32_dpp v23, v252, v23 row_newbcast:7 row_mask:0xf bank_mask:0xf\n"
    "v_mfma_f32_16x16x32_bf16 v[160:163], a[228:231], a[180:183], v[160:163]\n"
    "v_add_f32_dpp v24, v252, v24 row_newbcast:8 row_mask:0xf bank_mask:0xf\n"
    "v_add_f32_dpp v25, v252, v25 row_newbcast:9 row_mask:0xf bank_mask:0xf\n"
    "v_mfma_f32_16x16x32_bf16 v[160:163], a[224:227], a[148:151], v[160:163]\n"
    "s_waitcnt lgkmcnt(2)\n"
    "v_add_f32_dpp v26, v252, v26 row_newbcast:10 row_mask:0xf bank_mask:0xf\n"
    "v_mfma_f32_16x16x32_bf16 v[192:195], a[224:227], a[152:155], v[192:195]\n"
    "v_add_f32_dpp v27, v252, v27 row_newbcast:11 row_mask:0xf bank_mask:0xf\n"
    "v_mfma_f32_16x16x32_bf16 v[192:195], a[228:231], a[184:187], v[192:195]\n"
    "s_waitcnt lgkmcnt(0)\n"
    "v_add_f32_dpp v28, v252, v28 row_newbcast:12 row_mask:0xf bank_mask:0xf\n"
    "v_add_f32_dpp v29, v252, v29 row_newbcast:13 row_mask:0xf bank_mask:0xf\n"
    "v_mfma_f32_16x16x32_bf16 v[224:227], a[228:231], a[188:191], v[224:227]\n"
    "v_add_f32_dpp v30, v252, v30 row_newbcast:14 row_mask:0xf bank_mask:0xf\n"
    "v_mfma_f32_16x16x32_bf16 v[224:227], a[224:227], a[156:159], v[224:227]\n"
    "s_waitcnt vmcnt(12)\n"
    "s_addk_i32 s20, 4096\n"
    "v_add_f32_dpp v31, v252, v31 row_newbcast:15 row_mask:0xf bank_mask:0xf\n"
    "v_mfma_f32_16x16x32_bf16 v[228:231], a[232:235], a[156:159], v[228:231]\n"
    "buffer_load_dwordx4 a[224:227], v240, s[20:23], 0 offen offset:0 sc1\n"
    "v_add_f32_dpp v48, v252, v48 row_newbcast:0 row_mask:0xf bank_mask:0xf\n"
    "v_mfma_f32_16x16x32_bf16 v[228:231], a[236:239], a[188:191], v[228:231]\n"
    "buffer_load_dwordx4 a[228:231], v240, s[20:23], 0 offen offset:1024 sc1\n"
    "v_add_f32_dpp v49, v252, v49 row_newbcast:1 row_mask:0xf bank_mask:0xf\n"
    "v_mfma_f32_16x16x32_bf16 v[196:199], a[236:239], a[184:187], v[196:199]\n"
    "v_add_f32_dpp v50, v252, v50 row_newbcast:2 row_mask:0xf bank_mask:0xf\n"
    "v_add_f32_dpp v51, v252, v51 row_newbcast:3 row_mask:0xf bank_mask:0xf\n"
    "v_mfma_f32_16x16x32_bf16 v[196:199], a[232:235], a[152:155], v[196:199]\n"
    "v_add_f32_dpp v52, v252, v52 row_newbcast:4 row_mask:0xf bank_mask:0xf\n"
    "v_mfma_f32_16x16x32_bf16 v[164:167], a[232:235], a[148:151], v[164:167]\n"
    "v_add_f32_dpp v53, v252, v53 row_newbcast:5 row_mask:0xf bank_mask:0xf\n"
    "v_add_f32_dpp v54, v252, v54 row_newbcast:6 row_mask:0xf bank_mask:0xf\n"
    "v_mfma_f32_16x16x32_bf16 v[164:167], a[236:239], a[180:183], v[164:167]\n"
    "v_add_f32_dpp v55, v252, v55 row_newbcast:7 row_mask:0xf bank_mask:0xf\n"
    "v_mfma_f32_16x16x32_bf16 v[132:135], a[236:239], a[176:179], v[132:135]\n"
    "v_add_f32_dpp v56, v252, v56 row_newbcast:8 row_mask:0xf bank_mask:0xf\n"
    "v_mfma_f32_16x16x32_bf16 v[132:135], a[232:235], a[144:147], v[132:135]\n"
    "v_add_f32_dpp v57, v252, v57 row_newbcast:9 row_mask:0xf bank_mask:0xf\n"
    "v_add_f32_dpp v58, v252, v58 row_newbcast:10 row_mask:0xf bank_mask:0xf\n"
    "v_mfma_f32_16x16x32_bf16 v[100:103], a[232:235], a[140:143], v[100:103]\n"
    "v_add_f32_dpp v59, v252, v59 row_newbcast:11 row_mask:0xf bank_mask:0xf\n"
    "v_mfma_f32_16x16x32_bf16 v[100:103], a[236:239], a[172:175], v[100:103]\n"
    "v_add_f32_dpp v60, v252, v60 row_newbcast:12 row_mask:0xf bank_mask:0xf\n"
    "v_mfma_f32_16x16x32_bf16 v[68:71], a[236:239], a[168:171], v[68:71]\n"
    "s_movk_i32 s60, 0x70\n"
    "v_add_f32_dpp v61, v252, v61 row_newbcast:13 row_mask:0xf bank_mask:0xf\n"
    "v_add_f32_dpp v62, v252, v62 row_newbcast:14 row_mask:0xf bank_mask:0xf\n"
    "v_mfma_f32_16x16x32_bf16 v[68:71], a[232:235], a[136:139], v[68:71]\n"
    "v_add_f32_dpp v63, v252, v63 row_newbcast:15 row_mask:0xf bank_mask:0xf\n"
    "v_mfma_f32_16x16x32_bf16 v[36:39], a[232:235], a[132:135], v[36:39]\n"
    "v_lshrrev_b32 v248, 4, v240\n"
    "v_xor_b32 v248, v248, v240\n"
    "v_mfma_f32_16x16x32_bf16 v[36:39], a[236:239], a[164:167], v[36:39]\n"
    "v_and_b32 v248, 0x70, v248\n"
    "v_and_b32 v249, 0xf0, v240\n"
    "v_mfma_f32_16x16x32_bf16 v[4:7], a[236:239], a[160:163], v[4:7]\n"
    "v_lshl_add_u32 v248, v249, 3, v248\n"
    "s_lshl_b32 s61, s65, 12\n"
    "v_mfma_f32_16x16x32_bf16 v[4:7], a[232:235], a[128:131], v[4:7]\n"
    "s_waitcnt vmcnt(11)\n"
    "s_add_u32 s61, s61, s66\n"
    "v_add_u32 v248, s61, v248\n"
    "v_xor_b32 v249, 64, v248\n"
    "v_lshrrev_b32 v242, 3, v240\n"
    "v_and_b32 v242, 0x70, v242\n"
    "v_xor_b32 v242, v242, v240\n"
    "v_add_u32 v242, s61, v242\n"
    "v_and_b32 v243, 0x380, v240\n"
    "v_lshlrev_b32 v243, 4, v243\n"
    "v_and_or_b32 v243, v240, s60, v243\n"
    "v_mfma_f32_16x16x32_bf16 v[8:11], a[192:195], a[128:131], v[8:11]\n"
    "s_add_u32 s96, s20, 0xf000\n"
    "s_mov_b32 s97, s21\n"
    "v_add_f32_dpp v80, v252, v80 row_newbcast:0 row_mask:0xf bank_mask:0xf\n"
    "v_mfma_f32_16x16x32_bf16 v[8:11], a[196:199], a[160:163], v[8:11]\n"
    "buffer_load_dwordx4 a[232:235], v240, s[20:23], 0 offen offset:2048 sc1\n"
    "v_add_f32_dpp v81, v252, v81 row_newbcast:1 row_mask:0xf bank_mask:0xf\n"
    "v_mfma_f32_16x16x32_bf16 v[40:43], a[196:199], a[164:167], v[40:43]\n"
    "buffer_load_dwordx4 a[236:239], v240, s[20:23], 0 offen offset:3072 sc1\n"
    "v_add_f32_dpp v82, v252, v82 row_newbcast:2 row_mask:0xf bank_mask:0xf\n"
    "v_mfma_f32_16x16x32_bf16 v[40:43], a[192:195], a[132:135], v[40:43]\n"
    "v_add_f32_dpp v83, v252, v83 row_newbcast:3 row_mask:0xf bank_mask:0xf\n"
    "v_add_f32_dpp v84, v252, v84 row_newbcast:4 row_mask:0xf bank_mask:0xf\n"
    "v_mfma_f32_16x16x32_bf16 v[72:75], a[192:195], a[136:139], v[72:75]\n"
    "v_add_f32_dpp v85, v252, v85 row_newbcast:5 row_mask:0xf bank_mask:0xf\n"
    "v_add_f32_dpp v86, v252, v86 row_newbcast:6 row_mask:0xf bank_mask:0xf\n"
    "v_mfma_f32_16x16x32_bf16 v[72:75], a[196:199], a[168:171], v[72:75]\n"
    "v_add_f32_dpp v87, v252, v87 row_newbcast:7 row_mask:0xf bank_mask:0xf\n"
    "v_add_f32_dpp v88, v252, v88 row_newbcast:8 row_mask:0xf bank_mask:0xf\n"
    "v_mfma_f32_16x16x32_bf16 v[104:107], a[196:199], a[172:175], v[104:107]\n"
    "v_add_f32_dpp v89, v252, v89 row_newbcast:9 row_mask:0xf bank_mask:0xf\n"
    "v_add_f32_dpp v90, v252, v90 row_newbcast:10 row_mask:0xf bank_mask:0xf\n"
    "v_mfma_f32_16x16x32_bf16 v[104:107], a[192:195], a[140:143], v[104:107]\n"
    "v_add_f32_dpp v91, v252, v91 row_newbcast:11 row_mask:0xf bank_mask:0xf\n"
    "v_add_f32_dpp v92, v252, v92 row_newbcast:12 row_mask:0xf bank_mask:0xf\n"
    "v_mfma_f32_16x16x32_bf16 v[136:139], a[192:195], a[144:147], v[136:139]\n"
    "v_add_f32_dpp v93, v252, v93 row_newbcast:13 row_mask:0xf bank_mask:0xf\n"
    "v_add_f32_dpp v94, v252, v94 row_newbcast:14 row_mask:0xf bank_mask:0xf\n"
    "v_mfma_f32_16x16x32_bf16 v[136:139], a[196:199], a[176:179], v[136:139]\n"
    "v_add_f32_dpp v95, v252, v95 row_newbcast:15 row_mask:0xf bank_mask:0xf\n"
    "v_add_f32_dpp v112, v252, v112 row_newbcast:0 row_mask:0xf bank_mask:0xf\n"
    "v_mfma_f32_16x16x32_bf16 v[168:171], a[196:199], a[180:183], v[168:171]\n"
    "v_add_f32_dpp v113, v252, v113 row_newbcast:1 row_mask:0xf bank_mask:0xf\n"
    "v_add_f32_dpp v114, v252, v114 row_newbcast:2 row_mask:0xf bank_mask:0xf\n"
    "v_mfma_f32_16x16x32_bf16 v[168:171], a[192:195], a[148:151], v[168:171]\n"
    "v_add_f32_dpp v115, v252, v115 row_newbcast:3 row_mask:0xf bank_mask:0xf\n"
    "v_add_f32_dpp v116, v252, v116 row_newbcast:4 row_mask:0xf bank_mask:0xf\n"
    "v_mfma_f32_16x16x32_bf16 v[200:203], a[192:195], a[152:155], v[200:203]\n"
    "v_add_f32_dpp v117, v252, v117 row_newbcast:5 row_mask:0xf bank_mask:0xf\n"
    "v_mfma_f32_16x16x32_bf16 v[200:203], a[196:199], a[184:187], v[200:203]\n"
    "v_add_f32_dpp v118, v252, v118 row_newbcast:6 row_mask:0xf bank_mask:0xf\n"
    "v_add_f32_dpp v119, v252, v119 row_newbcast:7 row_mask:0xf bank_mask:0xf\n"
    "v_mfma_f32_16x16x32_bf16 v[232:235], a[196:199], a[188:191], v[232:235]\n"
    "v_add_f32_dpp v120, v252, v120 row_newbcast:8 row_mask:0xf bank_mask:0xf\n"
    "v_mfma_f32_16x16x32_bf16 v[232:235], a[192:195], a[156:159], v[232:235]\n"
    "s_waitcnt vmcnt(10)\n"
    "s_addk_i32 s20, 12288\n"
    "v_add_f32_dpp v121, v252, v121 row_newbcast:9 row_mask:0xf bank_mask:0xf\n"
    "v_mfma_f32_16x16x32_bf16 v[236:239], a[200:203], a[156:159], v[236:239]\n"
    "buffer_load_dwordx4 a[192:195], v240, s[20:23], 0 offen offset:0 sc1\n"
    "v_add_f32_dpp v122, v252, v122 row_newbcast:10 row_mask:0xf bank_mask:0xf\n"
    "v_mfma_f32_16x16x32_bf16 v[236:239], a[204:207], a[188:191], v[236:239]\n"
    "buffer_load_dwordx4 a[196:199], v240, s[20:23], 0 offen offset:1024 sc1\n"
    "v_add_f32_dpp v123, v252, v123 row_newbcast:11 row_mask:0xf bank_mask:0xf\n"
    "v_mfma_f32_16x16x32_bf16 v[204:207], a[204:207], a[184:187], v[204:207]\n"
    "ds_read_b128 a[156:159], v241 offset:47104\n"
    "v_add_f32_dpp v124, v252, v124 row_newbcast:12 row_mask:0xf bank_mask:0xf\n"
    "v_mfma_f32_16x16x32_bf16 v[204:207], a[200:203], a[152:155], v[204:207]\n"
    "ds_read_b128 a[188:191], v241 offset:48128\n"
    "v_add_f32_dpp v125, v252, v125 row_newbcast:13 row_mask:0xf bank_mask:0xf\n"
    "v_mfma_f32_16x16x32_bf16 v[172:175], a[200:203], a[148:151], v[172:175]\n"
    "ds_read_b128 a[184:187], v241 offset:46080\n"
    "v_add_f32_dpp v126, v252, v126 row_newbcast:14 row_mask:0xf bank_mask:0xf\n"
    "v_mfma_f32_16x16x32_bf16 v[172:175], a[204:207], a[180:183], v[172:175]\n"
    "ds_read_b128 a[152:155], v241 offset:45056\n"
    "v_add_f32_dpp v127, v252, v127 row_newbcast:15 row_mask:0xf bank_mask:0xf\n"
    "v_mfma_f32_16x16x32_bf16 v[140:143], a[204:207], a[176:179], v[140:143]\n"
    "ds_read_b128 a[148:151], v241 offset:43008\n"
    "v_add_f32_dpp v144, v252, v144 row_newbcast:0 row_mask:0xf bank_mask:0xf\n"
    "v_mfma_f32_16x16x32_bf16 v[140:143], a[200:203], a[144:147], v[140:143]\n"
    "ds_read_b128 a[180:183], v241 offset:44032\n"
    "v_add_f32_dpp v145, v252, v145 row_newbcast:1 row_mask:0xf bank_mask:0xf\n"
    "v_mfma_f32_16x16x32_bf16 v[108:111], a[200:203], a[140:143], v[108:111]\n"
    "ds_read_b128 a[176:179], v241 offset:41984\n"
    "v_add_f32_dpp v146, v252, v146 row_newbcast:2 row_mask:0xf bank_mask:0xf\n"
    "v_mfma_f32_16x16x32_bf16 v[108:111], a[204:207], a[172:175], v[108:111]\n"
    "ds_read_b128 a[144:147], v241 offset:40960\n"
    "v_add_f32_dpp v147, v252, v147 row_newbcast:3 row_mask:0xf bank_mask:0xf\n"
    "v_mfma_f32_16x16x32_bf16 v[76:79], a[204:207], a[168:171], v[76:79]\n"
    "ds_read_b128 a[140:143], v241 offset:38912\n"
    "v_add_f32_dpp v148, v252, v148 row_newbcast:4 row_mask:0xf bank_mask:0xf\n"
    "v_mfma_f32_16x16x32_bf16 v[76:79], a[200:203], a[136:139], v[76:79]\n"
    "ds_read_b128 a[172:175], v241 offset:39936\n"
    "v_add_f32_dpp v149, v252, v149 row_newbcast:5 row_mask:0xf bank_mask:0xf\n"
    "v_mfma_f32_16x16x32_bf16 v[44:47], a[200:203], a[132:135], v[44:47]\n"
    "ds_read_b128 a[168:171], v241 offset:37888\n"
    "v_add_f32_dpp v150, v252, v150 row_newbcast:6 row_mask:0xf bank_mask:0xf\n"
    "v_mfma_f32_16x16x32_bf16 v[44:47], a[204:207], a[164:167], v[44:47]\n"
    "ds_read_b128 a[136:139], v241 offset:36864\n"
    "v_add_f32_dpp v151, v252, v151 row_newbcast:7 row_mask:0xf bank_mask:0xf\n"
    "v_mfma_f32_16x16x32_bf16 v[12:15], a[204:207], a[160:163], v[12:15]\n"
    "ds_read_b128 a[132:135], v241 offset:34816\n"
    "v_add_f32_dpp v152, v252, v152 row_newbcast:8 row_mask:0xf bank_mask:0xf\n"
    "v_mfma_f32_16x16x32_bf16 v[12:15], a[200:203], a[128:131], v[12:15]\n"
    "s_waitcnt vmcnt(9) lgkmcnt(11)\n"
    "ds_read_b128 a[164:167], v241 offset:35840\n"
    "v_add_f32_dpp v153, v252, v153 row_newbcast:9 row_mask:0xf bank_mask:0xf\n"
    "v_mfma_f32_16x16x32_bf16 v[224:227], a[208:211], a[156:159], v[224:227]\n"
    "buffer_load_dwordx4 a[200:203], v240, s[20:23], 0 offen offset:2048 sc1\n"
    "v_add_f32_dpp v154, v252, v154 row_newbcast:10 row_mask:0xf bank_mask:0xf\n"
    "v_mfma_f32_16x16x32_bf16 v[224:227], a[212:215], a[188:191], v[224:227]\n"
    "s_waitcnt lgkmcnt(10)\n"
    "buffer_load_dwordx4 a[204:207], v240, s[20:23], 0 offen offset:3072 sc1\n"
    "v_mfma_f32_16x16x32_bf16 v[192:195], a[212:215], a[184:187], v[192:195]\n"
    "ds_read_b128 a[160:163], v241 offset:33792\n"
    "v_add_f32_dpp v155, v252, v155 row_newbcast:11 row_mask:0xf bank_mask:0xf\n"
    "v_mfma_f32_16x16x32_bf16 v[192:195], a[208:211], a[152:155], v[192:195]\n"
    "s_waitcnt lgkmcnt(9)\n"
    "ds_read_b128 a[128:131], v241 offset:32768\n"
    "v_add_f32_dpp v156, v252, v156 row_newbcast:12 row_mask:0xf bank_mask:0xf\n"
    "v_mfma_f32_16x16x32_bf16 v[160:163], a[208:211], a[148:151], v[160:163]\n"
    "v_add_f32_dpp v157, v252, v157 row_newbcast:13 row_mask:0xf bank_mask:0xf\n"
    "v_add_f32_dpp v158, v252, v158 row_newbcast:14 row_mask:0xf bank_mask:0xf\n"
    "v_mfma_f32_16x16x32_bf16 v[160:163], a[212:215], a[180:183], v[160:163]\n"
    "s_waitcnt lgkmcnt(8)\n"
    "v_add_f32_dpp v159, v252, v159 row_newbcast:15 row_mask:0xf bank_mask:0xf\n"
    "v_add_f32_dpp v176, v252, v176 row_newbcast:0 row_mask:0xf bank_mask:0xf\n"
    "v_mfma_f32_16x16x32_bf16 v[128:131], a[212:215], a[176:179], v[128:131]\n"
    "v_add_f32_dpp v177, v252, v177 row_newbcast:1 row_mask:0xf bank_mask:0xf\n"
    "v_add_f32_dpp v178, v252, v178 row_newbcast:2 row_mask:0xf bank_mask:0xf\n"
    "v_mfma_f32_16x16x32_bf16 v[128:131], a[208:211], a[144:147], v[128:131]\n"
    "s_waitcnt lgkmcnt(6)\n"
    "v_add_f32_dpp v179, v252, v179 row_newbcast:3 row_mask:0xf bank_mask:0xf\n"
    "v_add_f32_dpp v180, v252, v180 row_newbcast:4 row_mask:0xf bank_mask:0xf\n"
    "v_mfma_f32_16x16x32_bf16 v[96:99], a[208:211], a[140:143], v[96:99]\n"
    "v_add_f32_dpp v181, v252, v181 row_newbcast:5 row_mask:0xf bank_mask:0xf\n"
    "v_add_f32_dpp v182, v252, v182 row_newbcast:6 row_mask:0xf bank_mask:0xf\n"
    "v_mfma_f32_16x16x32_bf16 v[96:99], a[212:215], a[172:175], v[96:99]\n"
    "s_waitcnt lgkmcnt(4)\n"
    "v_add_f32_dpp v183, v252, v183 row_newbcast:7 row_mask:0xf bank_mask:0xf\n"
    "v_add_f32_dpp v184, v252, v184 row_newbcast:8 row_mask:0xf bank_mask:0xf\n"
    "v_mfma_f32_16x16x32_bf16 v[64:67], a[212:215], a[168:171], v[64:67]\n"
    "v_add_f32_dpp v185, v252, v185 row_newbcast:9 row_mask:0xf bank_mask:0xf\n"
    "v_add_f32_dpp v186, v252, v186 row_newbcast:10 row_mask:0xf bank_mask:0xf\n"
    "v_mfma_f32_16x16x32_bf16 v[64:67], a[208:211], a[136:139], v[64:67]\n"
    "s_waitcnt lgkmcnt(2)\n"
    "v_add_f32_dpp v187, v252, v187 row_newbcast:11 row_mask:0xf bank_mask:0xf\n"
    "v_add_f32_dpp v188, v252, v188 row_newbcast:12 row_mask:0xf bank_mask:0xf\n"
    "v_mfma_f32_16x16x32_bf16 v[32:35], a[208:211], a[132:135], v[32:35]\n"
    "v_add_f32_dpp v189, v252, v189 row_newbcast:13 row_mask:0xf bank_mask:0xf\n"
    "v_add_f32_dpp v190, v252, v190 row_newbcast:14 row_mask:0xf bank_mask:0xf\n"
    "v_mfma_f32_16x16x32_bf16 v[32:35], a[212:215], a[164:167], v[32:35]\n"
    "s_waitcnt lgkmcnt(0)\n"
    "v_add_f32_dpp v191, v252, v191 row_newbcast:15 row_mask:0xf bank_mask:0xf\n"
    "v_add_f32_dpp v208, v252, v208 row_newbcast:0 row_mask:0xf bank_mask:0xf\n"
    "v_mfma_f32_16x16x32_bf16 v[0:3], a[212:215], a[160:163], v[0:3]\n"
    "v_add_f32_dpp v209, v252, v209 row_newbcast:1 row_mask:0xf bank_mask:0xf\n"
    "v_add_f32_dpp v210, v252, v210 row_newbcast:2 row_mask:0xf bank_mask:0xf\n"
    "v_mfma_f32_16x16x32_bf16 v[0:3], a[208:211], a[128:131], v[0:3]\n"
    "s_waitcnt vmcnt(8)\n"
    "s_addk_i32 s20, 4096\n"
    "v_add_f32_dpp v211, v252, v211 row_newbcast:3 row_mask:0xf bank_mask:0xf\n"
    "v_mfma_f32_16x16x32_bf16 v[4:7], a[216:219], a[128:131], v[4:7]\n"
    "buffer_load_dwordx4 a[208:211], v240, s[20:23], 0 offen offset:0 sc1\n"
    "v_add_f32_dpp v212, v252, v212 row_newbcast:4 row_mask:0xf bank_mask:0xf\n"
    "v_mfma_f32_16x16x32_bf16 v[4:7], a[220:223], a[160:163], v[4:7]\n"
    "buffer_load_dwordx4 a[212:215], v240, s[20:23], 0 offen offset:1024 sc1\n"
    "v_add_f32_dpp v213, v252, v213 row_newbcast:5 row_mask:0xf bank_mask:0xf\n"
    "v_mfma_f32_16x16x32_bf16 v[36:39], a[220:223], a[164:167], v[36:39]\n"
    "v_add_f32_dpp v214, v252, v214 row_newbcast:6 row_mask:0xf bank_mask:0xf\n"
    "v_add_f32_dpp v215, v252, v215 row_newbcast:7 row_mask:0xf bank_mask:0xf\n"
    "v_mfma_f32_16x16x32_bf16 v[36:39], a[216:219], a[132:135], v[36:39]\n"
    "v_add_f32_dpp v216, v252, v216 row_newbcast:8 row_mask:0xf bank_mask:0xf\n"
    "v_mfma_f32_16x16x32_bf16 v[68:71], a[216:219], a[136:139], v[68:71]\n"
    "v_add_f32_dpp v217, v252, v217 row_newbcast:9 row_mask:0xf bank_mask:0xf\n"
    "v_add_f32_dpp v218, v252, v218 row_newbcast:10 row_mask:0xf bank_mask:0xf\n"
    "v_mfma_f32_16x16x32_bf16 v[68:71], a[220:223], a[168:171], v[68:71]\n"
    "v_add_f32_dpp v219, v252, v219 row_newbcast:11 row_mask:0xf bank_mask:0xf\n"
    "v_mfma_f32_16x16x32_bf16 v[100:103], a[220:223], a[172:175], v[100:103]\n"
    "v_add_f32_dpp v220, v252, v220 row_newbcast:12 row_mask:0xf bank_mask:0xf\n"
    "v_mfma_f32_16x16x32_bf16 v[100:103], a[216:219], a[140:143], v[100:103]\n"
    "v_add_f32_dpp v221, v252, v221 row_newbcast:13 row_mask:0xf bank_mask:0xf\n"
    "v_add_f32_dpp v222, v252, v222 row_newbcast:14 row_mask:0xf bank_mask:0xf\n"
    "v_mfma_f32_16x16x32_bf16 v[132:135], a[216:219], a[144:147], v[132:135]\n"
    "v_add_f32_dpp v223, v252, v223 row_newbcast:15 row_mask:0xf bank_mask:0xf\n"
    "v_mfma_f32_16x16x32_bf16 v[132:135], a[220:223], a[176:179], v[132:135]\n"
    "v_accvgpr_read_b32 v244, a240\n"
    "v_mfma_f32_16x16x32_bf16 v[164:167], a[220:223], a[180:183], v[164:167]\n"
    "v_accvgpr_read_b32 v245, a241\n"
    "v_accvgpr_read_b32 v246, a242\n"
    "v_mfma_f32_16x16x32_bf16 v[164:167], a[216:219], a[148:151], v[164:167]\n"
    "v_accvgpr_read_b32 v247, a243\n"
    "v_mfma_f32_16x16x32_bf16 v[196:199], a[216:219], a[152:155], v[196:199]\n"
    "v_add_f32_dpp v244, v252, v244 row_newbcast:0 row_mask:0xf bank_mask:0xf\n"
    "v_mfma_f32_16x16x32_bf16 v[196:199], a[220:223], a[184:187], v[196:199]\n"
    "v_add_f32_dpp v245, v252, v245 row_newbcast:1 row_mask:0xf bank_mask:0xf\n"
    "v_add_f32_dpp v246, v252, v246 row_newbcast:2 row_mask:0xf bank_mask:0xf\n"
    "v_mfma_f32_16x16x32_bf16 v[228:231], a[220:223], a[188:191], v[228:231]\n"
    "v_add_f32_dpp v247, v252, v247 row_newbcast:3 row_mask:0xf bank_mask:0xf\n"
    "v_mfma_f32_16x16x32_bf16 v[228:231], a[216:219], a[156:159], v[228:231]\n"
    "s_waitcnt vmcnt(8)\n"
    "buffer_load_dwordx4 a[216:219], v240, s[20:23], 0 offen offset:2048 sc1\n"
    "v_mfma_f32_16x16x32_bf16 v[232:235], a[224:227], a[156:159], v[232:235]\n"
    "buffer_load_dwordx4 a[220:223], v240, s[20:23], 0 offen offset:3072 sc1\n"
    "v_accvgpr_write_b32 a240, v244\n"
    "v_mfma_f32_16x16x32_bf16 v[232:235], a[228:231], a[188:191], v[232:235]\n"
    "v_accvgpr_write_b32 a241, v245\n"
    "v_accvgpr_write_b32 a242, v246\n"
    "v_mfma_f32_16x16x32_bf16 v[200:203], a[228:231], a[184:187], v[200:203]\n"
    "v_accvgpr_write_b32 a243, v247\n"
    "v_accvgpr_read_b32 v244, a244\n"
    "v_mfma_f32_16x16x32_bf16 v[200:203], a[224:227], a[152:155], v[200:203]\n"
    "v_accvgpr_read_b32 v245, a245\n"
    "v_mfma_f32_16x16x32_bf16 v[168:171], a[224:227], a[148:151], v[168:171]\n"
    "v_accvgpr_read_b32 v246, a246\n"
    "v_accvgpr_read_b32 v247, a247\n"
    "v_mfma_f32_16x16x32_bf16 v[168:171], a[228:231], a[180:183], v[168:171]\n"
    "v_add_f32_dpp v244, v252, v244 row_newbcast:4 row_mask:0xf bank_mask:0xf\n"
    "v_mfma_f32_16x16x32_bf16 v[136:139], a[228:231], a[176:179], v[136:139]\n"
    "v_add_f32_dpp v245, v252, v245 row_newbcast:5 row_mask:0xf bank_mask:0xf\n"
    "v_mfma_f32_16x16x32_bf16 v[136:139], a[224:227], a[144:147], v[136:139]\n"
    "v_add_f32_dpp v246, v252, v246 row_newbcast:6 row_mask:0xf bank_mask:0xf\n"
    "v_add_f32_dpp v247, v252, v247 row_newbcast:7 row_mask:0xf bank_mask:0xf\n"
    "v_mfma_f32_16x16x32_bf16 v[104:107], a[224:227], a[140:143], v[104:107]\n"
    "v_accvgpr_write_b32 a244, v244\n"
    "v_mfma_f32_16x16x32_bf16 v[104:107], a[228:231], a[172:175], v[104:107]\n"
    "v_accvgpr_write_b32 a245, v245\n"
    "v_mfma_f32_16x16x32_bf16 v[72:75], a[228:231], a[168:171], v[72:75]\n"
    "v_accvgpr_write_b32 a246, v246\n"
    "v_accvgpr_write_b32 a247, v247\n"
    "v_mfma_f32_16x16x32_bf16 v[72:75], a[224:227], a[136:139], v[72:75]\n"
    "v_accvgpr_read_b32 v244, a248\n"
    "v_mfma_f32_16x16x32_bf16 v[40:43], a[224:227], a[132:135], v[40:43]\n"
    "v_accvgpr_read_b32 v245, a249\n"
    "v_mfma_f32_16x16x32_bf16 v[40:43], a[228:231], a[164:167], v[40:43]\n"
    "v_accvgpr_read_b32 v246, a250\n"
    "v_accvgpr_read_b32 v247, a251\n"
    "v_mfma_f32_16x16x32_bf16 v[8:11], a[228:231], a[160:163], v[8:11]\n"
    "v_add_f32_dpp v244, v252, v244 row_newbcast:8 row_mask:0xf bank_mask:0xf\n"
    "v_mfma_f32_16x16x32_bf16 v[8:11], a[224:227], a[128:131], v[8:11]\n"
    "s_waitcnt vmcnt(8)\n"
    "s_addk_i32 s20, 12288\n"
    "v_add_f32_dpp v245, v252, v245 row_newbcast:9 row_mask:0xf bank_mask:0xf\n"
    "v_mfma_f32_16x16x32_bf16 v[12:15], a[232:235], a[128:131], v[12:15]\n"
    "buffer_load_dwordx4 a[224:227], v240, s[20:23], 0 offen offset:0 sc1\n"
    "v_add_f32_dpp v246, v252, v246 row_newbcast:10 row_mask:0xf bank_mask:0xf\n"
    "v_mfma_f32_16x16x32_bf16 v[12:15], a[236:239], a[160:163], v[12:15]\n"
    "buffer_load_dwordx4 a[228:231], v240, s[20:23], 0 offen offset:1024 sc1\n"
    "v_add_f32_dpp v247, v252, v247 row_newbcast:11 row_mask:0xf bank_mask:0xf\n"
    "v_mfma_f32_16x16x32_bf16 v[44:47], a[236:239], a[164:167], v[44:47]\n"
    "ds_read_b128 a[128:131], v241 offset:49152\n"
    "v_accvgpr_write_b32 a248, v244\n"
    "v_mfma_f32_16x16x32_bf16 v[44:47], a[232:235], a[132:135], v[44:47]\n"
    "ds_read_b128 a[160:163], v241 offset:50176\n"
    "v_accvgpr_write_b32 a249, v245\n"
    "v_mfma_f32_16x16x32_bf16 v[76:79], a[232:235], a[136:139], v[76:79]\n"
    "ds_read_b128 a[164:167], v241 offset:52224\n"
    "v_accvgpr_write_b32 a250, v246\n"
    "v_mfma_f32_16x16x32_bf16 v[76:79], a[236:239], a[168:171], v[76:79]\n"
    "ds_read_b128 a[132:135], v241 offset:51200\n"
    "v_accvgpr_write_b32 a251, v247\n"
    "v_mfma_f32_16x16x32_bf16 v[108:111], a[236:239], a[172:175], v[108:111]\n"
    "ds_read_b128 a[136:139], v241 offset:53248\n"
    "v_accvgpr_read_b32 v244, a252\n"
    "v_mfma_f32_16x16x32_bf16 v[108:111], a[232:235], a[140:143], v[108:111]\n"
    "ds_read_b128 a[168:171], v241 offset:54272\n"
    "v_accvgpr_read_b32 v245, a253\n"
    "v_mfma_f32_16x16x32_bf16 v[140:143], a[232:235], a[144:147], v[140:143]\n"
    "ds_read_b128 a[172:175], v241 offset:56320\n"
    "v_accvgpr_read_b32 v246, a254\n"
    "v_mfma_f32_16x16x32_bf16 v[140:143], a[236:239], a[176:179], v[140:143]\n"
    "ds_read_b128 a[140:143], v241 offset:55296\n"
    "v_accvgpr_read_b32 v247, a255\n"
    "v_mfma_f32_16x16x32_bf16 v[172:175], a[236:239], a[180:183], v[172:175]\n"
    "ds_read_b128 a[144:147], v241 offset:57344\n"
    "v_add_f32_dpp v244, v252, v244 row_newbcast:12 row_mask:0xf bank_mask:0xf\n"
    "v_mfma_f32_16x16x32_bf16 v[172:175], a[232:235], a[148:151], v[172:175]\n"
    "ds_read_b128 a[176:179], v241 offset:58368\n"
    "v_add_f32_dpp v245, v252, v245 row_newbcast:13 row_mask:0xf bank_mask:0xf\n"
    "v_mfma_f32_16x16x32_bf16 v[204:207], a[232:235], a[152:155], v[204:207]\n"
    "ds_read_b128 a[180:183], v241 offset:60416\n"
    "v_add_f32_dpp v246, v252, v246 row_newbcast:14 row_mask:0xf bank_mask:0xf\n"
    "v_mfma_f32_16x16x32_bf16 v[204:207], a[236:239], a[184:187], v[204:207]\n"
    "ds_read_b128 a[148:151], v241 offset:59392\n"
    "v_add_f32_dpp v247, v252, v247 row_newbcast:15 row_mask:0xf bank_mask:0xf\n"
    "v_mfma_f32_16x16x32_bf16 v[236:239], a[236:239], a[188:191], v[236:239]\n"
    "ds_read_b128 a[152:155], v241 offset:61440\n"
    "v_accvgpr_write_b32 a252, v244\n"
    "v_mfma_f32_16x16x32_bf16 v[236:239], a[232:235], a[156:159], v[236:239]\n"
    "s_waitcnt vmcnt(8) lgkmcnt(11)\n"
    "ds_read_b128 a[184:187], v241 offset:62464\n"
    "v_accvgpr_write_b32 a253, v245\n"
    "v_mfma_f32_16x16x32_bf16 v[0:3], a[192:195], a[128:131], v[0:3]\n"
    "buffer_load_dwordx4 a[232:235], v240, s[20:23], 0 offen offset:2048 sc1\n"
    "v_accvgpr_write_b32 a254, v246\n"
    "v_mfma_f32_16x16x32_bf16 v[0:3], a[196:199], a[160:163], v[0:3]\n"
    "s_waitcnt lgkmcnt(10)\n"
    "buffer_load_dwordx4 a[236:239], v240, s[20:23], 0 offen offset:3072 sc1\n"
    "v_mfma_f32_16x16x32_bf16 v[32:35], a[196:199], a[164:167], v[32:35]\n"
    "ds_read_b128 a[188:191], v241 offset:64512\n"
    "v_add_f32_dpp v8, v253, v8 row_newbcast:8 row_mask:0xf bank_mask:0xf\n"
    "v_mfma_f32_16x16x32_bf16 v[32:35], a[192:195], a[132:135], v[32:35]\n"
    "s_waitcnt lgkmcnt(9)\n"
    "ds_read_b128 a[156:159], v241 offset:63488\n"
    "v_accvgpr_write_b32 a255, v247\n"
    "v_mfma_f32_16x16x32_bf16 v[64:67], a[192:195], a[136:139], v[64:67]\n"
    "v_add_f32_dpp v9, v253, v9 row_newbcast:9 row_mask:0xf bank_mask:0xf\n"
    "v_add_f32_dpp v10, v253, v10 row_newbcast:10 row_mask:0xf bank_mask:0xf\n"
    "v_mfma_f32_16x16x32_bf16 v[64:67], a[196:199], a[168:171], v[64:67]\n"
    "s_waitcnt lgkmcnt(8)\n"
    "v_add_f32_dpp v11, v253, v11 row_newbcast:11 row_mask:0xf bank_mask:0xf\n"
    "v_add_f32_dpp v40, v253, v40 row_newbcast:8 row_mask:0xf bank_mask:0xf\n"
    "v_mfma_f32_16x16x32_bf16 v[96:99], a[196:199], a[172:175], v[96:99]\n"
    "v_add_f32_dpp v41, v253, v41 row_newbcast:9 row_mask:0xf bank_mask:0xf\n"
    "v_add_f32_dpp v42, v253, v42 row_newbcast:10 row_mask:0xf bank_mask:0xf\n"
    "v_mfma_f32_16x16x32_bf16 v[96:99], a[192:195], a[140:143], v[96:99]\n"
    "s_waitcnt lgkmcnt(6)\n"
    "v_add_f32_dpp v43, v253, v43 row_newbcast:11 row_mask:0xf bank_mask:0xf\n"
    "v_add_f32_dpp v72, v253, v72 row_newbcast:8 row_mask:0xf bank_mask:0xf\n"
    "v_mfma_f32_16x16x32_bf16 v[128:131], a[192:195], a[144:147], v[128:131]\n"
    "v_add_f32_dpp v73, v253, v73 row_newbcast:9 row_mask:0xf bank_mask:0xf\n"
    "v_add_f32_dpp v74, v253, v74 row_newbcast:10 row_mask:0xf bank_mask:0xf\n"
    "v_mfma_f32_16x16x32_bf16 v[128:131], a[196:199], a[176:179], v[128:131]\n"
    "s_waitcnt lgkmcnt(4)\n"
    "v_add_f32_dpp v75, v253, v75 row_newbcast:11 row_mask:0xf bank_mask:0xf\n"
    "v_add_f32_dpp v104, v253, v104 row_newbcast:8 row_mask:0xf bank_mask:0xf\n"
    "v_mfma_f32_16x16x32_bf16 v[160:163], a[196:199], a[180:183], v[160:163]\n"
    "v_add_f32_dpp v105, v253, v105 row_newbcast:9 row_mask:0xf bank_mask:0xf\n"
    "v_add_f32_dpp v106, v253, v106 row_newbcast:10 row_mask:0xf bank_mask:0xf\n"
    "v_mfma_f32_16x16x32_bf16 v[160:163], a[192:195], a[148:151], v[160:163]\n"
    "s_waitcnt lgkmcnt(2)\n"
    "v_add_f32_dpp v107, v253, v107 row_newbcast:11 row_mask:0xf bank_mask:0xf\n"
    "v_mfma_f32_16x16x32_bf16 v[192:195], a[192:195], a[152:155], v[192:195]\n"
    "v_add_f32_dpp v12, v253, v12 row_newbcast:12 row_mask:0xf bank_mask:0xf\n"
    "v_mfma_f32_16x16x32_bf16 v[192:195], a[196:199], a[184:187], v[192:195]\n"
    "s_waitcnt lgkmcnt(0)\n"
    "v_add_f32_dpp v13, v253, v13 row_newbcast:13 row_mask:0xf bank_mask:0xf\n"
    "v_mfma_f32_16x16x32_bf16 v[224:227], a[196:199], a[188:191], v[224:227]\n"
    "v_add_f32_dpp v14, v253, v14 row_newbcast:14 row_mask:0xf bank_mask:0xf\n"
    "v_mfma_f32_16x16x32_bf16 v[224:227], a[192:195], a[156:159], v[224:227]\n"
    "s_waitcnt vmcnt(8)\n"
    "s_addk_i32 s20, 4096\n"
    "v_add_f32_dpp v15, v253, v15 row_newbcast:15 row_mask:0xf bank_mask:0xf\n"
    "v_mfma_f32_16x16x32_bf16 v[228:231], a[200:203], a[156:159], v[228:231]\n"
    "buffer_load_dwordx4 a[192:195], v240, s[20:23], 0 offen offset:0 sc1\n"
    "v_add_f32_dpp v44, v253, v44 row_newbcast:12 row_mask:0xf bank_mask:0xf\n"
    "v_mfma_f32_16x16x32_bf16 v[228:231], a[204:207], a[188:191], v[228:231]\n"
    "buffer_load_dwordx4 a[196:199], v240, s[20:23], 0 offen offset:1024 sc1\n"
    "v_add_f32_dpp v45, v253, v45 row_newbcast:13 row_mask:0xf bank_mask:0xf\n"
    "v_mfma_f32_16x16x32_bf16 v[196:199], a[204:207], a[184:187], v[196:199]\n"
    "v_add_f32_dpp v46, v253, v46 row_newbcast:14 row_mask:0xf bank_mask:0xf\n"
    "v_mfma_f32_16x16x32_bf16 v[196:199], a[200:203], a[152:155], v[196:199]\n"
    "v_add_f32_dpp v47, v253, v47 row_newbcast:15 row_mask:0xf bank_mask:0xf\n"
    "v_mfma_f32_16x16x32_bf16 v[164:167], a[200:203], a[148:151], v[164:167]\n"
    "v_add_f32_dpp v76, v253, v76 row_newbcast:12 row_mask:0xf bank_mask:0xf\n"
    "v_mfma_f32_16x16x32_bf16 v[164:167], a[204:207], a[180:183], v[164:167]\n"
    "v_add_f32_dpp v77, v253, v77 row_newbcast:13 row_mask:0xf bank_mask:0xf\n"
    "v_mfma_f32_16x16x32_bf16 v[132:135], a[204:207], a[176:179], v[132:135]\n"
    "v_add_f32_dpp v78, v253, v78 row_newbcast:14 row_mask:0xf bank_mask:0xf\n"
    "v_mfma_f32_16x16x32_bf16 v[132:135], a[200:203], a[144:147], v[132:135]\n"
    "v_add_f32_dpp v79, v253, v79 row_newbcast:15 row_mask:0xf bank_mask:0xf\n"
    "v_mfma_f32_16x16x32_bf16 v[100:103], a[200:203], a[140:143], v[100:103]\n"
    "v_add_f32_dpp v108, v253, v108 row_newbcast:12 row_mask:0xf bank_mask:0xf\n"
    "v_mfma_f32_16x16x32_bf16 v[100:103], a[204:207], a[172:175], v[100:103]\n"
    "v_add_f32_dpp v109, v253, v109 row_newbcast:13 row_mask:0xf bank_mask:0xf\n"
    "v_mfma_f32_16x16x32_bf16 v[68:71], a[204:207], a[168:171], v[68:71]\n"
    "v_add_f32_dpp v110, v253, v110 row_newbcast:14 row_mask:0xf bank_mask:0xf\n"
    "v_mfma_f32_16x16x32_bf16 v[68:71], a[200:203], a[136:139], v[68:71]\n"
    "v_add_f32_dpp v111, v253, v111 row_newbcast:15 row_mask:0xf bank_mask:0xf\n"
    "v_mfma_f32_16x16x32_bf16 v[36:39], a[200:203], a[132:135], v[36:39]\n"
    "v_add_f32_dpp v0, v253, v0 row_newbcast:0 row_mask:0xf bank_mask:0xf\n"
    "v_mfma_f32_16x16x32_bf16 v[36:39], a[204:207], a[164:167], v[36:39]\n"
    "v_add_f32_dpp v1, v253, v1 row_newbcast:1 row_mask:0xf bank_mask:0xf\n"
    "v_mfma_f32_16x16x32_bf16 v[4:7], a[204:207], a[160:163], v[4:7]\n"
    "v_add_f32_dpp v2, v253, v2 row_newbcast:2 row_mask:0xf bank_mask:0xf\n"
    "v_mfma_f32_16x16x32_bf16 v[4:7], a[200:203], a[128:131], v[4:7]\n"
    "s_waitcnt vmcnt(8)\n"
    "buffer_load_dwordx4 a[200:203], v240, s[20:23], 0 offen offset:2048 sc1\n"
    "v_mfma_f32_16x16x32_bf16 v[8:11], a[208:211], a[128:131], v[8:11]\n"
    "buffer_load_dwordx4 a[204:207], v240, s[20:23], 0 offen offset:3072 sc1\n"
    "v_add_f32_dpp v3, v253, v3 row_newbcast:3 row_mask:0xf bank_mask:0xf\n"
    "v_mfma_f32_16x16x32_bf16 v[8:11], a[212:215], a[160:163], v[8:11]\n"
    "v_add_f32_dpp v32, v253, v32 row_newbcast:0 row_mask:0xf bank_mask:0xf\n"
    "v_add_f32_dpp v33, v253, v33 row_newbcast:1 row_mask:0xf bank_mask:0xf\n"
    "v_mfma_f32_16x16x32_bf16 v[40:43], a[212:215], a[164:167], v[40:43]\n"
    "v_add_f32_dpp v34, v253, v34 row_newbcast:2 row_mask:0xf bank_mask:0xf\n"
    "v_mfma_f32_16x16x32_bf16 v[40:43], a[208:211], a[132:135], v[40:43]\n"
    "v_add_f32_dpp v35, v253, v35 row_newbcast:3 row_mask:0xf bank_mask:0xf\n"
    "v_mfma_f32_16x16x32_bf16 v[72:75], a[208:211], a[136:139], v[72:75]\n"
    "v_add_f32_dpp v64, v253, v64 row_newbcast:0 row_mask:0xf bank_mask:0xf\n"
    "v_mfma_f32_16x16x32_bf16 v[72:75], a[212:215], a[168:171], v[72:75]\n"
    "v_add_f32_dpp v65, v253, v65 row_newbcast:1 row_mask:0xf bank_mask:0xf\n"
    "v_mfma_f32_16x16x32_bf16 v[104:107], a[212:215], a[172:175], v[104:107]\n"
    "v_add_f32_dpp v66, v253, v66 row_newbcast:2 row_mask:0xf bank_mask:0xf\n"
    "v_mfma_f32_16x16x32_bf16 v[104:107], a[208:211], a[140:143], v[104:107]\n"
    "v_add_f32_dpp v67, v253, v67 row_newbcast:3 row_mask:0xf bank_mask:0xf\n"
    "v_mfma_f32_16x16x32_bf16 v[136:139], a[208:211], a[144:147], v[136:139]\n"
    "v_add_f32_dpp v96, v253, v96 row_newbcast:0 row_mask:0xf bank_mask:0xf\n"
    "v_mfma_f32_16x16x32_bf16 v[136:139], a[212:215], a[176:179], v[136:139]\n"
    "v_add_f32_dpp v97, v253, v97 row_newbcast:1 row_mask:0xf bank_mask:0xf\n"
    "v_mfma_f32_16x16x32_bf16 v[168:171], a[212:215], a[180:183], v[168:171]\n"
    "v_add_f32_dpp v98, v253, v98 row_newbcast:2 row_mask:0xf bank_mask:0xf\n"
    "v_mfma_f32_16x16x32_bf16 v[168:171], a[208:211], a[148:151], v[168:171]\n"
    "v_add_f32_dpp v99, v253, v99 row_newbcast:3 row_mask:0xf bank_mask:0xf\n"
    "v_mfma_f32_16x16x32_bf16 v[200:203], a[208:211], a[152:155], v[200:203]\n"
    "v_add_f32_dpp v4, v253, v4 row_newbcast:4 row_mask:0xf bank_mask:0xf\n"
    "v_mfma_f32_16x16x32_bf16 v[200:203], a[212:215], a[184:187], v[200:203]\n"
    "s_barrier\n"
    "v_add_f32_dpp v5, v253, v5 row_newbcast:5 row_mask:0xf bank_mask:0xf\n"
    "v_mfma_f32_16x16x32_bf16 v[232:235], a[212:215], a[188:191], v[232:235]\n"
    "v_add_f32_dpp v6, v253, v6 row_newbcast:6 row_mask:0xf bank_mask:0xf\n"
    "v_mfma_f32_16x16x32_bf16 v[232:235], a[208:211], a[156:159], v[232:235]\n"
    "s_waitcnt vmcnt(8)\n"
    "s_addk_i32 s20, 12288\n"
    "v_add_f32_dpp v7, v253, v7 row_newbcast:7 row_mask:0xf bank_mask:0xf\n"
    "v_mfma_f32_16x16x32_bf16 v[236:239], a[216:219], a[156:159], v[236:239]\n"
    "buffer_load_dwordx4 a[208:211], v240, s[20:23], 0 offen offset:0 sc1\n"
    "v_add_f32_dpp v36, v253, v36 row_newbcast:4 row_mask:0xf bank_mask:0xf\n"
    "v_mfma_f32_16x16x32_bf16 v[236:239], a[220:223], a[188:191], v[236:239]\n"
    "buffer_load_dwordx4 a[212:215], v240, s[20:23], 0 offen offset:1024 sc1\n"
    "v_add_f32_dpp v37, v253, v37 row_newbcast:5 row_mask:0xf bank_mask:0xf\n"
    "v_mfma_f32_16x16x32_bf16 v[204:207], a[220:223], a[184:187], v[204:207]\n"
    "v_add_u32 v241, s67, v240\n"
    "v_add_f32_dpp v38, v253, v38 row_newbcast:6 row_mask:0xf bank_mask:0xf\n"
    "v_mfma_f32_16x16x32_bf16 v[204:207], a[216:219], a[152:155], v[204:207]\n"
    "ds_read_b128 a[156:159], v241 offset:14336\n"
    "v_add_f32_dpp v39, v253, v39 row_newbcast:7 row_mask:0xf bank_mask:0xf\n"
    "v_mfma_f32_16x16x32_bf16 v[172:175], a[216:219], a[148:151], v[172:175]\n"
    "ds_read_b128 a[188:191], v241 offset:15360\n"
    "v_add_f32_dpp v68, v253, v68 row_newbcast:4 row_mask:0xf bank_mask:0xf\n"
    "v_mfma_f32_16x16x32_bf16 v[172:175], a[220:223], a[180:183], v[172:175]\n"
    "ds_read_b128 a[184:187], v241 offset:13312\n"
    "v_add_f32_dpp v69, v253, v69 row_newbcast:5 row_mask:0xf bank_mask:0xf\n"
    "v_mfma_f32_16x16x32_bf16 v[140:143], a[220:223], a[176:179], v[140:143]\n"
    "ds_read_b128 a[152:155], v241 offset:12288\n"
    "v_add_f32_dpp v70, v253, v70 row_newbcast:6 row_mask:0xf bank_mask:0xf\n"
    "v_mfma_f32_16x16x32_bf16 v[140:143], a[216:219], a[144:147], v[140:143]\n"
    "ds_read_b128 a[148:151], v241 offset:10240\n"
    "v_add_f32_dpp v71, v253, v71 row_newbcast:7 row_mask:0xf bank_mask:0xf\n"
    "v_mfma_f32_16x16x32_bf16 v[108:111], a[216:219], a[140:143], v[108:111]\n"
    "ds_read_b128 a[180:183], v241 offset:11264\n"
    "v_add_f32_dpp v100, v253, v100 row_newbcast:4 row_mask:0xf bank_mask:0xf\n"
    "v_mfma_f32_16x16x32_bf16 v[108:111], a[220:223], a[172:175], v[108:111]\n"
    "ds_read_b128 a[176:179], v241 offset:9216\n"
    "v_add_f32_dpp v101, v253, v101 row_newbcast:5 row_mask:0xf bank_mask:0xf\n"
    "v_mfma_f32_16x16x32_bf16 v[76:79], a[220:223], a[168:171], v[76:79]\n"
    "ds_read_b128 a[144:147], v241 offset:8192\n"
    "v_add_f32_dpp v102, v253, v102 row_newbcast:6 row_mask:0xf bank_mask:0xf\n"
    "v_mfma_f32_16x16x32_bf16 v[76:79], a[216:219], a[136:139], v[76:79]\n"
    "ds_read_b128 a[140:143], v241 offset:6144\n"
    "v_add_f32_dpp v103, v253, v103 row_newbcast:7 row_mask:0xf bank_mask:0xf\n"
    "v_mfma_f32_16x16x32_bf16 v[44:47], a[216:219], a[132:135], v[44:47]\n"
    "ds_read_b128 a[172:175], v241 offset:7168\n"
    "v_add_f32_dpp v136, v253, v136 row_newbcast:8 row_mask:0xf bank_mask:0xf\n"
    "v_mfma_f32_16x16x32_bf16 v[44:47], a[220:223], a[164:167], v[44:47]\n"
    "ds_read_b128 a[168:171], v241 offset:5120\n"
    "v_add_f32_dpp v137, v253, v137 row_newbcast:9 row_mask:0xf bank_mask:0xf\n"
    "v_mfma_f32_16x16x32_bf16 v[12:15], a[220:223], a[160:163], v[12:15]\n"
    "ds_read_b128 a[136:139], v241 offset:4096\n"
    "v_add_f32_dpp v138, v253, v138 row_newbcast:10 row_mask:0xf bank_mask:0xf\n"
    "v_mfma_f32_16x16x32_bf16 v[12:15], a[216:219], a[128:131], v[12:15]\n"
    "s_waitcnt vmcnt(8) lgkmcnt(10)\n"
    "ds_read_b128 a[132:135], v241 offset:2048\n"
    "v_add_f32_dpp v139, v253, v139 row_newbcast:11 row_mask:0xf bank_mask:0xf\n"
    "v_mfma_f32_16x16x32_bf16 v[224:227], a[224:227], a[156:159], v[224:227]\n"
    "ds_read_b128 a[164:167], v241 offset:3072\n"
    "v_add_f32_dpp v168, v253, v168 row_newbcast:8 row_mask:0xf bank_mask:0xf\n"
    "v_mfma_f32_16x16x32_bf16 v[224:227], a[228:231], a[188:191], v[224:227]\n"
    "s_waitcnt lgkmcnt(10)\n"
    "buffer_load_dwordx4 a[216:219], v240, s[20:23], 0 offen offset:2048 sc1\n"
    "v_mfma_f32_16x16x32_bf16 v[192:195], a[228:231], a[184:187], v[192:195]\n"
    "buffer_load_dwordx4 a[220:223], v240, s[20:23], 0 offen offset:3072 sc1\n"
    "ds_read_b128 a[160:163], v241 offset:1024\n"
    "v_mfma_f32_16x16x32_bf16 v[192:195], a[224:227], a[152:155], v[192:195]\n"
    "s_waitcnt lgkmcnt(9)\n"
    "ds_read_b128 a[128:131], v241 offset:0\n"
    "v_add_f32_dpp v169, v253, v169 row_newbcast:9 row_mask:0xf bank_mask:0xf\n"
    "v_mfma_f32_16x16x32_bf16 v[160:163], a[224:227], a[148:151], v[160:163]\n"
    "v_add_f32_dpp v170, v253, v170 row_newbcast:10 row_mask:0xf bank_mask:0xf\n"
    "v_add_f32_dpp v171, v253, v171 row_newbcast:11 row_mask:0xf bank_mask:0xf\n"
    "v_mfma_f32_16x16x32_bf16 v[160:163], a[228:231], a[180:183], v[160:163]\n"
    "s_waitcnt lgkmcnt(8)\n"
    "v_add_f32_dpp v200, v253, v200 row_newbcast:8 row_mask:0xf bank_mask:0xf\n"
    "v_add_f32_dpp v201, v253, v201 row_newbcast:9 row_mask:0xf bank_mask:0xf\n"
    "v_mfma_f32_16x16x32_bf16 v[128:131], a[228:231], a[176:179], v[128:131]\n"
    "v_add_f32_dpp v202, v253, v202 row_newbcast:10 row_mask:0xf bank_mask:0xf\n"
    "v_mfma_f32_16x16x32_bf16 v[128:131], a[224:227], a[144:147], v[128:131]\n"
    "s_waitcnt lgkmcnt(6)\n"
    "v_add_f32_dpp v203, v253, v203 row_newbcast:11 row_mask:0xf bank_mask:0xf\n"
    "v_mfma_f32_16x16x32_bf16 v[96:99], a[224:227], a[140:143], v[96:99]\n"
    "v_add_f32_dpp v232, v253, v232 row_newbcast:8 row_mask:0xf bank_mask:0xf\n"
    "v_mfma_f32_16x16x32_bf16 v[96:99], a[228:231], a[172:175], v[96:99]\n"
    "s_waitcnt lgkmcnt(4)\n"
    "v_add_f32_dpp v233, v253, v233 row_newbcast:9 row_mask:0xf bank_mask:0xf\n"
    "v_mfma_f32_16x16x32_bf16 v[64:67], a[228:231], a[168:171], v[64:67]\n"
    "v_add_f32_dpp v234, v253, v234 row_newbcast:10 row_mask:0xf bank_mask:0xf\n"
    "v_mfma_f32_16x16x32_bf16 v[64:67], a[224:227], a[136:139], v[64:67]\n"
    "s_waitcnt lgkmcnt(2)\n"
    "v_add_f32_dpp v235, v253, v235 row_newbcast:11 row_mask:0xf bank_mask:0xf\n"
    "v_mfma_f32_16x16x32_bf16 v[32:35], a[224:227], a[132:135], v[32:35]\n"
    "v_add_f32_dpp v140, v253, v140 row_newbcast:12 row_mask:0xf bank_mask:0xf\n"
    "v_mfma_f32_16x16x32_bf16 v[32:35], a[228:231], a[164:167], v[32:35]\n"
    "s_waitcnt lgkmcnt(0)\n"
    "v_add_f32_dpp v141, v253, v141 row_newbcast:13 row_mask:0xf bank_mask:0xf\n"
    "v_mfma_f32_16x16x32_bf16 v[0:3], a[228:231], a[160:163], v[0:3]\n"
    "v_add_f32_dpp v142, v253, v142 row_newbcast:14 row_mask:0xf bank_mask:0xf\n"
    "v_mfma_f32_16x16x32_bf16 v[0:3], a[224:227], a[128:131], v[0:3]\n"
    "s_waitcnt vmcnt(8)\n"
    "s_addk_i32 s20, 4096\n"
    "v_add_f32_dpp v143, v253, v143 row_newbcast:15 row_mask:0xf bank_mask:0xf\n"
    "v_mfma_f32_16x16x32_bf16 v[4:7], a[232:235], a[128:131], v[4:7]\n"
    "buffer_load_dwordx4 a[224:227], v240, s[20:23], 0 offen offset:0 sc1\n"
    "v_add_f32_dpp v172, v253, v172 row_newbcast:12 row_mask:0xf bank_mask:0xf\n"
    "v_mfma_f32_16x16x32_bf16 v[4:7], a[236:239], a[160:163], v[4:7]\n"
    "buffer_load_dwordx4 a[228:231], v240, s[20:23], 0 offen offset:1024 sc1\n"
    "v_add_f32_dpp v173, v253, v173 row_newbcast:13 row_mask:0xf bank_mask:0xf\n"
    "v_mfma_f32_16x16x32_bf16 v[36:39], a[236:239], a[164:167], v[36:39]\n"
    "v_add_f32_dpp v174, v253, v174 row_newbcast:14 row_mask:0xf bank_mask:0xf\n"
    "v_mfma_f32_16x16x32_bf16 v[36:39], a[232:235], a[132:135], v[36:39]\n"
    "v_add_f32_dpp v175, v253, v175 row_newbcast:15 row_mask:0xf bank_mask:0xf\n"
    "v_mfma_f32_16x16x32_bf16 v[68:71], a[232:235], a[136:139], v[68:71]\n"
    "v_add_f32_dpp v204, v253, v204 row_newbcast:12 row_mask:0xf bank_mask:0xf\n"
    "v_mfma_f32_16x16x32_bf16 v[68:71], a[236:239], a[168:171], v[68:71]\n"
    "v_add_f32_dpp v205, v253, v205 row_newbcast:13 row_mask:0xf bank_mask:0xf\n"
    "v_mfma_f32_16x16x32_bf16 v[100:103], a[236:239], a[172:175], v[100:103]\n"
    "v_add_f32_dpp v206, v253, v206 row_newbcast:14 row_mask:0xf bank_mask:0xf\n"
    "v_mfma_f32_16x16x32_bf16 v[100:103], a[232:235], a[140:143], v[100:103]\n"
    "v_add_f32_dpp v207, v253, v207 row_newbcast:15 row_mask:0xf bank_mask:0xf\n"
    "v_mfma_f32_16x16x32_bf16 v[132:135], a[232:235], a[144:147], v[132:135]\n"
    "v_add_f32_dpp v236, v253, v236 row_newbcast:12 row_mask:0xf bank_mask:0xf\n"
    "v_mfma_f32_16x16x32_bf16 v[132:135], a[236:239], a[176:179], v[132:135]\n"
    "v_add_f32_dpp v237, v253, v237 row_newbcast:13 row_mask:0xf bank_mask:0xf\n"
    "v_mfma_f32_16x16x32_bf16 v[164:167], a[236:239], a[180:183], v[164:167]\n"
    "v_add_f32_dpp v238, v253, v238 row_newbcast:14 row_mask:0xf bank_mask:0xf\n"
    "v_mfma_f32_16x16x32_bf16 v[164:167], a[232:235], a[148:151], v[164:167]\n"
    "v_add_f32_dpp v239, v253, v239 row_newbcast:15 row_mask:0xf bank_mask:0xf\n"
    "v_mfma_f32_16x16x32_bf16 v[196:199], a[232:235], a[152:155], v[196:199]\n"
    "v_add_f32_dpp v128, v253, v128 row_newbcast:0 row_mask:0xf bank_mask:0xf\n"
    "v_mfma_f32_16x16x32_bf16 v[196:199], a[236:239], a[184:187], v[196:199]\n"
    "v_add_f32_dpp v129, v253, v129 row_newbcast:1 row_mask:0xf bank_mask:0xf\n"
    "v_mfma_f32_16x16x32_bf16 v[228:231], a[236:239], a[188:191], v[228:231]\n"
    "v_add_f32_dpp v130, v253, v130 row_newbcast:2 row_mask:0xf bank_mask:0xf\n"
    "v_mfma_f32_16x16x32_bf16 v[228:231], a[232:235], a[156:159], v[228:231]\n"
    "s_waitcnt vmcnt(8)\n"
    "buffer_load_dwordx4 a[232:235], v240, s[20:23], 0 offen offset:2048 sc1\n"
    "v_mfma_f32_16x16x32_bf16 v[232:235], a[192:195], a[156:159], v[232:235]\n"
    "buffer_load_dwordx4 a[236:239], v240, s[20:23], 0 offen offset:3072 sc1\n"
    "v_add_f32_dpp v131, v253, v131 row_newbcast:3 row_mask:0xf bank_mask:0xf\n"
    "v_mfma_f32_16x16x32_bf16 v[232:235], a[196:199], a[188:191], v[232:235]\n"
    "v_add_f32_dpp v160, v253, v160 row_newbcast:0 row_mask:0xf bank_mask:0xf\n"
    "v_add_f32_dpp v161, v253, v161 row_newbcast:1 row_mask:0xf bank_mask:0xf\n"
    "v_mfma_f32_16x16x32_bf16 v[200:203], a[196:199], a[184:187], v[200:203]\n"
    "v_add_f32_dpp v162, v253, v162 row_newbcast:2 row_mask:0xf bank_mask:0xf\n"
    "v_mfma_f32_16x16x32_bf16 v[200:203], a[192:195], a[152:155], v[200:203]\n"
    "v_add_f32_dpp v163, v253, v163 row_newbcast:3 row_mask:0xf bank_mask:0xf\n"
    "v_mfma_f32_16x16x32_bf16 v[168:171], a[192:195], a[148:151], v[168:171]\n"
    "v_add_f32_dpp v192, v253, v192 row_newbcast:0 row_mask:0xf bank_mask:0xf\n"
    "v_mfma_f32_16x16x32_bf16 v[168:171], a[196:199], a[180:183], v[168:171]\n"
    "v_add_f32_dpp v193, v253, v193 row_newbcast:1 row_mask:0xf bank_mask:0xf\n"
    "v_mfma_f32_16x16x32_bf16 v[136:139], a[196:199], a[176:179], v[136:139]\n"
    "v_add_f32_dpp v194, v253, v194 row_newbcast:2 row_mask:0xf bank_mask:0xf\n"
    "v_mfma_f32_16x16x32_bf16 v[136:139], a[192:195], a[144:147], v[136:139]\n"
    "v_add_f32_dpp v195, v253, v195 row_newbcast:3 row_mask:0xf bank_mask:0xf\n"
    "v_mfma_f32_16x16x32_bf16 v[104:107], a[192:195], a[140:143], v[104:107]\n"
    "v_add_f32_dpp v224, v253, v224 row_newbcast:0 row_mask:0xf bank_mask:0xf\n"
    "v_mfma_f32_16x16x32_bf16 v[104:107], a[196:199], a[172:175], v[104:107]\n"
    "v_add_f32_dpp v225, v253, v225 row_newbcast:1 row_mask:0xf bank_mask:0xf\n"
    "v_mfma_f32_16x16x32_bf16 v[72:75], a[196:199], a[168:171], v[72:75]\n"
    "v_add_f32_dpp v226, v253, v226 row_newbcast:2 row_mask:0xf bank_mask:0xf\n"
    "v_mfma_f32_16x16x32_bf16 v[72:75], a[192:195], a[136:139], v[72:75]\n"
    "v_add_f32_dpp v227, v253, v227 row_newbcast:3 row_mask:0xf bank_mask:0xf\n"
    "v_mfma_f32_16x16x32_bf16 v[40:43], a[192:195], a[132:135], v[40:43]\n"
    "v_add_f32_dpp v132, v253, v132 row_newbcast:4 row_mask:0xf bank_mask:0xf\n"
    "v_mfma_f32_16x16x32_bf16 v[40:43], a[196:199], a[164:167], v[40:43]\n"
    "v_add_f32_dpp v133, v253, v133 row_newbcast:5 row_mask:0xf bank_mask:0xf\n"
    "v_mfma_f32_16x16x32_bf16 v[8:11], a[196:199], a[160:163], v[8:11]\n"
    "v_add_f32_dpp v134, v253, v134 row_newbcast:6 row_mask:0xf bank_mask:0xf\n"
    "v_mfma_f32_16x16x32_bf16 v[8:11], a[192:195], a[128:131], v[8:11]\n"
    "s_waitcnt vmcnt(8)\n"
    "s_addk_i32 s20, 4096\n"
    "v_add_f32_dpp v135, v253, v135 row_newbcast:7 row_mask:0xf bank_mask:0xf\n"
    "v_mfma_f32_16x16x32_bf16 v[12:15], a[200:203], a[128:131], v[12:15]\n"
    "buffer_load_dwordx4 a[192:195], v240, s[20:23], 0 offen offset:0 sc1\n"
    "v_add_f32_dpp v164, v253, v164 row_newbcast:4 row_mask:0xf bank_mask:0xf\n"
    "v_mfma_f32_16x16x32_bf16 v[12:15], a[204:207], a[160:163], v[12:15]\n"
    "buffer_load_dwordx4 a[196:199], v240, s[20:23], 0 offen offset:1024 sc1\n"
    "v_add_f32_dpp v165, v253, v165 row_newbcast:5 row_mask:0xf bank_mask:0xf\n"
    "v_mfma_f32_16x16x32_bf16 v[44:47], a[204:207], a[164:167], v[44:47]\n"
    "ds_read_b128 a[128:131], v241 offset:16384\n"
    "v_add_f32_dpp v166, v253, v166 row_newbcast:6 row_mask:0xf bank_mask:0xf\n"
    "v_mfma_f32_16x16x32_bf16 v[44:47], a[200:203], a[132:135], v[44:47]\n"
    "ds_read_b128 a[160:163], v241 offset:17408\n"
    "v_add_f32_dpp v167, v253, v167 row_newbcast:7 row_mask:0xf bank_mask:0xf\n"
    "v_mfma_f32_16x16x32_bf16 v[76:79], a[200:203], a[136:139], v[76:79]\n"
    "ds_read_b128 a[164:167], v241 offset:19456\n"
    "v_add_f32_dpp v196, v253, v196 row_newbcast:4 row_mask:0xf bank_mask:0xf\n"
    "v_mfma_f32_16x16x32_bf16 v[76:79], a[204:207], a[168:171], v[76:79]\n"
    "ds_read_b128 a[132:135], v241 offset:18432\n"
    "v_add_f32_dpp v197, v253, v197 row_newbcast:5 row_mask:0xf bank_mask:0xf\n"
    "v_mfma_f32_16x16x32_bf16 v[108:111], a[204:207], a[172:175], v[108:111]\n"
    "ds_read_b128 a[136:139], v241 offset:20480\n"
    "v_add_f32_dpp v198, v253, v198 row_newbcast:6 row_mask:0xf bank_mask:0xf\n"
    "v_mfma_f32_16x16x32_bf16 v[108:111], a[200:203], a[140:143], v[108:111]\n"
    "ds_read_b128 a[168:171], v241 offset:21504\n"
    "v_add_f32_dpp v199, v253, v199 row_newbcast:7 row_mask:0xf bank_mask:0xf\n"
    "v_mfma_f32_16x16x32_bf16 v[140:143], a[200:203], a[144:147], v[140:143]\n"
    "ds_read_b128 a[172:175], v241 offset:23552\n"
    "v_add_f32_dpp v228, v253, v228 row_newbcast:4 row_mask:0xf bank_mask:0xf\n"
    "v_mfma_f32_16x16x32_bf16 v[140:143], a[204:207], a[176:179], v[140:143]\n"
    "ds_read_b128 a[140:143], v241 offset:22528\n"
    "v_add_f32_dpp v229, v253, v229 row_newbcast:5 row_mask:0xf bank_mask:0xf\n"
    "v_mfma_f32_16x16x32_bf16 v[172:175], a[204:207], a[180:183], v[172:175]\n"
    "ds_read_b128 a[144:147], v241 offset:24576\n"
    "v_add_f32_dpp v230, v253, v230 row_newbcast:6 row_mask:0xf bank_mask:0xf\n"
    "v_mfma_f32_16x16x32_bf16 v[172:175], a[200:203], a[148:151], v[172:175]\n"
    "ds_read_b128 a[176:179], v241 offset:25600\n"
    "v_add_f32_dpp v231, v253, v231 row_newbcast:7 row_mask:0xf bank_mask:0xf\n"
    "v_mfma_f32_16x16x32_bf16 v[204:207], a[200:203], a[152:155], v[204:207]\n"
    "ds_read_b128 a[180:183], v241 offset:27648\n"
    "v_mfma_f32_16x16x32_bf16 v[204:207], a[204:207], a[184:187], v[204:207]\n"
    "ds_read_b128 a[148:151], v241 offset:26624\n"
    "v_mfma_f32_16x16x32_bf16 v[236:239], a[204:207], a[188:191], v[236:239]\n"
    "ds_read_b128 a[152:155], v241 offset:28672\n"
    "v_mfma_f32_16x16x32_bf16 v[236:239], a[200:203], a[156:159], v[236:239]\n"
    "s_waitcnt vmcnt(8) lgkmcnt(11)\n"
    "ds_read_b128 a[184:187], v241 offset:29696\n"
    "v_mfma_f32_16x16x32_bf16 v[0:3], a[208:211], a[128:131], v[0:3]\n"
    "buffer_load_dwordx4 a[200:203], v240, s[20:23], 0 offen offset:2048 sc1\n"
    "v_mfma_f32_16x16x32_bf16 v[0:3], a[212:215], a[160:163], v[0:3]\n"
    "s_waitcnt lgkmcnt(10)\n"
    "buffer_load_dwordx4 a[204:207], v240, s[20:23], 0 offen offset:3072 sc1\n"
    "v_mfma_f32_16x16x32_bf16 v[32:35], a[212:215], a[164:167], v[32:35]\n"
    "ds_read_b128 a[188:191], v241 offset:31744\n"
    "v_mfma_f32_16x16x32_bf16 v[32:35], a[208:211], a[132:135], v[32:35]\n"
    "s_waitcnt lgkmcnt(9)\n"
    "ds_read_b128 a[156:159], v241 offset:30720\n"
    "v_mfma_f32_16x16x32_bf16 v[64:67], a[208:211], a[136:139], v[64:67]\n"
    "v_mfma_f32_16x16x32_bf16 v[64:67], a[212:215], a[168:171], v[64:67]\n"
    "s_waitcnt lgkmcnt(8)\n"
    "v_mfma_f32_16x16x32_bf16 v[96:99], a[212:215], a[172:175], v[96:99]\n"
    "v_mfma_f32_16x16x32_bf16 v[96:99], a[208:211], a[140:143], v[96:99]\n"
    "s_waitcnt lgkmcnt(6)\n"
    "v_mfma_f32_16x16x32_bf16 v[128:131], a[208:211], a[144:147], v[128:131]\n"
    "v_mfma_f32_16x16x32_bf16 v[128:131], a[212:215], a[176:179], v[128:131]\n"
    "s_waitcnt lgkmcnt(4)\n"
    "v_mfma_f32_16x16x32_bf16 v[160:163], a[212:215], a[180:183], v[160:163]\n"
    "v_mfma_f32_16x16x32_bf16 v[160:163], a[208:211], a[148:151], v[160:163]\n"
    "s_waitcnt lgkmcnt(2)\n"
    "v_mfma_f32_16x16x32_bf16 v[192:195], a[208:211], a[152:155], v[192:195]\n"
    "v_mfma_f32_16x16x32_bf16 v[192:195], a[212:215], a[184:187], v[192:195]\n"
    "s_waitcnt lgkmcnt(0)\n"
    "v_mfma_f32_16x16x32_bf16 v[224:227], a[212:215], a[188:191], v[224:227]\n"
    "v_mfma_f32_16x16x32_bf16 v[224:227], a[208:211], a[156:159], v[224:227]\n"
    "s_waitcnt vmcnt(8)\n"
    "s_addk_i32 s20, 4096\n"
    "v_mfma_f32_16x16x32_bf16 v[228:231], a[216:219], a[156:159], v[228:231]\n"
    "buffer_load_dwordx4 a[208:211], v240, s[20:23], 0 offen offset:0 sc1\n"
    "v_mfma_f32_16x16x32_bf16 v[228:231], a[220:223], a[188:191], v[228:231]\n"
    "buffer_load_dwordx4 a[212:215], v240, s[20:23], 0 offen offset:1024 sc1\n"
    "v_accvgpr_read_b32 v245, a28\n"
    "v_mfma_f32_16x16x32_bf16 v[196:199], a[220:223], a[184:187], v[196:199]\n"
    "v_accvgpr_read_b32 v247, a29\n"
    "v_lshlrev_b32 v244, 16, v245\n"
    "v_mfma_f32_16x16x32_bf16 v[196:199], a[216:219], a[152:155], v[196:199]\n"
    "v_lshlrev_b32 v246, 16, v247\n"
    "v_and_b32 v245, s58, v245\n"
    "v_mfma_f32_16x16x32_bf16 v[164:167], a[216:219], a[148:151], v[164:167]\n"
    "v_and_b32 v247, s58, v247\n"
    "v_mul_f32 v244, v244, v224\n"
    "v_mfma_f32_16x16x32_bf16 v[164:167], a[220:223], a[180:183], v[164:167]\n"
    "v_mul_f32 v246, v246, v226\n"
    "v_mul_f32 v245, v245, v225\n"
    "v_mfma_f32_16x16x32_bf16 v[132:135], a[220:223], a[176:179], v[132:135]\n"
    "v_mul_f32 v247, v247, v227\n"
    "v_cvt_pk_bf16_f32 v224, v244, v245\n"
    "v_mfma_f32_16x16x32_bf16 v[132:135], a[216:219], a[144:147], v[132:135]\n"
    "v_cvt_pk_bf16_f32 v225, v246, v247\n"
    "v_accvgpr_read_b32 v245, a30\n"
    "v_mfma_f32_16x16x32_bf16 v[100:103], a[216:219], a[140:143], v[100:103]\n"
    "v_accvgpr_read_b32 v247, a31\n"
    "v_lshlrev_b32 v244, 16, v245\n"
    "v_mfma_f32_16x16x32_bf16 v[100:103], a[220:223], a[172:175], v[100:103]\n"
    "v_lshlrev_b32 v246, 16, v247\n"
    "v_and_b32 v245, s58, v245\n"
    "v_mfma_f32_16x16x32_bf16 v[68:71], a[220:223], a[168:171], v[68:71]\n"
    "v_and_b32 v247, s58, v247\n"
    "v_mul_f32 v244, v244, v228\n"
    "v_mfma_f32_16x16x32_bf16 v[68:71], a[216:219], a[136:139], v[68:71]\n"
    "v_mul_f32 v246, v246, v230\n"
    "v_mul_f32 v245, v245, v229\n"
    "v_mfma_f32_16x16x32_bf16 v[36:39], a[216:219], a[132:135], v[36:39]\n"
    "v_mul_f32 v247, v247, v231\n"
    "v_cvt_pk_bf16_f32 v226, v244, v245\n"
    "v_mfma_f32_16x16x32_bf16 v[36:39], a[220:223], a[164:167], v[36:39]\n"
    "v_cvt_pk_bf16_f32 v227, v246, v247\n"
    "v_mfma_f32_16x16x32_bf16 v[4:7], a[220:223], a[160:163], v[4:7]\n"
    "ds_write_b128 v248, v[224:227] offset:2048\n"
    "v_mfma_f32_16x16x32_bf16 v[4:7], a[216:219], a[128:131], v[4:7]\n"
    "s_waitcnt vmcnt(8)\n"
    "buffer_load_dwordx4 a[216:219], v240, s[20:23], 0 offen offset:2048 sc1\n"
    "v_mfma_f32_16x16x32_bf16 v[8:11], a[224:227], a[128:131], v[8:11]\n"
    "buffer_load_dwordx4 a[220:223], v240, s[20:23], 0 offen offset:3072 sc1\n"
    "v_accvgpr_read_b32 v245, a24\n"
    "v_mfma_f32_16x16x32_bf16 v[8:11], a[228:231], a[160:163], v[8:11]\n"
    "v_accvgpr_read_b32 v247, a25\n"
    "v_lshlrev_b32 v244, 16, v245\n"
    "v_mfma_f32_16x16x32_bf16 v[40:43], a[228:231], a[164:167], v[40:43]\n"
    "v_lshlrev_b32 v246, 16, v247\n"
    "v_and_b32 v245, s58, v245\n"
    "v_mfma_f32_16x16x32_bf16 v[40:43], a[224:227], a[132:135], v[40:43]\n"
    "v_and_b32 v247, s58, v247\n"
    "v_mul_f32 v244, v244, v192\n"
    "v_mfma_f32_16x16x32_bf16 v[72:75], a[224:227], a[136:139], v[72:75]\n"
    "v_mul_f32 v246, v246, v194\n"
    "v_mul_f32 v245, v245, v193\n"
    "v_mfma_f32_16x16x32_bf16 v[72:75], a[228:231], a[168:171], v[72:75]\n"
    "v_mul_f32 v247, v247, v195\n"
    "v_cvt_pk_bf16_f32 v192, v244, v245\n"
    "v_mfma_f32_16x16x32_bf16 v[104:107], a[228:231], a[172:175], v[104:107]\n"
    "v_cvt_pk_bf16_f32 v193, v246, v247\n"
    "v_accvgpr_read_b32 v245, a26\n"
    "v_mfma_f32_16x16x32_bf16 v[104:107], a[224:227], a[140:143], v[104:107]\n"
    "v_accvgpr_read_b32 v247, a27\n"
    "v_lshlrev_b32 v244, 16, v245\n"
    "v_mfma_f32_16x16x32_bf16 v[136:139], a[224:227], a[144:147], v[136:139]\n"
    "buffer_load_dwordx4 a[28:31], v240, s[96:99], 0 offen offset:0 sc1\n"
    "v_lshlrev_b32 v246, 16, v247\n"
    "v_mfma_f32_16x16x32_bf16 v[136:139], a[228:231], a[176:179], v[136:139]\n"
    "v_and_b32 v245, s58, v245\n"
    "v_and_b32 v247, s58, v247\n"
    "v_mfma_f32_16x16x32_bf16 v[168:171], a[228:231], a[180:183], v[168:171]\n"
    "v_mul_f32 v244, v244, v196\n"
    "v_mul_f32 v246, v246, v198\n"
    "v_mfma_f32_16x16x32_bf16 v[168:171], a[224:227], a[148:151], v[168:171]\n"
    "v_mul_f32 v245, v245, v197\n"
    "v_mul_f32 v247, v247, v199\n"
    "v_mfma_f32_16x16x32_bf16 v[200:203], a[224:227], a[152:155], v[200:203]\n"
    "v_cvt_pk_bf16_f32 v194, v244, v245\n"
    "v_cvt_pk_bf16_f32 v195, v246, v247\n"
    "v_mfma_f32_16x16x32_bf16 v[200:203], a[228:231], a[184:187], v[200:203]\n"
    "ds_write_b128 v248, v[192:195] offset:0\n"
    "v_mfma_f32_16x16x32_bf16 v[232:235], a[228:231], a[188:191], v[232:235]\n"
    "v_mfma_f32_16x16x32_bf16 v[232:235], a[224:227], a[156:159], v[232:235]\n"
    "s_waitcnt vmcnt(9)\n"
    "s_addk_i32 s20, -20480\n"
    "v_mfma_f32_16x16x32_bf16 v[236:239], a[232:235], a[156:159], v[236:239]\n"
    "buffer_load_dwordx4 a[224:227], v240, s[20:23], 0 offen offset:0 sc1\n"
    "v_mfma_f32_16x16x32_bf16 v[236:239], a[236:239], a[188:191], v[236:239]\n"
    "buffer_load_dwordx4 a[228:231], v240, s[20:23], 0 offen offset:1024 sc1\n"
    "v_accvgpr_read_b32 v245, a60\n"
    "v_mfma_f32_16x16x32_bf16 v[204:207], a[236:239], a[184:187], v[204:207]\n"
    "v_accvgpr_read_b32 v247, a61\n"
    "v_lshlrev_b32 v244, 16, v245\n"
    "v_mfma_f32_16x16x32_bf16 v[204:207], a[232:235], a[152:155], v[204:207]\n"
    "v_lshlrev_b32 v246, 16, v247\n"
    "v_and_b32 v245, s58, v245\n"
    "v_mfma_f32_16x16x32_bf16 v[172:175], a[232:235], a[148:151], v[172:175]\n"
    "v_and_b32 v247, s58, v247\n"
    "v_mul_f32 v244, v244, v232\n"
    "v_mfma_f32_16x16x32_bf16 v[172:175], a[236:239], a[180:183], v[172:175]\n"
    "v_mul_f32 v246, v246, v234\n"
    "v_mul_f32 v245, v245, v233\n"
    "v_mfma_f32_16x16x32_bf16 v[140:143], a[236:239], a[176:179], v[140:143]\n"
    "v_mul_f32 v247, v247, v235\n"
    "v_cvt_pk_bf16_f32 v232, v244, v245\n"
    "v_mfma_f32_16x16x32_bf16 v[140:143], a[232:235], a[144:147], v[140:143]\n"
    "v_cvt_pk_bf16_f32 v233, v246, v247\n"
    "v_accvgpr_read_b32 v245, a62\n"
    "v_mfma_f32_16x16x32_bf16 v[108:111], a[232:235], a[140:143], v[108:111]\n"
    "buffer_load_dwordx4 v[224:227], v240, s[96:99], 0 offen offset:1024 sc1\n"
    "v_accvgpr_read_b32 v247, a63\n"
    "v_mfma_f32_16x16x32_bf16 v[108:111], a[236:239], a[172:175], v[108:111]\n"
    "v_lshlrev_b32 v244, 16, v245\n"
    "v_lshlrev_b32 v246, 16, v247\n"
    "v_mfma_f32_16x16x32_bf16 v[76:79], a[236:239], a[168:171], v[76:79]\n"
    "v_and_b32 v245, s58, v245\n"
    "v_and_b32 v247, s58, v247\n"
    "v_mfma_f32_16x16x32_bf16 v[76:79], a[232:235], a[136:139], v[76:79]\n"
    "v_mul_f32 v244, v244, v236\n"
    "v_mul_f32 v246, v246, v238\n"
    "v_mfma_f32_16x16x32_bf16 v[44:47], a[232:235], a[132:135], v[44:47]\n"
    "v_mul_f32 v245, v245, v237\n"
    "v_mul_f32 v247, v247, v239\n"
    "v_mfma_f32_16x16x32_bf16 v[44:47], a[236:239], a[164:167], v[44:47]\n"
    "v_cvt_pk_bf16_f32 v234, v244, v245\n"
    "v_cvt_pk_bf16_f32 v235, v246, v247\n"
    "v_mfma_f32_16x16x32_bf16 v[12:15], a[236:239], a[160:163], v[12:15]\n"
    "ds_write_b128 v249, v[232:235] offset:2048\n"
    "v_mfma_f32_16x16x32_bf16 v[12:15], a[232:235], a[128:131], v[12:15]\n"
    "s_waitcnt vmcnt(10)\n"
    "ds_read_b128 v[228:231], v242 offset:2048\n"
    "v_accvgpr_read_b32 v245, a56\n"
    "v_mfma_f32_16x16x32_bf16 v[16:19], a[192:195], a[128:131], v[16:19]\n"
    "ds_read_b128 v[236:239], v242 offset:3072\n"
    "v_accvgpr_read_b32 v247, a57\n"
    "v_mfma_f32_16x16x32_bf16 v[16:19], a[196:199], a[160:163], v[16:19]\n"
    "buffer_load_dwordx4 a[232:235], v240, s[20:23], 0 offen offset:2048 sc1\n"
    "v_lshlrev_b32 v244, 16, v245\n"
    "v_mfma_f32_16x16x32_bf16 v[48:51], a[196:199], a[164:167], v[48:51]\n"
    "buffer_load_dwordx4 a[236:239], v240, s[20:23], 0 offen offset:3072 sc1\n"
    "v_lshlrev_b32 v246, 16, v247\n"
    "v_mfma_f32_16x16x32_bf16 v[48:51], a[192:195], a[132:135], v[48:51]\n"
    "v_and_b32 v245, s58, v245\n"
    "v_and_b32 v247, s58, v247\n"
    "v_mfma_f32_16x16x32_bf16 v[80:83], a[192:195], a[136:139], v[80:83]\n"
    "v_mul_f32 v244, v244, v200\n"
    "v_mul_f32 v246, v246, v202\n"
    "v_mfma_f32_16x16x32_bf16 v[80:83], a[196:199], a[168:171], v[80:83]\n"
    "s_add_u32 s63, s70, 229376\n"
    "s_waitcnt lgkmcnt(1)\n"
    "v_mul_f32 v245, v245, v201\n"
    "v_mfma_f32_16x16x32_bf16 v[112:115], a[196:199], a[172:175], v[112:115]\n"
    "buffer_store_dwordx4 v[228:231], v243, s[40:43], s63 offen nt\n"
    "v_mul_f32 v247, v247, v203\n"
    "v_cvt_pk_bf16_f32 v200, v244, v245\n"
    "v_cvt_pk_bf16_f32 v201, v246, v247\n"
    "v_mfma_f32_16x16x32_bf16 v[112:115], a[192:195], a[140:143], v[112:115]\n"
    "v_accvgpr_read_b32 v245, a58\n"
    "v_accvgpr_read_b32 v247, a59\n"
    "v_mfma_f32_16x16x32_bf16 v[144:147], a[192:195], a[144:147], v[144:147]\n"
    "buffer_load_dwordx4 a[24:27], v240, s[96:99], 0 offen offset:2048 sc1\n"
    "v_lshlrev_b32 v244, 16, v245\n"
    "v_mfma_f32_16x16x32_bf16 v[144:147], a[196:199], a[176:179], v[144:147]\n"
    "v_lshlrev_b32 v246, 16, v247\n"
    "v_and_b32 v245, s58, v245\n"
    "v_mfma_f32_16x16x32_bf16 v[176:179], a[196:199], a[180:183], v[176:179]\n"
    "v_and_b32 v247, s58, v247\n"
    "v_mul_f32 v244, v244, v204\n"
    "v_mfma_f32_16x16x32_bf16 v[176:179], a[192:195], a[148:151], v[176:179]\n"
    "v_mul_f32 v246, v246, v206\n"
    "v_mul_f32 v245, v245, v205\n"
    "v_mfma_f32_16x16x32_bf16 v[208:211], a[192:195], a[152:155], v[208:211]\n"
    "v_mul_f32 v247, v247, v207\n"
    "v_cvt_pk_bf16_f32 v202, v244, v245\n"
    "v_mfma_f32_16x16x32_bf16 v[208:211], a[196:199], a[184:187], v[208:211]\n"
    "v_cvt_pk_bf16_f32 v203, v246, v247\n"
    "v_mfma_f32_16x16x32_bf16 a[240:243], a[196:199], a[188:191], a[240:243]\n"
    "ds_write_b128 v249, v[200:203] offset:0\n"
    "v_mfma_f32_16x16x32_bf16 a[240:243], a[192:195], a[156:159], a[240:243]\n"
    "s_waitcnt vmcnt(12)\n"
    "ds_read_b128 v[196:199], v242 offset:0\n"
    "v_accvgpr_read_b32 v245, a20\n"
    "v_mfma_f32_16x16x32_bf16 a[244:247], a[200:203], a[156:159], a[244:247]\n"
    "ds_read_b128 v[204:207], v242 offset:1024\n"
    "s_addk_i32 s20, 4096\n"
    "v_mfma_f32_16x16x32_bf16 a[244:247], a[204:207], a[188:191], a[244:247]\n"
    "buffer_load_dwordx4 a[192:195], v240, s[20:23], 0 offen offset:0 sc1\n"
    "v_accvgpr_read_b32 v247, a21\n"
    "v_mfma_f32_16x16x32_bf16 v[212:215], a[204:207], a[184:187], v[212:215]\n"
    "buffer_load_dwordx4 a[196:199], v240, s[20:23], 0 offen offset:1024 sc1\n"
    "v_lshlrev_b32 v244, 16, v245\n"
    "v_mfma_f32_16x16x32_bf16 v[212:215], a[200:203], a[152:155], v[212:215]\n"
    "s_add_u32 s63, s70, 245760\n"
    "s_waitcnt lgkmcnt(3)\n"
    "v_lshlrev_b32 v246, 16, v247\n"
    "v_mfma_f32_16x16x32_bf16 v[180:183], a[200:203], a[148:151], v[180:183]\n"
    "buffer_store_dwordx4 v[236:239], v243, s[40:43], s63 offen nt\n"
    "v_and_b32 v245, s58, v245\n"
    "v_and_b32 v247, s58, v247\n"
    "v_mul_f32 v244, v244, v160\n"
    "v_mfma_f32_16x16x32_bf16 v[180:183], a[204:207], a[180:183], v[180:183]\n"
    "v_mul_f32 v246, v246, v162\n"
    "v_mul_f32 v245, v245, v161\n"
    "v_mfma_f32_16x16x32_bf16 v[148:151], a[204:207], a[176:179], v[148:151]\n"
    "v_mul_f32 v247, v247, v163\n"
    "v_cvt_pk_bf16_f32 v160, v244, v245\n"
    "v_mfma_f32_16x16x32_bf16 v[148:151], a[200:203], a[144:147], v[148:151]\n"
    "v_cvt_pk_bf16_f32 v161, v246, v247\n"
    "v_accvgpr_read_b32 v245, a22\n"
    "v_mfma_f32_16x16x32_bf16 v[116:119], a[200:203], a[140:143], v[116:119]\n"
    "buffer_load_dwordx4 v[192:195], v240, s[96:99], 0 offen offset:3072 sc1\n"
    "s_addk_i32 s96, 4096\n"
    "v_mfma_f32_16x16x32_bf16 v[116:119], a[204:207], a[172:175], v[116:119]\n"
    "v_accvgpr_read_b32 v247, a23\n"
    "v_lshlrev_b32 v244, 16, v245\n"
    "v_mfma_f32_16x16x32_bf16 v[84:87], a[204:207], a[168:171], v[84:87]\n"
    "v_lshlrev_b32 v246, 16, v247\n"
    "v_and_b32 v245, s58, v245\n"
    "v_mfma_f32_16x16x32_bf16 v[84:87], a[200:203], a[136:139], v[84:87]\n"
    "v_and_b32 v247, s58, v247\n"
    "v_mul_f32 v244, v244, v164\n"
    "v_mfma_f32_16x16x32_bf16 v[52:55], a[200:203], a[132:135], v[52:55]\n"
    "v_mul_f32 v246, v246, v166\n"
    "v_mul_f32 v245, v245, v165\n"
    "v_mfma_f32_16x16x32_bf16 v[52:55], a[204:207], a[164:167], v[52:55]\n"
    "v_mul_f32 v247, v247, v167\n"
    "v_cvt_pk_bf16_f32 v162, v244, v245\n"
    "v_mfma_f32_16x16x32_bf16 v[20:23], a[204:207], a[160:163], v[20:23]\n"
    "v_cvt_pk_bf16_f32 v163, v246, v247\n"
    "v_mfma_f32_16x16x32_bf16 v[20:23], a[200:203], a[128:131], v[20:23]\n"
    "s_waitcnt vmcnt(14)\n"
    "v_mfma_f32_16x16x32_bf16 v[24:27], a[208:211], a[128:131], v[24:27]\n"
    "ds_write_b128 v248, v[160:163] offset:2048\n"
    "v_mfma_f32_16x16x32_bf16 v[24:27], a[212:215], a[160:163], v[24:27]\n"
    "buffer_load_dwordx4 a[200:203], v240, s[20:23], 0 offen offset:2048 sc1\n"
    "v_accvgpr_read_b32 v245, a52\n"
    "v_mfma_f32_16x16x32_bf16 v[56:59], a[212:215], a[164:167], v[56:59]\n"
    "buffer_load_dwordx4 a[204:207], v240, s[20:23], 0 offen offset:3072 sc1\n"
    "v_accvgpr_read_b32 v247, a53\n"
    "v_mfma_f32_16x16x32_bf16 v[56:59], a[208:211], a[132:135], v[56:59]\n"
    "s_add_u32 s63, s70, 196608\n"
    "s_waitcnt lgkmcnt(2)\n"
    "v_lshlrev_b32 v244, 16, v245\n"
    "v_mfma_f32_16x16x32_bf16 v[88:91], a[208:211], a[136:139], v[88:91]\n"
    "buffer_store_dwordx4 v[196:199], v243, s[40:43], s63 offen nt\n"
    "v_lshlrev_b32 v246, 16, v247\n"
    "v_and_b32 v245, s58, v245\n"
    "v_and_b32 v247, s58, v247\n"
    "v_mfma_f32_16x16x32_bf16 v[88:91], a[212:215], a[168:171], v[88:91]\n"
    "v_mul_f32 v244, v244, v168\n"
    "v_mul_f32 v246, v246, v170\n"
    "v_mfma_f32_16x16x32_bf16 v[120:123], a[212:215], a[172:175], v[120:123]\n"
    "v_mul_f32 v245, v245, v169\n"
    "v_mul_f32 v247, v247, v171\n"
    "v_mfma_f32_16x16x32_bf16 v[120:123], a[208:211], a[140:143], v[120:123]\n"
    "v_cvt_pk_bf16_f32 v168, v244, v245\n"
    "v_cvt_pk_bf16_f32 v169, v246, v247\n"
    "v_mfma_f32_16x16x32_bf16 v[152:155], a[208:211], a[144:147], v[152:155]\n"
    "buffer_load_dwordx4 a[60:63], v240, s[96:99], 0 offen offset:0 sc1\n"
    "v_accvgpr_read_b32 v245, a54\n"
    "v_mfma_f32_16x16x32_bf16 v[152:155], a[212:215], a[176:179], v[152:155]\n"
    "v_accvgpr_read_b32 v247, a55\n"
    "v_lshlrev_b32 v244, 16, v245\n"
    "v_mfma_f32_16x16x32_bf16 v[184:187], a[212:215], a[180:183], v[184:187]\n"
    "v_lshlrev_b32 v246, 16, v247\n"
    "v_and_b32 v245, s58, v245\n"
    "v_mfma_f32_16x16x32_bf16 v[184:187], a[208:211], a[148:151], v[184:187]\n"
    "v_and_b32 v247, s58, v247\n"
    "v_mul_f32 v244, v244, v172\n"
    "v_mfma_f32_16x16x32_bf16 v[216:219], a[208:211], a[152:155], v[216:219]\n"
    "v_mul_f32 v246, v246, v174\n"
    "v_mul_f32 v245, v245, v173\n"
    "v_mfma_f32_16x16x32_bf16 v[216:219], a[212:215], a[184:187], v[216:219]\n"
    "v_mul_f32 v247, v247, v175\n"
    "v_cvt_pk_bf16_f32 v170, v244, v245\n"
    "v_mfma_f32_16x16x32_bf16 a[248:251], a[212:215], a[188:191], a[248:251]\n"
    "v_cvt_pk_bf16_f32 v171, v246, v247\n"
    "v_mfma_f32_16x16x32_bf16 a[248:251], a[208:211], a[156:159], a[248:251]\n"
    "s_waitcnt vmcnt(16)\n"
    "v_mfma_f32_16x16x32_bf16 a[252:255], a[216:219], a[156:159], a[252:255]\n"
    "ds_write_b128 v249, v[168:171] offset:2048\n"
    "v_mfma_f32_16x16x32_bf16 a[252:255], a[220:223], a[188:191], a[252:255]\n"
    "s_addk_i32 s20, -20480\n"
    "buffer_load_dwordx4 a[208:211], v240, s[20:23], 0 offen offset:0 sc1\n"
    "v_mfma_f32_16x16x32_bf16 v[220:223], a[220:223], a[184:187], v[220:223]\n"
    "buffer_load_dwordx4 a[212:215], v240, s[20:23], 0 offen offset:1024 sc1\n"
    "ds_read_b128 v[164:167], v242 offset:2048\n"
    "v_mfma_f32_16x16x32_bf16 v[220:223], a[216:219], a[152:155], v[220:223]\n"
    "ds_read_b128 v[172:175], v242 offset:3072\n"
    "v_accvgpr_read_b32 v245, a16\n"
    "v_mfma_f32_16x16x32_bf16 v[188:191], a[216:219], a[148:151], v[188:191]\n"
    "ds_read_b128 a[156:159], v241 offset:14336\n"
    "s_add_u32 s63, s70, 212992\n"
    "v_mfma_f32_16x16x32_bf16 v[188:191], a[220:223], a[180:183], v[188:191]\n"
    "s_waitcnt lgkmcnt(5)\n"
    "v_mfma_f32_16x16x32_bf16 v[156:159], a[220:223], a[176:179], v[156:159]\n"
    "buffer_store_dwordx4 v[204:207], v243, s[40:43], s63 offen nt\n"
    "ds_read_b128 a[188:191], v241 offset:15360\n"
    "ds_read_b128 a[184:187], v241 offset:13312\n"
    "ds_read_b128 a[152:155], v241 offset:12288\n"
    "v_mfma_f32_16x16x32_bf16 v[156:159], a[216:219], a[144:147], v[156:159]\n"
    "ds_read_b128 a[148:151], v241 offset:10240\n"
    "v_accvgpr_read_b32 v247, a17\n"
    "v_mfma_f32_16x16x32_bf16 v[124:127], a[216:219], a[140:143], v[124:127]\n"
    "ds_read_b128 a[180:183], v241 offset:11264\n"
    "v_lshlrev_b32 v244, 16, v245\n"
    "v_mfma_f32_16x16x32_bf16 v[124:127], a[220:223], a[172:175], v[124:127]\n"
    "buffer_load_dwordx4 v[232:235], v240, s[96:99], 0 offen offset:1024 sc1\n"
    "ds_read_b128 a[176:179], v241 offset:9216\n"
    "v_mfma_f32_16x16x32_bf16 v[92:95], a[220:223], a[168:171], v[92:95]\n"
    "ds_read_b128 a[144:147], v241 offset:8192\n"
    "v_lshlrev_b32 v246, 16, v247\n"
    "v_mfma_f32_16x16x32_bf16 v[92:95], a[216:219], a[136:139], v[92:95]\n"
    "ds_read_b128 a[140:143], v241 offset:6144\n"
    "v_and_b32 v245, s58, v245\n"
    "v_mfma_f32_16x16x32_bf16 v[60:63], a[216:219], a[132:135], v[60:63]\n"
    "ds_read_b128 a[172:175], v241 offset:7168\n"
    "v_and_b32 v247, s58, v247\n"
    "v_mfma_f32_16x16x32_bf16 v[60:63], a[220:223], a[164:167], v[60:63]\n"
    "ds_read_b128 a[168:171], v241 offset:5120\n"
    "v_mul_f32 v244, v244, v128\n"
    "v_mfma_f32_16x16x32_bf16 v[28:31], a[220:223], a[160:163], v[28:31]\n"
    "ds_read_b128 a[136:139], v241 offset:4096\n"
    "v_mul_f32 v246, v246, v130\n"
    "v_mfma_f32_16x16x32_bf16 v[28:31], a[216:219], a[128:131], v[28:31]\n"
    "s_waitcnt vmcnt(17) lgkmcnt(10)\n"
    "ds_read_b128 a[132:135], v241 offset:2048\n"
    "v_mul_f32 v245, v245, v129\n"
    "v_mfma_f32_16x16x32_bf16 a[240:243], a[224:227], a[156:159], a[240:243]\n"
    "ds_read_b128 a[164:167], v241 offset:3072\n"
    "v_mul_f32 v247, v247, v131\n"
    "v_mfma_f32_16x16x32_bf16 a[240:243], a[228:231], a[188:191], a[240:243]\n"
    "s_waitcnt lgkmcnt(10)\n"
    "buffer_load_dwordx4 a[216:219], v240, s[20:23], 0 offen offset:2048 sc1\n"
    "v_mfma_f32_16x16x32_bf16 v[208:211], a[228:231], a[184:187], v[208:211]\n"
    "buffer_load_dwordx4 a[220:223], v240, s[20:23], 0 offen offset:3072 sc1\n"
    "ds_read_b128 a[160:163], v241 offset:1024\n"
    "v_mfma_f32_16x16x32_bf16 v[208:211], a[224:227], a[152:155], v[208:211]\n"
    "s_waitcnt lgkmcnt(9)\n"
    "ds_read_b128 a[128:131], v241 offset:0\n"
    "s_add_u32 s63, s70, 163840\n"
    "v_mfma_f32_16x16x32_bf16 v[176:179], a[224:227], a[148:151], v[176:179]\n"
    "buffer_store_dwordx4 v[164:167], v243, s[40:43], s63 offen nt\n"
    "v_cvt_pk_bf16_f32 v128, v244, v245\n"
    "v_cvt_pk_bf16_f32 v129, v246, v247\n"
    "v_accvgpr_read_b32 v245, a18\n"
    "v_mfma_f32_16x16x32_bf16 v[176:179], a[228:231], a[180:183], v[176:179]\n"
    "s_waitcnt lgkmcnt(8)\n"
    "v_accvgpr_read_b32 v247, a19\n"
    "v_lshlrev_b32 v244, 16, v245\n"
    "v_mfma_f32_16x16x32_bf16 v[144:147], a[228:231], a[176:179], v[144:147]\n"
    "v_lshlrev_b32 v246, 16, v247\n"
    "v_and_b32 v245, s58, v245\n"
    "v_mfma_f32_16x16x32_bf16 v[144:147], a[224:227], a[144:147], v[144:147]\n"
    "s_waitcnt lgkmcnt(6)\n"
    "v_and_b32 v247, s58, v247\n"
    "v_mul_f32 v244, v244, v132\n"
    "v_mfma_f32_16x16x32_bf16 v[112:115], a[224:227], a[140:143], v[112:115]\n"
    "buffer_load_dwordx4 v[228:231], v240, s[96:99], 0 offen offset:2048 sc1\n"
    "v_mul_f32 v246, v246, v134\n"
    "v_mfma_f32_16x16x32_bf16 v[112:115], a[228:231], a[172:175], v[112:115]\n"
    "s_waitcnt lgkmcnt(4)\n"
    "v_mul_f32 v245, v245, v133\n"
    "v_mul_f32 v247, v247, v135\n"
    "v_mfma_f32_16x16x32_bf16 v[80:83], a[228:231], a[168:171], v[80:83]\n"
    "v_cvt_pk_bf16_f32 v130, v244, v245\n"
    "v_cvt_pk_bf16_f32 v131, v246, v247\n"
    "v_mfma_f32_16x16x32_bf16 v[80:83], a[224:227], a[136:139], v[80:83]\n"
    "s_waitcnt lgkmcnt(2)\n"
    "v_mfma_f32_16x16x32_bf16 v[48:51], a[224:227], a[132:135], v[48:51]\n"
    "ds_write_b128 v248, v[128:131] offset:0\n"
    "v_mfma_f32_16x16x32_bf16 v[48:51], a[228:231], a[164:167], v[48:51]\n"
    "s_waitcnt lgkmcnt(1)\n"
    "v_accvgpr_read_b32 v245, a48\n"
    "v_accvgpr_read_b32 v247, a49\n"
    "v_mfma_f32_16x16x32_bf16 v[16:19], a[228:231], a[160:163], v[16:19]\n"
    "v_lshlrev_b32 v244, 16, v245\n"
    "v_lshlrev_b32 v246, 16, v247\n"
    "v_mfma_f32_16x16x32_bf16 v[16:19], a[224:227], a[128:131], v[16:19]\n"
    "s_waitcnt vmcnt(18)\n"
    "s_addk_i32 s20, 4096\n"
    "v_and_b32 v245, s58, v245\n"
    "v_mfma_f32_16x16x32_bf16 v[20:23], a[232:235], a[128:131], v[20:23]\n"
    "buffer_load_dwordx4 a[224:227], v240, s[20:23], 0 offen offset:0 sc1\n"
    "v_and_b32 v247, s58, v247\n"
    "v_mfma_f32_16x16x32_bf16 v[20:23], a[236:239], a[160:163], v[20:23]\n"
    "buffer_load_dwordx4 a[228:231], v240, s[20:23], 0 offen offset:1024 sc1\n"
    "v_mul_f32 v244, v244, v136\n"
    "v_mfma_f32_16x16x32_bf16 v[52:55], a[236:239], a[164:167], v[52:55]\n"
    "v_mul_f32 v246, v246, v138\n"
    "v_mul_f32 v245, v245, v137\n"
    "v_mfma_f32_16x16x32_bf16 v[52:55], a[232:235], a[132:135], v[52:55]\n"
    "s_add_u32 s63, s70, 180224\n"
    "v_mul_f32 v247, v247, v139\n"
    "v_mfma_f32_16x16x32_bf16 v[84:87], a[232:235], a[136:139], v[84:87]\n"
    "buffer_store_dwordx4 v[172:175], v243, s[40:43], s63 offen nt\n"
    "v_cvt_pk_bf16_f32 v136, v244, v245\n"
    "v_cvt_pk_bf16_f32 v137, v246, v247\n"
    "v_accvgpr_read_b32 v245, a50\n"
    "v_mfma_f32_16x16x32_bf16 v[84:87], a[236:239], a[168:171], v[84:87]\n"
    "v_accvgpr_read_b32 v247, a51\n"
    "v_lshlrev_b32 v244, 16, v245\n"
    "v_mfma_f32_16x16x32_bf16 v[116:119], a[236:239], a[172:175], v[116:119]\n"
    "v_lshlrev_b32 v246, 16, v247\n"
    "v_and_b32 v245, s58, v245\n"
    "v_mfma_f32_16x16x32_bf16 v[116:119], a[232:235], a[140:143], v[116:119]\n"
    "v_and_b32 v247, s58, v247\n"
    "v_mul_f32 v244, v244, v140\n"
    "v_mfma_f32_16x16x32_bf16 v[148:151], a[232:235], a[144:147], v[148:151]\n"
    "buffer_load_dwordx4 a[56:59], v240, s[96:99], 0 offen offset:3072 sc1\n"
    "s_addk_i32 s96, 4096\n"
    "v_mfma_f32_16x16x32_bf16 v[148:151], a[236:239], a[176:179], v[148:151]\n"
    "v_mul_f32 v246, v246, v142\n"
    "v_mul_f32 v245, v245, v141\n"
    "v_mfma_f32_16x16x32_bf16 v[180:183], a[236:239], a[180:183], v[180:183]\n"
    "v_mul_f32 v247, v247, v143\n"
    "v_cvt_pk_bf16_f32 v138, v244, v245\n"
    "v_mfma_f32_16x16x32_bf16 v[180:183], a[232:235], a[148:151], v[180:183]\n"
    "v_cvt_pk_bf16_f32 v139, v246, v247\n"
    "v_mfma_f32_16x16x32_bf16 v[212:215], a[232:235], a[152:155], v[212:215]\n"
    "ds_write_b128 v249, v[136:139] offset:0\n"
    "v_mfma_f32_16x16x32_bf16 v[212:215], a[236:239], a[184:187], v[212:215]\n"
    "ds_read_b128 v[132:135], v242 offset:0\n"
    "v_accvgpr_read_b32 v245, a12\n"
    "v_mfma_f32_16x16x32_bf16 a[244:247], a[236:239], a[188:191], a[244:247]\n"
    "ds_read_b128 v[140:143], v242 offset:1024\n"
    "v_accvgpr_read_b32 v247, a13\n"
    "v_mfma_f32_16x16x32_bf16 a[244:247], a[232:235], a[156:159], a[244:247]\n"
    "s_waitcnt vmcnt(18)\n"
    "buffer_load_dwordx4 a[232:235], v240, s[20:23], 0 offen offset:2048 sc1\n"
    "v_mfma_f32_16x16x32_bf16 a[248:251], a[192:195], a[156:159], a[248:251]\n"
    "buffer_load_dwordx4 a[236:239], v240, s[20:23], 0 offen offset:3072 sc1\n"
    "v_lshlrev_b32 v244, 16, v245\n"
    "v_mfma_f32_16x16x32_bf16 a[248:251], a[196:199], a[188:191], a[248:251]\n"
    "v_lshlrev_b32 v246, 16, v247\n"
    "v_and_b32 v245, s58, v245\n"
    "v_mfma_f32_16x16x32_bf16 v[216:219], a[196:199], a[184:187], v[216:219]\n"
    "v_and_b32 v247, s58, v247\n"
    "v_mul_f32 v244, v244, v96\n"
    "v_mfma_f32_16x16x32_bf16 v[216:219], a[192:195], a[152:155], v[216:219]\n"
    "s_add_u32 s63, s70, 131072\n"
    "s_waitcnt lgkmcnt(1)\n"
    "v_mul_f32 v246, v246, v98\n"
    "v_mfma_f32_16x16x32_bf16 v[184:187], a[192:195], a[148:151], v[184:187]\n"
    "buffer_store_dwordx4 v[132:135], v243, s[40:43], s63 offen nt\n"
    "v_mul_f32 v245, v245, v97\n"
    "v_mul_f32 v247, v247, v99\n"
    "v_cvt_pk_bf16_f32 v96, v244, v245\n"
    "v_mfma_f32_16x16x32_bf16 v[184:187], a[196:199], a[180:183], v[184:187]\n"
    "v_cvt_pk_bf16_f32 v97, v246, v247\n"
    "v_accvgpr_read_b32 v245, a14\n"
    "v_mfma_f32_16x16x32_bf16 v[152:155], a[196:199], a[176:179], v[152:155]\n"
    "v_accvgpr_read_b32 v247, a15\n"
    "v_lshlrev_b32 v244, 16, v245\n"
    "v_mfma_f32_16x16x32_bf16 v[152:155], a[192:195], a[144:147], v[152:155]\n"
    "v_lshlrev_b32 v246, 16, v247\n"
    "v_and_b32 v245, s58, v245\n"
    "v_mfma_f32_16x16x32_bf16 v[120:123], a[192:195], a[140:143], v[120:123]\n"
    "buffer_load_dwordx4 v[200:203], v240, s[96:99], 0 offen offset:0 sc1\n"
    "v_and_b32 v247, s58, v247\n"
    "v_mfma_f32_16x16x32_bf16 v[120:123], a[196:199], a[172:175], v[120:123]\n"
    "v_mul_f32 v244, v244, v100\n"
    "v_mul_f32 v246, v246, v102\n"
    "v_mfma_f32_16x16x32_bf16 v[88:91], a[196:199], a[168:171], v[88:91]\n"
    "v_mul_f32 v245, v245, v101\n"
    "v_mul_f32 v247, v247, v103\n"
    "v_mfma_f32_16x16x32_bf16 v[88:91], a[192:195], a[136:139], v[88:91]\n"
    "v_cvt_pk_bf16_f32 v98, v244, v245\n"
    "v_cvt_pk_bf16_f32 v99, v246, v247\n"
    "v_mfma_f32_16x16x32_bf16 v[56:59], a[192:195], a[132:135], v[56:59]\n"
    "ds_write_b128 v248, v[96:99] offset:2048\n"
    "v_mfma_f32_16x16x32_bf16 v[56:59], a[196:199], a[164:167], v[56:59]\n"
    "v_accvgpr_read_b32 v245, a44\n"
    "v_accvgpr_read_b32 v247, a45\n"
    "v_mfma_f32_16x16x32_bf16 v[24:27], a[196:199], a[160:163], v[24:27]\n"
    "v_lshlrev_b32 v244, 16, v245\n"
    "v_lshlrev_b32 v246, 16, v247\n"
    "v_mfma_f32_16x16x32_bf16 v[24:27], a[192:195], a[128:131], v[24:27]\n"
    "s_waitcnt vmcnt(18)\n"
    "s_addk_i32 s20, -20480\n"
    "v_and_b32 v245, s58, v245\n"
    "v_mfma_f32_16x16x32_bf16 v[28:31], a[200:203], a[128:131], v[28:31]\n"
    "buffer_load_dwordx4 a[192:195], v240, s[20:23], 0 offen offset:0 sc1\n"
    "v_and_b32 v247, s58, v247\n"
    "v_mfma_f32_16x16x32_bf16 v[28:31], a[204:207], a[160:163], v[28:31]\n"
    "buffer_load_dwordx4 a[196:199], v240, s[20:23], 0 offen offset:1024 sc1\n"
    "v_mul_f32 v244, v244, v104\n"
    "v_mfma_f32_16x16x32_bf16 v[60:63], a[204:207], a[164:167], v[60:63]\n"
    "v_add_u32 v241, s66, v240\n"
    "v_mul_f32 v246, v246, v106\n"
    "v_mfma_f32_16x16x32_bf16 v[60:63], a[200:203], a[132:135], v[60:63]\n"
    "ds_read_b128 a[128:131], v241 offset:49152\n"
    "s_add_u32 s63, s70, 147456\n"
    "v_mfma_f32_16x16x32_bf16 v[92:95], a[200:203], a[136:139], v[92:95]\n"
    "s_waitcnt lgkmcnt(2)\n"
    "v_mul_f32 v245, v245, v105\n"
    "v_mfma_f32_16x16x32_bf16 v[92:95], a[204:207], a[168:171], v[92:95]\n"
    "buffer_store_dwordx4 v[140:143], v243, s[40:43], s63 offen nt\n"
    "ds_read_b128 a[160:163], v241 offset:50176\n"
    "ds_read_b128 a[164:167], v241 offset:52224\n"
    "ds_read_b128 a[132:135], v241 offset:51200\n"
    "v_mfma_f32_16x16x32_bf16 v[124:127], a[204:207], a[172:175], v[124:127]\n"
    "ds_read_b128 a[136:139], v241 offset:53248\n"
    "v_mul_f32 v247, v247, v107\n"
    "v_mfma_f32_16x16x32_bf16 v[124:127], a[200:203], a[140:143], v[124:127]\n"
    "ds_read_b128 a[168:171], v241 offset:54272\n"
    "v_cvt_pk_bf16_f32 v104, v244, v245\n"
    "v_mfma_f32_16x16x32_bf16 v[156:159], a[200:203], a[144:147], v[156:159]\n"
    "buffer_load_dwordx4 v[236:239], v240, s[96:99], 0 offen offset:1024 sc1\n"
    "ds_read_b128 a[172:175], v241 offset:56320\n"
    "v_mfma_f32_16x16x32_bf16 v[156:159], a[204:207], a[176:179], v[156:159]\n"
    "ds_read_b128 a[140:143], v241 offset:55296\n"
    "v_cvt_pk_bf16_f32 v105, v246, v247\n"
    "v_mfma_f32_16x16x32_bf16 v[188:191], a[204:207], a[180:183], v[188:191]\n"
    "ds_read_b128 a[144:147], v241 offset:57344\n"
    "v_accvgpr_read_b32 v245, a46\n"
    "v_mfma_f32_16x16x32_bf16 v[188:191], a[200:203], a[148:151], v[188:191]\n"
    "ds_read_b128 a[176:179], v241 offset:58368\n"
    "v_accvgpr_read_b32 v247, a47\n"
    "v_mfma_f32_16x16x32_bf16 v[220:223], a[200:203], a[152:155], v[220:223]\n"
    "ds_read_b128 a[180:183], v241 offset:60416\n"
    "v_lshlrev_b32 v244, 16, v245\n"
    "v_mfma_f32_16x16x32_bf16 v[220:223], a[204:207], a[184:187], v[220:223]\n"
    "ds_read_b128 a[148:151], v241 offset:59392\n"
    "v_lshlrev_b32 v246, 16, v247\n"
    "v_mfma_f32_16x16x32_bf16 a[252:255], a[204:207], a[188:191], a[252:255]\n"
    "ds_read_b128 a[152:155], v241 offset:61440\n"
    "v_and_b32 v245, s58, v245\n"
    "v_mfma_f32_16x16x32_bf16 a[252:255], a[200:203], a[156:159], a[252:255]\n"
    "s_waitcnt vmcnt(18) lgkmcnt(11)\n"
    "ds_read_b128 a[184:187], v241 offset:62464\n"
    "v_and_b32 v247, s58, v247\n"
    "v_mfma_f32_16x16x32_bf16 v[16:19], a[208:211], a[128:131], v[16:19]\n"
    "buffer_load_dwordx4 a[200:203], v240, s[20:23], 0 offen offset:2048 sc1\n"
    "v_mul_f32 v244, v244, v108\n"
    "v_mfma_f32_16x16x32_bf16 v[16:19], a[212:215], a[160:163], v[16:19]\n"
    "s_waitcnt lgkmcnt(10)\n"
    "buffer_load_dwordx4 a[204:207], v240, s[20:23], 0 offen offset:3072 sc1\n"
    "v_mfma_f32_16x16x32_bf16 v[48:51], a[212:215], a[164:167], v[48:51]\n"
    "ds_read_b128 a[188:191], v241 offset:64512\n"
    "v_mul_f32 v246, v246, v110\n"
    "v_mfma_f32_16x16x32_bf16 v[48:51], a[208:211], a[132:135], v[48:51]\n"
    "s_waitcnt lgkmcnt(9)\n"
    "ds_read_b128 a[156:159], v241 offset:63488\n"
    "v_mul_f32 v245, v245, v109\n"
    "v_mfma_f32_16x16x32_bf16 v[80:83], a[208:211], a[136:139], v[80:83]\n"
    "v_mul_f32 v247, v247, v111\n"
    "v_cvt_pk_bf16_f32 v106, v244, v245\n"
    "v_mfma_f32_16x16x32_bf16 v[80:83], a[212:215], a[168:171], v[80:83]\n"
    "s_waitcnt lgkmcnt(8)\n"
    "v_cvt_pk_bf16_f32 v107, v246, v247\n"
    "v_mfma_f32_16x16x32_bf16 v[112:115], a[212:215], a[172:175], v[112:115]\n"
    "ds_write_b128 v249, v[104:107] offset:2048\n"
    "v_mfma_f32_16x16x32_bf16 v[112:115], a[208:211], a[140:143], v[112:115]\n"
    "s_waitcnt lgkmcnt(7)\n"
    "ds_read_b128 v[100:103], v242 offset:2048\n"
    "v_accvgpr_read_b32 v245, a8\n"
    "v_mfma_f32_16x16x32_bf16 v[144:147], a[208:211], a[144:147], v[144:147]\n"
    "ds_read_b128 v[108:111], v242 offset:3072\n"
    "v_accvgpr_read_b32 v247, a9\n"
    "v_mfma_f32_16x16x32_bf16 v[144:147], a[212:215], a[176:179], v[144:147]\n"
    "s_waitcnt lgkmcnt(7)\n"
    "buffer_load_dwordx4 a[20:23], v240, s[96:99], 0 offen offset:2048 sc1\n"
    "v_mfma_f32_16x16x32_bf16 v[176:179], a[212:215], a[180:183], v[176:179]\n"
    "v_lshlrev_b32 v244, 16, v245\n"
    "v_lshlrev_b32 v246, 16, v247\n"
    "v_mfma_f32_16x16x32_bf16 v[176:179], a[208:211], a[148:151], v[176:179]\n"
    "s_waitcnt lgkmcnt(5)\n"
    "v_and_b32 v245, s58, v245\n"
    "v_and_b32 v247, s58, v247\n"
    "v_mfma_f32_16x16x32_bf16 v[208:211], a[208:211], a[152:155], v[208:211]\n"
    "v_mul_f32 v244, v244, v64\n"
    "v_mul_f32 v246, v246, v66\n"
    "v_mfma_f32_16x16x32_bf16 v[208:211], a[212:215], a[184:187], v[208:211]\n"
    "s_waitcnt lgkmcnt(3)\n"
    "v_mul_f32 v245, v245, v65\n"
    "v_mul_f32 v247, v247, v67\n"
    "v_mfma_f32_16x16x32_bf16 a[240:243], a[212:215], a[188:191], a[240:243]\n"
    "v_cvt_pk_bf16_f32 v64, v244, v245\n"
    "v_cvt_pk_bf16_f32 v65, v246, v247\n"
    "v_mfma_f32_16x16x32_bf16 a[240:243], a[208:211], a[156:159], a[240:243]\n"
    "s_waitcnt vmcnt(17)\n"
    "s_addk_i32 s20, 4096\n"
    "v_accvgpr_read_b32 v245, a10\n"
    "v_mfma_f32_16x16x32_bf16 a[244:247], a[216:219], a[156:159], a[244:247]\n"
    "buffer_load_dwordx4 a[208:211], v240, s[20:23], 0 offen offset:0 sc1\n"
    "v_accvgpr_read_b32 v247, a11\n"
    "v_mfma_f32_16x16x32_bf16 a[244:247], a[220:223], a[188:191], a[244:247]\n"
    "buffer_load_dwordx4 a[212:215], v240, s[20:23], 0 offen offset:1024 sc1\n"
    "v_lshlrev_b32 v244, 16, v245\n"
    "v_mfma_f32_16x16x32_bf16 v[212:215], a[220:223], a[184:187], v[212:215]\n"
    "v_lshlrev_b32 v246, 16, v247\n"
    "v_and_b32 v245, s58, v245\n"
    "v_mfma_f32_16x16x32_bf16 v[212:215], a[216:219], a[152:155], v[212:215]\n"
    "s_add_u32 s63, s70, 98304\n"
    "s_waitcnt lgkmcnt(1)\n"
    "v_and_b32 v247, s58, v247\n"
    "v_mfma_f32_16x16x32_bf16 v[180:183], a[216:219], a[148:151], v[180:183]\n"
    "buffer_store_dwordx4 v[100:103], v243, s[40:43], s63 offen nt\n"
    "v_mul_f32 v244, v244, v68\n"
    "v_mul_f32 v246, v246, v70\n"
    "v_mul_f32 v245, v245, v69\n"
    "v_mfma_f32_16x16x32_bf16 v[180:183], a[220:223], a[180:183], v[180:183]\n"
    "v_mul_f32 v247, v247, v71\n"
    "v_cvt_pk_bf16_f32 v66, v244, v245\n"
    "v_mfma_f32_16x16x32_bf16 v[148:151], a[220:223], a[176:179], v[148:151]\n"
    "v_cvt_pk_bf16_f32 v67, v246, v247\n"
    "v_mfma_f32_16x16x32_bf16 v[148:151], a[216:219], a[144:147], v[148:151]\n"
    "ds_write_b128 v248, v[64:67] offset:0\n"
    "v_mfma_f32_16x16x32_bf16 v[116:119], a[216:219], a[140:143], v[116:119]\n"
    "buffer_load_dwordx4 v[160:163], v240, s[96:99], 0 offen offset:3072 sc1\n"
    "s_addk_i32 s96, 4096\n"
    "v_mfma_f32_16x16x32_bf16 v[116:119], a[220:223], a[172:175], v[116:119]\n"
    "v_accvgpr_read_b32 v245, a40\n"
    "v_accvgpr_read_b32 v247, a41\n"
    "v_mfma_f32_16x16x32_bf16 v[84:87], a[220:223], a[168:171], v[84:87]\n"
    "v_lshlrev_b32 v244, 16, v245\n"
    "v_lshlrev_b32 v246, 16, v247\n"
    "v_mfma_f32_16x16x32_bf16 v[84:87], a[216:219], a[136:139], v[84:87]\n"
    "v_and_b32 v245, s58, v245\n"
    "v_and_b32 v247, s58, v247\n"
    "v_mfma_f32_16x16x32_bf16 v[52:55], a[216:219], a[132:135], v[52:55]\n"
    "v_mul_f32 v244, v244, v72\n"
    "v_mul_f32 v246, v246, v74\n"
    "v_mfma_f32_16x16x32_bf16 v[52:55], a[220:223], a[164:167], v[52:55]\n"
    "v_mul_f32 v245, v245, v73\n"
    "v_mul_f32 v247, v247, v75\n"
    "v_mfma_f32_16x16x32_bf16 v[20:23], a[220:223], a[160:163], v[20:23]\n"
    "v_cvt_pk_bf16_f32 v72, v244, v245\n"
    "v_cvt_pk_bf16_f32 v73, v246, v247\n"
    "v_mfma_f32_16x16x32_bf16 v[20:23], a[216:219], a[128:131], v[20:23]\n"
    "s_waitcnt vmcnt(17)\n"
    "buffer_load_dwordx4 a[216:219], v240, s[20:23], 0 offen offset:2048 sc1\n"
    "v_mfma_f32_16x16x32_bf16 v[24:27], a[224:227], a[128:131], v[24:27]\n"
    "buffer_load_dwordx4 a[220:223], v240, s[20:23], 0 offen offset:3072 sc1\n"
    "v_accvgpr_read_b32 v245, a42\n"
    "v_mfma_f32_16x16x32_bf16 v[24:27], a[228:231], a[160:163], v[24:27]\n"
    "s_add_u32 s63, s70, 114688\n"
    "s_waitcnt lgkmcnt(1)\n"
    "v_accvgpr_read_b32 v247, a43\n"
    "v_mfma_f32_16x16x32_bf16 v[56:59], a[228:231], a[164:167], v[56:59]\n"
    "buffer_store_dwordx4 v[108:111], v243, s[40:43], s63 offen nt\n"
    "v_lshlrev_b32 v244, 16, v245\n"
    "v_lshlrev_b32 v246, 16, v247\n"
    "v_and_b32 v245, s58, v245\n"
    "v_mfma_f32_16x16x32_bf16 v[56:59], a[224:227], a[132:135], v[56:59]\n"
    "v_and_b32 v247, s58, v247\n"
    "v_mul_f32 v244, v244, v76\n"
    "v_mfma_f32_16x16x32_bf16 v[88:91], a[224:227], a[136:139], v[88:91]\n"
    "v_mul_f32 v246, v246, v78\n"
    "v_mul_f32 v245, v245, v77\n"
    "v_mfma_f32_16x16x32_bf16 v[88:91], a[228:231], a[168:171], v[88:91]\n"
    "v_mul_f32 v247, v247, v79\n"
    "v_cvt_pk_bf16_f32 v74, v244, v245\n"
    "v_mfma_f32_16x16x32_bf16 v[120:123], a[228:231], a[172:175], v[120:123]\n"
    "v_cvt_pk_bf16_f32 v75, v246, v247\n"
    "v_mfma_f32_16x16x32_bf16 v[120:123], a[224:227], a[140:143], v[120:123]\n"
    "ds_write_b128 v249, v[72:75] offset:0\n"
    "v_mfma_f32_16x16x32_bf16 v[152:155], a[224:227], a[144:147], v[152:155]\n"
    "ds_read_b128 v[68:71], v242 offset:0\n"
    "v_accvgpr_read_b32 v245, a4\n"
    "v_mfma_f32_16x16x32_bf16 v[152:155], a[228:231], a[176:179], v[152:155]\n"
    "ds_read_b128 v[76:79], v242 offset:1024\n"
    "v_accvgpr_read_b32 v247, a5\n"
    "v_mfma_f32_16x16x32_bf16 v[184:187], a[228:231], a[180:183], v[184:187]\n"
    "buffer_load_dwordx4 v[196:199], v240, s[96:99], 0 offen offset:0 sc1\n"
    "v_lshlrev_b32 v244, 16, v245\n"
    "v_mfma_f32_16x16x32_bf16 v[184:187], a[224:227], a[148:151], v[184:187]\n"
    "v_lshlrev_b32 v246, 16, v247\n"
    "v_and_b32 v245, s58, v245\n"
    "v_mfma_f32_16x16x32_bf16 v[216:219], a[224:227], a[152:155], v[216:219]\n"
    "v_and_b32 v247, s58, v247\n"
    "v_mul_f32 v244, v244, v32\n"
    "v_mfma_f32_16x16x32_bf16 v[216:219], a[228:231], a[184:187], v[216:219]\n"
    "v_mul_f32 v246, v246, v34\n"
    "v_mul_f32 v245, v245, v33\n"
    "v_mfma_f32_16x16x32_bf16 a[248:251], a[228:231], a[188:191], a[248:251]\n"
    "v_mul_f32 v247, v247, v35\n"
    "v_cvt_pk_bf16_f32 v32, v244, v245\n"
    "v_mfma_f32_16x16x32_bf16 a[248:251], a[224:227], a[156:159], a[248:251]\n"
    "s_waitcnt vmcnt(17)\n"
    "s_addk_i32 s20, -20480\n"
    "v_cvt_pk_bf16_f32 v33, v246, v247\n"
    "v_mfma_f32_16x16x32_bf16 a[252:255], a[232:235], a[156:159], a[252:255]\n"
    "buffer_load_dwordx4 a[224:227], v240, s[20:23], 0 offen offset:0 sc1\n"
    "v_accvgpr_read_b32 v245, a6\n"
    "v_mfma_f32_16x16x32_bf16 a[252:255], a[236:239], a[188:191], a[252:255]\n"
    "buffer_load_dwordx4 a[228:231], v240, s[20:23], 0 offen offset:1024 sc1\n"
    "v_accvgpr_read_b32 v247, a7\n"
    "v_mfma_f32_16x16x32_bf16 v[220:223], a[236:239], a[184:187], v[220:223]\n"
    "ds_read_b128 a[156:159], v241 offset:47104\n"
    "v_lshlrev_b32 v244, 16, v245\n"
    "v_mfma_f32_16x16x32_bf16 v[220:223], a[232:235], a[152:155], v[220:223]\n"
    "s_add_u32 s63, s70, 65536\n"
    "s_waitcnt lgkmcnt(2)\n"
    "v_lshlrev_b32 v246, 16, v247\n"
    "v_mfma_f32_16x16x32_bf16 v[188:191], a[232:235], a[148:151], v[188:191]\n"
    "buffer_store_dwordx4 v[68:71], v243, s[40:43], s63 offen nt\n"
    "ds_read_b128 a[188:191], v241 offset:48128\n"
    "ds_read_b128 a[184:187], v241 offset:46080\n"
    "v_and_b32 v245, s58, v245\n"
    "v_mfma_f32_16x16x32_bf16 v[188:191], a[236:239], a[180:183], v[188:191]\n"
    "ds_read_b128 a[152:155], v241 offset:45056\n"
    "v_and_b32 v247, s58, v247\n"
    "v_mfma_f32_16x16x32_bf16 v[156:159], a[236:239], a[176:179], v[156:159]\n"
    "ds_read_b128 a[148:151], v241 offset:43008\n"
    "v_mul_f32 v244, v244, v36\n"
    "v_mfma_f32_16x16x32_bf16 v[156:159], a[232:235], a[144:147], v[156:159]\n"
    "ds_read_b128 a[180:183], v241 offset:44032\n"
    "v_mul_f32 v246, v246, v38\n"
    "v_mfma_f32_16x16x32_bf16 v[124:127], a[232:235], a[140:143], v[124:127]\n"
    "buffer_load_dwordx4 a[52:55], v240, s[96:99], 0 offen offset:1024 sc1\n"
    "ds_read_b128 a[176:179], v241 offset:41984\n"
    "v_mfma_f32_16x16x32_bf16 v[124:127], a[236:239], a[172:175], v[124:127]\n"
    "ds_read_b128 a[144:147], v241 offset:40960\n"
    "v_mul_f32 v245, v245, v37\n"
    "v_mfma_f32_16x16x32_bf16 v[92:95], a[236:239], a[168:171], v[92:95]\n"
    "ds_read_b128 a[140:143], v241 offset:38912\n"
    "v_mul_f32 v247, v247, v39\n"
    "v_mfma_f32_16x16x32_bf16 v[92:95], a[232:235], a[136:139], v[92:95]\n"
    "ds_read_b128 a[172:175], v241 offset:39936\n"
    "v_cvt_pk_bf16_f32 v34, v244, v245\n"
    "v_mfma_f32_16x16x32_bf16 v[60:63], a[232:235], a[132:135], v[60:63]\n"
    "ds_read_b128 a[168:171], v241 offset:37888\n"
    "v_cvt_pk_bf16_f32 v35, v246, v247\n"
    "v_mfma_f32_16x16x32_bf16 v[60:63], a[236:239], a[164:167], v[60:63]\n"
    "ds_write_b128 v248, v[32:35] offset:2048\n"
    "v_mfma_f32_16x16x32_bf16 v[28:31], a[236:239], a[160:163], v[28:31]\n"
    "ds_read_b128 a[136:139], v241 offset:36864\n"
    "v_accvgpr_read_b32 v245, a36\n"
    "v_mfma_f32_16x16x32_bf16 v[28:31], a[232:235], a[128:131], v[28:31]\n"
    "s_waitcnt vmcnt(17) lgkmcnt(11)\n"
    "ds_read_b128 a[132:135], v241 offset:34816\n"
    "v_accvgpr_read_b32 v247, a37\n"
    "v_mfma_f32_16x16x32_bf16 a[240:243], a[192:195], a[156:159], a[240:243]\n"
    "ds_read_b128 a[164:167], v241 offset:35840\n"
    "v_lshlrev_b32 v244, 16, v245\n"
    "v_mfma_f32_16x16x32_bf16 a[240:243], a[196:199], a[188:191], a[240:243]\n"
    "s_waitcnt lgkmcnt(11)\n"
    "buffer_load_dwordx4 a[232:235], v240, s[20:23], 0 offen offset:2048 sc1\n"
    "v_mfma_f32_16x16x32_bf16 v[208:211], a[196:199], a[184:187], v[208:211]\n"
    "buffer_load_dwordx4 a[236:239], v240, s[20:23], 0 offen offset:3072 sc1\n"
    "ds_read_b128 a[160:163], v241 offset:33792\n"
    "v_mfma_f32_16x16x32_bf16 v[208:211], a[192:195], a[152:155], v[208:211]\n"
    "s_waitcnt lgkmcnt(10)\n"
    "ds_read_b128 a[128:131], v241 offset:32768\n"
    "s_add_u32 s63, s70, 81920\n"
    "v_mfma_f32_16x16x32_bf16 v[176:179], a[192:195], a[148:151], v[176:179]\n"
    "buffer_store_dwordx4 v[76:79], v243, s[40:43], s63 offen nt\n"
    "v_lshlrev_b32 v246, 16, v247\n"
    "v_and_b32 v245, s58, v245\n"
    "v_and_b32 v247, s58, v247\n"
    "v_mfma_f32_16x16x32_bf16 v[176:179], a[196:199], a[180:183], v[176:179]\n"
    "s_waitcnt lgkmcnt(9)\n"
    "v_mul_f32 v244, v244, v40\n"
    "v_mul_f32 v246, v246, v42\n"
    "v_mfma_f32_16x16x32_bf16 v[144:147], a[196:199], a[176:179], v[144:147]\n"
    "v_mul_f32 v245, v245, v41\n"
    "v_mul_f32 v247, v247, v43\n"
    "v_mfma_f32_16x16x32_bf16 v[144:147], a[192:195], a[144:147], v[144:147]\n"
    "s_waitcnt lgkmcnt(7)\n"
    "v_cvt_pk_bf16_f32 v40, v244, v245\n"
    "v_cvt_pk_bf16_f32 v41, v246, v247\n"
    "v_mfma_f32_16x16x32_bf16 v[112:115], a[192:195], a[140:143], v[112:115]\n"
    "buffer_load_dwordx4 v[168:171], v240, s[96:99], 0 offen offset:2048 sc1\n"
    "v_accvgpr_read_b32 v245, a38\n"
    "v_mfma_f32_16x16x32_bf16 v[112:115], a[196:199], a[172:175], v[112:115]\n"
    "s_waitcnt lgkmcnt(4)\n"
    "v_accvgpr_read_b32 v247, a39\n"
    "v_lshlrev_b32 v244, 16, v245\n"
    "v_mfma_f32_16x16x32_bf16 v[80:83], a[196:199], a[168:171], v[80:83]\n"
    "v_lshlrev_b32 v246, 16, v247\n"
    "v_and_b32 v245, s58, v245\n"
    "v_mfma_f32_16x16x32_bf16 v[80:83], a[192:195], a[136:139], v[80:83]\n"
    "s_waitcnt lgkmcnt(2)\n"
    "v_and_b32 v247, s58, v247\n"
    "v_mul_f32 v244, v244, v44\n"
    "v_mfma_f32_16x16x32_bf16 v[48:51], a[192:195], a[132:135], v[48:51]\n"
    "v_mul_f32 v246, v246, v46\n"
    "v_mul_f32 v245, v245, v45\n"
    "v_mfma_f32_16x16x32_bf16 v[48:51], a[196:199], a[164:167], v[48:51]\n"
    "s_waitcnt lgkmcnt(0)\n"
    "v_mul_f32 v247, v247, v47\n"
    "v_cvt_pk_bf16_f32 v42, v244, v245\n"
    "v_mfma_f32_16x16x32_bf16 v[16:19], a[196:199], a[160:163], v[16:19]\n"
    "v_cvt_pk_bf16_f32 v43, v246, v247\n"
    "v_mfma_f32_16x16x32_bf16 v[16:19], a[192:195], a[128:131], v[16:19]\n"
    "s_waitcnt vmcnt(17)\n"
    "v_mfma_f32_16x16x32_bf16 v[20:23], a[200:203], a[128:131], v[20:23]\n"
    "ds_write_b128 v249, v[40:43] offset:2048\n"
    "v_mfma_f32_16x16x32_bf16 v[20:23], a[204:207], a[160:163], v[20:23]\n"
    "s_addk_i32 s20, 4096\n"
    "buffer_load_dwordx4 a[192:195], v240, s[20:23], 0 offen offset:0 sc1\n"
    "v_mfma_f32_16x16x32_bf16 v[52:55], a[204:207], a[164:167], v[52:55]\n"
    "buffer_load_dwordx4 a[196:199], v240, s[20:23], 0 offen offset:1024 sc1\n"
    "ds_read_b128 v[36:39], v242 offset:2048\n"
    "v_mfma_f32_16x16x32_bf16 v[52:55], a[200:203], a[132:135], v[52:55]\n"
    "ds_read_b128 v[44:47], v242 offset:3072\n"
    "v_accvgpr_read_b32 v245, a0\n"
    "v_mfma_f32_16x16x32_bf16 v[84:87], a[200:203], a[136:139], v[84:87]\n"
    "v_accvgpr_read_b32 v247, a1\n"
    "v_lshlrev_b32 v244, 16, v245\n"
    "v_mfma_f32_16x16x32_bf16 v[84:87], a[204:207], a[168:171], v[84:87]\n"
    "v_lshlrev_b32 v246, 16, v247\n"
    "v_and_b32 v245, s58, v245\n"
    "v_mfma_f32_16x16x32_bf16 v[116:119], a[204:207], a[172:175], v[116:119]\n"
    "v_and_b32 v247, s58, v247\n"
    "v_mul_f32 v244, v244, v0\n"
    "v_mfma_f32_16x16x32_bf16 v[116:119], a[200:203], a[140:143], v[116:119]\n"
    "s_add_u32 s63, s70, 32768\n"
    "s_waitcnt lgkmcnt(1)\n"
    "v_mul_f32 v246, v246, v2\n"
    "v_mfma_f32_16x16x32_bf16 v[148:151], a[200:203], a[144:147], v[148:151]\n"
    "buffer_store_dwordx4 v[36:39], v243, s[40:43], s63 offen nt\n"
    "buffer_load_dwordx4 v[204:207], v240, s[96:99], 0 offen offset:3072 sc1\n"
    "s_addk_i32 s96, 4096\n"
    "v_mfma_f32_16x16x32_bf16 v[148:151], a[204:207], a[176:179], v[148:151]\n"
    "v_mul_f32 v245, v245, v1\n"
    "v_mul_f32 v247, v247, v3\n"
    "v_mfma_f32_16x16x32_bf16 v[180:183], a[204:207], a[180:183], v[180:183]\n"
    "v_cvt_pk_bf16_f32 v0, v244, v245\n"
    "v_cvt_pk_bf16_f32 v1, v246, v247\n"
    "v_mfma_f32_16x16x32_bf16 v[180:183], a[200:203], a[148:151], v[180:183]\n"
    "v_accvgpr_read_b32 v245, a2\n"
    "v_accvgpr_read_b32 v247, a3\n"
    "v_mfma_f32_16x16x32_bf16 v[212:215], a[200:203], a[152:155], v[212:215]\n"
    "v_lshlrev_b32 v244, 16, v245\n"
    "v_lshlrev_b32 v246, 16, v247\n"
    "v_mfma_f32_16x16x32_bf16 v[212:215], a[204:207], a[184:187], v[212:215]\n"
    "v_and_b32 v245, s58, v245\n"
    "v_and_b32 v247, s58, v247\n"
    "v_mfma_f32_16x16x32_bf16 a[244:247], a[204:207], a[188:191], a[244:247]\n"
    "v_mul_f32 v244, v244, v4\n"
    "v_mul_f32 v246, v246, v6\n"
    "v_mfma_f32_16x16x32_bf16 a[244:247], a[200:203], a[156:159], a[244:247]\n"
    "s_waitcnt vmcnt(18)\n"
    "buffer_load_dwordx4 a[200:203], v240, s[20:23], 0 offen offset:2048 sc1\n"
    "v_mfma_f32_16x16x32_bf16 a[248:251], a[208:211], a[156:159], a[248:251]\n"
    "buffer_load_dwordx4 a[204:207], v240, s[20:23], 0 offen offset:3072 sc1\n"
    "v_mul_f32 v245, v245, v5\n"
    "v_mfma_f32_16x16x32_bf16 a[248:251], a[212:215], a[188:191], a[248:251]\n"
    "buffer_load_dwordx4 v[164:167], v240, s[96:99], 0 offen offset:0 sc1\n"
    "v_mul_f32 v247, v247, v7\n"
    "v_mfma_f32_16x16x32_bf16 v[216:219], a[212:215], a[184:187], v[216:219]\n"
    "v_cvt_pk_bf16_f32 v2, v244, v245\n"
    "v_cvt_pk_bf16_f32 v3, v246, v247\n"
    "v_mfma_f32_16x16x32_bf16 v[216:219], a[208:211], a[152:155], v[216:219]\n"
    "ds_write_b128 v248, v[0:3] offset:0\n"
    "v_mfma_f32_16x16x32_bf16 v[184:187], a[208:211], a[148:151], v[184:187]\n"
    "s_add_u32 s63, s70, 49152\n"
    "s_waitcnt lgkmcnt(1)\n"
    "v_mfma_f32_16x16x32_bf16 v[184:187], a[212:215], a[180:183], v[184:187]\n"
    "buffer_store_dwordx4 v[44:47], v243, s[40:43], s63 offen nt\n"
    "buffer_load_dwordx4 a[16:19], v240, s[96:99], 0 offen offset:1024 sc1\n"
    "v_accvgpr_read_b32 v245, a32\n"
    "v_mfma_f32_16x16x32_bf16 v[152:155], a[212:215], a[176:179], v[152:155]\n"
    "v_accvgpr_read_b32 v247, a33\n"
    "v_lshlrev_b32 v244, 16, v245\n"
    "v_mfma_f32_16x16x32_bf16 v[152:155], a[208:211], a[144:147], v[152:155]\n"
    "v_lshlrev_b32 v246, 16, v247\n"
    "v_and_b32 v245, s58, v245\n"
    "v_mfma_f32_16x16x32_bf16 v[120:123], a[208:211], a[140:143], v[120:123]\n"
    "buffer_load_dwordx4 v[128:131], v240, s[96:99], 0 offen offset:2048 sc1\n"
    "v_and_b32 v247, s58, v247\n"
    "v_mfma_f32_16x16x32_bf16 v[120:123], a[212:215], a[172:175], v[120:123]\n"
    "v_mul_f32 v244, v244, v8\n"
    "v_mul_f32 v246, v246, v10\n"
    "v_mfma_f32_16x16x32_bf16 v[88:91], a[212:215], a[168:171], v[88:91]\n"
    "v_mul_f32 v245, v245, v9\n"
    "v_mul_f32 v247, v247, v11\n"
    "v_mfma_f32_16x16x32_bf16 v[88:91], a[208:211], a[136:139], v[88:91]\n"
    "v_cvt_pk_bf16_f32 v8, v244, v245\n"
    "v_cvt_pk_bf16_f32 v9, v246, v247\n"
    "v_mfma_f32_16x16x32_bf16 v[56:59], a[208:211], a[132:135], v[56:59]\n"
    "buffer_load_dwordx4 v[172:175], v240, s[96:99], 0 offen offset:3072 sc1\n"
    "s_addk_i32 s96, 4096\n"
    "v_mfma_f32_16x16x32_bf16 v[56:59], a[212:215], a[164:167], v[56:59]\n"
    "v_accvgpr_read_b32 v245, a34\n"
    "v_accvgpr_read_b32 v247, a35\n"
    "v_mfma_f32_16x16x32_bf16 v[24:27], a[212:215], a[160:163], v[24:27]\n"
    "v_lshlrev_b32 v244, 16, v245\n"
    "v_lshlrev_b32 v246, 16, v247\n"
    "v_mfma_f32_16x16x32_bf16 v[24:27], a[208:211], a[128:131], v[24:27]\n"
    "s_waitcnt vmcnt(21)\n"
    "v_and_b32 v245, s58, v245\n"
    "v_and_b32 v247, s58, v247\n"
    "v_mfma_f32_16x16x32_bf16 v[28:31], a[216:219], a[128:131], v[28:31]\n"
    "buffer_load_dwordx4 a[48:51], v240, s[96:99], 0 offen offset:0 sc1\n"
    "v_mul_f32 v244, v244, v12\n"
    "v_mfma_f32_16x16x32_bf16 v[28:31], a[220:223], a[160:163], v[28:31]\n"
    "v_mul_f32 v246, v246, v14\n"
    "v_mul_f32 v245, v245, v13\n"
    "v_mfma_f32_16x16x32_bf16 v[60:63], a[220:223], a[164:167], v[60:63]\n"
    "ds_read_b128 a[128:131], v241 offset:16384\n"
    "v_mul_f32 v247, v247, v15\n"
    "v_mfma_f32_16x16x32_bf16 v[60:63], a[216:219], a[132:135], v[60:63]\n"
    "ds_read_b128 a[160:163], v241 offset:17408\n"
    "v_cvt_pk_bf16_f32 v10, v244, v245\n"
    "v_mfma_f32_16x16x32_bf16 v[92:95], a[216:219], a[136:139], v[92:95]\n"
    "buffer_load_dwordx4 v[136:139], v240, s[96:99], 0 offen offset:1024 sc1\n"
    "ds_read_b128 a[164:167], v241 offset:19456\n"
    "v_mfma_f32_16x16x32_bf16 v[92:95], a[220:223], a[168:171], v[92:95]\n"
    "ds_read_b128 a[132:135], v241 offset:18432\n"
    "v_cvt_pk_bf16_f32 v11, v246, v247\n"
    "v_mfma_f32_16x16x32_bf16 v[124:127], a[220:223], a[172:175], v[124:127]\n"
    "ds_write_b128 v249, v[8:11] offset:0\n"
    "v_mfma_f32_16x16x32_bf16 v[124:127], a[216:219], a[140:143], v[124:127]\n"
    "ds_read_b128 a[136:139], v241 offset:20480\n"
    "v_mfma_f32_16x16x32_bf16 v[156:159], a[216:219], a[144:147], v[156:159]\n"
    "ds_read_b128 v[4:7], v242 offset:0\n"
    "v_mfma_f32_16x16x32_bf16 v[156:159], a[220:223], a[176:179], v[156:159]\n"
    "ds_read_b128 v[12:15], v242 offset:1024\n"
    "v_mfma_f32_16x16x32_bf16 v[188:191], a[220:223], a[180:183], v[188:191]\n"
    "ds_read_b128 a[168:171], v241 offset:21504\n"
    "v_mfma_f32_16x16x32_bf16 v[188:191], a[216:219], a[148:151], v[188:191]\n"
    "buffer_load_dwordx4 v[132:135], v240, s[96:99], 0 offen offset:2048 sc1\n"
    "ds_read_b128 a[172:175], v241 offset:23552\n"
    "v_mfma_f32_16x16x32_bf16 v[220:223], a[216:219], a[152:155], v[220:223]\n"
    "ds_read_b128 a[140:143], v241 offset:22528\n"
    "v_mfma_f32_16x16x32_bf16 v[220:223], a[220:223], a[184:187], v[220:223]\n"
    "ds_read_b128 a[144:147], v241 offset:24576\n"
    "v_mfma_f32_16x16x32_bf16 a[252:255], a[220:223], a[188:191], a[252:255]\n"
    "ds_read_b128 a[176:179], v241 offset:25600\n"
    "v_mfma_f32_16x16x32_bf16 a[252:255], a[216:219], a[156:159], a[252:255]\n"
    "s_waitcnt vmcnt(20) lgkmcnt(11)\n"
    "buffer_load_dwordx4 a[12:15], v240, s[96:99], 0 offen offset:3072 sc1\n"
    "v_mfma_f32_16x16x32_bf16 v[16:19], a[224:227], a[128:131], v[16:19]\n"
    "s_addk_i32 s96, 4096\n"
    "ds_read_b128 a[180:183], v241 offset:27648\n"
    "ds_read_b128 a[148:151], v241 offset:26624\n"
    "v_mfma_f32_16x16x32_bf16 v[16:19], a[228:231], a[160:163], v[16:19]\n"
    "s_waitcnt lgkmcnt(11)\n"
    "ds_read_b128 a[152:155], v241 offset:28672\n"
    "v_mfma_f32_16x16x32_bf16 v[48:51], a[228:231], a[164:167], v[48:51]\n"
    "ds_read_b128 a[184:187], v241 offset:29696\n"
    "v_mfma_f32_16x16x32_bf16 v[48:51], a[224:227], a[132:135], v[48:51]\n"
    "s_waitcnt lgkmcnt(8)\n"
    "buffer_load_dwordx4 v[96:99], v240, s[96:99], 0 offen offset:0 sc1\n"
    "ds_read_b128 a[188:191], v241 offset:31744\n"
    "v_mfma_f32_16x16x32_bf16 v[80:83], a[224:227], a[136:139], v[80:83]\n"
    "ds_read_b128 a[156:159], v241 offset:30720\n"
    "v_mfma_f32_16x16x32_bf16 v[80:83], a[228:231], a[168:171], v[80:83]\n"
    "s_waitcnt lgkmcnt(8)\n"
    "buffer_load_dwordx4 v[140:143], v240, s[96:99], 0 offen offset:1024 sc1\n"
    "v_mfma_f32_16x16x32_bf16 v[112:115], a[228:231], a[172:175], v[112:115]\n"
    "v_mfma_f32_16x16x32_bf16 v[112:115], a[224:227], a[140:143], v[112:115]\n"
    "s_waitcnt lgkmcnt(6)\n"
    "s_add_u32 s63, s70, 0\n"
    "v_mfma_f32_16x16x32_bf16 v[144:147], a[224:227], a[144:147], v[144:147]\n"
    "buffer_store_dwordx4 v[4:7], v243, s[40:43], s63 offen nt\n"
    "buffer_load_dwordx4 a[44:47], v240, s[96:99], 0 offen offset:2048 sc1\n"
    "v_mfma_f32_16x16x32_bf16 v[144:147], a[228:231], a[176:179], v[144:147]\n"
    "s_waitcnt lgkmcnt(4)\n"
    "v_mfma_f32_16x16x32_bf16 v[176:179], a[228:231], a[180:183], v[176:179]\n"
    "v_mfma_f32_16x16x32_bf16 v[176:179], a[224:227], a[148:151], v[176:179]\n"
    "s_waitcnt lgkmcnt(2)\n"
    "v_mfma_f32_16x16x32_bf16 v[208:211], a[224:227], a[152:155], v[208:211]\n"
    "buffer_load_dwordx4 v[104:107], v240, s[96:99], 0 offen offset:3072 sc1\n"
    "s_addk_i32 s96, 4096\n"
    "v_mfma_f32_16x16x32_bf16 v[208:211], a[228:231], a[184:187], v[208:211]\n"
    "s_waitcnt lgkmcnt(0)\n"
    "v_mfma_f32_16x16x32_bf16 a[240:243], a[228:231], a[188:191], a[240:243]\n"
    "v_mfma_f32_16x16x32_bf16 a[240:243], a[224:227], a[156:159], a[240:243]\n"
    "s_waitcnt vmcnt(22)\n"
    "v_mfma_f32_16x16x32_bf16 a[244:247], a[232:235], a[156:159], a[244:247]\n"
    "buffer_load_dwordx4 v[100:103], v240, s[96:99], 0 offen offset:0 sc1\n"
    "v_mfma_f32_16x16x32_bf16 a[244:247], a[236:239], a[188:191], a[244:247]\n"
    "s_add_u32 s63, s70, 16384\n"
    "v_mfma_f32_16x16x32_bf16 v[212:215], a[236:239], a[184:187], v[212:215]\n"
    "buffer_store_dwordx4 v[12:15], v243, s[40:43], s63 offen nt\n"
    "v_mfma_f32_16x16x32_bf16 v[212:215], a[232:235], a[152:155], v[212:215]\n"
    "v_mfma_f32_16x16x32_bf16 v[180:183], a[232:235], a[148:151], v[180:183]\n"
    "buffer_load_dwordx4 a[8:11], v240, s[96:99], 0 offen offset:1024 sc1\n"
    "v_mfma_f32_16x16x32_bf16 v[180:183], a[236:239], a[180:183], v[180:183]\n"
    "v_mfma_f32_16x16x32_bf16 v[148:151], a[236:239], a[176:179], v[148:151]\n"
    "v_mfma_f32_16x16x32_bf16 v[148:151], a[232:235], a[144:147], v[148:151]\n"
    "v_mfma_f32_16x16x32_bf16 v[116:119], a[232:235], a[140:143], v[116:119]\n"
    "buffer_load_dwordx4 v[64:67], v240, s[96:99], 0 offen offset:2048 sc1\n"
    "v_mfma_f32_16x16x32_bf16 v[116:119], a[236:239], a[172:175], v[116:119]\n"
    "v_mfma_f32_16x16x32_bf16 v[84:87], a[236:239], a[168:171], v[84:87]\n"
    "v_mfma_f32_16x16x32_bf16 v[84:87], a[232:235], a[136:139], v[84:87]\n"
    "v_mfma_f32_16x16x32_bf16 v[52:55], a[232:235], a[132:135], v[52:55]\n"
    "buffer_load_dwordx4 v[108:111], v240, s[96:99], 0 offen offset:3072 sc1\n"
    "s_addk_i32 s96, 4096\n"
    "v_mfma_f32_16x16x32_bf16 v[52:55], a[236:239], a[164:167], v[52:55]\n"
    "v_mfma_f32_16x16x32_bf16 v[20:23], a[236:239], a[160:163], v[20:23]\n"
    "v_mfma_f32_16x16x32_bf16 v[20:23], a[232:235], a[128:131], v[20:23]\n"
    "s_waitcnt vmcnt(23)\n"
    "v_mfma_f32_16x16x32_bf16 v[24:27], a[192:195], a[128:131], v[24:27]\n"
    "v_mfma_f32_16x16x32_bf16 v[24:27], a[196:199], a[160:163], v[24:27]\n"
    "v_mfma_f32_16x16x32_bf16 v[56:59], a[196:199], a[164:167], v[56:59]\n"
    "v_mfma_f32_16x16x32_bf16 v[56:59], a[192:195], a[132:135], v[56:59]\n"
    "v_mfma_f32_16x16x32_bf16 v[88:91], a[192:195], a[136:139], v[88:91]\n"
    "v_mfma_f32_16x16x32_bf16 v[88:91], a[196:199], a[168:171], v[88:91]\n"
    "v_mfma_f32_16x16x32_bf16 v[120:123], a[196:199], a[172:175], v[120:123]\n"
    "v_mfma_f32_16x16x32_bf16 v[120:123], a[192:195], a[140:143], v[120:123]\n"
    "v_mfma_f32_16x16x32_bf16 v[152:155], a[192:195], a[144:147], v[152:155]\n"
    "v_mfma_f32_16x16x32_bf16 v[152:155], a[196:199], a[176:179], v[152:155]\n"
    "v_mfma_f32_16x16x32_bf16 v[184:187], a[196:199], a[180:183], v[184:187]\n"
    "v_mfma_f32_16x16x32_bf16 v[184:187], a[192:195], a[148:151], v[184:187]\n"
    "v_mfma_f32_16x16x32_bf16 v[216:219], a[192:195], a[152:155], v[216:219]\n"
    "v_mfma_f32_16x16x32_bf16 v[216:219], a[196:199], a[184:187], v[216:219]\n"
    "v_mfma_f32_16x16x32_bf16 a[248:251], a[196:199], a[188:191], a[248:251]\n"
    "v_mfma_f32_16x16x32_bf16 a[248:251], a[192:195], a[156:159], a[248:251]\n"
    "s_waitcnt vmcnt(19)\n"
    "v_mfma_f32_16x16x32_bf16 a[252:255], a[200:203], a[156:159], a[252:255]\n"
    "v_mfma_f32_16x16x32_bf16 a[252:255], a[204:207], a[188:191], a[252:255]\n"
    "v_mfma_f32_16x16x32_bf16 v[220:223], a[204:207], a[184:187], v[220:223]\n"
    "v_add_u32 v241, s57, v240\n"
    "v_mfma_f32_16x16x32_bf16 v[220:223], a[200:203], a[152:155], v[220:223]\n"
    "ds_read_b128 a[156:159], v241 offset:0\n"
    "v_mfma_f32_16x16x32_bf16 v[188:191], a[200:203], a[148:151], v[188:191]\n"
    "ds_read_b128 a[188:191], v241 offset:1024\n"
    "v_mfma_f32_16x16x32_bf16 v[188:191], a[204:207], a[180:183], v[188:191]\n"
    "ds_read_b128 a[184:187], v241 offset:16384\n"
    "v_mfma_f32_16x16x32_bf16 v[156:159], a[204:207], a[176:179], v[156:159]\n"
    "ds_read_b128 a[152:155], v241 offset:17408\n"
    "v_mfma_f32_16x16x32_bf16 v[156:159], a[200:203], a[144:147], v[156:159]\n"
    "ds_read_b128 a[148:151], v241 offset:32768\n"
    "v_mfma_f32_16x16x32_bf16 v[124:127], a[200:203], a[140:143], v[124:127]\n"
    "ds_read_b128 a[180:183], v241 offset:33792\n"
    "v_mfma_f32_16x16x32_bf16 v[124:127], a[204:207], a[172:175], v[124:127]\n"
    "ds_read_b128 a[176:179], v241 offset:49152\n"
    "v_mfma_f32_16x16x32_bf16 v[92:95], a[204:207], a[168:171], v[92:95]\n"
    "ds_read_b128 a[144:147], v241 offset:50176\n"
    "v_mfma_f32_16x16x32_bf16 v[92:95], a[200:203], a[136:139], v[92:95]\n"
    "v_mfma_f32_16x16x32_bf16 v[60:63], a[200:203], a[132:135], v[60:63]\n"
    "v_mfma_f32_16x16x32_bf16 v[60:63], a[204:207], a[164:167], v[60:63]\n"
    "v_mfma_f32_16x16x32_bf16 v[28:31], a[204:207], a[160:163], v[28:31]\n"
    "v_mfma_f32_16x16x32_bf16 v[28:31], a[200:203], a[128:131], v[28:31]\n"
    "s_waitcnt lgkmcnt(7)\n"
    "v_mfma_f32_16x16x32_bf16 v[16:19], a[28:31], a[156:159], v[16:19]\n"
    "s_waitcnt lgkmcnt(6)\n"
    "v_mfma_f32_16x16x32_bf16 v[16:19], v[224:227], a[188:191], v[16:19]\n"
    "s_waitcnt lgkmcnt(5)\n"
    "ds_read_b128 a[140:143], v241 offset:2048\n"
    "v_mfma_f32_16x16x32_bf16 v[16:19], v[200:203], a[184:187], v[16:19]\n"
    "s_waitcnt lgkmcnt(5)\n"
    "v_mfma_f32_16x16x32_bf16 v[16:19], v[236:239], a[152:155], v[16:19]\n"
    "s_waitcnt vmcnt(18) lgkmcnt(4)\n"
    "v_mfma_f32_16x16x32_bf16 v[16:19], v[164:167], a[148:151], v[16:19]\n"
    "s_waitcnt vmcnt(16) lgkmcnt(3)\n"
    "ds_read_b128 a[172:175], v241 offset:3072\n"
    "v_mfma_f32_16x16x32_bf16 v[16:19], a[16:19], a[180:183], v[16:19]\n"
    "s_waitcnt vmcnt(9) lgkmcnt(3)\n"
    "v_mfma_f32_16x16x32_bf16 v[16:19], v[96:99], a[176:179], v[16:19]\n"
    "s_waitcnt vmcnt(8) lgkmcnt(2)\n"
    "v_mfma_f32_16x16x32_bf16 v[16:19], v[140:143], a[144:147], v[16:19]\n"
    "ds_read_b128 a[168:171], v241 offset:18432\n"
    "v_mfma_f32_16x16x32_bf16 v[20:23], a[24:27], a[156:159], v[20:23]\n"
    "v_mfma_f32_16x16x32_bf16 v[20:23], v[192:195], a[188:191], v[20:23]\n"
    "v_accvgpr_read_b32 v245, a64\n"
    "v_accvgpr_read_b32 v247, a65\n"
    "v_mfma_f32_16x16x32_bf16 v[20:23], a[20:23], a[184:187], v[20:23]\n"
    "ds_read_b128 a[136:139], v241 offset:19456\n"
    "v_lshlrev_b32 v244, 16, v245\n"
    "v_mfma_f32_16x16x32_bf16 v[20:23], v[160:163], a[152:155], v[20:23]\n"
    "v_lshlrev_b32 v246, 16, v247\n"
    "v_and_b32 v245, s58, v245\n"
    "v_mfma_f32_16x16x32_bf16 v[20:23], v[128:131], a[148:151], v[20:23]\n"
    "v_and_b32 v247, s58, v247\n"
    "v_mul_f32 v244, v244, v16\n"
    "v_mfma_f32_16x16x32_bf16 v[20:23], v[172:175], a[180:183], v[20:23]\n"
    "s_waitcnt vmcnt(6)\n"
    "ds_read_b128 a[132:135], v241 offset:34816\n"
    "v_mul_f32 v246, v246, v18\n"
    "v_mfma_f32_16x16x32_bf16 v[20:23], a[44:47], a[176:179], v[20:23]\n"
    "s_waitcnt vmcnt(5)\n"
    "v_mul_f32 v245, v245, v17\n"
    "v_mul_f32 v247, v247, v19\n"
    "v_mfma_f32_16x16x32_bf16 v[20:23], v[104:107], a[144:147], v[20:23]\n"
    "v_cvt_pk_bf16_f32 v16, v244, v245\n"
    "v_cvt_pk_bf16_f32 v17, v246, v247\n"
    "v_mfma_f32_16x16x32_bf16 v[24:27], a[60:63], a[156:159], v[24:27]\n"
    "ds_read_b128 a[164:167], v241 offset:35840\n"
    "v_mfma_f32_16x16x32_bf16 v[24:27], v[232:235], a[188:191], v[24:27]\n"
    "v_accvgpr_read_b32 v245, a66\n"
    "v_accvgpr_read_b32 v247, a67\n"
    "v_mfma_f32_16x16x32_bf16 v[24:27], v[196:199], a[184:187], v[24:27]\n"
    "v_lshlrev_b32 v244, 16, v245\n"
    "v_lshlrev_b32 v246, 16, v247\n"
    "v_mfma_f32_16x16x32_bf16 v[24:27], a[52:55], a[152:155], v[24:27]\n"
    "ds_read_b128 a[160:163], v241 offset:51200\n"
    "v_and_b32 v245, s58, v245\n"
    "v_mfma_f32_16x16x32_bf16 v[24:27], a[48:51], a[148:151], v[24:27]\n"
    "v_and_b32 v247, s58, v247\n"
    "v_mul_f32 v244, v244, v20\n"
    "v_mfma_f32_16x16x32_bf16 v[24:27], v[136:139], a[180:183], v[24:27]\n"
    "s_waitcnt vmcnt(4)\n"
    "v_mul_f32 v246, v246, v22\n"
    "v_mul_f32 v245, v245, v21\n"
    "v_mfma_f32_16x16x32_bf16 v[24:27], v[100:103], a[176:179], v[24:27]\n"
    "s_waitcnt vmcnt(2)\n"
    "ds_read_b128 a[128:131], v241 offset:52224\n"
    "v_mul_f32 v247, v247, v23\n"
    "v_mfma_f32_16x16x32_bf16 v[24:27], a[8:11], a[144:147], v[24:27]\n"
    "v_cvt_pk_bf16_f32 v18, v244, v245\n"
    "v_cvt_pk_bf16_f32 v19, v246, v247\n"
    "v_mfma_f32_16x16x32_bf16 v[28:31], v[228:231], a[156:159], v[28:31]\n"
    "ds_write_b128 v248, v[16:19] offset:0\n"
    "v_mfma_f32_16x16x32_bf16 v[28:31], a[56:59], a[188:191], v[28:31]\n"
    "v_accvgpr_read_b32 v245, a96\n"
    "v_accvgpr_read_b32 v247, a97\n"
    "v_mfma_f32_16x16x32_bf16 v[28:31], v[168:171], a[184:187], v[28:31]\n"
    "v_lshlrev_b32 v244, 16, v245\n"
    "v_lshlrev_b32 v246, 16, v247\n"
    "v_mfma_f32_16x16x32_bf16 v[28:31], v[204:207], a[152:155], v[28:31]\n"
    "v_and_b32 v245, s58, v245\n"
    "v_and_b32 v247, s58, v247\n"
    "v_mfma_f32_16x16x32_bf16 v[28:31], v[132:135], a[148:151], v[28:31]\n"
    "v_mul_f32 v244, v244, v24\n"
    "v_mul_f32 v246, v246, v26\n"
    "v_mfma_f32_16x16x32_bf16 v[28:31], a[12:15], a[180:183], v[28:31]\n"
    "s_waitcnt vmcnt(1)\n"
    "v_mul_f32 v245, v245, v25\n"
    "v_mul_f32 v247, v247, v27\n"
    "v_mfma_f32_16x16x32_bf16 v[28:31], v[64:67], a[176:179], v[28:31]\n"
    "s_waitcnt vmcnt(0)\n"
    "v_cvt_pk_bf16_f32 v24, v244, v245\n"
    "v_cvt_pk_bf16_f32 v25, v246, v247\n"
    "v_mfma_f32_16x16x32_bf16 v[28:31], v[108:111], a[144:147], v[28:31]\n"
    "s_waitcnt lgkmcnt(8)\n"
    "v_mfma_f32_16x16x32_bf16 v[48:51], a[28:31], a[140:143], v[48:51]\n"
    "s_waitcnt lgkmcnt(7)\n"
    "v_mfma_f32_16x16x32_bf16 v[48:51], v[224:227], a[172:175], v[48:51]\n"
    "s_waitcnt lgkmcnt(6)\n"
    "ds_read_b128 a[156:159], v241 offset:4096\n"
    "v_accvgpr_read_b32 v245, a98\n"
    "v_mfma_f32_16x16x32_bf16 v[48:51], v[200:203], a[168:171], v[48:51]\n"
    "s_waitcnt lgkmcnt(6)\n"
    "v_accvgpr_read_b32 v247, a99\n"
    "v_lshlrev_b32 v244, 16, v245\n"
    "v_mfma_f32_16x16x32_bf16 v[48:51], v[236:239], a[136:139], v[48:51]\n"
    "s_waitcnt lgkmcnt(5)\n"
    "v_lshlrev_b32 v246, 16, v247\n"
    "v_and_b32 v245, s58, v245\n"
    "v_mfma_f32_16x16x32_bf16 v[48:51], v[164:167], a[132:135], v[48:51]\n"
    "s_waitcnt lgkmcnt(4)\n"
    "ds_read_b128 a[188:191], v241 offset:5120\n"
    "v_and_b32 v247, s58, v247\n"
    "v_mfma_f32_16x16x32_bf16 v[48:51], a[16:19], a[164:167], v[48:51]\n"
    "s_waitcnt lgkmcnt(4)\n"
    "v_mul_f32 v244, v244, v28\n"
    "v_mul_f32 v246, v246, v30\n"
    "v_mfma_f32_16x16x32_bf16 v[48:51], v[96:99], a[160:163], v[48:51]\n"
    "s_waitcnt lgkmcnt(3)\n"
    "v_mul_f32 v245, v245, v29\n"
    "v_mul_f32 v247, v247, v31\n"
    "v_mfma_f32_16x16x32_bf16 v[48:51], v[140:143], a[128:131], v[48:51]\n"
    "ds_read_b128 a[184:187], v241 offset:20480\n"
    "v_cvt_pk_bf16_f32 v26, v244, v245\n"
    "v_mfma_f32_16x16x32_bf16 v[52:55], a[24:27], a[140:143], v[52:55]\n"
    "v_cvt_pk_bf16_f32 v27, v246, v247\n"
    "v_mfma_f32_16x16x32_bf16 v[52:55], v[192:195], a[172:175], v[52:55]\n"
    "ds_write_b128 v249, v[24:27] offset:0\n"
    "v_mfma_f32_16x16x32_bf16 v[52:55], a[20:23], a[168:171], v[52:55]\n"
    "ds_read_b128 v[20:23], v242 offset:0\n"
    "v_accvgpr_read_b32 v245, a68\n"
    "v_mfma_f32_16x16x32_bf16 v[52:55], v[160:163], a[136:139], v[52:55]\n"
    "ds_read_b128 v[28:31], v242 offset:1024\n"
    "v_accvgpr_read_b32 v247, a69\n"
    "v_mfma_f32_16x16x32_bf16 v[52:55], v[128:131], a[132:135], v[52:55]\n"
    "ds_read_b128 a[152:155], v241 offset:21504\n"
    "v_lshlrev_b32 v244, 16, v245\n"
    "v_mfma_f32_16x16x32_bf16 v[52:55], v[172:175], a[164:167], v[52:55]\n"
    "ds_read_b128 a[148:151], v241 offset:36864\n"
    "v_lshlrev_b32 v246, 16, v247\n"
    "v_mfma_f32_16x16x32_bf16 v[52:55], a[44:47], a[160:163], v[52:55]\n"
    "v_and_b32 v245, s58, v245\n"
    "v_and_b32 v247, s58, v247\n"
    "v_mfma_f32_16x16x32_bf16 v[52:55], v[104:107], a[128:131], v[52:55]\n"
    "s_add_u32 s63, s70, 512\n"
    "s_waitcnt lgkmcnt(3)\n"
    "v_mul_f32 v244, v244, v48\n"
    "v_mfma_f32_16x16x32_bf16 v[56:59], a[60:63], a[140:143], v[56:59]\n"
    "buffer_store_dwordx4 v[20:23], v243, s[40:43], s63 offen sc1\n"
    "ds_read_b128 a[180:183], v241 offset:37888\n"
    "v_mul_f32 v246, v246, v50\n"
    "v_mul_f32 v245, v245, v49\n"
    "v_mfma_f32_16x16x32_bf16 v[56:59], v[232:235], a[172:175], v[56:59]\n"
    "s_add_u32 s63, s70, 16896\n"
    "s_waitcnt lgkmcnt(3)\n"
    "v_mul_f32 v247, v247, v51\n"
    "v_mfma_f32_16x16x32_bf16 v[56:59], v[196:199], a[168:171], v[56:59]\n"
    "buffer_store_dwordx4 v[28:31], v243, s[40:43], s63 offen sc1\n"
    "v_cvt_pk_bf16_f32 v48, v244, v245\n"
    "v_cvt_pk_bf16_f32 v49, v246, v247\n"
    "v_accvgpr_read_b32 v245, a70\n"
    "v_mfma_f32_16x16x32_bf16 v[56:59], a[52:55], a[136:139], v[56:59]\n"
    "ds_read_b128 a[176:179], v241 offset:53248\n"
    "v_accvgpr_read_b32 v247, a71\n"
    "v_mfma_f32_16x16x32_bf16 v[56:59], a[48:51], a[132:135], v[56:59]\n"
    "v_lshlrev_b32 v244, 16, v245\n"
    "v_lshlrev_b32 v246, 16, v247\n"
    "v_mfma_f32_16x16x32_bf16 v[56:59], v[136:139], a[164:167], v[56:59]\n"
    "v_and_b32 v245, s58, v245\n"
    "v_and_b32 v247, s58, v247\n"
    "v_mfma_f32_16x16x32_bf16 v[56:59], v[100:103], a[160:163], v[56:59]\n"
    "ds_read_b128 a[144:147], v241 offset:54272\n"
    "v_mul_f32 v244, v244, v52\n"
    "v_mfma_f32_16x16x32_bf16 v[56:59], a[8:11], a[128:131], v[56:59]\n"
    "v_mul_f32 v246, v246, v54\n"
    "v_mul_f32 v245, v245, v53\n"
    "v_mfma_f32_16x16x32_bf16 v[60:63], v[228:231], a[140:143], v[60:63]\n"
    "v_mul_f32 v247, v247, v55\n"
    "v_cvt_pk_bf16_f32 v50, v244, v245\n"
    "v_mfma_f32_16x16x32_bf16 v[60:63], a[56:59], a[172:175], v[60:63]\n"
    "v_cvt_pk_bf16_f32 v51, v246, v247\n"
    "v_mfma_f32_16x16x32_bf16 v[60:63], v[168:171], a[168:171], v[60:63]\n"
    "ds_write_b128 v248, v[48:51] offset:2048\n"
    "v_mfma_f32_16x16x32_bf16 v[60:63], v[204:207], a[136:139], v[60:63]\n"
    "v_accvgpr_read_b32 v245, a100\n"
    "v_accvgpr_read_b32 v247, a101\n"
    "v_mfma_f32_16x16x32_bf16 v[60:63], v[132:135], a[132:135], v[60:63]\n"
    "v_lshlrev_b32 v244, 16, v245\n"
    "v_lshlrev_b32 v246, 16, v247\n"
    "v_mfma_f32_16x16x32_bf16 v[60:63], a[12:15], a[164:167], v[60:63]\n"
    "v_and_b32 v245, s58, v245\n"
    "v_and_b32 v247, s58, v247\n"
    "v_mfma_f32_16x16x32_bf16 v[60:63], v[64:67], a[160:163], v[60:63]\n"
    "v_mul_f32 v244, v244, v56\n"
    "v_mul_f32 v246, v246, v58\n"
    "v_mfma_f32_16x16x32_bf16 v[60:63], v[108:111], a[128:131], v[60:63]\n"
    "v_mul_f32 v245, v245, v57\n"
    "v_mul_f32 v247, v247, v59\n"
    "v_mfma_f32_16x16x32_bf16 v[80:83], a[28:31], a[156:159], v[80:83]\n"
    "v_cvt_pk_bf16_f32 v56, v244, v245\n"
    "v_cvt_pk_bf16_f32 v57, v246, v247\n"
    "v_mfma_f32_16x16x32_bf16 v[80:83], v[224:227], a[188:191], v[80:83]\n"
    "ds_read_b128 a[140:143], v241 offset:14336\n"
    "v_accvgpr_read_b32 v245, a102\n"
    "v_mfma_f32_16x16x32_bf16 v[80:83], v[200:203], a[184:187], v[80:83]\n"
    "s_waitcnt lgkmcnt(6)\n"
    "v_accvgpr_read_b32 v247, a103\n"
    "v_lshlrev_b32 v244, 16, v245\n"
    "v_mfma_f32_16x16x32_bf16 v[80:83], v[236:239], a[152:155], v[80:83]\n"
    "s_waitcnt lgkmcnt(5)\n"
    "v_lshlrev_b32 v246, 16, v247\n"
    "v_and_b32 v245, s58, v245\n"
    "v_mfma_f32_16x16x32_bf16 v[80:83], v[164:167], a[148:151], v[80:83]\n"
    "s_waitcnt lgkmcnt(4)\n"
    "ds_read_b128 a[172:175], v241 offset:15360\n"
    "v_and_b32 v247, s58, v247\n"
    "v_mfma_f32_16x16x32_bf16 v[80:83], a[16:19], a[180:183], v[80:83]\n"
    "s_waitcnt lgkmcnt(4)\n"
    "v_mul_f32 v244, v244, v60\n"
    "v_mul_f32 v246, v246, v62\n"
    "v_mfma_f32_16x16x32_bf16 v[80:83], v[96:99], a[176:179], v[80:83]\n"
    "s_waitcnt lgkmcnt(3)\n"
    "v_mul_f32 v245, v245, v61\n"
    "v_mul_f32 v247, v247, v63\n"
    "v_mfma_f32_16x16x32_bf16 v[80:83], v[140:143], a[144:147], v[80:83]\n"
    "ds_read_b128 a[168:171], v241 offset:30720\n"
    "v_cvt_pk_bf16_f32 v58, v244, v245\n"
    "v_mfma_f32_16x16x32_bf16 v[84:87], a[24:27], a[156:159], v[84:87]\n"
    "v_cvt_pk_bf16_f32 v59, v246, v247\n"
    "v_mfma_f32_16x16x32_bf16 v[84:87], v[192:195], a[188:191], v[84:87]\n"
    "ds_write_b128 v249, v[56:59] offset:2048\n"
    "v_mfma_f32_16x16x32_bf16 v[84:87], a[20:23], a[184:187], v[84:87]\n"
    "ds_read_b128 v[52:55], v242 offset:2048\n"
    "v_accvgpr_read_b32 v245, a72\n"
    "v_mfma_f32_16x16x32_bf16 v[84:87], v[160:163], a[152:155], v[84:87]\n"
    "ds_read_b128 v[60:63], v242 offset:3072\n"
    "v_accvgpr_read_b32 v247, a73\n"
    "v_mfma_f32_16x16x32_bf16 v[84:87], v[128:131], a[148:151], v[84:87]\n"
    "ds_read_b128 a[136:139], v241 offset:31744\n"
    "v_lshlrev_b32 v244, 16, v245\n"
    "v_mfma_f32_16x16x32_bf16 v[84:87], v[172:175], a[180:183], v[84:87]\n"
    "ds_read_b128 a[132:135], v241 offset:47104\n"
    "v_lshlrev_b32 v246, 16, v247\n"
    "v_mfma_f32_16x16x32_bf16 v[84:87], a[44:47], a[176:179], v[84:87]\n"
    "v_and_b32 v245, s58, v245\n"
    "v_and_b32 v247, s58, v247\n"
    "v_mfma_f32_16x16x32_bf16 v[84:87], v[104:107], a[144:147], v[84:87]\n"
    "s_add_u32 s63, s70, 33280\n"
    "s_waitcnt lgkmcnt(3)\n"
    "v_mul_f32 v244, v244, v80\n"
    "v_mfma_f32_16x16x32_bf16 v[88:91], a[60:63], a[156:159], v[88:91]\n"
    "buffer_store_dwordx4 v[52:55], v243, s[40:43], s63 offen sc1\n"
    "ds_read_b128 a[164:167], v241 offset:48128\n"
    "v_mul_f32 v246, v246, v82\n"
    "v_mul_f32 v245, v245, v81\n"
    "v_mfma_f32_16x16x32_bf16 v[88:91], v[232:235], a[188:191], v[88:91]\n"
    "s_add_u32 s63, s70, 49664\n"
    "s_waitcnt lgkmcnt(3)\n"
    "v_mul_f32 v247, v247, v83\n"
    "v_mfma_f32_16x16x32_bf16 v[88:91], v[196:199], a[184:187], v[88:91]\n"
    "buffer_store_dwordx4 v[60:63], v243, s[40:43], s63 offen sc1\n"
    "v_cvt_pk_bf16_f32 v80, v244, v245\n"
    "v_cvt_pk_bf16_f32 v81, v246, v247\n"
    "v_accvgpr_read_b32 v245, a74\n"
    "v_mfma_f32_16x16x32_bf16 v[88:91], a[52:55], a[152:155], v[88:91]\n"
    "ds_read_b128 a[160:163], v241 offset:63488\n"
    "v_accvgpr_read_b32 v247, a75\n"
    "v_mfma_f32_16x16x32_bf16 v[88:91], a[48:51], a[148:151], v[88:91]\n"
    "v_lshlrev_b32 v244, 16, v245\n"
    "v_lshlrev_b32 v246, 16, v247\n"
    "v_mfma_f32_16x16x32_bf16 v[88:91], v[136:139], a[180:183], v[88:91]\n"
    "v_and_b32 v245, s58, v245\n"
    "v_and_b32 v247, s58, v247\n"
    "v_mfma_f32_16x16x32_bf16 v[88:91], v[100:103], a[176:179], v[88:91]\n"
    "ds_read_b128 a[128:131], v241 offset:64512\n"
    "v_mul_f32 v244, v244, v84\n"
    "v_mfma_f32_16x16x32_bf16 v[88:91], a[8:11], a[144:147], v[88:91]\n"
    "v_mul_f32 v246, v246, v86\n"
    "v_mul_f32 v245, v245, v85\n"
    "v_mfma_f32_16x16x32_bf16 v[92:95], v[228:231], a[156:159], v[92:95]\n"
    "v_mul_f32 v247, v247, v87\n"
    "v_cvt_pk_bf16_f32 v82, v244, v245\n"
    "v_mfma_f32_16x16x32_bf16 v[92:95], a[56:59], a[188:191], v[92:95]\n"
    "v_cvt_pk_bf16_f32 v83, v246, v247\n"
    "v_mfma_f32_16x16x32_bf16 v[92:95], v[168:171], a[184:187], v[92:95]\n"
    "ds_write_b128 v248, v[80:83] offset:0\n"
    "v_mfma_f32_16x16x32_bf16 v[92:95], v[204:207], a[152:155], v[92:95]\n"
    "v_accvgpr_read_b32 v245, a104\n"
    "v_accvgpr_read_b32 v247, a105\n"
    "v_mfma_f32_16x16x32_bf16 v[92:95], v[132:135], a[148:151], v[92:95]\n"
    "v_lshlrev_b32 v244, 16, v245\n"
    "v_lshlrev_b32 v246, 16, v247\n"
    "v_mfma_f32_16x16x32_bf16 v[92:95], a[12:15], a[180:183], v[92:95]\n"
    "v_and_b32 v245, s58, v245\n"
    "v_and_b32 v247, s58, v247\n"
    "v_mfma_f32_16x16x32_bf16 v[92:95], v[64:67], a[176:179], v[92:95]\n"
    "v_mul_f32 v244, v244, v88\n"
    "v_mul_f32 v246, v246, v90\n"
    "v_mfma_f32_16x16x32_bf16 v[92:95], v[108:111], a[144:147], v[92:95]\n"
    "v_mul_f32 v245, v245, v89\n"
    "v_mul_f32 v247, v247, v91\n"
    "v_mfma_f32_16x16x32_bf16 a[240:243], a[28:31], a[140:143], a[240:243]\n"
    "v_cvt_pk_bf16_f32 v88, v244, v245\n"
    "v_cvt_pk_bf16_f32 v89, v246, v247\n"
    "v_mfma_f32_16x16x32_bf16 a[240:243], v[224:227], a[172:175], a[240:243]\n"
    "ds_read_b128 a[156:159], v241 offset:6144\n"
    "v_accvgpr_read_b32 v245, a106\n"
    "v_mfma_f32_16x16x32_bf16 a[240:243], v[200:203], a[168:171], a[240:243]\n"
    "s_waitcnt lgkmcnt(6)\n"
    "v_accvgpr_read_b32 v247, a107\n"
    "v_lshlrev_b32 v244, 16, v245\n"
    "v_mfma_f32_16x16x32_bf16 a[240:243], v[236:239], a[136:139], a[240:243]\n"
    "s_waitcnt lgkmcnt(5)\n"
    "v_lshlrev_b32 v246, 16, v247\n"
    "v_and_b32 v245, s58, v245\n"
    "v_mfma_f32_16x16x32_bf16 a[240:243], v[164:167], a[132:135], a[240:243]\n"
    "s_waitcnt lgkmcnt(4)\n"
    "ds_read_b128 a[188:191], v241 offset:7168\n"
    "v_and_b32 v247, s58, v247\n"
    "v_mfma_f32_16x16x32_bf16 a[240:243], a[16:19], a[164:167], a[240:243]\n"
    "s_waitcnt lgkmcnt(4)\n"
    "v_mul_f32 v244, v244, v92\n"
    "v_mul_f32 v246, v246, v94\n"
    "v_mfma_f32_16x16x32_bf16 a[240:243], v[96:99], a[160:163], a[240:243]\n"
    "s_waitcnt lgkmcnt(3)\n"
    "v_mul_f32 v245, v245, v93\n"
    "v_mul_f32 v247, v247, v95\n"
    "v_mfma_f32_16x16x32_bf16 a[240:243], v[140:143], a[128:131], a[240:243]\n"
    "ds_read_b128 a[184:187], v241 offset:22528\n"
    "v_cvt_pk_bf16_f32 v90, v244, v245\n"
    "v_mfma_f32_16x16x32_bf16 a[244:247], a[24:27], a[140:143], a[244:247]\n"
    "v_cvt_pk_bf16_f32 v91, v246, v247\n"
    "v_mfma_f32_16x16x32_bf16 a[244:247], v[192:195], a[172:175], a[244:247]\n"
    "ds_write_b128 v249, v[88:91] offset:0\n"
    "v_mfma_f32_16x16x32_bf16 a[244:247], a[20:23], a[168:171], a[244:247]\n"
    "ds_read_b128 v[84:87], v242 offset:0\n"
    "v_accvgpr_read_b32 v245, a92\n"
    "v_mfma_f32_16x16x32_bf16 a[244:247], v[160:163], a[136:139], a[244:247]\n"
    "ds_read_b128 v[92:95], v242 offset:1024\n"
    "v_accvgpr_read_b32 v247, a93\n"
    "v_mfma_f32_16x16x32_bf16 a[244:247], v[128:131], a[132:135], a[244:247]\n"
    "ds_read_b128 a[152:155], v241 offset:23552\n"
    "v_lshlrev_b32 v244, 16, v245\n"
    "v_mfma_f32_16x16x32_bf16 a[244:247], v[172:175], a[164:167], a[244:247]\n"
    "ds_read_b128 a[148:151], v241 offset:38912\n"
    "v_lshlrev_b32 v246, 16, v247\n"
    "v_mfma_f32_16x16x32_bf16 a[244:247], a[44:47], a[160:163], a[244:247]\n"
    "v_and_b32 v245, s58, v245\n"
    "v_and_b32 v247, s58, v247\n"
    "v_mfma_f32_16x16x32_bf16 a[244:247], v[104:107], a[128:131], a[244:247]\n"
    "s_add_u32 s63, s70, 66048\n"
    "s_waitcnt lgkmcnt(3)\n"
    "v_accvgpr_read_b32 v254, a240\n"
    "v_mfma_f32_16x16x32_bf16 a[248:251], a[60:63], a[140:143], a[248:251]\n"
    "buffer_store_dwordx4 v[84:87], v243, s[40:43], s63 offen sc1\n"
    "ds_read_b128 a[180:183], v241 offset:39936\n"
    "v_accvgpr_read_b32 v255, a242\n"
    "v_mul_f32 v244, v244, v254\n"
    "v_mfma_f32_16x16x32_bf16 a[248:251], v[232:235], a[172:175], a[248:251]\n"
    "s_add_u32 s63, s70, 82432\n"
    "s_waitcnt lgkmcnt(3)\n"
    "v_mul_f32 v246, v246, v255\n"
    "v_mfma_f32_16x16x32_bf16 a[248:251], v[196:199], a[168:171], a[248:251]\n"
    "buffer_store_dwordx4 v[92:95], v243, s[40:43], s63 offen sc1\n"
    "v_accvgpr_read_b32 v254, a241\n"
    "v_accvgpr_read_b32 v255, a243\n"
    "v_mul_f32 v245, v245, v254\n"
    "v_mfma_f32_16x16x32_bf16 a[248:251], a[52:55], a[136:139], a[248:251]\n"
    "ds_read_b128 a[176:179], v241 offset:55296\n"
    "v_mul_f32 v247, v247, v255\n"
    "v_mfma_f32_16x16x32_bf16 a[248:251], a[48:51], a[132:135], a[248:251]\n"
    "v_cvt_pk_bf16_f32 v16, v244, v245\n"
    "v_cvt_pk_bf16_f32 v17, v246, v247\n"
    "v_mfma_f32_16x16x32_bf16 a[248:251], v[136:139], a[164:167], a[248:251]\n"
    "v_accvgpr_read_b32 v245, a94\n"
    "v_accvgpr_read_b32 v247, a95\n"
    "v_mfma_f32_16x16x32_bf16 a[248:251], v[100:103], a[160:163], a[248:251]\n"
    "ds_read_b128 a[144:147], v241 offset:56320\n"
    "v_lshlrev_b32 v244, 16, v245\n"
    "v_mfma_f32_16x16x32_bf16 a[248:251], a[8:11], a[128:131], a[248:251]\n"
    "v_lshlrev_b32 v246, 16, v247\n"
    "v_and_b32 v245, s58, v245\n"
    "v_mfma_f32_16x16x32_bf16 a[252:255], v[228:231], a[140:143], a[252:255]\n"
    "v_and_b32 v247, s58, v247\n"
    "v_accvgpr_read_b32 v254, a244\n"
    "v_mfma_f32_16x16x32_bf16 a[252:255], a[56:59], a[172:175], a[252:255]\n"
    "v_accvgpr_read_b32 v255, a246\n"
    "v_mul_f32 v244, v244, v254\n"
    "v_mfma_f32_16x16x32_bf16 a[252:255], v[168:171], a[168:171], a[252:255]\n"
    "v_mul_f32 v246, v246, v255\n"
    "v_accvgpr_read_b32 v254, a245\n"
    "v_mfma_f32_16x16x32_bf16 a[252:255], v[204:207], a[136:139], a[252:255]\n"
    "v_accvgpr_read_b32 v255, a247\n"
    "v_mul_f32 v245, v245, v254\n"
    "v_mfma_f32_16x16x32_bf16 a[252:255], v[132:135], a[132:135], a[252:255]\n"
    "v_mul_f32 v247, v247, v255\n"
    "v_cvt_pk_bf16_f32 v18, v244, v245\n"
    "v_mfma_f32_16x16x32_bf16 a[252:255], a[12:15], a[164:167], a[252:255]\n"
    "v_cvt_pk_bf16_f32 v19, v246, v247\n"
    "v_mfma_f32_16x16x32_bf16 a[252:255], v[64:67], a[160:163], a[252:255]\n"
    "ds_write_b128 v248, v[16:19] offset:2048\n"
    "v_mfma_f32_16x16x32_bf16 a[252:255], v[108:111], a[128:131], a[252:255]\n"
    "v_accvgpr_read_b32 v245, a124\n"
    "v_accvgpr_read_b32 v247, a125\n"
    "v_mfma_f32_16x16x32_bf16 v[112:115], a[28:31], a[156:159], v[112:115]\n"
    "v_lshlrev_b32 v244, 16, v245\n"
    "v_lshlrev_b32 v246, 16, v247\n"
    "v_mfma_f32_16x16x32_bf16 v[112:115], v[224:227], a[188:191], v[112:115]\n"
    "ds_read_b128 a[140:143], v241 offset:8192\n"
    "v_and_b32 v245, s58, v245\n"
    "v_mfma_f32_16x16x32_bf16 v[112:115], v[200:203], a[184:187], v[112:115]\n"
    "s_waitcnt lgkmcnt(6)\n"
    "v_and_b32 v247, s58, v247\n"
    "v_accvgpr_read_b32 v254, a248\n"
    "v_mfma_f32_16x16x32_bf16 v[112:115], v[236:239], a[152:155], v[112:115]\n"
    "s_waitcnt lgkmcnt(5)\n"
    "v_accvgpr_read_b32 v255, a250\n"
    "v_mul_f32 v244, v244, v254\n"
    "v_mfma_f32_16x16x32_bf16 v[112:115], v[164:167], a[148:151], v[112:115]\n"
    "s_waitcnt lgkmcnt(4)\n"
    "ds_read_b128 a[172:175], v241 offset:9216\n"
    "v_mul_f32 v246, v246, v255\n"
    "v_mfma_f32_16x16x32_bf16 v[112:115], a[16:19], a[180:183], v[112:115]\n"
    "s_waitcnt lgkmcnt(4)\n"
    "v_accvgpr_read_b32 v254, a249\n"
    "v_accvgpr_read_b32 v255, a251\n"
    "v_mfma_f32_16x16x32_bf16 v[112:115], v[96:99], a[176:179], v[112:115]\n"
    "s_waitcnt lgkmcnt(3)\n"
    "v_mul_f32 v245, v245, v254\n"
    "v_mul_f32 v247, v247, v255\n"
    "v_mfma_f32_16x16x32_bf16 v[112:115], v[140:143], a[144:147], v[112:115]\n"
    "ds_read_b128 a[168:171], v241 offset:24576\n"
    "v_cvt_pk_bf16_f32 v24, v244, v245\n"
    "v_mfma_f32_16x16x32_bf16 v[116:119], a[24:27], a[156:159], v[116:119]\n"
    "v_cvt_pk_bf16_f32 v25, v246, v247\n"
    "v_accvgpr_read_b32 v245, a126\n"
    "v_mfma_f32_16x16x32_bf16 v[116:119], v[192:195], a[188:191], v[116:119]\n"
    "v_accvgpr_read_b32 v247, a127\n"
    "v_lshlrev_b32 v244, 16, v245\n"
    "v_mfma_f32_16x16x32_bf16 v[116:119], a[20:23], a[184:187], v[116:119]\n"
    "ds_read_b128 a[136:139], v241 offset:25600\n"
    "v_lshlrev_b32 v246, 16, v247\n"
    "v_mfma_f32_16x16x32_bf16 v[116:119], v[160:163], a[152:155], v[116:119]\n"
    "v_and_b32 v245, s58, v245\n"
    "v_and_b32 v247, s58, v247\n"
    "v_mfma_f32_16x16x32_bf16 v[116:119], v[128:131], a[148:151], v[116:119]\n"
    "v_accvgpr_read_b32 v254, a252\n"
    "v_accvgpr_read_b32 v255, a254\n"
    "v_mfma_f32_16x16x32_bf16 v[116:119], v[172:175], a[180:183], v[116:119]\n"
    "ds_read_b128 a[132:135], v241 offset:40960\n"
    "v_mul_f32 v244, v244, v254\n"
    "v_mfma_f32_16x16x32_bf16 v[116:119], a[44:47], a[176:179], v[116:119]\n"
    "v_mul_f32 v246, v246, v255\n"
    "v_accvgpr_read_b32 v254, a253\n"
    "v_mfma_f32_16x16x32_bf16 v[116:119], v[104:107], a[144:147], v[116:119]\n"
    "v_accvgpr_read_b32 v255, a255\n"
    "v_mul_f32 v245, v245, v254\n"
    "v_mfma_f32_16x16x32_bf16 v[120:123], a[60:63], a[156:159], v[120:123]\n"
    "ds_read_b128 a[164:167], v241 offset:41984\n"
    "v_mul_f32 v247, v247, v255\n"
    "v_mfma_f32_16x16x32_bf16 v[120:123], v[232:235], a[188:191], v[120:123]\n"
    "v_cvt_pk_bf16_f32 v26, v244, v245\n"
    "v_cvt_pk_bf16_f32 v27, v246, v247\n"
    "v_mfma_f32_16x16x32_bf16 v[120:123], v[196:199], a[184:187], v[120:123]\n"
    "ds_write_b128 v249, v[24:27] offset:2048\n"
    "v_mfma_f32_16x16x32_bf16 v[120:123], a[52:55], a[152:155], v[120:123]\n"
    "ds_read_b128 a[244:247], v242 offset:2048\n"
    "v_accvgpr_read_b32 v245, a76\n"
    "v_mfma_f32_16x16x32_bf16 v[120:123], a[48:51], a[148:151], v[120:123]\n"
    "ds_read_b128 a[252:255], v242 offset:3072\n"
    "v_accvgpr_read_b32 v247, a77\n"
    "v_mfma_f32_16x16x32_bf16 v[120:123], v[136:139], a[180:183], v[120:123]\n"
    "ds_read_b128 a[160:163], v241 offset:57344\n"
    "v_lshlrev_b32 v244, 16, v245\n"
    "v_mfma_f32_16x16x32_bf16 v[120:123], v[100:103], a[176:179], v[120:123]\n"
    "ds_read_b128 a[128:131], v241 offset:58368\n"
    "v_lshlrev_b32 v246, 16, v247\n"
    "v_mfma_f32_16x16x32_bf16 v[120:123], a[8:11], a[144:147], v[120:123]\n"
    "v_and_b32 v245, s58, v245\n"
    "v_and_b32 v247, s58, v247\n"
    "v_mfma_f32_16x16x32_bf16 v[124:127], v[228:231], a[156:159], v[124:127]\n"
    "v_mul_f32 v244, v244, v112\n"
    "v_mul_f32 v246, v246, v114\n"
    "v_mfma_f32_16x16x32_bf16 v[124:127], a[56:59], a[188:191], v[124:127]\n"
    "s_add_u32 s63, s70, 229888\n"
    "s_waitcnt lgkmcnt(3)\n"
    "v_mul_f32 v245, v245, v113\n"
    "v_mfma_f32_16x16x32_bf16 v[124:127], v[168:171], a[184:187], v[124:127]\n"
    "buffer_store_dwordx4 a[244:247], v243, s[40:43], s63 offen sc1\n"
    "v_mul_f32 v247, v247, v115\n"
    "v_cvt_pk_bf16_f32 v112, v244, v245\n"
    "v_cvt_pk_bf16_f32 v113, v246, v247\n"
    "v_mfma_f32_16x16x32_bf16 v[124:127], v[204:207], a[152:155], v[124:127]\n"
    "v_accvgpr_read_b32 v245, a78\n"
    "v_accvgpr_read_b32 v247, a79\n"
    "v_mfma_f32_16x16x32_bf16 v[124:127], v[132:135], a[148:151], v[124:127]\n"
    "v_lshlrev_b32 v244, 16, v245\n"
    "v_lshlrev_b32 v246, 16, v247\n"
    "v_mfma_f32_16x16x32_bf16 v[124:127], a[12:15], a[180:183], v[124:127]\n"
    "v_and_b32 v245, s58, v245\n"
    "v_and_b32 v247, s58, v247\n"
    "v_mfma_f32_16x16x32_bf16 v[124:127], v[64:67], a[176:179], v[124:127]\n"
    "v_mul_f32 v244, v244, v116\n"
    "v_mul_f32 v246, v246, v118\n"
    "v_mfma_f32_16x16x32_bf16 v[124:127], v[108:111], a[144:147], v[124:127]\n"
    "s_add_u32 s63, s70, 246272\n"
    "s_waitcnt lgkmcnt(2)\n"
    "v_mul_f32 v245, v245, v117\n"
    "v_mfma_f32_16x16x32_bf16 v[144:147], a[28:31], a[140:143], v[144:147]\n"
    "buffer_store_dwordx4 a[252:255], v243, s[40:43], s63 offen sc1\n"
    "v_mul_f32 v247, v247, v119\n"
    "v_cvt_pk_bf16_f32 v114, v244, v245\n"
    "v_cvt_pk_bf16_f32 v115, v246, v247\n"
    "v_mfma_f32_16x16x32_bf16 v[144:147], v[224:227], a[172:175], v[144:147]\n"
    "ds_write_b128 v248, v[112:115] offset:2048\n"
    "v_mfma_f32_16x16x32_bf16 v[144:147], v[200:203], a[168:171], v[144:147]\n"
    "ds_read_b128 a[156:159], v241 offset:10240\n"
    "v_accvgpr_read_b32 v245, a108\n"
    "v_mfma_f32_16x16x32_bf16 v[144:147], v[236:239], a[136:139], v[144:147]\n"
    "v_accvgpr_read_b32 v247, a109\n"
    "v_lshlrev_b32 v244, 16, v245\n"
    "v_mfma_f32_16x16x32_bf16 v[144:147], v[164:167], a[132:135], v[144:147]\n"
    "ds_read_b128 a[188:191], v241 offset:11264\n"
    "v_lshlrev_b32 v246, 16, v247\n"
    "v_mfma_f32_16x16x32_bf16 v[144:147], a[16:19], a[164:167], v[144:147]\n"
    "s_waitcnt lgkmcnt(4)\n"
    "v_and_b32 v245, s58, v245\n"
    "v_and_b32 v247, s58, v247\n"
    "v_mfma_f32_16x16x32_bf16 v[144:147], v[96:99], a[160:163], v[144:147]\n"
    "s_waitcnt lgkmcnt(3)\n"
    "v_mul_f32 v244, v244, v120\n"
    "v_mul_f32 v246, v246, v122\n"
    "v_mfma_f32_16x16x32_bf16 v[144:147], v[140:143], a[128:131], v[144:147]\n"
    "ds_read_b128 a[184:187], v241 offset:26624\n"
    "v_mul_f32 v245, v245, v121\n"
    "v_mfma_f32_16x16x32_bf16 v[148:151], a[24:27], a[140:143], v[148:151]\n"
    "v_mul_f32 v247, v247, v123\n"
    "v_cvt_pk_bf16_f32 v120, v244, v245\n"
    "v_mfma_f32_16x16x32_bf16 v[148:151], v[192:195], a[172:175], v[148:151]\n"
    "v_cvt_pk_bf16_f32 v121, v246, v247\n"
    "v_accvgpr_read_b32 v245, a110\n"
    "v_mfma_f32_16x16x32_bf16 v[148:151], a[20:23], a[168:171], v[148:151]\n"
    "ds_read_b128 a[152:155], v241 offset:27648\n"
    "v_accvgpr_read_b32 v247, a111\n"
    "v_mfma_f32_16x16x32_bf16 v[148:151], v[160:163], a[136:139], v[148:151]\n"
    "v_lshlrev_b32 v244, 16, v245\n"
    "v_lshlrev_b32 v246, 16, v247\n"
    "v_mfma_f32_16x16x32_bf16 v[148:151], v[128:131], a[132:135], v[148:151]\n"
    "v_and_b32 v245, s58, v245\n"
    "v_and_b32 v247, s58, v247\n"
    "v_mfma_f32_16x16x32_bf16 v[148:151], v[172:175], a[164:167], v[148:151]\n"
    "ds_read_b128 a[148:151], v241 offset:43008\n"
    "v_mul_f32 v244, v244, v124\n"
    "v_mfma_f32_16x16x32_bf16 v[148:151], a[44:47], a[160:163], v[148:151]\n"
    "v_mul_f32 v246, v246, v126\n"
    "v_mul_f32 v245, v245, v125\n"
    "v_mfma_f32_16x16x32_bf16 v[148:151], v[104:107], a[128:131], v[148:151]\n"
    "v_mul_f32 v247, v247, v127\n"
    "v_cvt_pk_bf16_f32 v122, v244, v245\n"
    "v_mfma_f32_16x16x32_bf16 v[152:155], a[60:63], a[140:143], v[152:155]\n"
    "ds_read_b128 a[180:183], v241 offset:44032\n"
    "v_cvt_pk_bf16_f32 v123, v246, v247\n"
    "v_mfma_f32_16x16x32_bf16 v[152:155], v[232:235], a[172:175], v[152:155]\n"
    "ds_write_b128 v249, v[120:123] offset:2048\n"
    "v_mfma_f32_16x16x32_bf16 v[152:155], v[196:199], a[168:171], v[152:155]\n"
    "ds_read_b128 v[116:119], v242 offset:2048\n"
    "v_accvgpr_read_b32 v245, a80\n"
    "v_mfma_f32_16x16x32_bf16 v[152:155], a[52:55], a[136:139], v[152:155]\n"
    "ds_read_b128 v[124:127], v242 offset:3072\n"
    "v_accvgpr_read_b32 v247, a81\n"
    "v_mfma_f32_16x16x32_bf16 v[152:155], a[48:51], a[132:135], v[152:155]\n"
    "ds_read_b128 a[176:179], v241 offset:59392\n"
    "v_lshlrev_b32 v244, 16, v245\n"
    "v_mfma_f32_16x16x32_bf16 v[152:155], v[136:139], a[164:167], v[152:155]\n"
    "v_lshlrev_b32 v246, 16, v247\n"
    "v_and_b32 v245, s58, v245\n"
    "v_mfma_f32_16x16x32_bf16 v[152:155], v[100:103], a[160:163], v[152:155]\n"
    "ds_read_b128 a[144:147], v241 offset:60416\n"
    "v_and_b32 v247, s58, v247\n"
    "v_mfma_f32_16x16x32_bf16 v[152:155], a[8:11], a[128:131], v[152:155]\n"
    "s_add_u32 s63, s70, 98816\n"
    "s_waitcnt lgkmcnt(3)\n"
    "v_mul_f32 v244, v244, v144\n"
    "v_mfma_f32_16x16x32_bf16 v[156:159], v[228:231], a[140:143], v[156:159]\n"
    "buffer_store_dwordx4 v[116:119], v243, s[40:43], s63 offen sc1\n"
    "v_mul_f32 v246, v246, v146\n"
    "v_mul_f32 v245, v245, v145\n"
    "v_mul_f32 v247, v247, v147\n"
    "v_mfma_f32_16x16x32_bf16 v[156:159], a[56:59], a[172:175], v[156:159]\n"
    "v_cvt_pk_bf16_f32 v144, v244, v245\n"
    "v_cvt_pk_bf16_f32 v145, v246, v247\n"
    "v_mfma_f32_16x16x32_bf16 v[156:159], v[168:171], a[168:171], v[156:159]\n"
    "v_accvgpr_read_b32 v245, a82\n"
    "v_accvgpr_read_b32 v247, a83\n"
    "v_mfma_f32_16x16x32_bf16 v[156:159], v[204:207], a[136:139], v[156:159]\n"
    "v_lshlrev_b32 v244, 16, v245\n"
    "v_lshlrev_b32 v246, 16, v247\n"
    "v_mfma_f32_16x16x32_bf16 v[156:159], v[132:135], a[132:135], v[156:159]\n"
    "v_and_b32 v245, s58, v245\n"
    "v_and_b32 v247, s58, v247\n"
    "v_mfma_f32_16x16x32_bf16 v[156:159], a[12:15], a[164:167], v[156:159]\n"
    "s_add_u32 s63, s70, 115200\n"
    "s_waitcnt lgkmcnt(2)\n"
    "v_mul_f32 v244, v244, v148\n"
    "v_mfma_f32_16x16x32_bf16 v[156:159], v[64:67], a[160:163], v[156:159]\n"
    "buffer_store_dwordx4 v[124:127], v243, s[40:43], s63 offen sc1\n"
    "v_mul_f32 v246, v246, v150\n"
    "v_mul_f32 v245, v245, v149\n"
    "v_mul_f32 v247, v247, v151\n"
    "v_mfma_f32_16x16x32_bf16 v[156:159], v[108:111], a[128:131], v[156:159]\n"
    "v_cvt_pk_bf16_f32 v146, v244, v245\n"
    "v_cvt_pk_bf16_f32 v147, v246, v247\n"
    "v_mfma_f32_16x16x32_bf16 v[176:179], a[28:31], a[156:159], v[176:179]\n"
    "ds_write_b128 v248, v[144:147] offset:0\n"
    "v_mfma_f32_16x16x32_bf16 v[176:179], v[224:227], a[188:191], v[176:179]\n"
    "ds_read_b128 a[140:143], v241 offset:12288\n"
    "v_accvgpr_read_b32 v245, a112\n"
    "v_mfma_f32_16x16x32_bf16 v[176:179], v[200:203], a[184:187], v[176:179]\n"
    "v_accvgpr_read_b32 v247, a113\n"
    "v_lshlrev_b32 v244, 16, v245\n"
    "v_mfma_f32_16x16x32_bf16 v[176:179], v[236:239], a[152:155], v[176:179]\n"
    "v_lshlrev_b32 v246, 16, v247\n"
    "v_and_b32 v245, s58, v245\n"
    "v_mfma_f32_16x16x32_bf16 v[176:179], v[164:167], a[148:151], v[176:179]\n"
    "ds_read_b128 a[172:175], v241 offset:13312\n"
    "v_and_b32 v247, s58, v247\n"
    "v_mfma_f32_16x16x32_bf16 v[176:179], a[16:19], a[180:183], v[176:179]\n"
    "s_waitcnt lgkmcnt(4)\n"
    "v_mul_f32 v244, v244, v152\n"
    "v_mul_f32 v246, v246, v154\n"
    "v_mfma_f32_16x16x32_bf16 v[176:179], v[96:99], a[176:179], v[176:179]\n"
    "s_waitcnt lgkmcnt(3)\n"
    "v_mul_f32 v245, v245, v153\n"
    "v_mul_f32 v247, v247, v155\n"
    "v_mfma_f32_16x16x32_bf16 v[176:179], v[140:143], a[144:147], v[176:179]\n"
    "ds_read_b128 a[168:171], v241 offset:28672\n"
    "v_cvt_pk_bf16_f32 v152, v244, v245\n"
    "v_mfma_f32_16x16x32_bf16 v[180:183], a[24:27], a[156:159], v[180:183]\n"
    "v_cvt_pk_bf16_f32 v153, v246, v247\n"
    "v_accvgpr_read_b32 v245, a114\n"
    "v_mfma_f32_16x16x32_bf16 v[180:183], v[192:195], a[188:191], v[180:183]\n"
    "v_accvgpr_read_b32 v247, a115\n"
    "v_lshlrev_b32 v244, 16, v245\n"
    "v_mfma_f32_16x16x32_bf16 v[180:183], a[20:23], a[184:187], v[180:183]\n"
    "ds_read_b128 a[136:139], v241 offset:29696\n"
    "v_lshlrev_b32 v246, 16, v247\n"
    "v_mfma_f32_16x16x32_bf16 v[180:183], v[160:163], a[152:155], v[180:183]\n"
    "v_and_b32 v245, s58, v245\n"
    "v_and_b32 v247, s58, v247\n"
    "v_mfma_f32_16x16x32_bf16 v[180:183], v[128:131], a[148:151], v[180:183]\n"
    "v_mul_f32 v244, v244, v156\n"
    "v_mul_f32 v246, v246, v158\n"
    "v_mfma_f32_16x16x32_bf16 v[180:183], v[172:175], a[180:183], v[180:183]\n"
    "ds_read_b128 a[132:135], v241 offset:45056\n"
    "v_mul_f32 v245, v245, v157\n"
    "v_mfma_f32_16x16x32_bf16 v[180:183], a[44:47], a[176:179], v[180:183]\n"
    "v_mul_f32 v247, v247, v159\n"
    "v_cvt_pk_bf16_f32 v154, v244, v245\n"
    "v_mfma_f32_16x16x32_bf16 v[180:183], v[104:107], a[144:147], v[180:183]\n"
    "v_cvt_pk_bf16_f32 v155, v246, v247\n"
    "v_mfma_f32_16x16x32_bf16 v[184:187], a[60:63], a[156:159], v[184:187]\n"
    "ds_write_b128 v249, v[152:155] offset:0\n"
    "v_mfma_f32_16x16x32_bf16 v[184:187], v[232:235], a[188:191], v[184:187]\n"
    "ds_read_b128 a[164:167], v241 offset:46080\n"
    "v_accvgpr_read_b32 v245, a84\n"
    "v_mfma_f32_16x16x32_bf16 v[184:187], v[196:199], a[184:187], v[184:187]\n"
    "ds_read_b128 v[148:151], v242 offset:0\n"
    "v_accvgpr_read_b32 v247, a85\n"
    "v_mfma_f32_16x16x32_bf16 v[184:187], a[52:55], a[152:155], v[184:187]\n"
    "ds_read_b128 v[156:159], v242 offset:1024\n"
    "v_lshlrev_b32 v244, 16, v245\n"
    "v_mfma_f32_16x16x32_bf16 v[184:187], a[48:51], a[148:151], v[184:187]\n"
    "ds_read_b128 a[160:163], v241 offset:61440\n"
    "v_lshlrev_b32 v246, 16, v247\n"
    "v_mfma_f32_16x16x32_bf16 v[184:187], v[136:139], a[180:183], v[184:187]\n"
    "v_and_b32 v245, s58, v245\n"
    "v_and_b32 v247, s58, v247\n"
    "v_mfma_f32_16x16x32_bf16 v[184:187], v[100:103], a[176:179], v[184:187]\n"
    "ds_read_b128 a[128:131], v241 offset:62464\n"
    "v_mul_f32 v244, v244, v176\n"
    "v_mfma_f32_16x16x32_bf16 v[184:187], a[8:11], a[144:147], v[184:187]\n"
    "s_add_u32 s63, s70, 131584\n"
    "s_waitcnt lgkmcnt(3)\n"
    "v_mul_f32 v246, v246, v178\n"
    "v_mfma_f32_16x16x32_bf16 v[188:191], v[228:231], a[156:159], v[188:191]\n"
    "buffer_store_dwordx4 v[148:151], v243, s[40:43], s63 offen sc1\n"
    "v_mul_f32 v245, v245, v177\n"
    "v_mul_f32 v247, v247, v179\n"
    "v_cvt_pk_bf16_f32 v176, v244, v245\n"
    "v_mfma_f32_16x16x32_bf16 v[188:191], a[56:59], a[188:191], v[188:191]\n"
    "v_cvt_pk_bf16_f32 v177, v246, v247\n"
    "v_accvgpr_read_b32 v245, a86\n"
    "v_mfma_f32_16x16x32_bf16 v[188:191], v[168:171], a[184:187], v[188:191]\n"
    "v_accvgpr_read_b32 v247, a87\n"
    "v_lshlrev_b32 v244, 16, v245\n"
    "v_mfma_f32_16x16x32_bf16 v[188:191], v[204:207], a[152:155], v[188:191]\n"
    "v_lshlrev_b32 v246, 16, v247\n"
    "v_and_b32 v245, s58, v245\n"
    "v_mfma_f32_16x16x32_bf16 v[188:191], v[132:135], a[148:151], v[188:191]\n"
    "v_and_b32 v247, s58, v247\n"
    "v_mul_f32 v244, v244, v180\n"
    "v_mfma_f32_16x16x32_bf16 v[188:191], a[12:15], a[180:183], v[188:191]\n"
    "s_add_u32 s63, s70, 147968\n"
    "s_waitcnt lgkmcnt(2)\n"
    "v_mul_f32 v246, v246, v182\n"
    "v_mfma_f32_16x16x32_bf16 v[188:191], v[64:67], a[176:179], v[188:191]\n"
    "buffer_store_dwordx4 v[156:159], v243, s[40:43], s63 offen sc1\n"
    "v_mul_f32 v245, v245, v181\n"
    "v_mul_f32 v247, v247, v183\n"
    "v_cvt_pk_bf16_f32 v178, v244, v245\n"
    "v_mfma_f32_16x16x32_bf16 v[188:191], v[108:111], a[144:147], v[188:191]\n"
    "v_cvt_pk_bf16_f32 v179, v246, v247\n"
    "v_mfma_f32_16x16x32_bf16 v[208:211], a[28:31], a[140:143], v[208:211]\n"
    "ds_write_b128 v248, v[176:179] offset:2048\n"
    "v_mfma_f32_16x16x32_bf16 v[208:211], v[224:227], a[172:175], v[208:211]\n"
    "v_accvgpr_read_b32 v245, a116\n"
    "v_accvgpr_read_b32 v247, a117\n"
    "v_mfma_f32_16x16x32_bf16 v[208:211], v[200:203], a[168:171], v[208:211]\n"
    "v_lshlrev_b32 v244, 16, v245\n"
    "v_lshlrev_b32 v246, 16, v247\n"
    "v_mfma_f32_16x16x32_bf16 v[208:211], v[236:239], a[136:139], v[208:211]\n"
    "v_and_b32 v245, s58, v245\n"
    "v_and_b32 v247, s58, v247\n"
    "v_mfma_f32_16x16x32_bf16 v[208:211], v[164:167], a[132:135], v[208:211]\n"
    "v_mul_f32 v244, v244, v184\n"
    "v_mul_f32 v246, v246, v186\n"
    "v_mfma_f32_16x16x32_bf16 v[208:211], a[16:19], a[164:167], v[208:211]\n"
    "s_waitcnt lgkmcnt(2)\n"
    "v_mul_f32 v245, v245, v185\n"
    "v_mul_f32 v247, v247, v187\n"
    "v_mfma_f32_16x16x32_bf16 v[208:211], v[96:99], a[160:163], v[208:211]\n"
    "s_waitcnt lgkmcnt(1)\n"
    "v_cvt_pk_bf16_f32 v184, v244, v245\n"
    "v_cvt_pk_bf16_f32 v185, v246, v247\n"
    "v_mfma_f32_16x16x32_bf16 v[208:211], v[140:143], a[128:131], v[208:211]\n"
    "v_accvgpr_read_b32 v245, a118\n"
    "v_accvgpr_read_b32 v247, a119\n"
    "v_mfma_f32_16x16x32_bf16 v[212:215], a[24:27], a[140:143], v[212:215]\n"
    "v_lshlrev_b32 v244, 16, v245\n"
    "v_lshlrev_b32 v246, 16, v247\n"
    "v_mfma_f32_16x16x32_bf16 v[212:215], v[192:195], a[172:175], v[212:215]\n"
    "v_and_b32 v245, s58, v245\n"
    "v_and_b32 v247, s58, v247\n"
    "v_mfma_f32_16x16x32_bf16 v[212:215], a[20:23], a[168:171], v[212:215]\n"
    "v_mul_f32 v244, v244, v188\n"
    "v_mul_f32 v246, v246, v190\n"
    "v_mfma_f32_16x16x32_bf16 v[212:215], v[160:163], a[136:139], v[212:215]\n"
    "v_mul_f32 v245, v245, v189\n"
    "v_mul_f32 v247, v247, v191\n"
    "v_mfma_f32_16x16x32_bf16 v[212:215], v[128:131], a[132:135], v[212:215]\n"
    "v_cvt_pk_bf16_f32 v186, v244, v245\n"
    "v_cvt_pk_bf16_f32 v187, v246, v247\n"
    "v_mfma_f32_16x16x32_bf16 v[212:215], v[172:175], a[164:167], v[212:215]\n"
    "ds_write_b128 v249, v[184:187] offset:2048\n"
    "v_mfma_f32_16x16x32_bf16 v[212:215], a[44:47], a[160:163], v[212:215]\n"
    "ds_read_b128 v[180:183], v242 offset:2048\n"
    "v_accvgpr_read_b32 v245, a88\n"
    "v_mfma_f32_16x16x32_bf16 v[212:215], v[104:107], a[128:131], v[212:215]\n"
    "ds_read_b128 v[188:191], v242 offset:3072\n"
    "v_accvgpr_read_b32 v247, a89\n"
    "v_mfma_f32_16x16x32_bf16 v[216:219], a[60:63], a[140:143], v[216:219]\n"
    "v_lshlrev_b32 v244, 16, v245\n"
    "v_lshlrev_b32 v246, 16, v247\n"
    "v_mfma_f32_16x16x32_bf16 v[216:219], v[232:235], a[172:175], v[216:219]\n"
    "v_and_b32 v245, s58, v245\n"
    "v_and_b32 v247, s58, v247\n"
    "v_mfma_f32_16x16x32_bf16 v[216:219], v[196:199], a[168:171], v[216:219]\n"
    "v_mul_f32 v244, v244, v208\n"
    "v_mul_f32 v246, v246, v210\n"
    "v_mfma_f32_16x16x32_bf16 v[216:219], a[52:55], a[136:139], v[216:219]\n"
    "s_add_u32 s63, s70, 164352\n"
    "s_waitcnt lgkmcnt(1)\n"
    "v_mul_f32 v245, v245, v209\n"
    "v_mfma_f32_16x16x32_bf16 v[216:219], a[48:51], a[132:135], v[216:219]\n"
    "buffer_store_dwordx4 v[180:183], v243, s[40:43], s63 offen sc1\n"
    "v_mul_f32 v247, v247, v211\n"
    "v_cvt_pk_bf16_f32 v208, v244, v245\n"
    "v_cvt_pk_bf16_f32 v209, v246, v247\n"
    "v_mfma_f32_16x16x32_bf16 v[216:219], v[136:139], a[164:167], v[216:219]\n"
    "v_accvgpr_read_b32 v245, a90\n"
    "v_accvgpr_read_b32 v247, a91\n"
    "v_mfma_f32_16x16x32_bf16 v[216:219], v[100:103], a[160:163], v[216:219]\n"
    "v_lshlrev_b32 v244, 16, v245\n"
    "v_lshlrev_b32 v246, 16, v247\n"
    "v_mfma_f32_16x16x32_bf16 v[216:219], a[8:11], a[128:131], v[216:219]\n"
    "v_and_b32 v245, s58, v245\n"
    "v_and_b32 v247, s58, v247\n"
    "v_mfma_f32_16x16x32_bf16 v[220:223], v[228:231], a[140:143], v[220:223]\n"
    "v_mul_f32 v244, v244, v212\n"
    "v_mul_f32 v246, v246, v214\n"
    "v_mfma_f32_16x16x32_bf16 v[220:223], a[56:59], a[172:175], v[220:223]\n"
    "s_add_u32 s63, s70, 180736\n"
    "s_waitcnt lgkmcnt(0)\n"
    "v_mul_f32 v245, v245, v213\n"
    "v_mfma_f32_16x16x32_bf16 v[220:223], v[168:171], a[168:171], v[220:223]\n"
    "buffer_store_dwordx4 v[188:191], v243, s[40:43], s63 offen sc1\n"
    "v_mul_f32 v247, v247, v215\n"
    "v_cvt_pk_bf16_f32 v210, v244, v245\n"
    "v_cvt_pk_bf16_f32 v211, v246, v247\n"
    "v_mfma_f32_16x16x32_bf16 v[220:223], v[204:207], a[136:139], v[220:223]\n"
    "ds_write_b128 v248, v[208:211] offset:0\n"
    "v_mfma_f32_16x16x32_bf16 v[220:223], v[132:135], a[132:135], v[220:223]\n"
    "v_accvgpr_read_b32 v245, a120\n"
    "v_accvgpr_read_b32 v247, a121\n"
    "v_mfma_f32_16x16x32_bf16 v[220:223], a[12:15], a[164:167], v[220:223]\n"
    "v_lshlrev_b32 v244, 16, v245\n"
    "v_lshlrev_b32 v246, 16, v247\n"
    "v_mfma_f32_16x16x32_bf16 v[220:223], v[64:67], a[160:163], v[220:223]\n"
    "v_and_b32 v245, s58, v245\n"
    "v_and_b32 v247, s58, v247\n"
    "v_mfma_f32_16x16x32_bf16 v[220:223], v[108:111], a[128:131], v[220:223]\n"
    "v_mul_f32 v244, v244, v216\n"
    "s_nop 7\n"
    "v_mul_f32 v246, v246, v218\n"
    "v_mul_f32 v245, v245, v217\n"
    "v_mul_f32 v247, v247, v219\n"
    "v_cvt_pk_bf16_f32 v216, v244, v245\n"
    "v_cvt_pk_bf16_f32 v217, v246, v247\n"
    "v_accvgpr_read_b32 v245, a122\n"
    "v_accvgpr_read_b32 v247, a123\n"
    "v_lshlrev_b32 v244, 16, v245\n"
    "v_lshlrev_b32 v246, 16, v247\n"
    "v_and_b32 v245, s58, v245\n"
    "v_and_b32 v247, s58, v247\n"
    "v_mul_f32 v244, v244, v220\n"
    "v_mul_f32 v246, v246, v222\n"
    "v_mul_f32 v245, v245, v221\n"
    "v_mul_f32 v247, v247, v223\n"
    "v_cvt_pk_bf16_f32 v218, v244, v245\n"
    "v_cvt_pk_bf16_f32 v219, v246, v247\n"
    "ds_write_b128 v249, v[216:219] offset:0\n"
    "ds_read_b128 v[212:215], v242 offset:0\n"
    "ds_read_b128 v[220:223], v242 offset:1024\n"
    "s_add_u32 s63, s70, 197120\n"
    "s_waitcnt lgkmcnt(1)\n"
    "buffer_store_dwordx4 v[212:215], v243, s[40:43], s63 offen sc1\n"
    "s_add_u32 s63, s70, 213504\n"
    "s_waitcnt lgkmcnt(0)\n"
    "buffer_store_dwordx4 v[220:223], v243, s[40:43], s63 offen sc1\n"
    "CODE_END_%=:\n"
    :
    : "s"(vb), "s"(wave), "s"(ldsb), "s"(pb), "s"(__builtin_amdgcn_kernarg_segment_ptr())
    : "v0", "v1", "v2", "v3", "v4", "v5", "v6", "v7", "v8", "v9", "v10", "v11", "v12", "v13", "v14", "v15", "v16", "v17", "v18", "v19", "v20", "v21", "v22", "v23", "v24", "v25", "v26", "v27", "v28", "v29", "v30", "v31", "v32", "v33", "v34", "v35", "v36", "v37", "v38", "v39", "v40", "v41", "v42", "v43", "v44", "v45", "v46", "v47", "v48", "v49", "v50", "v51", "v52", "v53", "v54", "v55", "v56", "v57", "v58", "v59", "v60", "v61", "v62", "v63", "v64", "v65", "v66", "v67", "v68", "v69", "v70", "v71", "v72", "v73", "v74", "v75", "v76", "v77", "v78", "v79", "v80", "v81", "v82", "v83", "v84", "v85", "v86", "v87", "v88", "v89", "v90", "v91", "v92", "v93", "v94", "v95", "v96", "v97", "v98", "v99", "v100", "v101", "v102", "v103", "v104", "v105", "v106", "v107", "v108", "v109", "v110", "v111", "v112", "v113", "v114", "v115", "v116", "v117", "v118", "v119", "v120", "v121", "v122", "v123", "v124", "v125", "v126", "v127", "v128", "v129", "v130", "v131", "v132", "v133", "v134", "v135", "v136", "v137", "v138", "v139", "v140", "v141", "v142", "v143", "v144", "v145", "v146", "v147", "v148", "v149", "v150", "v151", "v152", "v153", "v154", "v155", "v156", "v157", "v158", "v159", "v160", "v161", "v162", "v163", "v164", "v165", "v166", "v167", "v168", "v169", "v170", "v171", "v172", "v173", "v174", "v175", "v176", "v177", "v178", "v179", "v180", "v181", "v182", "v183", "v184", "v185", "v186", "v187", "v188", "v189", "v190", "v191", "v192", "v193", "v194", "v195", "v196", "v197", "v198", "v199", "v200", "v201", "v202", "v203", "v204", "v205", "v206", "v207", "v208", "v209", "v210", "v211", "v212", "v213", "v214", "v215", "v216", "v217", "v218", "v219", "v220", "v221", "v222", "v223", "v224", "v225", "v226", "v227", "v228", "v229", "v230", "v231", "v232", "v233", "v234", "v235", "v236", "v237", "v238", "v239", "v240", "v241", "v242", "v243", "v244", "v245", "v246", "v247", "v248", "v249", "v250", "v251", "v252", "v253", "v254", "v255", "a0", "a1", "a2", "a3", "a4", "a5", "a6", "a7", "a8", "a9", "a10", "a11", "a12", "a13", "a14", "a15", "a16", "a17", "a18", "a19", "a20", "a21", "a22", "a23", "a24", "a25", "a26", "a27", "a28", "a29", "a30", "a31", "a32", "a33", "a34", "a35", "a36", "a37", "a38", "a39", "a40", "a41", "a42", "a43", "a44", "a45", "a46", "a47", "a48", "a49", "a50", "a51", "a52", "a53", "a54", "a55", "a56", "a57", "a58", "a59", "a60", "a61", "a62", "a63", "a64", "a65", "a66", "a67", "a68", "a69", "a70", "a71", "a72", "a73", "a74", "a75", "a76", "a77", "a78", "a79", "a80", "a81", "a82", "a83", "a84", "a85", "a86", "a87", "a88", "a89", "a90", "a91", "a92", "a93", "a94", "a95", "a96", "a97", "a98", "a99", "a100", "a101", "a102", "a103", "a104", "a105", "a106", "a107", "a108", "a109", "a110", "a111", "a112", "a113", "a114", "a115", "a116", "a117", "a118", "a119", "a120", "a121", "a122", "a123", "a124", "a125", "a126", "a127", "a128", "a129", "a130", "a131", "a132", "a133", "a134", "a135", "a136", "a137", "a138", "a139", "a140", "a141", "a142", "a143", "a144", "a145", "a146", "a147", "a148", "a149", "a150", "a151", "a152", "a153", "a154", "a155", "a156", "a157", "a158", "a159", "a160", "a161", "a162", "a163", "a164", "a165", "a166", "a167", "a168", "a169", "a170", "a171", "a172", "a173", "a174", "a175", "a176", "a177", "a178", "a179", "a180", "a181", "a182", "a183", "a184", "a185", "a186", "a187", "a188", "a189", "a190", "a191", "a192", "a193", "a194", "a195", "a196", "a197", "a198", "a199", "a200", "a201", "a202", "a203", "a204", "a205", "a206", "a207", "a208", "a209", "a210", "a211", "a212", "a213", "a214", "a215", "a216", "a217", "a218", "a219", "a220", "a221", "a222", "a223", "a224", "a225", "a226", "a227", "a228", "a229", "a230", "a231", "a232", "a233", "a234", "a235", "a236", "a237", "a238", "a239", "a240", "a241", "a242", "a243", "a244", "a245", "a246", "a247", "a248", "a249", "a250", "a251", "a252", "a253", "a254", "a255", "s4", "s5", "s6", "s7", "s8", "s9", "s10", "s11", "s12", "s13", "s14", "s15", "s20", "s21", "s22", "s23", "s24", "s25", "s26", "s27", "s28", "s29", "s30", "s31", "s32", "s33", "s34", "s35", "s36", "s37", "s38", "s39", "s40", "s41", "s42", "s43", "s44", "s45", "s46", "s47", "s48", "s49", "s50", "s51", "s52", "s53", "s54", "s55", "s56", "s57", "s58", "s59", "s60", "s61", "s62", "s63", "s64", "s65", "s66", "s67", "s68", "s69", "s70", "s71", "s72", "s73", "s74", "s75", "s76", "s77", "s78", "s79", "s80", "s81", "s82", "s83", "s84", "s85", "s86", "s87", "s88", "s89", "s90", "s91", "s92", "s93", "s94", "s95", "s96", "s97", "s98", "s99", "vcc", "scc", "memory");
}

extern "C" void kernel_launch(void* const* d_in, const int* in_sizes, int n_in,
                              void* d_out, int out_size, void* d_ws, size_t ws_size,
                              hipStream_t stream) {
  const u16* x0 = (const u16*)d_in[0];
  const u16* ws = (const u16*)d_in[1];
  const u16* bs = (const u16*)d_in[2];
  char* base = (char*)d_ws + 0;
  unsigned* flags = (unsigned*)base;
  char* wtb = base + FLAG_BYTES + ((size_t)226 << 20);
  u16* wt = (u16*)wtb;
  u16* ex = (u16*)(base + ((size_t)32 << 20));
  dcn_pair<<<256, 256, 0, stream>>>(x0, wt, bs, ex, flags, (u16*)d_out, ws);
}
```
